```python
import torch

def _softmax(x, axis=-1):
    return torch.softmax(x, dim=axis)

def _gelu(x):
    return torch.nn.functional.gelu(x, approximate='tanh')

def _silu(x):
    return torch.nn.functional.silu(x)

def _sigmoid(x):
    return torch.sigmoid(x)

def _layernorm(x, axis=-1, eps=1e-05):
    m = x.mean(dim=axis, keepdim=True)
    v = x.var(dim=axis, keepdim=True, unbiased=False)
    return (x - m) / torch.sqrt(v + eps)

def _scatter_add(tgt, idx, src, axis=0):
    return tgt.clone().index_add_(axis, idx, src)

def _segment_sum(src, seg, n):
    out = torch.zeros((n,) + src.shape[1:], dtype=src.dtype)
    return out.index_add_(0, seg, src)

def _pad(x, pw):
    flat = tuple((v for pair in reversed(pw) for v in pair))
    return torch.nn.functional.pad(x, flat)
kernel_name = 'interleave_layers__memory_aug_recurrent_mem_tr__glu_gated_swiglu__prompt'

def setup_inputs():

    _w = lambda a: (lambda t: t.to(torch.bfloat16) if t.is_floating_point() else t)(torch.as_tensor(a))

    def _A_setup():
        B, S, Dm = (1, 2048, 576)
        H, Dh = (32, 128)
        M, seg = (8, 32)
        seg = min(seg, S)
        S = S // seg * seg
        return {'x': _w((lambda *s: torch.randn(*s, dtype=torch.bfloat16) * 0.05)(B, S, Dm)), 'mem0': _w((lambda *s: torch.randn(*s, dtype=torch.bfloat16) * 0.2)(B, M, Dm)), 'Wq': _w((lambda *s: torch.randn(*s, dtype=torch.bfloat16) * 1.6)(Dm, H, Dh) * Dm ** (-0.5)), 'Wk': _w((lambda *s: torch.randn(*s, dtype=torch.bfloat16) * 1.6)(Dm, H, Dh) * Dm ** (-0.5)), 'Wv': _w((lambda *s: torch.randn(*s, dtype=torch.bfloat16) * 0.2)(Dm, H, Dh) * Dm ** (-0.5)), 'Wo': _w((lambda *s: torch.randn(*s, dtype=torch.bfloat16) * 0.2)(H, Dh, Dm) * (H * Dh) ** (-0.5)), 'seg_len': seg}
    _A_in = _A_setup()

    def _B_setup():
        B, S, D, F = (1, 2048, 576, 4608)
        return {'x': _w((lambda *s: torch.randn(*s, dtype=torch.bfloat16) * 0.05)(B, S, D)), 'w1': _w((lambda *s: torch.randn(*s, dtype=torch.bfloat16) * 0.2)(D, F) * (2.0 / D) ** 0.5), 'w3': _w((lambda *s: torch.randn(*s, dtype=torch.bfloat16) * 0.2)(D, F) * (2.0 / D) ** 0.5), 'w2': _w((lambda *s: torch.randn(*s, dtype=torch.bfloat16) * 0.2)(F, D) * (2.0 / F) ** 0.5)}
    _B_in = _B_setup()
    return {'A_x': _A_in['x'], 'A_mem0': _A_in['mem0'], 'A_Wq': _A_in['Wq'], 'A_Wk': _A_in['Wk'], 'A_Wv': _A_in['Wv'], 'A_Wo': _A_in['Wo'], 'A_seg_len': _A_in['seg_len'], 'B_x': _B_in['x'], 'B_w1': _B_in['w1'], 'B_w3': _B_in['w3'], 'B_w2': _B_in['w2']}

def reference(A_x, A_mem0, A_Wq, A_Wk, A_Wv, A_Wo, A_seg_len, B_x, B_w1, B_w3, B_w2):

    def _A_ref(x, mem0, Wq, Wk, Wv, Wo, seg_len):
        B, S, Dm = x.shape
        H, Dh = (Wq.shape[1], Wq.shape[2])
        M = mem0.shape[1]
        scale = Dh ** (-0.5)
        n_seg = S // seg_len
        L = M + seg_len
        idx = torch.arange(L)
        mask = (idx[:, None] < M) | (idx[:, None] >= idx[None, :])
        mem = mem0
        outs = []
        for s in range(n_seg):
            xs = x[:, s * seg_len:(s + 1) * seg_len, :].clone()
            h = torch.concatenate([mem, xs], axis=1)
            q = torch.einsum('btd,dhe->bhte', h, Wq)
            k = torch.einsum('btd,dhe->bhte', h, Wk)
            v = torch.einsum('btd,dhe->bhte', h, Wv)
            scores = torch.einsum('bhqe,bhke->bhqk', q, k) * scale
            scores = torch.where(mask[None, None], scores, -1000000000.0)
            p = _softmax(scores, axis=-1)
            o = torch.einsum('bhqk,bhke->bhqe', p, v)
            o = torch.einsum('bhte,hed->btd', o, Wo)
            mem = o[:, :M, :].clone()
            outs.append(o[:, M:, :])
        return torch.concatenate(outs, axis=1)

    def _B_ref(x, w1, w3, w2):
        gate = torch.einsum('bsd,df->bsf', x, w1)
        up = torch.einsum('bsd,df->bsf', x, w3)
        h = _silu(gate) * up
        out = torch.einsum('bsf,fd->bsd', h, w2)
        return out
    x = A_x
    x = x + _A_ref(_layernorm(x, axis=-1), A_mem0, A_Wq, A_Wk, A_Wv, A_Wo, A_seg_len)
    x = x + _B_ref(_layernorm(x, axis=-1), B_w1, B_w3, B_w2)
    x = x + _A_ref(_layernorm(x, axis=-1), A_mem0, A_Wq, A_Wk, A_Wv, A_Wo, A_seg_len)
    x = x + _B_ref(_layernorm(x, axis=-1), B_w1, B_w3, B_w2)
    return x
```

```cpp
#include <hip/hip_runtime.h>
#include <stdint.h>
#include <type_traits>
#include <utility>

typedef unsigned short u16;
typedef unsigned int u32;
typedef short bf16x8 __attribute__((ext_vector_type(8)));
typedef short bf16x4 __attribute__((ext_vector_type(4)));
typedef float f32x4 __attribute__((ext_vector_type(4)));
typedef float f32x2 __attribute__((ext_vector_type(2)));
typedef __bf16 bf2_t __attribute__((ext_vector_type(2)));
typedef unsigned v16u __attribute__((ext_vector_type(16)));
typedef unsigned v8u __attribute__((ext_vector_type(8)));
typedef unsigned v4u __attribute__((ext_vector_type(4)));
typedef unsigned v2u __attribute__((ext_vector_type(2)));
typedef __amdgpu_buffer_rsrc_t rsrc_t;
#define DEVI __device__ __forceinline__

static constexpr int NSEG = 64;
static constexpr int PLACE = 0, RQ = 8, RO = 8, RP = 8, RH = 8, RX = 16, RX2 = 16, PS_ATT = 2, PS_RED = 2, DL_R1 = 6, DL_W0 = 4, DL_W1 = 12, DL_U0 = 6, DL_U1 = 14, DL_D0 = 9, DL_D1 = 16;

static constexpr u32 FR = 256;
static constexpr u32 INITF = 256 * FR;
static constexpr u32 PARW = INITF + 4096;
static constexpr u32 TICK = PARW + 512;
static constexpr u32 CTRL_BYTES = TICK + 1024;
static constexpr u32 HOTSZ = 2u << 20;
static constexpr u32 CHUNK = 16384, IMG0 = 72 * CHUNK;
static constexpr u32 BULK = 8u << 20;
static constexpr u32 SEGB = 36864, RESB = 40960, QSLOT = 32 * 24576, KSLOT = 32 * 6144;
static constexpr u32 OFF_XN0 = BULK;
static constexpr u32 OFF_XN1 = OFF_XN0 + 64 * SEGB;
static constexpr u32 OFF_X2 = OFF_XN1 + RX2 * SEGB;
static constexpr u32 OFF_L = OFF_X2 + RX2 * RESB;
static constexpr u32 L_XN2 = 0;
static constexpr u32 L_X1 = L_XN2 + RX * SEGB;
static constexpr u32 L_QKV = L_X1 + RX * RESB;
static constexpr u32 L_KVM = L_QKV + RQ * QSLOT;
static constexpr u32 L_O = L_KVM + RQ * KSLOT;
static constexpr u32 L_PA = L_O + RO * 4 * 65536;
static constexpr u32 L_H = L_PA + RP * 4 * SEGB;
static constexpr u32 L_PF = L_H + RH * 4 * 73728;
static constexpr u32 LSZ = L_PF + RP * 4 * SEGB;
static constexpr u32 OFF_DBG = OFF_L + 2 * LSZ;
static constexpr u32 WS_END = OFF_DBG + 256u * 66 * 256;
static constexpr u32 OOB = 0x80000000u;
static constexpr int LDS_BYTES = 163840;
static constexpr unsigned SPIN_LIMIT = 1u << 18;
static constexpr int SL_REC = 0, SL_P = 8, SL_ATT = 16, SL_WO = 18, SL_UP = 21, SL_DOWN = 27, SL_REDA = 30, SL_REDF = 31;
DEVI u32 mbox(int L, int j, int role, int slot) { return (u32)(((L * 4 + j) * 32 + role) * FR + 4 * slot); }
DEVI u32 mbox_att(int L, int j, int hl, int slot) { return mbox(L, j, SL_ATT + (hl >> 2), 16 * (hl & 3) + slot); }

struct Params {
  const u16* x; const u16* mem0; const u16* Wq; const u16* Wk; const u16* Wv; const u16* Wo;
  const u16* w1; const u16* w3; const u16* w2; u16* out; unsigned char* ws;
  int role_mask; int unused;
};

template <class F, int... I> DEVI void static_for_impl(F&& f, std::integer_sequence<int, I...>) { (f(std::integral_constant<int, I>{}), ...); }
template <int N, class F> DEVI void static_for(F&& f) { static_for_impl(f, std::make_integer_sequence<int, N>{}); }
DEVI float bflo(u32 d) { return __uint_as_float(d << 16); }
DEVI float bfhi(u32 d) { return __uint_as_float(d & 0xffff0000u); }
DEVI u32 pk2(float a, float b) {
  f32x2 f = {a, b};
  bf2_t r = __builtin_convertvector(f, bf2_t);
  return __builtin_bit_cast(u32, r);
}
DEVI bf16x8 as_frag(v4u v) { return __builtin_bit_cast(bf16x8, v); }
DEVI bf16x4 as_frag4(v2u v) { return __builtin_bit_cast(bf16x4, v); }
DEVI f32x4 mfma32(v4u a, v4u b, f32x4 c) { return __builtin_amdgcn_mfma_f32_16x16x32_bf16(as_frag(a), as_frag(b), c, 0, 0, 0); }
DEVI f32x4 mfma16(v2u a, v2u b, f32x4 c) { return __builtin_amdgcn_mfma_f32_16x16x16bf16_1k(as_frag4(a), as_frag4(b), c, 0, 0, 0); }
DEVI v4u ld16(rsrc_t r, u32 voff, u32 soff) { return __builtin_amdgcn_raw_buffer_load_b128(r, voff, soff, 16); }
DEVI v2u ld8(rsrc_t r, u32 voff, u32 soff) { return __builtin_amdgcn_raw_buffer_load_b64(r, voff, soff, 16); }
DEVI u32 ld4(rsrc_t r, u32 voff, u32 soff) { return __builtin_amdgcn_raw_buffer_load_b32(r, voff, soff, 16); }
DEVI void st16(rsrc_t r, v4u v, u32 voff, u32 soff) { __builtin_amdgcn_raw_buffer_store_b128(v, r, voff, soff, 16); }
DEVI void st8(rsrc_t r, v2u v, u32 voff, u32 soff) { __builtin_amdgcn_raw_buffer_store_b64(v, r, voff, soff, 16); }
DEVI void st4(rsrc_t r, u32 v, u32 voff, u32 soff) { __builtin_amdgcn_raw_buffer_store_b32(v, r, voff, soff, 16); }
DEVI void drain() { asm volatile("s_waitcnt vmcnt(0)" ::: "memory"); }
DEVI void cbar() { asm volatile("" ::: "memory"); }
DEVI void sbar() { __builtin_amdgcn_sched_barrier(0); }
DEVI u32 rfl(u32 v) { return (u32)__builtin_amdgcn_readfirstlane((int)v); }
DEVI float xmax16_32(float x) {
  u32 a = __float_as_uint(x);
  auto r = __builtin_amdgcn_permlane16_swap(a, a, false, false);
  x = fmaxf(__uint_as_float(r[0]), __uint_as_float(r[1]));
  a = __float_as_uint(x);
  auto q = __builtin_amdgcn_permlane32_swap(a, a, false, false);
  return fmaxf(__uint_as_float(q[0]), __uint_as_float(q[1]));
}
DEVI float xsum16_32(float x) {
  u32 a = __float_as_uint(x);
  auto r = __builtin_amdgcn_permlane16_swap(a, a, false, false);
  x = __uint_as_float(r[0]) + __uint_as_float(r[1]);
  a = __float_as_uint(x);
  auto q = __builtin_amdgcn_permlane32_swap(a, a, false, false);
  return __uint_as_float(q[0]) + __uint_as_float(q[1]);
}
DEVI float ror8(float x) { return __int_as_float(__builtin_amdgcn_update_dpp(0, __float_as_int(x), 0x128, 0xf, 0xf, false)); }
DEVI v4u swap16(v2u a, v2u b) {
  auto s0 = __builtin_amdgcn_permlane16_swap(a.x, b.x, false, false);
  auto s1 = __builtin_amdgcn_permlane16_swap(a.y, b.y, false, false);
  return (v4u){s0[0], s1[0], s0[1], s1[1]};
}

#define STAMP(row, slot) do { } while (0)

DEVI bool flags_ok(int v, int bias, int s) {
  asm volatile("" : "+v"(v));
  return __all(v + bias >= s);
}
template <int SLP> DEVI void wait_flags(rsrc_t r, u32 addr, int bias, int s, unsigned& budget) {
  for (;;) {
    cbar();
    const int v = (int)ld4(r, addr, 0);
    if (flags_ok(v, bias, s) || budget == 0) break;
    --budget;
    __builtin_amdgcn_s_sleep(SLP);
  }
  cbar();
}
DEVI void publish(rsrc_t r, u32 flag, int s, int lane) {
  drain();
  if (lane == 0) st4(r, (u32)s, flag, 0);
  cbar();
}
DEVI void delay_start(int us) {
  if (us == 0) return;
  const unsigned long long t0 = __builtin_amdgcn_s_memrealtime();
  while (__builtin_amdgcn_s_memrealtime() - t0 < (unsigned long long)us * 100u) __builtin_amdgcn_s_sleep(16);
}
DEVI v4u make_srd(const void* p, u32 bytes) {
  const unsigned long long a = (unsigned long long)p;
  return (v4u){rfl((u32)a), rfl((u32)(a >> 32) & 0xffffu), bytes, 0x00020000u};
}
DEVI v16u cat4(v4u a, v4u b, v4u c, v4u d) {
  return (v16u){a.x, a.y, a.z, a.w, b.x, b.y, b.z, b.w, c.x, c.y, c.z, c.w, d.x, d.y, d.z, d.w};
}
DEVI u32 wswz(int j, int g) { return (u32)(4 * ((j >> 1) & 1) ^ 2 * (g & 1)); }
DEVI u32 tr_base(int lane, int chunk) {
  const int g = lane >> 4, q = (lane >> 2) & 3;
  return (u32)(g * 1024 + q * 128) + (((u32)chunk ^ wswz(q, g)) << 4);
}

#define RUN_ASM(TEXT) do { for (int i_ = 0; i_ < 16; ++i_) { sa[i_] = rfl(sa[i_]); sb[i_] = rfl(sb[i_]); } \
    asm volatile(TEXT "s_endpgm\n" :: "{s[36:51]}"(sa), "{s[52:67]}"(sb), "{v[0:15]}"(va) : "v255", "a255", "s99", "vcc", "memory"); \
    __builtin_unreachable(); } while (0)

#define GEMM_DECL v4u g_w0, g_w1, g_w2; u32 g_ldw, g_in_base, g_in_stride, g_in_mask, g_out_base, g_out_stride, g_out_mask, g_flag, g_paddr; int g_pbias
#define GEMM_ARGS g_w0, g_w1, g_w2, g_ldw, g_in_base, g_in_stride, g_in_mask, g_out_base, g_out_stride, g_out_mask, g_flag, g_paddr, g_pbias
DEVI void gemm_args(v4u g_w0, v4u g_w1, v4u g_w2, u32 g_ldw, u32 g_in_base, u32 g_in_stride, u32 g_in_mask, u32 g_out_base, u32 g_out_stride,
                    u32 g_out_mask, u32 g_flag, u32 g_paddr, int g_pbias, const Params& p, u32 dbg, v16u& sa, v16u& sb, v16u& va) {
  const int tid = threadIdx.x, lane = tid & 63, w = (int)rfl(tid >> 6);
  const int c = lane & 15, gg = lane >> 4, pp = lane & 3;
  const unsigned long long fm = __ballot(g_flag != OOB);
  sa = cat4(make_srd(p.ws, WS_END), g_w0, g_w1, g_w2);
  sb = (v16u){g_in_base, g_in_stride, g_in_mask, g_out_base, g_out_stride, g_out_mask, 0, (u32)w * 1024, dbg + w * 64, 0, (u32)fm, (u32)(fm >> 32), 0, 0, 0, 0};
  va[0] = lane * 16;
  va[1] = lane * 16 + w * 2048;
  va[2] = g_paddr;
  va[3] = (u32)g_pbias;
  va[4] = (u32)((4 + (w >> 1)) * 2048 + (gg & 1) * 1024 + ((2 * (w & 1) + (gg >> 1)) * 16 + c) * 16);
  va[5] = (u32)(8 * w + (lane >> 3)) * g_ldw + ((((u32)lane & 7) ^ wswz(lane >> 3, w)) << 4);
  va[6] = (u32)(w >> 1) * 4096 + tr_base(lane, 4 * (w & 1) + pp);
  va[7] = 2 * 4096 + tr_base(lane, 2 * w + (pp >> 1)) + 8 * (pp & 1);
  for (int i = 8; i < 16; ++i) va[i] = 0;
  va[14] = g_flag;
}

DEVI void role_p(const Params& p, int L, int j, int i, u32 dbg) {
  const int lane = threadIdx.x & 63, w = threadIdx.x >> 6, h = 8 * j + i;
  const u32 wb = 576u * 4096 * 2 - h * 256;
  GEMM_DECL;
  g_w0 = make_srd(p.Wq + h * 128, wb); g_w1 = make_srd(p.Wk + h * 128, wb); g_w2 = make_srd(p.Wv + h * 128, wb);
  g_ldw = 8192;
  g_in_base = L ? OFF_XN1 : OFF_XN0; g_in_stride = SEGB; g_in_mask = L ? RX2 - 1 : 63;
  g_out_base = OFF_L + L * LSZ + L_QKV + h * 24576; g_out_stride = QSLOT; g_out_mask = RQ - 1;
  g_flag = lane == 0 ? mbox(L, j, SL_REC + i, w) : (lane == 1 ? mbox_att(L, j, i, 8 + w) : OOB);
  g_paddr = mbox(L, j, SL_P + i, lane);
  g_pbias = lane < 16 ? 0 : (lane == 16 ? RQ : 1000);
  v16u sa, sb, va;
  gemm_args(GEMM_ARGS, p, dbg, sa, sb, va);
  RUN_ASM(
    "s_nop 4\n"
    "v_add_u32 v8, s59, v0\n"
    "s_add_u32 m0, s59, 0\n"
    "s_mov_b32 s74, 0\n"
    "buffer_load_dwordx4 v5, s[40:43], s74 offen lds\n"
    "s_add_u32 m0, s59, 4096\n"
    "s_mov_b32 s74, 128\n"
    "buffer_load_dwordx4 v5, s[40:43], s74 offen lds\n"
    "s_add_u32 m0, s59, 8192\n"
    "s_mov_b32 s74, 0\n"
    "buffer_load_dwordx4 v5, s[44:47], s74 offen lds\n"
    "s_add_u32 m0, s59, 12288\n"
    "s_mov_b32 s74, 128\n"
    "buffer_load_dwordx4 v5, s[44:47], s74 offen lds\n"
    "s_add_u32 m0, s59, 16384\n"
    "s_mov_b32 s74, 0\n"
    "buffer_load_dwordx4 v5, s[48:51], s74 offen lds\n"
    "s_add_u32 m0, s59, 20480\n"
    "s_mov_b32 s74, 128\n"
    "buffer_load_dwordx4 v5, s[48:51], s74 offen lds\n"
    "s_add_u32 m0, s59, 24576\n"
    "s_mov_b32 s74, 262144\n"
    "buffer_load_dwordx4 v5, s[40:43], s74 offen lds\n"
    "s_add_u32 m0, s59, 28672\n"
    "s_mov_b32 s74, 262272\n"
    "buffer_load_dwordx4 v5, s[40:43], s74 offen lds\n"
    "s_add_u32 m0, s59, 32768\n"
    "s_mov_b32 s74, 262144\n"
    "buffer_load_dwordx4 v5, s[44:47], s74 offen lds\n"
    "s_add_u32 m0, s59, 36864\n"
    "s_mov_b32 s74, 262272\n"
    "buffer_load_dwordx4 v5, s[44:47], s74 offen lds\n"
    "s_add_u32 m0, s59, 40960\n"
    "s_mov_b32 s74, 262144\n"
    "buffer_load_dwordx4 v5, s[48:51], s74 offen lds\n"
    "s_add_u32 m0, s59, 45056\n"
    "s_mov_b32 s74, 262272\n"
    "buffer_load_dwordx4 v5, s[48:51], s74 offen lds\n"
    "s_add_u32 m0, s59, 49152\n"
    "s_mov_b32 s74, 524288\n"
    "buffer_load_dwordx4 v5, s[40:43], s74 offen lds\n"
    "s_add_u32 m0, s59, 53248\n"
    "s_mov_b32 s74, 524416\n"
    "buffer_load_dwordx4 v5, s[40:43], s74 offen lds\n"
    "s_add_u32 m0, s59, 57344\n"
    "s_mov_b32 s74, 524288\n"
    "buffer_load_dwordx4 v5, s[44:47], s74 offen lds\n"
    "s_add_u32 m0, s59, 61440\n"
    "s_mov_b32 s74, 524416\n"
    "buffer_load_dwordx4 v5, s[44:47], s74 offen lds\n"
    "s_add_u32 m0, s59, 65536\n"
    "s_mov_b32 s74, 524288\n"
    "buffer_load_dwordx4 v5, s[48:51], s74 offen lds\n"
    "s_add_u32 m0, s59, 69632\n"
    "s_mov_b32 s74, 524416\n"
    "buffer_load_dwordx4 v5, s[48:51], s74 offen lds\n"
    "s_add_u32 m0, s59, 73728\n"
    "s_mov_b32 s74, 786432\n"
    "buffer_load_dwordx4 v5, s[40:43], s74 offen lds\n"
    "s_add_u32 m0, s59, 77824\n"
    "s_mov_b32 s74, 786560\n"
    "buffer_load_dwordx4 v5, s[40:43], s74 offen lds\n"
    "s_add_u32 m0, s59, 81920\n"
    "s_mov_b32 s74, 786432\n"
    "buffer_load_dwordx4 v5, s[44:47], s74 offen lds\n"
    "s_add_u32 m0, s59, 86016\n"
    "s_mov_b32 s74, 786560\n"
    "buffer_load_dwordx4 v5, s[44:47], s74 offen lds\n"
    "s_add_u32 m0, s59, 90112\n"
    "s_mov_b32 s74, 786432\n"
    "buffer_load_dwordx4 v5, s[48:51], s74 offen lds\n"
    "s_add_u32 m0, s59, 94208\n"
    "s_mov_b32 s74, 786560\n"
    "buffer_load_dwordx4 v5, s[48:51], s74 offen lds\n"
    "s_add_u32 m0, s59, 98304\n"
    "s_mov_b32 s74, 1048576\n"
    "buffer_load_dwordx4 v5, s[40:43], s74 offen lds\n"
    "s_add_u32 m0, s59, 102400\n"
    "s_mov_b32 s74, 1048704\n"
    "buffer_load_dwordx4 v5, s[40:43], s74 offen lds\n"
    "s_add_u32 m0, s59, 106496\n"
    "s_mov_b32 s74, 1048576\n"
    "buffer_load_dwordx4 v5, s[44:47], s74 offen lds\n"
    "s_add_u32 m0, s59, 110592\n"
    "s_mov_b32 s74, 1048704\n"
    "buffer_load_dwordx4 v5, s[44:47], s74 offen lds\n"
    "s_add_u32 m0, s59, 114688\n"
    "s_mov_b32 s74, 1048576\n"
    "buffer_load_dwordx4 v5, s[48:51], s74 offen lds\n"
    "s_add_u32 m0, s59, 118784\n"
    "s_mov_b32 s74, 1048704\n"
    "buffer_load_dwordx4 v5, s[48:51], s74 offen lds\n"
    "v_add_u32 v16, 49152, v6\n"
    "v_add_u32 v17, 98304, v6\n"
    "v_add_u32 v18, 49152, v7\n"
    "v_add_u32 v19, 98304, v7\n"
    "s_waitcnt vmcnt(24) lgkmcnt(0)\n"
    "s_barrier\n"
    "s_add_u32 m0, s59, 122880\n"
    "s_mov_b32 s74, 1310720\n"
    "buffer_load_dwordx4 v5, s[40:43], s74 offen lds\n"
    "s_add_u32 m0, s59, 126976\n"
    "s_mov_b32 s74, 1310848\n"
    "buffer_load_dwordx4 v5, s[40:43], s74 offen lds\n"
    "s_add_u32 m0, s59, 131072\n"
    "s_mov_b32 s74, 1310720\n"
    "buffer_load_dwordx4 v5, s[44:47], s74 offen lds\n"
    "s_add_u32 m0, s59, 135168\n"
    "s_mov_b32 s74, 1310848\n"
    "buffer_load_dwordx4 v5, s[44:47], s74 offen lds\n"
    "s_add_u32 m0, s59, 139264\n"
    "s_mov_b32 s74, 1310720\n"
    "buffer_load_dwordx4 v5, s[48:51], s74 offen lds\n"
    "s_add_u32 m0, s59, 143360\n"
    "s_mov_b32 s74, 1310848\n"
    "buffer_load_dwordx4 v5, s[48:51], s74 offen lds\n"
    "ds_read_b64_tr_b16 a[0:1], v6 offset:8192\n"
    "ds_read_b64_tr_b16 a[2:3], v6 offset:8704\n"
    "ds_read_b64_tr_b16 a[4:5], v6 offset:8200\n"
    "ds_read_b64_tr_b16 a[6:7], v6 offset:8712\n"
    "ds_read_b64_tr_b16 a[8:9], v6 offset:16384\n"
    "ds_read_b64_tr_b16 a[10:11], v6 offset:16896\n"
    "ds_read_b64_tr_b16 a[12:13], v6 offset:16392\n"
    "ds_read_b64_tr_b16 a[14:15], v6 offset:16904\n"
    "ds_read_b64_tr_b16 a[16:17], v6 offset:0\n"
    "ds_read_b64_tr_b16 a[18:19], v6 offset:512\n"
    "ds_read_b64_tr_b16 a[20:21], v6 offset:8\n"
    "ds_read_b64_tr_b16 a[22:23], v6 offset:520\n"
    "s_waitcnt vmcnt(24) lgkmcnt(0)\n"
    "s_barrier\n"
    "s_add_u32 m0, s59, 0\n"
    "s_mov_b32 s74, 1572864\n"
    "buffer_load_dwordx4 v5, s[40:43], s74 offen lds\n"
    "s_add_u32 m0, s59, 4096\n"
    "s_mov_b32 s74, 1572992\n"
    "buffer_load_dwordx4 v5, s[40:43], s74 offen lds\n"
    "s_add_u32 m0, s59, 8192\n"
    "s_mov_b32 s74, 1572864\n"
    "buffer_load_dwordx4 v5, s[44:47], s74 offen lds\n"
    "s_add_u32 m0, s59, 12288\n"
    "s_mov_b32 s74, 1572992\n"
    "buffer_load_dwordx4 v5, s[44:47], s74 offen lds\n"
    "s_add_u32 m0, s59, 16384\n"
    "s_mov_b32 s74, 1572864\n"
    "buffer_load_dwordx4 v5, s[48:51], s74 offen lds\n"
    "s_add_u32 m0, s59, 20480\n"
    "s_mov_b32 s74, 1572992\n"
    "buffer_load_dwordx4 v5, s[48:51], s74 offen lds\n"
    "ds_read_b64_tr_b16 a[24:25], v6 offset:32768\n"
    "ds_read_b64_tr_b16 a[26:27], v6 offset:33280\n"
    "ds_read_b64_tr_b16 a[28:29], v6 offset:32776\n"
    "ds_read_b64_tr_b16 a[30:31], v6 offset:33288\n"
    "ds_read_b64_tr_b16 a[32:33], v6 offset:40960\n"
    "ds_read_b64_tr_b16 a[34:35], v6 offset:41472\n"
    "ds_read_b64_tr_b16 a[36:37], v6 offset:40968\n"
    "ds_read_b64_tr_b16 a[38:39], v6 offset:41480\n"
    "ds_read_b64_tr_b16 a[40:41], v6 offset:24576\n"
    "ds_read_b64_tr_b16 a[42:43], v6 offset:25088\n"
    "ds_read_b64_tr_b16 a[44:45], v6 offset:24584\n"
    "ds_read_b64_tr_b16 a[46:47], v6 offset:25096\n"
    "s_waitcnt vmcnt(24) lgkmcnt(0)\n"
    "s_barrier\n"
    "s_add_u32 m0, s59, 24576\n"
    "s_mov_b32 s74, 1835008\n"
    "buffer_load_dwordx4 v5, s[40:43], s74 offen lds\n"
    "s_add_u32 m0, s59, 28672\n"
    "s_mov_b32 s74, 1835136\n"
    "buffer_load_dwordx4 v5, s[40:43], s74 offen lds\n"
    "s_add_u32 m0, s59, 32768\n"
    "s_mov_b32 s74, 1835008\n"
    "buffer_load_dwordx4 v5, s[44:47], s74 offen lds\n"
    "s_add_u32 m0, s59, 36864\n"
    "s_mov_b32 s74, 1835136\n"
    "buffer_load_dwordx4 v5, s[44:47], s74 offen lds\n"
    "s_add_u32 m0, s59, 40960\n"
    "s_mov_b32 s74, 1835008\n"
    "buffer_load_dwordx4 v5, s[48:51], s74 offen lds\n"
    "s_add_u32 m0, s59, 45056\n"
    "s_mov_b32 s74, 1835136\n"
    "buffer_load_dwordx4 v5, s[48:51], s74 offen lds\n"
    "ds_read_b64_tr_b16 a[48:49], v16 offset:8192\n"
    "ds_read_b64_tr_b16 a[50:51], v16 offset:8704\n"
    "ds_read_b64_tr_b16 a[52:53], v16 offset:8200\n"
    "ds_read_b64_tr_b16 a[54:55], v16 offset:8712\n"
    "ds_read_b64_tr_b16 a[56:57], v16 offset:16384\n"
    "ds_read_b64_tr_b16 a[58:59], v16 offset:16896\n"
    "ds_read_b64_tr_b16 a[60:61], v16 offset:16392\n"
    "ds_read_b64_tr_b16 a[62:63], v16 offset:16904\n"
    "ds_read_b64_tr_b16 a[64:65], v16 offset:0\n"
    "ds_read_b64_tr_b16 a[66:67], v16 offset:512\n"
    "ds_read_b64_tr_b16 a[68:69], v16 offset:8\n"
    "ds_read_b64_tr_b16 a[70:71], v16 offset:520\n"
    "s_waitcnt vmcnt(24) lgkmcnt(0)\n"
    "s_barrier\n"
    "s_add_u32 m0, s59, 49152\n"
    "s_mov_b32 s74, 2097152\n"
    "buffer_load_dwordx4 v5, s[40:43], s74 offen lds\n"
    "s_add_u32 m0, s59, 53248\n"
    "s_mov_b32 s74, 2097280\n"
    "buffer_load_dwordx4 v5, s[40:43], s74 offen lds\n"
    "s_add_u32 m0, s59, 57344\n"
    "s_mov_b32 s74, 2097152\n"
    "buffer_load_dwordx4 v5, s[44:47], s74 offen lds\n"
    "s_add_u32 m0, s59, 61440\n"
    "s_mov_b32 s74, 2097280\n"
    "buffer_load_dwordx4 v5, s[44:47], s74 offen lds\n"
    "s_add_u32 m0, s59, 65536\n"
    "s_mov_b32 s74, 2097152\n"
    "buffer_load_dwordx4 v5, s[48:51], s74 offen lds\n"
    "s_add_u32 m0, s59, 69632\n"
    "s_mov_b32 s74, 2097280\n"
    "buffer_load_dwordx4 v5, s[48:51], s74 offen lds\n"
    "ds_read_b64_tr_b16 a[72:73], v16 offset:32768\n"
    "ds_read_b64_tr_b16 a[74:75], v16 offset:33280\n"
    "ds_read_b64_tr_b16 a[76:77], v16 offset:32776\n"
    "ds_read_b64_tr_b16 a[78:79], v16 offset:33288\n"
    "ds_read_b64_tr_b16 a[80:81], v16 offset:40960\n"
    "ds_read_b64_tr_b16 a[82:83], v16 offset:41472\n"
    "ds_read_b64_tr_b16 a[84:85], v16 offset:40968\n"
    "ds_read_b64_tr_b16 a[86:87], v16 offset:41480\n"
    "ds_read_b64_tr_b16 a[88:89], v16 offset:24576\n"
    "ds_read_b64_tr_b16 a[90:91], v16 offset:25088\n"
    "ds_read_b64_tr_b16 a[92:93], v16 offset:24584\n"
    "ds_read_b64_tr_b16 a[94:95], v16 offset:25096\n"
    "s_waitcnt vmcnt(24) lgkmcnt(0)\n"
    "s_barrier\n"
    "s_add_u32 m0, s59, 73728\n"
    "s_mov_b32 s74, 2359296\n"
    "buffer_load_dwordx4 v5, s[40:43], s74 offen lds\n"
    "s_add_u32 m0, s59, 77824\n"
    "s_mov_b32 s74, 2359424\n"
    "buffer_load_dwordx4 v5, s[40:43], s74 offen lds\n"
    "s_add_u32 m0, s59, 81920\n"
    "s_mov_b32 s74, 2359296\n"
    "buffer_load_dwordx4 v5, s[44:47], s74 offen lds\n"
    "s_add_u32 m0, s59, 86016\n"
    "s_mov_b32 s74, 2359424\n"
    "buffer_load_dwordx4 v5, s[44:47], s74 offen lds\n"
    "s_add_u32 m0, s59, 90112\n"
    "s_mov_b32 s74, 2359296\n"
    "buffer_load_dwordx4 v5, s[48:51], s74 offen lds\n"
    "s_add_u32 m0, s59, 94208\n"
    "s_mov_b32 s74, 2359424\n"
    "buffer_load_dwordx4 v5, s[48:51], s74 offen lds\n"
    "ds_read_b64_tr_b16 a[96:97], v17 offset:8192\n"
    "ds_read_b64_tr_b16 a[98:99], v17 offset:8704\n"
    "ds_read_b64_tr_b16 a[100:101], v17 offset:8200\n"
    "ds_read_b64_tr_b16 a[102:103], v17 offset:8712\n"
    "ds_read_b64_tr_b16 a[104:105], v17 offset:16384\n"
    "ds_read_b64_tr_b16 a[106:107], v17 offset:16896\n"
    "ds_read_b64_tr_b16 a[108:109], v17 offset:16392\n"
    "ds_read_b64_tr_b16 a[110:111], v17 offset:16904\n"
    "ds_read_b64_tr_b16 a[112:113], v17 offset:0\n"
    "ds_read_b64_tr_b16 a[114:115], v17 offset:512\n"
    "ds_read_b64_tr_b16 a[116:117], v17 offset:8\n"
    "ds_read_b64_tr_b16 a[118:119], v17 offset:520\n"
    "s_waitcnt vmcnt(24) lgkmcnt(0)\n"
    "s_barrier\n"
    "s_add_u32 m0, s59, 98304\n"
    "s_mov_b32 s74, 2621440\n"
    "buffer_load_dwordx4 v5, s[40:43], s74 offen lds\n"
    "s_add_u32 m0, s59, 102400\n"
    "s_mov_b32 s74, 2621568\n"
    "buffer_load_dwordx4 v5, s[40:43], s74 offen lds\n"
    "s_add_u32 m0, s59, 106496\n"
    "s_mov_b32 s74, 2621440\n"
    "buffer_load_dwordx4 v5, s[44:47], s74 offen lds\n"
    "s_add_u32 m0, s59, 110592\n"
    "s_mov_b32 s74, 2621568\n"
    "buffer_load_dwordx4 v5, s[44:47], s74 offen lds\n"
    "s_add_u32 m0, s59, 114688\n"
    "s_mov_b32 s74, 2621440\n"
    "buffer_load_dwordx4 v5, s[48:51], s74 offen lds\n"
    "s_add_u32 m0, s59, 118784\n"
    "s_mov_b32 s74, 2621568\n"
    "buffer_load_dwordx4 v5, s[48:51], s74 offen lds\n"
    "ds_read_b64_tr_b16 a[120:121], v17 offset:32768\n"
    "ds_read_b64_tr_b16 a[122:123], v17 offset:33280\n"
    "ds_read_b64_tr_b16 a[124:125], v17 offset:32776\n"
    "ds_read_b64_tr_b16 a[126:127], v17 offset:33288\n"
    "ds_read_b64_tr_b16 a[128:129], v17 offset:40960\n"
    "ds_read_b64_tr_b16 a[130:131], v17 offset:41472\n"
    "ds_read_b64_tr_b16 a[132:133], v17 offset:40968\n"
    "ds_read_b64_tr_b16 a[134:135], v17 offset:41480\n"
    "ds_read_b64_tr_b16 a[136:137], v17 offset:24576\n"
    "ds_read_b64_tr_b16 a[138:139], v17 offset:25088\n"
    "ds_read_b64_tr_b16 a[140:141], v17 offset:24584\n"
    "ds_read_b64_tr_b16 a[142:143], v17 offset:25096\n"
    "s_waitcnt vmcnt(24) lgkmcnt(0)\n"
    "s_barrier\n"
    "s_add_u32 m0, s59, 122880\n"
    "s_mov_b32 s74, 2883584\n"
    "buffer_load_dwordx4 v5, s[40:43], s74 offen lds\n"
    "s_add_u32 m0, s59, 126976\n"
    "s_mov_b32 s74, 2883712\n"
    "buffer_load_dwordx4 v5, s[40:43], s74 offen lds\n"
    "s_add_u32 m0, s59, 131072\n"
    "s_mov_b32 s74, 2883584\n"
    "buffer_load_dwordx4 v5, s[44:47], s74 offen lds\n"
    "s_add_u32 m0, s59, 135168\n"
    "s_mov_b32 s74, 2883712\n"
    "buffer_load_dwordx4 v5, s[44:47], s74 offen lds\n"
    "s_add_u32 m0, s59, 139264\n"
    "s_mov_b32 s74, 2883584\n"
    "buffer_load_dwordx4 v5, s[48:51], s74 offen lds\n"
    "s_add_u32 m0, s59, 143360\n"
    "s_mov_b32 s74, 2883712\n"
    "buffer_load_dwordx4 v5, s[48:51], s74 offen lds\n"
    "ds_read_b64_tr_b16 a[144:145], v6 offset:8192\n"
    "ds_read_b64_tr_b16 a[146:147], v6 offset:8704\n"
    "ds_read_b64_tr_b16 a[148:149], v6 offset:8200\n"
    "ds_read_b64_tr_b16 a[150:151], v6 offset:8712\n"
    "ds_read_b64_tr_b16 a[152:153], v6 offset:16384\n"
    "ds_read_b64_tr_b16 a[154:155], v6 offset:16896\n"
    "ds_read_b64_tr_b16 a[156:157], v6 offset:16392\n"
    "ds_read_b64_tr_b16 a[158:159], v6 offset:16904\n"
    "ds_read_b64_tr_b16 a[160:161], v6 offset:0\n"
    "ds_read_b64_tr_b16 a[162:163], v6 offset:512\n"
    "ds_read_b64_tr_b16 a[164:165], v6 offset:8\n"
    "ds_read_b64_tr_b16 a[166:167], v6 offset:520\n"
    "s_waitcnt vmcnt(24) lgkmcnt(0)\n"
    "s_barrier\n"
    "s_add_u32 m0, s59, 0\n"
    "s_mov_b32 s74, 3145728\n"
    "buffer_load_dwordx4 v5, s[40:43], s74 offen lds\n"
    "s_add_u32 m0, s59, 4096\n"
    "s_mov_b32 s74, 3145856\n"
    "buffer_load_dwordx4 v5, s[40:43], s74 offen lds\n"
    "s_add_u32 m0, s59, 8192\n"
    "s_mov_b32 s74, 3145728\n"
    "buffer_load_dwordx4 v5, s[44:47], s74 offen lds\n"
    "s_add_u32 m0, s59, 12288\n"
    "s_mov_b32 s74, 3145856\n"
    "buffer_load_dwordx4 v5, s[44:47], s74 offen lds\n"
    "s_add_u32 m0, s59, 16384\n"
    "s_mov_b32 s74, 3145728\n"
    "buffer_load_dwordx4 v5, s[48:51], s74 offen lds\n"
    "s_add_u32 m0, s59, 20480\n"
    "s_mov_b32 s74, 3145856\n"
    "buffer_load_dwordx4 v5, s[48:51], s74 offen lds\n"
    "ds_read_b64_tr_b16 a[168:169], v6 offset:32768\n"
    "ds_read_b64_tr_b16 a[170:171], v6 offset:33280\n"
    "ds_read_b64_tr_b16 a[172:173], v6 offset:32776\n"
    "ds_read_b64_tr_b16 a[174:175], v6 offset:33288\n"
    "ds_read_b64_tr_b16 a[176:177], v6 offset:40960\n"
    "ds_read_b64_tr_b16 a[178:179], v6 offset:41472\n"
    "ds_read_b64_tr_b16 a[180:181], v6 offset:40968\n"
    "ds_read_b64_tr_b16 a[182:183], v6 offset:41480\n"
    "ds_read_b64_tr_b16 a[184:185], v6 offset:24576\n"
    "ds_read_b64_tr_b16 a[186:187], v6 offset:25088\n"
    "ds_read_b64_tr_b16 a[188:189], v6 offset:24584\n"
    "ds_read_b64_tr_b16 a[190:191], v6 offset:25096\n"
    "s_waitcnt vmcnt(24) lgkmcnt(0)\n"
    "s_barrier\n"
    "s_add_u32 m0, s59, 24576\n"
    "s_mov_b32 s74, 3407872\n"
    "buffer_load_dwordx4 v5, s[40:43], s74 offen lds\n"
    "s_add_u32 m0, s59, 28672\n"
    "s_mov_b32 s74, 3408000\n"
    "buffer_load_dwordx4 v5, s[40:43], s74 offen lds\n"
    "s_add_u32 m0, s59, 32768\n"
    "s_mov_b32 s74, 3407872\n"
    "buffer_load_dwordx4 v5, s[44:47], s74 offen lds\n"
    "s_add_u32 m0, s59, 36864\n"
    "s_mov_b32 s74, 3408000\n"
    "buffer_load_dwordx4 v5, s[44:47], s74 offen lds\n"
    "s_add_u32 m0, s59, 40960\n"
    "s_mov_b32 s74, 3407872\n"
    "buffer_load_dwordx4 v5, s[48:51], s74 offen lds\n"
    "s_add_u32 m0, s59, 45056\n"
    "s_mov_b32 s74, 3408000\n"
    "buffer_load_dwordx4 v5, s[48:51], s74 offen lds\n"
    "ds_read_b64_tr_b16 a[192:193], v16 offset:8192\n"
    "ds_read_b64_tr_b16 a[194:195], v16 offset:8704\n"
    "ds_read_b64_tr_b16 a[196:197], v16 offset:8200\n"
    "ds_read_b64_tr_b16 a[198:199], v16 offset:8712\n"
    "ds_read_b64_tr_b16 a[200:201], v16 offset:16384\n"
    "ds_read_b64_tr_b16 a[202:203], v16 offset:16896\n"
    "ds_read_b64_tr_b16 a[204:205], v16 offset:16392\n"
    "ds_read_b64_tr_b16 a[206:207], v16 offset:16904\n"
    "ds_read_b64_tr_b16 a[208:209], v16 offset:0\n"
    "ds_read_b64_tr_b16 a[210:211], v16 offset:512\n"
    "ds_read_b64_tr_b16 a[212:213], v16 offset:8\n"
    "ds_read_b64_tr_b16 a[214:215], v16 offset:520\n"
    "s_waitcnt vmcnt(24) lgkmcnt(0)\n"
    "s_barrier\n"
    "s_add_u32 m0, s59, 49152\n"
    "s_mov_b32 s74, 3670016\n"
    "buffer_load_dwordx4 v5, s[40:43], s74 offen lds\n"
    "s_add_u32 m0, s59, 53248\n"
    "s_mov_b32 s74, 3670144\n"
    "buffer_load_dwordx4 v5, s[40:43], s74 offen lds\n"
    "s_add_u32 m0, s59, 57344\n"
    "s_mov_b32 s74, 3670016\n"
    "buffer_load_dwordx4 v5, s[44:47], s74 offen lds\n"
    "s_add_u32 m0, s59, 61440\n"
    "s_mov_b32 s74, 3670144\n"
    "buffer_load_dwordx4 v5, s[44:47], s74 offen lds\n"
    "s_add_u32 m0, s59, 65536\n"
    "s_mov_b32 s74, 3670016\n"
    "buffer_load_dwordx4 v5, s[48:51], s74 offen lds\n"
    "s_add_u32 m0, s59, 69632\n"
    "s_mov_b32 s74, 3670144\n"
    "buffer_load_dwordx4 v5, s[48:51], s74 offen lds\n"
    "ds_read_b64_tr_b16 a[216:217], v16 offset:32768\n"
    "ds_read_b64_tr_b16 a[218:219], v16 offset:33280\n"
    "ds_read_b64_tr_b16 a[220:221], v16 offset:32776\n"
    "ds_read_b64_tr_b16 a[222:223], v16 offset:33288\n"
    "ds_read_b64_tr_b16 a[224:225], v16 offset:40960\n"
    "ds_read_b64_tr_b16 a[226:227], v16 offset:41472\n"
    "ds_read_b64_tr_b16 a[228:229], v16 offset:40968\n"
    "ds_read_b64_tr_b16 a[230:231], v16 offset:41480\n"
    "ds_read_b64_tr_b16 a[232:233], v16 offset:24576\n"
    "ds_read_b64_tr_b16 a[234:235], v16 offset:25088\n"
    "ds_read_b64_tr_b16 a[236:237], v16 offset:24584\n"
    "ds_read_b64_tr_b16 a[238:239], v16 offset:25096\n"
    "s_waitcnt vmcnt(24) lgkmcnt(0)\n"
    "s_barrier\n"
    "s_add_u32 m0, s59, 73728\n"
    "s_mov_b32 s74, 3932160\n"
    "buffer_load_dwordx4 v5, s[40:43], s74 offen lds\n"
    "s_add_u32 m0, s59, 77824\n"
    "s_mov_b32 s74, 3932288\n"
    "buffer_load_dwordx4 v5, s[40:43], s74 offen lds\n"
    "s_add_u32 m0, s59, 81920\n"
    "s_mov_b32 s74, 3932160\n"
    "buffer_load_dwordx4 v5, s[44:47], s74 offen lds\n"
    "s_add_u32 m0, s59, 86016\n"
    "s_mov_b32 s74, 3932288\n"
    "buffer_load_dwordx4 v5, s[44:47], s74 offen lds\n"
    "s_add_u32 m0, s59, 90112\n"
    "s_mov_b32 s74, 3932160\n"
    "buffer_load_dwordx4 v5, s[48:51], s74 offen lds\n"
    "s_add_u32 m0, s59, 94208\n"
    "s_mov_b32 s74, 3932288\n"
    "buffer_load_dwordx4 v5, s[48:51], s74 offen lds\n"
    "ds_read_b64_tr_b16 a[240:241], v17 offset:8192\n"
    "ds_read_b64_tr_b16 a[242:243], v17 offset:8704\n"
    "ds_read_b64_tr_b16 a[244:245], v17 offset:8200\n"
    "ds_read_b64_tr_b16 a[246:247], v17 offset:8712\n"
    "ds_read_b64_tr_b16 a[248:249], v17 offset:16384\n"
    "ds_read_b64_tr_b16 a[250:251], v17 offset:16896\n"
    "ds_read_b64_tr_b16 a[252:253], v17 offset:16392\n"
    "ds_read_b64_tr_b16 a[254:255], v17 offset:16904\n"
    "ds_read_b64_tr_b16 v[80:81], v17 offset:0\n"
    "ds_read_b64_tr_b16 v[82:83], v17 offset:512\n"
    "ds_read_b64_tr_b16 v[84:85], v17 offset:8\n"
    "ds_read_b64_tr_b16 v[86:87], v17 offset:520\n"
    "s_waitcnt vmcnt(24) lgkmcnt(0)\n"
    "s_barrier\n"
    "s_add_u32 m0, s59, 98304\n"
    "s_mov_b32 s74, 4194304\n"
    "buffer_load_dwordx4 v5, s[40:43], s74 offen lds\n"
    "s_add_u32 m0, s59, 102400\n"
    "s_mov_b32 s74, 4194432\n"
    "buffer_load_dwordx4 v5, s[40:43], s74 offen lds\n"
    "s_add_u32 m0, s59, 106496\n"
    "s_mov_b32 s74, 4194304\n"
    "buffer_load_dwordx4 v5, s[44:47], s74 offen lds\n"
    "s_add_u32 m0, s59, 110592\n"
    "s_mov_b32 s74, 4194432\n"
    "buffer_load_dwordx4 v5, s[44:47], s74 offen lds\n"
    "s_add_u32 m0, s59, 114688\n"
    "s_mov_b32 s74, 4194304\n"
    "buffer_load_dwordx4 v5, s[48:51], s74 offen lds\n"
    "s_add_u32 m0, s59, 118784\n"
    "s_mov_b32 s74, 4194432\n"
    "buffer_load_dwordx4 v5, s[48:51], s74 offen lds\n"
    "ds_read_b64_tr_b16 v[88:89], v17 offset:32768\n"
    "ds_read_b64_tr_b16 v[90:91], v17 offset:33280\n"
    "ds_read_b64_tr_b16 v[92:93], v17 offset:32776\n"
    "ds_read_b64_tr_b16 v[94:95], v17 offset:33288\n"
    "ds_read_b64_tr_b16 v[96:97], v17 offset:40960\n"
    "ds_read_b64_tr_b16 v[98:99], v17 offset:41472\n"
    "ds_read_b64_tr_b16 v[100:101], v17 offset:40968\n"
    "ds_read_b64_tr_b16 v[102:103], v17 offset:41480\n"
    "ds_read_b64_tr_b16 v[104:105], v17 offset:24576\n"
    "ds_read_b64_tr_b16 v[106:107], v17 offset:25088\n"
    "ds_read_b64_tr_b16 v[108:109], v17 offset:24584\n"
    "ds_read_b64_tr_b16 v[110:111], v17 offset:25096\n"
    "s_waitcnt vmcnt(24) lgkmcnt(0)\n"
    "s_barrier\n"
    "s_add_u32 m0, s59, 122880\n"
    "s_mov_b32 s74, 4456448\n"
    "buffer_load_dwordx4 v5, s[40:43], s74 offen lds\n"
    "s_add_u32 m0, s59, 126976\n"
    "s_mov_b32 s74, 4456576\n"
    "buffer_load_dwordx4 v5, s[40:43], s74 offen lds\n"
    "s_add_u32 m0, s59, 131072\n"
    "s_mov_b32 s74, 4456448\n"
    "buffer_load_dwordx4 v5, s[44:47], s74 offen lds\n"
    "s_add_u32 m0, s59, 135168\n"
    "s_mov_b32 s74, 4456576\n"
    "buffer_load_dwordx4 v5, s[44:47], s74 offen lds\n"
    "s_add_u32 m0, s59, 139264\n"
    "s_mov_b32 s74, 4456448\n"
    "buffer_load_dwordx4 v5, s[48:51], s74 offen lds\n"
    "s_add_u32 m0, s59, 143360\n"
    "s_mov_b32 s74, 4456576\n"
    "buffer_load_dwordx4 v5, s[48:51], s74 offen lds\n"
    "ds_read_b64_tr_b16 v[112:113], v6 offset:8192\n"
    "ds_read_b64_tr_b16 v[114:115], v6 offset:8704\n"
    "ds_read_b64_tr_b16 v[116:117], v6 offset:8200\n"
    "ds_read_b64_tr_b16 v[118:119], v6 offset:8712\n"
    "ds_read_b64_tr_b16 v[120:121], v6 offset:16384\n"
    "ds_read_b64_tr_b16 v[122:123], v6 offset:16896\n"
    "ds_read_b64_tr_b16 v[124:125], v6 offset:16392\n"
    "ds_read_b64_tr_b16 v[126:127], v6 offset:16904\n"
    "ds_read_b64_tr_b16 v[128:129], v6 offset:0\n"
    "ds_read_b64_tr_b16 v[130:131], v6 offset:512\n"
    "ds_read_b64_tr_b16 v[132:133], v6 offset:8\n"
    "ds_read_b64_tr_b16 v[134:135], v6 offset:520\n"
    "s_waitcnt vmcnt(24) lgkmcnt(0)\n"
    "s_barrier\n"
    "ds_read_b64_tr_b16 v[136:137], v6 offset:32768\n"
    "ds_read_b64_tr_b16 v[138:139], v6 offset:33280\n"
    "ds_read_b64_tr_b16 v[140:141], v6 offset:32776\n"
    "ds_read_b64_tr_b16 v[142:143], v6 offset:33288\n"
    "ds_read_b64_tr_b16 v[144:145], v6 offset:40960\n"
    "ds_read_b64_tr_b16 v[146:147], v6 offset:41472\n"
    "ds_read_b64_tr_b16 v[148:149], v6 offset:40968\n"
    "ds_read_b64_tr_b16 v[150:151], v6 offset:41480\n"
    "ds_read_b64_tr_b16 v[152:153], v6 offset:24576\n"
    "ds_read_b64_tr_b16 v[154:155], v6 offset:25088\n"
    "ds_read_b64_tr_b16 v[156:157], v6 offset:24584\n"
    "ds_read_b64_tr_b16 v[158:159], v6 offset:25096\n"
    "s_waitcnt vmcnt(18) lgkmcnt(0)\n"
    "s_barrier\n"
    "ds_read_b64_tr_b16 v[160:161], v16 offset:8192\n"
    "ds_read_b64_tr_b16 v[162:163], v16 offset:8704\n"
    "ds_read_b64_tr_b16 v[164:165], v16 offset:8200\n"
    "ds_read_b64_tr_b16 v[166:167], v16 offset:8712\n"
    "ds_read_b64_tr_b16 v[168:169], v16 offset:16384\n"
    "ds_read_b64_tr_b16 v[170:171], v16 offset:16896\n"
    "ds_read_b64_tr_b16 v[172:173], v16 offset:16392\n"
    "ds_read_b64_tr_b16 v[174:175], v16 offset:16904\n"
    "ds_read_b64_tr_b16 v[176:177], v16 offset:0\n"
    "ds_read_b64_tr_b16 v[178:179], v16 offset:512\n"
    "ds_read_b64_tr_b16 v[180:181], v16 offset:8\n"
    "ds_read_b64_tr_b16 v[182:183], v16 offset:520\n"
    "s_waitcnt vmcnt(12) lgkmcnt(0)\n"
    "s_barrier\n"
    "ds_read_b64_tr_b16 v[184:185], v16 offset:32768\n"
    "ds_read_b64_tr_b16 v[186:187], v16 offset:33280\n"
    "ds_read_b64_tr_b16 v[188:189], v16 offset:32776\n"
    "ds_read_b64_tr_b16 v[190:191], v16 offset:33288\n"
    "ds_read_b64_tr_b16 v[192:193], v16 offset:40960\n"
    "ds_read_b64_tr_b16 v[194:195], v16 offset:41472\n"
    "ds_read_b64_tr_b16 v[196:197], v16 offset:40968\n"
    "ds_read_b64_tr_b16 v[198:199], v16 offset:41480\n"
    "ds_read_b64_tr_b16 v[200:201], v16 offset:24576\n"
    "ds_read_b64_tr_b16 v[202:203], v16 offset:25088\n"
    "ds_read_b64_tr_b16 v[204:205], v16 offset:24584\n"
    "ds_read_b64_tr_b16 v[206:207], v16 offset:25096\n"
    "s_waitcnt vmcnt(6) lgkmcnt(0)\n"
    "s_barrier\n"
    "ds_read_b64_tr_b16 v[208:209], v17 offset:8192\n"
    "ds_read_b64_tr_b16 v[210:211], v17 offset:8704\n"
    "ds_read_b64_tr_b16 v[212:213], v17 offset:8200\n"
    "ds_read_b64_tr_b16 v[214:215], v17 offset:8712\n"
    "ds_read_b64_tr_b16 v[216:217], v17 offset:16384\n"
    "ds_read_b64_tr_b16 v[218:219], v17 offset:16896\n"
    "ds_read_b64_tr_b16 v[220:221], v17 offset:16392\n"
    "ds_read_b64_tr_b16 v[222:223], v17 offset:16904\n"
    "ds_read_b64_tr_b16 v[224:225], v17 offset:0\n"
    "ds_read_b64_tr_b16 v[226:227], v17 offset:512\n"
    "ds_read_b64_tr_b16 v[228:229], v17 offset:8\n"
    "ds_read_b64_tr_b16 v[230:231], v17 offset:520\n"
    "s_waitcnt vmcnt(0) lgkmcnt(0)\n"
    "s_barrier\n"
    "ds_read_b64_tr_b16 v[232:233], v17 offset:32768\n"
    "ds_read_b64_tr_b16 v[234:235], v17 offset:33280\n"
    "ds_read_b64_tr_b16 v[236:237], v17 offset:32776\n"
    "ds_read_b64_tr_b16 v[238:239], v17 offset:33288\n"
    "ds_read_b64_tr_b16 v[240:241], v17 offset:40960\n"
    "ds_read_b64_tr_b16 v[242:243], v17 offset:41472\n"
    "ds_read_b64_tr_b16 v[244:245], v17 offset:40968\n"
    "ds_read_b64_tr_b16 v[246:247], v17 offset:41480\n"
    "ds_read_b64_tr_b16 v[248:249], v17 offset:24576\n"
    "ds_read_b64_tr_b16 v[250:251], v17 offset:25088\n"
    "ds_read_b64_tr_b16 v[252:253], v17 offset:24584\n"
    "ds_read_b64_tr_b16 v[254:255], v17 offset:25096\n"
    "s_waitcnt lgkmcnt(0)\n"
    "s_barrier\n"
    "s_mov_b32 s68, 0\n"
    "s_mov_b32 s71, 0\n"
    "v_mov_b32 v11, -1\n"
    "s_mov_b32 s72, 262144\n"
    "s_branch .LgP_block_%=\n"
    ".LgP_LOOPB_%=:\n"
    ".LgP_top_%=:\n"
    "s_waitcnt vmcnt(6)\n"
    ".LgP_bar_%=:\n"
    "s_and_b32 s78, s68, 1\n"
    "s_mul_i32 s78, s78, 36864\n"
    "v_add_u32 v9, s78, v0\n"
    "s_add_u32 s71, s68, 1\n"
    "s_cmp_eq_u32 s71, 64\n"
    "s_cselect_b32 s70, 1, 0\n"
    "s_barrier\n"
    "ds_read_b128 v[16:19], v9 offset:0\n"
    "ds_read_b128 v[20:23], v9 offset:1024\n"
    "v_add_u32 v13, v11, v3\n"
    "v_cmp_gt_i32 vcc, s71, v13\n"
    "s_nop 0\n"
    "s_or_b64 exec, vcc, 1\n"
    "buffer_atomic_smax v11, v2, s[36:39], 0 offen sc0 sc1\n"
    "s_mov_b64 exec, -1\n"
    "s_and_b32 s73, s68, s57\n"
    "s_mul_i32 s73, s73, s56\n"
    "s_add_u32 s73, s73, s55\n"
    "s_add_u32 s79, s73, 8192\n"
    "s_add_u32 s80, s73, 16384\n"
    "s_waitcnt lgkmcnt(0)\n"
    "v_mfma_f32_16x16x32_bf16 v[32:35], a[0:3], v[16:19], 0\n"
    "ds_read_b128 v[24:27], v9 offset:2048\n"
    "ds_read_b128 v[28:31], v9 offset:3072\n"
    "v_mfma_f32_16x16x32_bf16 v[40:43], a[0:3], v[20:23], 0\n"
    "v_mfma_f32_16x16x32_bf16 v[36:39], a[4:7], v[16:19], 0\n"
    "v_mfma_f32_16x16x32_bf16 v[44:47], a[4:7], v[20:23], 0\n"
    "v_mfma_f32_16x16x32_bf16 v[48:51], v[16:19], a[8:11], 0\n"
    "v_mfma_f32_16x16x32_bf16 v[52:55], v[20:23], a[8:11], 0\n"
    "v_mfma_f32_16x16x32_bf16 v[56:59], v[16:19], a[12:15], 0\n"
    "v_mfma_f32_16x16x32_bf16 v[60:63], v[20:23], a[12:15], 0\n"
    "v_mfma_f32_16x16x32_bf16 v[64:67], a[16:19], v[16:19], 0\n"
    "v_mfma_f32_16x16x32_bf16 v[72:75], a[16:19], v[20:23], 0\n"
    "v_mfma_f32_16x16x32_bf16 v[68:71], a[20:23], v[16:19], 0\n"
    "v_mfma_f32_16x16x32_bf16 v[76:79], a[20:23], v[20:23], 0\n"
    "s_waitcnt lgkmcnt(0)\n"
    "v_mfma_f32_16x16x32_bf16 v[32:35], a[24:27], v[24:27], v[32:35]\n"
    "ds_read_b128 v[16:19], v9 offset:4096\n"
    "ds_read_b128 v[20:23], v9 offset:5120\n"
    "v_mfma_f32_16x16x32_bf16 v[40:43], a[24:27], v[28:31], v[40:43]\n"
    "v_mfma_f32_16x16x32_bf16 v[36:39], a[28:31], v[24:27], v[36:39]\n"
    "v_mfma_f32_16x16x32_bf16 v[44:47], a[28:31], v[28:31], v[44:47]\n"
    "v_mfma_f32_16x16x32_bf16 v[48:51], v[24:27], a[32:35], v[48:51]\n"
    "v_mfma_f32_16x16x32_bf16 v[52:55], v[28:31], a[32:35], v[52:55]\n"
    "v_mfma_f32_16x16x32_bf16 v[56:59], v[24:27], a[36:39], v[56:59]\n"
    "v_mfma_f32_16x16x32_bf16 v[60:63], v[28:31], a[36:39], v[60:63]\n"
    "v_mfma_f32_16x16x32_bf16 v[64:67], a[40:43], v[24:27], v[64:67]\n"
    "v_mfma_f32_16x16x32_bf16 v[72:75], a[40:43], v[28:31], v[72:75]\n"
    "v_mfma_f32_16x16x32_bf16 v[68:71], a[44:47], v[24:27], v[68:71]\n"
    "v_mfma_f32_16x16x32_bf16 v[76:79], a[44:47], v[28:31], v[76:79]\n"
    "s_waitcnt lgkmcnt(0)\n"
    "v_mfma_f32_16x16x32_bf16 v[32:35], a[48:51], v[16:19], v[32:35]\n"
    "ds_read_b128 v[24:27], v9 offset:6144\n"
    "ds_read_b128 v[28:31], v9 offset:7168\n"
    "v_mfma_f32_16x16x32_bf16 v[40:43], a[48:51], v[20:23], v[40:43]\n"
    "v_mfma_f32_16x16x32_bf16 v[36:39], a[52:55], v[16:19], v[36:39]\n"
    "v_mfma_f32_16x16x32_bf16 v[44:47], a[52:55], v[20:23], v[44:47]\n"
    "v_mfma_f32_16x16x32_bf16 v[48:51], v[16:19], a[56:59], v[48:51]\n"
    "v_mfma_f32_16x16x32_bf16 v[52:55], v[20:23], a[56:59], v[52:55]\n"
    "v_mfma_f32_16x16x32_bf16 v[56:59], v[16:19], a[60:63], v[56:59]\n"
    "v_mfma_f32_16x16x32_bf16 v[60:63], v[20:23], a[60:63], v[60:63]\n"
    "v_mfma_f32_16x16x32_bf16 v[64:67], a[64:67], v[16:19], v[64:67]\n"
    "v_mfma_f32_16x16x32_bf16 v[72:75], a[64:67], v[20:23], v[72:75]\n"
    "v_mfma_f32_16x16x32_bf16 v[68:71], a[68:71], v[16:19], v[68:71]\n"
    "v_mfma_f32_16x16x32_bf16 v[76:79], a[68:71], v[20:23], v[76:79]\n"
    "s_waitcnt vmcnt(0)\n"
    "s_cmp_eq_u32 s69, 0\n"
    "s_cbranch_scc1 .LgP_nopub_%=\n"
    "s_sub_u32 s74, s68, 1\n"
    "v_mov_b32 v12, s74\n"
    "s_mov_b64 exec, s[62:63]\n"
    "buffer_store_dword v12, v14, s[36:39], 0 offen sc1\n"
    "s_mov_b64 exec, -1\n"
    ".LgP_nopub_%=:\n"
    "s_cmp_lg_u32 s70, 0\n"
    "s_cbranch_scc1 .LgP_plain0_%=\n"
    "v_add_u32 v13, v11, v3\n"
    "v_cmp_gt_i32 vcc, s71, v13\n"
    "s_cbranch_vccz .LgP_dma0_%=\n"
    "s_or_b64 exec, vcc, 1\n"
    "buffer_atomic_smax v11, v2, s[36:39], 0 offen sc0 sc1\n"
    "s_mov_b64 exec, -1\n"
    ".LgP_plain0_%=:\n"
    "s_waitcnt lgkmcnt(0)\n"
    "v_mfma_f32_16x16x32_bf16 v[32:35], a[72:75], v[24:27], v[32:35]\n"
    "ds_read_b128 v[16:19], v9 offset:8192\n"
    "ds_read_b128 v[20:23], v9 offset:9216\n"
    "v_mfma_f32_16x16x32_bf16 v[40:43], a[72:75], v[28:31], v[40:43]\n"
    "v_mfma_f32_16x16x32_bf16 v[36:39], a[76:79], v[24:27], v[36:39]\n"
    "v_mfma_f32_16x16x32_bf16 v[44:47], a[76:79], v[28:31], v[44:47]\n"
    "v_mfma_f32_16x16x32_bf16 v[48:51], v[24:27], a[80:83], v[48:51]\n"
    "v_mfma_f32_16x16x32_bf16 v[52:55], v[28:31], a[80:83], v[52:55]\n"
    "v_mfma_f32_16x16x32_bf16 v[56:59], v[24:27], a[84:87], v[56:59]\n"
    "v_mfma_f32_16x16x32_bf16 v[60:63], v[28:31], a[84:87], v[60:63]\n"
    "v_mfma_f32_16x16x32_bf16 v[64:67], a[88:91], v[24:27], v[64:67]\n"
    "v_mfma_f32_16x16x32_bf16 v[72:75], a[88:91], v[28:31], v[72:75]\n"
    "v_mfma_f32_16x16x32_bf16 v[68:71], a[92:95], v[24:27], v[68:71]\n"
    "v_mfma_f32_16x16x32_bf16 v[76:79], a[92:95], v[28:31], v[76:79]\n"
    "s_waitcnt lgkmcnt(0)\n"
    "v_mfma_f32_16x16x32_bf16 v[32:35], a[96:99], v[16:19], v[32:35]\n"
    "ds_read_b128 v[24:27], v9 offset:10240\n"
    "ds_read_b128 v[28:31], v9 offset:11264\n"
    "v_mfma_f32_16x16x32_bf16 v[40:43], a[96:99], v[20:23], v[40:43]\n"
    "v_mfma_f32_16x16x32_bf16 v[36:39], a[100:103], v[16:19], v[36:39]\n"
    "v_mfma_f32_16x16x32_bf16 v[44:47], a[100:103], v[20:23], v[44:47]\n"
    "v_mfma_f32_16x16x32_bf16 v[48:51], v[16:19], a[104:107], v[48:51]\n"
    "v_mfma_f32_16x16x32_bf16 v[52:55], v[20:23], a[104:107], v[52:55]\n"
    "v_mfma_f32_16x16x32_bf16 v[56:59], v[16:19], a[108:111], v[56:59]\n"
    "v_mfma_f32_16x16x32_bf16 v[60:63], v[20:23], a[108:111], v[60:63]\n"
    "v_mfma_f32_16x16x32_bf16 v[64:67], a[112:115], v[16:19], v[64:67]\n"
    "v_mfma_f32_16x16x32_bf16 v[72:75], a[112:115], v[20:23], v[72:75]\n"
    "v_mfma_f32_16x16x32_bf16 v[68:71], a[116:119], v[16:19], v[68:71]\n"
    "v_mfma_f32_16x16x32_bf16 v[76:79], a[116:119], v[20:23], v[76:79]\n"
    "s_waitcnt lgkmcnt(0)\n"
    "v_mfma_f32_16x16x32_bf16 v[32:35], a[120:123], v[24:27], v[32:35]\n"
    "ds_read_b128 v[16:19], v9 offset:12288\n"
    "ds_read_b128 v[20:23], v9 offset:13312\n"
    "v_mfma_f32_16x16x32_bf16 v[40:43], a[120:123], v[28:31], v[40:43]\n"
    "v_mfma_f32_16x16x32_bf16 v[36:39], a[124:127], v[24:27], v[36:39]\n"
    "v_mfma_f32_16x16x32_bf16 v[44:47], a[124:127], v[28:31], v[44:47]\n"
    "v_mfma_f32_16x16x32_bf16 v[48:51], v[24:27], a[128:131], v[48:51]\n"
    "v_mfma_f32_16x16x32_bf16 v[52:55], v[28:31], a[128:131], v[52:55]\n"
    "v_mfma_f32_16x16x32_bf16 v[56:59], v[24:27], a[132:135], v[56:59]\n"
    "v_mfma_f32_16x16x32_bf16 v[60:63], v[28:31], a[132:135], v[60:63]\n"
    "v_mfma_f32_16x16x32_bf16 v[64:67], a[136:139], v[24:27], v[64:67]\n"
    "v_mfma_f32_16x16x32_bf16 v[72:75], a[136:139], v[28:31], v[72:75]\n"
    "v_mfma_f32_16x16x32_bf16 v[68:71], a[140:143], v[24:27], v[68:71]\n"
    "v_mfma_f32_16x16x32_bf16 v[76:79], a[140:143], v[28:31], v[76:79]\n"
    "s_waitcnt lgkmcnt(0)\n"
    "v_mfma_f32_16x16x32_bf16 v[32:35], a[144:147], v[16:19], v[32:35]\n"
    "ds_read_b128 v[24:27], v9 offset:14336\n"
    "ds_read_b128 v[28:31], v9 offset:15360\n"
    "v_mfma_f32_16x16x32_bf16 v[40:43], a[144:147], v[20:23], v[40:43]\n"
    "v_mfma_f32_16x16x32_bf16 v[36:39], a[148:151], v[16:19], v[36:39]\n"
    "v_mfma_f32_16x16x32_bf16 v[44:47], a[148:151], v[20:23], v[44:47]\n"
    "v_mfma_f32_16x16x32_bf16 v[48:51], v[16:19], a[152:155], v[48:51]\n"
    "v_mfma_f32_16x16x32_bf16 v[52:55], v[20:23], a[152:155], v[52:55]\n"
    "v_mfma_f32_16x16x32_bf16 v[56:59], v[16:19], a[156:159], v[56:59]\n"
    "v_mfma_f32_16x16x32_bf16 v[60:63], v[20:23], a[156:159], v[60:63]\n"
    "v_mfma_f32_16x16x32_bf16 v[64:67], a[160:163], v[16:19], v[64:67]\n"
    "v_mfma_f32_16x16x32_bf16 v[72:75], a[160:163], v[20:23], v[72:75]\n"
    "v_mfma_f32_16x16x32_bf16 v[68:71], a[164:167], v[16:19], v[68:71]\n"
    "v_mfma_f32_16x16x32_bf16 v[76:79], a[164:167], v[20:23], v[76:79]\n"
    ".LgP_seg0_%=:\n"
    "s_cmp_lg_u32 s70, 0\n"
    "s_cbranch_scc1 .LgP_plain1_%=\n"
    "s_waitcnt vmcnt(0)\n"
    "v_add_u32 v13, v11, v3\n"
    "v_cmp_gt_i32 vcc, s71, v13\n"
    "s_cbranch_vccz .LgP_dma1_%=\n"
    "s_or_b64 exec, vcc, 1\n"
    "buffer_atomic_smax v11, v2, s[36:39], 0 offen sc0 sc1\n"
    "s_mov_b64 exec, -1\n"
    ".LgP_plain1_%=:\n"
    "s_waitcnt lgkmcnt(0)\n"
    "v_mfma_f32_16x16x32_bf16 v[32:35], a[168:171], v[24:27], v[32:35]\n"
    "ds_read_b128 v[16:19], v9 offset:16384\n"
    "ds_read_b128 v[20:23], v9 offset:17408\n"
    "v_mfma_f32_16x16x32_bf16 v[40:43], a[168:171], v[28:31], v[40:43]\n"
    "v_mfma_f32_16x16x32_bf16 v[36:39], a[172:175], v[24:27], v[36:39]\n"
    "v_mfma_f32_16x16x32_bf16 v[44:47], a[172:175], v[28:31], v[44:47]\n"
    "v_mfma_f32_16x16x32_bf16 v[48:51], v[24:27], a[176:179], v[48:51]\n"
    "v_mfma_f32_16x16x32_bf16 v[52:55], v[28:31], a[176:179], v[52:55]\n"
    "v_mfma_f32_16x16x32_bf16 v[56:59], v[24:27], a[180:183], v[56:59]\n"
    "v_mfma_f32_16x16x32_bf16 v[60:63], v[28:31], a[180:183], v[60:63]\n"
    "v_mfma_f32_16x16x32_bf16 v[64:67], a[184:187], v[24:27], v[64:67]\n"
    "v_mfma_f32_16x16x32_bf16 v[72:75], a[184:187], v[28:31], v[72:75]\n"
    "v_mfma_f32_16x16x32_bf16 v[68:71], a[188:191], v[24:27], v[68:71]\n"
    "v_mfma_f32_16x16x32_bf16 v[76:79], a[188:191], v[28:31], v[76:79]\n"
    "s_waitcnt lgkmcnt(0)\n"
    "v_mfma_f32_16x16x32_bf16 v[32:35], a[192:195], v[16:19], v[32:35]\n"
    "ds_read_b128 v[24:27], v9 offset:18432\n"
    "ds_read_b128 v[28:31], v9 offset:19456\n"
    "v_mfma_f32_16x16x32_bf16 v[40:43], a[192:195], v[20:23], v[40:43]\n"
    "v_mfma_f32_16x16x32_bf16 v[36:39], a[196:199], v[16:19], v[36:39]\n"
    "v_mfma_f32_16x16x32_bf16 v[44:47], a[196:199], v[20:23], v[44:47]\n"
    "v_mfma_f32_16x16x32_bf16 v[48:51], v[16:19], a[200:203], v[48:51]\n"
    "v_mfma_f32_16x16x32_bf16 v[52:55], v[20:23], a[200:203], v[52:55]\n"
    "v_mfma_f32_16x16x32_bf16 v[56:59], v[16:19], a[204:207], v[56:59]\n"
    "v_mfma_f32_16x16x32_bf16 v[60:63], v[20:23], a[204:207], v[60:63]\n"
    "v_mfma_f32_16x16x32_bf16 v[64:67], a[208:211], v[16:19], v[64:67]\n"
    "v_mfma_f32_16x16x32_bf16 v[72:75], a[208:211], v[20:23], v[72:75]\n"
    "v_mfma_f32_16x16x32_bf16 v[68:71], a[212:215], v[16:19], v[68:71]\n"
    "v_mfma_f32_16x16x32_bf16 v[76:79], a[212:215], v[20:23], v[76:79]\n"
    "s_waitcnt lgkmcnt(0)\n"
    "v_mfma_f32_16x16x32_bf16 v[32:35], a[216:219], v[24:27], v[32:35]\n"
    "ds_read_b128 v[16:19], v9 offset:20480\n"
    "ds_read_b128 v[20:23], v9 offset:21504\n"
    "v_mfma_f32_16x16x32_bf16 v[40:43], a[216:219], v[28:31], v[40:43]\n"
    "v_mfma_f32_16x16x32_bf16 v[36:39], a[220:223], v[24:27], v[36:39]\n"
    "v_mfma_f32_16x16x32_bf16 v[44:47], a[220:223], v[28:31], v[44:47]\n"
    "v_mfma_f32_16x16x32_bf16 v[48:51], v[24:27], a[224:227], v[48:51]\n"
    "v_mfma_f32_16x16x32_bf16 v[52:55], v[28:31], a[224:227], v[52:55]\n"
    "v_mfma_f32_16x16x32_bf16 v[56:59], v[24:27], a[228:231], v[56:59]\n"
    "v_mfma_f32_16x16x32_bf16 v[60:63], v[28:31], a[228:231], v[60:63]\n"
    "v_mfma_f32_16x16x32_bf16 v[64:67], a[232:235], v[24:27], v[64:67]\n"
    "v_mfma_f32_16x16x32_bf16 v[72:75], a[232:235], v[28:31], v[72:75]\n"
    "v_mfma_f32_16x16x32_bf16 v[68:71], a[236:239], v[24:27], v[68:71]\n"
    "v_mfma_f32_16x16x32_bf16 v[76:79], a[236:239], v[28:31], v[76:79]\n"
    "s_waitcnt lgkmcnt(0)\n"
    "v_mfma_f32_16x16x32_bf16 v[32:35], a[240:243], v[16:19], v[32:35]\n"
    "ds_read_b128 v[24:27], v9 offset:22528\n"
    "ds_read_b128 v[28:31], v9 offset:23552\n"
    "v_mfma_f32_16x16x32_bf16 v[40:43], a[240:243], v[20:23], v[40:43]\n"
    "v_mfma_f32_16x16x32_bf16 v[36:39], a[244:247], v[16:19], v[36:39]\n"
    "v_mfma_f32_16x16x32_bf16 v[44:47], a[244:247], v[20:23], v[44:47]\n"
    "v_mfma_f32_16x16x32_bf16 v[48:51], v[16:19], a[248:251], v[48:51]\n"
    "v_mfma_f32_16x16x32_bf16 v[52:55], v[20:23], a[248:251], v[52:55]\n"
    "v_mfma_f32_16x16x32_bf16 v[56:59], v[16:19], a[252:255], v[56:59]\n"
    "v_mfma_f32_16x16x32_bf16 v[60:63], v[20:23], a[252:255], v[60:63]\n"
    "v_mfma_f32_16x16x32_bf16 v[64:67], v[80:83], v[16:19], v[64:67]\n"
    "v_mfma_f32_16x16x32_bf16 v[72:75], v[80:83], v[20:23], v[72:75]\n"
    "v_mfma_f32_16x16x32_bf16 v[68:71], v[84:87], v[16:19], v[68:71]\n"
    "v_mfma_f32_16x16x32_bf16 v[76:79], v[84:87], v[20:23], v[76:79]\n"
    ".LgP_seg1_%=:\n"
    "s_cmp_lg_u32 s70, 0\n"
    "s_cbranch_scc1 .LgP_plain2_%=\n"
    "s_waitcnt vmcnt(0)\n"
    "v_add_u32 v13, v11, v3\n"
    "v_cmp_gt_i32 vcc, s71, v13\n"
    "s_cbranch_vccz .LgP_dma2_%=\n"
    ".LgP_plain2_%=:\n"
    "s_waitcnt lgkmcnt(0)\n"
    "v_mfma_f32_16x16x32_bf16 v[32:35], v[88:91], v[24:27], v[32:35]\n"
    "ds_read_b128 v[16:19], v9 offset:24576\n"
    "ds_read_b128 v[20:23], v9 offset:25600\n"
    "v_mfma_f32_16x16x32_bf16 v[40:43], v[88:91], v[28:31], v[40:43]\n"
    "v_mfma_f32_16x16x32_bf16 v[36:39], v[92:95], v[24:27], v[36:39]\n"
    "v_mfma_f32_16x16x32_bf16 v[44:47], v[92:95], v[28:31], v[44:47]\n"
    "v_mfma_f32_16x16x32_bf16 v[48:51], v[24:27], v[96:99], v[48:51]\n"
    "v_mfma_f32_16x16x32_bf16 v[52:55], v[28:31], v[96:99], v[52:55]\n"
    "v_mfma_f32_16x16x32_bf16 v[56:59], v[24:27], v[100:103], v[56:59]\n"
    "v_mfma_f32_16x16x32_bf16 v[60:63], v[28:31], v[100:103], v[60:63]\n"
    "v_mfma_f32_16x16x32_bf16 v[64:67], v[104:107], v[24:27], v[64:67]\n"
    "v_mfma_f32_16x16x32_bf16 v[72:75], v[104:107], v[28:31], v[72:75]\n"
    "v_mfma_f32_16x16x32_bf16 v[68:71], v[108:111], v[24:27], v[68:71]\n"
    "v_mfma_f32_16x16x32_bf16 v[76:79], v[108:111], v[28:31], v[76:79]\n"
    "s_waitcnt lgkmcnt(0)\n"
    "v_mfma_f32_16x16x32_bf16 v[32:35], v[112:115], v[16:19], v[32:35]\n"
    "ds_read_b128 v[24:27], v9 offset:26624\n"
    "ds_read_b128 v[28:31], v9 offset:27648\n"
    "v_mfma_f32_16x16x32_bf16 v[40:43], v[112:115], v[20:23], v[40:43]\n"
    "v_mfma_f32_16x16x32_bf16 v[36:39], v[116:119], v[16:19], v[36:39]\n"
    "v_mfma_f32_16x16x32_bf16 v[44:47], v[116:119], v[20:23], v[44:47]\n"
    "v_mfma_f32_16x16x32_bf16 v[48:51], v[16:19], v[120:123], v[48:51]\n"
    "v_mfma_f32_16x16x32_bf16 v[52:55], v[20:23], v[120:123], v[52:55]\n"
    "v_mfma_f32_16x16x32_bf16 v[56:59], v[16:19], v[124:127], v[56:59]\n"
    "v_mfma_f32_16x16x32_bf16 v[60:63], v[20:23], v[124:127], v[60:63]\n"
    "v_mfma_f32_16x16x32_bf16 v[64:67], v[128:131], v[16:19], v[64:67]\n"
    "v_mfma_f32_16x16x32_bf16 v[72:75], v[128:131], v[20:23], v[72:75]\n"
    "v_mfma_f32_16x16x32_bf16 v[68:71], v[132:135], v[16:19], v[68:71]\n"
    "v_mfma_f32_16x16x32_bf16 v[76:79], v[132:135], v[20:23], v[76:79]\n"
    "s_waitcnt lgkmcnt(0)\n"
    "v_mfma_f32_16x16x32_bf16 v[32:35], v[136:139], v[24:27], v[32:35]\n"
    "ds_read_b128 v[16:19], v9 offset:28672\n"
    "ds_read_b128 v[20:23], v9 offset:29696\n"
    "v_mfma_f32_16x16x32_bf16 v[40:43], v[136:139], v[28:31], v[40:43]\n"
    "v_mfma_f32_16x16x32_bf16 v[36:39], v[140:143], v[24:27], v[36:39]\n"
    "v_mfma_f32_16x16x32_bf16 v[44:47], v[140:143], v[28:31], v[44:47]\n"
    "v_mfma_f32_16x16x32_bf16 v[48:51], v[24:27], v[144:147], v[48:51]\n"
    "v_mfma_f32_16x16x32_bf16 v[52:55], v[28:31], v[144:147], v[52:55]\n"
    "v_mfma_f32_16x16x32_bf16 v[56:59], v[24:27], v[148:151], v[56:59]\n"
    "v_mfma_f32_16x16x32_bf16 v[60:63], v[28:31], v[148:151], v[60:63]\n"
    "v_mfma_f32_16x16x32_bf16 v[64:67], v[152:155], v[24:27], v[64:67]\n"
    "v_mfma_f32_16x16x32_bf16 v[72:75], v[152:155], v[28:31], v[72:75]\n"
    "v_mfma_f32_16x16x32_bf16 v[68:71], v[156:159], v[24:27], v[68:71]\n"
    "v_mfma_f32_16x16x32_bf16 v[76:79], v[156:159], v[28:31], v[76:79]\n"
    "s_waitcnt lgkmcnt(0)\n"
    "v_mfma_f32_16x16x32_bf16 v[32:35], v[160:163], v[16:19], v[32:35]\n"
    "ds_read_b128 v[24:27], v9 offset:30720\n"
    "ds_read_b128 v[28:31], v9 offset:31744\n"
    "v_mfma_f32_16x16x32_bf16 v[40:43], v[160:163], v[20:23], v[40:43]\n"
    "v_mfma_f32_16x16x32_bf16 v[36:39], v[164:167], v[16:19], v[36:39]\n"
    "v_mfma_f32_16x16x32_bf16 v[44:47], v[164:167], v[20:23], v[44:47]\n"
    "v_mfma_f32_16x16x32_bf16 v[48:51], v[16:19], v[168:171], v[48:51]\n"
    "v_mfma_f32_16x16x32_bf16 v[52:55], v[20:23], v[168:171], v[52:55]\n"
    "v_mfma_f32_16x16x32_bf16 v[56:59], v[16:19], v[172:175], v[56:59]\n"
    "v_mfma_f32_16x16x32_bf16 v[60:63], v[20:23], v[172:175], v[60:63]\n"
    "v_mfma_f32_16x16x32_bf16 v[64:67], v[176:179], v[16:19], v[64:67]\n"
    "v_mfma_f32_16x16x32_bf16 v[72:75], v[176:179], v[20:23], v[72:75]\n"
    "v_mfma_f32_16x16x32_bf16 v[68:71], v[180:183], v[16:19], v[68:71]\n"
    "v_mfma_f32_16x16x32_bf16 v[76:79], v[180:183], v[20:23], v[76:79]\n"
    ".LgP_seg2_%=:\n"
    "s_waitcnt lgkmcnt(0)\n"
    "v_mfma_f32_16x16x32_bf16 v[32:35], v[184:187], v[24:27], v[32:35]\n"
    "ds_read_b128 v[16:19], v9 offset:32768\n"
    "ds_read_b128 v[20:23], v9 offset:33792\n"
    "v_mfma_f32_16x16x32_bf16 v[40:43], v[184:187], v[28:31], v[40:43]\n"
    "v_mfma_f32_16x16x32_bf16 v[36:39], v[188:191], v[24:27], v[36:39]\n"
    "v_mfma_f32_16x16x32_bf16 v[44:47], v[188:191], v[28:31], v[44:47]\n"
    "v_mfma_f32_16x16x32_bf16 v[48:51], v[24:27], v[192:195], v[48:51]\n"
    "v_mfma_f32_16x16x32_bf16 v[52:55], v[28:31], v[192:195], v[52:55]\n"
    "v_mfma_f32_16x16x32_bf16 v[56:59], v[24:27], v[196:199], v[56:59]\n"
    "v_mfma_f32_16x16x32_bf16 v[60:63], v[28:31], v[196:199], v[60:63]\n"
    "v_mfma_f32_16x16x32_bf16 v[64:67], v[200:203], v[24:27], v[64:67]\n"
    "v_mfma_f32_16x16x32_bf16 v[72:75], v[200:203], v[28:31], v[72:75]\n"
    "v_mfma_f32_16x16x32_bf16 v[68:71], v[204:207], v[24:27], v[68:71]\n"
    "v_mfma_f32_16x16x32_bf16 v[76:79], v[204:207], v[28:31], v[76:79]\n"
    "s_waitcnt lgkmcnt(0)\n"
    "v_mfma_f32_16x16x32_bf16 v[32:35], v[208:211], v[16:19], v[32:35]\n"
    "ds_read_b128 v[24:27], v9 offset:34816\n"
    "ds_read_b128 v[28:31], v9 offset:35840\n"
    "v_mfma_f32_16x16x32_bf16 v[40:43], v[208:211], v[20:23], v[40:43]\n"
    "v_mfma_f32_16x16x32_bf16 v[36:39], v[212:215], v[16:19], v[36:39]\n"
    "v_mfma_f32_16x16x32_bf16 v[44:47], v[212:215], v[20:23], v[44:47]\n"
    "v_mfma_f32_16x16x32_bf16 v[48:51], v[16:19], v[216:219], v[48:51]\n"
    "v_mfma_f32_16x16x32_bf16 v[52:55], v[20:23], v[216:219], v[52:55]\n"
    "v_mfma_f32_16x16x32_bf16 v[56:59], v[16:19], v[220:223], v[56:59]\n"
    "v_mfma_f32_16x16x32_bf16 v[60:63], v[20:23], v[220:223], v[60:63]\n"
    "v_mfma_f32_16x16x32_bf16 v[64:67], v[224:227], v[16:19], v[64:67]\n"
    "v_mfma_f32_16x16x32_bf16 v[72:75], v[224:227], v[20:23], v[72:75]\n"
    "v_mfma_f32_16x16x32_bf16 v[68:71], v[228:231], v[16:19], v[68:71]\n"
    "v_mfma_f32_16x16x32_bf16 v[76:79], v[228:231], v[20:23], v[76:79]\n"
    "s_cmp_lg_u32 s70, 0\n"
    "s_cbranch_scc0 .LgP_lastp_%=\n"
    ".LgP_nolast_%=:\n"
    "s_waitcnt lgkmcnt(0)\n"
    "v_mfma_f32_16x16x32_bf16 v[32:35], v[232:235], v[24:27], v[32:35]\n"
    "v_mfma_f32_16x16x32_bf16 v[40:43], v[232:235], v[28:31], v[40:43]\n"
    "v_mfma_f32_16x16x32_bf16 v[36:39], v[236:239], v[24:27], v[36:39]\n"
    "v_mfma_f32_16x16x32_bf16 v[44:47], v[236:239], v[28:31], v[44:47]\n"
    "v_mfma_f32_16x16x32_bf16 v[48:51], v[24:27], v[240:243], v[48:51]\n"
    "v_mfma_f32_16x16x32_bf16 v[52:55], v[28:31], v[240:243], v[52:55]\n"
    "v_mfma_f32_16x16x32_bf16 v[56:59], v[24:27], v[244:247], v[56:59]\n"
    "v_mfma_f32_16x16x32_bf16 v[60:63], v[28:31], v[244:247], v[60:63]\n"
    "v_mfma_f32_16x16x32_bf16 v[64:67], v[248:251], v[24:27], v[64:67]\n"
    "v_mfma_f32_16x16x32_bf16 v[72:75], v[248:251], v[28:31], v[72:75]\n"
    "v_mfma_f32_16x16x32_bf16 v[68:71], v[252:255], v[24:27], v[68:71]\n"
    "v_mfma_f32_16x16x32_bf16 v[76:79], v[252:255], v[28:31], v[76:79]\n"
    "v_cvt_pk_bf16_f32 v32, v32, v33\n"
    "v_cvt_pk_bf16_f32 v33, v34, v35\n"
    "v_cvt_pk_bf16_f32 v34, v36, v37\n"
    "v_cvt_pk_bf16_f32 v35, v38, v39\n"
    "buffer_store_dwordx4 v[32:35], v1, s[36:39], s79 offen sc1\n"
    "v_cvt_pk_bf16_f32 v40, v40, v41\n"
    "v_cvt_pk_bf16_f32 v41, v42, v43\n"
    "v_cvt_pk_bf16_f32 v42, v44, v45\n"
    "v_cvt_pk_bf16_f32 v43, v46, v47\n"
    "buffer_store_dwordx4 v[40:43], v1, s[36:39], s79 offen offset:1024 sc1\n"
    "v_cvt_pk_bf16_f32 v48, v48, v49\n"
    "v_cvt_pk_bf16_f32 v49, v50, v51\n"
    "v_cvt_pk_bf16_f32 v50, v52, v53\n"
    "v_cvt_pk_bf16_f32 v51, v54, v55\n"
    "buffer_store_dwordx4 v[48:51], v1, s[36:39], s80 offen sc1\n"
    "v_cvt_pk_bf16_f32 v56, v56, v57\n"
    "v_cvt_pk_bf16_f32 v57, v58, v59\n"
    "v_cvt_pk_bf16_f32 v58, v60, v61\n"
    "v_cvt_pk_bf16_f32 v59, v62, v63\n"
    "buffer_store_dwordx4 v[56:59], v1, s[36:39], s80 offen offset:1024 sc1\n"
    "v_cvt_pk_bf16_f32 v64, v64, v65\n"
    "v_cvt_pk_bf16_f32 v65, v66, v67\n"
    "v_cvt_pk_bf16_f32 v66, v68, v69\n"
    "v_cvt_pk_bf16_f32 v67, v70, v71\n"
    "buffer_store_dwordx4 v[64:67], v1, s[36:39], s73 offen sc1\n"
    "v_cvt_pk_bf16_f32 v72, v72, v73\n"
    "v_cvt_pk_bf16_f32 v73, v74, v75\n"
    "v_cvt_pk_bf16_f32 v74, v76, v77\n"
    "v_cvt_pk_bf16_f32 v75, v78, v79\n"
    "buffer_store_dwordx4 v[72:75], v1, s[36:39], s73 offen offset:1024 sc1\n"
    "s_mov_b32 s68, s71\n"
    "s_mov_b32 s69, 1\n"
    "s_cmp_eq_u32 s70, 0\n"
    "s_cbranch_scc1 .LgP_idle_%=\n"
    "s_cmp_lg_u32 s68, 64\n"
    "s_cbranch_scc1 .LgP_top_%=\n"
    ".LgP_LOOPE_%=:\n"
    "s_waitcnt vmcnt(0)\n"
    "s_mov_b32 s74, 63\n"
    "v_mov_b32 v12, s74\n"
    "s_mov_b64 exec, s[62:63]\n"
    "buffer_store_dword v12, v14, s[36:39], 0 offen sc1\n"
    "s_mov_b64 exec, -1\n"
    "s_waitcnt vmcnt(0)\n"
    "s_branch .LgP_FIN_%=\n"
    ".LgP_dma0_%=:\n"
    "s_mov_b32 s70, 1\n"
    "s_and_b32 s76, s71, s54\n"
    "s_mul_i32 s76, s76, s53\n"
    "s_add_u32 s76, s76, s52\n"
    "s_and_b32 s77, s71, 1\n"
    "s_mul_i32 s77, s77, 36864\n"
    "s_add_u32 s77, s77, s59\n"
    "s_waitcnt lgkmcnt(0)\n"
    "v_mfma_f32_16x16x32_bf16 v[32:35], a[72:75], v[24:27], v[32:35]\n"
    "ds_read_b128 v[16:19], v9 offset:8192\n"
    "ds_read_b128 v[20:23], v9 offset:9216\n"
    "s_add_u32 m0, s77, s59\n"
    "s_add_u32 s77, s77, 8192\n"
    "buffer_load_dwordx4 v1, s[36:39], s76 offen sc1 lds\n"
    "v_mfma_f32_16x16x32_bf16 v[40:43], a[72:75], v[28:31], v[40:43]\n"
    "v_mfma_f32_16x16x32_bf16 v[36:39], a[76:79], v[24:27], v[36:39]\n"
    "v_mfma_f32_16x16x32_bf16 v[44:47], a[76:79], v[28:31], v[44:47]\n"
    "v_mfma_f32_16x16x32_bf16 v[48:51], v[24:27], a[80:83], v[48:51]\n"
    "v_mfma_f32_16x16x32_bf16 v[52:55], v[28:31], a[80:83], v[52:55]\n"
    "buffer_load_dwordx4 v1, s[36:39], s76 offen offset:1024 sc1 lds\n"
    "s_add_u32 s76, s76, 8192\n"
    "v_mfma_f32_16x16x32_bf16 v[56:59], v[24:27], a[84:87], v[56:59]\n"
    "v_mfma_f32_16x16x32_bf16 v[60:63], v[28:31], a[84:87], v[60:63]\n"
    "v_mfma_f32_16x16x32_bf16 v[64:67], a[88:91], v[24:27], v[64:67]\n"
    "v_mfma_f32_16x16x32_bf16 v[72:75], a[88:91], v[28:31], v[72:75]\n"
    "v_mfma_f32_16x16x32_bf16 v[68:71], a[92:95], v[24:27], v[68:71]\n"
    "s_add_u32 m0, s77, s59\n"
    "s_add_u32 s77, s77, 8192\n"
    "buffer_load_dwordx4 v1, s[36:39], s76 offen sc1 lds\n"
    "v_mfma_f32_16x16x32_bf16 v[76:79], a[92:95], v[28:31], v[76:79]\n"
    "s_waitcnt lgkmcnt(0)\n"
    "v_mfma_f32_16x16x32_bf16 v[32:35], a[96:99], v[16:19], v[32:35]\n"
    "ds_read_b128 v[24:27], v9 offset:10240\n"
    "ds_read_b128 v[28:31], v9 offset:11264\n"
    "v_mfma_f32_16x16x32_bf16 v[40:43], a[96:99], v[20:23], v[40:43]\n"
    "v_mfma_f32_16x16x32_bf16 v[36:39], a[100:103], v[16:19], v[36:39]\n"
    "v_mfma_f32_16x16x32_bf16 v[44:47], a[100:103], v[20:23], v[44:47]\n"
    "buffer_load_dwordx4 v1, s[36:39], s76 offen offset:1024 sc1 lds\n"
    "s_add_u32 s76, s76, 8192\n"
    "v_mfma_f32_16x16x32_bf16 v[48:51], v[16:19], a[104:107], v[48:51]\n"
    "v_mfma_f32_16x16x32_bf16 v[52:55], v[20:23], a[104:107], v[52:55]\n"
    "v_mfma_f32_16x16x32_bf16 v[56:59], v[16:19], a[108:111], v[56:59]\n"
    "v_mfma_f32_16x16x32_bf16 v[60:63], v[20:23], a[108:111], v[60:63]\n"
    "v_mfma_f32_16x16x32_bf16 v[64:67], a[112:115], v[16:19], v[64:67]\n"
    "s_add_u32 m0, s77, s59\n"
    "s_add_u32 s77, s77, 8192\n"
    "buffer_load_dwordx4 v1, s[36:39], s76 offen sc1 lds\n"
    "v_mfma_f32_16x16x32_bf16 v[72:75], a[112:115], v[20:23], v[72:75]\n"
    "v_mfma_f32_16x16x32_bf16 v[68:71], a[116:119], v[16:19], v[68:71]\n"
    "v_mfma_f32_16x16x32_bf16 v[76:79], a[116:119], v[20:23], v[76:79]\n"
    "s_waitcnt lgkmcnt(0)\n"
    "v_mfma_f32_16x16x32_bf16 v[32:35], a[120:123], v[24:27], v[32:35]\n"
    "ds_read_b128 v[16:19], v9 offset:12288\n"
    "ds_read_b128 v[20:23], v9 offset:13312\n"
    "v_mfma_f32_16x16x32_bf16 v[40:43], a[120:123], v[28:31], v[40:43]\n"
    "buffer_load_dwordx4 v1, s[36:39], s76 offen offset:1024 sc1 lds\n"
    "s_add_u32 s76, s76, 8192\n"
    "v_mfma_f32_16x16x32_bf16 v[36:39], a[124:127], v[24:27], v[36:39]\n"
    "v_mfma_f32_16x16x32_bf16 v[44:47], a[124:127], v[28:31], v[44:47]\n"
    "v_mfma_f32_16x16x32_bf16 v[48:51], v[24:27], a[128:131], v[48:51]\n"
    "v_mfma_f32_16x16x32_bf16 v[52:55], v[28:31], a[128:131], v[52:55]\n"
    "v_mfma_f32_16x16x32_bf16 v[56:59], v[24:27], a[132:135], v[56:59]\n"
    "s_add_u32 m0, s77, s59\n"
    "s_add_u32 s77, s77, 8192\n"
    "buffer_load_dwordx4 v1, s[36:39], s76 offen sc1 lds\n"
    "v_mfma_f32_16x16x32_bf16 v[60:63], v[28:31], a[132:135], v[60:63]\n"
    "v_mfma_f32_16x16x32_bf16 v[64:67], a[136:139], v[24:27], v[64:67]\n"
    "v_mfma_f32_16x16x32_bf16 v[72:75], a[136:139], v[28:31], v[72:75]\n"
    "v_mfma_f32_16x16x32_bf16 v[68:71], a[140:143], v[24:27], v[68:71]\n"
    "v_mfma_f32_16x16x32_bf16 v[76:79], a[140:143], v[28:31], v[76:79]\n"
    "buffer_load_dwordx4 v1, s[36:39], s76 offen offset:1024 sc1 lds\n"
    "s_add_u32 s76, s76, 8192\n"
    "s_waitcnt lgkmcnt(0)\n"
    "v_mfma_f32_16x16x32_bf16 v[32:35], a[144:147], v[16:19], v[32:35]\n"
    "ds_read_b128 v[24:27], v9 offset:14336\n"
    "ds_read_b128 v[28:31], v9 offset:15360\n"
    "v_mfma_f32_16x16x32_bf16 v[40:43], a[144:147], v[20:23], v[40:43]\n"
    "v_mfma_f32_16x16x32_bf16 v[36:39], a[148:151], v[16:19], v[36:39]\n"
    "v_mfma_f32_16x16x32_bf16 v[44:47], a[148:151], v[20:23], v[44:47]\n"
    "v_mfma_f32_16x16x32_bf16 v[48:51], v[16:19], a[152:155], v[48:51]\n"
    "s_mov_b32 m0, s77\n"
    "s_nop 0\n"
    "buffer_load_dwordx4 v8, s[36:39], s76 offen sc1 lds\n"
    "v_mfma_f32_16x16x32_bf16 v[52:55], v[20:23], a[152:155], v[52:55]\n"
    "v_mfma_f32_16x16x32_bf16 v[56:59], v[16:19], a[156:159], v[56:59]\n"
    "v_mfma_f32_16x16x32_bf16 v[60:63], v[20:23], a[156:159], v[60:63]\n"
    "v_mfma_f32_16x16x32_bf16 v[64:67], a[160:163], v[16:19], v[64:67]\n"
    "v_mfma_f32_16x16x32_bf16 v[72:75], a[160:163], v[20:23], v[72:75]\n"
    "v_mfma_f32_16x16x32_bf16 v[68:71], a[164:167], v[16:19], v[68:71]\n"
    "v_mfma_f32_16x16x32_bf16 v[76:79], a[164:167], v[20:23], v[76:79]\n"
    "s_branch .LgP_seg0_%=\n"
    ".LgP_dma1_%=:\n"
    "s_mov_b32 s70, 1\n"
    "s_and_b32 s76, s71, s54\n"
    "s_mul_i32 s76, s76, s53\n"
    "s_add_u32 s76, s76, s52\n"
    "s_and_b32 s77, s71, 1\n"
    "s_mul_i32 s77, s77, 36864\n"
    "s_add_u32 s77, s77, s59\n"
    "s_waitcnt lgkmcnt(0)\n"
    "v_mfma_f32_16x16x32_bf16 v[32:35], a[168:171], v[24:27], v[32:35]\n"
    "ds_read_b128 v[16:19], v9 offset:16384\n"
    "ds_read_b128 v[20:23], v9 offset:17408\n"
    "s_add_u32 m0, s77, s59\n"
    "s_add_u32 s77, s77, 8192\n"
    "buffer_load_dwordx4 v1, s[36:39], s76 offen sc1 lds\n"
    "v_mfma_f32_16x16x32_bf16 v[40:43], a[168:171], v[28:31], v[40:43]\n"
    "v_mfma_f32_16x16x32_bf16 v[36:39], a[172:175], v[24:27], v[36:39]\n"
    "v_mfma_f32_16x16x32_bf16 v[44:47], a[172:175], v[28:31], v[44:47]\n"
    "v_mfma_f32_16x16x32_bf16 v[48:51], v[24:27], a[176:179], v[48:51]\n"
    "v_mfma_f32_16x16x32_bf16 v[52:55], v[28:31], a[176:179], v[52:55]\n"
    "buffer_load_dwordx4 v1, s[36:39], s76 offen offset:1024 sc1 lds\n"
    "s_add_u32 s76, s76, 8192\n"
    "v_mfma_f32_16x16x32_bf16 v[56:59], v[24:27], a[180:183], v[56:59]\n"
    "v_mfma_f32_16x16x32_bf16 v[60:63], v[28:31], a[180:183], v[60:63]\n"
    "v_mfma_f32_16x16x32_bf16 v[64:67], a[184:187], v[24:27], v[64:67]\n"
    "v_mfma_f32_16x16x32_bf16 v[72:75], a[184:187], v[28:31], v[72:75]\n"
    "v_mfma_f32_16x16x32_bf16 v[68:71], a[188:191], v[24:27], v[68:71]\n"
    "s_add_u32 m0, s77, s59\n"
    "s_add_u32 s77, s77, 8192\n"
    "buffer_load_dwordx4 v1, s[36:39], s76 offen sc1 lds\n"
    "v_mfma_f32_16x16x32_bf16 v[76:79], a[188:191], v[28:31], v[76:79]\n"
    "s_waitcnt lgkmcnt(0)\n"
    "v_mfma_f32_16x16x32_bf16 v[32:35], a[192:195], v[16:19], v[32:35]\n"
    "ds_read_b128 v[24:27], v9 offset:18432\n"
    "ds_read_b128 v[28:31], v9 offset:19456\n"
    "v_mfma_f32_16x16x32_bf16 v[40:43], a[192:195], v[20:23], v[40:43]\n"
    "v_mfma_f32_16x16x32_bf16 v[36:39], a[196:199], v[16:19], v[36:39]\n"
    "v_mfma_f32_16x16x32_bf16 v[44:47], a[196:199], v[20:23], v[44:47]\n"
    "buffer_load_dwordx4 v1, s[36:39], s76 offen offset:1024 sc1 lds\n"
    "s_add_u32 s76, s76, 8192\n"
    "v_mfma_f32_16x16x32_bf16 v[48:51], v[16:19], a[200:203], v[48:51]\n"
    "v_mfma_f32_16x16x32_bf16 v[52:55], v[20:23], a[200:203], v[52:55]\n"
    "v_mfma_f32_16x16x32_bf16 v[56:59], v[16:19], a[204:207], v[56:59]\n"
    "v_mfma_f32_16x16x32_bf16 v[60:63], v[20:23], a[204:207], v[60:63]\n"
    "v_mfma_f32_16x16x32_bf16 v[64:67], a[208:211], v[16:19], v[64:67]\n"
    "s_add_u32 m0, s77, s59\n"
    "s_add_u32 s77, s77, 8192\n"
    "buffer_load_dwordx4 v1, s[36:39], s76 offen sc1 lds\n"
    "v_mfma_f32_16x16x32_bf16 v[72:75], a[208:211], v[20:23], v[72:75]\n"
    "v_mfma_f32_16x16x32_bf16 v[68:71], a[212:215], v[16:19], v[68:71]\n"
    "v_mfma_f32_16x16x32_bf16 v[76:79], a[212:215], v[20:23], v[76:79]\n"
    "s_waitcnt lgkmcnt(0)\n"
    "v_mfma_f32_16x16x32_bf16 v[32:35], a[216:219], v[24:27], v[32:35]\n"
    "ds_read_b128 v[16:19], v9 offset:20480\n"
    "ds_read_b128 v[20:23], v9 offset:21504\n"
    "v_mfma_f32_16x16x32_bf16 v[40:43], a[216:219], v[28:31], v[40:43]\n"
    "buffer_load_dwordx4 v1, s[36:39], s76 offen offset:1024 sc1 lds\n"
    "s_add_u32 s76, s76, 8192\n"
    "v_mfma_f32_16x16x32_bf16 v[36:39], a[220:223], v[24:27], v[36:39]\n"
    "v_mfma_f32_16x16x32_bf16 v[44:47], a[220:223], v[28:31], v[44:47]\n"
    "v_mfma_f32_16x16x32_bf16 v[48:51], v[24:27], a[224:227], v[48:51]\n"
    "v_mfma_f32_16x16x32_bf16 v[52:55], v[28:31], a[224:227], v[52:55]\n"
    "v_mfma_f32_16x16x32_bf16 v[56:59], v[24:27], a[228:231], v[56:59]\n"
    "s_add_u32 m0, s77, s59\n"
    "s_add_u32 s77, s77, 8192\n"
    "buffer_load_dwordx4 v1, s[36:39], s76 offen sc1 lds\n"
    "v_mfma_f32_16x16x32_bf16 v[60:63], v[28:31], a[228:231], v[60:63]\n"
    "v_mfma_f32_16x16x32_bf16 v[64:67], a[232:235], v[24:27], v[64:67]\n"
    "v_mfma_f32_16x16x32_bf16 v[72:75], a[232:235], v[28:31], v[72:75]\n"
    "v_mfma_f32_16x16x32_bf16 v[68:71], a[236:239], v[24:27], v[68:71]\n"
    "v_mfma_f32_16x16x32_bf16 v[76:79], a[236:239], v[28:31], v[76:79]\n"
    "buffer_load_dwordx4 v1, s[36:39], s76 offen offset:1024 sc1 lds\n"
    "s_add_u32 s76, s76, 8192\n"
    "s_waitcnt lgkmcnt(0)\n"
    "v_mfma_f32_16x16x32_bf16 v[32:35], a[240:243], v[16:19], v[32:35]\n"
    "ds_read_b128 v[24:27], v9 offset:22528\n"
    "ds_read_b128 v[28:31], v9 offset:23552\n"
    "v_mfma_f32_16x16x32_bf16 v[40:43], a[240:243], v[20:23], v[40:43]\n"
    "v_mfma_f32_16x16x32_bf16 v[36:39], a[244:247], v[16:19], v[36:39]\n"
    "v_mfma_f32_16x16x32_bf16 v[44:47], a[244:247], v[20:23], v[44:47]\n"
    "v_mfma_f32_16x16x32_bf16 v[48:51], v[16:19], a[248:251], v[48:51]\n"
    "s_mov_b32 m0, s77\n"
    "s_nop 0\n"
    "buffer_load_dwordx4 v8, s[36:39], s76 offen sc1 lds\n"
    "v_mfma_f32_16x16x32_bf16 v[52:55], v[20:23], a[248:251], v[52:55]\n"
    "v_mfma_f32_16x16x32_bf16 v[56:59], v[16:19], a[252:255], v[56:59]\n"
    "v_mfma_f32_16x16x32_bf16 v[60:63], v[20:23], a[252:255], v[60:63]\n"
    "v_mfma_f32_16x16x32_bf16 v[64:67], v[80:83], v[16:19], v[64:67]\n"
    "v_mfma_f32_16x16x32_bf16 v[72:75], v[80:83], v[20:23], v[72:75]\n"
    "v_mfma_f32_16x16x32_bf16 v[68:71], v[84:87], v[16:19], v[68:71]\n"
    "v_mfma_f32_16x16x32_bf16 v[76:79], v[84:87], v[20:23], v[76:79]\n"
    "s_branch .LgP_seg1_%=\n"
    ".LgP_dma2_%=:\n"
    "s_mov_b32 s70, 1\n"
    "s_and_b32 s76, s71, s54\n"
    "s_mul_i32 s76, s76, s53\n"
    "s_add_u32 s76, s76, s52\n"
    "s_and_b32 s77, s71, 1\n"
    "s_mul_i32 s77, s77, 36864\n"
    "s_add_u32 s77, s77, s59\n"
    "s_waitcnt lgkmcnt(0)\n"
    "v_mfma_f32_16x16x32_bf16 v[32:35], v[88:91], v[24:27], v[32:35]\n"
    "ds_read_b128 v[16:19], v9 offset:24576\n"
    "ds_read_b128 v[20:23], v9 offset:25600\n"
    "s_add_u32 m0, s77, s59\n"
    "s_add_u32 s77, s77, 8192\n"
    "buffer_load_dwordx4 v1, s[36:39], s76 offen sc1 lds\n"
    "v_mfma_f32_16x16x32_bf16 v[40:43], v[88:91], v[28:31], v[40:43]\n"
    "v_mfma_f32_16x16x32_bf16 v[36:39], v[92:95], v[24:27], v[36:39]\n"
    "v_mfma_f32_16x16x32_bf16 v[44:47], v[92:95], v[28:31], v[44:47]\n"
    "v_mfma_f32_16x16x32_bf16 v[48:51], v[24:27], v[96:99], v[48:51]\n"
    "v_mfma_f32_16x16x32_bf16 v[52:55], v[28:31], v[96:99], v[52:55]\n"
    "buffer_load_dwordx4 v1, s[36:39], s76 offen offset:1024 sc1 lds\n"
    "s_add_u32 s76, s76, 8192\n"
    "v_mfma_f32_16x16x32_bf16 v[56:59], v[24:27], v[100:103], v[56:59]\n"
    "v_mfma_f32_16x16x32_bf16 v[60:63], v[28:31], v[100:103], v[60:63]\n"
    "v_mfma_f32_16x16x32_bf16 v[64:67], v[104:107], v[24:27], v[64:67]\n"
    "v_mfma_f32_16x16x32_bf16 v[72:75], v[104:107], v[28:31], v[72:75]\n"
    "v_mfma_f32_16x16x32_bf16 v[68:71], v[108:111], v[24:27], v[68:71]\n"
    "s_add_u32 m0, s77, s59\n"
    "s_add_u32 s77, s77, 8192\n"
    "buffer_load_dwordx4 v1, s[36:39], s76 offen sc1 lds\n"
    "v_mfma_f32_16x16x32_bf16 v[76:79], v[108:111], v[28:31], v[76:79]\n"
    "s_waitcnt lgkmcnt(0)\n"
    "v_mfma_f32_16x16x32_bf16 v[32:35], v[112:115], v[16:19], v[32:35]\n"
    "ds_read_b128 v[24:27], v9 offset:26624\n"
    "ds_read_b128 v[28:31], v9 offset:27648\n"
    "v_mfma_f32_16x16x32_bf16 v[40:43], v[112:115], v[20:23], v[40:43]\n"
    "v_mfma_f32_16x16x32_bf16 v[36:39], v[116:119], v[16:19], v[36:39]\n"
    "v_mfma_f32_16x16x32_bf16 v[44:47], v[116:119], v[20:23], v[44:47]\n"
    "buffer_load_dwordx4 v1, s[36:39], s76 offen offset:1024 sc1 lds\n"
    "s_add_u32 s76, s76, 8192\n"
    "v_mfma_f32_16x16x32_bf16 v[48:51], v[16:19], v[120:123], v[48:51]\n"
    "v_mfma_f32_16x16x32_bf16 v[52:55], v[20:23], v[120:123], v[52:55]\n"
    "v_mfma_f32_16x16x32_bf16 v[56:59], v[16:19], v[124:127], v[56:59]\n"
    "v_mfma_f32_16x16x32_bf16 v[60:63], v[20:23], v[124:127], v[60:63]\n"
    "v_mfma_f32_16x16x32_bf16 v[64:67], v[128:131], v[16:19], v[64:67]\n"
    "s_add_u32 m0, s77, s59\n"
    "s_add_u32 s77, s77, 8192\n"
    "buffer_load_dwordx4 v1, s[36:39], s76 offen sc1 lds\n"
    "v_mfma_f32_16x16x32_bf16 v[72:75], v[128:131], v[20:23], v[72:75]\n"
    "v_mfma_f32_16x16x32_bf16 v[68:71], v[132:135], v[16:19], v[68:71]\n"
    "v_mfma_f32_16x16x32_bf16 v[76:79], v[132:135], v[20:23], v[76:79]\n"
    "s_waitcnt lgkmcnt(0)\n"
    "v_mfma_f32_16x16x32_bf16 v[32:35], v[136:139], v[24:27], v[32:35]\n"
    "ds_read_b128 v[16:19], v9 offset:28672\n"
    "ds_read_b128 v[20:23], v9 offset:29696\n"
    "v_mfma_f32_16x16x32_bf16 v[40:43], v[136:139], v[28:31], v[40:43]\n"
    "buffer_load_dwordx4 v1, s[36:39], s76 offen offset:1024 sc1 lds\n"
    "s_add_u32 s76, s76, 8192\n"
    "v_mfma_f32_16x16x32_bf16 v[36:39], v[140:143], v[24:27], v[36:39]\n"
    "v_mfma_f32_16x16x32_bf16 v[44:47], v[140:143], v[28:31], v[44:47]\n"
    "v_mfma_f32_16x16x32_bf16 v[48:51], v[24:27], v[144:147], v[48:51]\n"
    "v_mfma_f32_16x16x32_bf16 v[52:55], v[28:31], v[144:147], v[52:55]\n"
    "v_mfma_f32_16x16x32_bf16 v[56:59], v[24:27], v[148:151], v[56:59]\n"
    "s_add_u32 m0, s77, s59\n"
    "s_add_u32 s77, s77, 8192\n"
    "buffer_load_dwordx4 v1, s[36:39], s76 offen sc1 lds\n"
    "v_mfma_f32_16x16x32_bf16 v[60:63], v[28:31], v[148:151], v[60:63]\n"
    "v_mfma_f32_16x16x32_bf16 v[64:67], v[152:155], v[24:27], v[64:67]\n"
    "v_mfma_f32_16x16x32_bf16 v[72:75], v[152:155], v[28:31], v[72:75]\n"
    "v_mfma_f32_16x16x32_bf16 v[68:71], v[156:159], v[24:27], v[68:71]\n"
    "v_mfma_f32_16x16x32_bf16 v[76:79], v[156:159], v[28:31], v[76:79]\n"
    "buffer_load_dwordx4 v1, s[36:39], s76 offen offset:1024 sc1 lds\n"
    "s_add_u32 s76, s76, 8192\n"
    "s_waitcnt lgkmcnt(0)\n"
    "v_mfma_f32_16x16x32_bf16 v[32:35], v[160:163], v[16:19], v[32:35]\n"
    "ds_read_b128 v[24:27], v9 offset:30720\n"
    "ds_read_b128 v[28:31], v9 offset:31744\n"
    "v_mfma_f32_16x16x32_bf16 v[40:43], v[160:163], v[20:23], v[40:43]\n"
    "v_mfma_f32_16x16x32_bf16 v[36:39], v[164:167], v[16:19], v[36:39]\n"
    "v_mfma_f32_16x16x32_bf16 v[44:47], v[164:167], v[20:23], v[44:47]\n"
    "v_mfma_f32_16x16x32_bf16 v[48:51], v[16:19], v[168:171], v[48:51]\n"
    "s_mov_b32 m0, s77\n"
    "s_nop 0\n"
    "buffer_load_dwordx4 v8, s[36:39], s76 offen sc1 lds\n"
    "v_mfma_f32_16x16x32_bf16 v[52:55], v[20:23], v[168:171], v[52:55]\n"
    "v_mfma_f32_16x16x32_bf16 v[56:59], v[16:19], v[172:175], v[56:59]\n"
    "v_mfma_f32_16x16x32_bf16 v[60:63], v[20:23], v[172:175], v[60:63]\n"
    "v_mfma_f32_16x16x32_bf16 v[64:67], v[176:179], v[16:19], v[64:67]\n"
    "v_mfma_f32_16x16x32_bf16 v[72:75], v[176:179], v[20:23], v[72:75]\n"
    "v_mfma_f32_16x16x32_bf16 v[68:71], v[180:183], v[16:19], v[68:71]\n"
    "v_mfma_f32_16x16x32_bf16 v[76:79], v[180:183], v[20:23], v[76:79]\n"
    "s_branch .LgP_seg2_%=\n"
    ".LgP_lastp_%=:\n"
    "v_add_u32 v13, v11, v3\n"
    "v_cmp_gt_i32 vcc, s71, v13\n"
    "s_nop 0\n"
    "s_or_b64 exec, vcc, 1\n"
    "buffer_atomic_smax v11, v2, s[36:39], 0 offen sc0 sc1\n"
    "s_mov_b64 exec, -1\n"
    "s_branch .LgP_nolast_%=\n"
    ".LgP_idle_%=:\n"
    "s_waitcnt vmcnt(6)\n"
    "v_add_u32 v13, v11, v3\n"
    "v_cmp_gt_i32 vcc, s71, v13\n"
    "s_cbranch_vccnz .LgP_idle2_%=\n"
    "s_and_b32 s76, s71, s54\n"
    "s_mul_i32 s76, s76, s53\n"
    "s_add_u32 s76, s76, s52\n"
    "s_and_b32 s77, s71, 1\n"
    "s_mul_i32 s77, s77, 36864\n"
    "s_add_u32 s77, s77, s59\n"
    "s_add_u32 m0, s77, s59\n"
    "s_add_u32 s77, s77, 8192\n"
    "buffer_load_dwordx4 v1, s[36:39], s76 offen sc1 lds\n"
    "buffer_load_dwordx4 v1, s[36:39], s76 offen offset:1024 sc1 lds\n"
    "s_add_u32 s76, s76, 8192\n"
    "s_add_u32 m0, s77, s59\n"
    "s_add_u32 s77, s77, 8192\n"
    "buffer_load_dwordx4 v1, s[36:39], s76 offen sc1 lds\n"
    "buffer_load_dwordx4 v1, s[36:39], s76 offen offset:1024 sc1 lds\n"
    "s_add_u32 s76, s76, 8192\n"
    "s_add_u32 m0, s77, s59\n"
    "s_add_u32 s77, s77, 8192\n"
    "buffer_load_dwordx4 v1, s[36:39], s76 offen sc1 lds\n"
    "buffer_load_dwordx4 v1, s[36:39], s76 offen offset:1024 sc1 lds\n"
    "s_add_u32 s76, s76, 8192\n"
    "s_add_u32 m0, s77, s59\n"
    "s_add_u32 s77, s77, 8192\n"
    "buffer_load_dwordx4 v1, s[36:39], s76 offen sc1 lds\n"
    "buffer_load_dwordx4 v1, s[36:39], s76 offen offset:1024 sc1 lds\n"
    "s_add_u32 s76, s76, 8192\n"
    "s_mov_b32 m0, s77\n"
    "s_nop 0\n"
    "buffer_load_dwordx4 v8, s[36:39], s76 offen sc1 lds\n"
    "s_waitcnt vmcnt(9)\n"
    "s_sub_u32 s74, s68, 1\n"
    "v_mov_b32 v12, s74\n"
    "s_mov_b64 exec, s[62:63]\n"
    "buffer_store_dword v12, v14, s[36:39], 0 offen sc1\n"
    "s_mov_b64 exec, -1\n"
    "s_mov_b32 s69, 0\n"
    "s_waitcnt vmcnt(1)\n"
    "s_branch .LgP_bar_%=\n"
    ".LgP_idle2_%=:\n"
    "s_or_b64 exec, vcc, 1\n"
    "buffer_atomic_smax v11, v2, s[36:39], 0 offen sc0 sc1\n"
    "s_mov_b64 exec, -1\n"
    "s_waitcnt vmcnt(1)\n"
    "s_sub_u32 s74, s68, 1\n"
    "v_mov_b32 v12, s74\n"
    "s_mov_b64 exec, s[62:63]\n"
    "buffer_store_dword v12, v14, s[36:39], 0 offen sc1\n"
    "s_mov_b64 exec, -1\n"
    "s_waitcnt vmcnt(1)\n"
    "s_branch .LgP_block1_%=\n"
    ".LgP_block_%=:\n"
    "v_add_u32 v13, v11, v3\n"
    "v_cmp_gt_i32 vcc, s71, v13\n"
    "s_nop 0\n"
    "s_or_b64 exec, vcc, 1\n"
    "buffer_atomic_smax v11, v2, s[36:39], 0 offen sc0 sc1\n"
    "s_mov_b64 exec, -1\n"
    "s_waitcnt vmcnt(0)\n"
    ".LgP_block1_%=:\n"
    "v_add_u32 v13, v11, v3\n"
    "v_cmp_gt_i32 vcc, s71, v13\n"
    "s_cbranch_vccz .LgP_go_%=\n"
    "s_sleep 4\n"
    "s_sub_u32 s72, s72, 1\n"
    "s_cbranch_scc0 .LgP_block_%=\n"
    "s_mov_b32 s72, 0\n"
    ".LgP_go_%=:\n"
    "s_and_b32 s76, s71, s54\n"
    "s_mul_i32 s76, s76, s53\n"
    "s_add_u32 s76, s76, s52\n"
    "s_and_b32 s77, s71, 1\n"
    "s_mul_i32 s77, s77, 36864\n"
    "s_add_u32 s77, s77, s59\n"
    "s_add_u32 m0, s77, s59\n"
    "s_add_u32 s77, s77, 8192\n"
    "buffer_load_dwordx4 v1, s[36:39], s76 offen sc1 lds\n"
    "buffer_load_dwordx4 v1, s[36:39], s76 offen offset:1024 sc1 lds\n"
    "s_add_u32 s76, s76, 8192\n"
    "s_add_u32 m0, s77, s59\n"
    "s_add_u32 s77, s77, 8192\n"
    "buffer_load_dwordx4 v1, s[36:39], s76 offen sc1 lds\n"
    "buffer_load_dwordx4 v1, s[36:39], s76 offen offset:1024 sc1 lds\n"
    "s_add_u32 s76, s76, 8192\n"
    "s_add_u32 m0, s77, s59\n"
    "s_add_u32 s77, s77, 8192\n"
    "buffer_load_dwordx4 v1, s[36:39], s76 offen sc1 lds\n"
    "buffer_load_dwordx4 v1, s[36:39], s76 offen offset:1024 sc1 lds\n"
    "s_add_u32 s76, s76, 8192\n"
    "s_add_u32 m0, s77, s59\n"
    "s_add_u32 s77, s77, 8192\n"
    "buffer_load_dwordx4 v1, s[36:39], s76 offen sc1 lds\n"
    "buffer_load_dwordx4 v1, s[36:39], s76 offen offset:1024 sc1 lds\n"
    "s_add_u32 s76, s76, 8192\n"
    "s_mov_b32 m0, s77\n"
    "s_nop 0\n"
    "buffer_load_dwordx4 v8, s[36:39], s76 offen sc1 lds\n"
    "s_mov_b32 s69, 0\n"
    "s_waitcnt vmcnt(0)\n"
    "s_branch .LgP_bar_%=\n"
    ".LgP_FIN_%=:\n"
  );
}
DEVI void role_up(const Params& p, int L, int j, int i, u32 dbg) {
  const int lane = threadIdx.x & 63, w = threadIdx.x >> 6, col = (6 * j + i) * 192;
  const u32 wb = 576u * 4608 * 2 - col * 2;
  GEMM_DECL;
  g_w0 = make_srd(p.w1 + col, wb); g_w1 = make_srd(p.w3 + col, wb); g_w2 = g_w1;
  g_ldw = 9216;
  g_in_base = OFF_L + L * LSZ + L_XN2; g_in_stride = SEGB; g_in_mask = RX - 1;
  g_out_base = OFF_L + L * LSZ + L_H + j * 73728 + i * 12288; g_out_stride = 4 * 73728; g_out_mask = RH - 1;
  g_flag = lane < 3 ? mbox(L, j, SL_DOWN + lane, 4 * i + w) : OOB;
  g_paddr = mbox(L, j, SL_UP + i, lane);
  g_pbias = lane < 16 ? 0 : (lane < 19 ? RH : 1000);
  v16u sa, sb, va;
  gemm_args(GEMM_ARGS, p, dbg, sa, sb, va);
  RUN_ASM(
    "s_nop 4\n"
    "v_add_u32 v8, s59, v0\n"
    "s_add_u32 m0, s59, 0\n"
    "s_mov_b32 s74, 0\n"
    "buffer_load_dwordx4 v5, s[40:43], s74 offen lds\n"
    "s_add_u32 m0, s59, 4096\n"
    "s_mov_b32 s74, 128\n"
    "buffer_load_dwordx4 v5, s[40:43], s74 offen lds\n"
    "s_add_u32 m0, s59, 8192\n"
    "s_mov_b32 s74, 256\n"
    "buffer_load_dwordx4 v5, s[40:43], s74 offen lds\n"
    "s_add_u32 m0, s59, 12288\n"
    "s_mov_b32 s74, 0\n"
    "buffer_load_dwordx4 v5, s[44:47], s74 offen lds\n"
    "s_add_u32 m0, s59, 16384\n"
    "s_mov_b32 s74, 128\n"
    "buffer_load_dwordx4 v5, s[44:47], s74 offen lds\n"
    "s_add_u32 m0, s59, 20480\n"
    "s_mov_b32 s74, 256\n"
    "buffer_load_dwordx4 v5, s[44:47], s74 offen lds\n"
    "s_add_u32 m0, s59, 24576\n"
    "s_mov_b32 s74, 294912\n"
    "buffer_load_dwordx4 v5, s[40:43], s74 offen lds\n"
    "s_add_u32 m0, s59, 28672\n"
    "s_mov_b32 s74, 295040\n"
    "buffer_load_dwordx4 v5, s[40:43], s74 offen lds\n"
    "s_add_u32 m0, s59, 32768\n"
    "s_mov_b32 s74, 295168\n"
    "buffer_load_dwordx4 v5, s[40:43], s74 offen lds\n"
    "s_add_u32 m0, s59, 36864\n"
    "s_mov_b32 s74, 294912\n"
    "buffer_load_dwordx4 v5, s[44:47], s74 offen lds\n"
    "s_add_u32 m0, s59, 40960\n"
    "s_mov_b32 s74, 295040\n"
    "buffer_load_dwordx4 v5, s[44:47], s74 offen lds\n"
    "s_add_u32 m0, s59, 45056\n"
    "s_mov_b32 s74, 295168\n"
    "buffer_load_dwordx4 v5, s[44:47], s74 offen lds\n"
    "s_add_u32 m0, s59, 49152\n"
    "s_mov_b32 s74, 589824\n"
    "buffer_load_dwordx4 v5, s[40:43], s74 offen lds\n"
    "s_add_u32 m0, s59, 53248\n"
    "s_mov_b32 s74, 589952\n"
    "buffer_load_dwordx4 v5, s[40:43], s74 offen lds\n"
    "s_add_u32 m0, s59, 57344\n"
    "s_mov_b32 s74, 590080\n"
    "buffer_load_dwordx4 v5, s[40:43], s74 offen lds\n"
    "s_add_u32 m0, s59, 61440\n"
    "s_mov_b32 s74, 589824\n"
    "buffer_load_dwordx4 v5, s[44:47], s74 offen lds\n"
    "s_add_u32 m0, s59, 65536\n"
    "s_mov_b32 s74, 589952\n"
    "buffer_load_dwordx4 v5, s[44:47], s74 offen lds\n"
    "s_add_u32 m0, s59, 69632\n"
    "s_mov_b32 s74, 590080\n"
    "buffer_load_dwordx4 v5, s[44:47], s74 offen lds\n"
    "s_add_u32 m0, s59, 73728\n"
    "s_mov_b32 s74, 884736\n"
    "buffer_load_dwordx4 v5, s[40:43], s74 offen lds\n"
    "s_add_u32 m0, s59, 77824\n"
    "s_mov_b32 s74, 884864\n"
    "buffer_load_dwordx4 v5, s[40:43], s74 offen lds\n"
    "s_add_u32 m0, s59, 81920\n"
    "s_mov_b32 s74, 884992\n"
    "buffer_load_dwordx4 v5, s[40:43], s74 offen lds\n"
    "s_add_u32 m0, s59, 86016\n"
    "s_mov_b32 s74, 884736\n"
    "buffer_load_dwordx4 v5, s[44:47], s74 offen lds\n"
    "s_add_u32 m0, s59, 90112\n"
    "s_mov_b32 s74, 884864\n"
    "buffer_load_dwordx4 v5, s[44:47], s74 offen lds\n"
    "s_add_u32 m0, s59, 94208\n"
    "s_mov_b32 s74, 884992\n"
    "buffer_load_dwordx4 v5, s[44:47], s74 offen lds\n"
    "s_add_u32 m0, s59, 98304\n"
    "s_mov_b32 s74, 1179648\n"
    "buffer_load_dwordx4 v5, s[40:43], s74 offen lds\n"
    "s_add_u32 m0, s59, 102400\n"
    "s_mov_b32 s74, 1179776\n"
    "buffer_load_dwordx4 v5, s[40:43], s74 offen lds\n"
    "s_add_u32 m0, s59, 106496\n"
    "s_mov_b32 s74, 1179904\n"
    "buffer_load_dwordx4 v5, s[40:43], s74 offen lds\n"
    "s_add_u32 m0, s59, 110592\n"
    "s_mov_b32 s74, 1179648\n"
    "buffer_load_dwordx4 v5, s[44:47], s74 offen lds\n"
    "s_add_u32 m0, s59, 114688\n"
    "s_mov_b32 s74, 1179776\n"
    "buffer_load_dwordx4 v5, s[44:47], s74 offen lds\n"
    "s_add_u32 m0, s59, 118784\n"
    "s_mov_b32 s74, 1179904\n"
    "buffer_load_dwordx4 v5, s[44:47], s74 offen lds\n"
    "v_add_u32 v16, 49152, v6\n"
    "v_add_u32 v17, 98304, v6\n"
    "v_add_u32 v18, 49152, v7\n"
    "v_add_u32 v19, 98304, v7\n"
    "s_waitcnt vmcnt(24) lgkmcnt(0)\n"
    "s_barrier\n"
    "s_add_u32 m0, s59, 122880\n"
    "s_mov_b32 s74, 1474560\n"
    "buffer_load_dwordx4 v5, s[40:43], s74 offen lds\n"
    "s_add_u32 m0, s59, 126976\n"
    "s_mov_b32 s74, 1474688\n"
    "buffer_load_dwordx4 v5, s[40:43], s74 offen lds\n"
    "s_add_u32 m0, s59, 131072\n"
    "s_mov_b32 s74, 1474816\n"
    "buffer_load_dwordx4 v5, s[40:43], s74 offen lds\n"
    "s_add_u32 m0, s59, 135168\n"
    "s_mov_b32 s74, 1474560\n"
    "buffer_load_dwordx4 v5, s[44:47], s74 offen lds\n"
    "s_add_u32 m0, s59, 139264\n"
    "s_mov_b32 s74, 1474688\n"
    "buffer_load_dwordx4 v5, s[44:47], s74 offen lds\n"
    "s_add_u32 m0, s59, 143360\n"
    "s_mov_b32 s74, 1474816\n"
    "buffer_load_dwordx4 v5, s[44:47], s74 offen lds\n"
    "ds_read_b64_tr_b16 a[0:1], v6 offset:0\n"
    "ds_read_b64_tr_b16 a[2:3], v6 offset:512\n"
    "ds_read_b64_tr_b16 a[4:5], v6 offset:8\n"
    "ds_read_b64_tr_b16 a[6:7], v6 offset:520\n"
    "ds_read_b64_tr_b16 a[8:9], v7 offset:0\n"
    "ds_read_b64_tr_b16 a[10:11], v7 offset:512\n"
    "ds_read_b64_tr_b16 a[12:13], v6 offset:12288\n"
    "ds_read_b64_tr_b16 a[14:15], v6 offset:12800\n"
    "ds_read_b64_tr_b16 a[16:17], v6 offset:12296\n"
    "ds_read_b64_tr_b16 a[18:19], v6 offset:12808\n"
    "ds_read_b64_tr_b16 a[20:21], v7 offset:12288\n"
    "ds_read_b64_tr_b16 a[22:23], v7 offset:12800\n"
    "s_waitcnt vmcnt(24) lgkmcnt(0)\n"
    "s_barrier\n"
    "s_add_u32 m0, s59, 0\n"
    "s_mov_b32 s74, 1769472\n"
    "buffer_load_dwordx4 v5, s[40:43], s74 offen lds\n"
    "s_add_u32 m0, s59, 4096\n"
    "s_mov_b32 s74, 1769600\n"
    "buffer_load_dwordx4 v5, s[40:43], s74 offen lds\n"
    "s_add_u32 m0, s59, 8192\n"
    "s_mov_b32 s74, 1769728\n"
    "buffer_load_dwordx4 v5, s[40:43], s74 offen lds\n"
    "s_add_u32 m0, s59, 12288\n"
    "s_mov_b32 s74, 1769472\n"
    "buffer_load_dwordx4 v5, s[44:47], s74 offen lds\n"
    "s_add_u32 m0, s59, 16384\n"
    "s_mov_b32 s74, 1769600\n"
    "buffer_load_dwordx4 v5, s[44:47], s74 offen lds\n"
    "s_add_u32 m0, s59, 20480\n"
    "s_mov_b32 s74, 1769728\n"
    "buffer_load_dwordx4 v5, s[44:47], s74 offen lds\n"
    "ds_read_b64_tr_b16 a[24:25], v6 offset:24576\n"
    "ds_read_b64_tr_b16 a[26:27], v6 offset:25088\n"
    "ds_read_b64_tr_b16 a[28:29], v6 offset:24584\n"
    "ds_read_b64_tr_b16 a[30:31], v6 offset:25096\n"
    "ds_read_b64_tr_b16 a[32:33], v7 offset:24576\n"
    "ds_read_b64_tr_b16 a[34:35], v7 offset:25088\n"
    "ds_read_b64_tr_b16 a[36:37], v6 offset:36864\n"
    "ds_read_b64_tr_b16 a[38:39], v6 offset:37376\n"
    "ds_read_b64_tr_b16 a[40:41], v6 offset:36872\n"
    "ds_read_b64_tr_b16 a[42:43], v6 offset:37384\n"
    "ds_read_b64_tr_b16 a[44:45], v7 offset:36864\n"
    "ds_read_b64_tr_b16 a[46:47], v7 offset:37376\n"
    "s_waitcnt vmcnt(24) lgkmcnt(0)\n"
    "s_barrier\n"
    "s_add_u32 m0, s59, 24576\n"
    "s_mov_b32 s74, 2064384\n"
    "buffer_load_dwordx4 v5, s[40:43], s74 offen lds\n"
    "s_add_u32 m0, s59, 28672\n"
    "s_mov_b32 s74, 2064512\n"
    "buffer_load_dwordx4 v5, s[40:43], s74 offen lds\n"
    "s_add_u32 m0, s59, 32768\n"
    "s_mov_b32 s74, 2064640\n"
    "buffer_load_dwordx4 v5, s[40:43], s74 offen lds\n"
    "s_add_u32 m0, s59, 36864\n"
    "s_mov_b32 s74, 2064384\n"
    "buffer_load_dwordx4 v5, s[44:47], s74 offen lds\n"
    "s_add_u32 m0, s59, 40960\n"
    "s_mov_b32 s74, 2064512\n"
    "buffer_load_dwordx4 v5, s[44:47], s74 offen lds\n"
    "s_add_u32 m0, s59, 45056\n"
    "s_mov_b32 s74, 2064640\n"
    "buffer_load_dwordx4 v5, s[44:47], s74 offen lds\n"
    "ds_read_b64_tr_b16 a[48:49], v16 offset:0\n"
    "ds_read_b64_tr_b16 a[50:51], v16 offset:512\n"
    "ds_read_b64_tr_b16 a[52:53], v16 offset:8\n"
    "ds_read_b64_tr_b16 a[54:55], v16 offset:520\n"
    "ds_read_b64_tr_b16 a[56:57], v18 offset:0\n"
    "ds_read_b64_tr_b16 a[58:59], v18 offset:512\n"
    "ds_read_b64_tr_b16 a[60:61], v16 offset:12288\n"
    "ds_read_b64_tr_b16 a[62:63], v16 offset:12800\n"
    "ds_read_b64_tr_b16 a[64:65], v16 offset:12296\n"
    "ds_read_b64_tr_b16 a[66:67], v16 offset:12808\n"
    "ds_read_b64_tr_b16 a[68:69], v18 offset:12288\n"
    "ds_read_b64_tr_b16 a[70:71], v18 offset:12800\n"
    "s_waitcnt vmcnt(24) lgkmcnt(0)\n"
    "s_barrier\n"
    "s_add_u32 m0, s59, 49152\n"
    "s_mov_b32 s74, 2359296\n"
    "buffer_load_dwordx4 v5, s[40:43], s74 offen lds\n"
    "s_add_u32 m0, s59, 53248\n"
    "s_mov_b32 s74, 2359424\n"
    "buffer_load_dwordx4 v5, s[40:43], s74 offen lds\n"
    "s_add_u32 m0, s59, 57344\n"
    "s_mov_b32 s74, 2359552\n"
    "buffer_load_dwordx4 v5, s[40:43], s74 offen lds\n"
    "s_add_u32 m0, s59, 61440\n"
    "s_mov_b32 s74, 2359296\n"
    "buffer_load_dwordx4 v5, s[44:47], s74 offen lds\n"
    "s_add_u32 m0, s59, 65536\n"
    "s_mov_b32 s74, 2359424\n"
    "buffer_load_dwordx4 v5, s[44:47], s74 offen lds\n"
    "s_add_u32 m0, s59, 69632\n"
    "s_mov_b32 s74, 2359552\n"
    "buffer_load_dwordx4 v5, s[44:47], s74 offen lds\n"
    "ds_read_b64_tr_b16 a[72:73], v16 offset:24576\n"
    "ds_read_b64_tr_b16 a[74:75], v16 offset:25088\n"
    "ds_read_b64_tr_b16 a[76:77], v16 offset:24584\n"
    "ds_read_b64_tr_b16 a[78:79], v16 offset:25096\n"
    "ds_read_b64_tr_b16 a[80:81], v18 offset:24576\n"
    "ds_read_b64_tr_b16 a[82:83], v18 offset:25088\n"
    "ds_read_b64_tr_b16 a[84:85], v16 offset:36864\n"
    "ds_read_b64_tr_b16 a[86:87], v16 offset:37376\n"
    "ds_read_b64_tr_b16 a[88:89], v16 offset:36872\n"
    "ds_read_b64_tr_b16 a[90:91], v16 offset:37384\n"
    "ds_read_b64_tr_b16 a[92:93], v18 offset:36864\n"
    "ds_read_b64_tr_b16 a[94:95], v18 offset:37376\n"
    "s_waitcnt vmcnt(24) lgkmcnt(0)\n"
    "s_barrier\n"
    "s_add_u32 m0, s59, 73728\n"
    "s_mov_b32 s74, 2654208\n"
    "buffer_load_dwordx4 v5, s[40:43], s74 offen lds\n"
    "s_add_u32 m0, s59, 77824\n"
    "s_mov_b32 s74, 2654336\n"
    "buffer_load_dwordx4 v5, s[40:43], s74 offen lds\n"
    "s_add_u32 m0, s59, 81920\n"
    "s_mov_b32 s74, 2654464\n"
    "buffer_load_dwordx4 v5, s[40:43], s74 offen lds\n"
    "s_add_u32 m0, s59, 86016\n"
    "s_mov_b32 s74, 2654208\n"
    "buffer_load_dwordx4 v5, s[44:47], s74 offen lds\n"
    "s_add_u32 m0, s59, 90112\n"
    "s_mov_b32 s74, 2654336\n"
    "buffer_load_dwordx4 v5, s[44:47], s74 offen lds\n"
    "s_add_u32 m0, s59, 94208\n"
    "s_mov_b32 s74, 2654464\n"
    "buffer_load_dwordx4 v5, s[44:47], s74 offen lds\n"
    "ds_read_b64_tr_b16 a[96:97], v17 offset:0\n"
    "ds_read_b64_tr_b16 a[98:99], v17 offset:512\n"
    "ds_read_b64_tr_b16 a[100:101], v17 offset:8\n"
    "ds_read_b64_tr_b16 a[102:103], v17 offset:520\n"
    "ds_read_b64_tr_b16 a[104:105], v19 offset:0\n"
    "ds_read_b64_tr_b16 a[106:107], v19 offset:512\n"
    "ds_read_b64_tr_b16 a[108:109], v17 offset:12288\n"
    "ds_read_b64_tr_b16 a[110:111], v17 offset:12800\n"
    "ds_read_b64_tr_b16 a[112:113], v17 offset:12296\n"
    "ds_read_b64_tr_b16 a[114:115], v17 offset:12808\n"
    "ds_read_b64_tr_b16 a[116:117], v19 offset:12288\n"
    "ds_read_b64_tr_b16 a[118:119], v19 offset:12800\n"
    "s_waitcnt vmcnt(24) lgkmcnt(0)\n"
    "s_barrier\n"
    "s_add_u32 m0, s59, 98304\n"
    "s_mov_b32 s74, 2949120\n"
    "buffer_load_dwordx4 v5, s[40:43], s74 offen lds\n"
    "s_add_u32 m0, s59, 102400\n"
    "s_mov_b32 s74, 2949248\n"
    "buffer_load_dwordx4 v5, s[40:43], s74 offen lds\n"
    "s_add_u32 m0, s59, 106496\n"
    "s_mov_b32 s74, 2949376\n"
    "buffer_load_dwordx4 v5, s[40:43], s74 offen lds\n"
    "s_add_u32 m0, s59, 110592\n"
    "s_mov_b32 s74, 2949120\n"
    "buffer_load_dwordx4 v5, s[44:47], s74 offen lds\n"
    "s_add_u32 m0, s59, 114688\n"
    "s_mov_b32 s74, 2949248\n"
    "buffer_load_dwordx4 v5, s[44:47], s74 offen lds\n"
    "s_add_u32 m0, s59, 118784\n"
    "s_mov_b32 s74, 2949376\n"
    "buffer_load_dwordx4 v5, s[44:47], s74 offen lds\n"
    "ds_read_b64_tr_b16 a[120:121], v17 offset:24576\n"
    "ds_read_b64_tr_b16 a[122:123], v17 offset:25088\n"
    "ds_read_b64_tr_b16 a[124:125], v17 offset:24584\n"
    "ds_read_b64_tr_b16 a[126:127], v17 offset:25096\n"
    "ds_read_b64_tr_b16 a[128:129], v19 offset:24576\n"
    "ds_read_b64_tr_b16 a[130:131], v19 offset:25088\n"
    "ds_read_b64_tr_b16 a[132:133], v17 offset:36864\n"
    "ds_read_b64_tr_b16 a[134:135], v17 offset:37376\n"
    "ds_read_b64_tr_b16 a[136:137], v17 offset:36872\n"
    "ds_read_b64_tr_b16 a[138:139], v17 offset:37384\n"
    "ds_read_b64_tr_b16 a[140:141], v19 offset:36864\n"
    "ds_read_b64_tr_b16 a[142:143], v19 offset:37376\n"
    "s_waitcnt vmcnt(24) lgkmcnt(0)\n"
    "s_barrier\n"
    "s_add_u32 m0, s59, 122880\n"
    "s_mov_b32 s74, 3244032\n"
    "buffer_load_dwordx4 v5, s[40:43], s74 offen lds\n"
    "s_add_u32 m0, s59, 126976\n"
    "s_mov_b32 s74, 3244160\n"
    "buffer_load_dwordx4 v5, s[40:43], s74 offen lds\n"
    "s_add_u32 m0, s59, 131072\n"
    "s_mov_b32 s74, 3244288\n"
    "buffer_load_dwordx4 v5, s[40:43], s74 offen lds\n"
    "s_add_u32 m0, s59, 135168\n"
    "s_mov_b32 s74, 3244032\n"
    "buffer_load_dwordx4 v5, s[44:47], s74 offen lds\n"
    "s_add_u32 m0, s59, 139264\n"
    "s_mov_b32 s74, 3244160\n"
    "buffer_load_dwordx4 v5, s[44:47], s74 offen lds\n"
    "s_add_u32 m0, s59, 143360\n"
    "s_mov_b32 s74, 3244288\n"
    "buffer_load_dwordx4 v5, s[44:47], s74 offen lds\n"
    "ds_read_b64_tr_b16 a[144:145], v6 offset:0\n"
    "ds_read_b64_tr_b16 a[146:147], v6 offset:512\n"
    "ds_read_b64_tr_b16 a[148:149], v6 offset:8\n"
    "ds_read_b64_tr_b16 a[150:151], v6 offset:520\n"
    "ds_read_b64_tr_b16 a[152:153], v7 offset:0\n"
    "ds_read_b64_tr_b16 a[154:155], v7 offset:512\n"
    "ds_read_b64_tr_b16 a[156:157], v6 offset:12288\n"
    "ds_read_b64_tr_b16 a[158:159], v6 offset:12800\n"
    "ds_read_b64_tr_b16 a[160:161], v6 offset:12296\n"
    "ds_read_b64_tr_b16 a[162:163], v6 offset:12808\n"
    "ds_read_b64_tr_b16 a[164:165], v7 offset:12288\n"
    "ds_read_b64_tr_b16 a[166:167], v7 offset:12800\n"
    "s_waitcnt vmcnt(24) lgkmcnt(0)\n"
    "s_barrier\n"
    "s_add_u32 m0, s59, 0\n"
    "s_mov_b32 s74, 3538944\n"
    "buffer_load_dwordx4 v5, s[40:43], s74 offen lds\n"
    "s_add_u32 m0, s59, 4096\n"
    "s_mov_b32 s74, 3539072\n"
    "buffer_load_dwordx4 v5, s[40:43], s74 offen lds\n"
    "s_add_u32 m0, s59, 8192\n"
    "s_mov_b32 s74, 3539200\n"
    "buffer_load_dwordx4 v5, s[40:43], s74 offen lds\n"
    "s_add_u32 m0, s59, 12288\n"
    "s_mov_b32 s74, 3538944\n"
    "buffer_load_dwordx4 v5, s[44:47], s74 offen lds\n"
    "s_add_u32 m0, s59, 16384\n"
    "s_mov_b32 s74, 3539072\n"
    "buffer_load_dwordx4 v5, s[44:47], s74 offen lds\n"
    "s_add_u32 m0, s59, 20480\n"
    "s_mov_b32 s74, 3539200\n"
    "buffer_load_dwordx4 v5, s[44:47], s74 offen lds\n"
    "ds_read_b64_tr_b16 a[168:169], v6 offset:24576\n"
    "ds_read_b64_tr_b16 a[170:171], v6 offset:25088\n"
    "ds_read_b64_tr_b16 a[172:173], v6 offset:24584\n"
    "ds_read_b64_tr_b16 a[174:175], v6 offset:25096\n"
    "ds_read_b64_tr_b16 a[176:177], v7 offset:24576\n"
    "ds_read_b64_tr_b16 a[178:179], v7 offset:25088\n"
    "ds_read_b64_tr_b16 a[180:181], v6 offset:36864\n"
    "ds_read_b64_tr_b16 a[182:183], v6 offset:37376\n"
    "ds_read_b64_tr_b16 a[184:185], v6 offset:36872\n"
    "ds_read_b64_tr_b16 a[186:187], v6 offset:37384\n"
    "ds_read_b64_tr_b16 a[188:189], v7 offset:36864\n"
    "ds_read_b64_tr_b16 a[190:191], v7 offset:37376\n"
    "s_waitcnt vmcnt(24) lgkmcnt(0)\n"
    "s_barrier\n"
    "s_add_u32 m0, s59, 24576\n"
    "s_mov_b32 s74, 3833856\n"
    "buffer_load_dwordx4 v5, s[40:43], s74 offen lds\n"
    "s_add_u32 m0, s59, 28672\n"
    "s_mov_b32 s74, 3833984\n"
    "buffer_load_dwordx4 v5, s[40:43], s74 offen lds\n"
    "s_add_u32 m0, s59, 32768\n"
    "s_mov_b32 s74, 3834112\n"
    "buffer_load_dwordx4 v5, s[40:43], s74 offen lds\n"
    "s_add_u32 m0, s59, 36864\n"
    "s_mov_b32 s74, 3833856\n"
    "buffer_load_dwordx4 v5, s[44:47], s74 offen lds\n"
    "s_add_u32 m0, s59, 40960\n"
    "s_mov_b32 s74, 3833984\n"
    "buffer_load_dwordx4 v5, s[44:47], s74 offen lds\n"
    "s_add_u32 m0, s59, 45056\n"
    "s_mov_b32 s74, 3834112\n"
    "buffer_load_dwordx4 v5, s[44:47], s74 offen lds\n"
    "ds_read_b64_tr_b16 a[192:193], v16 offset:0\n"
    "ds_read_b64_tr_b16 a[194:195], v16 offset:512\n"
    "ds_read_b64_tr_b16 a[196:197], v16 offset:8\n"
    "ds_read_b64_tr_b16 a[198:199], v16 offset:520\n"
    "ds_read_b64_tr_b16 a[200:201], v18 offset:0\n"
    "ds_read_b64_tr_b16 a[202:203], v18 offset:512\n"
    "ds_read_b64_tr_b16 a[204:205], v16 offset:12288\n"
    "ds_read_b64_tr_b16 a[206:207], v16 offset:12800\n"
    "ds_read_b64_tr_b16 a[208:209], v16 offset:12296\n"
    "ds_read_b64_tr_b16 a[210:211], v16 offset:12808\n"
    "ds_read_b64_tr_b16 a[212:213], v18 offset:12288\n"
    "ds_read_b64_tr_b16 a[214:215], v18 offset:12800\n"
    "s_waitcnt vmcnt(24) lgkmcnt(0)\n"
    "s_barrier\n"
    "s_add_u32 m0, s59, 49152\n"
    "s_mov_b32 s74, 4128768\n"
    "buffer_load_dwordx4 v5, s[40:43], s74 offen lds\n"
    "s_add_u32 m0, s59, 53248\n"
    "s_mov_b32 s74, 4128896\n"
    "buffer_load_dwordx4 v5, s[40:43], s74 offen lds\n"
    "s_add_u32 m0, s59, 57344\n"
    "s_mov_b32 s74, 4129024\n"
    "buffer_load_dwordx4 v5, s[40:43], s74 offen lds\n"
    "s_add_u32 m0, s59, 61440\n"
    "s_mov_b32 s74, 4128768\n"
    "buffer_load_dwordx4 v5, s[44:47], s74 offen lds\n"
    "s_add_u32 m0, s59, 65536\n"
    "s_mov_b32 s74, 4128896\n"
    "buffer_load_dwordx4 v5, s[44:47], s74 offen lds\n"
    "s_add_u32 m0, s59, 69632\n"
    "s_mov_b32 s74, 4129024\n"
    "buffer_load_dwordx4 v5, s[44:47], s74 offen lds\n"
    "ds_read_b64_tr_b16 a[216:217], v16 offset:24576\n"
    "ds_read_b64_tr_b16 a[218:219], v16 offset:25088\n"
    "ds_read_b64_tr_b16 a[220:221], v16 offset:24584\n"
    "ds_read_b64_tr_b16 a[222:223], v16 offset:25096\n"
    "ds_read_b64_tr_b16 a[224:225], v18 offset:24576\n"
    "ds_read_b64_tr_b16 a[226:227], v18 offset:25088\n"
    "ds_read_b64_tr_b16 a[228:229], v16 offset:36864\n"
    "ds_read_b64_tr_b16 a[230:231], v16 offset:37376\n"
    "ds_read_b64_tr_b16 a[232:233], v16 offset:36872\n"
    "ds_read_b64_tr_b16 a[234:235], v16 offset:37384\n"
    "ds_read_b64_tr_b16 a[236:237], v18 offset:36864\n"
    "ds_read_b64_tr_b16 a[238:239], v18 offset:37376\n"
    "s_waitcnt vmcnt(24) lgkmcnt(0)\n"
    "s_barrier\n"
    "s_add_u32 m0, s59, 73728\n"
    "s_mov_b32 s74, 4423680\n"
    "buffer_load_dwordx4 v5, s[40:43], s74 offen lds\n"
    "s_add_u32 m0, s59, 77824\n"
    "s_mov_b32 s74, 4423808\n"
    "buffer_load_dwordx4 v5, s[40:43], s74 offen lds\n"
    "s_add_u32 m0, s59, 81920\n"
    "s_mov_b32 s74, 4423936\n"
    "buffer_load_dwordx4 v5, s[40:43], s74 offen lds\n"
    "s_add_u32 m0, s59, 86016\n"
    "s_mov_b32 s74, 4423680\n"
    "buffer_load_dwordx4 v5, s[44:47], s74 offen lds\n"
    "s_add_u32 m0, s59, 90112\n"
    "s_mov_b32 s74, 4423808\n"
    "buffer_load_dwordx4 v5, s[44:47], s74 offen lds\n"
    "s_add_u32 m0, s59, 94208\n"
    "s_mov_b32 s74, 4423936\n"
    "buffer_load_dwordx4 v5, s[44:47], s74 offen lds\n"
    "ds_read_b64_tr_b16 a[240:241], v17 offset:0\n"
    "ds_read_b64_tr_b16 a[242:243], v17 offset:512\n"
    "ds_read_b64_tr_b16 a[244:245], v17 offset:8\n"
    "ds_read_b64_tr_b16 a[246:247], v17 offset:520\n"
    "ds_read_b64_tr_b16 a[248:249], v19 offset:0\n"
    "ds_read_b64_tr_b16 a[250:251], v19 offset:512\n"
    "ds_read_b64_tr_b16 a[252:253], v17 offset:12288\n"
    "ds_read_b64_tr_b16 a[254:255], v17 offset:12800\n"
    "ds_read_b64_tr_b16 v[80:81], v17 offset:12296\n"
    "ds_read_b64_tr_b16 v[82:83], v17 offset:12808\n"
    "ds_read_b64_tr_b16 v[84:85], v19 offset:12288\n"
    "ds_read_b64_tr_b16 v[86:87], v19 offset:12800\n"
    "s_waitcnt vmcnt(24) lgkmcnt(0)\n"
    "s_barrier\n"
    "s_add_u32 m0, s59, 98304\n"
    "s_mov_b32 s74, 4718592\n"
    "buffer_load_dwordx4 v5, s[40:43], s74 offen lds\n"
    "s_add_u32 m0, s59, 102400\n"
    "s_mov_b32 s74, 4718720\n"
    "buffer_load_dwordx4 v5, s[40:43], s74 offen lds\n"
    "s_add_u32 m0, s59, 106496\n"
    "s_mov_b32 s74, 4718848\n"
    "buffer_load_dwordx4 v5, s[40:43], s74 offen lds\n"
    "s_add_u32 m0, s59, 110592\n"
    "s_mov_b32 s74, 4718592\n"
    "buffer_load_dwordx4 v5, s[44:47], s74 offen lds\n"
    "s_add_u32 m0, s59, 114688\n"
    "s_mov_b32 s74, 4718720\n"
    "buffer_load_dwordx4 v5, s[44:47], s74 offen lds\n"
    "s_add_u32 m0, s59, 118784\n"
    "s_mov_b32 s74, 4718848\n"
    "buffer_load_dwordx4 v5, s[44:47], s74 offen lds\n"
    "ds_read_b64_tr_b16 v[88:89], v17 offset:24576\n"
    "ds_read_b64_tr_b16 v[90:91], v17 offset:25088\n"
    "ds_read_b64_tr_b16 v[92:93], v17 offset:24584\n"
    "ds_read_b64_tr_b16 v[94:95], v17 offset:25096\n"
    "ds_read_b64_tr_b16 v[96:97], v19 offset:24576\n"
    "ds_read_b64_tr_b16 v[98:99], v19 offset:25088\n"
    "ds_read_b64_tr_b16 v[100:101], v17 offset:36864\n"
    "ds_read_b64_tr_b16 v[102:103], v17 offset:37376\n"
    "ds_read_b64_tr_b16 v[104:105], v17 offset:36872\n"
    "ds_read_b64_tr_b16 v[106:107], v17 offset:37384\n"
    "ds_read_b64_tr_b16 v[108:109], v19 offset:36864\n"
    "ds_read_b64_tr_b16 v[110:111], v19 offset:37376\n"
    "s_waitcnt vmcnt(24) lgkmcnt(0)\n"
    "s_barrier\n"
    "s_add_u32 m0, s59, 122880\n"
    "s_mov_b32 s74, 5013504\n"
    "buffer_load_dwordx4 v5, s[40:43], s74 offen lds\n"
    "s_add_u32 m0, s59, 126976\n"
    "s_mov_b32 s74, 5013632\n"
    "buffer_load_dwordx4 v5, s[40:43], s74 offen lds\n"
    "s_add_u32 m0, s59, 131072\n"
    "s_mov_b32 s74, 5013760\n"
    "buffer_load_dwordx4 v5, s[40:43], s74 offen lds\n"
    "s_add_u32 m0, s59, 135168\n"
    "s_mov_b32 s74, 5013504\n"
    "buffer_load_dwordx4 v5, s[44:47], s74 offen lds\n"
    "s_add_u32 m0, s59, 139264\n"
    "s_mov_b32 s74, 5013632\n"
    "buffer_load_dwordx4 v5, s[44:47], s74 offen lds\n"
    "s_add_u32 m0, s59, 143360\n"
    "s_mov_b32 s74, 5013760\n"
    "buffer_load_dwordx4 v5, s[44:47], s74 offen lds\n"
    "ds_read_b64_tr_b16 v[112:113], v6 offset:0\n"
    "ds_read_b64_tr_b16 v[114:115], v6 offset:512\n"
    "ds_read_b64_tr_b16 v[116:117], v6 offset:8\n"
    "ds_read_b64_tr_b16 v[118:119], v6 offset:520\n"
    "ds_read_b64_tr_b16 v[120:121], v7 offset:0\n"
    "ds_read_b64_tr_b16 v[122:123], v7 offset:512\n"
    "ds_read_b64_tr_b16 v[124:125], v6 offset:12288\n"
    "ds_read_b64_tr_b16 v[126:127], v6 offset:12800\n"
    "ds_read_b64_tr_b16 v[128:129], v6 offset:12296\n"
    "ds_read_b64_tr_b16 v[130:131], v6 offset:12808\n"
    "ds_read_b64_tr_b16 v[132:133], v7 offset:12288\n"
    "ds_read_b64_tr_b16 v[134:135], v7 offset:12800\n"
    "s_waitcnt vmcnt(24) lgkmcnt(0)\n"
    "s_barrier\n"
    "ds_read_b64_tr_b16 v[136:137], v6 offset:24576\n"
    "ds_read_b64_tr_b16 v[138:139], v6 offset:25088\n"
    "ds_read_b64_tr_b16 v[140:141], v6 offset:24584\n"
    "ds_read_b64_tr_b16 v[142:143], v6 offset:25096\n"
    "ds_read_b64_tr_b16 v[144:145], v7 offset:24576\n"
    "ds_read_b64_tr_b16 v[146:147], v7 offset:25088\n"
    "ds_read_b64_tr_b16 v[148:149], v6 offset:36864\n"
    "ds_read_b64_tr_b16 v[150:151], v6 offset:37376\n"
    "ds_read_b64_tr_b16 v[152:153], v6 offset:36872\n"
    "ds_read_b64_tr_b16 v[154:155], v6 offset:37384\n"
    "ds_read_b64_tr_b16 v[156:157], v7 offset:36864\n"
    "ds_read_b64_tr_b16 v[158:159], v7 offset:37376\n"
    "s_waitcnt vmcnt(18) lgkmcnt(0)\n"
    "s_barrier\n"
    "ds_read_b64_tr_b16 v[160:161], v16 offset:0\n"
    "ds_read_b64_tr_b16 v[162:163], v16 offset:512\n"
    "ds_read_b64_tr_b16 v[164:165], v16 offset:8\n"
    "ds_read_b64_tr_b16 v[166:167], v16 offset:520\n"
    "ds_read_b64_tr_b16 v[168:169], v18 offset:0\n"
    "ds_read_b64_tr_b16 v[170:171], v18 offset:512\n"
    "ds_read_b64_tr_b16 v[172:173], v16 offset:12288\n"
    "ds_read_b64_tr_b16 v[174:175], v16 offset:12800\n"
    "ds_read_b64_tr_b16 v[176:177], v16 offset:12296\n"
    "ds_read_b64_tr_b16 v[178:179], v16 offset:12808\n"
    "ds_read_b64_tr_b16 v[180:181], v18 offset:12288\n"
    "ds_read_b64_tr_b16 v[182:183], v18 offset:12800\n"
    "s_waitcnt vmcnt(12) lgkmcnt(0)\n"
    "s_barrier\n"
    "ds_read_b64_tr_b16 v[184:185], v16 offset:24576\n"
    "ds_read_b64_tr_b16 v[186:187], v16 offset:25088\n"
    "ds_read_b64_tr_b16 v[188:189], v16 offset:24584\n"
    "ds_read_b64_tr_b16 v[190:191], v16 offset:25096\n"
    "ds_read_b64_tr_b16 v[192:193], v18 offset:24576\n"
    "ds_read_b64_tr_b16 v[194:195], v18 offset:25088\n"
    "ds_read_b64_tr_b16 v[196:197], v16 offset:36864\n"
    "ds_read_b64_tr_b16 v[198:199], v16 offset:37376\n"
    "ds_read_b64_tr_b16 v[200:201], v16 offset:36872\n"
    "ds_read_b64_tr_b16 v[202:203], v16 offset:37384\n"
    "ds_read_b64_tr_b16 v[204:205], v18 offset:36864\n"
    "ds_read_b64_tr_b16 v[206:207], v18 offset:37376\n"
    "s_waitcnt vmcnt(6) lgkmcnt(0)\n"
    "s_barrier\n"
    "ds_read_b64_tr_b16 v[208:209], v17 offset:0\n"
    "ds_read_b64_tr_b16 v[210:211], v17 offset:512\n"
    "ds_read_b64_tr_b16 v[212:213], v17 offset:8\n"
    "ds_read_b64_tr_b16 v[214:215], v17 offset:520\n"
    "ds_read_b64_tr_b16 v[216:217], v19 offset:0\n"
    "ds_read_b64_tr_b16 v[218:219], v19 offset:512\n"
    "ds_read_b64_tr_b16 v[220:221], v17 offset:12288\n"
    "ds_read_b64_tr_b16 v[222:223], v17 offset:12800\n"
    "ds_read_b64_tr_b16 v[224:225], v17 offset:12296\n"
    "ds_read_b64_tr_b16 v[226:227], v17 offset:12808\n"
    "ds_read_b64_tr_b16 v[228:229], v19 offset:12288\n"
    "ds_read_b64_tr_b16 v[230:231], v19 offset:12800\n"
    "s_waitcnt vmcnt(0) lgkmcnt(0)\n"
    "s_barrier\n"
    "ds_read_b64_tr_b16 v[232:233], v17 offset:24576\n"
    "ds_read_b64_tr_b16 v[234:235], v17 offset:25088\n"
    "ds_read_b64_tr_b16 v[236:237], v17 offset:24584\n"
    "ds_read_b64_tr_b16 v[238:239], v17 offset:25096\n"
    "ds_read_b64_tr_b16 v[240:241], v19 offset:24576\n"
    "ds_read_b64_tr_b16 v[242:243], v19 offset:25088\n"
    "ds_read_b64_tr_b16 v[244:245], v17 offset:36864\n"
    "ds_read_b64_tr_b16 v[246:247], v17 offset:37376\n"
    "ds_read_b64_tr_b16 v[248:249], v17 offset:36872\n"
    "ds_read_b64_tr_b16 v[250:251], v17 offset:37384\n"
    "ds_read_b64_tr_b16 v[252:253], v19 offset:36864\n"
    "ds_read_b64_tr_b16 v[254:255], v19 offset:37376\n"
    "s_waitcnt lgkmcnt(0)\n"
    "s_barrier\n"
    "s_mov_b32 s68, 0\n"
    "s_mov_b32 s71, 0\n"
    "v_mov_b32 v11, -1\n"
    "s_mov_b32 s72, 262144\n"
    "s_branch .LgUP_block_%=\n"
    ".LgUP_LOOPB_%=:\n"
    ".LgUP_top_%=:\n"
    "s_waitcnt vmcnt(3)\n"
    ".LgUP_bar_%=:\n"
    "s_and_b32 s78, s68, 1\n"
    "s_mul_i32 s78, s78, 36864\n"
    "v_add_u32 v9, s78, v0\n"
    "s_add_u32 s71, s68, 1\n"
    "s_cmp_eq_u32 s71, 64\n"
    "s_cselect_b32 s70, 1, 0\n"
    "s_barrier\n"
    "ds_read_b128 v[16:19], v9 offset:0\n"
    "ds_read_b128 v[20:23], v9 offset:1024\n"
    "v_add_u32 v13, v11, v3\n"
    "v_cmp_gt_i32 vcc, s71, v13\n"
    "s_nop 0\n"
    "s_or_b64 exec, vcc, 1\n"
    "buffer_atomic_smax v11, v2, s[36:39], 0 offen sc0 sc1\n"
    "s_mov_b64 exec, -1\n"
    "s_and_b32 s73, s68, s57\n"
    "s_mul_i32 s73, s73, s56\n"
    "s_add_u32 s73, s73, s55\n"
    "s_waitcnt lgkmcnt(0)\n"
    "v_mfma_f32_16x16x32_bf16 v[32:35], a[0:3], v[16:19], 0\n"
    "ds_read_b128 v[24:27], v9 offset:2048\n"
    "ds_read_b128 v[28:31], v9 offset:3072\n"
    "v_mfma_f32_16x16x32_bf16 v[40:43], a[0:3], v[20:23], 0\n"
    "v_mfma_f32_16x16x32_bf16 v[36:39], a[4:7], v[16:19], 0\n"
    "v_mfma_f32_16x16x32_bf16 v[44:47], a[4:7], v[20:23], 0\n"
    "v_mfma_f32_16x16x32_bf16 v[48:51], a[8:11], v[16:19], 0\n"
    "v_mfma_f32_16x16x32_bf16 v[52:55], a[8:11], v[20:23], 0\n"
    "v_mfma_f32_16x16x32_bf16 v[56:59], a[12:15], v[16:19], 0\n"
    "v_mfma_f32_16x16x32_bf16 v[64:67], a[12:15], v[20:23], 0\n"
    "v_mfma_f32_16x16x32_bf16 v[60:63], a[16:19], v[16:19], 0\n"
    "v_mfma_f32_16x16x32_bf16 v[68:71], a[16:19], v[20:23], 0\n"
    "v_mfma_f32_16x16x32_bf16 v[72:75], a[20:23], v[16:19], 0\n"
    "v_mfma_f32_16x16x32_bf16 v[76:79], a[20:23], v[20:23], 0\n"
    "s_waitcnt lgkmcnt(0)\n"
    "v_mfma_f32_16x16x32_bf16 v[32:35], a[24:27], v[24:27], v[32:35]\n"
    "ds_read_b128 v[16:19], v9 offset:4096\n"
    "ds_read_b128 v[20:23], v9 offset:5120\n"
    "v_mfma_f32_16x16x32_bf16 v[40:43], a[24:27], v[28:31], v[40:43]\n"
    "v_mfma_f32_16x16x32_bf16 v[36:39], a[28:31], v[24:27], v[36:39]\n"
    "v_mfma_f32_16x16x32_bf16 v[44:47], a[28:31], v[28:31], v[44:47]\n"
    "v_mfma_f32_16x16x32_bf16 v[48:51], a[32:35], v[24:27], v[48:51]\n"
    "v_mfma_f32_16x16x32_bf16 v[52:55], a[32:35], v[28:31], v[52:55]\n"
    "v_mfma_f32_16x16x32_bf16 v[56:59], a[36:39], v[24:27], v[56:59]\n"
    "v_mfma_f32_16x16x32_bf16 v[64:67], a[36:39], v[28:31], v[64:67]\n"
    "v_mfma_f32_16x16x32_bf16 v[60:63], a[40:43], v[24:27], v[60:63]\n"
    "v_mfma_f32_16x16x32_bf16 v[68:71], a[40:43], v[28:31], v[68:71]\n"
    "v_mfma_f32_16x16x32_bf16 v[72:75], a[44:47], v[24:27], v[72:75]\n"
    "v_mfma_f32_16x16x32_bf16 v[76:79], a[44:47], v[28:31], v[76:79]\n"
    "s_waitcnt lgkmcnt(0)\n"
    "v_mfma_f32_16x16x32_bf16 v[32:35], a[48:51], v[16:19], v[32:35]\n"
    "ds_read_b128 v[24:27], v9 offset:6144\n"
    "ds_read_b128 v[28:31], v9 offset:7168\n"
    "v_mfma_f32_16x16x32_bf16 v[40:43], a[48:51], v[20:23], v[40:43]\n"
    "v_mfma_f32_16x16x32_bf16 v[36:39], a[52:55], v[16:19], v[36:39]\n"
    "v_mfma_f32_16x16x32_bf16 v[44:47], a[52:55], v[20:23], v[44:47]\n"
    "v_mfma_f32_16x16x32_bf16 v[48:51], a[56:59], v[16:19], v[48:51]\n"
    "v_mfma_f32_16x16x32_bf16 v[52:55], a[56:59], v[20:23], v[52:55]\n"
    "v_mfma_f32_16x16x32_bf16 v[56:59], a[60:63], v[16:19], v[56:59]\n"
    "v_mfma_f32_16x16x32_bf16 v[64:67], a[60:63], v[20:23], v[64:67]\n"
    "v_mfma_f32_16x16x32_bf16 v[60:63], a[64:67], v[16:19], v[60:63]\n"
    "v_mfma_f32_16x16x32_bf16 v[68:71], a[64:67], v[20:23], v[68:71]\n"
    "v_mfma_f32_16x16x32_bf16 v[72:75], a[68:71], v[16:19], v[72:75]\n"
    "v_mfma_f32_16x16x32_bf16 v[76:79], a[68:71], v[20:23], v[76:79]\n"
    "s_waitcnt vmcnt(0)\n"
    "s_cmp_eq_u32 s69, 0\n"
    "s_cbranch_scc1 .LgUP_nopub_%=\n"
    "s_sub_u32 s74, s68, 1\n"
    "v_mov_b32 v12, s74\n"
    "s_mov_b64 exec, s[62:63]\n"
    "buffer_store_dword v12, v14, s[36:39], 0 offen sc1\n"
    "s_mov_b64 exec, -1\n"
    ".LgUP_nopub_%=:\n"
    "s_cmp_lg_u32 s70, 0\n"
    "s_cbranch_scc1 .LgUP_plain0_%=\n"
    "v_add_u32 v13, v11, v3\n"
    "v_cmp_gt_i32 vcc, s71, v13\n"
    "s_cbranch_vccz .LgUP_dma0_%=\n"
    "s_or_b64 exec, vcc, 1\n"
    "buffer_atomic_smax v11, v2, s[36:39], 0 offen sc0 sc1\n"
    "s_mov_b64 exec, -1\n"
    ".LgUP_plain0_%=:\n"
    "s_waitcnt lgkmcnt(0)\n"
    "v_mfma_f32_16x16x32_bf16 v[32:35], a[72:75], v[24:27], v[32:35]\n"
    "ds_read_b128 v[16:19], v9 offset:8192\n"
    "ds_read_b128 v[20:23], v9 offset:9216\n"
    "v_mfma_f32_16x16x32_bf16 v[40:43], a[72:75], v[28:31], v[40:43]\n"
    "v_mfma_f32_16x16x32_bf16 v[36:39], a[76:79], v[24:27], v[36:39]\n"
    "v_mfma_f32_16x16x32_bf16 v[44:47], a[76:79], v[28:31], v[44:47]\n"
    "v_mfma_f32_16x16x32_bf16 v[48:51], a[80:83], v[24:27], v[48:51]\n"
    "v_mfma_f32_16x16x32_bf16 v[52:55], a[80:83], v[28:31], v[52:55]\n"
    "v_mfma_f32_16x16x32_bf16 v[56:59], a[84:87], v[24:27], v[56:59]\n"
    "v_mfma_f32_16x16x32_bf16 v[64:67], a[84:87], v[28:31], v[64:67]\n"
    "v_mfma_f32_16x16x32_bf16 v[60:63], a[88:91], v[24:27], v[60:63]\n"
    "v_mfma_f32_16x16x32_bf16 v[68:71], a[88:91], v[28:31], v[68:71]\n"
    "v_mfma_f32_16x16x32_bf16 v[72:75], a[92:95], v[24:27], v[72:75]\n"
    "v_mfma_f32_16x16x32_bf16 v[76:79], a[92:95], v[28:31], v[76:79]\n"
    "s_waitcnt lgkmcnt(0)\n"
    "v_mfma_f32_16x16x32_bf16 v[32:35], a[96:99], v[16:19], v[32:35]\n"
    "ds_read_b128 v[24:27], v9 offset:10240\n"
    "ds_read_b128 v[28:31], v9 offset:11264\n"
    "v_mfma_f32_16x16x32_bf16 v[40:43], a[96:99], v[20:23], v[40:43]\n"
    "v_mfma_f32_16x16x32_bf16 v[36:39], a[100:103], v[16:19], v[36:39]\n"
    "v_mfma_f32_16x16x32_bf16 v[44:47], a[100:103], v[20:23], v[44:47]\n"
    "v_mfma_f32_16x16x32_bf16 v[48:51], a[104:107], v[16:19], v[48:51]\n"
    "v_mfma_f32_16x16x32_bf16 v[52:55], a[104:107], v[20:23], v[52:55]\n"
    "v_mfma_f32_16x16x32_bf16 v[56:59], a[108:111], v[16:19], v[56:59]\n"
    "v_mfma_f32_16x16x32_bf16 v[64:67], a[108:111], v[20:23], v[64:67]\n"
    "v_mfma_f32_16x16x32_bf16 v[60:63], a[112:115], v[16:19], v[60:63]\n"
    "v_mfma_f32_16x16x32_bf16 v[68:71], a[112:115], v[20:23], v[68:71]\n"
    "v_mfma_f32_16x16x32_bf16 v[72:75], a[116:119], v[16:19], v[72:75]\n"
    "v_mfma_f32_16x16x32_bf16 v[76:79], a[116:119], v[20:23], v[76:79]\n"
    "s_waitcnt lgkmcnt(0)\n"
    "v_mfma_f32_16x16x32_bf16 v[32:35], a[120:123], v[24:27], v[32:35]\n"
    "ds_read_b128 v[16:19], v9 offset:12288\n"
    "ds_read_b128 v[20:23], v9 offset:13312\n"
    "v_mfma_f32_16x16x32_bf16 v[40:43], a[120:123], v[28:31], v[40:43]\n"
    "v_mfma_f32_16x16x32_bf16 v[36:39], a[124:127], v[24:27], v[36:39]\n"
    "v_mfma_f32_16x16x32_bf16 v[44:47], a[124:127], v[28:31], v[44:47]\n"
    "v_mfma_f32_16x16x32_bf16 v[48:51], a[128:131], v[24:27], v[48:51]\n"
    "v_mfma_f32_16x16x32_bf16 v[52:55], a[128:131], v[28:31], v[52:55]\n"
    "v_mfma_f32_16x16x32_bf16 v[56:59], a[132:135], v[24:27], v[56:59]\n"
    "v_mfma_f32_16x16x32_bf16 v[64:67], a[132:135], v[28:31], v[64:67]\n"
    "v_mfma_f32_16x16x32_bf16 v[60:63], a[136:139], v[24:27], v[60:63]\n"
    "v_mfma_f32_16x16x32_bf16 v[68:71], a[136:139], v[28:31], v[68:71]\n"
    "v_mfma_f32_16x16x32_bf16 v[72:75], a[140:143], v[24:27], v[72:75]\n"
    "v_mfma_f32_16x16x32_bf16 v[76:79], a[140:143], v[28:31], v[76:79]\n"
    "s_waitcnt lgkmcnt(0)\n"
    "v_mfma_f32_16x16x32_bf16 v[32:35], a[144:147], v[16:19], v[32:35]\n"
    "ds_read_b128 v[24:27], v9 offset:14336\n"
    "ds_read_b128 v[28:31], v9 offset:15360\n"
    "v_mfma_f32_16x16x32_bf16 v[40:43], a[144:147], v[20:23], v[40:43]\n"
    "v_mfma_f32_16x16x32_bf16 v[36:39], a[148:151], v[16:19], v[36:39]\n"
    "v_mfma_f32_16x16x32_bf16 v[44:47], a[148:151], v[20:23], v[44:47]\n"
    "v_mfma_f32_16x16x32_bf16 v[48:51], a[152:155], v[16:19], v[48:51]\n"
    "v_mfma_f32_16x16x32_bf16 v[52:55], a[152:155], v[20:23], v[52:55]\n"
    "v_mfma_f32_16x16x32_bf16 v[56:59], a[156:159], v[16:19], v[56:59]\n"
    "v_mfma_f32_16x16x32_bf16 v[64:67], a[156:159], v[20:23], v[64:67]\n"
    "v_mfma_f32_16x16x32_bf16 v[60:63], a[160:163], v[16:19], v[60:63]\n"
    "v_mfma_f32_16x16x32_bf16 v[68:71], a[160:163], v[20:23], v[68:71]\n"
    "v_mfma_f32_16x16x32_bf16 v[72:75], a[164:167], v[16:19], v[72:75]\n"
    "v_mfma_f32_16x16x32_bf16 v[76:79], a[164:167], v[20:23], v[76:79]\n"
    ".LgUP_seg0_%=:\n"
    "s_cmp_lg_u32 s70, 0\n"
    "s_cbranch_scc1 .LgUP_plain1_%=\n"
    "s_waitcnt vmcnt(0)\n"
    "v_add_u32 v13, v11, v3\n"
    "v_cmp_gt_i32 vcc, s71, v13\n"
    "s_cbranch_vccz .LgUP_dma1_%=\n"
    "s_or_b64 exec, vcc, 1\n"
    "buffer_atomic_smax v11, v2, s[36:39], 0 offen sc0 sc1\n"
    "s_mov_b64 exec, -1\n"
    ".LgUP_plain1_%=:\n"
    "s_waitcnt lgkmcnt(0)\n"
    "v_mfma_f32_16x16x32_bf16 v[32:35], a[168:171], v[24:27], v[32:35]\n"
    "ds_read_b128 v[16:19], v9 offset:16384\n"
    "ds_read_b128 v[20:23], v9 offset:17408\n"
    "v_mfma_f32_16x16x32_bf16 v[40:43], a[168:171], v[28:31], v[40:43]\n"
    "v_mfma_f32_16x16x32_bf16 v[36:39], a[172:175], v[24:27], v[36:39]\n"
    "v_mfma_f32_16x16x32_bf16 v[44:47], a[172:175], v[28:31], v[44:47]\n"
    "v_mfma_f32_16x16x32_bf16 v[48:51], a[176:179], v[24:27], v[48:51]\n"
    "v_mfma_f32_16x16x32_bf16 v[52:55], a[176:179], v[28:31], v[52:55]\n"
    "v_mfma_f32_16x16x32_bf16 v[56:59], a[180:183], v[24:27], v[56:59]\n"
    "v_mfma_f32_16x16x32_bf16 v[64:67], a[180:183], v[28:31], v[64:67]\n"
    "v_mfma_f32_16x16x32_bf16 v[60:63], a[184:187], v[24:27], v[60:63]\n"
    "v_mfma_f32_16x16x32_bf16 v[68:71], a[184:187], v[28:31], v[68:71]\n"
    "v_mfma_f32_16x16x32_bf16 v[72:75], a[188:191], v[24:27], v[72:75]\n"
    "v_mfma_f32_16x16x32_bf16 v[76:79], a[188:191], v[28:31], v[76:79]\n"
    "s_waitcnt lgkmcnt(0)\n"
    "v_mfma_f32_16x16x32_bf16 v[32:35], a[192:195], v[16:19], v[32:35]\n"
    "ds_read_b128 v[24:27], v9 offset:18432\n"
    "ds_read_b128 v[28:31], v9 offset:19456\n"
    "v_mfma_f32_16x16x32_bf16 v[40:43], a[192:195], v[20:23], v[40:43]\n"
    "v_mfma_f32_16x16x32_bf16 v[36:39], a[196:199], v[16:19], v[36:39]\n"
    "v_mfma_f32_16x16x32_bf16 v[44:47], a[196:199], v[20:23], v[44:47]\n"
    "v_mfma_f32_16x16x32_bf16 v[48:51], a[200:203], v[16:19], v[48:51]\n"
    "v_mfma_f32_16x16x32_bf16 v[52:55], a[200:203], v[20:23], v[52:55]\n"
    "v_mfma_f32_16x16x32_bf16 v[56:59], a[204:207], v[16:19], v[56:59]\n"
    "v_mfma_f32_16x16x32_bf16 v[64:67], a[204:207], v[20:23], v[64:67]\n"
    "v_mfma_f32_16x16x32_bf16 v[60:63], a[208:211], v[16:19], v[60:63]\n"
    "v_mfma_f32_16x16x32_bf16 v[68:71], a[208:211], v[20:23], v[68:71]\n"
    "v_mfma_f32_16x16x32_bf16 v[72:75], a[212:215], v[16:19], v[72:75]\n"
    "v_mfma_f32_16x16x32_bf16 v[76:79], a[212:215], v[20:23], v[76:79]\n"
    "s_waitcnt lgkmcnt(0)\n"
    "v_mfma_f32_16x16x32_bf16 v[32:35], a[216:219], v[24:27], v[32:35]\n"
    "ds_read_b128 v[16:19], v9 offset:20480\n"
    "ds_read_b128 v[20:23], v9 offset:21504\n"
    "v_mfma_f32_16x16x32_bf16 v[40:43], a[216:219], v[28:31], v[40:43]\n"
    "v_mfma_f32_16x16x32_bf16 v[36:39], a[220:223], v[24:27], v[36:39]\n"
    "v_mfma_f32_16x16x32_bf16 v[44:47], a[220:223], v[28:31], v[44:47]\n"
    "v_mfma_f32_16x16x32_bf16 v[48:51], a[224:227], v[24:27], v[48:51]\n"
    "v_mfma_f32_16x16x32_bf16 v[52:55], a[224:227], v[28:31], v[52:55]\n"
    "v_mfma_f32_16x16x32_bf16 v[56:59], a[228:231], v[24:27], v[56:59]\n"
    "v_mfma_f32_16x16x32_bf16 v[64:67], a[228:231], v[28:31], v[64:67]\n"
    "v_mfma_f32_16x16x32_bf16 v[60:63], a[232:235], v[24:27], v[60:63]\n"
    "v_mfma_f32_16x16x32_bf16 v[68:71], a[232:235], v[28:31], v[68:71]\n"
    "v_mfma_f32_16x16x32_bf16 v[72:75], a[236:239], v[24:27], v[72:75]\n"
    "v_mfma_f32_16x16x32_bf16 v[76:79], a[236:239], v[28:31], v[76:79]\n"
    "s_waitcnt lgkmcnt(0)\n"
    "v_mfma_f32_16x16x32_bf16 v[32:35], a[240:243], v[16:19], v[32:35]\n"
    "ds_read_b128 v[24:27], v9 offset:22528\n"
    "ds_read_b128 v[28:31], v9 offset:23552\n"
    "v_mfma_f32_16x16x32_bf16 v[40:43], a[240:243], v[20:23], v[40:43]\n"
    "v_mfma_f32_16x16x32_bf16 v[36:39], a[244:247], v[16:19], v[36:39]\n"
    "v_mfma_f32_16x16x32_bf16 v[44:47], a[244:247], v[20:23], v[44:47]\n"
    "v_mfma_f32_16x16x32_bf16 v[48:51], a[248:251], v[16:19], v[48:51]\n"
    "v_mfma_f32_16x16x32_bf16 v[52:55], a[248:251], v[20:23], v[52:55]\n"
    "v_mfma_f32_16x16x32_bf16 v[56:59], a[252:255], v[16:19], v[56:59]\n"
    "v_mfma_f32_16x16x32_bf16 v[64:67], a[252:255], v[20:23], v[64:67]\n"
    "v_mfma_f32_16x16x32_bf16 v[60:63], v[80:83], v[16:19], v[60:63]\n"
    "v_mfma_f32_16x16x32_bf16 v[68:71], v[80:83], v[20:23], v[68:71]\n"
    "v_mfma_f32_16x16x32_bf16 v[72:75], v[84:87], v[16:19], v[72:75]\n"
    "v_mfma_f32_16x16x32_bf16 v[76:79], v[84:87], v[20:23], v[76:79]\n"
    ".LgUP_seg1_%=:\n"
    "s_cmp_lg_u32 s70, 0\n"
    "s_cbranch_scc1 .LgUP_plain2_%=\n"
    "s_waitcnt vmcnt(0)\n"
    "v_add_u32 v13, v11, v3\n"
    "v_cmp_gt_i32 vcc, s71, v13\n"
    "s_cbranch_vccz .LgUP_dma2_%=\n"
    ".LgUP_plain2_%=:\n"
    "s_waitcnt lgkmcnt(0)\n"
    "v_mfma_f32_16x16x32_bf16 v[32:35], v[88:91], v[24:27], v[32:35]\n"
    "ds_read_b128 v[16:19], v9 offset:24576\n"
    "ds_read_b128 v[20:23], v9 offset:25600\n"
    "v_mfma_f32_16x16x32_bf16 v[40:43], v[88:91], v[28:31], v[40:43]\n"
    "v_mfma_f32_16x16x32_bf16 v[36:39], v[92:95], v[24:27], v[36:39]\n"
    "v_mfma_f32_16x16x32_bf16 v[44:47], v[92:95], v[28:31], v[44:47]\n"
    "v_mfma_f32_16x16x32_bf16 v[48:51], v[96:99], v[24:27], v[48:51]\n"
    "v_mfma_f32_16x16x32_bf16 v[52:55], v[96:99], v[28:31], v[52:55]\n"
    "v_mfma_f32_16x16x32_bf16 v[56:59], v[100:103], v[24:27], v[56:59]\n"
    "v_mfma_f32_16x16x32_bf16 v[64:67], v[100:103], v[28:31], v[64:67]\n"
    "v_mfma_f32_16x16x32_bf16 v[60:63], v[104:107], v[24:27], v[60:63]\n"
    "v_mfma_f32_16x16x32_bf16 v[68:71], v[104:107], v[28:31], v[68:71]\n"
    "v_mfma_f32_16x16x32_bf16 v[72:75], v[108:111], v[24:27], v[72:75]\n"
    "v_mfma_f32_16x16x32_bf16 v[76:79], v[108:111], v[28:31], v[76:79]\n"
    "s_waitcnt lgkmcnt(0)\n"
    "v_mfma_f32_16x16x32_bf16 v[32:35], v[112:115], v[16:19], v[32:35]\n"
    "ds_read_b128 v[24:27], v9 offset:26624\n"
    "ds_read_b128 v[28:31], v9 offset:27648\n"
    "v_mfma_f32_16x16x32_bf16 v[40:43], v[112:115], v[20:23], v[40:43]\n"
    "v_mfma_f32_16x16x32_bf16 v[36:39], v[116:119], v[16:19], v[36:39]\n"
    "v_mfma_f32_16x16x32_bf16 v[44:47], v[116:119], v[20:23], v[44:47]\n"
    "v_mfma_f32_16x16x32_bf16 v[48:51], v[120:123], v[16:19], v[48:51]\n"
    "v_mfma_f32_16x16x32_bf16 v[52:55], v[120:123], v[20:23], v[52:55]\n"
    "v_mfma_f32_16x16x32_bf16 v[56:59], v[124:127], v[16:19], v[56:59]\n"
    "v_mfma_f32_16x16x32_bf16 v[64:67], v[124:127], v[20:23], v[64:67]\n"
    "v_mfma_f32_16x16x32_bf16 v[60:63], v[128:131], v[16:19], v[60:63]\n"
    "v_mfma_f32_16x16x32_bf16 v[68:71], v[128:131], v[20:23], v[68:71]\n"
    "v_mfma_f32_16x16x32_bf16 v[72:75], v[132:135], v[16:19], v[72:75]\n"
    "v_mfma_f32_16x16x32_bf16 v[76:79], v[132:135], v[20:23], v[76:79]\n"
    "s_waitcnt lgkmcnt(0)\n"
    "v_mfma_f32_16x16x32_bf16 v[32:35], v[136:139], v[24:27], v[32:35]\n"
    "ds_read_b128 v[16:19], v9 offset:28672\n"
    "ds_read_b128 v[20:23], v9 offset:29696\n"
    "v_mfma_f32_16x16x32_bf16 v[40:43], v[136:139], v[28:31], v[40:43]\n"
    "v_mfma_f32_16x16x32_bf16 v[36:39], v[140:143], v[24:27], v[36:39]\n"
    "v_mfma_f32_16x16x32_bf16 v[44:47], v[140:143], v[28:31], v[44:47]\n"
    "v_mfma_f32_16x16x32_bf16 v[48:51], v[144:147], v[24:27], v[48:51]\n"
    "v_mfma_f32_16x16x32_bf16 v[52:55], v[144:147], v[28:31], v[52:55]\n"
    "v_mfma_f32_16x16x32_bf16 v[56:59], v[148:151], v[24:27], v[56:59]\n"
    "v_mfma_f32_16x16x32_bf16 v[64:67], v[148:151], v[28:31], v[64:67]\n"
    "v_mfma_f32_16x16x32_bf16 v[60:63], v[152:155], v[24:27], v[60:63]\n"
    "v_mfma_f32_16x16x32_bf16 v[68:71], v[152:155], v[28:31], v[68:71]\n"
    "v_mfma_f32_16x16x32_bf16 v[72:75], v[156:159], v[24:27], v[72:75]\n"
    "v_mfma_f32_16x16x32_bf16 v[76:79], v[156:159], v[28:31], v[76:79]\n"
    "s_waitcnt lgkmcnt(0)\n"
    "v_mfma_f32_16x16x32_bf16 v[32:35], v[160:163], v[16:19], v[32:35]\n"
    "ds_read_b128 v[24:27], v9 offset:30720\n"
    "ds_read_b128 v[28:31], v9 offset:31744\n"
    "v_mfma_f32_16x16x32_bf16 v[40:43], v[160:163], v[20:23], v[40:43]\n"
    "v_mfma_f32_16x16x32_bf16 v[36:39], v[164:167], v[16:19], v[36:39]\n"
    "v_mfma_f32_16x16x32_bf16 v[44:47], v[164:167], v[20:23], v[44:47]\n"
    "v_mfma_f32_16x16x32_bf16 v[48:51], v[168:171], v[16:19], v[48:51]\n"
    "v_mfma_f32_16x16x32_bf16 v[52:55], v[168:171], v[20:23], v[52:55]\n"
    "v_mfma_f32_16x16x32_bf16 v[56:59], v[172:175], v[16:19], v[56:59]\n"
    "v_mfma_f32_16x16x32_bf16 v[64:67], v[172:175], v[20:23], v[64:67]\n"
    "v_mfma_f32_16x16x32_bf16 v[60:63], v[176:179], v[16:19], v[60:63]\n"
    "v_mfma_f32_16x16x32_bf16 v[68:71], v[176:179], v[20:23], v[68:71]\n"
    "v_mfma_f32_16x16x32_bf16 v[72:75], v[180:183], v[16:19], v[72:75]\n"
    "v_mfma_f32_16x16x32_bf16 v[76:79], v[180:183], v[20:23], v[76:79]\n"
    ".LgUP_seg2_%=:\n"
    "s_waitcnt lgkmcnt(0)\n"
    "v_mfma_f32_16x16x32_bf16 v[32:35], v[184:187], v[24:27], v[32:35]\n"
    "ds_read_b128 v[16:19], v9 offset:32768\n"
    "ds_read_b128 v[20:23], v9 offset:33792\n"
    "v_mfma_f32_16x16x32_bf16 v[40:43], v[184:187], v[28:31], v[40:43]\n"
    "v_mfma_f32_16x16x32_bf16 v[36:39], v[188:191], v[24:27], v[36:39]\n"
    "v_mfma_f32_16x16x32_bf16 v[44:47], v[188:191], v[28:31], v[44:47]\n"
    "v_mfma_f32_16x16x32_bf16 v[48:51], v[192:195], v[24:27], v[48:51]\n"
    "v_mfma_f32_16x16x32_bf16 v[52:55], v[192:195], v[28:31], v[52:55]\n"
    "v_mfma_f32_16x16x32_bf16 v[56:59], v[196:199], v[24:27], v[56:59]\n"
    "v_mfma_f32_16x16x32_bf16 v[64:67], v[196:199], v[28:31], v[64:67]\n"
    "v_mfma_f32_16x16x32_bf16 v[60:63], v[200:203], v[24:27], v[60:63]\n"
    "v_mfma_f32_16x16x32_bf16 v[68:71], v[200:203], v[28:31], v[68:71]\n"
    "v_mfma_f32_16x16x32_bf16 v[72:75], v[204:207], v[24:27], v[72:75]\n"
    "v_mfma_f32_16x16x32_bf16 v[76:79], v[204:207], v[28:31], v[76:79]\n"
    "s_waitcnt lgkmcnt(0)\n"
    "v_mfma_f32_16x16x32_bf16 v[32:35], v[208:211], v[16:19], v[32:35]\n"
    "ds_read_b128 v[24:27], v9 offset:34816\n"
    "ds_read_b128 v[28:31], v9 offset:35840\n"
    "v_mfma_f32_16x16x32_bf16 v[40:43], v[208:211], v[20:23], v[40:43]\n"
    "v_mfma_f32_16x16x32_bf16 v[36:39], v[212:215], v[16:19], v[36:39]\n"
    "v_mfma_f32_16x16x32_bf16 v[44:47], v[212:215], v[20:23], v[44:47]\n"
    "v_mfma_f32_16x16x32_bf16 v[48:51], v[216:219], v[16:19], v[48:51]\n"
    "v_mfma_f32_16x16x32_bf16 v[52:55], v[216:219], v[20:23], v[52:55]\n"
    "v_mfma_f32_16x16x32_bf16 v[56:59], v[220:223], v[16:19], v[56:59]\n"
    "v_mfma_f32_16x16x32_bf16 v[64:67], v[220:223], v[20:23], v[64:67]\n"
    "v_mfma_f32_16x16x32_bf16 v[60:63], v[224:227], v[16:19], v[60:63]\n"
    "v_mfma_f32_16x16x32_bf16 v[68:71], v[224:227], v[20:23], v[68:71]\n"
    "v_mfma_f32_16x16x32_bf16 v[72:75], v[228:231], v[16:19], v[72:75]\n"
    "v_mfma_f32_16x16x32_bf16 v[76:79], v[228:231], v[20:23], v[76:79]\n"
    "s_cmp_lg_u32 s70, 0\n"
    "s_cbranch_scc0 .LgUP_lastp_%=\n"
    ".LgUP_nolast_%=:\n"
    "s_waitcnt lgkmcnt(0)\n"
    "v_mfma_f32_16x16x32_bf16 v[32:35], v[232:235], v[24:27], v[32:35]\n"
    "v_mfma_f32_16x16x32_bf16 v[40:43], v[232:235], v[28:31], v[40:43]\n"
    "v_mfma_f32_16x16x32_bf16 v[36:39], v[236:239], v[24:27], v[36:39]\n"
    "v_mfma_f32_16x16x32_bf16 v[44:47], v[236:239], v[28:31], v[44:47]\n"
    "v_mfma_f32_16x16x32_bf16 v[48:51], v[240:243], v[24:27], v[48:51]\n"
    "v_mfma_f32_16x16x32_bf16 v[52:55], v[240:243], v[28:31], v[52:55]\n"
    "v_mfma_f32_16x16x32_bf16 v[56:59], v[244:247], v[24:27], v[56:59]\n"
    "v_mfma_f32_16x16x32_bf16 v[64:67], v[244:247], v[28:31], v[64:67]\n"
    "v_mfma_f32_16x16x32_bf16 v[60:63], v[248:251], v[24:27], v[60:63]\n"
    "v_mfma_f32_16x16x32_bf16 v[68:71], v[248:251], v[28:31], v[68:71]\n"
    "v_mfma_f32_16x16x32_bf16 v[72:75], v[252:255], v[24:27], v[72:75]\n"
    "v_mfma_f32_16x16x32_bf16 v[76:79], v[252:255], v[28:31], v[76:79]\n"
    "v_mul_f32 v16, 0xbfb8aa3b, v32\n"
    "v_mul_f32 v17, 0xbfb8aa3b, v33\n"
    "v_mul_f32 v18, 0xbfb8aa3b, v34\n"
    "v_mul_f32 v19, 0xbfb8aa3b, v35\n"
    "v_mul_f32 v20, 0xbfb8aa3b, v36\n"
    "v_mul_f32 v21, 0xbfb8aa3b, v37\n"
    "v_mul_f32 v22, 0xbfb8aa3b, v38\n"
    "v_mul_f32 v23, 0xbfb8aa3b, v39\n"
    "v_exp_f32 v16, v16\n"
    "v_exp_f32 v17, v17\n"
    "v_exp_f32 v18, v18\n"
    "v_exp_f32 v19, v19\n"
    "v_exp_f32 v20, v20\n"
    "v_exp_f32 v21, v21\n"
    "v_exp_f32 v22, v22\n"
    "v_exp_f32 v23, v23\n"
    "v_pk_mul_f32 v[32:33], v[32:33], v[56:57]\n"
    "v_pk_mul_f32 v[34:35], v[34:35], v[58:59]\n"
    "v_pk_mul_f32 v[36:37], v[36:37], v[60:61]\n"
    "v_pk_mul_f32 v[38:39], v[38:39], v[62:63]\n"
    "v_add_f32 v16, 1.0, v16\n"
    "v_add_f32 v17, 1.0, v17\n"
    "v_add_f32 v18, 1.0, v18\n"
    "v_add_f32 v19, 1.0, v19\n"
    "v_add_f32 v20, 1.0, v20\n"
    "v_add_f32 v21, 1.0, v21\n"
    "v_add_f32 v22, 1.0, v22\n"
    "v_add_f32 v23, 1.0, v23\n"
    "v_rcp_f32 v16, v16\n"
    "v_rcp_f32 v17, v17\n"
    "v_rcp_f32 v18, v18\n"
    "v_rcp_f32 v19, v19\n"
    "v_rcp_f32 v20, v20\n"
    "v_rcp_f32 v21, v21\n"
    "v_rcp_f32 v22, v22\n"
    "v_rcp_f32 v23, v23\n"
    "v_pk_mul_f32 v[32:33], v[32:33], v[16:17]\n"
    "v_pk_mul_f32 v[34:35], v[34:35], v[18:19]\n"
    "v_pk_mul_f32 v[36:37], v[36:37], v[20:21]\n"
    "v_pk_mul_f32 v[38:39], v[38:39], v[22:23]\n"
    "v_cvt_pk_bf16_f32 v32, v32, v33\n"
    "v_cvt_pk_bf16_f32 v33, v34, v35\n"
    "v_cvt_pk_bf16_f32 v34, v36, v37\n"
    "v_cvt_pk_bf16_f32 v35, v38, v39\n"
    "buffer_store_dwordx4 v[32:35], v1, s[36:39], s73 offen sc1\n"
    "v_mul_f32 v16, 0xbfb8aa3b, v40\n"
    "v_mul_f32 v17, 0xbfb8aa3b, v41\n"
    "v_mul_f32 v18, 0xbfb8aa3b, v42\n"
    "v_mul_f32 v19, 0xbfb8aa3b, v43\n"
    "v_mul_f32 v20, 0xbfb8aa3b, v44\n"
    "v_mul_f32 v21, 0xbfb8aa3b, v45\n"
    "v_mul_f32 v22, 0xbfb8aa3b, v46\n"
    "v_mul_f32 v23, 0xbfb8aa3b, v47\n"
    "v_exp_f32 v16, v16\n"
    "v_exp_f32 v17, v17\n"
    "v_exp_f32 v18, v18\n"
    "v_exp_f32 v19, v19\n"
    "v_exp_f32 v20, v20\n"
    "v_exp_f32 v21, v21\n"
    "v_exp_f32 v22, v22\n"
    "v_exp_f32 v23, v23\n"
    "v_pk_mul_f32 v[40:41], v[40:41], v[64:65]\n"
    "v_pk_mul_f32 v[42:43], v[42:43], v[66:67]\n"
    "v_pk_mul_f32 v[44:45], v[44:45], v[68:69]\n"
    "v_pk_mul_f32 v[46:47], v[46:47], v[70:71]\n"
    "v_add_f32 v16, 1.0, v16\n"
    "v_add_f32 v17, 1.0, v17\n"
    "v_add_f32 v18, 1.0, v18\n"
    "v_add_f32 v19, 1.0, v19\n"
    "v_add_f32 v20, 1.0, v20\n"
    "v_add_f32 v21, 1.0, v21\n"
    "v_add_f32 v22, 1.0, v22\n"
    "v_add_f32 v23, 1.0, v23\n"
    "v_rcp_f32 v16, v16\n"
    "v_rcp_f32 v17, v17\n"
    "v_rcp_f32 v18, v18\n"
    "v_rcp_f32 v19, v19\n"
    "v_rcp_f32 v20, v20\n"
    "v_rcp_f32 v21, v21\n"
    "v_rcp_f32 v22, v22\n"
    "v_rcp_f32 v23, v23\n"
    "v_pk_mul_f32 v[40:41], v[40:41], v[16:17]\n"
    "v_pk_mul_f32 v[42:43], v[42:43], v[18:19]\n"
    "v_pk_mul_f32 v[44:45], v[44:45], v[20:21]\n"
    "v_pk_mul_f32 v[46:47], v[46:47], v[22:23]\n"
    "v_cvt_pk_bf16_f32 v40, v40, v41\n"
    "v_cvt_pk_bf16_f32 v41, v42, v43\n"
    "v_cvt_pk_bf16_f32 v42, v44, v45\n"
    "v_cvt_pk_bf16_f32 v43, v46, v47\n"
    "buffer_store_dwordx4 v[40:43], v1, s[36:39], s73 offen offset:1024 sc1\n"
    "v_mul_f32 v16, 0xbfb8aa3b, v48\n"
    "v_mul_f32 v17, 0xbfb8aa3b, v49\n"
    "v_mul_f32 v18, 0xbfb8aa3b, v50\n"
    "v_mul_f32 v19, 0xbfb8aa3b, v51\n"
    "v_mul_f32 v20, 0xbfb8aa3b, v52\n"
    "v_mul_f32 v21, 0xbfb8aa3b, v53\n"
    "v_mul_f32 v22, 0xbfb8aa3b, v54\n"
    "v_mul_f32 v23, 0xbfb8aa3b, v55\n"
    "v_exp_f32 v16, v16\n"
    "v_exp_f32 v17, v17\n"
    "v_exp_f32 v18, v18\n"
    "v_exp_f32 v19, v19\n"
    "v_exp_f32 v20, v20\n"
    "v_exp_f32 v21, v21\n"
    "v_exp_f32 v22, v22\n"
    "v_exp_f32 v23, v23\n"
    "v_pk_mul_f32 v[48:49], v[48:49], v[72:73]\n"
    "v_pk_mul_f32 v[50:51], v[50:51], v[74:75]\n"
    "v_pk_mul_f32 v[52:53], v[52:53], v[76:77]\n"
    "v_pk_mul_f32 v[54:55], v[54:55], v[78:79]\n"
    "v_add_f32 v16, 1.0, v16\n"
    "v_add_f32 v17, 1.0, v17\n"
    "v_add_f32 v18, 1.0, v18\n"
    "v_add_f32 v19, 1.0, v19\n"
    "v_add_f32 v20, 1.0, v20\n"
    "v_add_f32 v21, 1.0, v21\n"
    "v_add_f32 v22, 1.0, v22\n"
    "v_add_f32 v23, 1.0, v23\n"
    "v_rcp_f32 v16, v16\n"
    "v_rcp_f32 v17, v17\n"
    "v_rcp_f32 v18, v18\n"
    "v_rcp_f32 v19, v19\n"
    "v_rcp_f32 v20, v20\n"
    "v_rcp_f32 v21, v21\n"
    "v_rcp_f32 v22, v22\n"
    "v_rcp_f32 v23, v23\n"
    "v_pk_mul_f32 v[48:49], v[48:49], v[16:17]\n"
    "v_pk_mul_f32 v[50:51], v[50:51], v[18:19]\n"
    "v_pk_mul_f32 v[52:53], v[52:53], v[20:21]\n"
    "v_pk_mul_f32 v[54:55], v[54:55], v[22:23]\n"
    "v_cvt_pk_bf16_f32 v48, v48, v49\n"
    "v_cvt_pk_bf16_f32 v49, v50, v51\n"
    "v_cvt_pk_bf16_f32 v50, v52, v53\n"
    "v_cvt_pk_bf16_f32 v51, v54, v55\n"
    "s_nop 0\n"
    "v_permlane16_swap_b32 v48, v50\n"
    "v_permlane16_swap_b32 v49, v51\n"
    "buffer_store_dwordx4 v[48:51], v4, s[36:39], s73 offen sc1\n"
    "s_mov_b32 s68, s71\n"
    "s_mov_b32 s69, 1\n"
    "s_cmp_eq_u32 s70, 0\n"
    "s_cbranch_scc1 .LgUP_idle_%=\n"
    "s_cmp_lg_u32 s68, 64\n"
    "s_cbranch_scc1 .LgUP_top_%=\n"
    ".LgUP_LOOPE_%=:\n"
    "s_waitcnt vmcnt(0)\n"
    "s_mov_b32 s74, 63\n"
    "v_mov_b32 v12, s74\n"
    "s_mov_b64 exec, s[62:63]\n"
    "buffer_store_dword v12, v14, s[36:39], 0 offen sc1\n"
    "s_mov_b64 exec, -1\n"
    "s_waitcnt vmcnt(0)\n"
    "s_branch .LgUP_FIN_%=\n"
    ".LgUP_dma0_%=:\n"
    "s_mov_b32 s70, 1\n"
    "s_and_b32 s76, s71, s54\n"
    "s_mul_i32 s76, s76, s53\n"
    "s_add_u32 s76, s76, s52\n"
    "s_and_b32 s77, s71, 1\n"
    "s_mul_i32 s77, s77, 36864\n"
    "s_add_u32 s77, s77, s59\n"
    "s_waitcnt lgkmcnt(0)\n"
    "v_mfma_f32_16x16x32_bf16 v[32:35], a[72:75], v[24:27], v[32:35]\n"
    "ds_read_b128 v[16:19], v9 offset:8192\n"
    "ds_read_b128 v[20:23], v9 offset:9216\n"
    "s_add_u32 m0, s77, s59\n"
    "s_add_u32 s77, s77, 8192\n"
    "buffer_load_dwordx4 v1, s[36:39], s76 offen sc1 lds\n"
    "v_mfma_f32_16x16x32_bf16 v[40:43], a[72:75], v[28:31], v[40:43]\n"
    "v_mfma_f32_16x16x32_bf16 v[36:39], a[76:79], v[24:27], v[36:39]\n"
    "v_mfma_f32_16x16x32_bf16 v[44:47], a[76:79], v[28:31], v[44:47]\n"
    "v_mfma_f32_16x16x32_bf16 v[48:51], a[80:83], v[24:27], v[48:51]\n"
    "v_mfma_f32_16x16x32_bf16 v[52:55], a[80:83], v[28:31], v[52:55]\n"
    "buffer_load_dwordx4 v1, s[36:39], s76 offen offset:1024 sc1 lds\n"
    "s_add_u32 s76, s76, 8192\n"
    "v_mfma_f32_16x16x32_bf16 v[56:59], a[84:87], v[24:27], v[56:59]\n"
    "v_mfma_f32_16x16x32_bf16 v[64:67], a[84:87], v[28:31], v[64:67]\n"
    "v_mfma_f32_16x16x32_bf16 v[60:63], a[88:91], v[24:27], v[60:63]\n"
    "v_mfma_f32_16x16x32_bf16 v[68:71], a[88:91], v[28:31], v[68:71]\n"
    "v_mfma_f32_16x16x32_bf16 v[72:75], a[92:95], v[24:27], v[72:75]\n"
    "s_add_u32 m0, s77, s59\n"
    "s_add_u32 s77, s77, 8192\n"
    "buffer_load_dwordx4 v1, s[36:39], s76 offen sc1 lds\n"
    "v_mfma_f32_16x16x32_bf16 v[76:79], a[92:95], v[28:31], v[76:79]\n"
    "s_waitcnt lgkmcnt(0)\n"
    "v_mfma_f32_16x16x32_bf16 v[32:35], a[96:99], v[16:19], v[32:35]\n"
    "ds_read_b128 v[24:27], v9 offset:10240\n"
    "ds_read_b128 v[28:31], v9 offset:11264\n"
    "v_mfma_f32_16x16x32_bf16 v[40:43], a[96:99], v[20:23], v[40:43]\n"
    "v_mfma_f32_16x16x32_bf16 v[36:39], a[100:103], v[16:19], v[36:39]\n"
    "v_mfma_f32_16x16x32_bf16 v[44:47], a[100:103], v[20:23], v[44:47]\n"
    "buffer_load_dwordx4 v1, s[36:39], s76 offen offset:1024 sc1 lds\n"
    "s_add_u32 s76, s76, 8192\n"
    "v_mfma_f32_16x16x32_bf16 v[48:51], a[104:107], v[16:19], v[48:51]\n"
    "v_mfma_f32_16x16x32_bf16 v[52:55], a[104:107], v[20:23], v[52:55]\n"
    "v_mfma_f32_16x16x32_bf16 v[56:59], a[108:111], v[16:19], v[56:59]\n"
    "v_mfma_f32_16x16x32_bf16 v[64:67], a[108:111], v[20:23], v[64:67]\n"
    "v_mfma_f32_16x16x32_bf16 v[60:63], a[112:115], v[16:19], v[60:63]\n"
    "s_add_u32 m0, s77, s59\n"
    "s_add_u32 s77, s77, 8192\n"
    "buffer_load_dwordx4 v1, s[36:39], s76 offen sc1 lds\n"
    "v_mfma_f32_16x16x32_bf16 v[68:71], a[112:115], v[20:23], v[68:71]\n"
    "v_mfma_f32_16x16x32_bf16 v[72:75], a[116:119], v[16:19], v[72:75]\n"
    "v_mfma_f32_16x16x32_bf16 v[76:79], a[116:119], v[20:23], v[76:79]\n"
    "s_waitcnt lgkmcnt(0)\n"
    "v_mfma_f32_16x16x32_bf16 v[32:35], a[120:123], v[24:27], v[32:35]\n"
    "ds_read_b128 v[16:19], v9 offset:12288\n"
    "ds_read_b128 v[20:23], v9 offset:13312\n"
    "v_mfma_f32_16x16x32_bf16 v[40:43], a[120:123], v[28:31], v[40:43]\n"
    "buffer_load_dwordx4 v1, s[36:39], s76 offen offset:1024 sc1 lds\n"
    "s_add_u32 s76, s76, 8192\n"
    "v_mfma_f32_16x16x32_bf16 v[36:39], a[124:127], v[24:27], v[36:39]\n"
    "v_mfma_f32_16x16x32_bf16 v[44:47], a[124:127], v[28:31], v[44:47]\n"
    "v_mfma_f32_16x16x32_bf16 v[48:51], a[128:131], v[24:27], v[48:51]\n"
    "v_mfma_f32_16x16x32_bf16 v[52:55], a[128:131], v[28:31], v[52:55]\n"
    "v_mfma_f32_16x16x32_bf16 v[56:59], a[132:135], v[24:27], v[56:59]\n"
    "s_add_u32 m0, s77, s59\n"
    "s_add_u32 s77, s77, 8192\n"
    "buffer_load_dwordx4 v1, s[36:39], s76 offen sc1 lds\n"
    "v_mfma_f32_16x16x32_bf16 v[64:67], a[132:135], v[28:31], v[64:67]\n"
    "v_mfma_f32_16x16x32_bf16 v[60:63], a[136:139], v[24:27], v[60:63]\n"
    "v_mfma_f32_16x16x32_bf16 v[68:71], a[136:139], v[28:31], v[68:71]\n"
    "v_mfma_f32_16x16x32_bf16 v[72:75], a[140:143], v[24:27], v[72:75]\n"
    "v_mfma_f32_16x16x32_bf16 v[76:79], a[140:143], v[28:31], v[76:79]\n"
    "buffer_load_dwordx4 v1, s[36:39], s76 offen offset:1024 sc1 lds\n"
    "s_add_u32 s76, s76, 8192\n"
    "s_waitcnt lgkmcnt(0)\n"
    "v_mfma_f32_16x16x32_bf16 v[32:35], a[144:147], v[16:19], v[32:35]\n"
    "ds_read_b128 v[24:27], v9 offset:14336\n"
    "ds_read_b128 v[28:31], v9 offset:15360\n"
    "v_mfma_f32_16x16x32_bf16 v[40:43], a[144:147], v[20:23], v[40:43]\n"
    "v_mfma_f32_16x16x32_bf16 v[36:39], a[148:151], v[16:19], v[36:39]\n"
    "v_mfma_f32_16x16x32_bf16 v[44:47], a[148:151], v[20:23], v[44:47]\n"
    "v_mfma_f32_16x16x32_bf16 v[48:51], a[152:155], v[16:19], v[48:51]\n"
    "s_mov_b32 m0, s77\n"
    "s_nop 0\n"
    "buffer_load_dwordx4 v8, s[36:39], s76 offen sc1 lds\n"
    "v_mfma_f32_16x16x32_bf16 v[52:55], a[152:155], v[20:23], v[52:55]\n"
    "v_mfma_f32_16x16x32_bf16 v[56:59], a[156:159], v[16:19], v[56:59]\n"
    "v_mfma_f32_16x16x32_bf16 v[64:67], a[156:159], v[20:23], v[64:67]\n"
    "v_mfma_f32_16x16x32_bf16 v[60:63], a[160:163], v[16:19], v[60:63]\n"
    "v_mfma_f32_16x16x32_bf16 v[68:71], a[160:163], v[20:23], v[68:71]\n"
    "v_mfma_f32_16x16x32_bf16 v[72:75], a[164:167], v[16:19], v[72:75]\n"
    "v_mfma_f32_16x16x32_bf16 v[76:79], a[164:167], v[20:23], v[76:79]\n"
    "s_branch .LgUP_seg0_%=\n"
    ".LgUP_dma1_%=:\n"
    "s_mov_b32 s70, 1\n"
    "s_and_b32 s76, s71, s54\n"
    "s_mul_i32 s76, s76, s53\n"
    "s_add_u32 s76, s76, s52\n"
    "s_and_b32 s77, s71, 1\n"
    "s_mul_i32 s77, s77, 36864\n"
    "s_add_u32 s77, s77, s59\n"
    "s_waitcnt lgkmcnt(0)\n"
    "v_mfma_f32_16x16x32_bf16 v[32:35], a[168:171], v[24:27], v[32:35]\n"
    "ds_read_b128 v[16:19], v9 offset:16384\n"
    "ds_read_b128 v[20:23], v9 offset:17408\n"
    "s_add_u32 m0, s77, s59\n"
    "s_add_u32 s77, s77, 8192\n"
    "buffer_load_dwordx4 v1, s[36:39], s76 offen sc1 lds\n"
    "v_mfma_f32_16x16x32_bf16 v[40:43], a[168:171], v[28:31], v[40:43]\n"
    "v_mfma_f32_16x16x32_bf16 v[36:39], a[172:175], v[24:27], v[36:39]\n"
    "v_mfma_f32_16x16x32_bf16 v[44:47], a[172:175], v[28:31], v[44:47]\n"
    "v_mfma_f32_16x16x32_bf16 v[48:51], a[176:179], v[24:27], v[48:51]\n"
    "v_mfma_f32_16x16x32_bf16 v[52:55], a[176:179], v[28:31], v[52:55]\n"
    "buffer_load_dwordx4 v1, s[36:39], s76 offen offset:1024 sc1 lds\n"
    "s_add_u32 s76, s76, 8192\n"
    "v_mfma_f32_16x16x32_bf16 v[56:59], a[180:183], v[24:27], v[56:59]\n"
    "v_mfma_f32_16x16x32_bf16 v[64:67], a[180:183], v[28:31], v[64:67]\n"
    "v_mfma_f32_16x16x32_bf16 v[60:63], a[184:187], v[24:27], v[60:63]\n"
    "v_mfma_f32_16x16x32_bf16 v[68:71], a[184:187], v[28:31], v[68:71]\n"
    "v_mfma_f32_16x16x32_bf16 v[72:75], a[188:191], v[24:27], v[72:75]\n"
    "s_add_u32 m0, s77, s59\n"
    "s_add_u32 s77, s77, 8192\n"
    "buffer_load_dwordx4 v1, s[36:39], s76 offen sc1 lds\n"
    "v_mfma_f32_16x16x32_bf16 v[76:79], a[188:191], v[28:31], v[76:79]\n"
    "s_waitcnt lgkmcnt(0)\n"
    "v_mfma_f32_16x16x32_bf16 v[32:35], a[192:195], v[16:19], v[32:35]\n"
    "ds_read_b128 v[24:27], v9 offset:18432\n"
    "ds_read_b128 v[28:31], v9 offset:19456\n"
    "v_mfma_f32_16x16x32_bf16 v[40:43], a[192:195], v[20:23], v[40:43]\n"
    "v_mfma_f32_16x16x32_bf16 v[36:39], a[196:199], v[16:19], v[36:39]\n"
    "v_mfma_f32_16x16x32_bf16 v[44:47], a[196:199], v[20:23], v[44:47]\n"
    "buffer_load_dwordx4 v1, s[36:39], s76 offen offset:1024 sc1 lds\n"
    "s_add_u32 s76, s76, 8192\n"
    "v_mfma_f32_16x16x32_bf16 v[48:51], a[200:203], v[16:19], v[48:51]\n"
    "v_mfma_f32_16x16x32_bf16 v[52:55], a[200:203], v[20:23], v[52:55]\n"
    "v_mfma_f32_16x16x32_bf16 v[56:59], a[204:207], v[16:19], v[56:59]\n"
    "v_mfma_f32_16x16x32_bf16 v[64:67], a[204:207], v[20:23], v[64:67]\n"
    "v_mfma_f32_16x16x32_bf16 v[60:63], a[208:211], v[16:19], v[60:63]\n"
    "s_add_u32 m0, s77, s59\n"
    "s_add_u32 s77, s77, 8192\n"
    "buffer_load_dwordx4 v1, s[36:39], s76 offen sc1 lds\n"
    "v_mfma_f32_16x16x32_bf16 v[68:71], a[208:211], v[20:23], v[68:71]\n"
    "v_mfma_f32_16x16x32_bf16 v[72:75], a[212:215], v[16:19], v[72:75]\n"
    "v_mfma_f32_16x16x32_bf16 v[76:79], a[212:215], v[20:23], v[76:79]\n"
    "s_waitcnt lgkmcnt(0)\n"
    "v_mfma_f32_16x16x32_bf16 v[32:35], a[216:219], v[24:27], v[32:35]\n"
    "ds_read_b128 v[16:19], v9 offset:20480\n"
    "ds_read_b128 v[20:23], v9 offset:21504\n"
    "v_mfma_f32_16x16x32_bf16 v[40:43], a[216:219], v[28:31], v[40:43]\n"
    "buffer_load_dwordx4 v1, s[36:39], s76 offen offset:1024 sc1 lds\n"
    "s_add_u32 s76, s76, 8192\n"
    "v_mfma_f32_16x16x32_bf16 v[36:39], a[220:223], v[24:27], v[36:39]\n"
    "v_mfma_f32_16x16x32_bf16 v[44:47], a[220:223], v[28:31], v[44:47]\n"
    "v_mfma_f32_16x16x32_bf16 v[48:51], a[224:227], v[24:27], v[48:51]\n"
    "v_mfma_f32_16x16x32_bf16 v[52:55], a[224:227], v[28:31], v[52:55]\n"
    "v_mfma_f32_16x16x32_bf16 v[56:59], a[228:231], v[24:27], v[56:59]\n"
    "s_add_u32 m0, s77, s59\n"
    "s_add_u32 s77, s77, 8192\n"
    "buffer_load_dwordx4 v1, s[36:39], s76 offen sc1 lds\n"
    "v_mfma_f32_16x16x32_bf16 v[64:67], a[228:231], v[28:31], v[64:67]\n"
    "v_mfma_f32_16x16x32_bf16 v[60:63], a[232:235], v[24:27], v[60:63]\n"
    "v_mfma_f32_16x16x32_bf16 v[68:71], a[232:235], v[28:31], v[68:71]\n"
    "v_mfma_f32_16x16x32_bf16 v[72:75], a[236:239], v[24:27], v[72:75]\n"
    "v_mfma_f32_16x16x32_bf16 v[76:79], a[236:239], v[28:31], v[76:79]\n"
    "buffer_load_dwordx4 v1, s[36:39], s76 offen offset:1024 sc1 lds\n"
    "s_add_u32 s76, s76, 8192\n"
    "s_waitcnt lgkmcnt(0)\n"
    "v_mfma_f32_16x16x32_bf16 v[32:35], a[240:243], v[16:19], v[32:35]\n"
    "ds_read_b128 v[24:27], v9 offset:22528\n"
    "ds_read_b128 v[28:31], v9 offset:23552\n"
    "v_mfma_f32_16x16x32_bf16 v[40:43], a[240:243], v[20:23], v[40:43]\n"
    "v_mfma_f32_16x16x32_bf16 v[36:39], a[244:247], v[16:19], v[36:39]\n"
    "v_mfma_f32_16x16x32_bf16 v[44:47], a[244:247], v[20:23], v[44:47]\n"
    "v_mfma_f32_16x16x32_bf16 v[48:51], a[248:251], v[16:19], v[48:51]\n"
    "s_mov_b32 m0, s77\n"
    "s_nop 0\n"
    "buffer_load_dwordx4 v8, s[36:39], s76 offen sc1 lds\n"
    "v_mfma_f32_16x16x32_bf16 v[52:55], a[248:251], v[20:23], v[52:55]\n"
    "v_mfma_f32_16x16x32_bf16 v[56:59], a[252:255], v[16:19], v[56:59]\n"
    "v_mfma_f32_16x16x32_bf16 v[64:67], a[252:255], v[20:23], v[64:67]\n"
    "v_mfma_f32_16x16x32_bf16 v[60:63], v[80:83], v[16:19], v[60:63]\n"
    "v_mfma_f32_16x16x32_bf16 v[68:71], v[80:83], v[20:23], v[68:71]\n"
    "v_mfma_f32_16x16x32_bf16 v[72:75], v[84:87], v[16:19], v[72:75]\n"
    "v_mfma_f32_16x16x32_bf16 v[76:79], v[84:87], v[20:23], v[76:79]\n"
    "s_branch .LgUP_seg1_%=\n"
    ".LgUP_dma2_%=:\n"
    "s_mov_b32 s70, 1\n"
    "s_and_b32 s76, s71, s54\n"
    "s_mul_i32 s76, s76, s53\n"
    "s_add_u32 s76, s76, s52\n"
    "s_and_b32 s77, s71, 1\n"
    "s_mul_i32 s77, s77, 36864\n"
    "s_add_u32 s77, s77, s59\n"
    "s_waitcnt lgkmcnt(0)\n"
    "v_mfma_f32_16x16x32_bf16 v[32:35], v[88:91], v[24:27], v[32:35]\n"
    "ds_read_b128 v[16:19], v9 offset:24576\n"
    "ds_read_b128 v[20:23], v9 offset:25600\n"
    "s_add_u32 m0, s77, s59\n"
    "s_add_u32 s77, s77, 8192\n"
    "buffer_load_dwordx4 v1, s[36:39], s76 offen sc1 lds\n"
    "v_mfma_f32_16x16x32_bf16 v[40:43], v[88:91], v[28:31], v[40:43]\n"
    "v_mfma_f32_16x16x32_bf16 v[36:39], v[92:95], v[24:27], v[36:39]\n"
    "v_mfma_f32_16x16x32_bf16 v[44:47], v[92:95], v[28:31], v[44:47]\n"
    "v_mfma_f32_16x16x32_bf16 v[48:51], v[96:99], v[24:27], v[48:51]\n"
    "v_mfma_f32_16x16x32_bf16 v[52:55], v[96:99], v[28:31], v[52:55]\n"
    "buffer_load_dwordx4 v1, s[36:39], s76 offen offset:1024 sc1 lds\n"
    "s_add_u32 s76, s76, 8192\n"
    "v_mfma_f32_16x16x32_bf16 v[56:59], v[100:103], v[24:27], v[56:59]\n"
    "v_mfma_f32_16x16x32_bf16 v[64:67], v[100:103], v[28:31], v[64:67]\n"
    "v_mfma_f32_16x16x32_bf16 v[60:63], v[104:107], v[24:27], v[60:63]\n"
    "v_mfma_f32_16x16x32_bf16 v[68:71], v[104:107], v[28:31], v[68:71]\n"
    "v_mfma_f32_16x16x32_bf16 v[72:75], v[108:111], v[24:27], v[72:75]\n"
    "s_add_u32 m0, s77, s59\n"
    "s_add_u32 s77, s77, 8192\n"
    "buffer_load_dwordx4 v1, s[36:39], s76 offen sc1 lds\n"
    "v_mfma_f32_16x16x32_bf16 v[76:79], v[108:111], v[28:31], v[76:79]\n"
    "s_waitcnt lgkmcnt(0)\n"
    "v_mfma_f32_16x16x32_bf16 v[32:35], v[112:115], v[16:19], v[32:35]\n"
    "ds_read_b128 v[24:27], v9 offset:26624\n"
    "ds_read_b128 v[28:31], v9 offset:27648\n"
    "v_mfma_f32_16x16x32_bf16 v[40:43], v[112:115], v[20:23], v[40:43]\n"
    "v_mfma_f32_16x16x32_bf16 v[36:39], v[116:119], v[16:19], v[36:39]\n"
    "v_mfma_f32_16x16x32_bf16 v[44:47], v[116:119], v[20:23], v[44:47]\n"
    "buffer_load_dwordx4 v1, s[36:39], s76 offen offset:1024 sc1 lds\n"
    "s_add_u32 s76, s76, 8192\n"
    "v_mfma_f32_16x16x32_bf16 v[48:51], v[120:123], v[16:19], v[48:51]\n"
    "v_mfma_f32_16x16x32_bf16 v[52:55], v[120:123], v[20:23], v[52:55]\n"
    "v_mfma_f32_16x16x32_bf16 v[56:59], v[124:127], v[16:19], v[56:59]\n"
    "v_mfma_f32_16x16x32_bf16 v[64:67], v[124:127], v[20:23], v[64:67]\n"
    "v_mfma_f32_16x16x32_bf16 v[60:63], v[128:131], v[16:19], v[60:63]\n"
    "s_add_u32 m0, s77, s59\n"
    "s_add_u32 s77, s77, 8192\n"
    "buffer_load_dwordx4 v1, s[36:39], s76 offen sc1 lds\n"
    "v_mfma_f32_16x16x32_bf16 v[68:71], v[128:131], v[20:23], v[68:71]\n"
    "v_mfma_f32_16x16x32_bf16 v[72:75], v[132:135], v[16:19], v[72:75]\n"
    "v_mfma_f32_16x16x32_bf16 v[76:79], v[132:135], v[20:23], v[76:79]\n"
    "s_waitcnt lgkmcnt(0)\n"
    "v_mfma_f32_16x16x32_bf16 v[32:35], v[136:139], v[24:27], v[32:35]\n"
    "ds_read_b128 v[16:19], v9 offset:28672\n"
    "ds_read_b128 v[20:23], v9 offset:29696\n"
    "v_mfma_f32_16x16x32_bf16 v[40:43], v[136:139], v[28:31], v[40:43]\n"
    "buffer_load_dwordx4 v1, s[36:39], s76 offen offset:1024 sc1 lds\n"
    "s_add_u32 s76, s76, 8192\n"
    "v_mfma_f32_16x16x32_bf16 v[36:39], v[140:143], v[24:27], v[36:39]\n"
    "v_mfma_f32_16x16x32_bf16 v[44:47], v[140:143], v[28:31], v[44:47]\n"
    "v_mfma_f32_16x16x32_bf16 v[48:51], v[144:147], v[24:27], v[48:51]\n"
    "v_mfma_f32_16x16x32_bf16 v[52:55], v[144:147], v[28:31], v[52:55]\n"
    "v_mfma_f32_16x16x32_bf16 v[56:59], v[148:151], v[24:27], v[56:59]\n"
    "s_add_u32 m0, s77, s59\n"
    "s_add_u32 s77, s77, 8192\n"
    "buffer_load_dwordx4 v1, s[36:39], s76 offen sc1 lds\n"
    "v_mfma_f32_16x16x32_bf16 v[64:67], v[148:151], v[28:31], v[64:67]\n"
    "v_mfma_f32_16x16x32_bf16 v[60:63], v[152:155], v[24:27], v[60:63]\n"
    "v_mfma_f32_16x16x32_bf16 v[68:71], v[152:155], v[28:31], v[68:71]\n"
    "v_mfma_f32_16x16x32_bf16 v[72:75], v[156:159], v[24:27], v[72:75]\n"
    "v_mfma_f32_16x16x32_bf16 v[76:79], v[156:159], v[28:31], v[76:79]\n"
    "buffer_load_dwordx4 v1, s[36:39], s76 offen offset:1024 sc1 lds\n"
    "s_add_u32 s76, s76, 8192\n"
    "s_waitcnt lgkmcnt(0)\n"
    "v_mfma_f32_16x16x32_bf16 v[32:35], v[160:163], v[16:19], v[32:35]\n"
    "ds_read_b128 v[24:27], v9 offset:30720\n"
    "ds_read_b128 v[28:31], v9 offset:31744\n"
    "v_mfma_f32_16x16x32_bf16 v[40:43], v[160:163], v[20:23], v[40:43]\n"
    "v_mfma_f32_16x16x32_bf16 v[36:39], v[164:167], v[16:19], v[36:39]\n"
    "v_mfma_f32_16x16x32_bf16 v[44:47], v[164:167], v[20:23], v[44:47]\n"
    "v_mfma_f32_16x16x32_bf16 v[48:51], v[168:171], v[16:19], v[48:51]\n"
    "s_mov_b32 m0, s77\n"
    "s_nop 0\n"
    "buffer_load_dwordx4 v8, s[36:39], s76 offen sc1 lds\n"
    "v_mfma_f32_16x16x32_bf16 v[52:55], v[168:171], v[20:23], v[52:55]\n"
    "v_mfma_f32_16x16x32_bf16 v[56:59], v[172:175], v[16:19], v[56:59]\n"
    "v_mfma_f32_16x16x32_bf16 v[64:67], v[172:175], v[20:23], v[64:67]\n"
    "v_mfma_f32_16x16x32_bf16 v[60:63], v[176:179], v[16:19], v[60:63]\n"
    "v_mfma_f32_16x16x32_bf16 v[68:71], v[176:179], v[20:23], v[68:71]\n"
    "v_mfma_f32_16x16x32_bf16 v[72:75], v[180:183], v[16:19], v[72:75]\n"
    "v_mfma_f32_16x16x32_bf16 v[76:79], v[180:183], v[20:23], v[76:79]\n"
    "s_branch .LgUP_seg2_%=\n"
    ".LgUP_lastp_%=:\n"
    "v_add_u32 v13, v11, v3\n"
    "v_cmp_gt_i32 vcc, s71, v13\n"
    "s_nop 0\n"
    "s_or_b64 exec, vcc, 1\n"
    "buffer_atomic_smax v11, v2, s[36:39], 0 offen sc0 sc1\n"
    "s_mov_b64 exec, -1\n"
    "s_branch .LgUP_nolast_%=\n"
    ".LgUP_idle_%=:\n"
    "s_waitcnt vmcnt(3)\n"
    "v_add_u32 v13, v11, v3\n"
    "v_cmp_gt_i32 vcc, s71, v13\n"
    "s_cbranch_vccnz .LgUP_idle2_%=\n"
    "s_and_b32 s76, s71, s54\n"
    "s_mul_i32 s76, s76, s53\n"
    "s_add_u32 s76, s76, s52\n"
    "s_and_b32 s77, s71, 1\n"
    "s_mul_i32 s77, s77, 36864\n"
    "s_add_u32 s77, s77, s59\n"
    "s_add_u32 m0, s77, s59\n"
    "s_add_u32 s77, s77, 8192\n"
    "buffer_load_dwordx4 v1, s[36:39], s76 offen sc1 lds\n"
    "buffer_load_dwordx4 v1, s[36:39], s76 offen offset:1024 sc1 lds\n"
    "s_add_u32 s76, s76, 8192\n"
    "s_add_u32 m0, s77, s59\n"
    "s_add_u32 s77, s77, 8192\n"
    "buffer_load_dwordx4 v1, s[36:39], s76 offen sc1 lds\n"
    "buffer_load_dwordx4 v1, s[36:39], s76 offen offset:1024 sc1 lds\n"
    "s_add_u32 s76, s76, 8192\n"
    "s_add_u32 m0, s77, s59\n"
    "s_add_u32 s77, s77, 8192\n"
    "buffer_load_dwordx4 v1, s[36:39], s76 offen sc1 lds\n"
    "buffer_load_dwordx4 v1, s[36:39], s76 offen offset:1024 sc1 lds\n"
    "s_add_u32 s76, s76, 8192\n"
    "s_add_u32 m0, s77, s59\n"
    "s_add_u32 s77, s77, 8192\n"
    "buffer_load_dwordx4 v1, s[36:39], s76 offen sc1 lds\n"
    "buffer_load_dwordx4 v1, s[36:39], s76 offen offset:1024 sc1 lds\n"
    "s_add_u32 s76, s76, 8192\n"
    "s_mov_b32 m0, s77\n"
    "s_nop 0\n"
    "buffer_load_dwordx4 v8, s[36:39], s76 offen sc1 lds\n"
    "s_waitcnt vmcnt(9)\n"
    "s_sub_u32 s74, s68, 1\n"
    "v_mov_b32 v12, s74\n"
    "s_mov_b64 exec, s[62:63]\n"
    "buffer_store_dword v12, v14, s[36:39], 0 offen sc1\n"
    "s_mov_b64 exec, -1\n"
    "s_mov_b32 s69, 0\n"
    "s_waitcnt vmcnt(1)\n"
    "s_branch .LgUP_bar_%=\n"
    ".LgUP_idle2_%=:\n"
    "s_or_b64 exec, vcc, 1\n"
    "buffer_atomic_smax v11, v2, s[36:39], 0 offen sc0 sc1\n"
    "s_mov_b64 exec, -1\n"
    "s_waitcnt vmcnt(1)\n"
    "s_sub_u32 s74, s68, 1\n"
    "v_mov_b32 v12, s74\n"
    "s_mov_b64 exec, s[62:63]\n"
    "buffer_store_dword v12, v14, s[36:39], 0 offen sc1\n"
    "s_mov_b64 exec, -1\n"
    "s_waitcnt vmcnt(1)\n"
    "s_branch .LgUP_block1_%=\n"
    ".LgUP_block_%=:\n"
    "v_add_u32 v13, v11, v3\n"
    "v_cmp_gt_i32 vcc, s71, v13\n"
    "s_nop 0\n"
    "s_or_b64 exec, vcc, 1\n"
    "buffer_atomic_smax v11, v2, s[36:39], 0 offen sc0 sc1\n"
    "s_mov_b64 exec, -1\n"
    "s_waitcnt vmcnt(0)\n"
    ".LgUP_block1_%=:\n"
    "v_add_u32 v13, v11, v3\n"
    "v_cmp_gt_i32 vcc, s71, v13\n"
    "s_cbranch_vccz .LgUP_go_%=\n"
    "s_sleep 4\n"
    "s_sub_u32 s72, s72, 1\n"
    "s_cbranch_scc0 .LgUP_block_%=\n"
    "s_mov_b32 s72, 0\n"
    ".LgUP_go_%=:\n"
    "s_and_b32 s76, s71, s54\n"
    "s_mul_i32 s76, s76, s53\n"
    "s_add_u32 s76, s76, s52\n"
    "s_and_b32 s77, s71, 1\n"
    "s_mul_i32 s77, s77, 36864\n"
    "s_add_u32 s77, s77, s59\n"
    "s_add_u32 m0, s77, s59\n"
    "s_add_u32 s77, s77, 8192\n"
    "buffer_load_dwordx4 v1, s[36:39], s76 offen sc1 lds\n"
    "buffer_load_dwordx4 v1, s[36:39], s76 offen offset:1024 sc1 lds\n"
    "s_add_u32 s76, s76, 8192\n"
    "s_add_u32 m0, s77, s59\n"
    "s_add_u32 s77, s77, 8192\n"
    "buffer_load_dwordx4 v1, s[36:39], s76 offen sc1 lds\n"
    "buffer_load_dwordx4 v1, s[36:39], s76 offen offset:1024 sc1 lds\n"
    "s_add_u32 s76, s76, 8192\n"
    "s_add_u32 m0, s77, s59\n"
    "s_add_u32 s77, s77, 8192\n"
    "buffer_load_dwordx4 v1, s[36:39], s76 offen sc1 lds\n"
    "buffer_load_dwordx4 v1, s[36:39], s76 offen offset:1024 sc1 lds\n"
    "s_add_u32 s76, s76, 8192\n"
    "s_add_u32 m0, s77, s59\n"
    "s_add_u32 s77, s77, 8192\n"
    "buffer_load_dwordx4 v1, s[36:39], s76 offen sc1 lds\n"
    "buffer_load_dwordx4 v1, s[36:39], s76 offen offset:1024 sc1 lds\n"
    "s_add_u32 s76, s76, 8192\n"
    "s_mov_b32 m0, s77\n"
    "s_nop 0\n"
    "buffer_load_dwordx4 v8, s[36:39], s76 offen sc1 lds\n"
    "s_mov_b32 s69, 0\n"
    "s_waitcnt vmcnt(0)\n"
    "s_branch .LgUP_bar_%=\n"
    ".LgUP_FIN_%=:\n"
  );
}
DEVI void role_wo(const Params& p, int L, int j, int i, u32 dbg) {
  const int lane = threadIdx.x & 63, w = threadIdx.x >> 6, e0 = j * 1024 * 576 + i * 192;
  GEMM_DECL;
  g_w0 = make_srd(p.Wo + e0, 4096u * 576 * 2 - e0 * 2); g_w1 = g_w0; g_w2 = g_w0;
  g_ldw = 1152;
  g_in_base = OFF_L + L * LSZ + L_O + j * 65536; g_in_stride = 4 * 65536; g_in_mask = RO - 1;
  g_out_base = OFF_L + L * LSZ + L_PA + j * SEGB + i * 12288; g_out_stride = 4 * SEGB; g_out_mask = RP - 1;
  g_flag = lane < 4 ? mbox(L, lane, SL_REDA, 12 * j + 4 * i + w) : ((lane < 12 && w == 0) ? mbox_att(L, j, lane - 4, 4 + i) : OOB);
  g_paddr = mbox(L, j, SL_WO + i, lane);
  g_pbias = lane < 8 ? 0 : (lane < 24 ? RP : 1000);
  v16u sa, sb, va;
  gemm_args(GEMM_ARGS, p, dbg, sa, sb, va);
  RUN_ASM(
    "s_nop 4\n"
    "v_add_u32 v8, s59, v0\n"
    "s_add_u32 m0, s59, 0\n"
    "s_mov_b32 s74, 0\n"
    "buffer_load_dwordx4 v5, s[40:43], s74 offen lds\n"
    "s_add_u32 m0, s59, 4096\n"
    "s_mov_b32 s74, 128\n"
    "buffer_load_dwordx4 v5, s[40:43], s74 offen lds\n"
    "s_add_u32 m0, s59, 8192\n"
    "s_mov_b32 s74, 256\n"
    "buffer_load_dwordx4 v5, s[40:43], s74 offen lds\n"
    "s_add_u32 m0, s59, 12288\n"
    "s_mov_b32 s74, 36864\n"
    "buffer_load_dwordx4 v5, s[40:43], s74 offen lds\n"
    "s_add_u32 m0, s59, 16384\n"
    "s_mov_b32 s74, 36992\n"
    "buffer_load_dwordx4 v5, s[40:43], s74 offen lds\n"
    "s_add_u32 m0, s59, 20480\n"
    "s_mov_b32 s74, 37120\n"
    "buffer_load_dwordx4 v5, s[40:43], s74 offen lds\n"
    "s_add_u32 m0, s59, 24576\n"
    "s_mov_b32 s74, 73728\n"
    "buffer_load_dwordx4 v5, s[40:43], s74 offen lds\n"
    "s_add_u32 m0, s59, 28672\n"
    "s_mov_b32 s74, 73856\n"
    "buffer_load_dwordx4 v5, s[40:43], s74 offen lds\n"
    "s_add_u32 m0, s59, 32768\n"
    "s_mov_b32 s74, 73984\n"
    "buffer_load_dwordx4 v5, s[40:43], s74 offen lds\n"
    "s_add_u32 m0, s59, 36864\n"
    "s_mov_b32 s74, 110592\n"
    "buffer_load_dwordx4 v5, s[40:43], s74 offen lds\n"
    "s_add_u32 m0, s59, 40960\n"
    "s_mov_b32 s74, 110720\n"
    "buffer_load_dwordx4 v5, s[40:43], s74 offen lds\n"
    "s_add_u32 m0, s59, 45056\n"
    "s_mov_b32 s74, 110848\n"
    "buffer_load_dwordx4 v5, s[40:43], s74 offen lds\n"
    "s_add_u32 m0, s59, 49152\n"
    "s_mov_b32 s74, 147456\n"
    "buffer_load_dwordx4 v5, s[40:43], s74 offen lds\n"
    "s_add_u32 m0, s59, 53248\n"
    "s_mov_b32 s74, 147584\n"
    "buffer_load_dwordx4 v5, s[40:43], s74 offen lds\n"
    "s_add_u32 m0, s59, 57344\n"
    "s_mov_b32 s74, 147712\n"
    "buffer_load_dwordx4 v5, s[40:43], s74 offen lds\n"
    "s_add_u32 m0, s59, 61440\n"
    "s_mov_b32 s74, 184320\n"
    "buffer_load_dwordx4 v5, s[40:43], s74 offen lds\n"
    "s_add_u32 m0, s59, 65536\n"
    "s_mov_b32 s74, 184448\n"
    "buffer_load_dwordx4 v5, s[40:43], s74 offen lds\n"
    "s_add_u32 m0, s59, 69632\n"
    "s_mov_b32 s74, 184576\n"
    "buffer_load_dwordx4 v5, s[40:43], s74 offen lds\n"
    "s_add_u32 m0, s59, 73728\n"
    "s_mov_b32 s74, 221184\n"
    "buffer_load_dwordx4 v5, s[40:43], s74 offen lds\n"
    "s_add_u32 m0, s59, 77824\n"
    "s_mov_b32 s74, 221312\n"
    "buffer_load_dwordx4 v5, s[40:43], s74 offen lds\n"
    "s_add_u32 m0, s59, 81920\n"
    "s_mov_b32 s74, 221440\n"
    "buffer_load_dwordx4 v5, s[40:43], s74 offen lds\n"
    "s_add_u32 m0, s59, 86016\n"
    "s_mov_b32 s74, 258048\n"
    "buffer_load_dwordx4 v5, s[40:43], s74 offen lds\n"
    "s_add_u32 m0, s59, 90112\n"
    "s_mov_b32 s74, 258176\n"
    "buffer_load_dwordx4 v5, s[40:43], s74 offen lds\n"
    "s_add_u32 m0, s59, 94208\n"
    "s_mov_b32 s74, 258304\n"
    "buffer_load_dwordx4 v5, s[40:43], s74 offen lds\n"
    "s_add_u32 m0, s59, 98304\n"
    "s_mov_b32 s74, 294912\n"
    "buffer_load_dwordx4 v5, s[40:43], s74 offen lds\n"
    "s_add_u32 m0, s59, 102400\n"
    "s_mov_b32 s74, 295040\n"
    "buffer_load_dwordx4 v5, s[40:43], s74 offen lds\n"
    "s_add_u32 m0, s59, 106496\n"
    "s_mov_b32 s74, 295168\n"
    "buffer_load_dwordx4 v5, s[40:43], s74 offen lds\n"
    "s_add_u32 m0, s59, 110592\n"
    "s_mov_b32 s74, 331776\n"
    "buffer_load_dwordx4 v5, s[40:43], s74 offen lds\n"
    "s_add_u32 m0, s59, 114688\n"
    "s_mov_b32 s74, 331904\n"
    "buffer_load_dwordx4 v5, s[40:43], s74 offen lds\n"
    "s_add_u32 m0, s59, 118784\n"
    "s_mov_b32 s74, 332032\n"
    "buffer_load_dwordx4 v5, s[40:43], s74 offen lds\n"
    "v_add_u32 v16, 49152, v6\n"
    "v_add_u32 v17, 98304, v6\n"
    "v_add_u32 v18, 49152, v7\n"
    "v_add_u32 v19, 98304, v7\n"
    "s_waitcnt vmcnt(24) lgkmcnt(0)\n"
    "s_barrier\n"
    "s_add_u32 m0, s59, 122880\n"
    "s_mov_b32 s74, 368640\n"
    "buffer_load_dwordx4 v5, s[40:43], s74 offen lds\n"
    "s_add_u32 m0, s59, 126976\n"
    "s_mov_b32 s74, 368768\n"
    "buffer_load_dwordx4 v5, s[40:43], s74 offen lds\n"
    "s_add_u32 m0, s59, 131072\n"
    "s_mov_b32 s74, 368896\n"
    "buffer_load_dwordx4 v5, s[40:43], s74 offen lds\n"
    "s_add_u32 m0, s59, 135168\n"
    "s_mov_b32 s74, 405504\n"
    "buffer_load_dwordx4 v5, s[40:43], s74 offen lds\n"
    "s_add_u32 m0, s59, 139264\n"
    "s_mov_b32 s74, 405632\n"
    "buffer_load_dwordx4 v5, s[40:43], s74 offen lds\n"
    "s_add_u32 m0, s59, 143360\n"
    "s_mov_b32 s74, 405760\n"
    "buffer_load_dwordx4 v5, s[40:43], s74 offen lds\n"
    "ds_read_b64_tr_b16 a[0:1], v6 offset:0\n"
    "ds_read_b64_tr_b16 a[2:3], v6 offset:512\n"
    "ds_read_b64_tr_b16 a[4:5], v6 offset:8\n"
    "ds_read_b64_tr_b16 a[6:7], v6 offset:520\n"
    "ds_read_b64_tr_b16 a[8:9], v7 offset:0\n"
    "ds_read_b64_tr_b16 a[10:11], v7 offset:512\n"
    "ds_read_b64_tr_b16 a[12:13], v6 offset:12288\n"
    "ds_read_b64_tr_b16 a[14:15], v6 offset:12800\n"
    "ds_read_b64_tr_b16 a[16:17], v6 offset:12296\n"
    "ds_read_b64_tr_b16 a[18:19], v6 offset:12808\n"
    "ds_read_b64_tr_b16 a[20:21], v7 offset:12288\n"
    "ds_read_b64_tr_b16 a[22:23], v7 offset:12800\n"
    "s_waitcnt vmcnt(24) lgkmcnt(0)\n"
    "s_barrier\n"
    "s_add_u32 m0, s59, 0\n"
    "s_mov_b32 s74, 442368\n"
    "buffer_load_dwordx4 v5, s[40:43], s74 offen lds\n"
    "s_add_u32 m0, s59, 4096\n"
    "s_mov_b32 s74, 442496\n"
    "buffer_load_dwordx4 v5, s[40:43], s74 offen lds\n"
    "s_add_u32 m0, s59, 8192\n"
    "s_mov_b32 s74, 442624\n"
    "buffer_load_dwordx4 v5, s[40:43], s74 offen lds\n"
    "s_add_u32 m0, s59, 12288\n"
    "s_mov_b32 s74, 479232\n"
    "buffer_load_dwordx4 v5, s[40:43], s74 offen lds\n"
    "s_add_u32 m0, s59, 16384\n"
    "s_mov_b32 s74, 479360\n"
    "buffer_load_dwordx4 v5, s[40:43], s74 offen lds\n"
    "s_add_u32 m0, s59, 20480\n"
    "s_mov_b32 s74, 479488\n"
    "buffer_load_dwordx4 v5, s[40:43], s74 offen lds\n"
    "ds_read_b64_tr_b16 a[24:25], v6 offset:24576\n"
    "ds_read_b64_tr_b16 a[26:27], v6 offset:25088\n"
    "ds_read_b64_tr_b16 a[28:29], v6 offset:24584\n"
    "ds_read_b64_tr_b16 a[30:31], v6 offset:25096\n"
    "ds_read_b64_tr_b16 a[32:33], v7 offset:24576\n"
    "ds_read_b64_tr_b16 a[34:35], v7 offset:25088\n"
    "ds_read_b64_tr_b16 a[36:37], v6 offset:36864\n"
    "ds_read_b64_tr_b16 a[38:39], v6 offset:37376\n"
    "ds_read_b64_tr_b16 a[40:41], v6 offset:36872\n"
    "ds_read_b64_tr_b16 a[42:43], v6 offset:37384\n"
    "ds_read_b64_tr_b16 a[44:45], v7 offset:36864\n"
    "ds_read_b64_tr_b16 a[46:47], v7 offset:37376\n"
    "s_waitcnt vmcnt(24) lgkmcnt(0)\n"
    "s_barrier\n"
    "s_add_u32 m0, s59, 24576\n"
    "s_mov_b32 s74, 516096\n"
    "buffer_load_dwordx4 v5, s[40:43], s74 offen lds\n"
    "s_add_u32 m0, s59, 28672\n"
    "s_mov_b32 s74, 516224\n"
    "buffer_load_dwordx4 v5, s[40:43], s74 offen lds\n"
    "s_add_u32 m0, s59, 32768\n"
    "s_mov_b32 s74, 516352\n"
    "buffer_load_dwordx4 v5, s[40:43], s74 offen lds\n"
    "s_add_u32 m0, s59, 36864\n"
    "s_mov_b32 s74, 552960\n"
    "buffer_load_dwordx4 v5, s[40:43], s74 offen lds\n"
    "s_add_u32 m0, s59, 40960\n"
    "s_mov_b32 s74, 553088\n"
    "buffer_load_dwordx4 v5, s[40:43], s74 offen lds\n"
    "s_add_u32 m0, s59, 45056\n"
    "s_mov_b32 s74, 553216\n"
    "buffer_load_dwordx4 v5, s[40:43], s74 offen lds\n"
    "ds_read_b64_tr_b16 a[48:49], v16 offset:0\n"
    "ds_read_b64_tr_b16 a[50:51], v16 offset:512\n"
    "ds_read_b64_tr_b16 a[52:53], v16 offset:8\n"
    "ds_read_b64_tr_b16 a[54:55], v16 offset:520\n"
    "ds_read_b64_tr_b16 a[56:57], v18 offset:0\n"
    "ds_read_b64_tr_b16 a[58:59], v18 offset:512\n"
    "ds_read_b64_tr_b16 a[60:61], v16 offset:12288\n"
    "ds_read_b64_tr_b16 a[62:63], v16 offset:12800\n"
    "ds_read_b64_tr_b16 a[64:65], v16 offset:12296\n"
    "ds_read_b64_tr_b16 a[66:67], v16 offset:12808\n"
    "ds_read_b64_tr_b16 a[68:69], v18 offset:12288\n"
    "ds_read_b64_tr_b16 a[70:71], v18 offset:12800\n"
    "s_waitcnt vmcnt(24) lgkmcnt(0)\n"
    "s_barrier\n"
    "s_add_u32 m0, s59, 49152\n"
    "s_mov_b32 s74, 589824\n"
    "buffer_load_dwordx4 v5, s[40:43], s74 offen lds\n"
    "s_add_u32 m0, s59, 53248\n"
    "s_mov_b32 s74, 589952\n"
    "buffer_load_dwordx4 v5, s[40:43], s74 offen lds\n"
    "s_add_u32 m0, s59, 57344\n"
    "s_mov_b32 s74, 590080\n"
    "buffer_load_dwordx4 v5, s[40:43], s74 offen lds\n"
    "s_add_u32 m0, s59, 61440\n"
    "s_mov_b32 s74, 626688\n"
    "buffer_load_dwordx4 v5, s[40:43], s74 offen lds\n"
    "s_add_u32 m0, s59, 65536\n"
    "s_mov_b32 s74, 626816\n"
    "buffer_load_dwordx4 v5, s[40:43], s74 offen lds\n"
    "s_add_u32 m0, s59, 69632\n"
    "s_mov_b32 s74, 626944\n"
    "buffer_load_dwordx4 v5, s[40:43], s74 offen lds\n"
    "ds_read_b64_tr_b16 a[72:73], v16 offset:24576\n"
    "ds_read_b64_tr_b16 a[74:75], v16 offset:25088\n"
    "ds_read_b64_tr_b16 a[76:77], v16 offset:24584\n"
    "ds_read_b64_tr_b16 a[78:79], v16 offset:25096\n"
    "ds_read_b64_tr_b16 a[80:81], v18 offset:24576\n"
    "ds_read_b64_tr_b16 a[82:83], v18 offset:25088\n"
    "ds_read_b64_tr_b16 a[84:85], v16 offset:36864\n"
    "ds_read_b64_tr_b16 a[86:87], v16 offset:37376\n"
    "ds_read_b64_tr_b16 a[88:89], v16 offset:36872\n"
    "ds_read_b64_tr_b16 a[90:91], v16 offset:37384\n"
    "ds_read_b64_tr_b16 a[92:93], v18 offset:36864\n"
    "ds_read_b64_tr_b16 a[94:95], v18 offset:37376\n"
    "s_waitcnt vmcnt(24) lgkmcnt(0)\n"
    "s_barrier\n"
    "s_add_u32 m0, s59, 73728\n"
    "s_mov_b32 s74, 663552\n"
    "buffer_load_dwordx4 v5, s[40:43], s74 offen lds\n"
    "s_add_u32 m0, s59, 77824\n"
    "s_mov_b32 s74, 663680\n"
    "buffer_load_dwordx4 v5, s[40:43], s74 offen lds\n"
    "s_add_u32 m0, s59, 81920\n"
    "s_mov_b32 s74, 663808\n"
    "buffer_load_dwordx4 v5, s[40:43], s74 offen lds\n"
    "s_add_u32 m0, s59, 86016\n"
    "s_mov_b32 s74, 700416\n"
    "buffer_load_dwordx4 v5, s[40:43], s74 offen lds\n"
    "s_add_u32 m0, s59, 90112\n"
    "s_mov_b32 s74, 700544\n"
    "buffer_load_dwordx4 v5, s[40:43], s74 offen lds\n"
    "s_add_u32 m0, s59, 94208\n"
    "s_mov_b32 s74, 700672\n"
    "buffer_load_dwordx4 v5, s[40:43], s74 offen lds\n"
    "ds_read_b64_tr_b16 a[96:97], v17 offset:0\n"
    "ds_read_b64_tr_b16 a[98:99], v17 offset:512\n"
    "ds_read_b64_tr_b16 a[100:101], v17 offset:8\n"
    "ds_read_b64_tr_b16 a[102:103], v17 offset:520\n"
    "ds_read_b64_tr_b16 a[104:105], v19 offset:0\n"
    "ds_read_b64_tr_b16 a[106:107], v19 offset:512\n"
    "ds_read_b64_tr_b16 a[108:109], v17 offset:12288\n"
    "ds_read_b64_tr_b16 a[110:111], v17 offset:12800\n"
    "ds_read_b64_tr_b16 a[112:113], v17 offset:12296\n"
    "ds_read_b64_tr_b16 a[114:115], v17 offset:12808\n"
    "ds_read_b64_tr_b16 a[116:117], v19 offset:12288\n"
    "ds_read_b64_tr_b16 a[118:119], v19 offset:12800\n"
    "s_waitcnt vmcnt(24) lgkmcnt(0)\n"
    "s_barrier\n"
    "s_add_u32 m0, s59, 98304\n"
    "s_mov_b32 s74, 737280\n"
    "buffer_load_dwordx4 v5, s[40:43], s74 offen lds\n"
    "s_add_u32 m0, s59, 102400\n"
    "s_mov_b32 s74, 737408\n"
    "buffer_load_dwordx4 v5, s[40:43], s74 offen lds\n"
    "s_add_u32 m0, s59, 106496\n"
    "s_mov_b32 s74, 737536\n"
    "buffer_load_dwordx4 v5, s[40:43], s74 offen lds\n"
    "s_add_u32 m0, s59, 110592\n"
    "s_mov_b32 s74, 774144\n"
    "buffer_load_dwordx4 v5, s[40:43], s74 offen lds\n"
    "s_add_u32 m0, s59, 114688\n"
    "s_mov_b32 s74, 774272\n"
    "buffer_load_dwordx4 v5, s[40:43], s74 offen lds\n"
    "s_add_u32 m0, s59, 118784\n"
    "s_mov_b32 s74, 774400\n"
    "buffer_load_dwordx4 v5, s[40:43], s74 offen lds\n"
    "ds_read_b64_tr_b16 a[120:121], v17 offset:24576\n"
    "ds_read_b64_tr_b16 a[122:123], v17 offset:25088\n"
    "ds_read_b64_tr_b16 a[124:125], v17 offset:24584\n"
    "ds_read_b64_tr_b16 a[126:127], v17 offset:25096\n"
    "ds_read_b64_tr_b16 a[128:129], v19 offset:24576\n"
    "ds_read_b64_tr_b16 a[130:131], v19 offset:25088\n"
    "ds_read_b64_tr_b16 a[132:133], v17 offset:36864\n"
    "ds_read_b64_tr_b16 a[134:135], v17 offset:37376\n"
    "ds_read_b64_tr_b16 a[136:137], v17 offset:36872\n"
    "ds_read_b64_tr_b16 a[138:139], v17 offset:37384\n"
    "ds_read_b64_tr_b16 a[140:141], v19 offset:36864\n"
    "ds_read_b64_tr_b16 a[142:143], v19 offset:37376\n"
    "s_waitcnt vmcnt(24) lgkmcnt(0)\n"
    "s_barrier\n"
    "s_add_u32 m0, s59, 122880\n"
    "s_mov_b32 s74, 811008\n"
    "buffer_load_dwordx4 v5, s[40:43], s74 offen lds\n"
    "s_add_u32 m0, s59, 126976\n"
    "s_mov_b32 s74, 811136\n"
    "buffer_load_dwordx4 v5, s[40:43], s74 offen lds\n"
    "s_add_u32 m0, s59, 131072\n"
    "s_mov_b32 s74, 811264\n"
    "buffer_load_dwordx4 v5, s[40:43], s74 offen lds\n"
    "s_add_u32 m0, s59, 135168\n"
    "s_mov_b32 s74, 847872\n"
    "buffer_load_dwordx4 v5, s[40:43], s74 offen lds\n"
    "s_add_u32 m0, s59, 139264\n"
    "s_mov_b32 s74, 848000\n"
    "buffer_load_dwordx4 v5, s[40:43], s74 offen lds\n"
    "s_add_u32 m0, s59, 143360\n"
    "s_mov_b32 s74, 848128\n"
    "buffer_load_dwordx4 v5, s[40:43], s74 offen lds\n"
    "ds_read_b64_tr_b16 a[144:145], v6 offset:0\n"
    "ds_read_b64_tr_b16 a[146:147], v6 offset:512\n"
    "ds_read_b64_tr_b16 a[148:149], v6 offset:8\n"
    "ds_read_b64_tr_b16 a[150:151], v6 offset:520\n"
    "ds_read_b64_tr_b16 a[152:153], v7 offset:0\n"
    "ds_read_b64_tr_b16 a[154:155], v7 offset:512\n"
    "ds_read_b64_tr_b16 a[156:157], v6 offset:12288\n"
    "ds_read_b64_tr_b16 a[158:159], v6 offset:12800\n"
    "ds_read_b64_tr_b16 a[160:161], v6 offset:12296\n"
    "ds_read_b64_tr_b16 a[162:163], v6 offset:12808\n"
    "ds_read_b64_tr_b16 a[164:165], v7 offset:12288\n"
    "ds_read_b64_tr_b16 a[166:167], v7 offset:12800\n"
    "s_waitcnt vmcnt(24) lgkmcnt(0)\n"
    "s_barrier\n"
    "s_add_u32 m0, s59, 0\n"
    "s_mov_b32 s74, 884736\n"
    "buffer_load_dwordx4 v5, s[40:43], s74 offen lds\n"
    "s_add_u32 m0, s59, 4096\n"
    "s_mov_b32 s74, 884864\n"
    "buffer_load_dwordx4 v5, s[40:43], s74 offen lds\n"
    "s_add_u32 m0, s59, 8192\n"
    "s_mov_b32 s74, 884992\n"
    "buffer_load_dwordx4 v5, s[40:43], s74 offen lds\n"
    "s_add_u32 m0, s59, 12288\n"
    "s_mov_b32 s74, 921600\n"
    "buffer_load_dwordx4 v5, s[40:43], s74 offen lds\n"
    "s_add_u32 m0, s59, 16384\n"
    "s_mov_b32 s74, 921728\n"
    "buffer_load_dwordx4 v5, s[40:43], s74 offen lds\n"
    "s_add_u32 m0, s59, 20480\n"
    "s_mov_b32 s74, 921856\n"
    "buffer_load_dwordx4 v5, s[40:43], s74 offen lds\n"
    "ds_read_b64_tr_b16 a[168:169], v6 offset:24576\n"
    "ds_read_b64_tr_b16 a[170:171], v6 offset:25088\n"
    "ds_read_b64_tr_b16 a[172:173], v6 offset:24584\n"
    "ds_read_b64_tr_b16 a[174:175], v6 offset:25096\n"
    "ds_read_b64_tr_b16 a[176:177], v7 offset:24576\n"
    "ds_read_b64_tr_b16 a[178:179], v7 offset:25088\n"
    "ds_read_b64_tr_b16 a[180:181], v6 offset:36864\n"
    "ds_read_b64_tr_b16 a[182:183], v6 offset:37376\n"
    "ds_read_b64_tr_b16 a[184:185], v6 offset:36872\n"
    "ds_read_b64_tr_b16 a[186:187], v6 offset:37384\n"
    "ds_read_b64_tr_b16 a[188:189], v7 offset:36864\n"
    "ds_read_b64_tr_b16 a[190:191], v7 offset:37376\n"
    "s_waitcnt vmcnt(24) lgkmcnt(0)\n"
    "s_barrier\n"
    "s_add_u32 m0, s59, 24576\n"
    "s_mov_b32 s74, 958464\n"
    "buffer_load_dwordx4 v5, s[40:43], s74 offen lds\n"
    "s_add_u32 m0, s59, 28672\n"
    "s_mov_b32 s74, 958592\n"
    "buffer_load_dwordx4 v5, s[40:43], s74 offen lds\n"
    "s_add_u32 m0, s59, 32768\n"
    "s_mov_b32 s74, 958720\n"
    "buffer_load_dwordx4 v5, s[40:43], s74 offen lds\n"
    "s_add_u32 m0, s59, 36864\n"
    "s_mov_b32 s74, 995328\n"
    "buffer_load_dwordx4 v5, s[40:43], s74 offen lds\n"
    "s_add_u32 m0, s59, 40960\n"
    "s_mov_b32 s74, 995456\n"
    "buffer_load_dwordx4 v5, s[40:43], s74 offen lds\n"
    "s_add_u32 m0, s59, 45056\n"
    "s_mov_b32 s74, 995584\n"
    "buffer_load_dwordx4 v5, s[40:43], s74 offen lds\n"
    "ds_read_b64_tr_b16 a[192:193], v16 offset:0\n"
    "ds_read_b64_tr_b16 a[194:195], v16 offset:512\n"
    "ds_read_b64_tr_b16 a[196:197], v16 offset:8\n"
    "ds_read_b64_tr_b16 a[198:199], v16 offset:520\n"
    "ds_read_b64_tr_b16 a[200:201], v18 offset:0\n"
    "ds_read_b64_tr_b16 a[202:203], v18 offset:512\n"
    "ds_read_b64_tr_b16 a[204:205], v16 offset:12288\n"
    "ds_read_b64_tr_b16 a[206:207], v16 offset:12800\n"
    "ds_read_b64_tr_b16 a[208:209], v16 offset:12296\n"
    "ds_read_b64_tr_b16 a[210:211], v16 offset:12808\n"
    "ds_read_b64_tr_b16 a[212:213], v18 offset:12288\n"
    "ds_read_b64_tr_b16 a[214:215], v18 offset:12800\n"
    "s_waitcnt vmcnt(24) lgkmcnt(0)\n"
    "s_barrier\n"
    "s_add_u32 m0, s59, 49152\n"
    "s_mov_b32 s74, 1032192\n"
    "buffer_load_dwordx4 v5, s[40:43], s74 offen lds\n"
    "s_add_u32 m0, s59, 53248\n"
    "s_mov_b32 s74, 1032320\n"
    "buffer_load_dwordx4 v5, s[40:43], s74 offen lds\n"
    "s_add_u32 m0, s59, 57344\n"
    "s_mov_b32 s74, 1032448\n"
    "buffer_load_dwordx4 v5, s[40:43], s74 offen lds\n"
    "s_add_u32 m0, s59, 61440\n"
    "s_mov_b32 s74, 1069056\n"
    "buffer_load_dwordx4 v5, s[40:43], s74 offen lds\n"
    "s_add_u32 m0, s59, 65536\n"
    "s_mov_b32 s74, 1069184\n"
    "buffer_load_dwordx4 v5, s[40:43], s74 offen lds\n"
    "s_add_u32 m0, s59, 69632\n"
    "s_mov_b32 s74, 1069312\n"
    "buffer_load_dwordx4 v5, s[40:43], s74 offen lds\n"
    "ds_read_b64_tr_b16 a[216:217], v16 offset:24576\n"
    "ds_read_b64_tr_b16 a[218:219], v16 offset:25088\n"
    "ds_read_b64_tr_b16 a[220:221], v16 offset:24584\n"
    "ds_read_b64_tr_b16 a[222:223], v16 offset:25096\n"
    "ds_read_b64_tr_b16 a[224:225], v18 offset:24576\n"
    "ds_read_b64_tr_b16 a[226:227], v18 offset:25088\n"
    "ds_read_b64_tr_b16 a[228:229], v16 offset:36864\n"
    "ds_read_b64_tr_b16 a[230:231], v16 offset:37376\n"
    "ds_read_b64_tr_b16 a[232:233], v16 offset:36872\n"
    "ds_read_b64_tr_b16 a[234:235], v16 offset:37384\n"
    "ds_read_b64_tr_b16 a[236:237], v18 offset:36864\n"
    "ds_read_b64_tr_b16 a[238:239], v18 offset:37376\n"
    "s_waitcnt vmcnt(24) lgkmcnt(0)\n"
    "s_barrier\n"
    "s_add_u32 m0, s59, 73728\n"
    "s_mov_b32 s74, 1105920\n"
    "buffer_load_dwordx4 v5, s[40:43], s74 offen lds\n"
    "s_add_u32 m0, s59, 77824\n"
    "s_mov_b32 s74, 1106048\n"
    "buffer_load_dwordx4 v5, s[40:43], s74 offen lds\n"
    "s_add_u32 m0, s59, 81920\n"
    "s_mov_b32 s74, 1106176\n"
    "buffer_load_dwordx4 v5, s[40:43], s74 offen lds\n"
    "s_add_u32 m0, s59, 86016\n"
    "s_mov_b32 s74, 1142784\n"
    "buffer_load_dwordx4 v5, s[40:43], s74 offen lds\n"
    "s_add_u32 m0, s59, 90112\n"
    "s_mov_b32 s74, 1142912\n"
    "buffer_load_dwordx4 v5, s[40:43], s74 offen lds\n"
    "s_add_u32 m0, s59, 94208\n"
    "s_mov_b32 s74, 1143040\n"
    "buffer_load_dwordx4 v5, s[40:43], s74 offen lds\n"
    "ds_read_b64_tr_b16 a[240:241], v17 offset:0\n"
    "ds_read_b64_tr_b16 a[242:243], v17 offset:512\n"
    "ds_read_b64_tr_b16 a[244:245], v17 offset:8\n"
    "ds_read_b64_tr_b16 a[246:247], v17 offset:520\n"
    "ds_read_b64_tr_b16 a[248:249], v19 offset:0\n"
    "ds_read_b64_tr_b16 a[250:251], v19 offset:512\n"
    "ds_read_b64_tr_b16 a[252:253], v17 offset:12288\n"
    "ds_read_b64_tr_b16 a[254:255], v17 offset:12800\n"
    "ds_read_b64_tr_b16 v[128:129], v17 offset:12296\n"
    "ds_read_b64_tr_b16 v[130:131], v17 offset:12808\n"
    "ds_read_b64_tr_b16 v[132:133], v19 offset:12288\n"
    "ds_read_b64_tr_b16 v[134:135], v19 offset:12800\n"
    "s_waitcnt vmcnt(24) lgkmcnt(0)\n"
    "s_barrier\n"
    "ds_read_b64_tr_b16 v[136:137], v17 offset:24576\n"
    "ds_read_b64_tr_b16 v[138:139], v17 offset:25088\n"
    "ds_read_b64_tr_b16 v[140:141], v17 offset:24584\n"
    "ds_read_b64_tr_b16 v[142:143], v17 offset:25096\n"
    "ds_read_b64_tr_b16 v[144:145], v19 offset:24576\n"
    "ds_read_b64_tr_b16 v[146:147], v19 offset:25088\n"
    "ds_read_b64_tr_b16 v[148:149], v17 offset:36864\n"
    "ds_read_b64_tr_b16 v[150:151], v17 offset:37376\n"
    "ds_read_b64_tr_b16 v[152:153], v17 offset:36872\n"
    "ds_read_b64_tr_b16 v[154:155], v17 offset:37384\n"
    "ds_read_b64_tr_b16 v[156:157], v19 offset:36864\n"
    "ds_read_b64_tr_b16 v[158:159], v19 offset:37376\n"
    "s_waitcnt vmcnt(18) lgkmcnt(0)\n"
    "s_barrier\n"
    "ds_read_b64_tr_b16 v[160:161], v6 offset:0\n"
    "ds_read_b64_tr_b16 v[162:163], v6 offset:512\n"
    "ds_read_b64_tr_b16 v[164:165], v6 offset:8\n"
    "ds_read_b64_tr_b16 v[166:167], v6 offset:520\n"
    "ds_read_b64_tr_b16 v[168:169], v7 offset:0\n"
    "ds_read_b64_tr_b16 v[170:171], v7 offset:512\n"
    "ds_read_b64_tr_b16 v[172:173], v6 offset:12288\n"
    "ds_read_b64_tr_b16 v[174:175], v6 offset:12800\n"
    "ds_read_b64_tr_b16 v[176:177], v6 offset:12296\n"
    "ds_read_b64_tr_b16 v[178:179], v6 offset:12808\n"
    "ds_read_b64_tr_b16 v[180:181], v7 offset:12288\n"
    "ds_read_b64_tr_b16 v[182:183], v7 offset:12800\n"
    "s_waitcnt vmcnt(12) lgkmcnt(0)\n"
    "s_barrier\n"
    "ds_read_b64_tr_b16 v[184:185], v6 offset:24576\n"
    "ds_read_b64_tr_b16 v[186:187], v6 offset:25088\n"
    "ds_read_b64_tr_b16 v[188:189], v6 offset:24584\n"
    "ds_read_b64_tr_b16 v[190:191], v6 offset:25096\n"
    "ds_read_b64_tr_b16 v[192:193], v7 offset:24576\n"
    "ds_read_b64_tr_b16 v[194:195], v7 offset:25088\n"
    "ds_read_b64_tr_b16 v[196:197], v6 offset:36864\n"
    "ds_read_b64_tr_b16 v[198:199], v6 offset:37376\n"
    "ds_read_b64_tr_b16 v[200:201], v6 offset:36872\n"
    "ds_read_b64_tr_b16 v[202:203], v6 offset:37384\n"
    "ds_read_b64_tr_b16 v[204:205], v7 offset:36864\n"
    "ds_read_b64_tr_b16 v[206:207], v7 offset:37376\n"
    "s_waitcnt vmcnt(6) lgkmcnt(0)\n"
    "s_barrier\n"
    "ds_read_b64_tr_b16 v[208:209], v16 offset:0\n"
    "ds_read_b64_tr_b16 v[210:211], v16 offset:512\n"
    "ds_read_b64_tr_b16 v[212:213], v16 offset:8\n"
    "ds_read_b64_tr_b16 v[214:215], v16 offset:520\n"
    "ds_read_b64_tr_b16 v[216:217], v18 offset:0\n"
    "ds_read_b64_tr_b16 v[218:219], v18 offset:512\n"
    "ds_read_b64_tr_b16 v[220:221], v16 offset:12288\n"
    "ds_read_b64_tr_b16 v[222:223], v16 offset:12800\n"
    "ds_read_b64_tr_b16 v[224:225], v16 offset:12296\n"
    "ds_read_b64_tr_b16 v[226:227], v16 offset:12808\n"
    "ds_read_b64_tr_b16 v[228:229], v18 offset:12288\n"
    "ds_read_b64_tr_b16 v[230:231], v18 offset:12800\n"
    "s_waitcnt vmcnt(0) lgkmcnt(0)\n"
    "s_barrier\n"
    "ds_read_b64_tr_b16 v[232:233], v16 offset:24576\n"
    "ds_read_b64_tr_b16 v[234:235], v16 offset:25088\n"
    "ds_read_b64_tr_b16 v[236:237], v16 offset:24584\n"
    "ds_read_b64_tr_b16 v[238:239], v16 offset:25096\n"
    "ds_read_b64_tr_b16 v[240:241], v18 offset:24576\n"
    "ds_read_b64_tr_b16 v[242:243], v18 offset:25088\n"
    "ds_read_b64_tr_b16 v[244:245], v16 offset:36864\n"
    "ds_read_b64_tr_b16 v[246:247], v16 offset:37376\n"
    "ds_read_b64_tr_b16 v[248:249], v16 offset:36872\n"
    "ds_read_b64_tr_b16 v[250:251], v16 offset:37384\n"
    "ds_read_b64_tr_b16 v[252:253], v18 offset:36864\n"
    "ds_read_b64_tr_b16 v[254:255], v18 offset:37376\n"
    "s_waitcnt lgkmcnt(0)\n"
    "s_barrier\n"
    "s_mov_b32 s68, 0\n"
    "s_mov_b32 s71, 0\n"
    "v_mov_b32 v11, -1\n"
    "s_mov_b32 s72, 262144\n"
    "s_branch .LgWO_block_%=\n"
    ".LgWO_LOOPB_%=:\n"
    ".LgWO_top_%=:\n"
    "s_waitcnt vmcnt(3)\n"
    ".LgWO_bar_%=:\n"
    "s_and_b32 s78, s68, 1\n"
    "s_mul_i32 s78, s78, 65536\n"
    "v_add_u32 v9, s78, v0\n"
    "v_add_u32 v10, 49152, v9\n"
    "s_add_u32 s71, s68, 1\n"
    "s_cmp_eq_u32 s71, 64\n"
    "s_cselect_b32 s70, 1, 0\n"
    "s_barrier\n"
    "ds_read_b128 v[16:19], v9 offset:0\n"
    "ds_read_b128 v[20:23], v9 offset:1024\n"
    "ds_read_b128 v[24:27], v9 offset:2048\n"
    "ds_read_b128 v[28:31], v9 offset:3072\n"
    "ds_read_b128 v[32:35], v9 offset:4096\n"
    "ds_read_b128 v[36:39], v9 offset:5120\n"
    "v_add_u32 v13, v11, v3\n"
    "v_cmp_gt_i32 vcc, s71, v13\n"
    "s_nop 0\n"
    "s_or_b64 exec, vcc, 1\n"
    "buffer_atomic_smax v11, v2, s[36:39], 0 offen sc0 sc1\n"
    "s_mov_b64 exec, -1\n"
    "s_and_b32 s73, s68, s57\n"
    "s_mul_i32 s73, s73, s56\n"
    "s_add_u32 s73, s73, s55\n"
    "s_waitcnt lgkmcnt(4)\n"
    "v_mfma_f32_16x16x32_bf16 v[48:51], a[0:3], v[16:19], 0\n"
    "ds_read_b128 v[40:43], v9 offset:6144\n"
    "ds_read_b128 v[44:47], v9 offset:7168\n"
    "v_mfma_f32_16x16x32_bf16 v[56:59], a[0:3], v[20:23], 0\n"
    "v_mfma_f32_16x16x32_bf16 v[52:55], a[4:7], v[16:19], 0\n"
    "v_mfma_f32_16x16x32_bf16 v[60:63], a[4:7], v[20:23], 0\n"
    "v_mfma_f32_16x16x32_bf16 v[64:67], a[8:11], v[16:19], 0\n"
    "v_mfma_f32_16x16x32_bf16 v[68:71], a[8:11], v[20:23], 0\n"
    "s_waitcnt lgkmcnt(4)\n"
    "v_mfma_f32_16x16x32_bf16 v[48:51], a[12:15], v[24:27], v[48:51]\n"
    "ds_read_b128 v[16:19], v9 offset:8192\n"
    "ds_read_b128 v[20:23], v9 offset:9216\n"
    "v_mfma_f32_16x16x32_bf16 v[56:59], a[12:15], v[28:31], v[56:59]\n"
    "v_mfma_f32_16x16x32_bf16 v[52:55], a[16:19], v[24:27], v[52:55]\n"
    "v_mfma_f32_16x16x32_bf16 v[60:63], a[16:19], v[28:31], v[60:63]\n"
    "v_mfma_f32_16x16x32_bf16 v[64:67], a[20:23], v[24:27], v[64:67]\n"
    "v_mfma_f32_16x16x32_bf16 v[68:71], a[20:23], v[28:31], v[68:71]\n"
    "s_waitcnt lgkmcnt(4)\n"
    "v_mfma_f32_16x16x32_bf16 v[48:51], a[24:27], v[32:35], v[48:51]\n"
    "ds_read_b128 v[24:27], v9 offset:10240\n"
    "ds_read_b128 v[28:31], v9 offset:11264\n"
    "v_mfma_f32_16x16x32_bf16 v[56:59], a[24:27], v[36:39], v[56:59]\n"
    "v_mfma_f32_16x16x32_bf16 v[52:55], a[28:31], v[32:35], v[52:55]\n"
    "v_mfma_f32_16x16x32_bf16 v[60:63], a[28:31], v[36:39], v[60:63]\n"
    "v_mfma_f32_16x16x32_bf16 v[64:67], a[32:35], v[32:35], v[64:67]\n"
    "v_mfma_f32_16x16x32_bf16 v[68:71], a[32:35], v[36:39], v[68:71]\n"
    "s_waitcnt lgkmcnt(4)\n"
    "v_mfma_f32_16x16x32_bf16 v[48:51], a[36:39], v[40:43], v[48:51]\n"
    "ds_read_b128 v[32:35], v9 offset:12288\n"
    "ds_read_b128 v[36:39], v9 offset:13312\n"
    "v_mfma_f32_16x16x32_bf16 v[56:59], a[36:39], v[44:47], v[56:59]\n"
    "v_mfma_f32_16x16x32_bf16 v[52:55], a[40:43], v[40:43], v[52:55]\n"
    "v_mfma_f32_16x16x32_bf16 v[60:63], a[40:43], v[44:47], v[60:63]\n"
    "v_mfma_f32_16x16x32_bf16 v[64:67], a[44:47], v[40:43], v[64:67]\n"
    "v_mfma_f32_16x16x32_bf16 v[68:71], a[44:47], v[44:47], v[68:71]\n"
    "s_waitcnt lgkmcnt(4)\n"
    "v_mfma_f32_16x16x32_bf16 v[48:51], a[48:51], v[16:19], v[48:51]\n"
    "ds_read_b128 v[40:43], v9 offset:14336\n"
    "ds_read_b128 v[44:47], v9 offset:15360\n"
    "v_mfma_f32_16x16x32_bf16 v[56:59], a[48:51], v[20:23], v[56:59]\n"
    "v_mfma_f32_16x16x32_bf16 v[52:55], a[52:55], v[16:19], v[52:55]\n"
    "v_mfma_f32_16x16x32_bf16 v[60:63], a[52:55], v[20:23], v[60:63]\n"
    "v_mfma_f32_16x16x32_bf16 v[64:67], a[56:59], v[16:19], v[64:67]\n"
    "v_mfma_f32_16x16x32_bf16 v[68:71], a[56:59], v[20:23], v[68:71]\n"
    "s_waitcnt lgkmcnt(4)\n"
    "v_mfma_f32_16x16x32_bf16 v[48:51], a[60:63], v[24:27], v[48:51]\n"
    "ds_read_b128 v[16:19], v9 offset:16384\n"
    "ds_read_b128 v[20:23], v9 offset:17408\n"
    "v_mfma_f32_16x16x32_bf16 v[56:59], a[60:63], v[28:31], v[56:59]\n"
    "v_mfma_f32_16x16x32_bf16 v[52:55], a[64:67], v[24:27], v[52:55]\n"
    "v_mfma_f32_16x16x32_bf16 v[60:63], a[64:67], v[28:31], v[60:63]\n"
    "v_mfma_f32_16x16x32_bf16 v[64:67], a[68:71], v[24:27], v[64:67]\n"
    "v_mfma_f32_16x16x32_bf16 v[68:71], a[68:71], v[28:31], v[68:71]\n"
    "s_waitcnt vmcnt(0)\n"
    "s_cmp_eq_u32 s69, 0\n"
    "s_cbranch_scc1 .LgWO_nopub_%=\n"
    "s_sub_u32 s74, s68, 1\n"
    "v_mov_b32 v12, s74\n"
    "s_mov_b64 exec, s[62:63]\n"
    "buffer_store_dword v12, v14, s[36:39], 0 offen sc1\n"
    "s_mov_b64 exec, -1\n"
    ".LgWO_nopub_%=:\n"
    "s_cmp_lg_u32 s70, 0\n"
    "s_cbranch_scc1 .LgWO_plain0_%=\n"
    "v_add_u32 v13, v11, v3\n"
    "v_cmp_gt_i32 vcc, s71, v13\n"
    "s_cbranch_vccz .LgWO_dma0_%=\n"
    "s_or_b64 exec, vcc, 1\n"
    "buffer_atomic_smax v11, v2, s[36:39], 0 offen sc0 sc1\n"
    "s_mov_b64 exec, -1\n"
    ".LgWO_plain0_%=:\n"
    "s_waitcnt lgkmcnt(4)\n"
    "v_mfma_f32_16x16x32_bf16 v[48:51], a[72:75], v[32:35], v[48:51]\n"
    "ds_read_b128 v[24:27], v9 offset:18432\n"
    "ds_read_b128 v[28:31], v9 offset:19456\n"
    "v_mfma_f32_16x16x32_bf16 v[56:59], a[72:75], v[36:39], v[56:59]\n"
    "v_mfma_f32_16x16x32_bf16 v[52:55], a[76:79], v[32:35], v[52:55]\n"
    "v_mfma_f32_16x16x32_bf16 v[60:63], a[76:79], v[36:39], v[60:63]\n"
    "v_mfma_f32_16x16x32_bf16 v[64:67], a[80:83], v[32:35], v[64:67]\n"
    "v_mfma_f32_16x16x32_bf16 v[68:71], a[80:83], v[36:39], v[68:71]\n"
    "s_waitcnt lgkmcnt(4)\n"
    "v_mfma_f32_16x16x32_bf16 v[48:51], a[84:87], v[40:43], v[48:51]\n"
    "ds_read_b128 v[32:35], v9 offset:20480\n"
    "ds_read_b128 v[36:39], v9 offset:21504\n"
    "v_mfma_f32_16x16x32_bf16 v[56:59], a[84:87], v[44:47], v[56:59]\n"
    "v_mfma_f32_16x16x32_bf16 v[52:55], a[88:91], v[40:43], v[52:55]\n"
    "v_mfma_f32_16x16x32_bf16 v[60:63], a[88:91], v[44:47], v[60:63]\n"
    "v_mfma_f32_16x16x32_bf16 v[64:67], a[92:95], v[40:43], v[64:67]\n"
    "v_mfma_f32_16x16x32_bf16 v[68:71], a[92:95], v[44:47], v[68:71]\n"
    "s_waitcnt lgkmcnt(4)\n"
    "v_mfma_f32_16x16x32_bf16 v[48:51], a[96:99], v[16:19], v[48:51]\n"
    "ds_read_b128 v[40:43], v9 offset:22528\n"
    "ds_read_b128 v[44:47], v9 offset:23552\n"
    "v_mfma_f32_16x16x32_bf16 v[56:59], a[96:99], v[20:23], v[56:59]\n"
    "v_mfma_f32_16x16x32_bf16 v[52:55], a[100:103], v[16:19], v[52:55]\n"
    "v_mfma_f32_16x16x32_bf16 v[60:63], a[100:103], v[20:23], v[60:63]\n"
    "v_mfma_f32_16x16x32_bf16 v[64:67], a[104:107], v[16:19], v[64:67]\n"
    "v_mfma_f32_16x16x32_bf16 v[68:71], a[104:107], v[20:23], v[68:71]\n"
    "s_waitcnt lgkmcnt(4)\n"
    "v_mfma_f32_16x16x32_bf16 v[48:51], a[108:111], v[24:27], v[48:51]\n"
    "ds_read_b128 v[16:19], v9 offset:24576\n"
    "ds_read_b128 v[20:23], v9 offset:25600\n"
    "v_mfma_f32_16x16x32_bf16 v[56:59], a[108:111], v[28:31], v[56:59]\n"
    "v_mfma_f32_16x16x32_bf16 v[52:55], a[112:115], v[24:27], v[52:55]\n"
    "v_mfma_f32_16x16x32_bf16 v[60:63], a[112:115], v[28:31], v[60:63]\n"
    "v_mfma_f32_16x16x32_bf16 v[64:67], a[116:119], v[24:27], v[64:67]\n"
    "v_mfma_f32_16x16x32_bf16 v[68:71], a[116:119], v[28:31], v[68:71]\n"
    "s_waitcnt lgkmcnt(4)\n"
    "v_mfma_f32_16x16x32_bf16 v[48:51], a[120:123], v[32:35], v[48:51]\n"
    "ds_read_b128 v[24:27], v9 offset:26624\n"
    "ds_read_b128 v[28:31], v9 offset:27648\n"
    "v_mfma_f32_16x16x32_bf16 v[56:59], a[120:123], v[36:39], v[56:59]\n"
    "v_mfma_f32_16x16x32_bf16 v[52:55], a[124:127], v[32:35], v[52:55]\n"
    "v_mfma_f32_16x16x32_bf16 v[60:63], a[124:127], v[36:39], v[60:63]\n"
    "v_mfma_f32_16x16x32_bf16 v[64:67], a[128:131], v[32:35], v[64:67]\n"
    "v_mfma_f32_16x16x32_bf16 v[68:71], a[128:131], v[36:39], v[68:71]\n"
    "s_waitcnt lgkmcnt(4)\n"
    "v_mfma_f32_16x16x32_bf16 v[48:51], a[132:135], v[40:43], v[48:51]\n"
    "ds_read_b128 v[32:35], v9 offset:28672\n"
    "ds_read_b128 v[36:39], v9 offset:29696\n"
    "v_mfma_f32_16x16x32_bf16 v[56:59], a[132:135], v[44:47], v[56:59]\n"
    "v_mfma_f32_16x16x32_bf16 v[52:55], a[136:139], v[40:43], v[52:55]\n"
    "v_mfma_f32_16x16x32_bf16 v[60:63], a[136:139], v[44:47], v[60:63]\n"
    "v_mfma_f32_16x16x32_bf16 v[64:67], a[140:143], v[40:43], v[64:67]\n"
    "v_mfma_f32_16x16x32_bf16 v[68:71], a[140:143], v[44:47], v[68:71]\n"
    "s_waitcnt lgkmcnt(4)\n"
    "v_mfma_f32_16x16x32_bf16 v[48:51], a[144:147], v[16:19], v[48:51]\n"
    "ds_read_b128 v[40:43], v9 offset:30720\n"
    "ds_read_b128 v[44:47], v9 offset:31744\n"
    "v_mfma_f32_16x16x32_bf16 v[56:59], a[144:147], v[20:23], v[56:59]\n"
    "v_mfma_f32_16x16x32_bf16 v[52:55], a[148:151], v[16:19], v[52:55]\n"
    "v_mfma_f32_16x16x32_bf16 v[60:63], a[148:151], v[20:23], v[60:63]\n"
    "v_mfma_f32_16x16x32_bf16 v[64:67], a[152:155], v[16:19], v[64:67]\n"
    "v_mfma_f32_16x16x32_bf16 v[68:71], a[152:155], v[20:23], v[68:71]\n"
    "s_waitcnt lgkmcnt(4)\n"
    "v_mfma_f32_16x16x32_bf16 v[48:51], a[156:159], v[24:27], v[48:51]\n"
    "ds_read_b128 v[16:19], v9 offset:32768\n"
    "ds_read_b128 v[20:23], v9 offset:33792\n"
    "v_mfma_f32_16x16x32_bf16 v[56:59], a[156:159], v[28:31], v[56:59]\n"
    "v_mfma_f32_16x16x32_bf16 v[52:55], a[160:163], v[24:27], v[52:55]\n"
    "v_mfma_f32_16x16x32_bf16 v[60:63], a[160:163], v[28:31], v[60:63]\n"
    "v_mfma_f32_16x16x32_bf16 v[64:67], a[164:167], v[24:27], v[64:67]\n"
    "v_mfma_f32_16x16x32_bf16 v[68:71], a[164:167], v[28:31], v[68:71]\n"
    "s_waitcnt lgkmcnt(4)\n"
    "v_mfma_f32_16x16x32_bf16 v[48:51], a[168:171], v[32:35], v[48:51]\n"
    "ds_read_b128 v[24:27], v9 offset:34816\n"
    "ds_read_b128 v[28:31], v9 offset:35840\n"
    "v_mfma_f32_16x16x32_bf16 v[56:59], a[168:171], v[36:39], v[56:59]\n"
    "v_mfma_f32_16x16x32_bf16 v[52:55], a[172:175], v[32:35], v[52:55]\n"
    "v_mfma_f32_16x16x32_bf16 v[60:63], a[172:175], v[36:39], v[60:63]\n"
    "v_mfma_f32_16x16x32_bf16 v[64:67], a[176:179], v[32:35], v[64:67]\n"
    "v_mfma_f32_16x16x32_bf16 v[68:71], a[176:179], v[36:39], v[68:71]\n"
    "s_waitcnt lgkmcnt(4)\n"
    "v_mfma_f32_16x16x32_bf16 v[48:51], a[180:183], v[40:43], v[48:51]\n"
    "ds_read_b128 v[32:35], v9 offset:36864\n"
    "ds_read_b128 v[36:39], v9 offset:37888\n"
    "v_mfma_f32_16x16x32_bf16 v[56:59], a[180:183], v[44:47], v[56:59]\n"
    "v_mfma_f32_16x16x32_bf16 v[52:55], a[184:187], v[40:43], v[52:55]\n"
    "v_mfma_f32_16x16x32_bf16 v[60:63], a[184:187], v[44:47], v[60:63]\n"
    "v_mfma_f32_16x16x32_bf16 v[64:67], a[188:191], v[40:43], v[64:67]\n"
    "v_mfma_f32_16x16x32_bf16 v[68:71], a[188:191], v[44:47], v[68:71]\n"
    "s_waitcnt lgkmcnt(4)\n"
    "v_mfma_f32_16x16x32_bf16 v[48:51], a[192:195], v[16:19], v[48:51]\n"
    "ds_read_b128 v[40:43], v9 offset:38912\n"
    "ds_read_b128 v[44:47], v9 offset:39936\n"
    "v_mfma_f32_16x16x32_bf16 v[56:59], a[192:195], v[20:23], v[56:59]\n"
    "v_mfma_f32_16x16x32_bf16 v[52:55], a[196:199], v[16:19], v[52:55]\n"
    "v_mfma_f32_16x16x32_bf16 v[60:63], a[196:199], v[20:23], v[60:63]\n"
    "v_mfma_f32_16x16x32_bf16 v[64:67], a[200:203], v[16:19], v[64:67]\n"
    "v_mfma_f32_16x16x32_bf16 v[68:71], a[200:203], v[20:23], v[68:71]\n"
    ".LgWO_seg0_%=:\n"
    "s_cmp_lg_u32 s70, 0\n"
    "s_cbranch_scc1 .LgWO_plain1_%=\n"
    "s_waitcnt vmcnt(0)\n"
    "v_add_u32 v13, v11, v3\n"
    "v_cmp_gt_i32 vcc, s71, v13\n"
    "s_cbranch_vccz .LgWO_dma1_%=\n"
    ".LgWO_plain1_%=:\n"
    "s_waitcnt lgkmcnt(4)\n"
    "v_mfma_f32_16x16x32_bf16 v[48:51], a[204:207], v[24:27], v[48:51]\n"
    "ds_read_b128 v[16:19], v9 offset:40960\n"
    "ds_read_b128 v[20:23], v9 offset:41984\n"
    "v_mfma_f32_16x16x32_bf16 v[56:59], a[204:207], v[28:31], v[56:59]\n"
    "v_mfma_f32_16x16x32_bf16 v[52:55], a[208:211], v[24:27], v[52:55]\n"
    "v_mfma_f32_16x16x32_bf16 v[60:63], a[208:211], v[28:31], v[60:63]\n"
    "v_mfma_f32_16x16x32_bf16 v[64:67], a[212:215], v[24:27], v[64:67]\n"
    "v_mfma_f32_16x16x32_bf16 v[68:71], a[212:215], v[28:31], v[68:71]\n"
    "s_waitcnt lgkmcnt(4)\n"
    "v_mfma_f32_16x16x32_bf16 v[48:51], a[216:219], v[32:35], v[48:51]\n"
    "ds_read_b128 v[24:27], v9 offset:43008\n"
    "ds_read_b128 v[28:31], v9 offset:44032\n"
    "v_mfma_f32_16x16x32_bf16 v[56:59], a[216:219], v[36:39], v[56:59]\n"
    "v_mfma_f32_16x16x32_bf16 v[52:55], a[220:223], v[32:35], v[52:55]\n"
    "v_mfma_f32_16x16x32_bf16 v[60:63], a[220:223], v[36:39], v[60:63]\n"
    "v_mfma_f32_16x16x32_bf16 v[64:67], a[224:227], v[32:35], v[64:67]\n"
    "v_mfma_f32_16x16x32_bf16 v[68:71], a[224:227], v[36:39], v[68:71]\n"
    "s_waitcnt lgkmcnt(4)\n"
    "v_mfma_f32_16x16x32_bf16 v[48:51], a[228:231], v[40:43], v[48:51]\n"
    "ds_read_b128 v[32:35], v9 offset:45056\n"
    "ds_read_b128 v[36:39], v9 offset:46080\n"
    "v_mfma_f32_16x16x32_bf16 v[56:59], a[228:231], v[44:47], v[56:59]\n"
    "v_mfma_f32_16x16x32_bf16 v[52:55], a[232:235], v[40:43], v[52:55]\n"
    "v_mfma_f32_16x16x32_bf16 v[60:63], a[232:235], v[44:47], v[60:63]\n"
    "v_mfma_f32_16x16x32_bf16 v[64:67], a[236:239], v[40:43], v[64:67]\n"
    "v_mfma_f32_16x16x32_bf16 v[68:71], a[236:239], v[44:47], v[68:71]\n"
    "s_waitcnt lgkmcnt(4)\n"
    "v_mfma_f32_16x16x32_bf16 v[48:51], a[240:243], v[16:19], v[48:51]\n"
    "ds_read_b128 v[40:43], v9 offset:47104\n"
    "ds_read_b128 v[44:47], v9 offset:48128\n"
    "v_mfma_f32_16x16x32_bf16 v[56:59], a[240:243], v[20:23], v[56:59]\n"
    "v_mfma_f32_16x16x32_bf16 v[52:55], a[244:247], v[16:19], v[52:55]\n"
    "v_mfma_f32_16x16x32_bf16 v[60:63], a[244:247], v[20:23], v[60:63]\n"
    "v_mfma_f32_16x16x32_bf16 v[64:67], a[248:251], v[16:19], v[64:67]\n"
    "v_mfma_f32_16x16x32_bf16 v[68:71], a[248:251], v[20:23], v[68:71]\n"
    "s_waitcnt lgkmcnt(4)\n"
    "v_mfma_f32_16x16x32_bf16 v[48:51], a[252:255], v[24:27], v[48:51]\n"
    "ds_read_b128 v[16:19], v10 offset:0\n"
    "ds_read_b128 v[20:23], v10 offset:1024\n"
    "v_mfma_f32_16x16x32_bf16 v[56:59], a[252:255], v[28:31], v[56:59]\n"
    "v_mfma_f32_16x16x32_bf16 v[52:55], v[128:131], v[24:27], v[52:55]\n"
    "v_mfma_f32_16x16x32_bf16 v[60:63], v[128:131], v[28:31], v[60:63]\n"
    "v_mfma_f32_16x16x32_bf16 v[64:67], v[132:135], v[24:27], v[64:67]\n"
    "v_mfma_f32_16x16x32_bf16 v[68:71], v[132:135], v[28:31], v[68:71]\n"
    "s_waitcnt lgkmcnt(4)\n"
    "v_mfma_f32_16x16x32_bf16 v[48:51], v[136:139], v[32:35], v[48:51]\n"
    "ds_read_b128 v[24:27], v10 offset:2048\n"
    "ds_read_b128 v[28:31], v10 offset:3072\n"
    "v_mfma_f32_16x16x32_bf16 v[56:59], v[136:139], v[36:39], v[56:59]\n"
    "v_mfma_f32_16x16x32_bf16 v[52:55], v[140:143], v[32:35], v[52:55]\n"
    "v_mfma_f32_16x16x32_bf16 v[60:63], v[140:143], v[36:39], v[60:63]\n"
    "v_mfma_f32_16x16x32_bf16 v[64:67], v[144:147], v[32:35], v[64:67]\n"
    "v_mfma_f32_16x16x32_bf16 v[68:71], v[144:147], v[36:39], v[68:71]\n"
    "s_waitcnt lgkmcnt(4)\n"
    "v_mfma_f32_16x16x32_bf16 v[48:51], v[148:151], v[40:43], v[48:51]\n"
    "ds_read_b128 v[32:35], v10 offset:4096\n"
    "ds_read_b128 v[36:39], v10 offset:5120\n"
    "v_mfma_f32_16x16x32_bf16 v[56:59], v[148:151], v[44:47], v[56:59]\n"
    "v_mfma_f32_16x16x32_bf16 v[52:55], v[152:155], v[40:43], v[52:55]\n"
    "v_mfma_f32_16x16x32_bf16 v[60:63], v[152:155], v[44:47], v[60:63]\n"
    "v_mfma_f32_16x16x32_bf16 v[64:67], v[156:159], v[40:43], v[64:67]\n"
    "v_mfma_f32_16x16x32_bf16 v[68:71], v[156:159], v[44:47], v[68:71]\n"
    "s_waitcnt lgkmcnt(4)\n"
    "v_mfma_f32_16x16x32_bf16 v[48:51], v[160:163], v[16:19], v[48:51]\n"
    "ds_read_b128 v[40:43], v10 offset:6144\n"
    "ds_read_b128 v[44:47], v10 offset:7168\n"
    "v_mfma_f32_16x16x32_bf16 v[56:59], v[160:163], v[20:23], v[56:59]\n"
    "v_mfma_f32_16x16x32_bf16 v[52:55], v[164:167], v[16:19], v[52:55]\n"
    "v_mfma_f32_16x16x32_bf16 v[60:63], v[164:167], v[20:23], v[60:63]\n"
    "v_mfma_f32_16x16x32_bf16 v[64:67], v[168:171], v[16:19], v[64:67]\n"
    "v_mfma_f32_16x16x32_bf16 v[68:71], v[168:171], v[20:23], v[68:71]\n"
    "s_waitcnt lgkmcnt(4)\n"
    "v_mfma_f32_16x16x32_bf16 v[48:51], v[172:175], v[24:27], v[48:51]\n"
    "ds_read_b128 v[16:19], v10 offset:8192\n"
    "ds_read_b128 v[20:23], v10 offset:9216\n"
    "v_mfma_f32_16x16x32_bf16 v[56:59], v[172:175], v[28:31], v[56:59]\n"
    "v_mfma_f32_16x16x32_bf16 v[52:55], v[176:179], v[24:27], v[52:55]\n"
    "v_mfma_f32_16x16x32_bf16 v[60:63], v[176:179], v[28:31], v[60:63]\n"
    "v_mfma_f32_16x16x32_bf16 v[64:67], v[180:183], v[24:27], v[64:67]\n"
    "v_mfma_f32_16x16x32_bf16 v[68:71], v[180:183], v[28:31], v[68:71]\n"
    "s_waitcnt lgkmcnt(4)\n"
    "v_mfma_f32_16x16x32_bf16 v[48:51], v[184:187], v[32:35], v[48:51]\n"
    "ds_read_b128 v[24:27], v10 offset:10240\n"
    "ds_read_b128 v[28:31], v10 offset:11264\n"
    "v_mfma_f32_16x16x32_bf16 v[56:59], v[184:187], v[36:39], v[56:59]\n"
    "v_mfma_f32_16x16x32_bf16 v[52:55], v[188:191], v[32:35], v[52:55]\n"
    "v_mfma_f32_16x16x32_bf16 v[60:63], v[188:191], v[36:39], v[60:63]\n"
    "v_mfma_f32_16x16x32_bf16 v[64:67], v[192:195], v[32:35], v[64:67]\n"
    "v_mfma_f32_16x16x32_bf16 v[68:71], v[192:195], v[36:39], v[68:71]\n"
    "s_waitcnt lgkmcnt(4)\n"
    "v_mfma_f32_16x16x32_bf16 v[48:51], v[196:199], v[40:43], v[48:51]\n"
    "ds_read_b128 v[32:35], v10 offset:12288\n"
    "ds_read_b128 v[36:39], v10 offset:13312\n"
    "v_mfma_f32_16x16x32_bf16 v[56:59], v[196:199], v[44:47], v[56:59]\n"
    "v_mfma_f32_16x16x32_bf16 v[52:55], v[200:203], v[40:43], v[52:55]\n"
    "v_mfma_f32_16x16x32_bf16 v[60:63], v[200:203], v[44:47], v[60:63]\n"
    "v_mfma_f32_16x16x32_bf16 v[64:67], v[204:207], v[40:43], v[64:67]\n"
    "v_mfma_f32_16x16x32_bf16 v[68:71], v[204:207], v[44:47], v[68:71]\n"
    ".LgWO_seg1_%=:\n"
    "s_waitcnt lgkmcnt(4)\n"
    "v_mfma_f32_16x16x32_bf16 v[48:51], v[208:211], v[16:19], v[48:51]\n"
    "ds_read_b128 v[40:43], v10 offset:14336\n"
    "ds_read_b128 v[44:47], v10 offset:15360\n"
    "v_mfma_f32_16x16x32_bf16 v[56:59], v[208:211], v[20:23], v[56:59]\n"
    "v_mfma_f32_16x16x32_bf16 v[52:55], v[212:215], v[16:19], v[52:55]\n"
    "v_mfma_f32_16x16x32_bf16 v[60:63], v[212:215], v[20:23], v[60:63]\n"
    "v_mfma_f32_16x16x32_bf16 v[64:67], v[216:219], v[16:19], v[64:67]\n"
    "v_mfma_f32_16x16x32_bf16 v[68:71], v[216:219], v[20:23], v[68:71]\n"
    "s_waitcnt lgkmcnt(4)\n"
    "v_mfma_f32_16x16x32_bf16 v[48:51], v[220:223], v[24:27], v[48:51]\n"
    "v_mfma_f32_16x16x32_bf16 v[56:59], v[220:223], v[28:31], v[56:59]\n"
    "v_mfma_f32_16x16x32_bf16 v[52:55], v[224:227], v[24:27], v[52:55]\n"
    "v_mfma_f32_16x16x32_bf16 v[60:63], v[224:227], v[28:31], v[60:63]\n"
    "v_mfma_f32_16x16x32_bf16 v[64:67], v[228:231], v[24:27], v[64:67]\n"
    "v_mfma_f32_16x16x32_bf16 v[68:71], v[228:231], v[28:31], v[68:71]\n"
    "s_waitcnt lgkmcnt(2)\n"
    "v_mfma_f32_16x16x32_bf16 v[48:51], v[232:235], v[32:35], v[48:51]\n"
    "v_mfma_f32_16x16x32_bf16 v[56:59], v[232:235], v[36:39], v[56:59]\n"
    "v_mfma_f32_16x16x32_bf16 v[52:55], v[236:239], v[32:35], v[52:55]\n"
    "v_mfma_f32_16x16x32_bf16 v[60:63], v[236:239], v[36:39], v[60:63]\n"
    "v_mfma_f32_16x16x32_bf16 v[64:67], v[240:243], v[32:35], v[64:67]\n"
    "v_mfma_f32_16x16x32_bf16 v[68:71], v[240:243], v[36:39], v[68:71]\n"
    "s_cmp_lg_u32 s70, 0\n"
    "s_cbranch_scc0 .LgWO_lastp_%=\n"
    ".LgWO_nolast_%=:\n"
    "s_waitcnt lgkmcnt(0)\n"
    "v_mfma_f32_16x16x32_bf16 v[48:51], v[244:247], v[40:43], v[48:51]\n"
    "v_mfma_f32_16x16x32_bf16 v[56:59], v[244:247], v[44:47], v[56:59]\n"
    "v_mfma_f32_16x16x32_bf16 v[52:55], v[248:251], v[40:43], v[52:55]\n"
    "v_mfma_f32_16x16x32_bf16 v[60:63], v[248:251], v[44:47], v[60:63]\n"
    "v_mfma_f32_16x16x32_bf16 v[64:67], v[252:255], v[40:43], v[64:67]\n"
    "v_mfma_f32_16x16x32_bf16 v[68:71], v[252:255], v[44:47], v[68:71]\n"
    "v_cvt_pk_bf16_f32 v48, v48, v49\n"
    "v_cvt_pk_bf16_f32 v49, v50, v51\n"
    "v_cvt_pk_bf16_f32 v50, v52, v53\n"
    "v_cvt_pk_bf16_f32 v51, v54, v55\n"
    "buffer_store_dwordx4 v[48:51], v1, s[36:39], s73 offen sc1\n"
    "v_cvt_pk_bf16_f32 v56, v56, v57\n"
    "v_cvt_pk_bf16_f32 v57, v58, v59\n"
    "v_cvt_pk_bf16_f32 v58, v60, v61\n"
    "v_cvt_pk_bf16_f32 v59, v62, v63\n"
    "buffer_store_dwordx4 v[56:59], v1, s[36:39], s73 offen offset:1024 sc1\n"
    "v_cvt_pk_bf16_f32 v64, v64, v65\n"
    "v_cvt_pk_bf16_f32 v65, v66, v67\n"
    "v_cvt_pk_bf16_f32 v66, v68, v69\n"
    "v_cvt_pk_bf16_f32 v67, v70, v71\n"
    "s_nop 0\n"
    "v_permlane16_swap_b32 v64, v66\n"
    "v_permlane16_swap_b32 v65, v67\n"
    "buffer_store_dwordx4 v[64:67], v4, s[36:39], s73 offen sc1\n"
    "s_mov_b32 s68, s71\n"
    "s_mov_b32 s69, 1\n"
    "s_cmp_eq_u32 s70, 0\n"
    "s_cbranch_scc1 .LgWO_idle_%=\n"
    "s_cmp_lg_u32 s68, 64\n"
    "s_cbranch_scc1 .LgWO_top_%=\n"
    ".LgWO_LOOPE_%=:\n"
    "s_waitcnt vmcnt(0)\n"
    "s_mov_b32 s74, 63\n"
    "v_mov_b32 v12, s74\n"
    "s_mov_b64 exec, s[62:63]\n"
    "buffer_store_dword v12, v14, s[36:39], 0 offen sc1\n"
    "s_mov_b64 exec, -1\n"
    "s_waitcnt vmcnt(0)\n"
    "s_branch .LgWO_FIN_%=\n"
    ".LgWO_dma0_%=:\n"
    "s_mov_b32 s70, 1\n"
    "s_and_b32 s76, s71, s54\n"
    "s_mul_i32 s76, s76, s53\n"
    "s_add_u32 s76, s76, s52\n"
    "s_and_b32 s77, s71, 1\n"
    "s_mul_i32 s77, s77, 65536\n"
    "s_add_u32 s77, s77, s59\n"
    "s_waitcnt lgkmcnt(4)\n"
    "v_mfma_f32_16x16x32_bf16 v[48:51], a[72:75], v[32:35], v[48:51]\n"
    "ds_read_b128 v[24:27], v9 offset:18432\n"
    "ds_read_b128 v[28:31], v9 offset:19456\n"
    "s_add_u32 m0, s77, s59\n"
    "s_add_u32 s77, s77, 8192\n"
    "buffer_load_dwordx4 v1, s[36:39], s76 offen sc1 lds\n"
    "v_mfma_f32_16x16x32_bf16 v[56:59], a[72:75], v[36:39], v[56:59]\n"
    "v_mfma_f32_16x16x32_bf16 v[52:55], a[76:79], v[32:35], v[52:55]\n"
    "v_mfma_f32_16x16x32_bf16 v[60:63], a[76:79], v[36:39], v[60:63]\n"
    "v_mfma_f32_16x16x32_bf16 v[64:67], a[80:83], v[32:35], v[64:67]\n"
    "buffer_load_dwordx4 v1, s[36:39], s76 offen offset:1024 sc1 lds\n"
    "s_add_u32 s76, s76, 8192\n"
    "v_mfma_f32_16x16x32_bf16 v[68:71], a[80:83], v[36:39], v[68:71]\n"
    "s_waitcnt lgkmcnt(4)\n"
    "v_mfma_f32_16x16x32_bf16 v[48:51], a[84:87], v[40:43], v[48:51]\n"
    "ds_read_b128 v[32:35], v9 offset:20480\n"
    "ds_read_b128 v[36:39], v9 offset:21504\n"
    "v_mfma_f32_16x16x32_bf16 v[56:59], a[84:87], v[44:47], v[56:59]\n"
    "v_mfma_f32_16x16x32_bf16 v[52:55], a[88:91], v[40:43], v[52:55]\n"
    "s_add_u32 m0, s77, s59\n"
    "s_add_u32 s77, s77, 8192\n"
    "buffer_load_dwordx4 v1, s[36:39], s76 offen sc1 lds\n"
    "v_mfma_f32_16x16x32_bf16 v[60:63], a[88:91], v[44:47], v[60:63]\n"
    "v_mfma_f32_16x16x32_bf16 v[64:67], a[92:95], v[40:43], v[64:67]\n"
    "v_mfma_f32_16x16x32_bf16 v[68:71], a[92:95], v[44:47], v[68:71]\n"
    "s_waitcnt lgkmcnt(4)\n"
    "v_mfma_f32_16x16x32_bf16 v[48:51], a[96:99], v[16:19], v[48:51]\n"
    "ds_read_b128 v[40:43], v9 offset:22528\n"
    "ds_read_b128 v[44:47], v9 offset:23552\n"
    "buffer_load_dwordx4 v1, s[36:39], s76 offen offset:1024 sc1 lds\n"
    "s_add_u32 s76, s76, 8192\n"
    "v_mfma_f32_16x16x32_bf16 v[56:59], a[96:99], v[20:23], v[56:59]\n"
    "v_mfma_f32_16x16x32_bf16 v[52:55], a[100:103], v[16:19], v[52:55]\n"
    "v_mfma_f32_16x16x32_bf16 v[60:63], a[100:103], v[20:23], v[60:63]\n"
    "v_mfma_f32_16x16x32_bf16 v[64:67], a[104:107], v[16:19], v[64:67]\n"
    "s_add_u32 m0, s77, s59\n"
    "s_add_u32 s77, s77, 8192\n"
    "buffer_load_dwordx4 v1, s[36:39], s76 offen sc1 lds\n"
    "v_mfma_f32_16x16x32_bf16 v[68:71], a[104:107], v[20:23], v[68:71]\n"
    "s_waitcnt lgkmcnt(4)\n"
    "v_mfma_f32_16x16x32_bf16 v[48:51], a[108:111], v[24:27], v[48:51]\n"
    "ds_read_b128 v[16:19], v9 offset:24576\n"
    "ds_read_b128 v[20:23], v9 offset:25600\n"
    "v_mfma_f32_16x16x32_bf16 v[56:59], a[108:111], v[28:31], v[56:59]\n"
    "v_mfma_f32_16x16x32_bf16 v[52:55], a[112:115], v[24:27], v[52:55]\n"
    "buffer_load_dwordx4 v1, s[36:39], s76 offen offset:1024 sc1 lds\n"
    "s_add_u32 s76, s76, 8192\n"
    "v_mfma_f32_16x16x32_bf16 v[60:63], a[112:115], v[28:31], v[60:63]\n"
    "v_mfma_f32_16x16x32_bf16 v[64:67], a[116:119], v[24:27], v[64:67]\n"
    "v_mfma_f32_16x16x32_bf16 v[68:71], a[116:119], v[28:31], v[68:71]\n"
    "s_waitcnt lgkmcnt(4)\n"
    "v_mfma_f32_16x16x32_bf16 v[48:51], a[120:123], v[32:35], v[48:51]\n"
    "ds_read_b128 v[24:27], v9 offset:26624\n"
    "ds_read_b128 v[28:31], v9 offset:27648\n"
    "s_add_u32 m0, s77, s59\n"
    "s_add_u32 s77, s77, 8192\n"
    "buffer_load_dwordx4 v1, s[36:39], s76 offen sc1 lds\n"
    "v_mfma_f32_16x16x32_bf16 v[56:59], a[120:123], v[36:39], v[56:59]\n"
    "v_mfma_f32_16x16x32_bf16 v[52:55], a[124:127], v[32:35], v[52:55]\n"
    "v_mfma_f32_16x16x32_bf16 v[60:63], a[124:127], v[36:39], v[60:63]\n"
    "v_mfma_f32_16x16x32_bf16 v[64:67], a[128:131], v[32:35], v[64:67]\n"
    "buffer_load_dwordx4 v1, s[36:39], s76 offen offset:1024 sc1 lds\n"
    "s_add_u32 s76, s76, 8192\n"
    "v_mfma_f32_16x16x32_bf16 v[68:71], a[128:131], v[36:39], v[68:71]\n"
    "s_waitcnt lgkmcnt(4)\n"
    "v_mfma_f32_16x16x32_bf16 v[48:51], a[132:135], v[40:43], v[48:51]\n"
    "ds_read_b128 v[32:35], v9 offset:28672\n"
    "ds_read_b128 v[36:39], v9 offset:29696\n"
    "v_mfma_f32_16x16x32_bf16 v[56:59], a[132:135], v[44:47], v[56:59]\n"
    "v_mfma_f32_16x16x32_bf16 v[52:55], a[136:139], v[40:43], v[52:55]\n"
    "s_add_u32 m0, s77, s59\n"
    "s_add_u32 s77, s77, 8192\n"
    "buffer_load_dwordx4 v1, s[36:39], s76 offen sc1 lds\n"
    "v_mfma_f32_16x16x32_bf16 v[60:63], a[136:139], v[44:47], v[60:63]\n"
    "v_mfma_f32_16x16x32_bf16 v[64:67], a[140:143], v[40:43], v[64:67]\n"
    "v_mfma_f32_16x16x32_bf16 v[68:71], a[140:143], v[44:47], v[68:71]\n"
    "s_waitcnt lgkmcnt(4)\n"
    "v_mfma_f32_16x16x32_bf16 v[48:51], a[144:147], v[16:19], v[48:51]\n"
    "ds_read_b128 v[40:43], v9 offset:30720\n"
    "ds_read_b128 v[44:47], v9 offset:31744\n"
    "buffer_load_dwordx4 v1, s[36:39], s76 offen offset:1024 sc1 lds\n"
    "s_add_u32 s76, s76, 8192\n"
    "v_mfma_f32_16x16x32_bf16 v[56:59], a[144:147], v[20:23], v[56:59]\n"
    "v_mfma_f32_16x16x32_bf16 v[52:55], a[148:151], v[16:19], v[52:55]\n"
    "v_mfma_f32_16x16x32_bf16 v[60:63], a[148:151], v[20:23], v[60:63]\n"
    "v_mfma_f32_16x16x32_bf16 v[64:67], a[152:155], v[16:19], v[64:67]\n"
    "s_add_u32 m0, s77, s59\n"
    "s_add_u32 s77, s77, 8192\n"
    "buffer_load_dwordx4 v1, s[36:39], s76 offen sc1 lds\n"
    "v_mfma_f32_16x16x32_bf16 v[68:71], a[152:155], v[20:23], v[68:71]\n"
    "s_waitcnt lgkmcnt(4)\n"
    "v_mfma_f32_16x16x32_bf16 v[48:51], a[156:159], v[24:27], v[48:51]\n"
    "ds_read_b128 v[16:19], v9 offset:32768\n"
    "ds_read_b128 v[20:23], v9 offset:33792\n"
    "v_mfma_f32_16x16x32_bf16 v[56:59], a[156:159], v[28:31], v[56:59]\n"
    "v_mfma_f32_16x16x32_bf16 v[52:55], a[160:163], v[24:27], v[52:55]\n"
    "buffer_load_dwordx4 v1, s[36:39], s76 offen offset:1024 sc1 lds\n"
    "s_add_u32 s76, s76, 8192\n"
    "v_mfma_f32_16x16x32_bf16 v[60:63], a[160:163], v[28:31], v[60:63]\n"
    "v_mfma_f32_16x16x32_bf16 v[64:67], a[164:167], v[24:27], v[64:67]\n"
    "v_mfma_f32_16x16x32_bf16 v[68:71], a[164:167], v[28:31], v[68:71]\n"
    "s_waitcnt lgkmcnt(4)\n"
    "v_mfma_f32_16x16x32_bf16 v[48:51], a[168:171], v[32:35], v[48:51]\n"
    "ds_read_b128 v[24:27], v9 offset:34816\n"
    "ds_read_b128 v[28:31], v9 offset:35840\n"
    "s_add_u32 m0, s77, s59\n"
    "s_add_u32 s77, s77, 8192\n"
    "buffer_load_dwordx4 v1, s[36:39], s76 offen sc1 lds\n"
    "v_mfma_f32_16x16x32_bf16 v[56:59], a[168:171], v[36:39], v[56:59]\n"
    "v_mfma_f32_16x16x32_bf16 v[52:55], a[172:175], v[32:35], v[52:55]\n"
    "v_mfma_f32_16x16x32_bf16 v[60:63], a[172:175], v[36:39], v[60:63]\n"
    "v_mfma_f32_16x16x32_bf16 v[64:67], a[176:179], v[32:35], v[64:67]\n"
    "buffer_load_dwordx4 v1, s[36:39], s76 offen offset:1024 sc1 lds\n"
    "s_add_u32 s76, s76, 8192\n"
    "v_mfma_f32_16x16x32_bf16 v[68:71], a[176:179], v[36:39], v[68:71]\n"
    "s_waitcnt lgkmcnt(4)\n"
    "v_mfma_f32_16x16x32_bf16 v[48:51], a[180:183], v[40:43], v[48:51]\n"
    "ds_read_b128 v[32:35], v9 offset:36864\n"
    "ds_read_b128 v[36:39], v9 offset:37888\n"
    "v_mfma_f32_16x16x32_bf16 v[56:59], a[180:183], v[44:47], v[56:59]\n"
    "v_mfma_f32_16x16x32_bf16 v[52:55], a[184:187], v[40:43], v[52:55]\n"
    "s_add_u32 m0, s77, s59\n"
    "s_add_u32 s77, s77, 8192\n"
    "buffer_load_dwordx4 v1, s[36:39], s76 offen sc1 lds\n"
    "v_mfma_f32_16x16x32_bf16 v[60:63], a[184:187], v[44:47], v[60:63]\n"
    "v_mfma_f32_16x16x32_bf16 v[64:67], a[188:191], v[40:43], v[64:67]\n"
    "v_mfma_f32_16x16x32_bf16 v[68:71], a[188:191], v[44:47], v[68:71]\n"
    "s_waitcnt lgkmcnt(4)\n"
    "v_mfma_f32_16x16x32_bf16 v[48:51], a[192:195], v[16:19], v[48:51]\n"
    "ds_read_b128 v[40:43], v9 offset:38912\n"
    "ds_read_b128 v[44:47], v9 offset:39936\n"
    "buffer_load_dwordx4 v1, s[36:39], s76 offen offset:1024 sc1 lds\n"
    "s_add_u32 s76, s76, 8192\n"
    "v_mfma_f32_16x16x32_bf16 v[56:59], a[192:195], v[20:23], v[56:59]\n"
    "v_mfma_f32_16x16x32_bf16 v[52:55], a[196:199], v[16:19], v[52:55]\n"
    "v_mfma_f32_16x16x32_bf16 v[60:63], a[196:199], v[20:23], v[60:63]\n"
    "v_mfma_f32_16x16x32_bf16 v[64:67], a[200:203], v[16:19], v[64:67]\n"
    "v_mfma_f32_16x16x32_bf16 v[68:71], a[200:203], v[20:23], v[68:71]\n"
    "s_branch .LgWO_seg0_%=\n"
    ".LgWO_dma1_%=:\n"
    "s_mov_b32 s70, 1\n"
    "s_and_b32 s76, s71, s54\n"
    "s_mul_i32 s76, s76, s53\n"
    "s_add_u32 s76, s76, s52\n"
    "s_and_b32 s77, s71, 1\n"
    "s_mul_i32 s77, s77, 65536\n"
    "s_add_u32 s77, s77, s59\n"
    "s_waitcnt lgkmcnt(4)\n"
    "v_mfma_f32_16x16x32_bf16 v[48:51], a[204:207], v[24:27], v[48:51]\n"
    "ds_read_b128 v[16:19], v9 offset:40960\n"
    "ds_read_b128 v[20:23], v9 offset:41984\n"
    "s_add_u32 m0, s77, s59\n"
    "s_add_u32 s77, s77, 8192\n"
    "buffer_load_dwordx4 v1, s[36:39], s76 offen sc1 lds\n"
    "v_mfma_f32_16x16x32_bf16 v[56:59], a[204:207], v[28:31], v[56:59]\n"
    "v_mfma_f32_16x16x32_bf16 v[52:55], a[208:211], v[24:27], v[52:55]\n"
    "v_mfma_f32_16x16x32_bf16 v[60:63], a[208:211], v[28:31], v[60:63]\n"
    "v_mfma_f32_16x16x32_bf16 v[64:67], a[212:215], v[24:27], v[64:67]\n"
    "buffer_load_dwordx4 v1, s[36:39], s76 offen offset:1024 sc1 lds\n"
    "s_add_u32 s76, s76, 8192\n"
    "v_mfma_f32_16x16x32_bf16 v[68:71], a[212:215], v[28:31], v[68:71]\n"
    "s_waitcnt lgkmcnt(4)\n"
    "v_mfma_f32_16x16x32_bf16 v[48:51], a[216:219], v[32:35], v[48:51]\n"
    "ds_read_b128 v[24:27], v9 offset:43008\n"
    "ds_read_b128 v[28:31], v9 offset:44032\n"
    "v_mfma_f32_16x16x32_bf16 v[56:59], a[216:219], v[36:39], v[56:59]\n"
    "v_mfma_f32_16x16x32_bf16 v[52:55], a[220:223], v[32:35], v[52:55]\n"
    "s_add_u32 m0, s77, s59\n"
    "s_add_u32 s77, s77, 8192\n"
    "buffer_load_dwordx4 v1, s[36:39], s76 offen sc1 lds\n"
    "v_mfma_f32_16x16x32_bf16 v[60:63], a[220:223], v[36:39], v[60:63]\n"
    "v_mfma_f32_16x16x32_bf16 v[64:67], a[224:227], v[32:35], v[64:67]\n"
    "v_mfma_f32_16x16x32_bf16 v[68:71], a[224:227], v[36:39], v[68:71]\n"
    "s_waitcnt lgkmcnt(4)\n"
    "v_mfma_f32_16x16x32_bf16 v[48:51], a[228:231], v[40:43], v[48:51]\n"
    "ds_read_b128 v[32:35], v9 offset:45056\n"
    "ds_read_b128 v[36:39], v9 offset:46080\n"
    "buffer_load_dwordx4 v1, s[36:39], s76 offen offset:1024 sc1 lds\n"
    "s_add_u32 s76, s76, 8192\n"
    "v_mfma_f32_16x16x32_bf16 v[56:59], a[228:231], v[44:47], v[56:59]\n"
    "v_mfma_f32_16x16x32_bf16 v[52:55], a[232:235], v[40:43], v[52:55]\n"
    "v_mfma_f32_16x16x32_bf16 v[60:63], a[232:235], v[44:47], v[60:63]\n"
    "v_mfma_f32_16x16x32_bf16 v[64:67], a[236:239], v[40:43], v[64:67]\n"
    "s_add_u32 m0, s77, s59\n"
    "s_add_u32 s77, s77, 8192\n"
    "buffer_load_dwordx4 v1, s[36:39], s76 offen sc1 lds\n"
    "v_mfma_f32_16x16x32_bf16 v[68:71], a[236:239], v[44:47], v[68:71]\n"
    "s_waitcnt lgkmcnt(4)\n"
    "v_mfma_f32_16x16x32_bf16 v[48:51], a[240:243], v[16:19], v[48:51]\n"
    "ds_read_b128 v[40:43], v9 offset:47104\n"
    "ds_read_b128 v[44:47], v9 offset:48128\n"
    "v_mfma_f32_16x16x32_bf16 v[56:59], a[240:243], v[20:23], v[56:59]\n"
    "v_mfma_f32_16x16x32_bf16 v[52:55], a[244:247], v[16:19], v[52:55]\n"
    "buffer_load_dwordx4 v1, s[36:39], s76 offen offset:1024 sc1 lds\n"
    "s_add_u32 s76, s76, 8192\n"
    "v_mfma_f32_16x16x32_bf16 v[60:63], a[244:247], v[20:23], v[60:63]\n"
    "v_mfma_f32_16x16x32_bf16 v[64:67], a[248:251], v[16:19], v[64:67]\n"
    "v_mfma_f32_16x16x32_bf16 v[68:71], a[248:251], v[20:23], v[68:71]\n"
    "s_waitcnt lgkmcnt(4)\n"
    "v_mfma_f32_16x16x32_bf16 v[48:51], a[252:255], v[24:27], v[48:51]\n"
    "ds_read_b128 v[16:19], v10 offset:0\n"
    "ds_read_b128 v[20:23], v10 offset:1024\n"
    "s_add_u32 m0, s77, s59\n"
    "s_add_u32 s77, s77, 8192\n"
    "buffer_load_dwordx4 v1, s[36:39], s76 offen sc1 lds\n"
    "v_mfma_f32_16x16x32_bf16 v[56:59], a[252:255], v[28:31], v[56:59]\n"
    "v_mfma_f32_16x16x32_bf16 v[52:55], v[128:131], v[24:27], v[52:55]\n"
    "v_mfma_f32_16x16x32_bf16 v[60:63], v[128:131], v[28:31], v[60:63]\n"
    "v_mfma_f32_16x16x32_bf16 v[64:67], v[132:135], v[24:27], v[64:67]\n"
    "buffer_load_dwordx4 v1, s[36:39], s76 offen offset:1024 sc1 lds\n"
    "s_add_u32 s76, s76, 8192\n"
    "v_mfma_f32_16x16x32_bf16 v[68:71], v[132:135], v[28:31], v[68:71]\n"
    "s_waitcnt lgkmcnt(4)\n"
    "v_mfma_f32_16x16x32_bf16 v[48:51], v[136:139], v[32:35], v[48:51]\n"
    "ds_read_b128 v[24:27], v10 offset:2048\n"
    "ds_read_b128 v[28:31], v10 offset:3072\n"
    "v_mfma_f32_16x16x32_bf16 v[56:59], v[136:139], v[36:39], v[56:59]\n"
    "v_mfma_f32_16x16x32_bf16 v[52:55], v[140:143], v[32:35], v[52:55]\n"
    "s_add_u32 m0, s77, s59\n"
    "s_add_u32 s77, s77, 8192\n"
    "buffer_load_dwordx4 v1, s[36:39], s76 offen sc1 lds\n"
    "v_mfma_f32_16x16x32_bf16 v[60:63], v[140:143], v[36:39], v[60:63]\n"
    "v_mfma_f32_16x16x32_bf16 v[64:67], v[144:147], v[32:35], v[64:67]\n"
    "v_mfma_f32_16x16x32_bf16 v[68:71], v[144:147], v[36:39], v[68:71]\n"
    "s_waitcnt lgkmcnt(4)\n"
    "v_mfma_f32_16x16x32_bf16 v[48:51], v[148:151], v[40:43], v[48:51]\n"
    "ds_read_b128 v[32:35], v10 offset:4096\n"
    "ds_read_b128 v[36:39], v10 offset:5120\n"
    "buffer_load_dwordx4 v1, s[36:39], s76 offen offset:1024 sc1 lds\n"
    "s_add_u32 s76, s76, 8192\n"
    "v_mfma_f32_16x16x32_bf16 v[56:59], v[148:151], v[44:47], v[56:59]\n"
    "v_mfma_f32_16x16x32_bf16 v[52:55], v[152:155], v[40:43], v[52:55]\n"
    "v_mfma_f32_16x16x32_bf16 v[60:63], v[152:155], v[44:47], v[60:63]\n"
    "v_mfma_f32_16x16x32_bf16 v[64:67], v[156:159], v[40:43], v[64:67]\n"
    "s_add_u32 m0, s77, s59\n"
    "s_add_u32 s77, s77, 8192\n"
    "buffer_load_dwordx4 v1, s[36:39], s76 offen sc1 lds\n"
    "v_mfma_f32_16x16x32_bf16 v[68:71], v[156:159], v[44:47], v[68:71]\n"
    "s_waitcnt lgkmcnt(4)\n"
    "v_mfma_f32_16x16x32_bf16 v[48:51], v[160:163], v[16:19], v[48:51]\n"
    "ds_read_b128 v[40:43], v10 offset:6144\n"
    "ds_read_b128 v[44:47], v10 offset:7168\n"
    "v_mfma_f32_16x16x32_bf16 v[56:59], v[160:163], v[20:23], v[56:59]\n"
    "v_mfma_f32_16x16x32_bf16 v[52:55], v[164:167], v[16:19], v[52:55]\n"
    "buffer_load_dwordx4 v1, s[36:39], s76 offen offset:1024 sc1 lds\n"
    "s_add_u32 s76, s76, 8192\n"
    "v_mfma_f32_16x16x32_bf16 v[60:63], v[164:167], v[20:23], v[60:63]\n"
    "v_mfma_f32_16x16x32_bf16 v[64:67], v[168:171], v[16:19], v[64:67]\n"
    "v_mfma_f32_16x16x32_bf16 v[68:71], v[168:171], v[20:23], v[68:71]\n"
    "s_waitcnt lgkmcnt(4)\n"
    "v_mfma_f32_16x16x32_bf16 v[48:51], v[172:175], v[24:27], v[48:51]\n"
    "ds_read_b128 v[16:19], v10 offset:8192\n"
    "ds_read_b128 v[20:23], v10 offset:9216\n"
    "s_add_u32 m0, s77, s59\n"
    "s_add_u32 s77, s77, 8192\n"
    "buffer_load_dwordx4 v1, s[36:39], s76 offen sc1 lds\n"
    "v_mfma_f32_16x16x32_bf16 v[56:59], v[172:175], v[28:31], v[56:59]\n"
    "v_mfma_f32_16x16x32_bf16 v[52:55], v[176:179], v[24:27], v[52:55]\n"
    "v_mfma_f32_16x16x32_bf16 v[60:63], v[176:179], v[28:31], v[60:63]\n"
    "v_mfma_f32_16x16x32_bf16 v[64:67], v[180:183], v[24:27], v[64:67]\n"
    "buffer_load_dwordx4 v1, s[36:39], s76 offen offset:1024 sc1 lds\n"
    "s_add_u32 s76, s76, 8192\n"
    "v_mfma_f32_16x16x32_bf16 v[68:71], v[180:183], v[28:31], v[68:71]\n"
    "s_waitcnt lgkmcnt(4)\n"
    "v_mfma_f32_16x16x32_bf16 v[48:51], v[184:187], v[32:35], v[48:51]\n"
    "ds_read_b128 v[24:27], v10 offset:10240\n"
    "ds_read_b128 v[28:31], v10 offset:11264\n"
    "v_mfma_f32_16x16x32_bf16 v[56:59], v[184:187], v[36:39], v[56:59]\n"
    "v_mfma_f32_16x16x32_bf16 v[52:55], v[188:191], v[32:35], v[52:55]\n"
    "s_add_u32 m0, s77, s59\n"
    "s_add_u32 s77, s77, 8192\n"
    "buffer_load_dwordx4 v1, s[36:39], s76 offen sc1 lds\n"
    "v_mfma_f32_16x16x32_bf16 v[60:63], v[188:191], v[36:39], v[60:63]\n"
    "v_mfma_f32_16x16x32_bf16 v[64:67], v[192:195], v[32:35], v[64:67]\n"
    "v_mfma_f32_16x16x32_bf16 v[68:71], v[192:195], v[36:39], v[68:71]\n"
    "s_waitcnt lgkmcnt(4)\n"
    "v_mfma_f32_16x16x32_bf16 v[48:51], v[196:199], v[40:43], v[48:51]\n"
    "ds_read_b128 v[32:35], v10 offset:12288\n"
    "ds_read_b128 v[36:39], v10 offset:13312\n"
    "buffer_load_dwordx4 v1, s[36:39], s76 offen offset:1024 sc1 lds\n"
    "s_add_u32 s76, s76, 8192\n"
    "v_mfma_f32_16x16x32_bf16 v[56:59], v[196:199], v[44:47], v[56:59]\n"
    "v_mfma_f32_16x16x32_bf16 v[52:55], v[200:203], v[40:43], v[52:55]\n"
    "v_mfma_f32_16x16x32_bf16 v[60:63], v[200:203], v[44:47], v[60:63]\n"
    "v_mfma_f32_16x16x32_bf16 v[64:67], v[204:207], v[40:43], v[64:67]\n"
    "v_mfma_f32_16x16x32_bf16 v[68:71], v[204:207], v[44:47], v[68:71]\n"
    "s_branch .LgWO_seg1_%=\n"
    ".LgWO_lastp_%=:\n"
    "v_add_u32 v13, v11, v3\n"
    "v_cmp_gt_i32 vcc, s71, v13\n"
    "s_nop 0\n"
    "s_or_b64 exec, vcc, 1\n"
    "buffer_atomic_smax v11, v2, s[36:39], 0 offen sc0 sc1\n"
    "s_mov_b64 exec, -1\n"
    "s_branch .LgWO_nolast_%=\n"
    ".LgWO_idle_%=:\n"
    "s_waitcnt vmcnt(3)\n"
    "v_add_u32 v13, v11, v3\n"
    "v_cmp_gt_i32 vcc, s71, v13\n"
    "s_cbranch_vccnz .LgWO_idle2_%=\n"
    "s_and_b32 s76, s71, s54\n"
    "s_mul_i32 s76, s76, s53\n"
    "s_add_u32 s76, s76, s52\n"
    "s_and_b32 s77, s71, 1\n"
    "s_mul_i32 s77, s77, 65536\n"
    "s_add_u32 s77, s77, s59\n"
    "s_add_u32 m0, s77, s59\n"
    "s_add_u32 s77, s77, 8192\n"
    "buffer_load_dwordx4 v1, s[36:39], s76 offen sc1 lds\n"
    "buffer_load_dwordx4 v1, s[36:39], s76 offen offset:1024 sc1 lds\n"
    "s_add_u32 s76, s76, 8192\n"
    "s_add_u32 m0, s77, s59\n"
    "s_add_u32 s77, s77, 8192\n"
    "buffer_load_dwordx4 v1, s[36:39], s76 offen sc1 lds\n"
    "buffer_load_dwordx4 v1, s[36:39], s76 offen offset:1024 sc1 lds\n"
    "s_add_u32 s76, s76, 8192\n"
    "s_add_u32 m0, s77, s59\n"
    "s_add_u32 s77, s77, 8192\n"
    "buffer_load_dwordx4 v1, s[36:39], s76 offen sc1 lds\n"
    "buffer_load_dwordx4 v1, s[36:39], s76 offen offset:1024 sc1 lds\n"
    "s_add_u32 s76, s76, 8192\n"
    "s_add_u32 m0, s77, s59\n"
    "s_add_u32 s77, s77, 8192\n"
    "buffer_load_dwordx4 v1, s[36:39], s76 offen sc1 lds\n"
    "buffer_load_dwordx4 v1, s[36:39], s76 offen offset:1024 sc1 lds\n"
    "s_add_u32 s76, s76, 8192\n"
    "s_add_u32 m0, s77, s59\n"
    "s_add_u32 s77, s77, 8192\n"
    "buffer_load_dwordx4 v1, s[36:39], s76 offen sc1 lds\n"
    "buffer_load_dwordx4 v1, s[36:39], s76 offen offset:1024 sc1 lds\n"
    "s_add_u32 s76, s76, 8192\n"
    "s_add_u32 m0, s77, s59\n"
    "s_add_u32 s77, s77, 8192\n"
    "buffer_load_dwordx4 v1, s[36:39], s76 offen sc1 lds\n"
    "buffer_load_dwordx4 v1, s[36:39], s76 offen offset:1024 sc1 lds\n"
    "s_add_u32 s76, s76, 8192\n"
    "s_add_u32 m0, s77, s59\n"
    "s_add_u32 s77, s77, 8192\n"
    "buffer_load_dwordx4 v1, s[36:39], s76 offen sc1 lds\n"
    "buffer_load_dwordx4 v1, s[36:39], s76 offen offset:1024 sc1 lds\n"
    "s_add_u32 s76, s76, 8192\n"
    "s_add_u32 m0, s77, s59\n"
    "s_add_u32 s77, s77, 8192\n"
    "buffer_load_dwordx4 v1, s[36:39], s76 offen sc1 lds\n"
    "buffer_load_dwordx4 v1, s[36:39], s76 offen offset:1024 sc1 lds\n"
    "s_add_u32 s76, s76, 8192\n"
    "s_waitcnt vmcnt(16)\n"
    "s_sub_u32 s74, s68, 1\n"
    "v_mov_b32 v12, s74\n"
    "s_mov_b64 exec, s[62:63]\n"
    "buffer_store_dword v12, v14, s[36:39], 0 offen sc1\n"
    "s_mov_b64 exec, -1\n"
    "s_mov_b32 s69, 0\n"
    "s_waitcnt vmcnt(1)\n"
    "s_branch .LgWO_bar_%=\n"
    ".LgWO_idle2_%=:\n"
    "s_or_b64 exec, vcc, 1\n"
    "buffer_atomic_smax v11, v2, s[36:39], 0 offen sc0 sc1\n"
    "s_mov_b64 exec, -1\n"
    "s_waitcnt vmcnt(1)\n"
    "s_sub_u32 s74, s68, 1\n"
    "v_mov_b32 v12, s74\n"
    "s_mov_b64 exec, s[62:63]\n"
    "buffer_store_dword v12, v14, s[36:39], 0 offen sc1\n"
    "s_mov_b64 exec, -1\n"
    "s_waitcnt vmcnt(1)\n"
    "s_branch .LgWO_block1_%=\n"
    ".LgWO_block_%=:\n"
    "v_add_u32 v13, v11, v3\n"
    "v_cmp_gt_i32 vcc, s71, v13\n"
    "s_nop 0\n"
    "s_or_b64 exec, vcc, 1\n"
    "buffer_atomic_smax v11, v2, s[36:39], 0 offen sc0 sc1\n"
    "s_mov_b64 exec, -1\n"
    "s_waitcnt vmcnt(0)\n"
    ".LgWO_block1_%=:\n"
    "v_add_u32 v13, v11, v3\n"
    "v_cmp_gt_i32 vcc, s71, v13\n"
    "s_cbranch_vccz .LgWO_go_%=\n"
    "s_sleep 4\n"
    "s_sub_u32 s72, s72, 1\n"
    "s_cbranch_scc0 .LgWO_block_%=\n"
    "s_mov_b32 s72, 0\n"
    ".LgWO_go_%=:\n"
    "s_and_b32 s76, s71, s54\n"
    "s_mul_i32 s76, s76, s53\n"
    "s_add_u32 s76, s76, s52\n"
    "s_and_b32 s77, s71, 1\n"
    "s_mul_i32 s77, s77, 65536\n"
    "s_add_u32 s77, s77, s59\n"
    "s_add_u32 m0, s77, s59\n"
    "s_add_u32 s77, s77, 8192\n"
    "buffer_load_dwordx4 v1, s[36:39], s76 offen sc1 lds\n"
    "buffer_load_dwordx4 v1, s[36:39], s76 offen offset:1024 sc1 lds\n"
    "s_add_u32 s76, s76, 8192\n"
    "s_add_u32 m0, s77, s59\n"
    "s_add_u32 s77, s77, 8192\n"
    "buffer_load_dwordx4 v1, s[36:39], s76 offen sc1 lds\n"
    "buffer_load_dwordx4 v1, s[36:39], s76 offen offset:1024 sc1 lds\n"
    "s_add_u32 s76, s76, 8192\n"
    "s_add_u32 m0, s77, s59\n"
    "s_add_u32 s77, s77, 8192\n"
    "buffer_load_dwordx4 v1, s[36:39], s76 offen sc1 lds\n"
    "buffer_load_dwordx4 v1, s[36:39], s76 offen offset:1024 sc1 lds\n"
    "s_add_u32 s76, s76, 8192\n"
    "s_add_u32 m0, s77, s59\n"
    "s_add_u32 s77, s77, 8192\n"
    "buffer_load_dwordx4 v1, s[36:39], s76 offen sc1 lds\n"
    "buffer_load_dwordx4 v1, s[36:39], s76 offen offset:1024 sc1 lds\n"
    "s_add_u32 s76, s76, 8192\n"
    "s_add_u32 m0, s77, s59\n"
    "s_add_u32 s77, s77, 8192\n"
    "buffer_load_dwordx4 v1, s[36:39], s76 offen sc1 lds\n"
    "buffer_load_dwordx4 v1, s[36:39], s76 offen offset:1024 sc1 lds\n"
    "s_add_u32 s76, s76, 8192\n"
    "s_add_u32 m0, s77, s59\n"
    "s_add_u32 s77, s77, 8192\n"
    "buffer_load_dwordx4 v1, s[36:39], s76 offen sc1 lds\n"
    "buffer_load_dwordx4 v1, s[36:39], s76 offen offset:1024 sc1 lds\n"
    "s_add_u32 s76, s76, 8192\n"
    "s_add_u32 m0, s77, s59\n"
    "s_add_u32 s77, s77, 8192\n"
    "buffer_load_dwordx4 v1, s[36:39], s76 offen sc1 lds\n"
    "buffer_load_dwordx4 v1, s[36:39], s76 offen offset:1024 sc1 lds\n"
    "s_add_u32 s76, s76, 8192\n"
    "s_add_u32 m0, s77, s59\n"
    "s_add_u32 s77, s77, 8192\n"
    "buffer_load_dwordx4 v1, s[36:39], s76 offen sc1 lds\n"
    "buffer_load_dwordx4 v1, s[36:39], s76 offen offset:1024 sc1 lds\n"
    "s_add_u32 s76, s76, 8192\n"
    "s_mov_b32 s69, 0\n"
    "s_waitcnt vmcnt(0)\n"
    "s_branch .LgWO_bar_%=\n"
    ".LgWO_FIN_%=:\n"
  );
}
DEVI void role_down(const Params& p, int L, int j, int i, u32 dbg) {
  const int lane = threadIdx.x & 63, w = threadIdx.x >> 6, e0 = j * 1152 * 576 + i * 192;
  GEMM_DECL;
  g_w0 = make_srd(p.w2 + e0, 4608u * 576 * 2 - e0 * 2); g_w1 = g_w0; g_w2 = g_w0;
  g_ldw = 1152;
  g_in_base = OFF_L + L * LSZ + L_H + j * 73728; g_in_stride = 4 * 73728; g_in_mask = RH - 1;
  g_out_base = OFF_L + L * LSZ + L_PF + j * SEGB + i * 12288; g_out_stride = 4 * SEGB; g_out_mask = RP - 1;
  g_flag = lane < 4 ? mbox(L, lane, SL_REDF, 12 * j + 4 * i + w) : ((lane < 10 && w == 0) ? mbox(L, j, SL_UP + lane - 4, 16 + i) : OOB);
  g_paddr = mbox(L, j, SL_DOWN + i, lane);
  g_pbias = lane < 24 ? 0 : (lane < 40 ? RP : 1000);
  v16u sa, sb, va;
  gemm_args(GEMM_ARGS, p, dbg, sa, sb, va);
  RUN_ASM(
    "s_nop 4\n"
    "v_add_u32 v8, s59, v0\n"
    "s_add_u32 m0, s59, 0\n"
    "s_mov_b32 s74, 0\n"
    "buffer_load_dwordx4 v5, s[40:43], s74 offen lds\n"
    "s_add_u32 m0, s59, 4096\n"
    "s_mov_b32 s74, 128\n"
    "buffer_load_dwordx4 v5, s[40:43], s74 offen lds\n"
    "s_add_u32 m0, s59, 8192\n"
    "s_mov_b32 s74, 256\n"
    "buffer_load_dwordx4 v5, s[40:43], s74 offen lds\n"
    "s_add_u32 m0, s59, 12288\n"
    "s_mov_b32 s74, 36864\n"
    "buffer_load_dwordx4 v5, s[40:43], s74 offen lds\n"
    "s_add_u32 m0, s59, 16384\n"
    "s_mov_b32 s74, 36992\n"
    "buffer_load_dwordx4 v5, s[40:43], s74 offen lds\n"
    "s_add_u32 m0, s59, 20480\n"
    "s_mov_b32 s74, 37120\n"
    "buffer_load_dwordx4 v5, s[40:43], s74 offen lds\n"
    "s_add_u32 m0, s59, 24576\n"
    "s_mov_b32 s74, 73728\n"
    "buffer_load_dwordx4 v5, s[40:43], s74 offen lds\n"
    "s_add_u32 m0, s59, 28672\n"
    "s_mov_b32 s74, 73856\n"
    "buffer_load_dwordx4 v5, s[40:43], s74 offen lds\n"
    "s_add_u32 m0, s59, 32768\n"
    "s_mov_b32 s74, 73984\n"
    "buffer_load_dwordx4 v5, s[40:43], s74 offen lds\n"
    "s_add_u32 m0, s59, 36864\n"
    "s_mov_b32 s74, 110592\n"
    "buffer_load_dwordx4 v5, s[40:43], s74 offen lds\n"
    "s_add_u32 m0, s59, 40960\n"
    "s_mov_b32 s74, 110720\n"
    "buffer_load_dwordx4 v5, s[40:43], s74 offen lds\n"
    "s_add_u32 m0, s59, 45056\n"
    "s_mov_b32 s74, 110848\n"
    "buffer_load_dwordx4 v5, s[40:43], s74 offen lds\n"
    "s_add_u32 m0, s59, 49152\n"
    "s_mov_b32 s74, 147456\n"
    "buffer_load_dwordx4 v5, s[40:43], s74 offen lds\n"
    "s_add_u32 m0, s59, 53248\n"
    "s_mov_b32 s74, 147584\n"
    "buffer_load_dwordx4 v5, s[40:43], s74 offen lds\n"
    "s_add_u32 m0, s59, 57344\n"
    "s_mov_b32 s74, 147712\n"
    "buffer_load_dwordx4 v5, s[40:43], s74 offen lds\n"
    "s_add_u32 m0, s59, 61440\n"
    "s_mov_b32 s74, 184320\n"
    "buffer_load_dwordx4 v5, s[40:43], s74 offen lds\n"
    "s_add_u32 m0, s59, 65536\n"
    "s_mov_b32 s74, 184448\n"
    "buffer_load_dwordx4 v5, s[40:43], s74 offen lds\n"
    "s_add_u32 m0, s59, 69632\n"
    "s_mov_b32 s74, 184576\n"
    "buffer_load_dwordx4 v5, s[40:43], s74 offen lds\n"
    "s_add_u32 m0, s59, 73728\n"
    "s_mov_b32 s74, 221184\n"
    "buffer_load_dwordx4 v5, s[40:43], s74 offen lds\n"
    "s_add_u32 m0, s59, 77824\n"
    "s_mov_b32 s74, 221312\n"
    "buffer_load_dwordx4 v5, s[40:43], s74 offen lds\n"
    "s_add_u32 m0, s59, 81920\n"
    "s_mov_b32 s74, 221440\n"
    "buffer_load_dwordx4 v5, s[40:43], s74 offen lds\n"
    "s_add_u32 m0, s59, 86016\n"
    "s_mov_b32 s74, 258048\n"
    "buffer_load_dwordx4 v5, s[40:43], s74 offen lds\n"
    "s_add_u32 m0, s59, 90112\n"
    "s_mov_b32 s74, 258176\n"
    "buffer_load_dwordx4 v5, s[40:43], s74 offen lds\n"
    "s_add_u32 m0, s59, 94208\n"
    "s_mov_b32 s74, 258304\n"
    "buffer_load_dwordx4 v5, s[40:43], s74 offen lds\n"
    "s_add_u32 m0, s59, 98304\n"
    "s_mov_b32 s74, 294912\n"
    "buffer_load_dwordx4 v5, s[40:43], s74 offen lds\n"
    "s_add_u32 m0, s59, 102400\n"
    "s_mov_b32 s74, 295040\n"
    "buffer_load_dwordx4 v5, s[40:43], s74 offen lds\n"
    "s_add_u32 m0, s59, 106496\n"
    "s_mov_b32 s74, 295168\n"
    "buffer_load_dwordx4 v5, s[40:43], s74 offen lds\n"
    "s_add_u32 m0, s59, 110592\n"
    "s_mov_b32 s74, 331776\n"
    "buffer_load_dwordx4 v5, s[40:43], s74 offen lds\n"
    "s_add_u32 m0, s59, 114688\n"
    "s_mov_b32 s74, 331904\n"
    "buffer_load_dwordx4 v5, s[40:43], s74 offen lds\n"
    "s_add_u32 m0, s59, 118784\n"
    "s_mov_b32 s74, 332032\n"
    "buffer_load_dwordx4 v5, s[40:43], s74 offen lds\n"
    "v_add_u32 v16, 49152, v6\n"
    "v_add_u32 v17, 98304, v6\n"
    "v_add_u32 v18, 49152, v7\n"
    "v_add_u32 v19, 98304, v7\n"
    "s_waitcnt vmcnt(24) lgkmcnt(0)\n"
    "s_barrier\n"
    "s_add_u32 m0, s59, 122880\n"
    "s_mov_b32 s74, 368640\n"
    "buffer_load_dwordx4 v5, s[40:43], s74 offen lds\n"
    "s_add_u32 m0, s59, 126976\n"
    "s_mov_b32 s74, 368768\n"
    "buffer_load_dwordx4 v5, s[40:43], s74 offen lds\n"
    "s_add_u32 m0, s59, 131072\n"
    "s_mov_b32 s74, 368896\n"
    "buffer_load_dwordx4 v5, s[40:43], s74 offen lds\n"
    "s_add_u32 m0, s59, 135168\n"
    "s_mov_b32 s74, 405504\n"
    "buffer_load_dwordx4 v5, s[40:43], s74 offen lds\n"
    "s_add_u32 m0, s59, 139264\n"
    "s_mov_b32 s74, 405632\n"
    "buffer_load_dwordx4 v5, s[40:43], s74 offen lds\n"
    "s_add_u32 m0, s59, 143360\n"
    "s_mov_b32 s74, 405760\n"
    "buffer_load_dwordx4 v5, s[40:43], s74 offen lds\n"
    "ds_read_b64_tr_b16 a[0:1], v6 offset:0\n"
    "ds_read_b64_tr_b16 a[2:3], v6 offset:512\n"
    "ds_read_b64_tr_b16 a[4:5], v6 offset:8\n"
    "ds_read_b64_tr_b16 a[6:7], v6 offset:520\n"
    "ds_read_b64_tr_b16 a[8:9], v7 offset:0\n"
    "ds_read_b64_tr_b16 a[10:11], v7 offset:512\n"
    "ds_read_b64_tr_b16 a[12:13], v6 offset:12288\n"
    "ds_read_b64_tr_b16 a[14:15], v6 offset:12800\n"
    "ds_read_b64_tr_b16 a[16:17], v6 offset:12296\n"
    "ds_read_b64_tr_b16 a[18:19], v6 offset:12808\n"
    "ds_read_b64_tr_b16 a[20:21], v7 offset:12288\n"
    "ds_read_b64_tr_b16 a[22:23], v7 offset:12800\n"
    "s_waitcnt vmcnt(24) lgkmcnt(0)\n"
    "s_barrier\n"
    "s_add_u32 m0, s59, 0\n"
    "s_mov_b32 s74, 442368\n"
    "buffer_load_dwordx4 v5, s[40:43], s74 offen lds\n"
    "s_add_u32 m0, s59, 4096\n"
    "s_mov_b32 s74, 442496\n"
    "buffer_load_dwordx4 v5, s[40:43], s74 offen lds\n"
    "s_add_u32 m0, s59, 8192\n"
    "s_mov_b32 s74, 442624\n"
    "buffer_load_dwordx4 v5, s[40:43], s74 offen lds\n"
    "s_add_u32 m0, s59, 12288\n"
    "s_mov_b32 s74, 479232\n"
    "buffer_load_dwordx4 v5, s[40:43], s74 offen lds\n"
    "s_add_u32 m0, s59, 16384\n"
    "s_mov_b32 s74, 479360\n"
    "buffer_load_dwordx4 v5, s[40:43], s74 offen lds\n"
    "s_add_u32 m0, s59, 20480\n"
    "s_mov_b32 s74, 479488\n"
    "buffer_load_dwordx4 v5, s[40:43], s74 offen lds\n"
    "ds_read_b64_tr_b16 a[24:25], v6 offset:24576\n"
    "ds_read_b64_tr_b16 a[26:27], v6 offset:25088\n"
    "ds_read_b64_tr_b16 a[28:29], v6 offset:24584\n"
    "ds_read_b64_tr_b16 a[30:31], v6 offset:25096\n"
    "ds_read_b64_tr_b16 a[32:33], v7 offset:24576\n"
    "ds_read_b64_tr_b16 a[34:35], v7 offset:25088\n"
    "ds_read_b64_tr_b16 a[36:37], v6 offset:36864\n"
    "ds_read_b64_tr_b16 a[38:39], v6 offset:37376\n"
    "ds_read_b64_tr_b16 a[40:41], v6 offset:36872\n"
    "ds_read_b64_tr_b16 a[42:43], v6 offset:37384\n"
    "ds_read_b64_tr_b16 a[44:45], v7 offset:36864\n"
    "ds_read_b64_tr_b16 a[46:47], v7 offset:37376\n"
    "s_waitcnt vmcnt(24) lgkmcnt(0)\n"
    "s_barrier\n"
    "s_add_u32 m0, s59, 24576\n"
    "s_mov_b32 s74, 516096\n"
    "buffer_load_dwordx4 v5, s[40:43], s74 offen lds\n"
    "s_add_u32 m0, s59, 28672\n"
    "s_mov_b32 s74, 516224\n"
    "buffer_load_dwordx4 v5, s[40:43], s74 offen lds\n"
    "s_add_u32 m0, s59, 32768\n"
    "s_mov_b32 s74, 516352\n"
    "buffer_load_dwordx4 v5, s[40:43], s74 offen lds\n"
    "s_add_u32 m0, s59, 36864\n"
    "s_mov_b32 s74, 552960\n"
    "buffer_load_dwordx4 v5, s[40:43], s74 offen lds\n"
    "s_add_u32 m0, s59, 40960\n"
    "s_mov_b32 s74, 553088\n"
    "buffer_load_dwordx4 v5, s[40:43], s74 offen lds\n"
    "s_add_u32 m0, s59, 45056\n"
    "s_mov_b32 s74, 553216\n"
    "buffer_load_dwordx4 v5, s[40:43], s74 offen lds\n"
    "ds_read_b64_tr_b16 a[48:49], v16 offset:0\n"
    "ds_read_b64_tr_b16 a[50:51], v16 offset:512\n"
    "ds_read_b64_tr_b16 a[52:53], v16 offset:8\n"
    "ds_read_b64_tr_b16 a[54:55], v16 offset:520\n"
    "ds_read_b64_tr_b16 a[56:57], v18 offset:0\n"
    "ds_read_b64_tr_b16 a[58:59], v18 offset:512\n"
    "ds_read_b64_tr_b16 a[60:61], v16 offset:12288\n"
    "ds_read_b64_tr_b16 a[62:63], v16 offset:12800\n"
    "ds_read_b64_tr_b16 a[64:65], v16 offset:12296\n"
    "ds_read_b64_tr_b16 a[66:67], v16 offset:12808\n"
    "ds_read_b64_tr_b16 a[68:69], v18 offset:12288\n"
    "ds_read_b64_tr_b16 a[70:71], v18 offset:12800\n"
    "s_waitcnt vmcnt(24) lgkmcnt(0)\n"
    "s_barrier\n"
    "s_add_u32 m0, s59, 49152\n"
    "s_mov_b32 s74, 589824\n"
    "buffer_load_dwordx4 v5, s[40:43], s74 offen lds\n"
    "s_add_u32 m0, s59, 53248\n"
    "s_mov_b32 s74, 589952\n"
    "buffer_load_dwordx4 v5, s[40:43], s74 offen lds\n"
    "s_add_u32 m0, s59, 57344\n"
    "s_mov_b32 s74, 590080\n"
    "buffer_load_dwordx4 v5, s[40:43], s74 offen lds\n"
    "s_add_u32 m0, s59, 61440\n"
    "s_mov_b32 s74, 626688\n"
    "buffer_load_dwordx4 v5, s[40:43], s74 offen lds\n"
    "s_add_u32 m0, s59, 65536\n"
    "s_mov_b32 s74, 626816\n"
    "buffer_load_dwordx4 v5, s[40:43], s74 offen lds\n"
    "s_add_u32 m0, s59, 69632\n"
    "s_mov_b32 s74, 626944\n"
    "buffer_load_dwordx4 v5, s[40:43], s74 offen lds\n"
    "ds_read_b64_tr_b16 a[72:73], v16 offset:24576\n"
    "ds_read_b64_tr_b16 a[74:75], v16 offset:25088\n"
    "ds_read_b64_tr_b16 a[76:77], v16 offset:24584\n"
    "ds_read_b64_tr_b16 a[78:79], v16 offset:25096\n"
    "ds_read_b64_tr_b16 a[80:81], v18 offset:24576\n"
    "ds_read_b64_tr_b16 a[82:83], v18 offset:25088\n"
    "ds_read_b64_tr_b16 a[84:85], v16 offset:36864\n"
    "ds_read_b64_tr_b16 a[86:87], v16 offset:37376\n"
    "ds_read_b64_tr_b16 a[88:89], v16 offset:36872\n"
    "ds_read_b64_tr_b16 a[90:91], v16 offset:37384\n"
    "ds_read_b64_tr_b16 a[92:93], v18 offset:36864\n"
    "ds_read_b64_tr_b16 a[94:95], v18 offset:37376\n"
    "s_waitcnt vmcnt(24) lgkmcnt(0)\n"
    "s_barrier\n"
    "s_add_u32 m0, s59, 73728\n"
    "s_mov_b32 s74, 663552\n"
    "buffer_load_dwordx4 v5, s[40:43], s74 offen lds\n"
    "s_add_u32 m0, s59, 77824\n"
    "s_mov_b32 s74, 663680\n"
    "buffer_load_dwordx4 v5, s[40:43], s74 offen lds\n"
    "s_add_u32 m0, s59, 81920\n"
    "s_mov_b32 s74, 663808\n"
    "buffer_load_dwordx4 v5, s[40:43], s74 offen lds\n"
    "s_add_u32 m0, s59, 86016\n"
    "s_mov_b32 s74, 700416\n"
    "buffer_load_dwordx4 v5, s[40:43], s74 offen lds\n"
    "s_add_u32 m0, s59, 90112\n"
    "s_mov_b32 s74, 700544\n"
    "buffer_load_dwordx4 v5, s[40:43], s74 offen lds\n"
    "s_add_u32 m0, s59, 94208\n"
    "s_mov_b32 s74, 700672\n"
    "buffer_load_dwordx4 v5, s[40:43], s74 offen lds\n"
    "ds_read_b64_tr_b16 a[96:97], v17 offset:0\n"
    "ds_read_b64_tr_b16 a[98:99], v17 offset:512\n"
    "ds_read_b64_tr_b16 a[100:101], v17 offset:8\n"
    "ds_read_b64_tr_b16 a[102:103], v17 offset:520\n"
    "ds_read_b64_tr_b16 a[104:105], v19 offset:0\n"
    "ds_read_b64_tr_b16 a[106:107], v19 offset:512\n"
    "ds_read_b64_tr_b16 a[108:109], v17 offset:12288\n"
    "ds_read_b64_tr_b16 a[110:111], v17 offset:12800\n"
    "ds_read_b64_tr_b16 a[112:113], v17 offset:12296\n"
    "ds_read_b64_tr_b16 a[114:115], v17 offset:12808\n"
    "ds_read_b64_tr_b16 a[116:117], v19 offset:12288\n"
    "ds_read_b64_tr_b16 a[118:119], v19 offset:12800\n"
    "s_waitcnt vmcnt(24) lgkmcnt(0)\n"
    "s_barrier\n"
    "s_add_u32 m0, s59, 98304\n"
    "s_mov_b32 s74, 737280\n"
    "buffer_load_dwordx4 v5, s[40:43], s74 offen lds\n"
    "s_add_u32 m0, s59, 102400\n"
    "s_mov_b32 s74, 737408\n"
    "buffer_load_dwordx4 v5, s[40:43], s74 offen lds\n"
    "s_add_u32 m0, s59, 106496\n"
    "s_mov_b32 s74, 737536\n"
    "buffer_load_dwordx4 v5, s[40:43], s74 offen lds\n"
    "s_add_u32 m0, s59, 110592\n"
    "s_mov_b32 s74, 774144\n"
    "buffer_load_dwordx4 v5, s[40:43], s74 offen lds\n"
    "s_add_u32 m0, s59, 114688\n"
    "s_mov_b32 s74, 774272\n"
    "buffer_load_dwordx4 v5, s[40:43], s74 offen lds\n"
    "s_add_u32 m0, s59, 118784\n"
    "s_mov_b32 s74, 774400\n"
    "buffer_load_dwordx4 v5, s[40:43], s74 offen lds\n"
    "ds_read_b64_tr_b16 a[120:121], v17 offset:24576\n"
    "ds_read_b64_tr_b16 a[122:123], v17 offset:25088\n"
    "ds_read_b64_tr_b16 a[124:125], v17 offset:24584\n"
    "ds_read_b64_tr_b16 a[126:127], v17 offset:25096\n"
    "ds_read_b64_tr_b16 a[128:129], v19 offset:24576\n"
    "ds_read_b64_tr_b16 a[130:131], v19 offset:25088\n"
    "ds_read_b64_tr_b16 a[132:133], v17 offset:36864\n"
    "ds_read_b64_tr_b16 a[134:135], v17 offset:37376\n"
    "ds_read_b64_tr_b16 a[136:137], v17 offset:36872\n"
    "ds_read_b64_tr_b16 a[138:139], v17 offset:37384\n"
    "ds_read_b64_tr_b16 a[140:141], v19 offset:36864\n"
    "ds_read_b64_tr_b16 a[142:143], v19 offset:37376\n"
    "s_waitcnt vmcnt(24) lgkmcnt(0)\n"
    "s_barrier\n"
    "s_add_u32 m0, s59, 122880\n"
    "s_mov_b32 s74, 811008\n"
    "buffer_load_dwordx4 v5, s[40:43], s74 offen lds\n"
    "s_add_u32 m0, s59, 126976\n"
    "s_mov_b32 s74, 811136\n"
    "buffer_load_dwordx4 v5, s[40:43], s74 offen lds\n"
    "s_add_u32 m0, s59, 131072\n"
    "s_mov_b32 s74, 811264\n"
    "buffer_load_dwordx4 v5, s[40:43], s74 offen lds\n"
    "s_add_u32 m0, s59, 135168\n"
    "s_mov_b32 s74, 847872\n"
    "buffer_load_dwordx4 v5, s[40:43], s74 offen lds\n"
    "s_add_u32 m0, s59, 139264\n"
    "s_mov_b32 s74, 848000\n"
    "buffer_load_dwordx4 v5, s[40:43], s74 offen lds\n"
    "s_add_u32 m0, s59, 143360\n"
    "s_mov_b32 s74, 848128\n"
    "buffer_load_dwordx4 v5, s[40:43], s74 offen lds\n"
    "ds_read_b64_tr_b16 a[144:145], v6 offset:0\n"
    "ds_read_b64_tr_b16 a[146:147], v6 offset:512\n"
    "ds_read_b64_tr_b16 a[148:149], v6 offset:8\n"
    "ds_read_b64_tr_b16 a[150:151], v6 offset:520\n"
    "ds_read_b64_tr_b16 a[152:153], v7 offset:0\n"
    "ds_read_b64_tr_b16 a[154:155], v7 offset:512\n"
    "ds_read_b64_tr_b16 a[156:157], v6 offset:12288\n"
    "ds_read_b64_tr_b16 a[158:159], v6 offset:12800\n"
    "ds_read_b64_tr_b16 a[160:161], v6 offset:12296\n"
    "ds_read_b64_tr_b16 a[162:163], v6 offset:12808\n"
    "ds_read_b64_tr_b16 a[164:165], v7 offset:12288\n"
    "ds_read_b64_tr_b16 a[166:167], v7 offset:12800\n"
    "s_waitcnt vmcnt(24) lgkmcnt(0)\n"
    "s_barrier\n"
    "s_add_u32 m0, s59, 0\n"
    "s_mov_b32 s74, 884736\n"
    "buffer_load_dwordx4 v5, s[40:43], s74 offen lds\n"
    "s_add_u32 m0, s59, 4096\n"
    "s_mov_b32 s74, 884864\n"
    "buffer_load_dwordx4 v5, s[40:43], s74 offen lds\n"
    "s_add_u32 m0, s59, 8192\n"
    "s_mov_b32 s74, 884992\n"
    "buffer_load_dwordx4 v5, s[40:43], s74 offen lds\n"
    "s_add_u32 m0, s59, 12288\n"
    "s_mov_b32 s74, 921600\n"
    "buffer_load_dwordx4 v5, s[40:43], s74 offen lds\n"
    "s_add_u32 m0, s59, 16384\n"
    "s_mov_b32 s74, 921728\n"
    "buffer_load_dwordx4 v5, s[40:43], s74 offen lds\n"
    "s_add_u32 m0, s59, 20480\n"
    "s_mov_b32 s74, 921856\n"
    "buffer_load_dwordx4 v5, s[40:43], s74 offen lds\n"
    "ds_read_b64_tr_b16 a[168:169], v6 offset:24576\n"
    "ds_read_b64_tr_b16 a[170:171], v6 offset:25088\n"
    "ds_read_b64_tr_b16 a[172:173], v6 offset:24584\n"
    "ds_read_b64_tr_b16 a[174:175], v6 offset:25096\n"
    "ds_read_b64_tr_b16 a[176:177], v7 offset:24576\n"
    "ds_read_b64_tr_b16 a[178:179], v7 offset:25088\n"
    "ds_read_b64_tr_b16 a[180:181], v6 offset:36864\n"
    "ds_read_b64_tr_b16 a[182:183], v6 offset:37376\n"
    "ds_read_b64_tr_b16 a[184:185], v6 offset:36872\n"
    "ds_read_b64_tr_b16 a[186:187], v6 offset:37384\n"
    "ds_read_b64_tr_b16 a[188:189], v7 offset:36864\n"
    "ds_read_b64_tr_b16 a[190:191], v7 offset:37376\n"
    "s_waitcnt vmcnt(24) lgkmcnt(0)\n"
    "s_barrier\n"
    "s_add_u32 m0, s59, 24576\n"
    "s_mov_b32 s74, 958464\n"
    "buffer_load_dwordx4 v5, s[40:43], s74 offen lds\n"
    "s_add_u32 m0, s59, 28672\n"
    "s_mov_b32 s74, 958592\n"
    "buffer_load_dwordx4 v5, s[40:43], s74 offen lds\n"
    "s_add_u32 m0, s59, 32768\n"
    "s_mov_b32 s74, 958720\n"
    "buffer_load_dwordx4 v5, s[40:43], s74 offen lds\n"
    "s_add_u32 m0, s59, 36864\n"
    "s_mov_b32 s74, 995328\n"
    "buffer_load_dwordx4 v5, s[40:43], s74 offen lds\n"
    "s_add_u32 m0, s59, 40960\n"
    "s_mov_b32 s74, 995456\n"
    "buffer_load_dwordx4 v5, s[40:43], s74 offen lds\n"
    "s_add_u32 m0, s59, 45056\n"
    "s_mov_b32 s74, 995584\n"
    "buffer_load_dwordx4 v5, s[40:43], s74 offen lds\n"
    "ds_read_b64_tr_b16 a[192:193], v16 offset:0\n"
    "ds_read_b64_tr_b16 a[194:195], v16 offset:512\n"
    "ds_read_b64_tr_b16 a[196:197], v16 offset:8\n"
    "ds_read_b64_tr_b16 a[198:199], v16 offset:520\n"
    "ds_read_b64_tr_b16 a[200:201], v18 offset:0\n"
    "ds_read_b64_tr_b16 a[202:203], v18 offset:512\n"
    "ds_read_b64_tr_b16 a[204:205], v16 offset:12288\n"
    "ds_read_b64_tr_b16 a[206:207], v16 offset:12800\n"
    "ds_read_b64_tr_b16 a[208:209], v16 offset:12296\n"
    "ds_read_b64_tr_b16 a[210:211], v16 offset:12808\n"
    "ds_read_b64_tr_b16 a[212:213], v18 offset:12288\n"
    "ds_read_b64_tr_b16 a[214:215], v18 offset:12800\n"
    "s_waitcnt vmcnt(24) lgkmcnt(0)\n"
    "s_barrier\n"
    "s_add_u32 m0, s59, 49152\n"
    "s_mov_b32 s74, 1032192\n"
    "buffer_load_dwordx4 v5, s[40:43], s74 offen lds\n"
    "s_add_u32 m0, s59, 53248\n"
    "s_mov_b32 s74, 1032320\n"
    "buffer_load_dwordx4 v5, s[40:43], s74 offen lds\n"
    "s_add_u32 m0, s59, 57344\n"
    "s_mov_b32 s74, 1032448\n"
    "buffer_load_dwordx4 v5, s[40:43], s74 offen lds\n"
    "s_add_u32 m0, s59, 61440\n"
    "s_mov_b32 s74, 1069056\n"
    "buffer_load_dwordx4 v5, s[40:43], s74 offen lds\n"
    "s_add_u32 m0, s59, 65536\n"
    "s_mov_b32 s74, 1069184\n"
    "buffer_load_dwordx4 v5, s[40:43], s74 offen lds\n"
    "s_add_u32 m0, s59, 69632\n"
    "s_mov_b32 s74, 1069312\n"
    "buffer_load_dwordx4 v5, s[40:43], s74 offen lds\n"
    "ds_read_b64_tr_b16 a[216:217], v16 offset:24576\n"
    "ds_read_b64_tr_b16 a[218:219], v16 offset:25088\n"
    "ds_read_b64_tr_b16 a[220:221], v16 offset:24584\n"
    "ds_read_b64_tr_b16 a[222:223], v16 offset:25096\n"
    "ds_read_b64_tr_b16 a[224:225], v18 offset:24576\n"
    "ds_read_b64_tr_b16 a[226:227], v18 offset:25088\n"
    "ds_read_b64_tr_b16 a[228:229], v16 offset:36864\n"
    "ds_read_b64_tr_b16 a[230:231], v16 offset:37376\n"
    "ds_read_b64_tr_b16 a[232:233], v16 offset:36872\n"
    "ds_read_b64_tr_b16 a[234:235], v16 offset:37384\n"
    "ds_read_b64_tr_b16 a[236:237], v18 offset:36864\n"
    "ds_read_b64_tr_b16 a[238:239], v18 offset:37376\n"
    "s_waitcnt vmcnt(24) lgkmcnt(0)\n"
    "s_barrier\n"
    "s_add_u32 m0, s59, 73728\n"
    "s_mov_b32 s74, 1105920\n"
    "buffer_load_dwordx4 v5, s[40:43], s74 offen lds\n"
    "s_add_u32 m0, s59, 77824\n"
    "s_mov_b32 s74, 1106048\n"
    "buffer_load_dwordx4 v5, s[40:43], s74 offen lds\n"
    "s_add_u32 m0, s59, 81920\n"
    "s_mov_b32 s74, 1106176\n"
    "buffer_load_dwordx4 v5, s[40:43], s74 offen lds\n"
    "s_add_u32 m0, s59, 86016\n"
    "s_mov_b32 s74, 1142784\n"
    "buffer_load_dwordx4 v5, s[40:43], s74 offen lds\n"
    "s_add_u32 m0, s59, 90112\n"
    "s_mov_b32 s74, 1142912\n"
    "buffer_load_dwordx4 v5, s[40:43], s74 offen lds\n"
    "s_add_u32 m0, s59, 94208\n"
    "s_mov_b32 s74, 1143040\n"
    "buffer_load_dwordx4 v5, s[40:43], s74 offen lds\n"
    "ds_read_b64_tr_b16 a[240:241], v17 offset:0\n"
    "ds_read_b64_tr_b16 a[242:243], v17 offset:512\n"
    "ds_read_b64_tr_b16 a[244:245], v17 offset:8\n"
    "ds_read_b64_tr_b16 a[246:247], v17 offset:520\n"
    "ds_read_b64_tr_b16 a[248:249], v19 offset:0\n"
    "ds_read_b64_tr_b16 a[250:251], v19 offset:512\n"
    "ds_read_b64_tr_b16 a[252:253], v17 offset:12288\n"
    "ds_read_b64_tr_b16 a[254:255], v17 offset:12800\n"
    "ds_read_b64_tr_b16 v[80:81], v17 offset:12296\n"
    "ds_read_b64_tr_b16 v[82:83], v17 offset:12808\n"
    "ds_read_b64_tr_b16 v[84:85], v19 offset:12288\n"
    "ds_read_b64_tr_b16 v[86:87], v19 offset:12800\n"
    "s_waitcnt vmcnt(24) lgkmcnt(0)\n"
    "s_barrier\n"
    "s_add_u32 m0, s59, 98304\n"
    "s_mov_b32 s74, 1179648\n"
    "buffer_load_dwordx4 v5, s[40:43], s74 offen lds\n"
    "s_add_u32 m0, s59, 102400\n"
    "s_mov_b32 s74, 1179776\n"
    "buffer_load_dwordx4 v5, s[40:43], s74 offen lds\n"
    "s_add_u32 m0, s59, 106496\n"
    "s_mov_b32 s74, 1179904\n"
    "buffer_load_dwordx4 v5, s[40:43], s74 offen lds\n"
    "s_add_u32 m0, s59, 110592\n"
    "s_mov_b32 s74, 1216512\n"
    "buffer_load_dwordx4 v5, s[40:43], s74 offen lds\n"
    "s_add_u32 m0, s59, 114688\n"
    "s_mov_b32 s74, 1216640\n"
    "buffer_load_dwordx4 v5, s[40:43], s74 offen lds\n"
    "s_add_u32 m0, s59, 118784\n"
    "s_mov_b32 s74, 1216768\n"
    "buffer_load_dwordx4 v5, s[40:43], s74 offen lds\n"
    "ds_read_b64_tr_b16 v[88:89], v17 offset:24576\n"
    "ds_read_b64_tr_b16 v[90:91], v17 offset:25088\n"
    "ds_read_b64_tr_b16 v[92:93], v17 offset:24584\n"
    "ds_read_b64_tr_b16 v[94:95], v17 offset:25096\n"
    "ds_read_b64_tr_b16 v[96:97], v19 offset:24576\n"
    "ds_read_b64_tr_b16 v[98:99], v19 offset:25088\n"
    "ds_read_b64_tr_b16 v[100:101], v17 offset:36864\n"
    "ds_read_b64_tr_b16 v[102:103], v17 offset:37376\n"
    "ds_read_b64_tr_b16 v[104:105], v17 offset:36872\n"
    "ds_read_b64_tr_b16 v[106:107], v17 offset:37384\n"
    "ds_read_b64_tr_b16 v[108:109], v19 offset:36864\n"
    "ds_read_b64_tr_b16 v[110:111], v19 offset:37376\n"
    "s_waitcnt vmcnt(24) lgkmcnt(0)\n"
    "s_barrier\n"
    "s_add_u32 m0, s59, 122880\n"
    "s_mov_b32 s74, 1253376\n"
    "buffer_load_dwordx4 v5, s[40:43], s74 offen lds\n"
    "s_add_u32 m0, s59, 126976\n"
    "s_mov_b32 s74, 1253504\n"
    "buffer_load_dwordx4 v5, s[40:43], s74 offen lds\n"
    "s_add_u32 m0, s59, 131072\n"
    "s_mov_b32 s74, 1253632\n"
    "buffer_load_dwordx4 v5, s[40:43], s74 offen lds\n"
    "s_add_u32 m0, s59, 135168\n"
    "s_mov_b32 s74, 1290240\n"
    "buffer_load_dwordx4 v5, s[40:43], s74 offen lds\n"
    "s_add_u32 m0, s59, 139264\n"
    "s_mov_b32 s74, 1290368\n"
    "buffer_load_dwordx4 v5, s[40:43], s74 offen lds\n"
    "s_add_u32 m0, s59, 143360\n"
    "s_mov_b32 s74, 1290496\n"
    "buffer_load_dwordx4 v5, s[40:43], s74 offen lds\n"
    "ds_read_b64_tr_b16 v[112:113], v6 offset:0\n"
    "ds_read_b64_tr_b16 v[114:115], v6 offset:512\n"
    "ds_read_b64_tr_b16 v[116:117], v6 offset:8\n"
    "ds_read_b64_tr_b16 v[118:119], v6 offset:520\n"
    "ds_read_b64_tr_b16 v[120:121], v7 offset:0\n"
    "ds_read_b64_tr_b16 v[122:123], v7 offset:512\n"
    "ds_read_b64_tr_b16 v[124:125], v6 offset:12288\n"
    "ds_read_b64_tr_b16 v[126:127], v6 offset:12800\n"
    "ds_read_b64_tr_b16 v[128:129], v6 offset:12296\n"
    "ds_read_b64_tr_b16 v[130:131], v6 offset:12808\n"
    "ds_read_b64_tr_b16 v[132:133], v7 offset:12288\n"
    "ds_read_b64_tr_b16 v[134:135], v7 offset:12800\n"
    "s_waitcnt vmcnt(24) lgkmcnt(0)\n"
    "s_barrier\n"
    "ds_read_b64_tr_b16 v[136:137], v6 offset:24576\n"
    "ds_read_b64_tr_b16 v[138:139], v6 offset:25088\n"
    "ds_read_b64_tr_b16 v[140:141], v6 offset:24584\n"
    "ds_read_b64_tr_b16 v[142:143], v6 offset:25096\n"
    "ds_read_b64_tr_b16 v[144:145], v7 offset:24576\n"
    "ds_read_b64_tr_b16 v[146:147], v7 offset:25088\n"
    "ds_read_b64_tr_b16 v[148:149], v6 offset:36864\n"
    "ds_read_b64_tr_b16 v[150:151], v6 offset:37376\n"
    "ds_read_b64_tr_b16 v[152:153], v6 offset:36872\n"
    "ds_read_b64_tr_b16 v[154:155], v6 offset:37384\n"
    "ds_read_b64_tr_b16 v[156:157], v7 offset:36864\n"
    "ds_read_b64_tr_b16 v[158:159], v7 offset:37376\n"
    "s_waitcnt vmcnt(18) lgkmcnt(0)\n"
    "s_barrier\n"
    "ds_read_b64_tr_b16 v[160:161], v16 offset:0\n"
    "ds_read_b64_tr_b16 v[162:163], v16 offset:512\n"
    "ds_read_b64_tr_b16 v[164:165], v16 offset:8\n"
    "ds_read_b64_tr_b16 v[166:167], v16 offset:520\n"
    "ds_read_b64_tr_b16 v[168:169], v18 offset:0\n"
    "ds_read_b64_tr_b16 v[170:171], v18 offset:512\n"
    "ds_read_b64_tr_b16 v[172:173], v16 offset:12288\n"
    "ds_read_b64_tr_b16 v[174:175], v16 offset:12800\n"
    "ds_read_b64_tr_b16 v[176:177], v16 offset:12296\n"
    "ds_read_b64_tr_b16 v[178:179], v16 offset:12808\n"
    "ds_read_b64_tr_b16 v[180:181], v18 offset:12288\n"
    "ds_read_b64_tr_b16 v[182:183], v18 offset:12800\n"
    "s_waitcnt vmcnt(12) lgkmcnt(0)\n"
    "s_barrier\n"
    "ds_read_b64_tr_b16 v[184:185], v16 offset:24576\n"
    "ds_read_b64_tr_b16 v[186:187], v16 offset:25088\n"
    "ds_read_b64_tr_b16 v[188:189], v16 offset:24584\n"
    "ds_read_b64_tr_b16 v[190:191], v16 offset:25096\n"
    "ds_read_b64_tr_b16 v[192:193], v18 offset:24576\n"
    "ds_read_b64_tr_b16 v[194:195], v18 offset:25088\n"
    "ds_read_b64_tr_b16 v[196:197], v16 offset:36864\n"
    "ds_read_b64_tr_b16 v[198:199], v16 offset:37376\n"
    "ds_read_b64_tr_b16 v[200:201], v16 offset:36872\n"
    "ds_read_b64_tr_b16 v[202:203], v16 offset:37384\n"
    "ds_read_b64_tr_b16 v[204:205], v18 offset:36864\n"
    "ds_read_b64_tr_b16 v[206:207], v18 offset:37376\n"
    "s_waitcnt vmcnt(6) lgkmcnt(0)\n"
    "s_barrier\n"
    "ds_read_b64_tr_b16 v[208:209], v17 offset:0\n"
    "ds_read_b64_tr_b16 v[210:211], v17 offset:512\n"
    "ds_read_b64_tr_b16 v[212:213], v17 offset:8\n"
    "ds_read_b64_tr_b16 v[214:215], v17 offset:520\n"
    "ds_read_b64_tr_b16 v[216:217], v19 offset:0\n"
    "ds_read_b64_tr_b16 v[218:219], v19 offset:512\n"
    "ds_read_b64_tr_b16 v[220:221], v17 offset:12288\n"
    "ds_read_b64_tr_b16 v[222:223], v17 offset:12800\n"
    "ds_read_b64_tr_b16 v[224:225], v17 offset:12296\n"
    "ds_read_b64_tr_b16 v[226:227], v17 offset:12808\n"
    "ds_read_b64_tr_b16 v[228:229], v19 offset:12288\n"
    "ds_read_b64_tr_b16 v[230:231], v19 offset:12800\n"
    "s_waitcnt vmcnt(0) lgkmcnt(0)\n"
    "s_barrier\n"
    "ds_read_b64_tr_b16 v[232:233], v17 offset:24576\n"
    "ds_read_b64_tr_b16 v[234:235], v17 offset:25088\n"
    "ds_read_b64_tr_b16 v[236:237], v17 offset:24584\n"
    "ds_read_b64_tr_b16 v[238:239], v17 offset:25096\n"
    "ds_read_b64_tr_b16 v[240:241], v19 offset:24576\n"
    "ds_read_b64_tr_b16 v[242:243], v19 offset:25088\n"
    "ds_read_b64_tr_b16 v[244:245], v17 offset:36864\n"
    "ds_read_b64_tr_b16 v[246:247], v17 offset:37376\n"
    "ds_read_b64_tr_b16 v[248:249], v17 offset:36872\n"
    "ds_read_b64_tr_b16 v[250:251], v17 offset:37384\n"
    "ds_read_b64_tr_b16 v[252:253], v19 offset:36864\n"
    "ds_read_b64_tr_b16 v[254:255], v19 offset:37376\n"
    "s_waitcnt lgkmcnt(0)\n"
    "s_barrier\n"
    "s_mov_b32 s68, 0\n"
    "s_mov_b32 s71, 0\n"
    "v_mov_b32 v11, -1\n"
    "s_mov_b32 s72, 262144\n"
    "s_branch .LgDOWN_block_%=\n"
    ".LgDOWN_LOOPB_%=:\n"
    ".LgDOWN_top_%=:\n"
    "s_waitcnt vmcnt(3)\n"
    ".LgDOWN_bar_%=:\n"
    "s_and_b32 s78, s68, 1\n"
    "s_mul_i32 s78, s78, 73728\n"
    "v_add_u32 v9, s78, v0\n"
    "v_add_u32 v10, 49152, v9\n"
    "s_add_u32 s71, s68, 1\n"
    "s_cmp_eq_u32 s71, 64\n"
    "s_cselect_b32 s70, 1, 0\n"
    "s_barrier\n"
    "ds_read_b128 v[16:19], v9 offset:0\n"
    "ds_read_b128 v[20:23], v9 offset:1024\n"
    "ds_read_b128 v[24:27], v9 offset:2048\n"
    "ds_read_b128 v[28:31], v9 offset:3072\n"
    "ds_read_b128 v[32:35], v9 offset:4096\n"
    "ds_read_b128 v[36:39], v9 offset:5120\n"
    "v_add_u32 v13, v11, v3\n"
    "v_cmp_gt_i32 vcc, s71, v13\n"
    "s_nop 0\n"
    "s_or_b64 exec, vcc, 1\n"
    "buffer_atomic_smax v11, v2, s[36:39], 0 offen sc0 sc1\n"
    "s_mov_b64 exec, -1\n"
    "s_and_b32 s73, s68, s57\n"
    "s_mul_i32 s73, s73, s56\n"
    "s_add_u32 s73, s73, s55\n"
    "s_waitcnt lgkmcnt(4)\n"
    "v_mfma_f32_16x16x32_bf16 v[48:51], a[0:3], v[16:19], 0\n"
    "ds_read_b128 v[40:43], v9 offset:6144\n"
    "ds_read_b128 v[44:47], v9 offset:7168\n"
    "v_mfma_f32_16x16x32_bf16 v[56:59], a[0:3], v[20:23], 0\n"
    "v_mfma_f32_16x16x32_bf16 v[52:55], a[4:7], v[16:19], 0\n"
    "v_mfma_f32_16x16x32_bf16 v[60:63], a[4:7], v[20:23], 0\n"
    "v_mfma_f32_16x16x32_bf16 v[64:67], a[8:11], v[16:19], 0\n"
    "v_mfma_f32_16x16x32_bf16 v[68:71], a[8:11], v[20:23], 0\n"
    "s_waitcnt lgkmcnt(4)\n"
    "v_mfma_f32_16x16x32_bf16 v[48:51], a[12:15], v[24:27], v[48:51]\n"
    "ds_read_b128 v[16:19], v9 offset:8192\n"
    "ds_read_b128 v[20:23], v9 offset:9216\n"
    "v_mfma_f32_16x16x32_bf16 v[56:59], a[12:15], v[28:31], v[56:59]\n"
    "v_mfma_f32_16x16x32_bf16 v[52:55], a[16:19], v[24:27], v[52:55]\n"
    "v_mfma_f32_16x16x32_bf16 v[60:63], a[16:19], v[28:31], v[60:63]\n"
    "v_mfma_f32_16x16x32_bf16 v[64:67], a[20:23], v[24:27], v[64:67]\n"
    "v_mfma_f32_16x16x32_bf16 v[68:71], a[20:23], v[28:31], v[68:71]\n"
    "s_waitcnt lgkmcnt(4)\n"
    "v_mfma_f32_16x16x32_bf16 v[48:51], a[24:27], v[32:35], v[48:51]\n"
    "ds_read_b128 v[24:27], v9 offset:10240\n"
    "ds_read_b128 v[28:31], v9 offset:11264\n"
    "v_mfma_f32_16x16x32_bf16 v[56:59], a[24:27], v[36:39], v[56:59]\n"
    "v_mfma_f32_16x16x32_bf16 v[52:55], a[28:31], v[32:35], v[52:55]\n"
    "v_mfma_f32_16x16x32_bf16 v[60:63], a[28:31], v[36:39], v[60:63]\n"
    "v_mfma_f32_16x16x32_bf16 v[64:67], a[32:35], v[32:35], v[64:67]\n"
    "v_mfma_f32_16x16x32_bf16 v[68:71], a[32:35], v[36:39], v[68:71]\n"
    "s_waitcnt lgkmcnt(4)\n"
    "v_mfma_f32_16x16x32_bf16 v[48:51], a[36:39], v[40:43], v[48:51]\n"
    "ds_read_b128 v[32:35], v9 offset:12288\n"
    "ds_read_b128 v[36:39], v9 offset:13312\n"
    "v_mfma_f32_16x16x32_bf16 v[56:59], a[36:39], v[44:47], v[56:59]\n"
    "v_mfma_f32_16x16x32_bf16 v[52:55], a[40:43], v[40:43], v[52:55]\n"
    "v_mfma_f32_16x16x32_bf16 v[60:63], a[40:43], v[44:47], v[60:63]\n"
    "v_mfma_f32_16x16x32_bf16 v[64:67], a[44:47], v[40:43], v[64:67]\n"
    "v_mfma_f32_16x16x32_bf16 v[68:71], a[44:47], v[44:47], v[68:71]\n"
    "s_waitcnt lgkmcnt(4)\n"
    "v_mfma_f32_16x16x32_bf16 v[48:51], a[48:51], v[16:19], v[48:51]\n"
    "ds_read_b128 v[40:43], v9 offset:14336\n"
    "ds_read_b128 v[44:47], v9 offset:15360\n"
    "v_mfma_f32_16x16x32_bf16 v[56:59], a[48:51], v[20:23], v[56:59]\n"
    "v_mfma_f32_16x16x32_bf16 v[52:55], a[52:55], v[16:19], v[52:55]\n"
    "v_mfma_f32_16x16x32_bf16 v[60:63], a[52:55], v[20:23], v[60:63]\n"
    "v_mfma_f32_16x16x32_bf16 v[64:67], a[56:59], v[16:19], v[64:67]\n"
    "v_mfma_f32_16x16x32_bf16 v[68:71], a[56:59], v[20:23], v[68:71]\n"
    "s_waitcnt lgkmcnt(4)\n"
    "v_mfma_f32_16x16x32_bf16 v[48:51], a[60:63], v[24:27], v[48:51]\n"
    "ds_read_b128 v[16:19], v9 offset:16384\n"
    "ds_read_b128 v[20:23], v9 offset:17408\n"
    "v_mfma_f32_16x16x32_bf16 v[56:59], a[60:63], v[28:31], v[56:59]\n"
    "v_mfma_f32_16x16x32_bf16 v[52:55], a[64:67], v[24:27], v[52:55]\n"
    "v_mfma_f32_16x16x32_bf16 v[60:63], a[64:67], v[28:31], v[60:63]\n"
    "v_mfma_f32_16x16x32_bf16 v[64:67], a[68:71], v[24:27], v[64:67]\n"
    "v_mfma_f32_16x16x32_bf16 v[68:71], a[68:71], v[28:31], v[68:71]\n"
    "s_waitcnt vmcnt(0)\n"
    "s_cmp_eq_u32 s69, 0\n"
    "s_cbranch_scc1 .LgDOWN_nopub_%=\n"
    "s_sub_u32 s74, s68, 1\n"
    "v_mov_b32 v12, s74\n"
    "s_mov_b64 exec, s[62:63]\n"
    "buffer_store_dword v12, v14, s[36:39], 0 offen sc1\n"
    "s_mov_b64 exec, -1\n"
    ".LgDOWN_nopub_%=:\n"
    "s_cmp_lg_u32 s70, 0\n"
    "s_cbranch_scc1 .LgDOWN_plain0_%=\n"
    "v_add_u32 v13, v11, v3\n"
    "v_cmp_gt_i32 vcc, s71, v13\n"
    "s_cbranch_vccz .LgDOWN_dma0_%=\n"
    "s_or_b64 exec, vcc, 1\n"
    "buffer_atomic_smax v11, v2, s[36:39], 0 offen sc0 sc1\n"
    "s_mov_b64 exec, -1\n"
    ".LgDOWN_plain0_%=:\n"
    "s_waitcnt lgkmcnt(4)\n"
    "v_mfma_f32_16x16x32_bf16 v[48:51], a[72:75], v[32:35], v[48:51]\n"
    "ds_read_b128 v[24:27], v9 offset:18432\n"
    "ds_read_b128 v[28:31], v9 offset:19456\n"
    "v_mfma_f32_16x16x32_bf16 v[56:59], a[72:75], v[36:39], v[56:59]\n"
    "v_mfma_f32_16x16x32_bf16 v[52:55], a[76:79], v[32:35], v[52:55]\n"
    "v_mfma_f32_16x16x32_bf16 v[60:63], a[76:79], v[36:39], v[60:63]\n"
    "v_mfma_f32_16x16x32_bf16 v[64:67], a[80:83], v[32:35], v[64:67]\n"
    "v_mfma_f32_16x16x32_bf16 v[68:71], a[80:83], v[36:39], v[68:71]\n"
    "s_waitcnt lgkmcnt(4)\n"
    "v_mfma_f32_16x16x32_bf16 v[48:51], a[84:87], v[40:43], v[48:51]\n"
    "ds_read_b128 v[32:35], v9 offset:20480\n"
    "ds_read_b128 v[36:39], v9 offset:21504\n"
    "v_mfma_f32_16x16x32_bf16 v[56:59], a[84:87], v[44:47], v[56:59]\n"
    "v_mfma_f32_16x16x32_bf16 v[52:55], a[88:91], v[40:43], v[52:55]\n"
    "v_mfma_f32_16x16x32_bf16 v[60:63], a[88:91], v[44:47], v[60:63]\n"
    "v_mfma_f32_16x16x32_bf16 v[64:67], a[92:95], v[40:43], v[64:67]\n"
    "v_mfma_f32_16x16x32_bf16 v[68:71], a[92:95], v[44:47], v[68:71]\n"
    "s_waitcnt lgkmcnt(4)\n"
    "v_mfma_f32_16x16x32_bf16 v[48:51], a[96:99], v[16:19], v[48:51]\n"
    "ds_read_b128 v[40:43], v9 offset:22528\n"
    "ds_read_b128 v[44:47], v9 offset:23552\n"
    "v_mfma_f32_16x16x32_bf16 v[56:59], a[96:99], v[20:23], v[56:59]\n"
    "v_mfma_f32_16x16x32_bf16 v[52:55], a[100:103], v[16:19], v[52:55]\n"
    "v_mfma_f32_16x16x32_bf16 v[60:63], a[100:103], v[20:23], v[60:63]\n"
    "v_mfma_f32_16x16x32_bf16 v[64:67], a[104:107], v[16:19], v[64:67]\n"
    "v_mfma_f32_16x16x32_bf16 v[68:71], a[104:107], v[20:23], v[68:71]\n"
    "s_waitcnt lgkmcnt(4)\n"
    "v_mfma_f32_16x16x32_bf16 v[48:51], a[108:111], v[24:27], v[48:51]\n"
    "ds_read_b128 v[16:19], v9 offset:24576\n"
    "ds_read_b128 v[20:23], v9 offset:25600\n"
    "v_mfma_f32_16x16x32_bf16 v[56:59], a[108:111], v[28:31], v[56:59]\n"
    "v_mfma_f32_16x16x32_bf16 v[52:55], a[112:115], v[24:27], v[52:55]\n"
    "v_mfma_f32_16x16x32_bf16 v[60:63], a[112:115], v[28:31], v[60:63]\n"
    "v_mfma_f32_16x16x32_bf16 v[64:67], a[116:119], v[24:27], v[64:67]\n"
    "v_mfma_f32_16x16x32_bf16 v[68:71], a[116:119], v[28:31], v[68:71]\n"
    "s_waitcnt lgkmcnt(4)\n"
    "v_mfma_f32_16x16x32_bf16 v[48:51], a[120:123], v[32:35], v[48:51]\n"
    "ds_read_b128 v[24:27], v9 offset:26624\n"
    "ds_read_b128 v[28:31], v9 offset:27648\n"
    "v_mfma_f32_16x16x32_bf16 v[56:59], a[120:123], v[36:39], v[56:59]\n"
    "v_mfma_f32_16x16x32_bf16 v[52:55], a[124:127], v[32:35], v[52:55]\n"
    "v_mfma_f32_16x16x32_bf16 v[60:63], a[124:127], v[36:39], v[60:63]\n"
    "v_mfma_f32_16x16x32_bf16 v[64:67], a[128:131], v[32:35], v[64:67]\n"
    "v_mfma_f32_16x16x32_bf16 v[68:71], a[128:131], v[36:39], v[68:71]\n"
    "s_waitcnt lgkmcnt(4)\n"
    "v_mfma_f32_16x16x32_bf16 v[48:51], a[132:135], v[40:43], v[48:51]\n"
    "ds_read_b128 v[32:35], v9 offset:28672\n"
    "ds_read_b128 v[36:39], v9 offset:29696\n"
    "v_mfma_f32_16x16x32_bf16 v[56:59], a[132:135], v[44:47], v[56:59]\n"
    "v_mfma_f32_16x16x32_bf16 v[52:55], a[136:139], v[40:43], v[52:55]\n"
    "v_mfma_f32_16x16x32_bf16 v[60:63], a[136:139], v[44:47], v[60:63]\n"
    "v_mfma_f32_16x16x32_bf16 v[64:67], a[140:143], v[40:43], v[64:67]\n"
    "v_mfma_f32_16x16x32_bf16 v[68:71], a[140:143], v[44:47], v[68:71]\n"
    "s_waitcnt lgkmcnt(4)\n"
    "v_mfma_f32_16x16x32_bf16 v[48:51], a[144:147], v[16:19], v[48:51]\n"
    "ds_read_b128 v[40:43], v9 offset:30720\n"
    "ds_read_b128 v[44:47], v9 offset:31744\n"
    "v_mfma_f32_16x16x32_bf16 v[56:59], a[144:147], v[20:23], v[56:59]\n"
    "v_mfma_f32_16x16x32_bf16 v[52:55], a[148:151], v[16:19], v[52:55]\n"
    "v_mfma_f32_16x16x32_bf16 v[60:63], a[148:151], v[20:23], v[60:63]\n"
    "v_mfma_f32_16x16x32_bf16 v[64:67], a[152:155], v[16:19], v[64:67]\n"
    "v_mfma_f32_16x16x32_bf16 v[68:71], a[152:155], v[20:23], v[68:71]\n"
    "s_waitcnt lgkmcnt(4)\n"
    "v_mfma_f32_16x16x32_bf16 v[48:51], a[156:159], v[24:27], v[48:51]\n"
    "ds_read_b128 v[16:19], v9 offset:32768\n"
    "ds_read_b128 v[20:23], v9 offset:33792\n"
    "v_mfma_f32_16x16x32_bf16 v[56:59], a[156:159], v[28:31], v[56:59]\n"
    "v_mfma_f32_16x16x32_bf16 v[52:55], a[160:163], v[24:27], v[52:55]\n"
    "v_mfma_f32_16x16x32_bf16 v[60:63], a[160:163], v[28:31], v[60:63]\n"
    "v_mfma_f32_16x16x32_bf16 v[64:67], a[164:167], v[24:27], v[64:67]\n"
    "v_mfma_f32_16x16x32_bf16 v[68:71], a[164:167], v[28:31], v[68:71]\n"
    "s_waitcnt lgkmcnt(4)\n"
    "v_mfma_f32_16x16x32_bf16 v[48:51], a[168:171], v[32:35], v[48:51]\n"
    "ds_read_b128 v[24:27], v9 offset:34816\n"
    "ds_read_b128 v[28:31], v9 offset:35840\n"
    "v_mfma_f32_16x16x32_bf16 v[56:59], a[168:171], v[36:39], v[56:59]\n"
    "v_mfma_f32_16x16x32_bf16 v[52:55], a[172:175], v[32:35], v[52:55]\n"
    "v_mfma_f32_16x16x32_bf16 v[60:63], a[172:175], v[36:39], v[60:63]\n"
    "v_mfma_f32_16x16x32_bf16 v[64:67], a[176:179], v[32:35], v[64:67]\n"
    "v_mfma_f32_16x16x32_bf16 v[68:71], a[176:179], v[36:39], v[68:71]\n"
    "s_waitcnt lgkmcnt(4)\n"
    "v_mfma_f32_16x16x32_bf16 v[48:51], a[180:183], v[40:43], v[48:51]\n"
    "ds_read_b128 v[32:35], v9 offset:36864\n"
    "ds_read_b128 v[36:39], v9 offset:37888\n"
    "v_mfma_f32_16x16x32_bf16 v[56:59], a[180:183], v[44:47], v[56:59]\n"
    "v_mfma_f32_16x16x32_bf16 v[52:55], a[184:187], v[40:43], v[52:55]\n"
    "v_mfma_f32_16x16x32_bf16 v[60:63], a[184:187], v[44:47], v[60:63]\n"
    "v_mfma_f32_16x16x32_bf16 v[64:67], a[188:191], v[40:43], v[64:67]\n"
    "v_mfma_f32_16x16x32_bf16 v[68:71], a[188:191], v[44:47], v[68:71]\n"
    "s_waitcnt lgkmcnt(4)\n"
    "v_mfma_f32_16x16x32_bf16 v[48:51], a[192:195], v[16:19], v[48:51]\n"
    "ds_read_b128 v[40:43], v9 offset:38912\n"
    "ds_read_b128 v[44:47], v9 offset:39936\n"
    "v_mfma_f32_16x16x32_bf16 v[56:59], a[192:195], v[20:23], v[56:59]\n"
    "v_mfma_f32_16x16x32_bf16 v[52:55], a[196:199], v[16:19], v[52:55]\n"
    "v_mfma_f32_16x16x32_bf16 v[60:63], a[196:199], v[20:23], v[60:63]\n"
    "v_mfma_f32_16x16x32_bf16 v[64:67], a[200:203], v[16:19], v[64:67]\n"
    "v_mfma_f32_16x16x32_bf16 v[68:71], a[200:203], v[20:23], v[68:71]\n"
    "s_waitcnt lgkmcnt(4)\n"
    "v_mfma_f32_16x16x32_bf16 v[48:51], a[204:207], v[24:27], v[48:51]\n"
    "ds_read_b128 v[16:19], v9 offset:40960\n"
    "ds_read_b128 v[20:23], v9 offset:41984\n"
    "v_mfma_f32_16x16x32_bf16 v[56:59], a[204:207], v[28:31], v[56:59]\n"
    "v_mfma_f32_16x16x32_bf16 v[52:55], a[208:211], v[24:27], v[52:55]\n"
    "v_mfma_f32_16x16x32_bf16 v[60:63], a[208:211], v[28:31], v[60:63]\n"
    "v_mfma_f32_16x16x32_bf16 v[64:67], a[212:215], v[24:27], v[64:67]\n"
    "v_mfma_f32_16x16x32_bf16 v[68:71], a[212:215], v[28:31], v[68:71]\n"
    ".LgDOWN_seg0_%=:\n"
    "s_cmp_lg_u32 s70, 0\n"
    "s_cbranch_scc1 .LgDOWN_plain1_%=\n"
    "s_waitcnt vmcnt(0)\n"
    "v_add_u32 v13, v11, v3\n"
    "v_cmp_gt_i32 vcc, s71, v13\n"
    "s_cbranch_vccz .LgDOWN_dma1_%=\n"
    ".LgDOWN_plain1_%=:\n"
    "s_waitcnt lgkmcnt(4)\n"
    "v_mfma_f32_16x16x32_bf16 v[48:51], a[216:219], v[32:35], v[48:51]\n"
    "ds_read_b128 v[24:27], v9 offset:43008\n"
    "ds_read_b128 v[28:31], v9 offset:44032\n"
    "v_mfma_f32_16x16x32_bf16 v[56:59], a[216:219], v[36:39], v[56:59]\n"
    "v_mfma_f32_16x16x32_bf16 v[52:55], a[220:223], v[32:35], v[52:55]\n"
    "v_mfma_f32_16x16x32_bf16 v[60:63], a[220:223], v[36:39], v[60:63]\n"
    "v_mfma_f32_16x16x32_bf16 v[64:67], a[224:227], v[32:35], v[64:67]\n"
    "v_mfma_f32_16x16x32_bf16 v[68:71], a[224:227], v[36:39], v[68:71]\n"
    "s_waitcnt lgkmcnt(4)\n"
    "v_mfma_f32_16x16x32_bf16 v[48:51], a[228:231], v[40:43], v[48:51]\n"
    "ds_read_b128 v[32:35], v9 offset:45056\n"
    "ds_read_b128 v[36:39], v9 offset:46080\n"
    "v_mfma_f32_16x16x32_bf16 v[56:59], a[228:231], v[44:47], v[56:59]\n"
    "v_mfma_f32_16x16x32_bf16 v[52:55], a[232:235], v[40:43], v[52:55]\n"
    "v_mfma_f32_16x16x32_bf16 v[60:63], a[232:235], v[44:47], v[60:63]\n"
    "v_mfma_f32_16x16x32_bf16 v[64:67], a[236:239], v[40:43], v[64:67]\n"
    "v_mfma_f32_16x16x32_bf16 v[68:71], a[236:239], v[44:47], v[68:71]\n"
    "s_waitcnt lgkmcnt(4)\n"
    "v_mfma_f32_16x16x32_bf16 v[48:51], a[240:243], v[16:19], v[48:51]\n"
    "ds_read_b128 v[40:43], v9 offset:47104\n"
    "ds_read_b128 v[44:47], v9 offset:48128\n"
    "v_mfma_f32_16x16x32_bf16 v[56:59], a[240:243], v[20:23], v[56:59]\n"
    "v_mfma_f32_16x16x32_bf16 v[52:55], a[244:247], v[16:19], v[52:55]\n"
    "v_mfma_f32_16x16x32_bf16 v[60:63], a[244:247], v[20:23], v[60:63]\n"
    "v_mfma_f32_16x16x32_bf16 v[64:67], a[248:251], v[16:19], v[64:67]\n"
    "v_mfma_f32_16x16x32_bf16 v[68:71], a[248:251], v[20:23], v[68:71]\n"
    "s_waitcnt lgkmcnt(4)\n"
    "v_mfma_f32_16x16x32_bf16 v[48:51], a[252:255], v[24:27], v[48:51]\n"
    "ds_read_b128 v[16:19], v10 offset:0\n"
    "ds_read_b128 v[20:23], v10 offset:1024\n"
    "v_mfma_f32_16x16x32_bf16 v[56:59], a[252:255], v[28:31], v[56:59]\n"
    "v_mfma_f32_16x16x32_bf16 v[52:55], v[80:83], v[24:27], v[52:55]\n"
    "v_mfma_f32_16x16x32_bf16 v[60:63], v[80:83], v[28:31], v[60:63]\n"
    "v_mfma_f32_16x16x32_bf16 v[64:67], v[84:87], v[24:27], v[64:67]\n"
    "v_mfma_f32_16x16x32_bf16 v[68:71], v[84:87], v[28:31], v[68:71]\n"
    "s_waitcnt lgkmcnt(4)\n"
    "v_mfma_f32_16x16x32_bf16 v[48:51], v[88:91], v[32:35], v[48:51]\n"
    "ds_read_b128 v[24:27], v10 offset:2048\n"
    "ds_read_b128 v[28:31], v10 offset:3072\n"
    "v_mfma_f32_16x16x32_bf16 v[56:59], v[88:91], v[36:39], v[56:59]\n"
    "v_mfma_f32_16x16x32_bf16 v[52:55], v[92:95], v[32:35], v[52:55]\n"
    "v_mfma_f32_16x16x32_bf16 v[60:63], v[92:95], v[36:39], v[60:63]\n"
    "v_mfma_f32_16x16x32_bf16 v[64:67], v[96:99], v[32:35], v[64:67]\n"
    "v_mfma_f32_16x16x32_bf16 v[68:71], v[96:99], v[36:39], v[68:71]\n"
    "s_waitcnt lgkmcnt(4)\n"
    "v_mfma_f32_16x16x32_bf16 v[48:51], v[100:103], v[40:43], v[48:51]\n"
    "ds_read_b128 v[32:35], v10 offset:4096\n"
    "ds_read_b128 v[36:39], v10 offset:5120\n"
    "v_mfma_f32_16x16x32_bf16 v[56:59], v[100:103], v[44:47], v[56:59]\n"
    "v_mfma_f32_16x16x32_bf16 v[52:55], v[104:107], v[40:43], v[52:55]\n"
    "v_mfma_f32_16x16x32_bf16 v[60:63], v[104:107], v[44:47], v[60:63]\n"
    "v_mfma_f32_16x16x32_bf16 v[64:67], v[108:111], v[40:43], v[64:67]\n"
    "v_mfma_f32_16x16x32_bf16 v[68:71], v[108:111], v[44:47], v[68:71]\n"
    "s_waitcnt lgkmcnt(4)\n"
    "v_mfma_f32_16x16x32_bf16 v[48:51], v[112:115], v[16:19], v[48:51]\n"
    "ds_read_b128 v[40:43], v10 offset:6144\n"
    "ds_read_b128 v[44:47], v10 offset:7168\n"
    "v_mfma_f32_16x16x32_bf16 v[56:59], v[112:115], v[20:23], v[56:59]\n"
    "v_mfma_f32_16x16x32_bf16 v[52:55], v[116:119], v[16:19], v[52:55]\n"
    "v_mfma_f32_16x16x32_bf16 v[60:63], v[116:119], v[20:23], v[60:63]\n"
    "v_mfma_f32_16x16x32_bf16 v[64:67], v[120:123], v[16:19], v[64:67]\n"
    "v_mfma_f32_16x16x32_bf16 v[68:71], v[120:123], v[20:23], v[68:71]\n"
    "s_waitcnt lgkmcnt(4)\n"
    "v_mfma_f32_16x16x32_bf16 v[48:51], v[124:127], v[24:27], v[48:51]\n"
    "ds_read_b128 v[16:19], v10 offset:8192\n"
    "ds_read_b128 v[20:23], v10 offset:9216\n"
    "v_mfma_f32_16x16x32_bf16 v[56:59], v[124:127], v[28:31], v[56:59]\n"
    "v_mfma_f32_16x16x32_bf16 v[52:55], v[128:131], v[24:27], v[52:55]\n"
    "v_mfma_f32_16x16x32_bf16 v[60:63], v[128:131], v[28:31], v[60:63]\n"
    "v_mfma_f32_16x16x32_bf16 v[64:67], v[132:135], v[24:27], v[64:67]\n"
    "v_mfma_f32_16x16x32_bf16 v[68:71], v[132:135], v[28:31], v[68:71]\n"
    "s_waitcnt lgkmcnt(4)\n"
    "v_mfma_f32_16x16x32_bf16 v[48:51], v[136:139], v[32:35], v[48:51]\n"
    "ds_read_b128 v[24:27], v10 offset:10240\n"
    "ds_read_b128 v[28:31], v10 offset:11264\n"
    "v_mfma_f32_16x16x32_bf16 v[56:59], v[136:139], v[36:39], v[56:59]\n"
    "v_mfma_f32_16x16x32_bf16 v[52:55], v[140:143], v[32:35], v[52:55]\n"
    "v_mfma_f32_16x16x32_bf16 v[60:63], v[140:143], v[36:39], v[60:63]\n"
    "v_mfma_f32_16x16x32_bf16 v[64:67], v[144:147], v[32:35], v[64:67]\n"
    "v_mfma_f32_16x16x32_bf16 v[68:71], v[144:147], v[36:39], v[68:71]\n"
    "s_waitcnt lgkmcnt(4)\n"
    "v_mfma_f32_16x16x32_bf16 v[48:51], v[148:151], v[40:43], v[48:51]\n"
    "ds_read_b128 v[32:35], v10 offset:12288\n"
    "ds_read_b128 v[36:39], v10 offset:13312\n"
    "v_mfma_f32_16x16x32_bf16 v[56:59], v[148:151], v[44:47], v[56:59]\n"
    "v_mfma_f32_16x16x32_bf16 v[52:55], v[152:155], v[40:43], v[52:55]\n"
    "v_mfma_f32_16x16x32_bf16 v[60:63], v[152:155], v[44:47], v[60:63]\n"
    "v_mfma_f32_16x16x32_bf16 v[64:67], v[156:159], v[40:43], v[64:67]\n"
    "v_mfma_f32_16x16x32_bf16 v[68:71], v[156:159], v[44:47], v[68:71]\n"
    "s_waitcnt lgkmcnt(4)\n"
    "v_mfma_f32_16x16x32_bf16 v[48:51], v[160:163], v[16:19], v[48:51]\n"
    "ds_read_b128 v[40:43], v10 offset:14336\n"
    "ds_read_b128 v[44:47], v10 offset:15360\n"
    "v_mfma_f32_16x16x32_bf16 v[56:59], v[160:163], v[20:23], v[56:59]\n"
    "v_mfma_f32_16x16x32_bf16 v[52:55], v[164:167], v[16:19], v[52:55]\n"
    "v_mfma_f32_16x16x32_bf16 v[60:63], v[164:167], v[20:23], v[60:63]\n"
    "v_mfma_f32_16x16x32_bf16 v[64:67], v[168:171], v[16:19], v[64:67]\n"
    "v_mfma_f32_16x16x32_bf16 v[68:71], v[168:171], v[20:23], v[68:71]\n"
    "s_waitcnt lgkmcnt(4)\n"
    "v_mfma_f32_16x16x32_bf16 v[48:51], v[172:175], v[24:27], v[48:51]\n"
    "ds_read_b128 v[16:19], v10 offset:16384\n"
    "ds_read_b128 v[20:23], v10 offset:17408\n"
    "v_mfma_f32_16x16x32_bf16 v[56:59], v[172:175], v[28:31], v[56:59]\n"
    "v_mfma_f32_16x16x32_bf16 v[52:55], v[176:179], v[24:27], v[52:55]\n"
    "v_mfma_f32_16x16x32_bf16 v[60:63], v[176:179], v[28:31], v[60:63]\n"
    "v_mfma_f32_16x16x32_bf16 v[64:67], v[180:183], v[24:27], v[64:67]\n"
    "v_mfma_f32_16x16x32_bf16 v[68:71], v[180:183], v[28:31], v[68:71]\n"
    ".LgDOWN_seg1_%=:\n"
    "s_waitcnt lgkmcnt(4)\n"
    "v_mfma_f32_16x16x32_bf16 v[48:51], v[184:187], v[32:35], v[48:51]\n"
    "ds_read_b128 v[24:27], v10 offset:18432\n"
    "ds_read_b128 v[28:31], v10 offset:19456\n"
    "v_mfma_f32_16x16x32_bf16 v[56:59], v[184:187], v[36:39], v[56:59]\n"
    "v_mfma_f32_16x16x32_bf16 v[52:55], v[188:191], v[32:35], v[52:55]\n"
    "v_mfma_f32_16x16x32_bf16 v[60:63], v[188:191], v[36:39], v[60:63]\n"
    "v_mfma_f32_16x16x32_bf16 v[64:67], v[192:195], v[32:35], v[64:67]\n"
    "v_mfma_f32_16x16x32_bf16 v[68:71], v[192:195], v[36:39], v[68:71]\n"
    "s_waitcnt lgkmcnt(4)\n"
    "v_mfma_f32_16x16x32_bf16 v[48:51], v[196:199], v[40:43], v[48:51]\n"
    "ds_read_b128 v[32:35], v10 offset:20480\n"
    "ds_read_b128 v[36:39], v10 offset:21504\n"
    "v_mfma_f32_16x16x32_bf16 v[56:59], v[196:199], v[44:47], v[56:59]\n"
    "v_mfma_f32_16x16x32_bf16 v[52:55], v[200:203], v[40:43], v[52:55]\n"
    "v_mfma_f32_16x16x32_bf16 v[60:63], v[200:203], v[44:47], v[60:63]\n"
    "v_mfma_f32_16x16x32_bf16 v[64:67], v[204:207], v[40:43], v[64:67]\n"
    "v_mfma_f32_16x16x32_bf16 v[68:71], v[204:207], v[44:47], v[68:71]\n"
    "s_waitcnt lgkmcnt(4)\n"
    "v_mfma_f32_16x16x32_bf16 v[48:51], v[208:211], v[16:19], v[48:51]\n"
    "ds_read_b128 v[40:43], v10 offset:22528\n"
    "ds_read_b128 v[44:47], v10 offset:23552\n"
    "v_mfma_f32_16x16x32_bf16 v[56:59], v[208:211], v[20:23], v[56:59]\n"
    "v_mfma_f32_16x16x32_bf16 v[52:55], v[212:215], v[16:19], v[52:55]\n"
    "v_mfma_f32_16x16x32_bf16 v[60:63], v[212:215], v[20:23], v[60:63]\n"
    "v_mfma_f32_16x16x32_bf16 v[64:67], v[216:219], v[16:19], v[64:67]\n"
    "v_mfma_f32_16x16x32_bf16 v[68:71], v[216:219], v[20:23], v[68:71]\n"
    "s_waitcnt lgkmcnt(4)\n"
    "v_mfma_f32_16x16x32_bf16 v[48:51], v[220:223], v[24:27], v[48:51]\n"
    "v_mfma_f32_16x16x32_bf16 v[56:59], v[220:223], v[28:31], v[56:59]\n"
    "v_mfma_f32_16x16x32_bf16 v[52:55], v[224:227], v[24:27], v[52:55]\n"
    "v_mfma_f32_16x16x32_bf16 v[60:63], v[224:227], v[28:31], v[60:63]\n"
    "v_mfma_f32_16x16x32_bf16 v[64:67], v[228:231], v[24:27], v[64:67]\n"
    "v_mfma_f32_16x16x32_bf16 v[68:71], v[228:231], v[28:31], v[68:71]\n"
    "s_waitcnt lgkmcnt(2)\n"
    "v_mfma_f32_16x16x32_bf16 v[48:51], v[232:235], v[32:35], v[48:51]\n"
    "v_mfma_f32_16x16x32_bf16 v[56:59], v[232:235], v[36:39], v[56:59]\n"
    "v_mfma_f32_16x16x32_bf16 v[52:55], v[236:239], v[32:35], v[52:55]\n"
    "v_mfma_f32_16x16x32_bf16 v[60:63], v[236:239], v[36:39], v[60:63]\n"
    "v_mfma_f32_16x16x32_bf16 v[64:67], v[240:243], v[32:35], v[64:67]\n"
    "v_mfma_f32_16x16x32_bf16 v[68:71], v[240:243], v[36:39], v[68:71]\n"
    "s_cmp_lg_u32 s70, 0\n"
    "s_cbranch_scc0 .LgDOWN_lastp_%=\n"
    ".LgDOWN_nolast_%=:\n"
    "s_waitcnt lgkmcnt(0)\n"
    "v_mfma_f32_16x16x32_bf16 v[48:51], v[244:247], v[40:43], v[48:51]\n"
    "v_mfma_f32_16x16x32_bf16 v[56:59], v[244:247], v[44:47], v[56:59]\n"
    "v_mfma_f32_16x16x32_bf16 v[52:55], v[248:251], v[40:43], v[52:55]\n"
    "v_mfma_f32_16x16x32_bf16 v[60:63], v[248:251], v[44:47], v[60:63]\n"
    "v_mfma_f32_16x16x32_bf16 v[64:67], v[252:255], v[40:43], v[64:67]\n"
    "v_mfma_f32_16x16x32_bf16 v[68:71], v[252:255], v[44:47], v[68:71]\n"
    "v_cvt_pk_bf16_f32 v48, v48, v49\n"
    "v_cvt_pk_bf16_f32 v49, v50, v51\n"
    "v_cvt_pk_bf16_f32 v50, v52, v53\n"
    "v_cvt_pk_bf16_f32 v51, v54, v55\n"
    "buffer_store_dwordx4 v[48:51], v1, s[36:39], s73 offen sc1\n"
    "v_cvt_pk_bf16_f32 v56, v56, v57\n"
    "v_cvt_pk_bf16_f32 v57, v58, v59\n"
    "v_cvt_pk_bf16_f32 v58, v60, v61\n"
    "v_cvt_pk_bf16_f32 v59, v62, v63\n"
    "buffer_store_dwordx4 v[56:59], v1, s[36:39], s73 offen offset:1024 sc1\n"
    "v_cvt_pk_bf16_f32 v64, v64, v65\n"
    "v_cvt_pk_bf16_f32 v65, v66, v67\n"
    "v_cvt_pk_bf16_f32 v66, v68, v69\n"
    "v_cvt_pk_bf16_f32 v67, v70, v71\n"
    "s_nop 0\n"
    "v_permlane16_swap_b32 v64, v66\n"
    "v_permlane16_swap_b32 v65, v67\n"
    "buffer_store_dwordx4 v[64:67], v4, s[36:39], s73 offen sc1\n"
    "s_mov_b32 s68, s71\n"
    "s_mov_b32 s69, 1\n"
    "s_cmp_eq_u32 s70, 0\n"
    "s_cbranch_scc1 .LgDOWN_idle_%=\n"
    "s_cmp_lg_u32 s68, 64\n"
    "s_cbranch_scc1 .LgDOWN_top_%=\n"
    ".LgDOWN_LOOPE_%=:\n"
    "s_waitcnt vmcnt(0)\n"
    "s_mov_b32 s74, 63\n"
    "v_mov_b32 v12, s74\n"
    "s_mov_b64 exec, s[62:63]\n"
    "buffer_store_dword v12, v14, s[36:39], 0 offen sc1\n"
    "s_mov_b64 exec, -1\n"
    "s_waitcnt vmcnt(0)\n"
    "s_branch .LgDOWN_FIN_%=\n"
    ".LgDOWN_dma0_%=:\n"
    "s_mov_b32 s70, 1\n"
    "s_and_b32 s76, s71, s54\n"
    "s_mul_i32 s76, s76, s53\n"
    "s_add_u32 s76, s76, s52\n"
    "s_and_b32 s77, s71, 1\n"
    "s_mul_i32 s77, s77, 73728\n"
    "s_add_u32 s77, s77, s59\n"
    "s_waitcnt lgkmcnt(4)\n"
    "v_mfma_f32_16x16x32_bf16 v[48:51], a[72:75], v[32:35], v[48:51]\n"
    "ds_read_b128 v[24:27], v9 offset:18432\n"
    "ds_read_b128 v[28:31], v9 offset:19456\n"
    "s_add_u32 m0, s77, s59\n"
    "s_add_u32 s77, s77, 8192\n"
    "buffer_load_dwordx4 v1, s[36:39], s76 offen sc1 lds\n"
    "v_mfma_f32_16x16x32_bf16 v[56:59], a[72:75], v[36:39], v[56:59]\n"
    "v_mfma_f32_16x16x32_bf16 v[52:55], a[76:79], v[32:35], v[52:55]\n"
    "v_mfma_f32_16x16x32_bf16 v[60:63], a[76:79], v[36:39], v[60:63]\n"
    "buffer_load_dwordx4 v1, s[36:39], s76 offen offset:1024 sc1 lds\n"
    "s_add_u32 s76, s76, 8192\n"
    "v_mfma_f32_16x16x32_bf16 v[64:67], a[80:83], v[32:35], v[64:67]\n"
    "v_mfma_f32_16x16x32_bf16 v[68:71], a[80:83], v[36:39], v[68:71]\n"
    "s_waitcnt lgkmcnt(4)\n"
    "v_mfma_f32_16x16x32_bf16 v[48:51], a[84:87], v[40:43], v[48:51]\n"
    "ds_read_b128 v[32:35], v9 offset:20480\n"
    "ds_read_b128 v[36:39], v9 offset:21504\n"
    "v_mfma_f32_16x16x32_bf16 v[56:59], a[84:87], v[44:47], v[56:59]\n"
    "s_add_u32 m0, s77, s59\n"
    "s_add_u32 s77, s77, 8192\n"
    "buffer_load_dwordx4 v1, s[36:39], s76 offen sc1 lds\n"
    "v_mfma_f32_16x16x32_bf16 v[52:55], a[88:91], v[40:43], v[52:55]\n"
    "v_mfma_f32_16x16x32_bf16 v[60:63], a[88:91], v[44:47], v[60:63]\n"
    "v_mfma_f32_16x16x32_bf16 v[64:67], a[92:95], v[40:43], v[64:67]\n"
    "v_mfma_f32_16x16x32_bf16 v[68:71], a[92:95], v[44:47], v[68:71]\n"
    "buffer_load_dwordx4 v1, s[36:39], s76 offen offset:1024 sc1 lds\n"
    "s_add_u32 s76, s76, 8192\n"
    "s_waitcnt lgkmcnt(4)\n"
    "v_mfma_f32_16x16x32_bf16 v[48:51], a[96:99], v[16:19], v[48:51]\n"
    "ds_read_b128 v[40:43], v9 offset:22528\n"
    "ds_read_b128 v[44:47], v9 offset:23552\n"
    "v_mfma_f32_16x16x32_bf16 v[56:59], a[96:99], v[20:23], v[56:59]\n"
    "v_mfma_f32_16x16x32_bf16 v[52:55], a[100:103], v[16:19], v[52:55]\n"
    "v_mfma_f32_16x16x32_bf16 v[60:63], a[100:103], v[20:23], v[60:63]\n"
    "s_add_u32 m0, s77, s59\n"
    "s_add_u32 s77, s77, 8192\n"
    "buffer_load_dwordx4 v1, s[36:39], s76 offen sc1 lds\n"
    "v_mfma_f32_16x16x32_bf16 v[64:67], a[104:107], v[16:19], v[64:67]\n"
    "v_mfma_f32_16x16x32_bf16 v[68:71], a[104:107], v[20:23], v[68:71]\n"
    "s_waitcnt lgkmcnt(4)\n"
    "v_mfma_f32_16x16x32_bf16 v[48:51], a[108:111], v[24:27], v[48:51]\n"
    "ds_read_b128 v[16:19], v9 offset:24576\n"
    "ds_read_b128 v[20:23], v9 offset:25600\n"
    "v_mfma_f32_16x16x32_bf16 v[56:59], a[108:111], v[28:31], v[56:59]\n"
    "buffer_load_dwordx4 v1, s[36:39], s76 offen offset:1024 sc1 lds\n"
    "s_add_u32 s76, s76, 8192\n"
    "v_mfma_f32_16x16x32_bf16 v[52:55], a[112:115], v[24:27], v[52:55]\n"
    "v_mfma_f32_16x16x32_bf16 v[60:63], a[112:115], v[28:31], v[60:63]\n"
    "v_mfma_f32_16x16x32_bf16 v[64:67], a[116:119], v[24:27], v[64:67]\n"
    "v_mfma_f32_16x16x32_bf16 v[68:71], a[116:119], v[28:31], v[68:71]\n"
    "s_add_u32 m0, s77, s59\n"
    "s_add_u32 s77, s77, 8192\n"
    "buffer_load_dwordx4 v1, s[36:39], s76 offen sc1 lds\n"
    "s_waitcnt lgkmcnt(4)\n"
    "v_mfma_f32_16x16x32_bf16 v[48:51], a[120:123], v[32:35], v[48:51]\n"
    "ds_read_b128 v[24:27], v9 offset:26624\n"
    "ds_read_b128 v[28:31], v9 offset:27648\n"
    "v_mfma_f32_16x16x32_bf16 v[56:59], a[120:123], v[36:39], v[56:59]\n"
    "v_mfma_f32_16x16x32_bf16 v[52:55], a[124:127], v[32:35], v[52:55]\n"
    "v_mfma_f32_16x16x32_bf16 v[60:63], a[124:127], v[36:39], v[60:63]\n"
    "buffer_load_dwordx4 v1, s[36:39], s76 offen offset:1024 sc1 lds\n"
    "s_add_u32 s76, s76, 8192\n"
    "v_mfma_f32_16x16x32_bf16 v[64:67], a[128:131], v[32:35], v[64:67]\n"
    "v_mfma_f32_16x16x32_bf16 v[68:71], a[128:131], v[36:39], v[68:71]\n"
    "s_waitcnt lgkmcnt(4)\n"
    "v_mfma_f32_16x16x32_bf16 v[48:51], a[132:135], v[40:43], v[48:51]\n"
    "ds_read_b128 v[32:35], v9 offset:28672\n"
    "ds_read_b128 v[36:39], v9 offset:29696\n"
    "v_mfma_f32_16x16x32_bf16 v[56:59], a[132:135], v[44:47], v[56:59]\n"
    "s_add_u32 m0, s77, s59\n"
    "s_add_u32 s77, s77, 8192\n"
    "buffer_load_dwordx4 v1, s[36:39], s76 offen sc1 lds\n"
    "v_mfma_f32_16x16x32_bf16 v[52:55], a[136:139], v[40:43], v[52:55]\n"
    "v_mfma_f32_16x16x32_bf16 v[60:63], a[136:139], v[44:47], v[60:63]\n"
    "v_mfma_f32_16x16x32_bf16 v[64:67], a[140:143], v[40:43], v[64:67]\n"
    "v_mfma_f32_16x16x32_bf16 v[68:71], a[140:143], v[44:47], v[68:71]\n"
    "buffer_load_dwordx4 v1, s[36:39], s76 offen offset:1024 sc1 lds\n"
    "s_add_u32 s76, s76, 8192\n"
    "s_waitcnt lgkmcnt(4)\n"
    "v_mfma_f32_16x16x32_bf16 v[48:51], a[144:147], v[16:19], v[48:51]\n"
    "ds_read_b128 v[40:43], v9 offset:30720\n"
    "ds_read_b128 v[44:47], v9 offset:31744\n"
    "v_mfma_f32_16x16x32_bf16 v[56:59], a[144:147], v[20:23], v[56:59]\n"
    "v_mfma_f32_16x16x32_bf16 v[52:55], a[148:151], v[16:19], v[52:55]\n"
    "s_add_u32 m0, s77, s59\n"
    "s_add_u32 s77, s77, 8192\n"
    "buffer_load_dwordx4 v1, s[36:39], s76 offen sc1 lds\n"
    "v_mfma_f32_16x16x32_bf16 v[60:63], a[148:151], v[20:23], v[60:63]\n"
    "v_mfma_f32_16x16x32_bf16 v[64:67], a[152:155], v[16:19], v[64:67]\n"
    "v_mfma_f32_16x16x32_bf16 v[68:71], a[152:155], v[20:23], v[68:71]\n"
    "s_waitcnt lgkmcnt(4)\n"
    "v_mfma_f32_16x16x32_bf16 v[48:51], a[156:159], v[24:27], v[48:51]\n"
    "ds_read_b128 v[16:19], v9 offset:32768\n"
    "ds_read_b128 v[20:23], v9 offset:33792\n"
    "buffer_load_dwordx4 v1, s[36:39], s76 offen offset:1024 sc1 lds\n"
    "s_add_u32 s76, s76, 8192\n"
    "v_mfma_f32_16x16x32_bf16 v[56:59], a[156:159], v[28:31], v[56:59]\n"
    "v_mfma_f32_16x16x32_bf16 v[52:55], a[160:163], v[24:27], v[52:55]\n"
    "v_mfma_f32_16x16x32_bf16 v[60:63], a[160:163], v[28:31], v[60:63]\n"
    "v_mfma_f32_16x16x32_bf16 v[64:67], a[164:167], v[24:27], v[64:67]\n"
    "s_add_u32 m0, s77, s59\n"
    "s_add_u32 s77, s77, 8192\n"
    "buffer_load_dwordx4 v1, s[36:39], s76 offen sc1 lds\n"
    "v_mfma_f32_16x16x32_bf16 v[68:71], a[164:167], v[28:31], v[68:71]\n"
    "s_waitcnt lgkmcnt(4)\n"
    "v_mfma_f32_16x16x32_bf16 v[48:51], a[168:171], v[32:35], v[48:51]\n"
    "ds_read_b128 v[24:27], v9 offset:34816\n"
    "ds_read_b128 v[28:31], v9 offset:35840\n"
    "v_mfma_f32_16x16x32_bf16 v[56:59], a[168:171], v[36:39], v[56:59]\n"
    "v_mfma_f32_16x16x32_bf16 v[52:55], a[172:175], v[32:35], v[52:55]\n"
    "buffer_load_dwordx4 v1, s[36:39], s76 offen offset:1024 sc1 lds\n"
    "s_add_u32 s76, s76, 8192\n"
    "v_mfma_f32_16x16x32_bf16 v[60:63], a[172:175], v[36:39], v[60:63]\n"
    "v_mfma_f32_16x16x32_bf16 v[64:67], a[176:179], v[32:35], v[64:67]\n"
    "v_mfma_f32_16x16x32_bf16 v[68:71], a[176:179], v[36:39], v[68:71]\n"
    "s_waitcnt lgkmcnt(4)\n"
    "v_mfma_f32_16x16x32_bf16 v[48:51], a[180:183], v[40:43], v[48:51]\n"
    "ds_read_b128 v[32:35], v9 offset:36864\n"
    "ds_read_b128 v[36:39], v9 offset:37888\n"
    "s_add_u32 m0, s77, s59\n"
    "s_add_u32 s77, s77, 8192\n"
    "buffer_load_dwordx4 v1, s[36:39], s76 offen sc1 lds\n"
    "v_mfma_f32_16x16x32_bf16 v[56:59], a[180:183], v[44:47], v[56:59]\n"
    "v_mfma_f32_16x16x32_bf16 v[52:55], a[184:187], v[40:43], v[52:55]\n"
    "v_mfma_f32_16x16x32_bf16 v[60:63], a[184:187], v[44:47], v[60:63]\n"
    "v_mfma_f32_16x16x32_bf16 v[64:67], a[188:191], v[40:43], v[64:67]\n"
    "buffer_load_dwordx4 v1, s[36:39], s76 offen offset:1024 sc1 lds\n"
    "s_add_u32 s76, s76, 8192\n"
    "v_mfma_f32_16x16x32_bf16 v[68:71], a[188:191], v[44:47], v[68:71]\n"
    "s_waitcnt lgkmcnt(4)\n"
    "v_mfma_f32_16x16x32_bf16 v[48:51], a[192:195], v[16:19], v[48:51]\n"
    "ds_read_b128 v[40:43], v9 offset:38912\n"
    "ds_read_b128 v[44:47], v9 offset:39936\n"
    "v_mfma_f32_16x16x32_bf16 v[56:59], a[192:195], v[20:23], v[56:59]\n"
    "v_mfma_f32_16x16x32_bf16 v[52:55], a[196:199], v[16:19], v[52:55]\n"
    "s_add_u32 m0, s77, s59\n"
    "s_add_u32 s77, s77, 8192\n"
    "buffer_load_dwordx4 v1, s[36:39], s76 offen sc1 lds\n"
    "v_mfma_f32_16x16x32_bf16 v[60:63], a[196:199], v[20:23], v[60:63]\n"
    "v_mfma_f32_16x16x32_bf16 v[64:67], a[200:203], v[16:19], v[64:67]\n"
    "v_mfma_f32_16x16x32_bf16 v[68:71], a[200:203], v[20:23], v[68:71]\n"
    "s_waitcnt lgkmcnt(4)\n"
    "v_mfma_f32_16x16x32_bf16 v[48:51], a[204:207], v[24:27], v[48:51]\n"
    "ds_read_b128 v[16:19], v9 offset:40960\n"
    "ds_read_b128 v[20:23], v9 offset:41984\n"
    "buffer_load_dwordx4 v1, s[36:39], s76 offen offset:1024 sc1 lds\n"
    "s_add_u32 s76, s76, 8192\n"
    "v_mfma_f32_16x16x32_bf16 v[56:59], a[204:207], v[28:31], v[56:59]\n"
    "v_mfma_f32_16x16x32_bf16 v[52:55], a[208:211], v[24:27], v[52:55]\n"
    "v_mfma_f32_16x16x32_bf16 v[60:63], a[208:211], v[28:31], v[60:63]\n"
    "v_mfma_f32_16x16x32_bf16 v[64:67], a[212:215], v[24:27], v[64:67]\n"
    "v_mfma_f32_16x16x32_bf16 v[68:71], a[212:215], v[28:31], v[68:71]\n"
    "s_branch .LgDOWN_seg0_%=\n"
    ".LgDOWN_dma1_%=:\n"
    "s_mov_b32 s70, 1\n"
    "s_and_b32 s76, s71, s54\n"
    "s_mul_i32 s76, s76, s53\n"
    "s_add_u32 s76, s76, s52\n"
    "s_and_b32 s77, s71, 1\n"
    "s_mul_i32 s77, s77, 73728\n"
    "s_add_u32 s77, s77, s59\n"
    "s_waitcnt lgkmcnt(4)\n"
    "v_mfma_f32_16x16x32_bf16 v[48:51], a[216:219], v[32:35], v[48:51]\n"
    "ds_read_b128 v[24:27], v9 offset:43008\n"
    "ds_read_b128 v[28:31], v9 offset:44032\n"
    "s_add_u32 m0, s77, s59\n"
    "s_add_u32 s77, s77, 8192\n"
    "buffer_load_dwordx4 v1, s[36:39], s76 offen sc1 lds\n"
    "v_mfma_f32_16x16x32_bf16 v[56:59], a[216:219], v[36:39], v[56:59]\n"
    "v_mfma_f32_16x16x32_bf16 v[52:55], a[220:223], v[32:35], v[52:55]\n"
    "v_mfma_f32_16x16x32_bf16 v[60:63], a[220:223], v[36:39], v[60:63]\n"
    "buffer_load_dwordx4 v1, s[36:39], s76 offen offset:1024 sc1 lds\n"
    "s_add_u32 s76, s76, 8192\n"
    "v_mfma_f32_16x16x32_bf16 v[64:67], a[224:227], v[32:35], v[64:67]\n"
    "v_mfma_f32_16x16x32_bf16 v[68:71], a[224:227], v[36:39], v[68:71]\n"
    "s_waitcnt lgkmcnt(4)\n"
    "v_mfma_f32_16x16x32_bf16 v[48:51], a[228:231], v[40:43], v[48:51]\n"
    "ds_read_b128 v[32:35], v9 offset:45056\n"
    "ds_read_b128 v[36:39], v9 offset:46080\n"
    "v_mfma_f32_16x16x32_bf16 v[56:59], a[228:231], v[44:47], v[56:59]\n"
    "s_add_u32 m0, s77, s59\n"
    "s_add_u32 s77, s77, 8192\n"
    "buffer_load_dwordx4 v1, s[36:39], s76 offen sc1 lds\n"
    "v_mfma_f32_16x16x32_bf16 v[52:55], a[232:235], v[40:43], v[52:55]\n"
    "v_mfma_f32_16x16x32_bf16 v[60:63], a[232:235], v[44:47], v[60:63]\n"
    "v_mfma_f32_16x16x32_bf16 v[64:67], a[236:239], v[40:43], v[64:67]\n"
    "v_mfma_f32_16x16x32_bf16 v[68:71], a[236:239], v[44:47], v[68:71]\n"
    "buffer_load_dwordx4 v1, s[36:39], s76 offen offset:1024 sc1 lds\n"
    "s_add_u32 s76, s76, 8192\n"
    "s_waitcnt lgkmcnt(4)\n"
    "v_mfma_f32_16x16x32_bf16 v[48:51], a[240:243], v[16:19], v[48:51]\n"
    "ds_read_b128 v[40:43], v9 offset:47104\n"
    "ds_read_b128 v[44:47], v9 offset:48128\n"
    "v_mfma_f32_16x16x32_bf16 v[56:59], a[240:243], v[20:23], v[56:59]\n"
    "v_mfma_f32_16x16x32_bf16 v[52:55], a[244:247], v[16:19], v[52:55]\n"
    "v_mfma_f32_16x16x32_bf16 v[60:63], a[244:247], v[20:23], v[60:63]\n"
    "s_add_u32 m0, s77, s59\n"
    "s_add_u32 s77, s77, 8192\n"
    "buffer_load_dwordx4 v1, s[36:39], s76 offen sc1 lds\n"
    "v_mfma_f32_16x16x32_bf16 v[64:67], a[248:251], v[16:19], v[64:67]\n"
    "v_mfma_f32_16x16x32_bf16 v[68:71], a[248:251], v[20:23], v[68:71]\n"
    "s_waitcnt lgkmcnt(4)\n"
    "v_mfma_f32_16x16x32_bf16 v[48:51], a[252:255], v[24:27], v[48:51]\n"
    "ds_read_b128 v[16:19], v10 offset:0\n"
    "ds_read_b128 v[20:23], v10 offset:1024\n"
    "v_mfma_f32_16x16x32_bf16 v[56:59], a[252:255], v[28:31], v[56:59]\n"
    "buffer_load_dwordx4 v1, s[36:39], s76 offen offset:1024 sc1 lds\n"
    "s_add_u32 s76, s76, 8192\n"
    "v_mfma_f32_16x16x32_bf16 v[52:55], v[80:83], v[24:27], v[52:55]\n"
    "v_mfma_f32_16x16x32_bf16 v[60:63], v[80:83], v[28:31], v[60:63]\n"
    "v_mfma_f32_16x16x32_bf16 v[64:67], v[84:87], v[24:27], v[64:67]\n"
    "v_mfma_f32_16x16x32_bf16 v[68:71], v[84:87], v[28:31], v[68:71]\n"
    "s_add_u32 m0, s77, s59\n"
    "s_add_u32 s77, s77, 8192\n"
    "buffer_load_dwordx4 v1, s[36:39], s76 offen sc1 lds\n"
    "s_waitcnt lgkmcnt(4)\n"
    "v_mfma_f32_16x16x32_bf16 v[48:51], v[88:91], v[32:35], v[48:51]\n"
    "ds_read_b128 v[24:27], v10 offset:2048\n"
    "ds_read_b128 v[28:31], v10 offset:3072\n"
    "v_mfma_f32_16x16x32_bf16 v[56:59], v[88:91], v[36:39], v[56:59]\n"
    "v_mfma_f32_16x16x32_bf16 v[52:55], v[92:95], v[32:35], v[52:55]\n"
    "v_mfma_f32_16x16x32_bf16 v[60:63], v[92:95], v[36:39], v[60:63]\n"
    "buffer_load_dwordx4 v1, s[36:39], s76 offen offset:1024 sc1 lds\n"
    "s_add_u32 s76, s76, 8192\n"
    "v_mfma_f32_16x16x32_bf16 v[64:67], v[96:99], v[32:35], v[64:67]\n"
    "v_mfma_f32_16x16x32_bf16 v[68:71], v[96:99], v[36:39], v[68:71]\n"
    "s_waitcnt lgkmcnt(4)\n"
    "v_mfma_f32_16x16x32_bf16 v[48:51], v[100:103], v[40:43], v[48:51]\n"
    "ds_read_b128 v[32:35], v10 offset:4096\n"
    "ds_read_b128 v[36:39], v10 offset:5120\n"
    "v_mfma_f32_16x16x32_bf16 v[56:59], v[100:103], v[44:47], v[56:59]\n"
    "s_add_u32 m0, s77, s59\n"
    "s_add_u32 s77, s77, 8192\n"
    "buffer_load_dwordx4 v1, s[36:39], s76 offen sc1 lds\n"
    "v_mfma_f32_16x16x32_bf16 v[52:55], v[104:107], v[40:43], v[52:55]\n"
    "v_mfma_f32_16x16x32_bf16 v[60:63], v[104:107], v[44:47], v[60:63]\n"
    "v_mfma_f32_16x16x32_bf16 v[64:67], v[108:111], v[40:43], v[64:67]\n"
    "v_mfma_f32_16x16x32_bf16 v[68:71], v[108:111], v[44:47], v[68:71]\n"
    "buffer_load_dwordx4 v1, s[36:39], s76 offen offset:1024 sc1 lds\n"
    "s_add_u32 s76, s76, 8192\n"
    "s_waitcnt lgkmcnt(4)\n"
    "v_mfma_f32_16x16x32_bf16 v[48:51], v[112:115], v[16:19], v[48:51]\n"
    "ds_read_b128 v[40:43], v10 offset:6144\n"
    "ds_read_b128 v[44:47], v10 offset:7168\n"
    "v_mfma_f32_16x16x32_bf16 v[56:59], v[112:115], v[20:23], v[56:59]\n"
    "v_mfma_f32_16x16x32_bf16 v[52:55], v[116:119], v[16:19], v[52:55]\n"
    "s_add_u32 m0, s77, s59\n"
    "s_add_u32 s77, s77, 8192\n"
    "buffer_load_dwordx4 v1, s[36:39], s76 offen sc1 lds\n"
    "v_mfma_f32_16x16x32_bf16 v[60:63], v[116:119], v[20:23], v[60:63]\n"
    "v_mfma_f32_16x16x32_bf16 v[64:67], v[120:123], v[16:19], v[64:67]\n"
    "v_mfma_f32_16x16x32_bf16 v[68:71], v[120:123], v[20:23], v[68:71]\n"
    "s_waitcnt lgkmcnt(4)\n"
    "v_mfma_f32_16x16x32_bf16 v[48:51], v[124:127], v[24:27], v[48:51]\n"
    "ds_read_b128 v[16:19], v10 offset:8192\n"
    "ds_read_b128 v[20:23], v10 offset:9216\n"
    "buffer_load_dwordx4 v1, s[36:39], s76 offen offset:1024 sc1 lds\n"
    "s_add_u32 s76, s76, 8192\n"
    "v_mfma_f32_16x16x32_bf16 v[56:59], v[124:127], v[28:31], v[56:59]\n"
    "v_mfma_f32_16x16x32_bf16 v[52:55], v[128:131], v[24:27], v[52:55]\n"
    "v_mfma_f32_16x16x32_bf16 v[60:63], v[128:131], v[28:31], v[60:63]\n"
    "v_mfma_f32_16x16x32_bf16 v[64:67], v[132:135], v[24:27], v[64:67]\n"
    "s_add_u32 m0, s77, s59\n"
    "s_add_u32 s77, s77, 8192\n"
    "buffer_load_dwordx4 v1, s[36:39], s76 offen sc1 lds\n"
    "v_mfma_f32_16x16x32_bf16 v[68:71], v[132:135], v[28:31], v[68:71]\n"
    "s_waitcnt lgkmcnt(4)\n"
    "v_mfma_f32_16x16x32_bf16 v[48:51], v[136:139], v[32:35], v[48:51]\n"
    "ds_read_b128 v[24:27], v10 offset:10240\n"
    "ds_read_b128 v[28:31], v10 offset:11264\n"
    "v_mfma_f32_16x16x32_bf16 v[56:59], v[136:139], v[36:39], v[56:59]\n"
    "v_mfma_f32_16x16x32_bf16 v[52:55], v[140:143], v[32:35], v[52:55]\n"
    "buffer_load_dwordx4 v1, s[36:39], s76 offen offset:1024 sc1 lds\n"
    "s_add_u32 s76, s76, 8192\n"
    "v_mfma_f32_16x16x32_bf16 v[60:63], v[140:143], v[36:39], v[60:63]\n"
    "v_mfma_f32_16x16x32_bf16 v[64:67], v[144:147], v[32:35], v[64:67]\n"
    "v_mfma_f32_16x16x32_bf16 v[68:71], v[144:147], v[36:39], v[68:71]\n"
    "s_waitcnt lgkmcnt(4)\n"
    "v_mfma_f32_16x16x32_bf16 v[48:51], v[148:151], v[40:43], v[48:51]\n"
    "ds_read_b128 v[32:35], v10 offset:12288\n"
    "ds_read_b128 v[36:39], v10 offset:13312\n"
    "s_add_u32 m0, s77, s59\n"
    "s_add_u32 s77, s77, 8192\n"
    "buffer_load_dwordx4 v1, s[36:39], s76 offen sc1 lds\n"
    "v_mfma_f32_16x16x32_bf16 v[56:59], v[148:151], v[44:47], v[56:59]\n"
    "v_mfma_f32_16x16x32_bf16 v[52:55], v[152:155], v[40:43], v[52:55]\n"
    "v_mfma_f32_16x16x32_bf16 v[60:63], v[152:155], v[44:47], v[60:63]\n"
    "v_mfma_f32_16x16x32_bf16 v[64:67], v[156:159], v[40:43], v[64:67]\n"
    "buffer_load_dwordx4 v1, s[36:39], s76 offen offset:1024 sc1 lds\n"
    "s_add_u32 s76, s76, 8192\n"
    "v_mfma_f32_16x16x32_bf16 v[68:71], v[156:159], v[44:47], v[68:71]\n"
    "s_waitcnt lgkmcnt(4)\n"
    "v_mfma_f32_16x16x32_bf16 v[48:51], v[160:163], v[16:19], v[48:51]\n"
    "ds_read_b128 v[40:43], v10 offset:14336\n"
    "ds_read_b128 v[44:47], v10 offset:15360\n"
    "v_mfma_f32_16x16x32_bf16 v[56:59], v[160:163], v[20:23], v[56:59]\n"
    "v_mfma_f32_16x16x32_bf16 v[52:55], v[164:167], v[16:19], v[52:55]\n"
    "s_add_u32 m0, s77, s59\n"
    "s_add_u32 s77, s77, 8192\n"
    "buffer_load_dwordx4 v1, s[36:39], s76 offen sc1 lds\n"
    "v_mfma_f32_16x16x32_bf16 v[60:63], v[164:167], v[20:23], v[60:63]\n"
    "v_mfma_f32_16x16x32_bf16 v[64:67], v[168:171], v[16:19], v[64:67]\n"
    "v_mfma_f32_16x16x32_bf16 v[68:71], v[168:171], v[20:23], v[68:71]\n"
    "s_waitcnt lgkmcnt(4)\n"
    "v_mfma_f32_16x16x32_bf16 v[48:51], v[172:175], v[24:27], v[48:51]\n"
    "ds_read_b128 v[16:19], v10 offset:16384\n"
    "ds_read_b128 v[20:23], v10 offset:17408\n"
    "buffer_load_dwordx4 v1, s[36:39], s76 offen offset:1024 sc1 lds\n"
    "s_add_u32 s76, s76, 8192\n"
    "v_mfma_f32_16x16x32_bf16 v[56:59], v[172:175], v[28:31], v[56:59]\n"
    "v_mfma_f32_16x16x32_bf16 v[52:55], v[176:179], v[24:27], v[52:55]\n"
    "v_mfma_f32_16x16x32_bf16 v[60:63], v[176:179], v[28:31], v[60:63]\n"
    "v_mfma_f32_16x16x32_bf16 v[64:67], v[180:183], v[24:27], v[64:67]\n"
    "v_mfma_f32_16x16x32_bf16 v[68:71], v[180:183], v[28:31], v[68:71]\n"
    "s_branch .LgDOWN_seg1_%=\n"
    ".LgDOWN_lastp_%=:\n"
    "v_add_u32 v13, v11, v3\n"
    "v_cmp_gt_i32 vcc, s71, v13\n"
    "s_nop 0\n"
    "s_or_b64 exec, vcc, 1\n"
    "buffer_atomic_smax v11, v2, s[36:39], 0 offen sc0 sc1\n"
    "s_mov_b64 exec, -1\n"
    "s_branch .LgDOWN_nolast_%=\n"
    ".LgDOWN_idle_%=:\n"
    "s_waitcnt vmcnt(3)\n"
    "v_add_u32 v13, v11, v3\n"
    "v_cmp_gt_i32 vcc, s71, v13\n"
    "s_cbranch_vccnz .LgDOWN_idle2_%=\n"
    "s_and_b32 s76, s71, s54\n"
    "s_mul_i32 s76, s76, s53\n"
    "s_add_u32 s76, s76, s52\n"
    "s_and_b32 s77, s71, 1\n"
    "s_mul_i32 s77, s77, 73728\n"
    "s_add_u32 s77, s77, s59\n"
    "s_add_u32 m0, s77, s59\n"
    "s_add_u32 s77, s77, 8192\n"
    "buffer_load_dwordx4 v1, s[36:39], s76 offen sc1 lds\n"
    "buffer_load_dwordx4 v1, s[36:39], s76 offen offset:1024 sc1 lds\n"
    "s_add_u32 s76, s76, 8192\n"
    "s_add_u32 m0, s77, s59\n"
    "s_add_u32 s77, s77, 8192\n"
    "buffer_load_dwordx4 v1, s[36:39], s76 offen sc1 lds\n"
    "buffer_load_dwordx4 v1, s[36:39], s76 offen offset:1024 sc1 lds\n"
    "s_add_u32 s76, s76, 8192\n"
    "s_add_u32 m0, s77, s59\n"
    "s_add_u32 s77, s77, 8192\n"
    "buffer_load_dwordx4 v1, s[36:39], s76 offen sc1 lds\n"
    "buffer_load_dwordx4 v1, s[36:39], s76 offen offset:1024 sc1 lds\n"
    "s_add_u32 s76, s76, 8192\n"
    "s_add_u32 m0, s77, s59\n"
    "s_add_u32 s77, s77, 8192\n"
    "buffer_load_dwordx4 v1, s[36:39], s76 offen sc1 lds\n"
    "buffer_load_dwordx4 v1, s[36:39], s76 offen offset:1024 sc1 lds\n"
    "s_add_u32 s76, s76, 8192\n"
    "s_add_u32 m0, s77, s59\n"
    "s_add_u32 s77, s77, 8192\n"
    "buffer_load_dwordx4 v1, s[36:39], s76 offen sc1 lds\n"
    "buffer_load_dwordx4 v1, s[36:39], s76 offen offset:1024 sc1 lds\n"
    "s_add_u32 s76, s76, 8192\n"
    "s_add_u32 m0, s77, s59\n"
    "s_add_u32 s77, s77, 8192\n"
    "buffer_load_dwordx4 v1, s[36:39], s76 offen sc1 lds\n"
    "buffer_load_dwordx4 v1, s[36:39], s76 offen offset:1024 sc1 lds\n"
    "s_add_u32 s76, s76, 8192\n"
    "s_add_u32 m0, s77, s59\n"
    "s_add_u32 s77, s77, 8192\n"
    "buffer_load_dwordx4 v1, s[36:39], s76 offen sc1 lds\n"
    "buffer_load_dwordx4 v1, s[36:39], s76 offen offset:1024 sc1 lds\n"
    "s_add_u32 s76, s76, 8192\n"
    "s_add_u32 m0, s77, s59\n"
    "s_add_u32 s77, s77, 8192\n"
    "buffer_load_dwordx4 v1, s[36:39], s76 offen sc1 lds\n"
    "buffer_load_dwordx4 v1, s[36:39], s76 offen offset:1024 sc1 lds\n"
    "s_add_u32 s76, s76, 8192\n"
    "s_add_u32 m0, s77, s59\n"
    "s_add_u32 s77, s77, 8192\n"
    "buffer_load_dwordx4 v1, s[36:39], s76 offen sc1 lds\n"
    "buffer_load_dwordx4 v1, s[36:39], s76 offen offset:1024 sc1 lds\n"
    "s_add_u32 s76, s76, 8192\n"
    "s_waitcnt vmcnt(18)\n"
    "s_sub_u32 s74, s68, 1\n"
    "v_mov_b32 v12, s74\n"
    "s_mov_b64 exec, s[62:63]\n"
    "buffer_store_dword v12, v14, s[36:39], 0 offen sc1\n"
    "s_mov_b64 exec, -1\n"
    "s_mov_b32 s69, 0\n"
    "s_waitcnt vmcnt(1)\n"
    "s_branch .LgDOWN_bar_%=\n"
    ".LgDOWN_idle2_%=:\n"
    "s_or_b64 exec, vcc, 1\n"
    "buffer_atomic_smax v11, v2, s[36:39], 0 offen sc0 sc1\n"
    "s_mov_b64 exec, -1\n"
    "s_waitcnt vmcnt(1)\n"
    "s_sub_u32 s74, s68, 1\n"
    "v_mov_b32 v12, s74\n"
    "s_mov_b64 exec, s[62:63]\n"
    "buffer_store_dword v12, v14, s[36:39], 0 offen sc1\n"
    "s_mov_b64 exec, -1\n"
    "s_waitcnt vmcnt(1)\n"
    "s_branch .LgDOWN_block1_%=\n"
    ".LgDOWN_block_%=:\n"
    "v_add_u32 v13, v11, v3\n"
    "v_cmp_gt_i32 vcc, s71, v13\n"
    "s_nop 0\n"
    "s_or_b64 exec, vcc, 1\n"
    "buffer_atomic_smax v11, v2, s[36:39], 0 offen sc0 sc1\n"
    "s_mov_b64 exec, -1\n"
    "s_waitcnt vmcnt(0)\n"
    ".LgDOWN_block1_%=:\n"
    "v_add_u32 v13, v11, v3\n"
    "v_cmp_gt_i32 vcc, s71, v13\n"
    "s_cbranch_vccz .LgDOWN_go_%=\n"
    "s_sleep 4\n"
    "s_sub_u32 s72, s72, 1\n"
    "s_cbranch_scc0 .LgDOWN_block_%=\n"
    "s_mov_b32 s72, 0\n"
    ".LgDOWN_go_%=:\n"
    "s_and_b32 s76, s71, s54\n"
    "s_mul_i32 s76, s76, s53\n"
    "s_add_u32 s76, s76, s52\n"
    "s_and_b32 s77, s71, 1\n"
    "s_mul_i32 s77, s77, 73728\n"
    "s_add_u32 s77, s77, s59\n"
    "s_add_u32 m0, s77, s59\n"
    "s_add_u32 s77, s77, 8192\n"
    "buffer_load_dwordx4 v1, s[36:39], s76 offen sc1 lds\n"
    "buffer_load_dwordx4 v1, s[36:39], s76 offen offset:1024 sc1 lds\n"
    "s_add_u32 s76, s76, 8192\n"
    "s_add_u32 m0, s77, s59\n"
    "s_add_u32 s77, s77, 8192\n"
    "buffer_load_dwordx4 v1, s[36:39], s76 offen sc1 lds\n"
    "buffer_load_dwordx4 v1, s[36:39], s76 offen offset:1024 sc1 lds\n"
    "s_add_u32 s76, s76, 8192\n"
    "s_add_u32 m0, s77, s59\n"
    "s_add_u32 s77, s77, 8192\n"
    "buffer_load_dwordx4 v1, s[36:39], s76 offen sc1 lds\n"
    "buffer_load_dwordx4 v1, s[36:39], s76 offen offset:1024 sc1 lds\n"
    "s_add_u32 s76, s76, 8192\n"
    "s_add_u32 m0, s77, s59\n"
    "s_add_u32 s77, s77, 8192\n"
    "buffer_load_dwordx4 v1, s[36:39], s76 offen sc1 lds\n"
    "buffer_load_dwordx4 v1, s[36:39], s76 offen offset:1024 sc1 lds\n"
    "s_add_u32 s76, s76, 8192\n"
    "s_add_u32 m0, s77, s59\n"
    "s_add_u32 s77, s77, 8192\n"
    "buffer_load_dwordx4 v1, s[36:39], s76 offen sc1 lds\n"
    "buffer_load_dwordx4 v1, s[36:39], s76 offen offset:1024 sc1 lds\n"
    "s_add_u32 s76, s76, 8192\n"
    "s_add_u32 m0, s77, s59\n"
    "s_add_u32 s77, s77, 8192\n"
    "buffer_load_dwordx4 v1, s[36:39], s76 offen sc1 lds\n"
    "buffer_load_dwordx4 v1, s[36:39], s76 offen offset:1024 sc1 lds\n"
    "s_add_u32 s76, s76, 8192\n"
    "s_add_u32 m0, s77, s59\n"
    "s_add_u32 s77, s77, 8192\n"
    "buffer_load_dwordx4 v1, s[36:39], s76 offen sc1 lds\n"
    "buffer_load_dwordx4 v1, s[36:39], s76 offen offset:1024 sc1 lds\n"
    "s_add_u32 s76, s76, 8192\n"
    "s_add_u32 m0, s77, s59\n"
    "s_add_u32 s77, s77, 8192\n"
    "buffer_load_dwordx4 v1, s[36:39], s76 offen sc1 lds\n"
    "buffer_load_dwordx4 v1, s[36:39], s76 offen offset:1024 sc1 lds\n"
    "s_add_u32 s76, s76, 8192\n"
    "s_add_u32 m0, s77, s59\n"
    "s_add_u32 s77, s77, 8192\n"
    "buffer_load_dwordx4 v1, s[36:39], s76 offen sc1 lds\n"
    "buffer_load_dwordx4 v1, s[36:39], s76 offen offset:1024 sc1 lds\n"
    "s_add_u32 s76, s76, 8192\n"
    "s_mov_b32 s69, 0\n"
    "s_waitcnt vmcnt(0)\n"
    "s_branch .LgDOWN_bar_%=\n"
    ".LgDOWN_FIN_%=:\n"
  );
}

DEVI u32 hot_page(const Params& p, int L) {
  const u32 lo = (u32)(unsigned long long)p.ws;
  return ((((lo + CTRL_BYTES + HOTSZ - 1) & ~(HOTSZ - 1)) - lo)) + (u32)L * HOTSZ;
}
DEVI void role_rec(const Params& p, int L, int j, int i, u32 dbg, unsigned char* smem) {
  const int tid = threadIdx.x, lane = tid & 63, w = (int)rfl(tid >> 6), h = 8 * j + i;
  const int c = lane & 15, g = lane >> 4, pp = lane & 3;
  {
    v4u t;
    if (w == 0 || w == 3) {
#pragma unroll
      for (int d = 0; d < 4; ++d) {
        u32 v = 0;
#pragma unroll
        for (int e = 0; e < 2; ++e) {
          const int q = 2 * d + e;
          const bool on = w == 0 ? ((q >> 2) == (c >> 3) && ((4 * g + (q & 3)) & 7) == (c & 7)) : (q == (c & 7));
          v |= on ? 0x3F80u << (16 * e) : 0u;
        }
        t[d] = v;
      }
    } else if (w == 1) {
      t.x = (u32)(((g & 1) * 16 + c) * 8);
      t.y = (u32)(g * 128 + (c & 7) * 16 + (c >> 3) * 8);
      t.z = c < 8 ? (u32)(g * 128 + c * 16) : OOB;
      t.w = (u32)(g * 32768 + c * 16);
    } else {
      t.x = (u32)(g * 16384 + c * 16);
      t.y = g < 2 ? (u32)(c * 16 + g * 8) : OOB;
      t.z = (u32)((g >> 1) * 128 + (c & 7) * 16 + (2 * (g & 1) + (c >> 3)) * 32768);
      t.w = lane < 32 ? (u32)lane * 64 : OOB;
    }
    *(v4u*)(smem + 12288 + tid * 16) = t;
    asm volatile("s_waitcnt lgkmcnt(0)" ::: "memory");
  }
  const u32 wb = 576u * 4096 * 2 - h * 256;
  v16u sa = cat4(make_srd(p.ws, WS_END), make_srd(p.Wq + h * 128, wb), make_srd(p.Wk + h * 128, wb), make_srd(p.Wv + h * 128, wb));
  const v4u so = make_srd(p.Wo + h * 128 * 576, 128u * 1152);
  const u32 lb = OFF_L + L * LSZ;
  const int xtra = i == 0 ? 32 + j : -1;
  v16u sb = {so.x, so.y, so.z, so.w,
             lb + L_QKV + h * 24576 + 8192 + w * 2048, lb + L_KVM + h * 6144 + w * 1536, rfl(hot_page(p, L)), mbox_att(L, j, i, w),
             mbox(L, j, SL_REC + i, w), (u32)w * 1024, (u32)h * 256, (u32)(w == 0 ? h : (w == 1 ? xtra : -1)),
             (u32)j, dbg + w * 64, INITF + L * 2048, w == 3 ? (32 + j) * CHUNK + i * 1024 : (w == 2 ? IMG0 + j * 2 * CHUNK + i * 1024 : (w == 1 && i == 0 ? IMG0 + (j * 2 + 1) * CHUNK : ~0u))};
  const int jr = lane >> 3;
  const u32 row = (u32)(16 * (w >> 1) + 8 * ((jr >> 1) & 1) + 4 * (w & 1) + 2 * (jr >> 2) + (jr & 1));
  const u32 ch = (((u32)lane & 7) ^ wswz(jr, w)) << 4;
  v16u va = {0, 0, 0, 0, 0, 0, 0, 0, 0, 0, 0, 0, 0, 0, 0, 0};
  va[0] = lane * 16;
  va[1] = 16384 + w * 2048 + lane * 16;
  va[2] = (u32)((c >> 3) * 0xAAAA) | 0x4444u;
  const int qq = (lane >> 2) & 3;
  va[3] = (u32)((g >> 1) * 36864 + 32768 + (g & 1) * 2048 + (4 * (qq >> 1) + (qq & 1)) * 128 + ((2 * w + (pp >> 1)) << 4) + 8 * (pp & 1));
  va[4] = row * 8192 + ch;
  va[5] = row * 1152 + ch;
  va[6] = (u32)(w >> 1) * 4096 + tr_base(lane, 4 * (w & 1) + pp);
  va[7] = (u32)w * 8192 + tr_base(lane, 4 * (qq >> 1) + (pp >> 1)) + 8 * (pp & 1);
  RUN_ASM(
    "s_nop 4\n"
    "s_add_u32 m0, s61, 16384\n"
    "s_mov_b32 s73, 0\n"
    "buffer_load_dwordx4 v5, s[52:55], s73 offen lds\n"
    "s_add_u32 m0, s61, 20480\n"
    "s_mov_b32 s73, 128\n"
    "buffer_load_dwordx4 v5, s[52:55], s73 offen lds\n"
    "s_add_u32 m0, s61, 24576\n"
    "s_mov_b32 s73, 256\n"
    "buffer_load_dwordx4 v5, s[52:55], s73 offen lds\n"
    "s_add_u32 m0, s61, 28672\n"
    "s_mov_b32 s73, 384\n"
    "buffer_load_dwordx4 v5, s[52:55], s73 offen lds\n"
    "s_add_u32 m0, s61, 32768\n"
    "s_mov_b32 s73, 512\n"
    "buffer_load_dwordx4 v5, s[52:55], s73 offen lds\n"
    "s_add_u32 m0, s61, 36864\n"
    "s_mov_b32 s73, 640\n"
    "buffer_load_dwordx4 v5, s[52:55], s73 offen lds\n"
    "s_add_u32 m0, s61, 40960\n"
    "s_mov_b32 s73, 768\n"
    "buffer_load_dwordx4 v5, s[52:55], s73 offen lds\n"
    "s_add_u32 m0, s61, 45056\n"
    "s_mov_b32 s73, 896\n"
    "buffer_load_dwordx4 v5, s[52:55], s73 offen lds\n"
    "s_add_u32 m0, s61, 49152\n"
    "s_mov_b32 s73, 1024\n"
    "buffer_load_dwordx4 v5, s[52:55], s73 offen lds\n"
    "s_add_u32 m0, s61, 53248\n"
    "s_mov_b32 s73, 36864\n"
    "buffer_load_dwordx4 v5, s[52:55], s73 offen lds\n"
    "s_add_u32 m0, s61, 57344\n"
    "s_mov_b32 s73, 36992\n"
    "buffer_load_dwordx4 v5, s[52:55], s73 offen lds\n"
    "s_add_u32 m0, s61, 61440\n"
    "s_mov_b32 s73, 37120\n"
    "buffer_load_dwordx4 v5, s[52:55], s73 offen lds\n"
    "s_add_u32 m0, s61, 65536\n"
    "s_mov_b32 s73, 37248\n"
    "buffer_load_dwordx4 v5, s[52:55], s73 offen lds\n"
    "s_add_u32 m0, s61, 69632\n"
    "s_mov_b32 s73, 37376\n"
    "buffer_load_dwordx4 v5, s[52:55], s73 offen lds\n"
    "s_add_u32 m0, s61, 73728\n"
    "s_mov_b32 s73, 37504\n"
    "buffer_load_dwordx4 v5, s[52:55], s73 offen lds\n"
    "s_add_u32 m0, s61, 77824\n"
    "s_mov_b32 s73, 37632\n"
    "buffer_load_dwordx4 v5, s[52:55], s73 offen lds\n"
    "s_add_u32 m0, s61, 81920\n"
    "s_mov_b32 s73, 37760\n"
    "buffer_load_dwordx4 v5, s[52:55], s73 offen lds\n"
    "s_add_u32 m0, s61, 86016\n"
    "s_mov_b32 s73, 37888\n"
    "buffer_load_dwordx4 v5, s[52:55], s73 offen lds\n"
    "s_add_u32 m0, s61, 90112\n"
    "s_mov_b32 s73, 73728\n"
    "buffer_load_dwordx4 v5, s[52:55], s73 offen lds\n"
    "s_add_u32 m0, s61, 94208\n"
    "s_mov_b32 s73, 73856\n"
    "buffer_load_dwordx4 v5, s[52:55], s73 offen lds\n"
    "s_add_u32 m0, s61, 98304\n"
    "s_mov_b32 s73, 73984\n"
    "buffer_load_dwordx4 v5, s[52:55], s73 offen lds\n"
    "s_add_u32 m0, s61, 102400\n"
    "s_mov_b32 s73, 74112\n"
    "buffer_load_dwordx4 v5, s[52:55], s73 offen lds\n"
    "s_add_u32 m0, s61, 106496\n"
    "s_mov_b32 s73, 74240\n"
    "buffer_load_dwordx4 v5, s[52:55], s73 offen lds\n"
    "s_add_u32 m0, s61, 110592\n"
    "s_mov_b32 s73, 74368\n"
    "buffer_load_dwordx4 v5, s[52:55], s73 offen lds\n"
    "s_add_u32 m0, s61, 114688\n"
    "s_mov_b32 s73, 74496\n"
    "buffer_load_dwordx4 v5, s[52:55], s73 offen lds\n"
    "s_add_u32 m0, s61, 118784\n"
    "s_mov_b32 s73, 74624\n"
    "buffer_load_dwordx4 v5, s[52:55], s73 offen lds\n"
    "s_add_u32 m0, s61, 122880\n"
    "s_mov_b32 s73, 74752\n"
    "buffer_load_dwordx4 v5, s[52:55], s73 offen lds\n"
    "s_add_u32 m0, s61, 126976\n"
    "s_mov_b32 s73, 110592\n"
    "buffer_load_dwordx4 v5, s[52:55], s73 offen lds\n"
    "s_add_u32 m0, s61, 131072\n"
    "s_mov_b32 s73, 110720\n"
    "buffer_load_dwordx4 v5, s[52:55], s73 offen lds\n"
    "s_add_u32 m0, s61, 135168\n"
    "s_mov_b32 s73, 110848\n"
    "buffer_load_dwordx4 v5, s[52:55], s73 offen lds\n"
    "s_add_u32 m0, s61, 139264\n"
    "s_mov_b32 s73, 110976\n"
    "buffer_load_dwordx4 v5, s[52:55], s73 offen lds\n"
    "s_add_u32 m0, s61, 143360\n"
    "s_mov_b32 s73, 111104\n"
    "buffer_load_dwordx4 v5, s[52:55], s73 offen lds\n"
    "s_add_u32 m0, s61, 147456\n"
    "s_mov_b32 s73, 111232\n"
    "buffer_load_dwordx4 v5, s[52:55], s73 offen lds\n"
    "s_add_u32 m0, s61, 151552\n"
    "s_mov_b32 s73, 111360\n"
    "buffer_load_dwordx4 v5, s[52:55], s73 offen lds\n"
    "s_add_u32 m0, s61, 155648\n"
    "s_mov_b32 s73, 111488\n"
    "buffer_load_dwordx4 v5, s[52:55], s73 offen lds\n"
    "s_add_u32 m0, s61, 159744\n"
    "s_mov_b32 s73, 111616\n"
    "buffer_load_dwordx4 v5, s[52:55], s73 offen lds\n"
    "s_waitcnt vmcnt(18) lgkmcnt(0)\n"
    "s_barrier\n"
    "v_add_u32 v8, 16384, v7\n"
    "v_xor_b32 v9, 32, v8\n"
    "v_xor_b32 v10, 320, v8\n"
    "v_xor_b32 v11, 32, v10\n"
    "v_add_u32 v12, 16384, v3\n"
    "v_xor_b32 v13, 320, v12\n"
    "ds_read_b64_tr_b16 v[112:113], v8 offset:0\n"
    "ds_read_b64_tr_b16 v[114:115], v8 offset:512\n"
    "ds_read_b64_tr_b16 v[116:117], v8 offset:36864\n"
    "ds_read_b64_tr_b16 v[118:119], v8 offset:37376\n"
    "ds_read_b64_tr_b16 v[144:145], v10 offset:0\n"
    "ds_read_b64_tr_b16 v[146:147], v10 offset:512\n"
    "ds_read_b64_tr_b16 v[148:149], v10 offset:36864\n"
    "ds_read_b64_tr_b16 v[150:151], v10 offset:37376\n"
    "s_waitcnt lgkmcnt(6)\n"
    "ds_read_b64_tr_b16 v[128:129], v9 offset:0\n"
    "ds_read_b64_tr_b16 v[130:131], v9 offset:512\n"
    "ds_read_b64_tr_b16 v[132:133], v9 offset:36864\n"
    "ds_read_b64_tr_b16 v[134:135], v9 offset:37376\n"
    "ds_read_b64_tr_b16 v[152:153], v11 offset:0\n"
    "ds_read_b64_tr_b16 v[154:155], v11 offset:512\n"
    "ds_read_b64_tr_b16 v[156:157], v11 offset:36864\n"
    "ds_read_b64_tr_b16 v[158:159], v11 offset:37376\n"
    "s_waitcnt lgkmcnt(6)\n"
    "ds_read_b64_tr_b16 v[160:161], v8 offset:4096\n"
    "ds_read_b64_tr_b16 v[162:163], v8 offset:4608\n"
    "ds_read_b64_tr_b16 v[164:165], v8 offset:40960\n"
    "ds_read_b64_tr_b16 v[166:167], v8 offset:41472\n"
    "ds_read_b64_tr_b16 v[176:177], v10 offset:4096\n"
    "ds_read_b64_tr_b16 v[178:179], v10 offset:4608\n"
    "ds_read_b64_tr_b16 v[180:181], v10 offset:40960\n"
    "ds_read_b64_tr_b16 v[182:183], v10 offset:41472\n"
    "s_waitcnt lgkmcnt(6)\n"
    "ds_read_b64_tr_b16 v[168:169], v9 offset:4096\n"
    "ds_read_b64_tr_b16 v[170:171], v9 offset:4608\n"
    "ds_read_b64_tr_b16 v[172:173], v9 offset:40960\n"
    "ds_read_b64_tr_b16 v[174:175], v9 offset:41472\n"
    "ds_read_b64_tr_b16 v[184:185], v11 offset:4096\n"
    "ds_read_b64_tr_b16 v[186:187], v11 offset:4608\n"
    "ds_read_b64_tr_b16 v[188:189], v11 offset:40960\n"
    "ds_read_b64_tr_b16 v[190:191], v11 offset:41472\n"
    "s_waitcnt lgkmcnt(6)\n"
    "v_xor_b32 v8, 32, v12\n"
    "v_xor_b32 v9, 32, v13\n"
    "ds_read_b64_tr_b16 v[192:193], v12 offset:0\n"
    "ds_read_b64_tr_b16 v[194:195], v8 offset:1024\n"
    "ds_read_b64_tr_b16 v[196:197], v13 offset:0\n"
    "ds_read_b64_tr_b16 v[198:199], v9 offset:1024\n"
    "s_waitcnt vmcnt(0) lgkmcnt(0)\n"
    "s_barrier\n"
    "s_add_u32 m0, s61, 16384\n"
    "s_mov_b32 s73, 0\n"
    "buffer_load_dwordx4 v4, s[40:43], s73 offen lds\n"
    "s_add_u32 m0, s61, 20480\n"
    "s_mov_b32 s73, 128\n"
    "buffer_load_dwordx4 v4, s[40:43], s73 offen lds\n"
    "s_add_u32 m0, s61, 24576\n"
    "s_mov_b32 s73, 0\n"
    "buffer_load_dwordx4 v4, s[44:47], s73 offen lds\n"
    "s_add_u32 m0, s61, 28672\n"
    "s_mov_b32 s73, 128\n"
    "buffer_load_dwordx4 v4, s[44:47], s73 offen lds\n"
    "s_add_u32 m0, s61, 32768\n"
    "s_mov_b32 s73, 262144\n"
    "buffer_load_dwordx4 v4, s[40:43], s73 offen lds\n"
    "s_add_u32 m0, s61, 36864\n"
    "s_mov_b32 s73, 262272\n"
    "buffer_load_dwordx4 v4, s[40:43], s73 offen lds\n"
    "s_add_u32 m0, s61, 40960\n"
    "s_mov_b32 s73, 262144\n"
    "buffer_load_dwordx4 v4, s[44:47], s73 offen lds\n"
    "s_add_u32 m0, s61, 45056\n"
    "s_mov_b32 s73, 262272\n"
    "buffer_load_dwordx4 v4, s[44:47], s73 offen lds\n"
    "s_add_u32 m0, s61, 49152\n"
    "s_mov_b32 s73, 524288\n"
    "buffer_load_dwordx4 v4, s[40:43], s73 offen lds\n"
    "s_add_u32 m0, s61, 53248\n"
    "s_mov_b32 s73, 524416\n"
    "buffer_load_dwordx4 v4, s[40:43], s73 offen lds\n"
    "s_add_u32 m0, s61, 57344\n"
    "s_mov_b32 s73, 524288\n"
    "buffer_load_dwordx4 v4, s[44:47], s73 offen lds\n"
    "s_add_u32 m0, s61, 61440\n"
    "s_mov_b32 s73, 524416\n"
    "buffer_load_dwordx4 v4, s[44:47], s73 offen lds\n"
    "s_add_u32 m0, s61, 65536\n"
    "s_mov_b32 s73, 786432\n"
    "buffer_load_dwordx4 v4, s[40:43], s73 offen lds\n"
    "s_add_u32 m0, s61, 69632\n"
    "s_mov_b32 s73, 786560\n"
    "buffer_load_dwordx4 v4, s[40:43], s73 offen lds\n"
    "s_add_u32 m0, s61, 73728\n"
    "s_mov_b32 s73, 786432\n"
    "buffer_load_dwordx4 v4, s[44:47], s73 offen lds\n"
    "s_add_u32 m0, s61, 77824\n"
    "s_mov_b32 s73, 786560\n"
    "buffer_load_dwordx4 v4, s[44:47], s73 offen lds\n"
    "v_add_u32 v8, 90112, v7\n"
    "v_xor_b32 v9, 32, v8\n"
    "v_xor_b32 v10, 320, v8\n"
    "v_xor_b32 v11, 32, v10\n"
    "v_add_u32 v12, 90112, v3\n"
    "v_xor_b32 v13, 320, v12\n"
    "ds_read_b64_tr_b16 v[120:121], v8 offset:0\n"
    "ds_read_b64_tr_b16 v[122:123], v8 offset:512\n"
    "ds_read_b64_tr_b16 v[124:125], v8 offset:36864\n"
    "ds_read_b64_tr_b16 v[126:127], v8 offset:37376\n"
    "ds_read_b64_tr_b16 v[208:209], v10 offset:0\n"
    "ds_read_b64_tr_b16 v[210:211], v10 offset:512\n"
    "ds_read_b64_tr_b16 v[212:213], v10 offset:36864\n"
    "ds_read_b64_tr_b16 v[214:215], v10 offset:37376\n"
    "s_waitcnt lgkmcnt(6)\n"
    "ds_read_b64_tr_b16 v[136:137], v9 offset:0\n"
    "ds_read_b64_tr_b16 v[138:139], v9 offset:512\n"
    "ds_read_b64_tr_b16 v[140:141], v9 offset:36864\n"
    "ds_read_b64_tr_b16 v[142:143], v9 offset:37376\n"
    "ds_read_b64_tr_b16 v[216:217], v11 offset:0\n"
    "ds_read_b64_tr_b16 v[218:219], v11 offset:512\n"
    "ds_read_b64_tr_b16 v[220:221], v11 offset:36864\n"
    "ds_read_b64_tr_b16 v[222:223], v11 offset:37376\n"
    "s_waitcnt lgkmcnt(6)\n"
    "ds_read_b64_tr_b16 v[224:225], v8 offset:4096\n"
    "ds_read_b64_tr_b16 v[226:227], v8 offset:4608\n"
    "ds_read_b64_tr_b16 v[228:229], v8 offset:40960\n"
    "ds_read_b64_tr_b16 v[230:231], v8 offset:41472\n"
    "ds_read_b64_tr_b16 v[240:241], v10 offset:4096\n"
    "ds_read_b64_tr_b16 v[242:243], v10 offset:4608\n"
    "ds_read_b64_tr_b16 v[244:245], v10 offset:40960\n"
    "ds_read_b64_tr_b16 v[246:247], v10 offset:41472\n"
    "s_waitcnt lgkmcnt(6)\n"
    "ds_read_b64_tr_b16 v[232:233], v9 offset:4096\n"
    "ds_read_b64_tr_b16 v[234:235], v9 offset:4608\n"
    "ds_read_b64_tr_b16 v[236:237], v9 offset:40960\n"
    "ds_read_b64_tr_b16 v[238:239], v9 offset:41472\n"
    "ds_read_b64_tr_b16 v[248:249], v11 offset:4096\n"
    "ds_read_b64_tr_b16 v[250:251], v11 offset:4608\n"
    "ds_read_b64_tr_b16 v[252:253], v11 offset:40960\n"
    "ds_read_b64_tr_b16 v[254:255], v11 offset:41472\n"
    "s_waitcnt lgkmcnt(6)\n"
    "v_xor_b32 v8, 32, v12\n"
    "v_xor_b32 v9, 32, v13\n"
    "ds_read_b64_tr_b16 v[200:201], v12 offset:0\n"
    "ds_read_b64_tr_b16 v[202:203], v8 offset:1024\n"
    "ds_read_b64_tr_b16 v[204:205], v13 offset:0\n"
    "ds_read_b64_tr_b16 v[206:207], v9 offset:1024\n"
    "s_waitcnt vmcnt(12) lgkmcnt(0)\n"
    "s_barrier\n"
    "s_add_u32 m0, s61, 81920\n"
    "s_mov_b32 s73, 1048576\n"
    "buffer_load_dwordx4 v4, s[40:43], s73 offen lds\n"
    "s_add_u32 m0, s61, 86016\n"
    "s_mov_b32 s73, 1048704\n"
    "buffer_load_dwordx4 v4, s[40:43], s73 offen lds\n"
    "s_add_u32 m0, s61, 90112\n"
    "s_mov_b32 s73, 1048576\n"
    "buffer_load_dwordx4 v4, s[44:47], s73 offen lds\n"
    "s_add_u32 m0, s61, 94208\n"
    "s_mov_b32 s73, 1048704\n"
    "buffer_load_dwordx4 v4, s[44:47], s73 offen lds\n"
    "s_add_u32 m0, s61, 98304\n"
    "s_mov_b32 s73, 1310720\n"
    "buffer_load_dwordx4 v4, s[40:43], s73 offen lds\n"
    "s_add_u32 m0, s61, 102400\n"
    "s_mov_b32 s73, 1310848\n"
    "buffer_load_dwordx4 v4, s[40:43], s73 offen lds\n"
    "s_add_u32 m0, s61, 106496\n"
    "s_mov_b32 s73, 1310720\n"
    "buffer_load_dwordx4 v4, s[44:47], s73 offen lds\n"
    "s_add_u32 m0, s61, 110592\n"
    "s_mov_b32 s73, 1310848\n"
    "buffer_load_dwordx4 v4, s[44:47], s73 offen lds\n"
    "s_add_u32 m0, s61, 114688\n"
    "s_mov_b32 s73, 1572864\n"
    "buffer_load_dwordx4 v4, s[40:43], s73 offen lds\n"
    "s_add_u32 m0, s61, 118784\n"
    "s_mov_b32 s73, 1572992\n"
    "buffer_load_dwordx4 v4, s[40:43], s73 offen lds\n"
    "s_add_u32 m0, s61, 122880\n"
    "s_mov_b32 s73, 1572864\n"
    "buffer_load_dwordx4 v4, s[44:47], s73 offen lds\n"
    "s_add_u32 m0, s61, 126976\n"
    "s_mov_b32 s73, 1572992\n"
    "buffer_load_dwordx4 v4, s[44:47], s73 offen lds\n"
    "s_add_u32 m0, s61, 131072\n"
    "s_mov_b32 s73, 1835008\n"
    "buffer_load_dwordx4 v4, s[40:43], s73 offen lds\n"
    "s_add_u32 m0, s61, 135168\n"
    "s_mov_b32 s73, 1835136\n"
    "buffer_load_dwordx4 v4, s[40:43], s73 offen lds\n"
    "s_add_u32 m0, s61, 139264\n"
    "s_mov_b32 s73, 1835008\n"
    "buffer_load_dwordx4 v4, s[44:47], s73 offen lds\n"
    "s_add_u32 m0, s61, 143360\n"
    "s_mov_b32 s73, 1835136\n"
    "buffer_load_dwordx4 v4, s[44:47], s73 offen lds\n"
    "s_add_u32 m0, s61, 147456\n"
    "s_mov_b32 s73, 2097152\n"
    "buffer_load_dwordx4 v4, s[40:43], s73 offen lds\n"
    "s_add_u32 m0, s61, 151552\n"
    "s_mov_b32 s73, 2097280\n"
    "buffer_load_dwordx4 v4, s[40:43], s73 offen lds\n"
    "s_add_u32 m0, s61, 155648\n"
    "s_mov_b32 s73, 2097152\n"
    "buffer_load_dwordx4 v4, s[44:47], s73 offen lds\n"
    "s_add_u32 m0, s61, 159744\n"
    "s_mov_b32 s73, 2097280\n"
    "buffer_load_dwordx4 v4, s[44:47], s73 offen lds\n"
    "v_add_u32 v8, 16384, v6\n"
    "ds_read_b64_tr_b16 a[0:1], v8 offset:0\n"
    "ds_read_b64_tr_b16 a[2:3], v8 offset:512\n"
    "ds_read_b64_tr_b16 a[8:9], v8 offset:8\n"
    "ds_read_b64_tr_b16 a[10:11], v8 offset:520\n"
    "ds_read_b64_tr_b16 a[144:145], v8 offset:8192\n"
    "ds_read_b64_tr_b16 a[146:147], v8 offset:8704\n"
    "ds_read_b64_tr_b16 a[152:153], v8 offset:8200\n"
    "ds_read_b64_tr_b16 a[154:155], v8 offset:8712\n"
    "s_waitcnt vmcnt(28) lgkmcnt(0)\n"
    "s_barrier\n"
    "s_add_u32 m0, s61, 16384\n"
    "s_mov_b32 s73, 2359296\n"
    "buffer_load_dwordx4 v4, s[40:43], s73 offen lds\n"
    "s_add_u32 m0, s61, 20480\n"
    "s_mov_b32 s73, 2359424\n"
    "buffer_load_dwordx4 v4, s[40:43], s73 offen lds\n"
    "s_add_u32 m0, s61, 24576\n"
    "s_mov_b32 s73, 2359296\n"
    "buffer_load_dwordx4 v4, s[44:47], s73 offen lds\n"
    "s_add_u32 m0, s61, 28672\n"
    "s_mov_b32 s73, 2359424\n"
    "buffer_load_dwordx4 v4, s[44:47], s73 offen lds\n"
    "v_add_u32 v8, 32768, v6\n"
    "ds_read_b64_tr_b16 a[4:5], v8 offset:0\n"
    "ds_read_b64_tr_b16 a[6:7], v8 offset:512\n"
    "ds_read_b64_tr_b16 a[12:13], v8 offset:8\n"
    "ds_read_b64_tr_b16 a[14:15], v8 offset:520\n"
    "ds_read_b64_tr_b16 a[148:149], v8 offset:8192\n"
    "ds_read_b64_tr_b16 a[150:151], v8 offset:8704\n"
    "ds_read_b64_tr_b16 a[156:157], v8 offset:8200\n"
    "ds_read_b64_tr_b16 a[158:159], v8 offset:8712\n"
    "s_waitcnt vmcnt(28) lgkmcnt(0)\n"
    "s_barrier\n"
    "s_add_u32 m0, s61, 32768\n"
    "s_mov_b32 s73, 2621440\n"
    "buffer_load_dwordx4 v4, s[40:43], s73 offen lds\n"
    "s_add_u32 m0, s61, 36864\n"
    "s_mov_b32 s73, 2621568\n"
    "buffer_load_dwordx4 v4, s[40:43], s73 offen lds\n"
    "s_add_u32 m0, s61, 40960\n"
    "s_mov_b32 s73, 2621440\n"
    "buffer_load_dwordx4 v4, s[44:47], s73 offen lds\n"
    "s_add_u32 m0, s61, 45056\n"
    "s_mov_b32 s73, 2621568\n"
    "buffer_load_dwordx4 v4, s[44:47], s73 offen lds\n"
    "v_add_u32 v8, 49152, v6\n"
    "ds_read_b64_tr_b16 a[16:17], v8 offset:0\n"
    "ds_read_b64_tr_b16 a[18:19], v8 offset:512\n"
    "ds_read_b64_tr_b16 a[24:25], v8 offset:8\n"
    "ds_read_b64_tr_b16 a[26:27], v8 offset:520\n"
    "ds_read_b64_tr_b16 a[160:161], v8 offset:8192\n"
    "ds_read_b64_tr_b16 a[162:163], v8 offset:8704\n"
    "ds_read_b64_tr_b16 a[168:169], v8 offset:8200\n"
    "ds_read_b64_tr_b16 a[170:171], v8 offset:8712\n"
    "s_waitcnt vmcnt(28) lgkmcnt(0)\n"
    "s_barrier\n"
    "s_add_u32 m0, s61, 49152\n"
    "s_mov_b32 s73, 2883584\n"
    "buffer_load_dwordx4 v4, s[40:43], s73 offen lds\n"
    "s_add_u32 m0, s61, 53248\n"
    "s_mov_b32 s73, 2883712\n"
    "buffer_load_dwordx4 v4, s[40:43], s73 offen lds\n"
    "s_add_u32 m0, s61, 57344\n"
    "s_mov_b32 s73, 2883584\n"
    "buffer_load_dwordx4 v4, s[44:47], s73 offen lds\n"
    "s_add_u32 m0, s61, 61440\n"
    "s_mov_b32 s73, 2883712\n"
    "buffer_load_dwordx4 v4, s[44:47], s73 offen lds\n"
    "v_add_u32 v8, 65536, v6\n"
    "ds_read_b64_tr_b16 a[20:21], v8 offset:0\n"
    "ds_read_b64_tr_b16 a[22:23], v8 offset:512\n"
    "ds_read_b64_tr_b16 a[28:29], v8 offset:8\n"
    "ds_read_b64_tr_b16 a[30:31], v8 offset:520\n"
    "ds_read_b64_tr_b16 a[164:165], v8 offset:8192\n"
    "ds_read_b64_tr_b16 a[166:167], v8 offset:8704\n"
    "ds_read_b64_tr_b16 a[172:173], v8 offset:8200\n"
    "ds_read_b64_tr_b16 a[174:175], v8 offset:8712\n"
    "s_waitcnt vmcnt(28) lgkmcnt(0)\n"
    "s_barrier\n"
    "s_add_u32 m0, s61, 65536\n"
    "s_mov_b32 s73, 3145728\n"
    "buffer_load_dwordx4 v4, s[40:43], s73 offen lds\n"
    "s_add_u32 m0, s61, 69632\n"
    "s_mov_b32 s73, 3145856\n"
    "buffer_load_dwordx4 v4, s[40:43], s73 offen lds\n"
    "s_add_u32 m0, s61, 73728\n"
    "s_mov_b32 s73, 3145728\n"
    "buffer_load_dwordx4 v4, s[44:47], s73 offen lds\n"
    "s_add_u32 m0, s61, 77824\n"
    "s_mov_b32 s73, 3145856\n"
    "buffer_load_dwordx4 v4, s[44:47], s73 offen lds\n"
    "v_add_u32 v8, 81920, v6\n"
    "ds_read_b64_tr_b16 a[32:33], v8 offset:0\n"
    "ds_read_b64_tr_b16 a[34:35], v8 offset:512\n"
    "ds_read_b64_tr_b16 a[40:41], v8 offset:8\n"
    "ds_read_b64_tr_b16 a[42:43], v8 offset:520\n"
    "ds_read_b64_tr_b16 a[176:177], v8 offset:8192\n"
    "ds_read_b64_tr_b16 a[178:179], v8 offset:8704\n"
    "ds_read_b64_tr_b16 a[184:185], v8 offset:8200\n"
    "ds_read_b64_tr_b16 a[186:187], v8 offset:8712\n"
    "s_waitcnt vmcnt(28) lgkmcnt(0)\n"
    "s_barrier\n"
    "s_add_u32 m0, s61, 81920\n"
    "s_mov_b32 s73, 3407872\n"
    "buffer_load_dwordx4 v4, s[40:43], s73 offen lds\n"
    "s_add_u32 m0, s61, 86016\n"
    "s_mov_b32 s73, 3408000\n"
    "buffer_load_dwordx4 v4, s[40:43], s73 offen lds\n"
    "s_add_u32 m0, s61, 90112\n"
    "s_mov_b32 s73, 3407872\n"
    "buffer_load_dwordx4 v4, s[44:47], s73 offen lds\n"
    "s_add_u32 m0, s61, 94208\n"
    "s_mov_b32 s73, 3408000\n"
    "buffer_load_dwordx4 v4, s[44:47], s73 offen lds\n"
    "v_add_u32 v8, 98304, v6\n"
    "ds_read_b64_tr_b16 a[36:37], v8 offset:0\n"
    "ds_read_b64_tr_b16 a[38:39], v8 offset:512\n"
    "ds_read_b64_tr_b16 a[44:45], v8 offset:8\n"
    "ds_read_b64_tr_b16 a[46:47], v8 offset:520\n"
    "ds_read_b64_tr_b16 a[180:181], v8 offset:8192\n"
    "ds_read_b64_tr_b16 a[182:183], v8 offset:8704\n"
    "ds_read_b64_tr_b16 a[188:189], v8 offset:8200\n"
    "ds_read_b64_tr_b16 a[190:191], v8 offset:8712\n"
    "s_waitcnt vmcnt(28) lgkmcnt(0)\n"
    "s_barrier\n"
    "s_add_u32 m0, s61, 98304\n"
    "s_mov_b32 s73, 3670016\n"
    "buffer_load_dwordx4 v4, s[40:43], s73 offen lds\n"
    "s_add_u32 m0, s61, 102400\n"
    "s_mov_b32 s73, 3670144\n"
    "buffer_load_dwordx4 v4, s[40:43], s73 offen lds\n"
    "s_add_u32 m0, s61, 106496\n"
    "s_mov_b32 s73, 3670016\n"
    "buffer_load_dwordx4 v4, s[44:47], s73 offen lds\n"
    "s_add_u32 m0, s61, 110592\n"
    "s_mov_b32 s73, 3670144\n"
    "buffer_load_dwordx4 v4, s[44:47], s73 offen lds\n"
    "v_add_u32 v8, 114688, v6\n"
    "ds_read_b64_tr_b16 a[48:49], v8 offset:0\n"
    "ds_read_b64_tr_b16 a[50:51], v8 offset:512\n"
    "ds_read_b64_tr_b16 a[56:57], v8 offset:8\n"
    "ds_read_b64_tr_b16 a[58:59], v8 offset:520\n"
    "ds_read_b64_tr_b16 a[192:193], v8 offset:8192\n"
    "ds_read_b64_tr_b16 a[194:195], v8 offset:8704\n"
    "ds_read_b64_tr_b16 a[200:201], v8 offset:8200\n"
    "ds_read_b64_tr_b16 a[202:203], v8 offset:8712\n"
    "s_waitcnt vmcnt(28) lgkmcnt(0)\n"
    "s_barrier\n"
    "s_add_u32 m0, s61, 114688\n"
    "s_mov_b32 s73, 3932160\n"
    "buffer_load_dwordx4 v4, s[40:43], s73 offen lds\n"
    "s_add_u32 m0, s61, 118784\n"
    "s_mov_b32 s73, 3932288\n"
    "buffer_load_dwordx4 v4, s[40:43], s73 offen lds\n"
    "s_add_u32 m0, s61, 122880\n"
    "s_mov_b32 s73, 3932160\n"
    "buffer_load_dwordx4 v4, s[44:47], s73 offen lds\n"
    "s_add_u32 m0, s61, 126976\n"
    "s_mov_b32 s73, 3932288\n"
    "buffer_load_dwordx4 v4, s[44:47], s73 offen lds\n"
    "v_add_u32 v8, 131072, v6\n"
    "ds_read_b64_tr_b16 a[52:53], v8 offset:0\n"
    "ds_read_b64_tr_b16 a[54:55], v8 offset:512\n"
    "ds_read_b64_tr_b16 a[60:61], v8 offset:8\n"
    "ds_read_b64_tr_b16 a[62:63], v8 offset:520\n"
    "ds_read_b64_tr_b16 a[196:197], v8 offset:8192\n"
    "ds_read_b64_tr_b16 a[198:199], v8 offset:8704\n"
    "ds_read_b64_tr_b16 a[204:205], v8 offset:8200\n"
    "ds_read_b64_tr_b16 a[206:207], v8 offset:8712\n"
    "s_waitcnt vmcnt(28) lgkmcnt(0)\n"
    "s_barrier\n"
    "s_add_u32 m0, s61, 131072\n"
    "s_mov_b32 s73, 4194304\n"
    "buffer_load_dwordx4 v4, s[40:43], s73 offen lds\n"
    "s_add_u32 m0, s61, 135168\n"
    "s_mov_b32 s73, 4194432\n"
    "buffer_load_dwordx4 v4, s[40:43], s73 offen lds\n"
    "s_add_u32 m0, s61, 139264\n"
    "s_mov_b32 s73, 4194304\n"
    "buffer_load_dwordx4 v4, s[44:47], s73 offen lds\n"
    "s_add_u32 m0, s61, 143360\n"
    "s_mov_b32 s73, 4194432\n"
    "buffer_load_dwordx4 v4, s[44:47], s73 offen lds\n"
    "v_add_u32 v8, 147456, v6\n"
    "ds_read_b64_tr_b16 a[64:65], v8 offset:0\n"
    "ds_read_b64_tr_b16 a[66:67], v8 offset:512\n"
    "ds_read_b64_tr_b16 a[72:73], v8 offset:8\n"
    "ds_read_b64_tr_b16 a[74:75], v8 offset:520\n"
    "ds_read_b64_tr_b16 a[208:209], v8 offset:8192\n"
    "ds_read_b64_tr_b16 a[210:211], v8 offset:8704\n"
    "ds_read_b64_tr_b16 a[216:217], v8 offset:8200\n"
    "ds_read_b64_tr_b16 a[218:219], v8 offset:8712\n"
    "s_waitcnt vmcnt(28) lgkmcnt(0)\n"
    "s_barrier\n"
    "s_add_u32 m0, s61, 147456\n"
    "s_mov_b32 s73, 4456448\n"
    "buffer_load_dwordx4 v4, s[40:43], s73 offen lds\n"
    "s_add_u32 m0, s61, 151552\n"
    "s_mov_b32 s73, 4456576\n"
    "buffer_load_dwordx4 v4, s[40:43], s73 offen lds\n"
    "s_add_u32 m0, s61, 155648\n"
    "s_mov_b32 s73, 4456448\n"
    "buffer_load_dwordx4 v4, s[44:47], s73 offen lds\n"
    "s_add_u32 m0, s61, 159744\n"
    "s_mov_b32 s73, 4456576\n"
    "buffer_load_dwordx4 v4, s[44:47], s73 offen lds\n"
    "v_add_u32 v8, 16384, v6\n"
    "ds_read_b64_tr_b16 a[68:69], v8 offset:0\n"
    "ds_read_b64_tr_b16 a[70:71], v8 offset:512\n"
    "ds_read_b64_tr_b16 a[76:77], v8 offset:8\n"
    "ds_read_b64_tr_b16 a[78:79], v8 offset:520\n"
    "ds_read_b64_tr_b16 a[212:213], v8 offset:8192\n"
    "ds_read_b64_tr_b16 a[214:215], v8 offset:8704\n"
    "ds_read_b64_tr_b16 a[220:221], v8 offset:8200\n"
    "ds_read_b64_tr_b16 a[222:223], v8 offset:8712\n"
    "s_waitcnt vmcnt(28) lgkmcnt(0)\n"
    "s_barrier\n"
    "s_add_u32 m0, s61, 16384\n"
    "s_mov_b32 s73, 0\n"
    "buffer_load_dwordx4 v4, s[48:51], s73 offen lds\n"
    "s_add_u32 m0, s61, 20480\n"
    "s_mov_b32 s73, 128\n"
    "buffer_load_dwordx4 v4, s[48:51], s73 offen lds\n"
    "s_add_u32 m0, s61, 24576\n"
    "s_mov_b32 s73, 262144\n"
    "buffer_load_dwordx4 v4, s[48:51], s73 offen lds\n"
    "s_add_u32 m0, s61, 28672\n"
    "s_mov_b32 s73, 262272\n"
    "buffer_load_dwordx4 v4, s[48:51], s73 offen lds\n"
    "v_add_u32 v8, 32768, v6\n"
    "ds_read_b64_tr_b16 a[80:81], v8 offset:0\n"
    "ds_read_b64_tr_b16 a[82:83], v8 offset:512\n"
    "ds_read_b64_tr_b16 a[88:89], v8 offset:8\n"
    "ds_read_b64_tr_b16 a[90:91], v8 offset:520\n"
    "ds_read_b64_tr_b16 a[224:225], v8 offset:8192\n"
    "ds_read_b64_tr_b16 a[226:227], v8 offset:8704\n"
    "ds_read_b64_tr_b16 a[232:233], v8 offset:8200\n"
    "ds_read_b64_tr_b16 a[234:235], v8 offset:8712\n"
    "s_waitcnt vmcnt(28) lgkmcnt(0)\n"
    "s_barrier\n"
    "s_add_u32 m0, s61, 32768\n"
    "s_mov_b32 s73, 524288\n"
    "buffer_load_dwordx4 v4, s[48:51], s73 offen lds\n"
    "s_add_u32 m0, s61, 36864\n"
    "s_mov_b32 s73, 524416\n"
    "buffer_load_dwordx4 v4, s[48:51], s73 offen lds\n"
    "s_add_u32 m0, s61, 40960\n"
    "s_mov_b32 s73, 786432\n"
    "buffer_load_dwordx4 v4, s[48:51], s73 offen lds\n"
    "s_add_u32 m0, s61, 45056\n"
    "s_mov_b32 s73, 786560\n"
    "buffer_load_dwordx4 v4, s[48:51], s73 offen lds\n"
    "v_add_u32 v8, 49152, v6\n"
    "ds_read_b64_tr_b16 a[84:85], v8 offset:0\n"
    "ds_read_b64_tr_b16 a[86:87], v8 offset:512\n"
    "ds_read_b64_tr_b16 a[92:93], v8 offset:8\n"
    "ds_read_b64_tr_b16 a[94:95], v8 offset:520\n"
    "ds_read_b64_tr_b16 a[228:229], v8 offset:8192\n"
    "ds_read_b64_tr_b16 a[230:231], v8 offset:8704\n"
    "ds_read_b64_tr_b16 a[236:237], v8 offset:8200\n"
    "ds_read_b64_tr_b16 a[238:239], v8 offset:8712\n"
    "s_waitcnt vmcnt(28) lgkmcnt(0)\n"
    "s_barrier\n"
    "s_add_u32 m0, s61, 49152\n"
    "s_mov_b32 s73, 1048576\n"
    "buffer_load_dwordx4 v4, s[48:51], s73 offen lds\n"
    "s_add_u32 m0, s61, 53248\n"
    "s_mov_b32 s73, 1048704\n"
    "buffer_load_dwordx4 v4, s[48:51], s73 offen lds\n"
    "s_add_u32 m0, s61, 57344\n"
    "s_mov_b32 s73, 1310720\n"
    "buffer_load_dwordx4 v4, s[48:51], s73 offen lds\n"
    "s_add_u32 m0, s61, 61440\n"
    "s_mov_b32 s73, 1310848\n"
    "buffer_load_dwordx4 v4, s[48:51], s73 offen lds\n"
    "v_add_u32 v8, 65536, v6\n"
    "ds_read_b64_tr_b16 a[96:97], v8 offset:0\n"
    "ds_read_b64_tr_b16 a[98:99], v8 offset:512\n"
    "ds_read_b64_tr_b16 a[104:105], v8 offset:8\n"
    "ds_read_b64_tr_b16 a[106:107], v8 offset:520\n"
    "ds_read_b64_tr_b16 a[240:241], v8 offset:8192\n"
    "ds_read_b64_tr_b16 a[242:243], v8 offset:8704\n"
    "ds_read_b64_tr_b16 a[248:249], v8 offset:8200\n"
    "ds_read_b64_tr_b16 a[250:251], v8 offset:8712\n"
    "s_waitcnt vmcnt(28) lgkmcnt(0)\n"
    "s_barrier\n"
    "s_add_u32 m0, s61, 65536\n"
    "s_mov_b32 s73, 1572864\n"
    "buffer_load_dwordx4 v4, s[48:51], s73 offen lds\n"
    "s_add_u32 m0, s61, 69632\n"
    "s_mov_b32 s73, 1572992\n"
    "buffer_load_dwordx4 v4, s[48:51], s73 offen lds\n"
    "s_add_u32 m0, s61, 73728\n"
    "s_mov_b32 s73, 1835008\n"
    "buffer_load_dwordx4 v4, s[48:51], s73 offen lds\n"
    "s_add_u32 m0, s61, 77824\n"
    "s_mov_b32 s73, 1835136\n"
    "buffer_load_dwordx4 v4, s[48:51], s73 offen lds\n"
    "v_add_u32 v8, 81920, v6\n"
    "ds_read_b64_tr_b16 a[100:101], v8 offset:0\n"
    "ds_read_b64_tr_b16 a[102:103], v8 offset:512\n"
    "ds_read_b64_tr_b16 a[108:109], v8 offset:8\n"
    "ds_read_b64_tr_b16 a[110:111], v8 offset:520\n"
    "ds_read_b64_tr_b16 a[244:245], v8 offset:8192\n"
    "ds_read_b64_tr_b16 a[246:247], v8 offset:8704\n"
    "ds_read_b64_tr_b16 a[252:253], v8 offset:8200\n"
    "ds_read_b64_tr_b16 a[254:255], v8 offset:8712\n"
    "s_waitcnt vmcnt(28) lgkmcnt(0)\n"
    "s_barrier\n"
    "s_add_u32 m0, s61, 81920\n"
    "s_mov_b32 s73, 2097152\n"
    "buffer_load_dwordx4 v4, s[48:51], s73 offen lds\n"
    "s_add_u32 m0, s61, 86016\n"
    "s_mov_b32 s73, 2097280\n"
    "buffer_load_dwordx4 v4, s[48:51], s73 offen lds\n"
    "s_add_u32 m0, s61, 90112\n"
    "s_mov_b32 s73, 2359296\n"
    "buffer_load_dwordx4 v4, s[48:51], s73 offen lds\n"
    "s_add_u32 m0, s61, 94208\n"
    "s_mov_b32 s73, 2359424\n"
    "buffer_load_dwordx4 v4, s[48:51], s73 offen lds\n"
    "v_add_u32 v8, 98304, v6\n"
    "ds_read_b64_tr_b16 a[112:113], v8 offset:0\n"
    "ds_read_b64_tr_b16 a[114:115], v8 offset:512\n"
    "ds_read_b64_tr_b16 a[120:121], v8 offset:8\n"
    "ds_read_b64_tr_b16 a[122:123], v8 offset:520\n"
    "ds_read_b64_tr_b16 v[80:81], v8 offset:8192\n"
    "ds_read_b64_tr_b16 v[82:83], v8 offset:8704\n"
    "ds_read_b64_tr_b16 v[88:89], v8 offset:8200\n"
    "ds_read_b64_tr_b16 v[90:91], v8 offset:8712\n"
    "s_waitcnt vmcnt(28) lgkmcnt(0)\n"
    "s_barrier\n"
    "s_add_u32 m0, s61, 98304\n"
    "s_mov_b32 s73, 2621440\n"
    "buffer_load_dwordx4 v4, s[48:51], s73 offen lds\n"
    "s_add_u32 m0, s61, 102400\n"
    "s_mov_b32 s73, 2621568\n"
    "buffer_load_dwordx4 v4, s[48:51], s73 offen lds\n"
    "s_add_u32 m0, s61, 106496\n"
    "s_mov_b32 s73, 2883584\n"
    "buffer_load_dwordx4 v4, s[48:51], s73 offen lds\n"
    "s_add_u32 m0, s61, 110592\n"
    "s_mov_b32 s73, 2883712\n"
    "buffer_load_dwordx4 v4, s[48:51], s73 offen lds\n"
    "v_add_u32 v8, 114688, v6\n"
    "ds_read_b64_tr_b16 a[116:117], v8 offset:0\n"
    "ds_read_b64_tr_b16 a[118:119], v8 offset:512\n"
    "ds_read_b64_tr_b16 a[124:125], v8 offset:8\n"
    "ds_read_b64_tr_b16 a[126:127], v8 offset:520\n"
    "ds_read_b64_tr_b16 v[84:85], v8 offset:8192\n"
    "ds_read_b64_tr_b16 v[86:87], v8 offset:8704\n"
    "ds_read_b64_tr_b16 v[92:93], v8 offset:8200\n"
    "ds_read_b64_tr_b16 v[94:95], v8 offset:8712\n"
    "s_waitcnt vmcnt(28) lgkmcnt(0)\n"
    "s_barrier\n"
    "s_add_u32 m0, s61, 114688\n"
    "s_mov_b32 s73, 3145728\n"
    "buffer_load_dwordx4 v4, s[48:51], s73 offen lds\n"
    "s_add_u32 m0, s61, 118784\n"
    "s_mov_b32 s73, 3145856\n"
    "buffer_load_dwordx4 v4, s[48:51], s73 offen lds\n"
    "s_add_u32 m0, s61, 122880\n"
    "s_mov_b32 s73, 3407872\n"
    "buffer_load_dwordx4 v4, s[48:51], s73 offen lds\n"
    "s_add_u32 m0, s61, 126976\n"
    "s_mov_b32 s73, 3408000\n"
    "buffer_load_dwordx4 v4, s[48:51], s73 offen lds\n"
    "v_add_u32 v8, 131072, v6\n"
    "ds_read_b64_tr_b16 a[128:129], v8 offset:0\n"
    "ds_read_b64_tr_b16 a[130:131], v8 offset:512\n"
    "ds_read_b64_tr_b16 a[136:137], v8 offset:8\n"
    "ds_read_b64_tr_b16 a[138:139], v8 offset:520\n"
    "ds_read_b64_tr_b16 v[96:97], v8 offset:8192\n"
    "ds_read_b64_tr_b16 v[98:99], v8 offset:8704\n"
    "ds_read_b64_tr_b16 v[104:105], v8 offset:8200\n"
    "ds_read_b64_tr_b16 v[106:107], v8 offset:8712\n"
    "s_waitcnt vmcnt(28) lgkmcnt(0)\n"
    "s_barrier\n"
    "s_add_u32 m0, s61, 131072\n"
    "s_mov_b32 s73, 3670016\n"
    "buffer_load_dwordx4 v4, s[48:51], s73 offen lds\n"
    "s_add_u32 m0, s61, 135168\n"
    "s_mov_b32 s73, 3670144\n"
    "buffer_load_dwordx4 v4, s[48:51], s73 offen lds\n"
    "s_add_u32 m0, s61, 139264\n"
    "s_mov_b32 s73, 3932160\n"
    "buffer_load_dwordx4 v4, s[48:51], s73 offen lds\n"
    "s_add_u32 m0, s61, 143360\n"
    "s_mov_b32 s73, 3932288\n"
    "buffer_load_dwordx4 v4, s[48:51], s73 offen lds\n"
    "v_add_u32 v8, 147456, v6\n"
    "ds_read_b64_tr_b16 a[132:133], v8 offset:0\n"
    "ds_read_b64_tr_b16 a[134:135], v8 offset:512\n"
    "ds_read_b64_tr_b16 a[140:141], v8 offset:8\n"
    "ds_read_b64_tr_b16 a[142:143], v8 offset:520\n"
    "ds_read_b64_tr_b16 v[100:101], v8 offset:8192\n"
    "ds_read_b64_tr_b16 v[102:103], v8 offset:8704\n"
    "ds_read_b64_tr_b16 v[108:109], v8 offset:8200\n"
    "ds_read_b64_tr_b16 v[110:111], v8 offset:8712\n"
    "s_waitcnt vmcnt(28) lgkmcnt(0)\n"
    "s_barrier\n"
    "s_add_u32 m0, s61, 147456\n"
    "s_mov_b32 s73, 4194304\n"
    "buffer_load_dwordx4 v4, s[48:51], s73 offen lds\n"
    "s_add_u32 m0, s61, 151552\n"
    "s_mov_b32 s73, 4194432\n"
    "buffer_load_dwordx4 v4, s[48:51], s73 offen lds\n"
    "s_add_u32 m0, s61, 155648\n"
    "s_mov_b32 s73, 4456448\n"
    "buffer_load_dwordx4 v4, s[48:51], s73 offen lds\n"
    "s_add_u32 m0, s61, 159744\n"
    "s_mov_b32 s73, 4456576\n"
    "buffer_load_dwordx4 v4, s[48:51], s73 offen lds\n"
    "v_add_u32 v8, 16384, v6\n"
    "ds_read_b64_tr_b16 v[16:17], v8 offset:0\n"
    "ds_read_b64_tr_b16 v[18:19], v8 offset:512\n"
    "ds_read_b64_tr_b16 v[24:25], v8 offset:8\n"
    "ds_read_b64_tr_b16 v[26:27], v8 offset:520\n"
    "ds_read_b64_tr_b16 v[20:21], v8 offset:8192\n"
    "ds_read_b64_tr_b16 v[22:23], v8 offset:8704\n"
    "ds_read_b64_tr_b16 v[28:29], v8 offset:8200\n"
    "ds_read_b64_tr_b16 v[30:31], v8 offset:8712\n"
    "s_waitcnt vmcnt(28) lgkmcnt(0)\n"
    "s_barrier\n"
    "v_add_u32 v14, 0, v1\n"
    "ds_write_b128 v14, v[112:115] offset:0\n"
    "ds_write_b128 v14, v[116:119] offset:1024\n"
    "ds_write_b128 v14, v[24:27] offset:8192\n"
    "ds_write_b128 v14, v[28:31] offset:9216\n"
    "v_add_u32 v8, 32768, v6\n"
    "ds_read_b64_tr_b16 v[32:33], v8 offset:0\n"
    "ds_read_b64_tr_b16 v[34:35], v8 offset:512\n"
    "ds_read_b64_tr_b16 v[40:41], v8 offset:8\n"
    "ds_read_b64_tr_b16 v[42:43], v8 offset:520\n"
    "ds_read_b64_tr_b16 v[36:37], v8 offset:8192\n"
    "ds_read_b64_tr_b16 v[38:39], v8 offset:8704\n"
    "ds_read_b64_tr_b16 v[44:45], v8 offset:8200\n"
    "ds_read_b64_tr_b16 v[46:47], v8 offset:8712\n"
    "s_waitcnt vmcnt(24) lgkmcnt(0)\n"
    "s_barrier\n"
    "v_mov_b64 v[112:113], v[16:17]\n"
    "v_mov_b64 v[114:115], v[18:19]\n"
    "v_mov_b64 v[116:117], v[20:21]\n"
    "v_mov_b64 v[118:119], v[22:23]\n"
    "v_add_u32 v14, 16384, v1\n"
    "ds_write_b128 v14, v[120:123] offset:0\n"
    "ds_write_b128 v14, v[124:127] offset:1024\n"
    "ds_write_b128 v14, v[40:43] offset:8192\n"
    "ds_write_b128 v14, v[44:47] offset:9216\n"
    "v_add_u32 v8, 49152, v6\n"
    "ds_read_b64_tr_b16 v[16:17], v8 offset:0\n"
    "ds_read_b64_tr_b16 v[18:19], v8 offset:512\n"
    "ds_read_b64_tr_b16 v[24:25], v8 offset:8\n"
    "ds_read_b64_tr_b16 v[26:27], v8 offset:520\n"
    "ds_read_b64_tr_b16 v[20:21], v8 offset:8192\n"
    "ds_read_b64_tr_b16 v[22:23], v8 offset:8704\n"
    "ds_read_b64_tr_b16 v[28:29], v8 offset:8200\n"
    "ds_read_b64_tr_b16 v[30:31], v8 offset:8712\n"
    "s_waitcnt vmcnt(20) lgkmcnt(0)\n"
    "s_barrier\n"
    "v_mov_b64 v[120:121], v[32:33]\n"
    "v_mov_b64 v[122:123], v[34:35]\n"
    "v_mov_b64 v[124:125], v[36:37]\n"
    "v_mov_b64 v[126:127], v[38:39]\n"
    "v_add_u32 v14, 32768, v1\n"
    "ds_write_b128 v14, v[128:131] offset:0\n"
    "ds_write_b128 v14, v[132:135] offset:1024\n"
    "ds_write_b128 v14, v[24:27] offset:8192\n"
    "ds_write_b128 v14, v[28:31] offset:9216\n"
    "v_add_u32 v8, 65536, v6\n"
    "ds_read_b64_tr_b16 v[32:33], v8 offset:0\n"
    "ds_read_b64_tr_b16 v[34:35], v8 offset:512\n"
    "ds_read_b64_tr_b16 v[40:41], v8 offset:8\n"
    "ds_read_b64_tr_b16 v[42:43], v8 offset:520\n"
    "ds_read_b64_tr_b16 v[36:37], v8 offset:8192\n"
    "ds_read_b64_tr_b16 v[38:39], v8 offset:8704\n"
    "ds_read_b64_tr_b16 v[44:45], v8 offset:8200\n"
    "ds_read_b64_tr_b16 v[46:47], v8 offset:8712\n"
    "s_waitcnt vmcnt(16) lgkmcnt(0)\n"
    "s_barrier\n"
    "v_mov_b64 v[128:129], v[16:17]\n"
    "v_mov_b64 v[130:131], v[18:19]\n"
    "v_mov_b64 v[132:133], v[20:21]\n"
    "v_mov_b64 v[134:135], v[22:23]\n"
    "v_add_u32 v14, 49152, v1\n"
    "ds_write_b128 v14, v[136:139] offset:0\n"
    "ds_write_b128 v14, v[140:143] offset:1024\n"
    "ds_write_b128 v14, v[40:43] offset:8192\n"
    "ds_write_b128 v14, v[44:47] offset:9216\n"
    "v_add_u32 v8, 81920, v6\n"
    "ds_read_b64_tr_b16 v[16:17], v8 offset:0\n"
    "ds_read_b64_tr_b16 v[18:19], v8 offset:512\n"
    "ds_read_b64_tr_b16 v[24:25], v8 offset:8\n"
    "ds_read_b64_tr_b16 v[26:27], v8 offset:520\n"
    "ds_read_b64_tr_b16 v[20:21], v8 offset:8192\n"
    "ds_read_b64_tr_b16 v[22:23], v8 offset:8704\n"
    "ds_read_b64_tr_b16 v[28:29], v8 offset:8200\n"
    "ds_read_b64_tr_b16 v[30:31], v8 offset:8712\n"
    "s_waitcnt vmcnt(12) lgkmcnt(0)\n"
    "s_barrier\n"
    "v_mov_b64 v[136:137], v[32:33]\n"
    "v_mov_b64 v[138:139], v[34:35]\n"
    "v_mov_b64 v[140:141], v[36:37]\n"
    "v_mov_b64 v[142:143], v[38:39]\n"
    "v_add_u32 v14, 65536, v1\n"
    "ds_write_b128 v14, v[144:147] offset:0\n"
    "ds_write_b128 v14, v[148:151] offset:1024\n"
    "ds_write_b128 v14, v[24:27] offset:8192\n"
    "ds_write_b128 v14, v[28:31] offset:9216\n"
    "v_add_u32 v8, 98304, v6\n"
    "ds_read_b64_tr_b16 v[32:33], v8 offset:0\n"
    "ds_read_b64_tr_b16 v[34:35], v8 offset:512\n"
    "ds_read_b64_tr_b16 v[40:41], v8 offset:8\n"
    "ds_read_b64_tr_b16 v[42:43], v8 offset:520\n"
    "ds_read_b64_tr_b16 v[36:37], v8 offset:8192\n"
    "ds_read_b64_tr_b16 v[38:39], v8 offset:8704\n"
    "ds_read_b64_tr_b16 v[44:45], v8 offset:8200\n"
    "ds_read_b64_tr_b16 v[46:47], v8 offset:8712\n"
    "s_waitcnt vmcnt(8) lgkmcnt(0)\n"
    "s_barrier\n"
    "v_mov_b64 v[144:145], v[16:17]\n"
    "v_mov_b64 v[146:147], v[18:19]\n"
    "v_mov_b64 v[148:149], v[20:21]\n"
    "v_mov_b64 v[150:151], v[22:23]\n"
    "v_add_u32 v14, 81920, v1\n"
    "ds_write_b128 v14, v[152:155] offset:0\n"
    "ds_write_b128 v14, v[156:159] offset:1024\n"
    "ds_write_b128 v14, v[40:43] offset:8192\n"
    "ds_write_b128 v14, v[44:47] offset:9216\n"
    "v_add_u32 v8, 114688, v6\n"
    "ds_read_b64_tr_b16 v[16:17], v8 offset:0\n"
    "ds_read_b64_tr_b16 v[18:19], v8 offset:512\n"
    "ds_read_b64_tr_b16 v[24:25], v8 offset:8\n"
    "ds_read_b64_tr_b16 v[26:27], v8 offset:520\n"
    "ds_read_b64_tr_b16 v[20:21], v8 offset:8192\n"
    "ds_read_b64_tr_b16 v[22:23], v8 offset:8704\n"
    "ds_read_b64_tr_b16 v[28:29], v8 offset:8200\n"
    "ds_read_b64_tr_b16 v[30:31], v8 offset:8712\n"
    "s_waitcnt vmcnt(4) lgkmcnt(0)\n"
    "s_barrier\n"
    "v_mov_b64 v[152:153], v[32:33]\n"
    "v_mov_b64 v[154:155], v[34:35]\n"
    "v_mov_b64 v[156:157], v[36:37]\n"
    "v_mov_b64 v[158:159], v[38:39]\n"
    "v_add_u32 v14, 98304, v1\n"
    "ds_write_b128 v14, v[160:163] offset:0\n"
    "ds_write_b128 v14, v[164:167] offset:1024\n"
    "ds_write_b128 v14, v[168:171] offset:8192\n"
    "ds_write_b128 v14, v[172:175] offset:9216\n"
    "v_add_u32 v8, 131072, v6\n"
    "ds_read_b64_tr_b16 v[32:33], v8 offset:0\n"
    "ds_read_b64_tr_b16 v[34:35], v8 offset:512\n"
    "ds_read_b64_tr_b16 v[40:41], v8 offset:8\n"
    "ds_read_b64_tr_b16 v[42:43], v8 offset:520\n"
    "ds_read_b64_tr_b16 v[36:37], v8 offset:8192\n"
    "ds_read_b64_tr_b16 v[38:39], v8 offset:8704\n"
    "ds_read_b64_tr_b16 v[44:45], v8 offset:8200\n"
    "ds_read_b64_tr_b16 v[46:47], v8 offset:8712\n"
    "s_waitcnt vmcnt(0) lgkmcnt(0)\n"
    "s_barrier\n"
    "v_mov_b64 v[160:161], v[16:17]\n"
    "v_mov_b64 v[162:163], v[18:19]\n"
    "v_mov_b64 v[164:165], v[20:21]\n"
    "v_mov_b64 v[166:167], v[22:23]\n"
    "v_mov_b64 v[168:169], v[24:25]\n"
    "v_mov_b64 v[170:171], v[26:27]\n"
    "v_mov_b64 v[172:173], v[28:29]\n"
    "v_mov_b64 v[174:175], v[30:31]\n"
    "v_add_u32 v14, 114688, v1\n"
    "ds_write_b128 v14, v[176:179] offset:0\n"
    "ds_write_b128 v14, v[180:183] offset:1024\n"
    "ds_write_b128 v14, v[184:187] offset:8192\n"
    "ds_write_b128 v14, v[188:191] offset:9216\n"
    "v_add_u32 v8, 147456, v6\n"
    "ds_read_b64_tr_b16 v[16:17], v8 offset:0\n"
    "ds_read_b64_tr_b16 v[18:19], v8 offset:512\n"
    "ds_read_b64_tr_b16 v[24:25], v8 offset:8\n"
    "ds_read_b64_tr_b16 v[26:27], v8 offset:520\n"
    "ds_read_b64_tr_b16 v[20:21], v8 offset:8192\n"
    "ds_read_b64_tr_b16 v[22:23], v8 offset:8704\n"
    "ds_read_b64_tr_b16 v[28:29], v8 offset:8200\n"
    "ds_read_b64_tr_b16 v[30:31], v8 offset:8712\n"
    "s_waitcnt lgkmcnt(0)\n"
    "s_barrier\n"
    "v_mov_b64 v[176:177], v[32:33]\n"
    "v_mov_b64 v[178:179], v[34:35]\n"
    "v_mov_b64 v[180:181], v[36:37]\n"
    "v_mov_b64 v[182:183], v[38:39]\n"
    "v_mov_b64 v[184:185], v[40:41]\n"
    "v_mov_b64 v[186:187], v[42:43]\n"
    "v_mov_b64 v[188:189], v[44:45]\n"
    "v_mov_b64 v[190:191], v[46:47]\n"
    "v_add_u32 v14, 131072, v1\n"
    "ds_write_b128 v14, v[192:195] offset:0\n"
    "ds_write_b128 v14, v[196:199] offset:1024\n"
    "ds_write_b128 v14, v[200:203] offset:8192\n"
    "ds_write_b128 v14, v[204:207] offset:9216\n"
    "s_waitcnt lgkmcnt(0)\n"
    "v_mov_b64 v[192:193], v[16:17]\n"
    "v_mov_b64 v[194:195], v[18:19]\n"
    "v_mov_b64 v[196:197], v[20:21]\n"
    "v_mov_b64 v[198:199], v[22:23]\n"
    "v_mov_b64 v[200:201], v[24:25]\n"
    "v_mov_b64 v[202:203], v[26:27]\n"
    "v_mov_b64 v[204:205], v[28:29]\n"
    "v_mov_b64 v[206:207], v[30:31]\n"
    "s_mov_b32 s69, 262144\n"
    "s_mov_b32 s84, 0x3e0293ee\n"
    "v_lshlrev_b32 v8, 3, v0\n"
    "v_mov_b32 v9, 0x80000000\n"
    "v_cmp_gt_u32 vcc, 192, v0\n"
    "s_nop 1\n"
    "v_cndmask_b32 v8, v9, v8, vcc\n"
    ".Lrec_init_%=:\n"
    "buffer_load_dword v9, v8, s[36:39], s66 offen sc1\n"
    "s_waitcnt vmcnt(0)\n"
    "v_cmp_gt_i32 vcc, 0, v9\n"
    "s_cbranch_vccz .Lrec_init1_%=\n"
    "s_sleep 2\n"
    "s_sub_u32 s69, s69, 1\n"
    "s_cbranch_scc0 .Lrec_init_%=\n"
    "s_mov_b32 s69, 0\n"
    ".Lrec_init1_%=:\n"
    "v_readfirstlane_b32 s73, v9\n"
    "s_nop 0\n"
    "s_and_b32 s73, s73, 1\n"
    "s_lshl_b32 s73, s73, 13\n"
    "s_add_u32 s58, s58, s73\n"
    "s_lshl_b32 s86, s61, 7\n"
    "s_add_u32 s86, s86, s62\n"
    "s_add_u32 s86, s86, s58\n"
    "s_lshl_b32 s91, s61, 4\n"
    "s_add_u32 s91, s91, s62\n"
    "s_add_u32 s91, s91, s58\n"
    "s_add_u32 s91, s91, 524288\n"
    "s_lshl_b32 s87, s63, 14\n"
    "s_add_u32 s87, s87, s58\n"
    "s_and_b32 s88, s63, 31\n"
    "s_lshl_b32 s88, s88, 8\n"
    "s_lshr_b32 s73, s63, 5\n"
    "s_lshl_b32 s73, s73, 14\n"
    "s_add_u32 s88, s88, s73\n"
    "s_add_u32 s88, s88, s58\n"
    "s_add_u32 s88, s88, 1179648\n"
    "s_cmp_lt_i32 s63, 0\n"
    "s_cselect_b32 s88, 0x80000000, s88\n"
    "s_lshl_b32 s83, s64, 15\n"
    "s_add_u32 s83, s83, s58\n"
    "s_add_u32 s83, s83, 1179648\n"
    "s_lshl_b32 s3, s62, 6\n"
    "s_lshl_b32 s73, s61, 1\n"
    "s_add_u32 s3, s3, s73\n"
    "s_add_u32 s3, s3, s58\n"
    "s_add_u32 s89, s58, s67\n"
    "s_cmp_lt_i32 s67, 0\n"
    "s_cselect_b32 s89, 0x80000000, s89\n"
    "s_cmp_eq_u32 s61, 3072\n"
    "s_cselect_b32 s34, 1, 3\n"
    "s_mov_b32 s35, 131072\n"
    "s_cselect_b32 s35, 589824, s35\n"
    "s_add_u32 s92, s58, 1703936\n"
    "s_lshr_b32 s94, s62, 2\n"
    "s_lshr_b32 s73, s61, 8\n"
    "s_add_u32 s94, s94, s73\n"
    "s_mov_b32 s68, -1\n"
    "s_mov_b32 s81, 0\n"
    "s_and_b32 s80, s68, s34\n"
    "s_mul_i32 s80, s80, s35\n"
    "s_add_u32 s80, s80, s89\n"
    "s_and_b32 s2, s68, 1\n"
    "s_mul_i32 s2, s2, 589824\n"
    "s_add_u32 s2, s2, s3\n"
    "s_add_u32 s68, s68, 1\n"
    "s_and_b32 s70, s68, 3\n"
    "s_lshl_b32 s70, s70, 17\n"
    "s_add_u32 s70, s70, s83\n"
    "s_add_u32 s71, s70, 4096\n"
    "s_add_u32 s72, s70, 16384\n"
    "s_lshr_b32 s93, s68, 2\n"
    "s_lshl_b32 s93, s93, 14\n"
    "s_and_b32 s90, s68, 3\n"
    "s_lshl_b32 s90, s90, 11\n"
    "s_add_u32 s93, s93, s90\n"
    "s_add_u32 s93, s93, s92\n"
    "s_and_b32 s76, s68, 7\n"
    "s_mul_i32 s76, s76, 196608\n"
    "s_add_u32 s76, s76, s57\n"
    "v_mov_b64 v[40:41], 0\n"
    "v_mov_b64 v[42:43], 0\n"
    "v_mov_b64 v[44:45], 0\n"
    "v_mov_b64 v[46:47], 0\n"
    "v_mov_b64 v[48:49], 0\n"
    "v_mov_b64 v[50:51], 0\n"
    "v_mov_b64 v[52:53], 0\n"
    "v_mov_b64 v[54:55], 0\n"
    "v_mov_b64 v[56:57], 0\n"
    "v_mov_b64 v[58:59], 0\n"
    "v_mov_b64 v[60:61], 0\n"
    "v_mov_b64 v[62:63], 0\n"
    "s_waitcnt lgkmcnt(0)\n"
    "s_barrier\n"
    "s_branch .Lrec_entry_%=\n"
    ".Lrec_LOOPB_%=:\n"
    ".Lrec_top_%=:\n"
    "buffer_load_dwordx4 v[4:7], v0, s[36:39], s70 offen sc1\n"
    "s_nop 3\n"
    "buffer_load_dwordx4 v[8:11], v0, s[36:39], s70 offen offset:1024 sc1\n"
    "s_nop 3\n"
    "buffer_load_dwordx4 v[12:15], v0, s[36:39], s70 offen offset:2048 sc1\n"
    "s_nop 3\n"
    "buffer_load_dwordx4 v[16:19], v0, s[36:39], s70 offen offset:3072 sc1\n"
    "s_nop 3\n"
    "buffer_load_dwordx4 v[20:23], v0, s[36:39], s71 offen sc1\n"
    "s_nop 3\n"
    "buffer_load_dwordx4 v[24:27], v0, s[36:39], s71 offen offset:1024 sc1\n"
    "s_nop 3\n"
    "buffer_load_dwordx4 v[28:31], v0, s[36:39], s71 offen offset:2048 sc1\n"
    "s_nop 3\n"
    "buffer_load_dwordx4 v[32:35], v0, s[36:39], s71 offen offset:3072 sc1\n"
    "s_sleep 1\n"
    "buffer_load_dwordx4 v[36:39], v0, s[36:39], s72 offen sc1\n"
    "s_waitcnt vmcnt(8)\n"
    "v_max3_u32 v3, v4, v5, v6\n"
    "v_max_u32 v3, v3, v7\n"
    "v_cmp_eq_u32 vcc, -1, v3\n"
    "s_cbranch_vccnz .Lrec_retry0_%=\n"
    ".Lrec_back0_%=:\n"
    "v_smfmac_f32_16x16x64_bf16 v[40:43], v[4:7], a[0:7], v2\n"
    "v_smfmac_f32_16x16x64_bf16 v[44:47], v[4:7], a[8:15], v2\n"
    "s_waitcnt vmcnt(7)\n"
    "v_max3_u32 v3, v8, v9, v10\n"
    "v_smfmac_f32_16x16x64_bf16 v[48:51], v[4:7], a[144:151], v2\n"
    "v_max_u32 v3, v3, v11\n"
    "v_smfmac_f32_16x16x64_bf16 v[52:55], v[4:7], a[152:159], v2\n"
    "v_cmp_eq_u32 vcc, -1, v3\n"
    "v_smfmac_f32_16x16x64_bf16 v[56:59], v[4:7], v[112:119], v2\n"
    "s_waitcnt lgkmcnt(0)\n"
    "v_smfmac_f32_16x16x64_bf16 v[60:63], v[4:7], v[72:79], v2\n"
    "s_cbranch_vccnz .Lrec_retry1_%=\n"
    ".Lrec_back1_%=:\n"
    "ds_read_b128 v[72:75], v1 offset:24576\n"
    "v_smfmac_f32_16x16x64_bf16 v[40:43], v[8:11], a[16:23], v2\n"
    "s_waitcnt vmcnt(6)\n"
    "ds_read_b128 v[76:79], v1 offset:25600\n"
    "v_max3_u32 v3, v12, v13, v14\n"
    "v_smfmac_f32_16x16x64_bf16 v[44:47], v[8:11], a[24:31], v2\n"
    "v_max_u32 v3, v3, v15\n"
    "v_smfmac_f32_16x16x64_bf16 v[48:51], v[8:11], a[160:167], v2\n"
    "v_cmp_eq_u32 vcc, -1, v3\n"
    "v_smfmac_f32_16x16x64_bf16 v[52:55], v[8:11], a[168:175], v2\n"
    "v_smfmac_f32_16x16x64_bf16 v[56:59], v[8:11], v[120:127], v2\n"
    "s_cbranch_vccnz .Lrec_retry2_%=\n"
    ".Lrec_back2_%=:\n"
    "s_waitcnt lgkmcnt(0)\n"
    "v_smfmac_f32_16x16x64_bf16 v[60:63], v[8:11], v[72:79], v2\n"
    "s_waitcnt vmcnt(5)\n"
    "ds_read_b128 v[72:75], v1 offset:40960\n"
    "v_max3_u32 v3, v16, v17, v18\n"
    "v_smfmac_f32_16x16x64_bf16 v[40:43], v[12:15], a[32:39], v2\n"
    "ds_read_b128 v[76:79], v1 offset:41984\n"
    "v_max_u32 v3, v3, v19\n"
    "v_smfmac_f32_16x16x64_bf16 v[44:47], v[12:15], a[40:47], v2\n"
    "s_add_u32 s73, s75, 8192\n"
    "v_cmp_eq_u32 vcc, -1, v3\n"
    "v_smfmac_f32_16x16x64_bf16 v[48:51], v[12:15], a[176:183], v2\n"
    "buffer_load_dwordx4 v[4:7], v0, s[36:39], s73 offen sc1\n"
    "v_smfmac_f32_16x16x64_bf16 v[52:55], v[12:15], a[184:191], v2\n"
    "s_cbranch_vccnz .Lrec_retry3_%=\n"
    ".Lrec_back3_%=:\n"
    "v_smfmac_f32_16x16x64_bf16 v[56:59], v[12:15], v[128:135], v2\n"
    "s_waitcnt vmcnt(5)\n"
    "s_waitcnt lgkmcnt(0)\n"
    "v_max3_u32 v3, v20, v21, v22\n"
    "v_smfmac_f32_16x16x64_bf16 v[60:63], v[12:15], v[72:79], v2\n"
    "v_add_u32 v1, 57344, v1\n"
    "ds_read_b128 v[72:75], v1 offset:0\n"
    "v_max_u32 v3, v3, v23\n"
    "v_smfmac_f32_16x16x64_bf16 v[40:43], v[16:19], a[48:55], v2\n"
    "ds_read_b128 v[76:79], v1 offset:1024\n"
    "v_cmp_eq_u32 vcc, -1, v3\n"
    "v_smfmac_f32_16x16x64_bf16 v[44:47], v[16:19], a[56:63], v2\n"
    "buffer_load_dwordx4 v[8:11], v0, s[36:39], s73 offen offset:1024 sc1\n"
    "v_smfmac_f32_16x16x64_bf16 v[48:51], v[16:19], a[192:199], v2\n"
    "s_cbranch_vccnz .Lrec_retry4_%=\n"
    ".Lrec_back4_%=:\n"
    "v_smfmac_f32_16x16x64_bf16 v[52:55], v[16:19], a[200:207], v2\n"
    "s_waitcnt vmcnt(5)\n"
    "s_waitcnt lgkmcnt(0)\n"
    "v_max3_u32 v3, v24, v25, v26\n"
    "v_smfmac_f32_16x16x64_bf16 v[60:63], v[16:19], v[72:79], v2\n"
    "ds_read_b128 v[72:75], v1 offset:16384\n"
    "v_max_u32 v3, v3, v27\n"
    "v_smfmac_f32_16x16x64_bf16 v[40:43], v[20:23], a[64:71], v2\n"
    "ds_read_b128 v[76:79], v1 offset:17408\n"
    "v_cmp_eq_u32 vcc, -1, v3\n"
    "v_smfmac_f32_16x16x64_bf16 v[44:47], v[20:23], a[72:79], v2\n"
    "v_smfmac_f32_16x16x64_bf16 v[48:51], v[20:23], a[208:215], v2\n"
    "s_cbranch_vccnz .Lrec_retry5_%=\n"
    ".Lrec_back5_%=:\n"
    "v_smfmac_f32_16x16x64_bf16 v[52:55], v[20:23], a[216:223], v2\n"
    "s_waitcnt vmcnt(4)\n"
    "s_waitcnt lgkmcnt(0)\n"
    "v_max3_u32 v3, v28, v29, v30\n"
    "v_smfmac_f32_16x16x64_bf16 v[60:63], v[20:23], v[72:79], v2\n"
    "s_waitcnt vmcnt(3)\n"
    "ds_read_b128 v[72:75], v1 offset:32768\n"
    "v_max3_u32 v3, v3, v31, v32\n"
    "v_smfmac_f32_16x16x64_bf16 v[40:43], v[24:27], a[80:87], v2\n"
    "ds_read_b128 v[76:79], v1 offset:33792\n"
    "v_max3_u32 v3, v3, v33, v34\n"
    "v_smfmac_f32_16x16x64_bf16 v[44:47], v[24:27], a[88:95], v2\n"
    "ds_read_b128 v[12:15], v0 offset:12288\n"
    "v_max_u32 v3, v3, v35\n"
    "v_smfmac_f32_16x16x64_bf16 v[48:51], v[24:27], a[224:231], v2\n"
    "v_cmp_eq_u32 vcc, -1, v3\n"
    "v_smfmac_f32_16x16x64_bf16 v[52:55], v[24:27], a[232:239], v2\n"
    "s_waitcnt lgkmcnt(1)\n"
    "v_smfmac_f32_16x16x64_bf16 v[60:63], v[24:27], v[72:79], v2\n"
    "s_cbranch_vccnz .Lrec_retry6_%=\n"
    ".Lrec_back6_%=:\n"
    "v_smfmac_f32_16x16x64_bf16 v[40:43], v[28:31], a[96:103], v2\n"
    "s_waitcnt vmcnt(2)\n"
    "v_max3_u32 v3, v36, v37, v38\n"
    "v_smfmac_f32_16x16x64_bf16 v[44:47], v[28:31], a[104:111], v2\n"
    "ds_read_b128 v[72:75], v0 offset:13312\n"
    "v_max_u32 v3, v3, v39\n"
    "v_smfmac_f32_16x16x64_bf16 v[40:43], v[32:35], a[112:119], v2\n"
    "v_cmp_eq_u32 vcc, -1, v3\n"
    "v_smfmac_f32_16x16x64_bf16 v[44:47], v[32:35], a[120:127], v2\n"
    "v_smfmac_f32_16x16x64_bf16 v[48:51], v[28:31], a[240:247], v2\n"
    "s_cbranch_vccnz .Lrec_retry8_%=\n"
    ".Lrec_back8_%=:\n"
    "v_smfmac_f32_16x16x64_bf16 v[40:43], v[36:39], a[128:135], v2\n"
    "s_waitcnt lgkmcnt(0)\n"
    "s_memrealtime s[28:29]\n"
    "v_add_u32 v79, s61, v0\n"
    "v_smfmac_f32_16x16x64_bf16 v[44:47], v[36:39], a[136:143], v2\n"
    "s_add_u32 s74, s93, s94\n"
    "v_xor_b32 v76, 1024, v79\n"
    "v_smfmac_f32_16x16x64_bf16 v[52:55], v[28:31], a[248:255], v2\n"
    "s_memrealtime s[30:31]\n"
    "v_xor_b32 v77, 2048, v79\n"
    "v_smfmac_f32_16x16x64_bf16 v[48:51], v[32:35], v[80:87], v2\n"
    "s_memrealtime s[96:97]\n"
    "v_cvt_pk_bf16_f32 v40, v40, v41\n"
    "v_smfmac_f32_16x16x64_bf16 v[52:55], v[32:35], v[88:95], v2\n"
    "s_memtime s[32:33]\n"
    "v_cvt_pk_bf16_f32 v41, v42, v43\n"
    "v_smfmac_f32_16x16x64_bf16 v[48:51], v[36:39], v[96:103], v2\n"
    "s_memrealtime s[98:99]\n"
    "v_cvt_pk_bf16_f32 v42, v44, v45\n"
    "v_cvt_pk_bf16_f32 v43, v46, v47\n"
    "v_smfmac_f32_16x16x64_bf16 v[52:55], v[36:39], v[104:111], v2\n"
    "v_mfma_f32_16x16x32_bf16 v[44:47], v[40:43], v[12:15], 0\n"
    "s_lshr_b32 s73, s61, 1\n"
    "v_cvt_pk_bf16_f32 v48, v48, v49\n"
    "v_cvt_pk_bf16_f32 v49, v50, v51\n"
    "v_xor_b32 v78, 3072, v79\n"
    "v_cvt_pk_bf16_f32 v50, v52, v53\n"
    "v_cvt_pk_bf16_f32 v51, v54, v55\n"
    "v_add_u32 v73, s73, v73\n"
    "v_lshrrev_b32 v3, 1, v79\n"
    "v_mfma_f32_16x16x32_bf16 v[52:55], v[48:51], v[12:15], 0\n"
    "v_cvt_pk_bf16_f32 v40, v44, v45\n"
    "v_cvt_pk_bf16_f32 v41, v46, v47\n"
    "v_smfmac_f32_16x16x64_bf16 v[56:59], v[16:19], v[136:143], v2\n"
    "s_waitcnt lgkmcnt(0)\n"
    "s_add_u32 s28, s28, s30\n"
    "v_mov_b32_dpp v42, v40 row_ror:8 row_mask:0xf bank_mask:0xf\n"
    "v_mov_b32_dpp v43, v41 row_ror:8 row_mask:0xf bank_mask:0xf\n"
    "s_add_u32 s96, s96, s98\n"
    "s_add_u32 s28, s28, s96\n"
    "v_cvt_pk_bf16_f32 v48, v52, v53\n"
    "v_cvt_pk_bf16_f32 v49, v54, v55\n"
    "v_mfma_f32_16x16x32_bf16 v[44:47], v[68:71], v[40:43], 0\n"
    "v_mov_b32_dpp v50, v48 row_ror:8 row_mask:0xf bank_mask:0xf\n"
    "v_mov_b32_dpp v51, v49 row_ror:8 row_mask:0xf bank_mask:0xf\n"
    "v_mfma_f32_16x16x32_bf16 v[68:71], v[64:67], v[40:43], 0\n"
    "v_mov_b32 v75, s28\n"
    "v_mfma_f32_16x16x32_bf16 v[52:55], v[48:51], v[40:43], 0\n"
    "buffer_store_dword v75, off, s[36:39], s74 sc1\n"
    "v_subrev_u32 v1, 57344, v1\n"
    "s_nop 1\n"
    "v_mov_b32_dpp v68, v44 row_ror:8 row_mask:0xf bank_mask:0xc\n"
    "v_mov_b32_dpp v69, v45 row_ror:8 row_mask:0xf bank_mask:0xc\n"
    "v_mov_b32_dpp v70, v46 row_ror:8 row_mask:0xf bank_mask:0xc\n"
    "v_mov_b32_dpp v71, v47 row_ror:8 row_mask:0xf bank_mask:0xc\n"
    "ds_write_b128 v79, v[68:71] offset:0\n"
    "v_mov_b32_dpp v52, v54 row_ror:8 row_mask:0xf bank_mask:0xc\n"
    "v_mov_b32_dpp v53, v55 row_ror:8 row_mask:0xf bank_mask:0xc\n"
    "ds_write_b64 v3, v[52:53] offset:4096\n"
    "v_mov_b64 v[12:13], -1\n"
    "v_mov_b64 v[14:15], -1\n"
    "v_smfmac_f32_16x16x64_bf16 v[56:59], v[20:23], v[144:151], v2\n"
    "buffer_store_dwordx4 v[12:15], v0, s[36:39], s2 offen sc1\n"
    "v_smfmac_f32_16x16x64_bf16 v[56:59], v[24:27], v[152:159], v2\n"
    "buffer_store_dwordx4 v[12:15], v0, s[36:39], s2 offen offset:1024 sc1\n"
    "v_smfmac_f32_16x16x64_bf16 v[56:59], v[28:31], v[160:167], v2\n"
    "buffer_store_dwordx4 v[12:15], v0, s[36:39], s80 offen sc1\n"
    "s_waitcnt lgkmcnt(0)\n"
    "s_barrier\n"
    "ds_read_b128 v[64:67], v76 offset:0\n"
    "v_smfmac_f32_16x16x64_bf16 v[60:63], v[28:31], v[168:175], v2\n"
    "ds_read_b128 v[40:43], v77 offset:0\n"
    "v_smfmac_f32_16x16x64_bf16 v[56:59], v[32:35], v[176:183], v2\n"
    "ds_read_b128 v[44:47], v78 offset:0\n"
    "v_smfmac_f32_16x16x64_bf16 v[60:63], v[32:35], v[184:191], v2\n"
    "ds_read_b64 v[12:13], v72 offset:4096\n"
    "ds_read_b64 v[14:15], v72 offset:4608\n"
    "v_smfmac_f32_16x16x64_bf16 v[56:59], v[36:39], v[192:199], v2\n"
    "ds_read_b64 v[52:53], v72 offset:5120\n"
    "ds_read_b64 v[54:55], v72 offset:5632\n"
    "v_smfmac_f32_16x16x64_bf16 v[60:63], v[36:39], v[200:207], v2\n"
    "buffer_store_dwordx4 v[48:51], v74, s[36:39], s76 offen sc1\n"
    "s_waitcnt lgkmcnt(6)\n"
    "v_pk_add_f32 v[68:69], v[68:69], v[64:65]\n"
    "v_pk_add_f32 v[70:71], v[70:71], v[66:67]\n"
    "s_waitcnt lgkmcnt(4)\n"
    "v_pk_add_f32 v[40:41], v[40:41], v[44:45]\n"
    "v_pk_add_f32 v[42:43], v[42:43], v[46:47]\n"
    "s_waitcnt lgkmcnt(0)\n"
    "v_pk_add_f32 v[12:13], v[12:13], v[14:15]\n"
    "v_pk_add_f32 v[52:53], v[52:53], v[54:55]\n"
    "v_pk_add_f32 v[68:69], v[68:69], v[40:41]\n"
    "v_pk_add_f32 v[70:71], v[70:71], v[42:43]\n"
    "v_pk_add_f32 v[12:13], v[12:13], v[52:53]\n"
    "v_max3_f32 v46, v68, v69, v70\n"
    "v_max3_f32 v47, v71, v12, v13\n"
    "v_max_f32 v3, v46, v47\n"
    "v_cvt_pk_bf16_f32 v56, v56, v57\n"
    "v_cvt_pk_bf16_f32 v57, v58, v59\n"
    "v_max_f32_dpp v46, v3, v3 row_ror:8 row_mask:0xf bank_mask:0xf\n"
    "v_max_f32_dpp v47, v3, v3 row_ror:8 row_mask:0xf bank_mask:0xf\n"
    "v_cvt_pk_bf16_f32 v58, v60, v61\n"
    "v_cvt_pk_bf16_f32 v59, v62, v63\n"
    "v_permlane16_swap_b32 v46, v47\n"
    "v_max_f32 v3, v46, v47\n"
    "v_max_f32 v46, v46, v47\n"
    "v_mov_b32 v14, v73\n"
    "s_nop 0\n"
    "v_permlane32_swap_b32 v3, v46\n"
    "v_max_f32 v3, v3, v46\n"
    "v_mul_f32 v46, 0xbe0293ee, v3\n"
    "v_pk_fma_f32 v[68:69], v[68:69], s[84:85], v[46:47] op_sel_hi:[1,0,0]\n"
    "v_pk_fma_f32 v[70:71], v[70:71], s[84:85], v[46:47] op_sel_hi:[1,0,0]\n"
    "v_pk_fma_f32 v[12:13], v[12:13], s[84:85], v[46:47] op_sel_hi:[1,0,0]\n"
    "v_exp_f32 v68, v68\n"
    "v_exp_f32 v69, v69\n"
    "v_exp_f32 v70, v70\n"
    "v_exp_f32 v71, v71\n"
    "v_exp_f32 v12, v12\n"
    "v_cvt_pk_bf16_f32 v40, v68, v69\n"
    "v_cvt_pk_bf16_f32 v41, v70, v71\n"
    "v_exp_f32 v13, v13\n"
    "v_mov_b32_dpp v42, v40 row_ror:8 row_mask:0xf bank_mask:0xf\n"
    "v_mov_b32_dpp v43, v41 row_ror:8 row_mask:0xf bank_mask:0xf\n"
    "v_cvt_pk_bf16_f32 v44, v12, v13\n"
    "v_pk_add_f32 v[68:69], v[68:69], v[70:71]\n"
    "s_waitcnt vmcnt(5)\n"
    "v_mfma_f32_16x16x32_bf16 v[48:51], v[4:7], v[40:43], 0\n"
    "ds_read_b128 v[16:19], v1 offset:0\n"
    "v_mov_b32_dpp v45, v44 row_ror:8 row_mask:0xf bank_mask:0xf\n"
    "v_add_f32 v3, v12, v13\n"
    "v_mfma_f32_16x16x32_bf16 v[52:55], v[8:11], v[40:43], 0\n"
    "s_cmp_eq_u32 s68, 63\n"
    "v_add_f32 v46, v68, v69\n"
    "v_fma_f32 v3, v3, 0.5, v46\n"
    "v_mfma_f32_16x16x16_bf16 v[48:51], v[56:57], v[44:45], v[48:51]\n"
    "s_cbranch_scc1 .Lrec_fin_%=\n"
    "v_add_f32_dpp v46, v3, v3 row_ror:8 row_mask:0xf bank_mask:0xf\n"
    "v_add_f32_dpp v47, v3, v3 row_ror:8 row_mask:0xf bank_mask:0xf\n"
    "v_mfma_f32_16x16x16_bf16 v[52:55], v[58:59], v[44:45], v[52:55]\n"
    "s_nop 0\n"
    "v_permlane16_swap_b32 v46, v47\n"
    "v_add_f32 v3, v46, v47\n"
    "v_add_f32 v46, v46, v47\n"
    "ds_read_b128 v[20:23], v1 offset:1024\n"
    "s_nop 0\n"
    "v_permlane32_swap_b32 v3, v46\n"
    "v_add_f32 v3, v3, v46\n"
    "v_rcp_f32 v46, v3\n"
    "v_mov_b32_dpp v48, v52 row_ror:8 row_mask:0xf bank_mask:0xc\n"
    "v_mov_b32_dpp v49, v53 row_ror:8 row_mask:0xf bank_mask:0xc\n"
    "v_mov_b32_dpp v50, v54 row_ror:8 row_mask:0xf bank_mask:0xc\n"
    "v_mov_b32_dpp v51, v55 row_ror:8 row_mask:0xf bank_mask:0xc\n"
    "v_pk_mul_f32 v[48:49], v[48:49], v[46:47] op_sel_hi:[1,0]\n"
    "v_pk_mul_f32 v[50:51], v[50:51], v[46:47] op_sel_hi:[1,0]\n"
    "v_cvt_pk_bf16_f32 v48, v48, v49\n"
    "v_cvt_pk_bf16_f32 v49, v50, v51\n"
    "ds_write_b64 v14, v[48:49] offset:8192\n"
    "ds_read_b128 v[24:27], v1 offset:16384\n"
    "ds_read_b128 v[28:31], v1 offset:17408\n"
    "ds_read_b128 v[32:35], v1 offset:32768\n"
    "ds_read_b128 v[36:39], v1 offset:33792\n"
    "buffer_store_dwordx4 v[56:59], v0, s[36:39], s76 offen offset:512 sc1\n"
    "v_xor_b32 v13, 128, v0\n"
    "v_lshlrev_b32 v12, 2, v0\n"
    "s_waitcnt vmcnt(2) lgkmcnt(4)\n"
    "s_barrier\n"
    "ds_read_b128 v[40:43], v0 offset:8192\n"
    "ds_read_b128 v[44:47], v0 offset:9216\n"
    "ds_read_b128 v[72:75], v1 offset:49152\n"
    "ds_read_b128 v[76:79], v1 offset:50176\n"
    "ds_read_b128 v[48:51], v13 offset:8192\n"
    "v_add_u32 v1, 57344, v1\n"
    "ds_read_b128 v[64:67], v1 offset:8192\n"
    "ds_read_b128 v[68:71], v1 offset:9216\n"
    "ds_read_b128 v[52:55], v13 offset:9216\n"
    "v_mov_b64 v[56:57], 0\n"
    "v_mov_b64 v[58:59], 0\n"
    "v_mov_b64 v[60:61], 0\n"
    "v_mov_b64 v[62:63], 0\n"
    "s_and_b32 s82, s68, 1\n"
    "s_mul_i32 s82, s82, 589824\n"
    "s_add_u32 s77, s86, s82\n"
    "s_add_u32 s73, s77, 65536\n"
    "s_add_u32 s74, s91, s82\n"
    "ds_read_b128 v[8:11], v0 offset:14336\n"
    "s_waitcnt lgkmcnt(8)\n"
    "v_smfmac_f32_16x16x64_bf16 v[56:59], v[40:43], v[16:23], v2\n"
    "ds_read_b128 v[16:19], v1 offset:24576\n"
    "s_mov_b32 exec_hi, 0\n"
    "buffer_load_dwordx4 v[12:15], v12, s[36:39], s93 offen sc1\n"
    "s_mov_b32 exec_hi, -1\n"
    "s_waitcnt lgkmcnt(8)\n"
    "v_smfmac_f32_16x16x64_bf16 v[56:59], v[44:47], v[24:31], v2\n"
    "ds_read_b128 v[20:23], v1 offset:25600\n"
    "v_mov_b32 v3, -1\n"
    "v_smfmac_f32_16x16x64_bf16 v[60:63], v[40:43], v[32:39], v2\n"
    "ds_read_b128 v[24:27], v1 offset:40960\n"
    "s_waitcnt lgkmcnt(8)\n"
    "v_smfmac_f32_16x16x64_bf16 v[60:63], v[44:47], v[72:79], v2\n"
    "ds_read_b128 v[28:31], v1 offset:41984\n"
    "s_waitcnt lgkmcnt(8)\n"
    "s_waitcnt lgkmcnt(6)\n"
    "v_smfmac_f32_16x16x64_bf16 v[56:59], v[48:51], v[64:71], v2\n"
    "ds_read_b128 v[32:35], v1 offset:49152\n"
    "v_mov_b64 v[64:65], 0\n"
    "s_waitcnt lgkmcnt(6)\n"
    "v_smfmac_f32_16x16x64_bf16 v[56:59], v[52:55], v[208:215], v2\n"
    "ds_read_b128 v[36:39], v1 offset:50176\n"
    "v_mov_b64 v[66:67], 0\n"
    "s_waitcnt lgkmcnt(4)\n"
    "v_smfmac_f32_16x16x64_bf16 v[60:63], v[48:51], v[16:23], v2\n"
    "v_add_u32 v1, 57344, v1\n"
    "ds_read_b128 v[72:75], v1 offset:0\n"
    "v_mov_b64 v[68:69], 0\n"
    "v_smfmac_f32_16x16x64_bf16 v[60:63], v[52:55], v[216:223], v2\n"
    "ds_read_b128 v[76:79], v1 offset:1024\n"
    "v_mov_b64 v[70:71], 0\n"
    "s_waitcnt lgkmcnt(4)\n"
    "v_smfmac_f32_16x16x64_bf16 v[64:67], v[40:43], v[24:31], v2\n"
    "ds_read_b128 v[16:19], v1 offset:8192\n"
    "s_mov_b64 exec, 1\n"
    "buffer_atomic_smax v3, off, s[36:39], s60 sc0 sc1\n"
    "s_mov_b64 exec, -1\n"
    "v_cvt_pk_bf16_f32 v56, v56, v57\n"
    "v_smfmac_f32_16x16x64_bf16 v[64:67], v[44:47], v[224:231], v2\n"
    "ds_read_b128 v[20:23], v1 offset:9216\n"
    "v_cvt_pk_bf16_f32 v57, v58, v59\n"
    "s_waitcnt lgkmcnt(4)\n"
    "v_smfmac_f32_16x16x64_bf16 v[68:71], v[40:43], v[32:39], v2\n"
    "ds_read_b128 v[24:27], v1 offset:16384\n"
    "v_cvt_pk_bf16_f32 v58, v60, v61\n"
    "v_smfmac_f32_16x16x64_bf16 v[68:71], v[44:47], v[232:239], v2\n"
    "ds_read_b128 v[28:31], v1 offset:17408\n"
    "v_cvt_pk_bf16_f32 v59, v62, v63\n"
    "s_waitcnt lgkmcnt(4)\n"
    "v_smfmac_f32_16x16x64_bf16 v[64:67], v[48:51], v[72:79], v2\n"
    "ds_read_b128 v[32:35], v1 offset:24576\n"
    "v_smfmac_f32_16x16x64_bf16 v[64:67], v[52:55], v[240:247], v2\n"
    "ds_read_b128 v[36:39], v1 offset:25600\n"
    "v_permlane16_swap_b32 v56, v58\n"
    "v_permlane16_swap_b32 v57, v59\n"
    "s_waitcnt lgkmcnt(4)\n"
    "v_smfmac_f32_16x16x64_bf16 v[68:71], v[48:51], v[16:23], v2\n"
    "buffer_store_dwordx4 v[56:59], v8, s[36:39], s77 offen sc1\n"
    "v_smfmac_f32_16x16x64_bf16 v[68:71], v[52:55], v[248:255], v2\n"
    "v_cvt_pk_bf16_f32 v64, v64, v65\n"
    "s_waitcnt lgkmcnt(2)\n"
    "v_mfma_f32_16x16x32_bf16 v[4:7], v[40:43], v[24:27], 0\n"
    "v_cvt_pk_bf16_f32 v65, v66, v67\n"
    "v_mfma_f32_16x16x32_bf16 v[4:7], v[48:51], v[28:31], v[4:7]\n"
    "v_cvt_pk_bf16_f32 v66, v68, v69\n"
    "v_cvt_pk_bf16_f32 v67, v70, v71\n"
    "s_waitcnt lgkmcnt(0)\n"
    "v_mfma_f32_16x16x32_bf16 v[4:7], v[44:47], v[32:35], v[4:7]\n"
    "s_nop 0\n"
    "v_permlane16_swap_b32 v64, v66\n"
    "v_mfma_f32_16x16x32_bf16 v[4:7], v[52:55], v[36:39], v[4:7]\n"
    "v_permlane16_swap_b32 v65, v67\n"
    "buffer_store_dwordx4 v[64:67], v8, s[36:39], s73 offen sc1\n"
    "v_subrev_u32 v1, 114688, v1\n"
    "s_nop 4\n"
    "v_cvt_pk_bf16_f32 v4, v4, v5\n"
    "v_cvt_pk_bf16_f32 v5, v6, v7\n"
    "buffer_store_dwordx2 v[4:5], v9, s[36:39], s74 offen sc1\n"
    "ds_read_b128 v[72:75], v0 offset:15360\n"
    "v_mov_b32 v76, v10\n"
    "s_waitcnt vmcnt(4)\n"
    "s_memtime s[96:97]\n"
    "v_max3_u32 v12, v12, v13, v14\n"
    "v_max_u32 v12, v12, v15\n"
    "s_add_u32 s74, s68, 1\n"
    "s_and_b32 s75, s74, 7\n"
    "v_max_u32_dpp v12, v12, v12 row_ror:8 row_mask:0xf bank_mask:0xf\n"
    "s_mul_i32 s75, s75, 786432\n"
    "s_add_u32 s75, s75, s56\n"
    "v_max_u32_dpp v12, v12, v12 row_ror:4 row_mask:0xf bank_mask:0xf\n"
    "s_and_b32 s79, s74, 3\n"
    "s_lshl_b32 s79, s79, 17\n"
    "v_max_u32_dpp v12, v12, v12 row_ror:2 row_mask:0xf bank_mask:0xf\n"
    "s_add_u32 s79, s79, s88\n"
    "s_add_u32 s78, s87, s82\n"
    "v_max_u32_dpp v12, v12, v12 row_ror:1 row_mask:0xf bank_mask:0xf\n"
    "s_add_u32 s73, s78, 4096\n"
    "v_readlane_b32 s95, v12, 0\n"
    "v_readlane_b32 s99, v12, 16\n"
    "s_nop 0\n"
    "s_max_u32 s95, s95, s99\n"
    "s_cmp_eq_u32 s95, -1\n"
    "s_cbranch_scc1 .Lrec_tsretry_%=\n"
    ".Lrec_tsgo_%=:\n"
    "s_sub_u32 s95, s95, s28\n"
    "s_min_u32 s95, s95, 240\n"
    "s_mul_i32 s95, s95, 23\n"
    "s_lshr_b32 s95, s95, 2\n"
    "s_add_u32 s32, s32, s95\n"
    "s_add_u32 s32, s32, 1900\n"
    "s_waitcnt lgkmcnt(0)\n"
    "s_sub_u32 s95, s32, s96\n"
    "s_mul_i32 s95, s95, 43\n"
    "s_ashr_i32 s95, s95, 10\n"
    "s_max_i32 s95, s95, 0\n"
    "s_min_i32 s95, s95, 100\n"
    ".Lrec_tsloop_%=:\n"
    "s_sub_u32 s95, s95, 1\n"
    "s_cbranch_scc0 .Lrec_tsloop_%=\n"
    "s_cmp_lg_u32 s61, 0\n"
    "s_cbranch_scc1 .Lrec_nored_%=\n"
    ".Lrec_red_%=:\n"
    "buffer_load_dwordx4 v[4:7], v0, s[36:39], s78 offen sc1\n"
    "buffer_load_dwordx4 v[8:11], v0, s[36:39], s78 offen offset:1024 sc1\n"
    "buffer_load_dwordx4 v[12:15], v0, s[36:39], s78 offen offset:2048 sc1\n"
    "buffer_load_dwordx4 v[16:19], v0, s[36:39], s78 offen offset:3072 sc1\n"
    "buffer_load_dwordx4 v[20:23], v0, s[36:39], s73 offen sc1\n"
    "buffer_load_dwordx4 v[24:27], v0, s[36:39], s73 offen offset:1024 sc1\n"
    "buffer_load_dwordx4 v[28:31], v0, s[36:39], s73 offen offset:2048 sc1\n"
    "buffer_load_dwordx4 v[32:35], v0, s[36:39], s73 offen offset:3072 sc1\n"
    "s_waitcnt vmcnt(11)\n"
    "buffer_load_dwordx4 v[64:67], v0, s[36:39], s75 offen sc1\n"
    "buffer_load_dwordx4 v[68:71], v0, s[36:39], s75 offen offset:1024 sc1\n"
    "v_readfirstlane_b32 s81, v3\n"
    "v_mov_b32 v77, s68\n"
    "s_mov_b64 exec, 1\n"
    "buffer_store_dword v77, off, s[36:39], s59 sc1\n"
    "s_mov_b64 exec, -1\n"
    "s_cmp_gt_i32 s81, s68\n"
    "s_cselect_b32 s81, 1, 0\n"
    "s_and_b32 s80, s68, s34\n"
    "s_mul_i32 s80, s80, s35\n"
    "s_add_u32 s80, s80, s89\n"
    "s_and_b32 s2, s68, 1\n"
    "s_mul_i32 s2, s2, 589824\n"
    "s_add_u32 s2, s2, s3\n"
    "s_add_u32 s68, s68, 1\n"
    "s_and_b32 s70, s68, 3\n"
    "s_lshl_b32 s70, s70, 17\n"
    "s_add_u32 s70, s70, s83\n"
    "s_add_u32 s71, s70, 4096\n"
    "s_add_u32 s72, s70, 16384\n"
    "s_lshr_b32 s93, s68, 2\n"
    "s_lshl_b32 s93, s93, 14\n"
    "s_and_b32 s90, s68, 3\n"
    "s_lshl_b32 s90, s90, 11\n"
    "s_add_u32 s93, s93, s90\n"
    "s_add_u32 s93, s93, s92\n"
    "s_and_b32 s76, s68, 7\n"
    "s_mul_i32 s76, s76, 196608\n"
    "s_add_u32 s76, s76, s57\n"
    "v_mov_b64 v[40:41], 0\n"
    "v_mov_b64 v[42:43], 0\n"
    "v_mov_b64 v[44:45], 0\n"
    "v_mov_b64 v[46:47], 0\n"
    "v_mov_b64 v[48:49], 0\n"
    "v_mov_b64 v[50:51], 0\n"
    "v_mov_b64 v[52:53], 0\n"
    "v_mov_b64 v[54:55], 0\n"
    "v_mov_b64 v[56:57], 0\n"
    "v_mov_b64 v[58:59], 0\n"
    "v_mov_b64 v[60:61], 0\n"
    "v_mov_b64 v[62:63], 0\n"
    "s_waitcnt lgkmcnt(0)\n"
    ".Lrec_rsum_%=:\n"
    "s_waitcnt vmcnt(10)\n"
    "v_mfma_f32_16x16x32_bf16 v[36:39], v[4:7], v[72:75], 0\n"
    "v_max3_u32 v3, v4, v5, v6\n"
    "v_max_u32 v3, v3, v7\n"
    "s_waitcnt vmcnt(9)\n"
    "v_mfma_f32_16x16x32_bf16 v[36:39], v[8:11], v[72:75], v[36:39]\n"
    "v_max3_u32 v3, v3, v8, v9\n"
    "v_max3_u32 v3, v3, v10, v11\n"
    "s_waitcnt vmcnt(8)\n"
    "v_mfma_f32_16x16x32_bf16 v[36:39], v[12:15], v[72:75], v[36:39]\n"
    "v_max3_u32 v3, v3, v12, v13\n"
    "v_max3_u32 v3, v3, v14, v15\n"
    "s_waitcnt vmcnt(7)\n"
    "v_mfma_f32_16x16x32_bf16 v[36:39], v[16:19], v[72:75], v[36:39]\n"
    "v_max3_u32 v3, v3, v16, v17\n"
    "v_max3_u32 v3, v3, v18, v19\n"
    "s_waitcnt vmcnt(6)\n"
    "v_mfma_f32_16x16x32_bf16 v[36:39], v[20:23], v[72:75], v[36:39]\n"
    "v_max3_u32 v3, v3, v20, v21\n"
    "v_max3_u32 v3, v3, v22, v23\n"
    "s_waitcnt vmcnt(5)\n"
    "v_mfma_f32_16x16x32_bf16 v[36:39], v[24:27], v[72:75], v[36:39]\n"
    "v_max3_u32 v3, v3, v24, v25\n"
    "v_max3_u32 v3, v3, v26, v27\n"
    "s_waitcnt vmcnt(4)\n"
    "v_mfma_f32_16x16x32_bf16 v[36:39], v[28:31], v[72:75], v[36:39]\n"
    "v_max3_u32 v3, v3, v28, v29\n"
    "v_max3_u32 v3, v3, v30, v31\n"
    "s_waitcnt vmcnt(3)\n"
    "v_mfma_f32_16x16x32_bf16 v[36:39], v[32:35], v[72:75], v[36:39]\n"
    "v_max3_u32 v3, v3, v32, v33\n"
    "v_max3_u32 v3, v3, v34, v35\n"
    "v_cmp_eq_u32 vcc, -1, v3\n"
    "s_nop 4\n"
    "v_cvt_pk_bf16_f32 v4, v36, v37\n"
    "v_cvt_pk_bf16_f32 v6, v36, v37\n"
    "v_cvt_pk_bf16_f32 v5, v38, v39\n"
    "v_cvt_pk_bf16_f32 v7, v38, v39\n"
    "s_cbranch_vccnz .Lrec_rretry_%=\n"
    ".Lrec_rgo_%=:\n"
    "v_permlane16_swap_b32 v4, v6\n"
    "v_permlane16_swap_b32 v5, v7\n"
    "buffer_store_dwordx4 v[4:7], v76, s[36:39], s79 offen sc1\n"
    ".Lrec_b3_%=:\n"
    ".Lrec_entry_%=:\n"
    "ds_read_b128 v[72:75], v1 offset:8192\n"
    "ds_read_b128 v[76:79], v1 offset:9216\n"
    "s_cmp_eq_u32 s81, 0\n"
    "s_cbranch_scc1 .Lrec_waitp_%=\n"
    "s_memtime s[96:97]\n"
    "s_add_u32 s32, s32, 1300\n"
    "s_waitcnt lgkmcnt(0)\n"
    "s_sub_u32 s95, s32, s96\n"
    "s_mul_i32 s95, s95, 43\n"
    "s_ashr_i32 s95, s95, 10\n"
    "s_max_i32 s95, s95, 0\n"
    "s_min_i32 s95, s95, 100\n"
    ".Lrec_gloop_%=:\n"
    "s_sub_u32 s95, s95, 1\n"
    "s_cbranch_scc0 .Lrec_gloop_%=\n"
    "s_branch .Lrec_top_%=\n"
    ".Lrec_LOOPE_%=:\n"
    ".Lrec_fin_%=:\n"
    "buffer_store_dwordx4 v[56:59], v0, s[36:39], s76 offen offset:512 sc1\n"
    "s_waitcnt vmcnt(0) lgkmcnt(0)\n"
    "v_mov_b32 v46, s68\n"
    "s_mov_b64 exec, 1\n"
    "buffer_store_dword v46, off, s[36:39], s59 sc1\n"
    "s_mov_b64 exec, -1\n"
    "s_waitcnt vmcnt(0)\n"
    "s_branch .Lrec_FIN_%=\n"
    ".Lrec_retry0_%=:\n"
    "s_nop 7\n"
    "buffer_load_dwordx4 v[4:7], v0, s[36:39], s70 offen sc1\n"
    "buffer_load_dwordx4 v[8:11], v0, s[36:39], s70 offen offset:1024 sc1\n"
    "buffer_load_dwordx4 v[12:15], v0, s[36:39], s70 offen offset:2048 sc1\n"
    "buffer_load_dwordx4 v[16:19], v0, s[36:39], s70 offen offset:3072 sc1\n"
    "buffer_load_dwordx4 v[20:23], v0, s[36:39], s71 offen sc1\n"
    "buffer_load_dwordx4 v[24:27], v0, s[36:39], s71 offen offset:1024 sc1\n"
    "buffer_load_dwordx4 v[28:31], v0, s[36:39], s71 offen offset:2048 sc1\n"
    "buffer_load_dwordx4 v[32:35], v0, s[36:39], s71 offen offset:3072 sc1\n"
    "buffer_load_dwordx4 v[36:39], v0, s[36:39], s72 offen sc1\n"
    "s_waitcnt vmcnt(0)\n"
    "v_max3_u32 v3, v4, v5, v6\n"
    "v_max_u32 v3, v3, v7\n"
    "v_cmp_eq_u32 vcc, -1, v3\n"
    "s_cbranch_vccz .Lrec_back0_%=\n"
    "s_sub_u32 s69, s69, 1\n"
    "s_cbranch_scc0 .Lrec_retry0_%=\n"
    "s_mov_b32 s69, 0\n"
    "s_branch .Lrec_back0_%=\n"
    ".Lrec_retry1_%=:\n"
    "s_nop 7\n"
    "buffer_load_dwordx4 v[8:11], v0, s[36:39], s70 offen offset:1024 sc1\n"
    "buffer_load_dwordx4 v[12:15], v0, s[36:39], s70 offen offset:2048 sc1\n"
    "buffer_load_dwordx4 v[16:19], v0, s[36:39], s70 offen offset:3072 sc1\n"
    "buffer_load_dwordx4 v[20:23], v0, s[36:39], s71 offen sc1\n"
    "buffer_load_dwordx4 v[24:27], v0, s[36:39], s71 offen offset:1024 sc1\n"
    "buffer_load_dwordx4 v[28:31], v0, s[36:39], s71 offen offset:2048 sc1\n"
    "buffer_load_dwordx4 v[32:35], v0, s[36:39], s71 offen offset:3072 sc1\n"
    "buffer_load_dwordx4 v[36:39], v0, s[36:39], s72 offen sc1\n"
    "s_waitcnt vmcnt(0)\n"
    "v_max3_u32 v3, v8, v9, v10\n"
    "v_max_u32 v3, v3, v11\n"
    "v_cmp_eq_u32 vcc, -1, v3\n"
    "s_cbranch_vccz .Lrec_back1_%=\n"
    "s_sub_u32 s69, s69, 1\n"
    "s_cbranch_scc0 .Lrec_retry1_%=\n"
    "s_mov_b32 s69, 0\n"
    "s_branch .Lrec_back1_%=\n"
    ".Lrec_retry2_%=:\n"
    "s_nop 7\n"
    "buffer_load_dwordx4 v[12:15], v0, s[36:39], s70 offen offset:2048 sc1\n"
    "buffer_load_dwordx4 v[16:19], v0, s[36:39], s70 offen offset:3072 sc1\n"
    "buffer_load_dwordx4 v[20:23], v0, s[36:39], s71 offen sc1\n"
    "buffer_load_dwordx4 v[24:27], v0, s[36:39], s71 offen offset:1024 sc1\n"
    "buffer_load_dwordx4 v[28:31], v0, s[36:39], s71 offen offset:2048 sc1\n"
    "buffer_load_dwordx4 v[32:35], v0, s[36:39], s71 offen offset:3072 sc1\n"
    "buffer_load_dwordx4 v[36:39], v0, s[36:39], s72 offen sc1\n"
    "s_waitcnt vmcnt(0)\n"
    "v_max3_u32 v3, v12, v13, v14\n"
    "v_max_u32 v3, v3, v15\n"
    "v_cmp_eq_u32 vcc, -1, v3\n"
    "s_cbranch_vccz .Lrec_back2_%=\n"
    "s_sub_u32 s69, s69, 1\n"
    "s_cbranch_scc0 .Lrec_retry2_%=\n"
    "s_mov_b32 s69, 0\n"
    "s_branch .Lrec_back2_%=\n"
    ".Lrec_retry3_%=:\n"
    "s_nop 7\n"
    "buffer_load_dwordx4 v[16:19], v0, s[36:39], s70 offen offset:3072 sc1\n"
    "buffer_load_dwordx4 v[20:23], v0, s[36:39], s71 offen sc1\n"
    "buffer_load_dwordx4 v[24:27], v0, s[36:39], s71 offen offset:1024 sc1\n"
    "buffer_load_dwordx4 v[28:31], v0, s[36:39], s71 offen offset:2048 sc1\n"
    "buffer_load_dwordx4 v[32:35], v0, s[36:39], s71 offen offset:3072 sc1\n"
    "buffer_load_dwordx4 v[36:39], v0, s[36:39], s72 offen sc1\n"
    "s_waitcnt vmcnt(0)\n"
    "v_max3_u32 v3, v16, v17, v18\n"
    "v_max_u32 v3, v3, v19\n"
    "v_cmp_eq_u32 vcc, -1, v3\n"
    "s_cbranch_vccz .Lrec_back3_%=\n"
    "s_sub_u32 s69, s69, 1\n"
    "s_cbranch_scc0 .Lrec_retry3_%=\n"
    "s_mov_b32 s69, 0\n"
    "s_branch .Lrec_back3_%=\n"
    ".Lrec_retry4_%=:\n"
    "s_nop 7\n"
    "buffer_load_dwordx4 v[20:23], v0, s[36:39], s71 offen sc1\n"
    "buffer_load_dwordx4 v[24:27], v0, s[36:39], s71 offen offset:1024 sc1\n"
    "buffer_load_dwordx4 v[28:31], v0, s[36:39], s71 offen offset:2048 sc1\n"
    "buffer_load_dwordx4 v[32:35], v0, s[36:39], s71 offen offset:3072 sc1\n"
    "buffer_load_dwordx4 v[36:39], v0, s[36:39], s72 offen sc1\n"
    "s_waitcnt vmcnt(0)\n"
    "v_max3_u32 v3, v20, v21, v22\n"
    "v_max_u32 v3, v3, v23\n"
    "v_cmp_eq_u32 vcc, -1, v3\n"
    "s_cbranch_vccz .Lrec_back4_%=\n"
    "s_sub_u32 s69, s69, 1\n"
    "s_cbranch_scc0 .Lrec_retry4_%=\n"
    "s_mov_b32 s69, 0\n"
    "s_branch .Lrec_back4_%=\n"
    ".Lrec_retry5_%=:\n"
    "s_nop 7\n"
    "buffer_load_dwordx4 v[24:27], v0, s[36:39], s71 offen offset:1024 sc1\n"
    "buffer_load_dwordx4 v[28:31], v0, s[36:39], s71 offen offset:2048 sc1\n"
    "buffer_load_dwordx4 v[32:35], v0, s[36:39], s71 offen offset:3072 sc1\n"
    "buffer_load_dwordx4 v[36:39], v0, s[36:39], s72 offen sc1\n"
    "s_waitcnt vmcnt(0)\n"
    "v_max3_u32 v3, v24, v25, v26\n"
    "v_max_u32 v3, v3, v27\n"
    "v_cmp_eq_u32 vcc, -1, v3\n"
    "s_cbranch_vccz .Lrec_back5_%=\n"
    "s_sub_u32 s69, s69, 1\n"
    "s_cbranch_scc0 .Lrec_retry5_%=\n"
    "s_mov_b32 s69, 0\n"
    "s_branch .Lrec_back5_%=\n"
    ".Lrec_retry6_%=:\n"
    "s_nop 7\n"
    "buffer_load_dwordx4 v[28:31], v0, s[36:39], s71 offen offset:2048 sc1\n"
    "buffer_load_dwordx4 v[32:35], v0, s[36:39], s71 offen offset:3072 sc1\n"
    "buffer_load_dwordx4 v[36:39], v0, s[36:39], s72 offen sc1\n"
    "s_waitcnt vmcnt(0)\n"
    "v_max3_u32 v3, v28, v29, v30\n"
    "v_max3_u32 v3, v3, v31, v32\n"
    "v_max3_u32 v3, v3, v33, v34\n"
    "v_max_u32 v3, v3, v35\n"
    "v_cmp_eq_u32 vcc, -1, v3\n"
    "s_cbranch_vccz .Lrec_back6_%=\n"
    "s_sub_u32 s69, s69, 1\n"
    "s_cbranch_scc0 .Lrec_retry6_%=\n"
    "s_mov_b32 s69, 0\n"
    "s_branch .Lrec_back6_%=\n"
    ".Lrec_retry8_%=:\n"
    "s_nop 7\n"
    "buffer_load_dwordx4 v[36:39], v0, s[36:39], s72 offen sc1\n"
    "s_waitcnt vmcnt(0)\n"
    "v_max3_u32 v3, v36, v37, v38\n"
    "v_max_u32 v3, v3, v39\n"
    "v_cmp_eq_u32 vcc, -1, v3\n"
    "s_cbranch_vccz .Lrec_back8_%=\n"
    "s_sub_u32 s69, s69, 1\n"
    "s_cbranch_scc0 .Lrec_retry8_%=\n"
    "s_mov_b32 s69, 0\n"
    "s_branch .Lrec_back8_%=\n"
    ".Lrec_tsretry_%=:\n"
    "s_mov_b32 s98, 6\n"
    ".Lrec_tsretry1_%=:\n"
    "s_mov_b32 s95, s28\n"
    "s_sub_u32 s98, s98, 1\n"
    "s_cbranch_scc1 .Lrec_tsgo_%=\n"
    "buffer_load_dwordx4 v[12:15], v11, s[36:39], s93 offen sc1\n"
    "s_waitcnt vmcnt(0)\n"
    "s_memtime s[96:97]\n"
    "v_max3_u32 v12, v12, v13, v14\n"
    "v_max_u32 v12, v12, v15\n"
    "s_nop 1\n"
    "v_max_u32_dpp v12, v12, v12 row_ror:8 row_mask:0xf bank_mask:0xf\n"
    "s_nop 1\n"
    "v_max_u32_dpp v12, v12, v12 row_ror:4 row_mask:0xf bank_mask:0xf\n"
    "s_nop 1\n"
    "v_max_u32_dpp v12, v12, v12 row_ror:2 row_mask:0xf bank_mask:0xf\n"
    "s_nop 1\n"
    "v_max_u32_dpp v12, v12, v12 row_ror:1 row_mask:0xf bank_mask:0xf\n"
    "s_nop 0\n"
    "v_readlane_b32 s95, v12, 0\n"
    "v_readlane_b32 s99, v12, 16\n"
    "s_nop 0\n"
    "s_max_u32 s95, s95, s99\n"
    "s_cmp_eq_u32 s95, -1\n"
    "s_cbranch_scc1 .Lrec_tsretry1_%=\n"
    "s_branch .Lrec_tsgo_%=\n"
    ".Lrec_rretry_%=:\n"
    "s_sub_u32 s69, s69, 1\n"
    "s_cbranch_scc1 .Lrec_rdead_%=\n"
    "s_nop 7\n"
    "buffer_load_dwordx4 v[4:7], v0, s[36:39], s78 offen sc1\n"
    "buffer_load_dwordx4 v[8:11], v0, s[36:39], s78 offen offset:1024 sc1\n"
    "buffer_load_dwordx4 v[12:15], v0, s[36:39], s78 offen offset:2048 sc1\n"
    "buffer_load_dwordx4 v[16:19], v0, s[36:39], s78 offen offset:3072 sc1\n"
    "buffer_load_dwordx4 v[20:23], v0, s[36:39], s73 offen sc1\n"
    "buffer_load_dwordx4 v[24:27], v0, s[36:39], s73 offen offset:1024 sc1\n"
    "buffer_load_dwordx4 v[28:31], v0, s[36:39], s73 offen offset:2048 sc1\n"
    "buffer_load_dwordx4 v[32:35], v0, s[36:39], s73 offen offset:3072 sc1\n"
    "s_waitcnt vmcnt(0)\n"
    "s_branch .Lrec_rsum_%=\n"
    ".Lrec_rdead_%=:\n"
    "s_mov_b32 s69, 0\n"
    "s_nop 7\n"
    "s_branch .Lrec_rgo_%=\n"
    ".Lrec_nored_%=:\n"
    "s_nop 8\n"
    "s_cmp_lt_i32 s63, 0\n"
    "s_cbranch_scc0 .Lrec_red_%=\n"
    "s_sleep 3\n"
    "s_waitcnt vmcnt(3)\n"
    "buffer_load_dwordx4 v[64:67], v0, s[36:39], s75 offen sc1\n"
    "buffer_load_dwordx4 v[68:71], v0, s[36:39], s75 offen offset:1024 sc1\n"
    "v_readfirstlane_b32 s81, v3\n"
    "v_mov_b32 v77, s68\n"
    "s_mov_b64 exec, 1\n"
    "buffer_store_dword v77, off, s[36:39], s59 sc1\n"
    "s_mov_b64 exec, -1\n"
    "s_cmp_gt_i32 s81, s68\n"
    "s_cselect_b32 s81, 1, 0\n"
    "s_and_b32 s80, s68, s34\n"
    "s_mul_i32 s80, s80, s35\n"
    "s_add_u32 s80, s80, s89\n"
    "s_and_b32 s2, s68, 1\n"
    "s_mul_i32 s2, s2, 589824\n"
    "s_add_u32 s2, s2, s3\n"
    "s_add_u32 s68, s68, 1\n"
    "s_and_b32 s70, s68, 3\n"
    "s_lshl_b32 s70, s70, 17\n"
    "s_add_u32 s70, s70, s83\n"
    "s_add_u32 s71, s70, 4096\n"
    "s_add_u32 s72, s70, 16384\n"
    "s_lshr_b32 s93, s68, 2\n"
    "s_lshl_b32 s93, s93, 14\n"
    "s_and_b32 s90, s68, 3\n"
    "s_lshl_b32 s90, s90, 11\n"
    "s_add_u32 s93, s93, s90\n"
    "s_add_u32 s93, s93, s92\n"
    "s_and_b32 s76, s68, 7\n"
    "s_mul_i32 s76, s76, 196608\n"
    "s_add_u32 s76, s76, s57\n"
    "v_mov_b64 v[40:41], 0\n"
    "v_mov_b64 v[42:43], 0\n"
    "v_mov_b64 v[44:45], 0\n"
    "v_mov_b64 v[46:47], 0\n"
    "v_mov_b64 v[48:49], 0\n"
    "v_mov_b64 v[50:51], 0\n"
    "v_mov_b64 v[52:53], 0\n"
    "v_mov_b64 v[54:55], 0\n"
    "v_mov_b64 v[56:57], 0\n"
    "v_mov_b64 v[58:59], 0\n"
    "v_mov_b64 v[60:61], 0\n"
    "v_mov_b64 v[62:63], 0\n"
    "s_branch .Lrec_b3_%=\n"
    ".Lrec_waitp_%=:\n"
    "v_mov_b32 v3, -1\n"
    "s_mov_b64 exec, 1\n"
    "buffer_atomic_smax v3, off, s[36:39], s60 sc0 sc1\n"
    "s_mov_b64 exec, -1\n"
    "s_waitcnt vmcnt(0)\n"
    "v_readfirstlane_b32 s81, v3\n"
    "s_nop 0\n"
    "s_cmp_ge_i32 s81, s68\n"
    "s_cbranch_scc1 .Lrec_waitp1_%=\n"
    "s_sleep 1\n"
    "s_sub_u32 s69, s69, 1\n"
    "s_cbranch_scc0 .Lrec_waitp_%=\n"
    "s_mov_b32 s69, 0\n"
    ".Lrec_waitp1_%=:\n"
    "s_and_b32 s75, s68, 7\n"
    "s_mul_i32 s75, s75, 786432\n"
    "s_add_u32 s75, s75, s56\n"
    "buffer_load_dwordx4 v[64:67], v0, s[36:39], s75 offen sc1\n"
    "buffer_load_dwordx4 v[68:71], v0, s[36:39], s75 offen offset:1024 sc1\n"
    "s_branch .Lrec_top_%=\n"
    ".Lrec_FIN_%=:\n"
  );
}

DEVI void hot_init(const Params& p, rsrc_t rw, int L, int n) {
  const int tid = threadIdx.x, lane = tid & 63, w = (int)rfl(tid >> 6);
  u32 hot = hot_page(p, L);
  if (n == 8 && w == 0) {
    u32 best[2] = {~0u, ~0u};
    const u32 probe = hot + 120 * CHUNK;
#pragma unroll 1
    for (int r = 0; r < 6; ++r) {
      const u32 a = probe + (u32)(r & 1) * 8192;
      st4(rw, 0u, lane == 0 ? a : OOB, 0);
      drain();
      const unsigned long long t0 = __builtin_amdgcn_s_memtime();
      asm volatile("s_waitcnt lgkmcnt(0)" ::: "memory");
      u32 v = ld4(rw, lane == 0 ? a : OOB, 0);
      asm volatile("s_waitcnt vmcnt(0)" : "+v"(v) :: "memory");
      const u32 dt = (u32)(__builtin_amdgcn_s_memtime() - t0);
      if (r & 1) best[1] = min(best[1], dt); else best[0] = min(best[0], dt);
    }
    if (lane == 0) st4(rw, best[1] < best[0] ? 1u : 0u, PARW + L * 128, 0);
  }
  unsigned budget = SPIN_LIMIT;
  wait_flags<2>(rw, PARW + L * 128, 0, 0, budget);
  const u32 par = rfl(ld4(rw, PARW + L * 128, 0)) & 1u;
  hot += par * 8192;
  const v4u ones = {~0u, ~0u, ~0u, ~0u};
#pragma unroll
  for (int k = 0; k < 6; ++k) {
    st16(rw, ones, tid * 16, hot + (u32)(6 * n + k) * CHUNK);
    st16(rw, ones, tid * 16 + 4096, hot + (u32)(6 * n + k) * CHUNK);
  }
  {
    const u32 b = hot + IMG0 + (u32)(4 + n) * 2 * CHUNK;
    st16(rw, ones, tid * 16, b); st16(rw, ones, tid * 16 + 4096, b);
    st16(rw, ones, w == 0 ? (u32)tid * 16 : OOB, b + CHUNK);
  }
  if (n < 8) {
#pragma unroll
    for (int k = 0; k < 2; ++k) {
      st16(rw, ones, tid * 16, hot + (u32)(104 + 2 * n + k) * CHUNK);
      st16(rw, ones, tid * 16 + 4096, hot + (u32)(104 + 2 * n + k) * CHUNK);
    }
  }
  if (n >= 8) {
    const u32 b = hot + IMG0 + (u32)(n - 8) * 2 * CHUNK;
#pragma unroll
    for (int k = 0; k < 3; ++k) {
      const int pc = tid + 256 * k;
      if (pc < 576) {
        v4u v = *(const v4u*)(p.mem0 + (pc & 7) * 576 + (pc >> 3) * 8);
#pragma unroll
        for (int d = 0; d < 4; ++d) v[d] = min(v[d], 0xFFFFFFFEu);
        st16(rw, v, pc < 512 ? (u32)pc * 16 : CHUNK + (u32)(pc - 512) * 16, b);
      }
    }
  }
  drain();
  __syncthreads();
  if (tid == 0) st4(rw, par, INITF + L * 2048 + n * 128, 0);
}

DEVI void role_att(const Params& p, int L, int j, int a, u32 dbg) {
  const int tid = threadIdx.x, lane = tid & 63, w = (int)rfl(tid >> 6);
  const int c = lane & 15, g = lane >> 4;
  const int hl = 4 * a + w, h = 8 * j + hl;
  const u32 lb = OFF_L + L * LSZ;
  const v4u ws = make_srd(p.ws, WS_END);
  v16u sa = cat4(ws, ws, ws, ws);
  v16u sb = {lb + L_QKV + h * 24576, lb + L_KVM + h * 6144, lb + L_O + j * 65536 + hl * 8192, 0, dbg + w * 64,
             0, 0, 0, 0, 0, 0, 0, 0, 0, 0, 0};
  v16u va = {0, 0, 0, 0, 0, 0, 0, 0, 0, 0, 0, 0, 0, 0, 0, 0};
  va[0] = lane * 16;
  va[1] = (u32)(g * 128 + (c & 7) * 16);
  va[2] = lane < 3 ? mbox(L, j, SL_WO + lane, hl) : mbox(L, j, SL_P + hl, 16);
  va[4] = mbox_att(L, j, hl, lane & 15);
  va[5] = (u32)(lane < 4 ? 0 : (lane < 7 ? RO : ((lane >> 2) == 2 ? -1 : 1000)));
  va[6] = (u32)(c - 4 * g);
  va[7] = 0;
  RUN_ASM(
    "s_nop 4\n"
    "v_cmp_gt_i32 s[10:11], 0, v6\n"
    "v_cmp_gt_i32 s[12:13], 1, v6\n"
    "v_cmp_gt_i32 s[14:15], 2, v6\n"
    "v_cmp_gt_i32 s[16:17], 3, v6\n"
    "v_mov_b32 v10, 0xff61b1e6\n"
    "s_mov_b32 s18, 0x3e0293ee\n"
    "s_mov_b32 s68, 0\n"
    "v_mov_b32 v8, -1\n"
    "s_mov_b32 s69, 262144\n"
    "s_branch .Latt_waitp_%=\n"
    ".Latt_LOOPB_%=:\n"
    ".Latt_top_%=:\n"
    "s_waitcnt vmcnt(23)\n"
    "v_mfma_f32_16x16x32_bf16 v[160:163], v[16:19], v[48:51], 0\n"
    "v_mfma_f32_16x16x32_bf16 v[164:167], v[16:19], v[52:55], 0\n"
    "v_mfma_f32_16x16x32_bf16 v[168:171], v[20:23], v[52:55], 0\n"
    "s_waitcnt vmcnt(19)\n"
    "v_mfma_f32_16x16x32_bf16 v[160:163], v[24:27], v[56:59], v[160:163]\n"
    "v_mfma_f32_16x16x32_bf16 v[164:167], v[24:27], v[60:63], v[164:167]\n"
    "v_mfma_f32_16x16x32_bf16 v[168:171], v[28:31], v[60:63], v[168:171]\n"
    "s_waitcnt vmcnt(15)\n"
    "v_mfma_f32_16x16x32_bf16 v[160:163], v[32:35], v[64:67], v[160:163]\n"
    "v_mfma_f32_16x16x32_bf16 v[164:167], v[32:35], v[68:71], v[164:167]\n"
    "v_mfma_f32_16x16x32_bf16 v[168:171], v[36:39], v[68:71], v[168:171]\n"
    "s_waitcnt vmcnt(9)\n"
    "v_mfma_f32_16x16x32_bf16 v[160:163], v[40:43], v[72:75], v[160:163]\n"
    "v_mfma_f32_16x16x32_bf16 v[164:167], v[40:43], v[76:79], v[164:167]\n"
    "v_mfma_f32_16x16x32_bf16 v[168:171], v[44:47], v[76:79], v[168:171]\n"
    "v_add_u32 v11, v8, v5\n"
    "v_cmp_gt_i32 vcc, s68, v11\n"
    "s_nop 0\n"
    "s_or_b64 exec, vcc, 1\n"
    "buffer_atomic_smax v8, v4, s[36:39], 0 offen sc0 sc1\n"
    "s_mov_b64 exec, -1\n"
    "s_add_u32 s72, s68, 1\n"
    "s_and_b32 s4, s72, 7\n"
    "s_mul_i32 s4, s4, 786432\n"
    "s_add_u32 s4, s4, s52\n"
    "s_add_u32 s5, s4, 4096\n"
    "s_add_u32 s6, s4, 8192\n"
    "s_add_u32 s7, s4, 12288\n"
    "s_add_u32 s8, s4, 16384\n"
    "s_add_u32 s9, s4, 20480\n"
    "s_and_b32 s73, s68, 7\n"
    "s_mul_i32 s73, s73, 196608\n"
    "s_add_u32 s73, s73, s53\n"
    "s_add_u32 s74, s73, 3072\n"
    "s_and_b32 s75, s68, 7\n"
    "s_lshl_b32 s75, s75, 18\n"
    "s_add_u32 s75, s75, s54\n"
    "s_add_u32 s76, s75, 4096\n"
    "v_cndmask_b32 v160, v160, v10, s[10:11]\n"
    "v_cndmask_b32 v161, v161, v10, s[12:13]\n"
    "v_cndmask_b32 v162, v162, v10, s[14:15]\n"
    "v_cndmask_b32 v163, v163, v10, s[16:17]\n"
    "v_cndmask_b32 v168, v168, v10, s[10:11]\n"
    "v_cndmask_b32 v169, v169, v10, s[12:13]\n"
    "v_cndmask_b32 v170, v170, v10, s[14:15]\n"
    "v_cndmask_b32 v171, v171, v10, s[16:17]\n"
    "v_max3_f32 v224, v160, v161, v162\n"
    "v_max3_f32 v226, v164, v165, v166\n"
    "v_max_f32 v224, v224, v163\n"
    "v_max3_f32 v226, v226, v167, v168\n"
    "v_max3_f32 v226, v226, v169, v170\n"
    "v_max_f32 v226, v226, v171\n"
    "v_mov_b32 v238, v224\n"
    "v_mov_b32 v239, v226\n"
    "s_nop 0\n"
    "v_permlane16_swap_b32 v224, v238\n"
    "v_permlane16_swap_b32 v226, v239\n"
    "v_max_f32 v224, v224, v238\n"
    "v_max_f32 v226, v226, v239\n"
    "v_mov_b32 v238, v224\n"
    "v_mov_b32 v239, v226\n"
    "s_nop 0\n"
    "v_permlane32_swap_b32 v224, v238\n"
    "v_permlane32_swap_b32 v226, v239\n"
    "v_max_f32 v224, v224, v238\n"
    "v_max_f32 v226, v226, v239\n"
    "v_mul_f32 v230, 0xbe0293ee, v224\n"
    "v_mul_f32 v232, 0xbe0293ee, v226\n"
    "v_pk_fma_f32 v[160:161], v[160:161], s[18:19], v[230:231] op_sel_hi:[1,0,0]\n"
    "v_pk_fma_f32 v[162:163], v[162:163], s[18:19], v[230:231] op_sel_hi:[1,0,0]\n"
    "v_pk_fma_f32 v[164:165], v[164:165], s[18:19], v[232:233] op_sel_hi:[1,0,0]\n"
    "v_pk_fma_f32 v[166:167], v[166:167], s[18:19], v[232:233] op_sel_hi:[1,0,0]\n"
    "v_pk_fma_f32 v[168:169], v[168:169], s[18:19], v[232:233] op_sel_hi:[1,0,0]\n"
    "v_pk_fma_f32 v[170:171], v[170:171], s[18:19], v[232:233] op_sel_hi:[1,0,0]\n"
    "v_exp_f32 v160, v160\n"
    "v_exp_f32 v161, v161\n"
    "v_exp_f32 v162, v162\n"
    "v_exp_f32 v163, v163\n"
    "v_exp_f32 v164, v164\n"
    "v_exp_f32 v165, v165\n"
    "v_exp_f32 v166, v166\n"
    "v_exp_f32 v167, v167\n"
    "v_exp_f32 v168, v168\n"
    "v_exp_f32 v169, v169\n"
    "v_exp_f32 v170, v170\n"
    "v_exp_f32 v171, v171\n"
    "v_pk_add_f32 v[230:231], v[160:161], v[162:163]\n"
    "v_pk_add_f32 v[232:233], v[164:165], v[166:167]\n"
    "v_pk_add_f32 v[234:235], v[168:169], v[170:171]\n"
    "v_pk_add_f32 v[232:233], v[232:233], v[234:235]\n"
    "v_add_f32 v228, v230, v231\n"
    "v_add_f32 v229, v232, v233\n"
    "v_mov_b32 v238, v228\n"
    "v_mov_b32 v239, v229\n"
    "s_nop 0\n"
    "v_permlane16_swap_b32 v228, v238\n"
    "v_permlane16_swap_b32 v229, v239\n"
    "v_add_f32 v228, v228, v238\n"
    "v_add_f32 v229, v229, v239\n"
    "v_mov_b32 v238, v228\n"
    "v_mov_b32 v239, v229\n"
    "s_nop 0\n"
    "v_permlane32_swap_b32 v228, v238\n"
    "v_permlane32_swap_b32 v229, v239\n"
    "v_add_f32 v228, v228, v238\n"
    "v_add_f32 v229, v229, v239\n"
    "s_waitcnt vmcnt(0)\n"
    ".Latt_pollr_%=:\n"
    "v_add_u32 v11, v8, v5\n"
    "v_cmp_gt_i32 vcc, s68, v11\n"
    "s_nop 0\n"
    "s_and_b32 s71, vcc_lo, 0xff\n"
    "s_cbranch_scc1 .Latt_waitr_%=\n"
    ".Latt_gor_%=:\n"
    "buffer_load_dwordx4 v[128:131], v1, s[36:39], s73 offen sc1\n"
    "buffer_load_dwordx4 v[132:135], v1, s[36:39], s73 offen offset:1536 sc1\n"
    "buffer_load_dwordx4 v[136:139], v1, s[36:39], s74 offen sc1\n"
    "buffer_load_dwordx4 v[140:143], v1, s[36:39], s74 offen offset:1536 sc1\n"
    "buffer_load_dwordx4 v[144:147], v0, s[36:39], s73 offen offset:512 sc1\n"
    "buffer_load_dwordx4 v[148:151], v0, s[36:39], s73 offen offset:2048 sc1\n"
    "buffer_load_dwordx4 v[152:155], v0, s[36:39], s74 offen offset:512 sc1\n"
    "buffer_load_dwordx4 v[156:159], v0, s[36:39], s74 offen offset:2048 sc1\n"
    "s_and_b32 s71, vcc_lo, 0xf00\n"
    "s_cselect_b32 s70, 0, 1\n"
    "s_cselect_b32 s71, 0x80000000, 0\n"
    "v_or_b32 v7, s71, v0\n"
    "s_nop 15\n"
    "buffer_load_dwordx4 v[16:19], v7, s[36:39], s6 offen sc1\n"
    "s_nop 15\n"
    "buffer_load_dwordx4 v[20:23], v7, s[36:39], s6 offen offset:1024 sc1\n"
    "s_nop 15\n"
    "buffer_load_dwordx4 v[24:27], v7, s[36:39], s6 offen offset:2048 sc1\n"
    "s_nop 15\n"
    "buffer_load_dwordx4 v[28:31], v7, s[36:39], s6 offen offset:3072 sc1\n"
    "s_waitcnt vmcnt(11)\n"
    "v_mfma_f32_16x16x32_bf16 v[172:175], v[128:131], v[48:51], 0\n"
    "v_mfma_f32_16x16x32_bf16 v[176:179], v[128:131], v[52:55], 0\n"
    "s_waitcnt vmcnt(10)\n"
    "v_mfma_f32_16x16x32_bf16 v[172:175], v[132:135], v[56:59], v[172:175]\n"
    "v_mfma_f32_16x16x32_bf16 v[176:179], v[132:135], v[60:63], v[176:179]\n"
    "s_waitcnt vmcnt(9)\n"
    "v_mfma_f32_16x16x32_bf16 v[172:175], v[136:139], v[64:67], v[172:175]\n"
    "v_mfma_f32_16x16x32_bf16 v[176:179], v[136:139], v[68:71], v[176:179]\n"
    "s_waitcnt vmcnt(8)\n"
    "v_mfma_f32_16x16x32_bf16 v[172:175], v[140:143], v[72:75], v[172:175]\n"
    "v_mfma_f32_16x16x32_bf16 v[176:179], v[140:143], v[76:79], v[176:179]\n"
    "s_waitcnt vmcnt(4)\n"
    "v_mov_b64 v[80:81], v[144:145]\n"
    "v_mov_b64 v[86:87], v[146:147]\n"
    "v_mov_b64 v[92:93], v[148:149]\n"
    "v_mov_b64 v[98:99], v[150:151]\n"
    "v_mov_b64 v[104:105], v[152:153]\n"
    "v_mov_b64 v[110:111], v[154:155]\n"
    "v_mov_b64 v[116:117], v[156:157]\n"
    "v_mov_b64 v[122:123], v[158:159]\n"
    "v_max3_f32 v230, v172, v173, v174\n"
    "v_max3_f32 v232, v176, v177, v178\n"
    "v_max3_f32 v230, v230, v175, v224\n"
    "v_max3_f32 v232, v232, v179, v226\n"
    "v_mov_b32 v238, v230\n"
    "v_mov_b32 v239, v232\n"
    "s_nop 0\n"
    "v_permlane16_swap_b32 v230, v238\n"
    "v_permlane16_swap_b32 v232, v239\n"
    "v_max_f32 v230, v230, v238\n"
    "v_max_f32 v232, v232, v239\n"
    "v_mul_f32 v230, 0xbe0293ee, v230\n"
    "v_mul_f32 v232, 0xbe0293ee, v232\n"
    "v_pk_fma_f32 v[172:173], v[172:173], s[18:19], v[230:231] op_sel_hi:[1,0,0]\n"
    "v_pk_fma_f32 v[174:175], v[174:175], s[18:19], v[230:231] op_sel_hi:[1,0,0]\n"
    "v_pk_fma_f32 v[176:177], v[176:177], s[18:19], v[232:233] op_sel_hi:[1,0,0]\n"
    "v_pk_fma_f32 v[178:179], v[178:179], s[18:19], v[232:233] op_sel_hi:[1,0,0]\n"
    "v_fma_f32 v224, v224, s18, v230\n"
    "v_fma_f32 v226, v226, s18, v232\n"
    "v_exp_f32 v172, v172\n"
    "v_exp_f32 v173, v173\n"
    "v_exp_f32 v174, v174\n"
    "v_exp_f32 v175, v175\n"
    "buffer_load_dwordx4 v[32:35], v7, s[36:39], s7 offen sc1\n"
    "v_exp_f32 v176, v176\n"
    "v_exp_f32 v177, v177\n"
    "v_exp_f32 v178, v178\n"
    "v_exp_f32 v179, v179\n"
    "buffer_load_dwordx4 v[36:39], v7, s[36:39], s7 offen offset:1024 sc1\n"
    "v_exp_f32 v224, v224\n"
    "v_exp_f32 v226, v226\n"
    "v_pk_add_f32 v[230:231], v[172:173], v[174:175]\n"
    "v_pk_add_f32 v[232:233], v[176:177], v[178:179]\n"
    "v_add_f32 v230, v230, v231\n"
    "v_add_f32 v232, v232, v233\n"
    "v_mov_b32 v238, v230\n"
    "v_mov_b32 v239, v232\n"
    "s_nop 0\n"
    "v_permlane16_swap_b32 v230, v238\n"
    "v_permlane16_swap_b32 v232, v239\n"
    "v_add_f32 v230, v230, v238\n"
    "v_add_f32 v232, v232, v239\n"
    "v_fmac_f32 v230, v228, v224\n"
    "v_fmac_f32 v232, v229, v226\n"
    "v_rcp_f32 v230, v230\n"
    "v_rcp_f32 v232, v232\n"
    "buffer_load_dwordx4 v[40:43], v7, s[36:39], s7 offen offset:2048 sc1\n"
    "v_mul_f32 v224, v224, v230\n"
    "v_mul_f32 v226, v226, v232\n"
    "v_pk_mul_f32 v[164:165], v[164:165], v[226:227] op_sel_hi:[1,0]\n"
    "v_pk_mul_f32 v[166:167], v[166:167], v[226:227] op_sel_hi:[1,0]\n"
    "v_pk_mul_f32 v[168:169], v[168:169], v[226:227] op_sel_hi:[1,0]\n"
    "v_pk_mul_f32 v[170:171], v[170:171], v[226:227] op_sel_hi:[1,0]\n"
    "v_pk_mul_f32 v[176:177], v[176:177], v[232:233] op_sel_hi:[1,0]\n"
    "v_pk_mul_f32 v[178:179], v[178:179], v[232:233] op_sel_hi:[1,0]\n"
    "v_pk_mul_f32 v[172:173], v[172:173], v[230:231] op_sel_hi:[1,0]\n"
    "v_pk_mul_f32 v[174:175], v[174:175], v[230:231] op_sel_hi:[1,0]\n"
    "v_pk_mul_f32 v[160:161], v[160:161], v[224:225] op_sel_hi:[1,0]\n"
    "v_pk_mul_f32 v[162:163], v[162:163], v[224:225] op_sel_hi:[1,0]\n"
    "v_cvt_pk_bf16_f32 v216, v164, v165\n"
    "v_cvt_pk_bf16_f32 v217, v166, v167\n"
    "v_cvt_pk_bf16_f32 v218, v168, v169\n"
    "v_cvt_pk_bf16_f32 v219, v170, v171\n"
    "v_cvt_pk_bf16_f32 v220, v176, v177\n"
    "v_cvt_pk_bf16_f32 v221, v178, v179\n"
    "v_cvt_pk_bf16_f32 v212, v172, v173\n"
    "v_cvt_pk_bf16_f32 v213, v174, v175\n"
    "v_cvt_pk_bf16_f32 v214, v160, v161\n"
    "v_cvt_pk_bf16_f32 v215, v162, v163\n"
    "v_mfma_f32_16x16x32_bf16 v[188:191], v[82:85], v[216:219], 0\n"
    "buffer_load_dwordx4 v[44:47], v7, s[36:39], s7 offen offset:3072 sc1\n"
    "v_mfma_f32_16x16x32_bf16 v[192:195], v[88:91], v[216:219], 0\n"
    "v_mfma_f32_16x16x32_bf16 v[180:183], v[80:83], v[212:215], 0\n"
    "v_mfma_f32_16x16x32_bf16 v[184:187], v[86:89], v[212:215], 0\n"
    "buffer_load_dwordx4 v[48:51], v7, s[36:39], s4 offen sc1\n"
    "v_mfma_f32_16x16x16_bf16 v[188:191], v[80:81], v[220:221], v[188:191]\n"
    "v_mfma_f32_16x16x16_bf16 v[192:195], v[86:87], v[220:221], v[192:195]\n"
    "v_mfma_f32_16x16x32_bf16 v[204:207], v[94:97], v[216:219], 0\n"
    "buffer_load_dwordx4 v[52:55], v7, s[36:39], s4 offen offset:1024 sc1\n"
    "v_mfma_f32_16x16x32_bf16 v[208:211], v[100:103], v[216:219], 0\n"
    "v_cvt_pk_bf16_f32 v180, v180, v181\n"
    "v_cvt_pk_bf16_f32 v181, v182, v183\n"
    "v_mfma_f32_16x16x32_bf16 v[196:199], v[92:95], v[212:215], 0\n"
    "v_cvt_pk_bf16_f32 v182, v184, v185\n"
    "v_cvt_pk_bf16_f32 v183, v186, v187\n"
    "v_mfma_f32_16x16x32_bf16 v[200:203], v[98:101], v[212:215], 0\n"
    "buffer_load_dwordx4 v[56:59], v7, s[36:39], s4 offen offset:2048 sc1\n"
    "buffer_store_dwordx4 v[180:183], v0, s[36:39], s75 offen sc1\n"
    "v_cvt_pk_bf16_f32 v188, v188, v189\n"
    "v_mfma_f32_16x16x16_bf16 v[204:207], v[92:93], v[220:221], v[204:207]\n"
    "v_cvt_pk_bf16_f32 v189, v190, v191\n"
    "v_cvt_pk_bf16_f32 v190, v192, v193\n"
    "v_mfma_f32_16x16x16_bf16 v[208:211], v[98:99], v[220:221], v[208:211]\n"
    "v_cvt_pk_bf16_f32 v191, v194, v195\n"
    "buffer_store_dwordx4 v[188:191], v0, s[36:39], s75 offen offset:1024 sc1\n"
    "s_nop 1\n"
    "v_mfma_f32_16x16x32_bf16 v[188:191], v[106:109], v[216:219], 0\n"
    "buffer_load_dwordx4 v[60:63], v7, s[36:39], s4 offen offset:3072 sc1\n"
    "v_mfma_f32_16x16x32_bf16 v[192:195], v[112:115], v[216:219], 0\n"
    "v_cvt_pk_bf16_f32 v196, v196, v197\n"
    "v_cvt_pk_bf16_f32 v197, v198, v199\n"
    "v_mfma_f32_16x16x32_bf16 v[180:183], v[104:107], v[212:215], 0\n"
    "v_cvt_pk_bf16_f32 v198, v200, v201\n"
    "v_cvt_pk_bf16_f32 v199, v202, v203\n"
    "v_mfma_f32_16x16x32_bf16 v[184:187], v[110:113], v[212:215], 0\n"
    "buffer_load_dwordx4 v[64:67], v7, s[36:39], s5 offen sc1\n"
    "buffer_store_dwordx4 v[196:199], v0, s[36:39], s75 offen offset:2048 sc1\n"
    "v_cvt_pk_bf16_f32 v204, v204, v205\n"
    "v_mfma_f32_16x16x16_bf16 v[188:191], v[104:105], v[220:221], v[188:191]\n"
    "v_cvt_pk_bf16_f32 v205, v206, v207\n"
    "v_cvt_pk_bf16_f32 v206, v208, v209\n"
    "v_mfma_f32_16x16x16_bf16 v[192:195], v[110:111], v[220:221], v[192:195]\n"
    "v_cvt_pk_bf16_f32 v207, v210, v211\n"
    "buffer_store_dwordx4 v[204:207], v0, s[36:39], s75 offen offset:3072 sc1\n"
    "s_nop 1\n"
    "v_mfma_f32_16x16x32_bf16 v[204:207], v[118:121], v[216:219], 0\n"
    "buffer_load_dwordx4 v[68:71], v7, s[36:39], s5 offen offset:1024 sc1\n"
    "v_mfma_f32_16x16x32_bf16 v[208:211], v[124:127], v[216:219], 0\n"
    "v_cvt_pk_bf16_f32 v180, v180, v181\n"
    "v_cvt_pk_bf16_f32 v181, v182, v183\n"
    "v_mfma_f32_16x16x32_bf16 v[196:199], v[116:119], v[212:215], 0\n"
    "v_cvt_pk_bf16_f32 v182, v184, v185\n"
    "v_cvt_pk_bf16_f32 v183, v186, v187\n"
    "v_mfma_f32_16x16x32_bf16 v[200:203], v[122:125], v[212:215], 0\n"
    "buffer_load_dwordx4 v[72:75], v7, s[36:39], s5 offen offset:2048 sc1\n"
    "buffer_store_dwordx4 v[180:183], v0, s[36:39], s76 offen sc1\n"
    "v_cvt_pk_bf16_f32 v188, v188, v189\n"
    "v_mfma_f32_16x16x16_bf16 v[204:207], v[116:117], v[220:221], v[204:207]\n"
    "v_cvt_pk_bf16_f32 v189, v190, v191\n"
    "v_cvt_pk_bf16_f32 v190, v192, v193\n"
    "v_mfma_f32_16x16x16_bf16 v[208:211], v[122:123], v[220:221], v[208:211]\n"
    "v_cvt_pk_bf16_f32 v191, v194, v195\n"
    "buffer_store_dwordx4 v[188:191], v0, s[36:39], s76 offen offset:1024 sc1\n"
    "v_cvt_pk_bf16_f32 v196, v196, v197\n"
    "v_cvt_pk_bf16_f32 v197, v198, v199\n"
    "v_cvt_pk_bf16_f32 v198, v200, v201\n"
    "v_cvt_pk_bf16_f32 v199, v202, v203\n"
    "buffer_store_dwordx4 v[196:199], v0, s[36:39], s76 offen offset:2048 sc1\n"
    "v_cvt_pk_bf16_f32 v204, v204, v205\n"
    "v_cvt_pk_bf16_f32 v205, v206, v207\n"
    "v_cvt_pk_bf16_f32 v206, v208, v209\n"
    "v_cvt_pk_bf16_f32 v207, v210, v211\n"
    "buffer_store_dwordx4 v[204:207], v0, s[36:39], s76 offen offset:3072 sc1\n"
    "buffer_load_dwordx4 v[76:79], v7, s[36:39], s5 offen offset:3072 sc1\n"
    "s_nop 7\n"
    "buffer_load_dwordx4 v[82:85], v7, s[36:39], s8 offen sc1\n"
    "s_nop 7\n"
    "buffer_load_dwordx4 v[88:91], v7, s[36:39], s8 offen offset:1024 sc1\n"
    "s_nop 7\n"
    "buffer_load_dwordx4 v[94:97], v7, s[36:39], s8 offen offset:2048 sc1\n"
    "s_nop 7\n"
    "buffer_load_dwordx4 v[100:103], v7, s[36:39], s8 offen offset:3072 sc1\n"
    "s_nop 7\n"
    "buffer_load_dwordx4 v[106:109], v7, s[36:39], s9 offen sc1\n"
    "s_nop 7\n"
    "buffer_load_dwordx4 v[112:115], v7, s[36:39], s9 offen offset:1024 sc1\n"
    "s_nop 7\n"
    "buffer_load_dwordx4 v[118:121], v7, s[36:39], s9 offen offset:2048 sc1\n"
    "s_nop 7\n"
    "buffer_load_dwordx4 v[124:127], v7, s[36:39], s9 offen offset:3072 sc1\n"
    "s_waitcnt vmcnt(9)\n"
    "v_mov_b32 v9, s68\n"
    "s_mov_b64 exec, 15\n"
    "buffer_store_dword v9, v2, s[36:39], 0 offen sc1\n"
    "s_mov_b64 exec, -1\n"
    "s_add_u32 s68, s68, 1\n"
    "s_cmp_lg_u32 s70, 0\n"
    "s_cbranch_scc1 .Latt_top_%=\n"
    ".Latt_LOOPE_%=:\n"
    "s_cmp_lg_u32 s68, 64\n"
    "s_cbranch_scc1 .Latt_waitp_%=\n"
    "s_waitcnt vmcnt(0)\n"
    "s_branch .Latt_FIN_%=\n"
    ".Latt_waitp_%=:\n"
    "s_sub_u32 s72, s68, 1\n"
    "v_add_u32 v11, v8, v5\n"
    "v_cmp_gt_i32 vcc, s72, v11\n"
    "s_nop 0\n"
    "s_or_b64 exec, vcc, 1\n"
    "buffer_atomic_smax v8, v4, s[36:39], 0 offen sc0 sc1\n"
    "s_mov_b64 exec, -1\n"
    "s_waitcnt vmcnt(0)\n"
    "v_add_u32 v11, v8, v5\n"
    "v_cmp_gt_i32 vcc, s72, v11\n"
    "s_cbranch_vccz .Latt_gop_%=\n"
    "s_sleep 2\n"
    "s_sub_u32 s69, s69, 1\n"
    "s_cbranch_scc0 .Latt_waitp_%=\n"
    "s_mov_b32 s69, 0\n"
    ".Latt_gop_%=:\n"
    "s_and_b32 s4, s68, 7\n"
    "s_mul_i32 s4, s4, 786432\n"
    "s_add_u32 s4, s4, s52\n"
    "s_add_u32 s5, s4, 4096\n"
    "s_add_u32 s6, s4, 8192\n"
    "s_add_u32 s7, s4, 12288\n"
    "s_add_u32 s8, s4, 16384\n"
    "s_add_u32 s9, s4, 20480\n"
    "buffer_load_dwordx4 v[16:19], v0, s[36:39], s6 offen sc1\n"
    "buffer_load_dwordx4 v[20:23], v0, s[36:39], s6 offen offset:1024 sc1\n"
    "buffer_load_dwordx4 v[24:27], v0, s[36:39], s6 offen offset:2048 sc1\n"
    "buffer_load_dwordx4 v[28:31], v0, s[36:39], s6 offen offset:3072 sc1\n"
    "buffer_load_dwordx4 v[32:35], v0, s[36:39], s7 offen sc1\n"
    "buffer_load_dwordx4 v[36:39], v0, s[36:39], s7 offen offset:1024 sc1\n"
    "buffer_load_dwordx4 v[40:43], v0, s[36:39], s7 offen offset:2048 sc1\n"
    "buffer_load_dwordx4 v[44:47], v0, s[36:39], s7 offen offset:3072 sc1\n"
    "buffer_load_dwordx4 v[48:51], v0, s[36:39], s4 offen sc1\n"
    "buffer_load_dwordx4 v[52:55], v0, s[36:39], s4 offen offset:1024 sc1\n"
    "buffer_load_dwordx4 v[56:59], v0, s[36:39], s4 offen offset:2048 sc1\n"
    "buffer_load_dwordx4 v[60:63], v0, s[36:39], s4 offen offset:3072 sc1\n"
    "buffer_load_dwordx4 v[64:67], v0, s[36:39], s5 offen sc1\n"
    "buffer_load_dwordx4 v[68:71], v0, s[36:39], s5 offen offset:1024 sc1\n"
    "buffer_load_dwordx4 v[72:75], v0, s[36:39], s5 offen offset:2048 sc1\n"
    "buffer_load_dwordx4 v[76:79], v0, s[36:39], s5 offen offset:3072 sc1\n"
    "buffer_load_dwordx4 v[82:85], v0, s[36:39], s8 offen sc1\n"
    "buffer_load_dwordx4 v[88:91], v0, s[36:39], s8 offen offset:1024 sc1\n"
    "buffer_load_dwordx4 v[94:97], v0, s[36:39], s8 offen offset:2048 sc1\n"
    "buffer_load_dwordx4 v[100:103], v0, s[36:39], s8 offen offset:3072 sc1\n"
    "buffer_load_dwordx4 v[106:109], v0, s[36:39], s9 offen sc1\n"
    "buffer_load_dwordx4 v[112:115], v0, s[36:39], s9 offen offset:1024 sc1\n"
    "buffer_load_dwordx4 v[118:121], v0, s[36:39], s9 offen offset:2048 sc1\n"
    "buffer_load_dwordx4 v[124:127], v0, s[36:39], s9 offen offset:3072 sc1\n"
    "s_waitcnt vmcnt(8)\n"
    "s_branch .Latt_top_%=\n"
    ".Latt_waitr_%=:\n"
    "s_sub_u32 s69, s69, 1\n"
    "s_cbranch_scc1 .Latt_deadr_%=\n"
    "s_sleep 2\n"
    "s_or_b64 exec, vcc, 1\n"
    "buffer_atomic_smax v8, v4, s[36:39], 0 offen sc0 sc1\n"
    "s_mov_b64 exec, -1\n"
    "s_waitcnt vmcnt(0)\n"
    "s_branch .Latt_pollr_%=\n"
    ".Latt_deadr_%=:\n"
    "s_mov_b32 s69, 0\n"
    "s_branch .Latt_gor_%=\n"
    ".Latt_FIN_%=:\n"
  );
}

template <int MODE>
DEVI void red_args(const Params& p, int L, int q, u32 dbg, v16u& sa, v16u& sb, v16u& va, v16u& vb) {
  const int tid = threadIdx.x, lane = tid & 63, w = (int)rfl(tid >> 6);
  const int c = lane & 15, g = lane >> 4;
  const u32 lb = OFF_L + L * LSZ;
  const bool rowres = MODE == 0 || (MODE == 1 && L == 0);
  const bool fin = MODE == 2 && L == 1;
  const v4u ws = make_srd(p.ws, WS_END), rx = make_srd(p.x, 2048 * 1152), ro = make_srd(p.out, 2048 * 1152);
  u32 ft = OOB;
  if (lane < 32) {
    if (MODE == 1) { if (lane < 24) ft = mbox(L, lane / 6, SL_UP + lane % 6, 4 * q + w); }
    else if (!fin) ft = mbox(MODE == 0 ? 0 : 1, lane >> 3, SL_P + (lane & 7), 4 * q + w);
  } else if (MODE != 0) {
    if (lane < 44) ft = mbox(L, (lane - 32) / 3, (MODE == 1 ? SL_WO : SL_DOWN) + (lane - 32) % 3, (MODE == 1 ? 8 : 24) + 4 * q + w);
    else if (lane == 44 && !rowres) ft = MODE == 1 ? mbox(0, q, SL_REDF, 48 + w) : mbox(L, q, SL_REDA, 48 + w);
  }
  const unsigned long long fm = __ballot(ft != OOB);
  sa = cat4(ws, rowres ? rx : ws, fin ? ro : ws, ws);
  sb = (v16u){lb + (MODE == 1 ? L_PA : L_PF),
              rowres ? 0u : (MODE == 1 ? OFF_X2 : lb + L_X1), rowres ? SEGB : RESB, (u32)(rowres ? 63 : (MODE == 1 ? RX2 - 1 : RX - 1)), rowres ? 128u : 1024u,
              MODE == 1 ? lb + L_X1 : OFF_X2, (u32)((MODE == 0 || fin) ? 63 : (MODE == 1 ? RX - 1 : RX2 - 1)),
              fin ? 0u : (MODE == 0 ? OFF_XN0 : (MODE == 1 ? lb + L_XN2 : OFF_XN1)), fin ? 128u : 4096u,
              0, dbg + w * 64, 0, (u32)fm, (u32)(fm >> 32), 0, 0};
  const int need = w == 0 ? 0x10F : (w == 1 ? 0x23F : (w == 2 ? 0x4F0 : 0xF00));
  const bool in = MODE != 0 && lane < 48 && ((need >> (lane % 12)) & 1);
  const bool back = MODE != 0 && lane >= 48 && lane < 52 && !fin;
  auto frag = [&](int I) { return (u32)((I >> 1) * 2048 + (I & 1) * 512 + (q >> 1) * 1024 + (c >> 3) * 256 + (8 * (q & 1) + (c & 7)) * 16); };
  auto rowm = [&](int I) { return (u32)((8 * q + (c & 7)) * 1152 + (2 * I + (c >> 3)) * 16); };
  const int I0 = 8 * w + 2 * (g & 1) + (g >> 1);
  const u32 pv = (u32)(q * 10240 + w * 2560 + lane * 16);
  va[0] = mbox(L, q, MODE == 1 ? SL_REDA : SL_REDF, lane);
  va[1] = (u32)(in ? 0 : (back ? (MODE == 1 ? RX : RX2) : 1000));
  va[2] = (u32)g * SEGB + frag(8 * w);
  va[3] = (u32)g * SEGB + frag(32 + w);
  va[4] = fin ? rowm(I0) : frag(I0);
  va[5] = g == 0 ? (fin ? rowm(32 + w) : frag(32 + w)) : OOB;
  va[6] = rowres ? rowm(I0) : pv;
  va[7] = g == 0 ? (rowres ? rowm(32 + w) : pv + 2048) : OOB;
  va[8] = pv;
  va[9] = g == 0 ? pv + 2048 : OOB;
  va[10] = (u32)(((c & 7) * 4 + w) * 8);
  va[11] = (u32)((c & 7) * 32);
  va[12] = g < 2 ? 0x3F800000u : 0u;
  va[13] = ft; va[14] = 0; va[15] = 0;
  const u32 one = 0x3F80u << (16 * (c & 1));
#pragma unroll
  for (int d = 0; d < 4; ++d) {
    const bool on = ((c & 7) >> 1) == d;
    vb[d] = (on && c < 8) ? one : 0u;
    vb[4 + d] = (on && c >= 8) ? one : 0u;
    vb[8 + d] = (on && g == 2 * (c >> 3)) ? one : 0u;
    vb[12 + d] = (on && g == 2 * (c >> 3) + 1) ? one : 0u;
  }
#pragma unroll
  for (int i = 0; i < 16; ++i) { sa[i] = rfl(sa[i]); sb[i] = rfl(sb[i]); }
}
#define RUN_RED(TEXT) asm volatile(TEXT : "+{s[36:51]}"(sa), "+{s[52:67]}"(sb), "+{v[0:15]}"(va), "+{v[16:31]}"(vb) :: CLOB_RED)
#define CLOB_RED \
  "v32", "v33", "v34", "v35", "v36", "v37", "v38", "v39", "v40", "v41", "v42", "v43", "v44", "v45", "v46", "v47", "v48", "v49", "v50", "v51", "v52", "v53", "v54", "v55", \
  "v56", "v57", "v58", "v59", "v60", "v61", "v62", "v63", "v64", "v65", "v66", "v67", "v68", "v69", "v70", "v71", "v72", "v73", "v74", "v75", "v76", "v77", "v78", "v79", \
  "v80", "v81", "v82", "v83", "v84", "v85", "v86", "v87", "v88", "v89", "v90", "v91", "v92", "v93", "v94", "v95", "v96", "v97", "v98", "v99", "v100", "v101", "v102", "v103", \
  "v104", "v105", "v106", "v107", "v108", "v109", "v110", "v111", "v112", "v113", "v114", "v115", "v116", "v117", "v118", "v119", "v120", "v121", "v122", "v123", "s68", "s69", "s70", "s71", \
  "s72", "s73", "s74", "s75", "s76", "s77", "s78", "s79", "s80", "s81", "s82", "s83", "s84", "s85", "s86", "s87", "s88", "s89", "s90", "s91", "s92", "s93", "s94", "s95", \
  "s96", "s97", "s98", "s99", "vcc", "scc", "memory"
DEVI void role_ln0(const Params& p, int q, u32 dbg) {
  v16u sa, sb, va, vb;
  red_args<0>(p, 0, q, dbg, sa, sb, va, vb);
  RUN_RED(
    "s_nop 4\n"
    "s_mov_b32 s69, 0\n"
    "v_mov_b32 v122, -1\n"
    "s_mov_b32 s70, 262144\n"
    "s_branch .LrLN0_go_%=\n"
    ".LrLN0_LOOPB_%=:\n"
    ".LrLN0_top_%=:\n"
    "s_mov_b32 s68, s69\n"
    "s_add_u32 s69, s68, 1\n"
    "s_and_b32 s75, s68, s58\n"
    "s_mul_i32 s77, s75, 36864\n"
    "s_add_u32 s77, s77, s59\n"
    "s_add_u32 s78, s77, s60\n"
    "s_and_b32 s73, s69, s55\n"
    "s_mul_i32 s73, s73, s54\n"
    "s_add_u32 s73, s73, s53\n"
    "s_add_u32 s74, s73, s56\n"
    "s_waitcnt vmcnt(3)\n"
    "v_mfma_f32_16x16x32_bf16 v[80:83], v[24:27], v[68:71], 0\n"
    "v_mfma_f32_16x16x32_bf16 v[84:87], v[28:31], v[68:71], 0\n"
    "s_waitcnt vmcnt(2)\n"
    "v_mfma_f32_16x16x32_bf16 v[88:91], v[24:27], v[72:75], 0\n"
    "v_mfma_f32_16x16x32_bf16 v[92:95], v[28:31], v[72:75], 0\n"
    "s_waitcnt vmcnt(1)\n"
    "v_mfma_f32_16x16x32_bf16 v[96:99], v[24:27], v[76:79], 0\n"
    "v_pk_add_f32 v[112:113], v[80:81], v[82:83]\n"
    "v_pk_add_f32 v[114:115], v[84:85], v[86:87]\n"
    "v_pk_add_f32 v[112:113], v[112:113], v[88:89]\n"
    "v_pk_add_f32 v[114:115], v[114:115], v[90:91]\n"
    "v_pk_add_f32 v[112:113], v[112:113], v[92:93]\n"
    "v_pk_add_f32 v[114:115], v[114:115], v[94:95]\n"
    "s_nop 1\n"
    "v_pk_add_f32 v[112:113], v[112:113], v[96:97]\n"
    "v_pk_add_f32 v[114:115], v[114:115], v[98:99]\n"
    "v_pk_add_f32 v[112:113], v[112:113], v[114:115]\n"
    "v_add_f32 v112, v112, v113\n"
    "s_nop 1\n"
    "v_add_f32_dpp v14, v112, v112 row_ror:8 row_mask:0xf bank_mask:0xf\n"
    "v_add_f32_dpp v15, v112, v112 row_ror:8 row_mask:0xf bank_mask:0xf\n"
    "s_nop 1\n"
    "v_permlane16_swap_b32 v14, v15\n"
    "v_add_f32 v112, v14, v15\n"
    "v_add_f32 v14, v14, v15\n"
    "s_nop 1\n"
    "v_permlane32_swap_b32 v112, v14\n"
    "v_add_f32 v112, v112, v14\n"
    "v_mul_f32 v112, 0x3be38e39, v112\n"
    "v_mul_f32 v114, v112, v12\n"
    "v_pk_add_f32 v[80:81], v[80:81], v[112:113] op_sel_hi:[1,0] neg_lo:[0,1] neg_hi:[0,1]\n"
    "v_pk_add_f32 v[82:83], v[82:83], v[112:113] op_sel_hi:[1,0] neg_lo:[0,1] neg_hi:[0,1]\n"
    "v_pk_add_f32 v[84:85], v[84:85], v[112:113] op_sel_hi:[1,0] neg_lo:[0,1] neg_hi:[0,1]\n"
    "v_pk_add_f32 v[86:87], v[86:87], v[112:113] op_sel_hi:[1,0] neg_lo:[0,1] neg_hi:[0,1]\n"
    "v_pk_add_f32 v[88:89], v[88:89], v[112:113] op_sel_hi:[1,0] neg_lo:[0,1] neg_hi:[0,1]\n"
    "v_pk_add_f32 v[90:91], v[90:91], v[112:113] op_sel_hi:[1,0] neg_lo:[0,1] neg_hi:[0,1]\n"
    "v_pk_add_f32 v[92:93], v[92:93], v[112:113] op_sel_hi:[1,0] neg_lo:[0,1] neg_hi:[0,1]\n"
    "v_pk_add_f32 v[94:95], v[94:95], v[112:113] op_sel_hi:[1,0] neg_lo:[0,1] neg_hi:[0,1]\n"
    "v_pk_add_f32 v[96:97], v[96:97], v[114:115] op_sel_hi:[1,0] neg_lo:[0,1] neg_hi:[0,1]\n"
    "v_pk_add_f32 v[98:99], v[98:99], v[114:115] op_sel_hi:[1,0] neg_lo:[0,1] neg_hi:[0,1]\n"
    "v_pk_mul_f32 v[116:117], v[80:81], v[80:81]\n"
    "v_pk_mul_f32 v[118:119], v[82:83], v[82:83]\n"
    "v_pk_fma_f32 v[116:117], v[84:85], v[84:85], v[116:117]\n"
    "v_pk_fma_f32 v[118:119], v[86:87], v[86:87], v[118:119]\n"
    "v_pk_fma_f32 v[116:117], v[88:89], v[88:89], v[116:117]\n"
    "v_pk_fma_f32 v[118:119], v[90:91], v[90:91], v[118:119]\n"
    "v_pk_fma_f32 v[116:117], v[92:93], v[92:93], v[116:117]\n"
    "v_pk_fma_f32 v[118:119], v[94:95], v[94:95], v[118:119]\n"
    "v_pk_fma_f32 v[116:117], v[96:97], v[96:97], v[116:117]\n"
    "v_pk_fma_f32 v[118:119], v[98:99], v[98:99], v[118:119]\n"
    "v_pk_add_f32 v[116:117], v[116:117], v[118:119]\n"
    "v_add_f32 v113, v116, v117\n"
    "s_nop 1\n"
    "v_add_f32_dpp v14, v113, v113 row_ror:8 row_mask:0xf bank_mask:0xf\n"
    "v_add_f32_dpp v15, v113, v113 row_ror:8 row_mask:0xf bank_mask:0xf\n"
    "s_nop 1\n"
    "v_permlane16_swap_b32 v14, v15\n"
    "v_add_f32 v113, v14, v15\n"
    "v_add_f32 v14, v14, v15\n"
    "s_nop 1\n"
    "v_permlane32_swap_b32 v113, v14\n"
    "v_add_f32 v113, v113, v14\n"
    "ds_write_b64 v10, v[112:113]\n"
    "s_waitcnt lgkmcnt(0)\n"
    "s_barrier\n"
    "ds_read_b128 v[32:35], v11\n"
    "ds_read_b128 v[36:39], v11 offset:16\n"
    "v_xor_b32 v10, 256, v10\n"
    "v_xor_b32 v11, 256, v11\n"
    "s_waitcnt lgkmcnt(0)\n"
    "v_pk_add_f32 v[114:115], v[32:33], v[34:35]\n"
    "v_pk_add_f32 v[116:117], v[36:37], v[38:39]\n"
    "v_pk_add_f32 v[114:115], v[114:115], v[116:117]\n"
    "v_fmac_f32 v32, 0xbe800000, v114\n"
    "v_fmac_f32 v34, 0xbe800000, v114\n"
    "v_fmac_f32 v36, 0xbe800000, v114\n"
    "v_fmac_f32 v38, 0xbe800000, v114\n"
    "v_fmac_f32 v112, 0xbe800000, v114\n"
    "v_mul_f32 v116, v32, v32\n"
    "v_fmac_f32 v116, v34, v34\n"
    "v_fmac_f32 v116, v36, v36\n"
    "v_fmac_f32 v116, v38, v38\n"
    "v_fmac_f32 v115, 0x43100000, v116\n"
    "v_mov_b32 v116, 0x3727c5ac\n"
    "v_fmac_f32 v116, 0x3ae38e39, v115\n"
    "v_rsq_f32 v116, v116\n"
    "s_nop 0\n"
    "v_mul_f32 v114, v112, v116\n"
    "v_pk_fma_f32 v[80:81], v[80:81], v[116:117], v[114:115] op_sel_hi:[1,0,0]\n"
    "v_pk_fma_f32 v[82:83], v[82:83], v[116:117], v[114:115] op_sel_hi:[1,0,0]\n"
    "v_pk_fma_f32 v[84:85], v[84:85], v[116:117], v[114:115] op_sel_hi:[1,0,0]\n"
    "v_pk_fma_f32 v[86:87], v[86:87], v[116:117], v[114:115] op_sel_hi:[1,0,0]\n"
    "v_pk_fma_f32 v[88:89], v[88:89], v[116:117], v[114:115] op_sel_hi:[1,0,0]\n"
    "v_pk_fma_f32 v[90:91], v[90:91], v[116:117], v[114:115] op_sel_hi:[1,0,0]\n"
    "v_pk_fma_f32 v[92:93], v[92:93], v[116:117], v[114:115] op_sel_hi:[1,0,0]\n"
    "v_pk_fma_f32 v[94:95], v[94:95], v[116:117], v[114:115] op_sel_hi:[1,0,0]\n"
    "v_pk_fma_f32 v[96:97], v[96:97], v[116:117], v[114:115] op_sel_hi:[1,0,0]\n"
    "v_pk_fma_f32 v[98:99], v[98:99], v[116:117], v[114:115] op_sel_hi:[1,0,0]\n"
    "v_cvt_pk_bf16_f32 v100, v80, v81\n"
    "v_cvt_pk_bf16_f32 v101, v82, v83\n"
    "v_cvt_pk_bf16_f32 v102, v84, v85\n"
    "v_cvt_pk_bf16_f32 v103, v86, v87\n"
    "v_cvt_pk_bf16_f32 v104, v88, v89\n"
    "v_cvt_pk_bf16_f32 v105, v90, v91\n"
    "v_cvt_pk_bf16_f32 v106, v92, v93\n"
    "v_cvt_pk_bf16_f32 v107, v94, v95\n"
    "v_cvt_pk_bf16_f32 v108, v96, v97\n"
    "v_cvt_pk_bf16_f32 v109, v98, v99\n"
    "v_permlane16_swap_b32 v100, v102\n"
    "v_permlane16_swap_b32 v101, v103\n"
    "buffer_store_dwordx4 v[100:103], v4, s[44:47], s77 offen sc1\n"
    "v_permlane16_swap_b32 v104, v106\n"
    "v_permlane16_swap_b32 v105, v107\n"
    "buffer_store_dwordx4 v[104:107], v4, s[44:47], s78 offen sc1\n"
    "v_permlane16_swap_b32 v108, v110\n"
    "v_permlane16_swap_b32 v109, v111\n"
    "buffer_store_dwordx4 v[108:111], v5, s[44:47], s77 offen sc1\n"
    "s_cmp_eq_u32 s69, 64\n"
    "s_cbranch_scc1 .LrLN0_fin_%=\n"
    "buffer_load_dwordx4 v[68:71], v6, s[40:43], s73 offen sc1\n"
    "buffer_load_dwordx4 v[72:75], v6, s[40:43], s74 offen sc1\n"
    "buffer_load_dwordx4 v[76:79], v7, s[40:43], s73 offen sc1\n"
    "s_waitcnt vmcnt(3)\n"
    "v_mov_b32 v14, s68\n"
    "s_mov_b64 exec, s[64:65]\n"
    "buffer_store_dword v14, v13, s[36:39], 0 offen sc1\n"
    "s_mov_b64 exec, -1\n"
    "s_branch .LrLN0_top_%=\n"
    ".LrLN0_LOOPE_%=:\n"
    ".LrLN0_fin_%=:\n"
    "s_waitcnt vmcnt(0)\n"
    "v_mov_b32 v14, s68\n"
    "s_mov_b64 exec, s[64:65]\n"
    "buffer_store_dword v14, v13, s[36:39], 0 offen sc1\n"
    "s_mov_b64 exec, -1\n"
    "s_waitcnt vmcnt(0)\n"
    "s_branch .LrLN0_FIN_%=\n"
    ".LrLN0_idle_%=:\n"
    "v_add_u32 v120, v122, v1\n"
    "v_cmp_gt_i32 vcc, s69, v120\n"
    "s_nop 0\n"
    "s_or_b64 exec, vcc, 1\n"
    "buffer_atomic_smax v122, v0, s[36:39], 0 offen sc0 sc1\n"
    "s_mov_b64 exec, -1\n"
    "s_waitcnt vmcnt(1)\n"
    "v_mov_b32 v14, s68\n"
    "s_mov_b64 exec, s[64:65]\n"
    "buffer_store_dword v14, v13, s[36:39], 0 offen sc1\n"
    "s_mov_b64 exec, -1\n"
    "s_waitcnt vmcnt(1)\n"
    "s_branch .LrLN0_block1_%=\n"
    ".LrLN0_block_%=:\n"
    "v_add_u32 v120, v122, v1\n"
    "v_cmp_gt_i32 vcc, s69, v120\n"
    "s_nop 0\n"
    "s_or_b64 exec, vcc, 1\n"
    "buffer_atomic_smax v122, v0, s[36:39], 0 offen sc0 sc1\n"
    "s_mov_b64 exec, -1\n"
    "s_waitcnt vmcnt(0)\n"
    ".LrLN0_block1_%=:\n"
    "v_add_u32 v120, v122, v1\n"
    "v_cmp_gt_i32 vcc, s69, v120\n"
    "s_cbranch_vccz .LrLN0_go_%=\n"
    "s_sleep 2\n"
    "s_sub_u32 s70, s70, 1\n"
    "s_cbranch_scc0 .LrLN0_block_%=\n"
    "s_mov_b32 s70, 0\n"
    ".LrLN0_go_%=:\n"
    "s_and_b32 s73, s69, s55\n"
    "s_mul_i32 s73, s73, s54\n"
    "s_add_u32 s73, s73, s53\n"
    "s_add_u32 s74, s73, s56\n"
    "buffer_load_dwordx4 v[68:71], v6, s[40:43], s73 offen sc1\n"
    "buffer_load_dwordx4 v[72:75], v6, s[40:43], s74 offen sc1\n"
    "buffer_load_dwordx4 v[76:79], v7, s[40:43], s73 offen sc1\n"
    "buffer_load_dword v121, v7, s[40:43], s73 offen\n"
    "s_branch .LrLN0_top_%=\n"
    ".LrLN0_FIN_%=:\n"
  );
}
DEVI void role_mid(v16u sa, v16u sb, v16u va, v16u vb) {
  RUN_RED(
    "s_nop 4\n"
    "s_mov_b32 s69, 0\n"
    "v_mov_b32 v122, -1\n"
    "s_mov_b32 s70, 262144\n"
    "s_branch .LrMID_block_%=\n"
    ".LrMID_LOOPB_%=:\n"
    ".LrMID_top_%=:\n"
    "s_mov_b32 s68, s69\n"
    "s_add_u32 s69, s68, 1\n"
    "s_and_b32 s75, s68, s58\n"
    "s_mul_i32 s77, s75, 36864\n"
    "s_add_u32 s77, s77, s59\n"
    "s_add_u32 s78, s77, s60\n"
    "s_and_b32 s71, s69, 7\n"
    "s_mul_i32 s71, s71, 147456\n"
    "s_add_u32 s71, s71, s52\n"
    "s_add_u32 s72, s71, 4096\n"
    "s_and_b32 s73, s69, s55\n"
    "s_mul_i32 s73, s73, s54\n"
    "s_add_u32 s73, s73, s53\n"
    "s_add_u32 s74, s73, s56\n"
    "s_mul_i32 s75, s75, 40960\n"
    "s_add_u32 s75, s75, s57\n"
    "s_add_u32 s76, s75, 1024\n"
    "s_waitcnt vmcnt(12)\n"
    "v_mfma_f32_16x16x32_bf16 v[80:83], v[16:19], v[32:35], 0\n"
    "s_waitcnt vmcnt(11)\n"
    "v_mfma_f32_16x16x32_bf16 v[80:83], v[20:23], v[36:39], v[80:83]\n"
    "s_waitcnt vmcnt(10)\n"
    "v_mfma_f32_16x16x32_bf16 v[84:87], v[16:19], v[40:43], 0\n"
    "s_waitcnt vmcnt(9)\n"
    "v_mfma_f32_16x16x32_bf16 v[84:87], v[20:23], v[44:47], v[84:87]\n"
    "s_waitcnt vmcnt(8)\n"
    "v_mfma_f32_16x16x32_bf16 v[88:91], v[16:19], v[48:51], 0\n"
    "s_waitcnt vmcnt(7)\n"
    "v_mfma_f32_16x16x32_bf16 v[88:91], v[20:23], v[52:55], v[88:91]\n"
    "s_waitcnt vmcnt(6)\n"
    "v_mfma_f32_16x16x32_bf16 v[92:95], v[16:19], v[56:59], 0\n"
    "s_waitcnt vmcnt(5)\n"
    "v_mfma_f32_16x16x32_bf16 v[92:95], v[20:23], v[60:63], v[92:95]\n"
    "s_waitcnt vmcnt(4)\n"
    "v_mfma_f32_16x16x32_bf16 v[96:99], v[16:19], v[64:67], 0\n"
    "s_waitcnt vmcnt(3)\n"
    "v_mfma_f32_16x16x32_bf16 v[80:83], v[24:27], v[68:71], v[80:83]\n"
    "v_mfma_f32_16x16x32_bf16 v[84:87], v[28:31], v[68:71], v[84:87]\n"
    "s_waitcnt vmcnt(2)\n"
    "v_mfma_f32_16x16x32_bf16 v[88:91], v[24:27], v[72:75], v[88:91]\n"
    "v_mfma_f32_16x16x32_bf16 v[92:95], v[28:31], v[72:75], v[92:95]\n"
    "s_waitcnt vmcnt(1)\n"
    "v_mfma_f32_16x16x32_bf16 v[96:99], v[24:27], v[76:79], v[96:99]\n"
    "v_pk_add_f32 v[112:113], v[80:81], v[82:83]\n"
    "v_pk_add_f32 v[114:115], v[84:85], v[86:87]\n"
    "v_pk_add_f32 v[112:113], v[112:113], v[88:89]\n"
    "v_pk_add_f32 v[114:115], v[114:115], v[90:91]\n"
    "v_pk_add_f32 v[112:113], v[112:113], v[92:93]\n"
    "v_pk_add_f32 v[114:115], v[114:115], v[94:95]\n"
    "s_nop 1\n"
    "v_pk_add_f32 v[112:113], v[112:113], v[96:97]\n"
    "v_pk_add_f32 v[114:115], v[114:115], v[98:99]\n"
    "v_pk_add_f32 v[112:113], v[112:113], v[114:115]\n"
    "v_add_f32 v112, v112, v113\n"
    "v_cvt_pk_bf16_f32 v100, v80, v81\n"
    "v_cvt_pk_bf16_f32 v101, v82, v83\n"
    "v_cvt_pk_bf16_f32 v102, v84, v85\n"
    "v_cvt_pk_bf16_f32 v103, v86, v87\n"
    "v_cvt_pk_bf16_f32 v104, v88, v89\n"
    "v_cvt_pk_bf16_f32 v105, v90, v91\n"
    "v_cvt_pk_bf16_f32 v106, v92, v93\n"
    "v_cvt_pk_bf16_f32 v107, v94, v95\n"
    "v_cvt_pk_bf16_f32 v108, v96, v97\n"
    "v_cvt_pk_bf16_f32 v109, v98, v99\n"
    "v_add_f32_dpp v14, v112, v112 row_ror:8 row_mask:0xf bank_mask:0xf\n"
    "v_add_f32_dpp v15, v112, v112 row_ror:8 row_mask:0xf bank_mask:0xf\n"
    "s_nop 1\n"
    "v_permlane16_swap_b32 v14, v15\n"
    "v_add_f32 v112, v14, v15\n"
    "v_add_f32 v14, v14, v15\n"
    "s_nop 1\n"
    "v_permlane32_swap_b32 v112, v14\n"
    "v_add_f32 v112, v112, v14\n"
    "v_mul_f32 v112, 0x3be38e39, v112\n"
    "v_mul_f32 v114, v112, v12\n"
    "v_pk_add_f32 v[80:81], v[80:81], v[112:113] op_sel_hi:[1,0] neg_lo:[0,1] neg_hi:[0,1]\n"
    "v_pk_add_f32 v[82:83], v[82:83], v[112:113] op_sel_hi:[1,0] neg_lo:[0,1] neg_hi:[0,1]\n"
    "v_pk_add_f32 v[84:85], v[84:85], v[112:113] op_sel_hi:[1,0] neg_lo:[0,1] neg_hi:[0,1]\n"
    "v_pk_add_f32 v[86:87], v[86:87], v[112:113] op_sel_hi:[1,0] neg_lo:[0,1] neg_hi:[0,1]\n"
    "v_pk_add_f32 v[88:89], v[88:89], v[112:113] op_sel_hi:[1,0] neg_lo:[0,1] neg_hi:[0,1]\n"
    "v_pk_add_f32 v[90:91], v[90:91], v[112:113] op_sel_hi:[1,0] neg_lo:[0,1] neg_hi:[0,1]\n"
    "v_pk_add_f32 v[92:93], v[92:93], v[112:113] op_sel_hi:[1,0] neg_lo:[0,1] neg_hi:[0,1]\n"
    "v_pk_add_f32 v[94:95], v[94:95], v[112:113] op_sel_hi:[1,0] neg_lo:[0,1] neg_hi:[0,1]\n"
    "v_pk_add_f32 v[96:97], v[96:97], v[114:115] op_sel_hi:[1,0] neg_lo:[0,1] neg_hi:[0,1]\n"
    "v_pk_add_f32 v[98:99], v[98:99], v[114:115] op_sel_hi:[1,0] neg_lo:[0,1] neg_hi:[0,1]\n"
    "v_pk_mul_f32 v[116:117], v[80:81], v[80:81]\n"
    "v_pk_mul_f32 v[118:119], v[82:83], v[82:83]\n"
    "v_pk_fma_f32 v[116:117], v[84:85], v[84:85], v[116:117]\n"
    "v_pk_fma_f32 v[118:119], v[86:87], v[86:87], v[118:119]\n"
    "v_pk_fma_f32 v[116:117], v[88:89], v[88:89], v[116:117]\n"
    "v_pk_fma_f32 v[118:119], v[90:91], v[90:91], v[118:119]\n"
    "v_pk_fma_f32 v[116:117], v[92:93], v[92:93], v[116:117]\n"
    "v_pk_fma_f32 v[118:119], v[94:95], v[94:95], v[118:119]\n"
    "v_pk_fma_f32 v[116:117], v[96:97], v[96:97], v[116:117]\n"
    "v_pk_fma_f32 v[118:119], v[98:99], v[98:99], v[118:119]\n"
    "v_pk_add_f32 v[116:117], v[116:117], v[118:119]\n"
    "v_add_f32 v113, v116, v117\n"
    "s_nop 1\n"
    "v_add_f32_dpp v14, v113, v113 row_ror:8 row_mask:0xf bank_mask:0xf\n"
    "v_add_f32_dpp v15, v113, v113 row_ror:8 row_mask:0xf bank_mask:0xf\n"
    "s_nop 1\n"
    "v_permlane16_swap_b32 v14, v15\n"
    "v_add_f32 v113, v14, v15\n"
    "v_add_f32 v14, v14, v15\n"
    "s_nop 1\n"
    "v_permlane32_swap_b32 v113, v14\n"
    "v_add_f32 v113, v113, v14\n"
    "ds_write_b64 v10, v[112:113]\n"
    "v_add_u32 v120, v122, v1\n"
    "v_cmp_gt_i32 vcc, s69, v120\n"
    "s_nop 0\n"
    "s_or_b64 exec, vcc, 1\n"
    "buffer_atomic_smax v122, v0, s[36:39], 0 offen sc0 sc1\n"
    "s_mov_b64 exec, -1\n"
    "s_nop 0\n"
    "v_permlane16_swap_b32 v100, v102\n"
    "v_permlane16_swap_b32 v101, v103\n"
    "v_permlane16_swap_b32 v104, v106\n"
    "v_permlane16_swap_b32 v105, v107\n"
    "v_permlane16_swap_b32 v108, v110\n"
    "v_permlane16_swap_b32 v109, v111\n"
    "s_waitcnt lgkmcnt(0)\n"
    "s_barrier\n"
    "ds_read_b128 v[32:35], v11\n"
    "ds_read_b128 v[36:39], v11 offset:16\n"
    "buffer_store_dwordx4 v[100:103], v8, s[36:39], s75 offen sc1\n"
    "buffer_store_dwordx4 v[104:107], v8, s[36:39], s76 offen sc1\n"
    "buffer_store_dwordx4 v[108:111], v9, s[36:39], s75 offen sc1\n"
    "v_xor_b32 v10, 256, v10\n"
    "v_xor_b32 v11, 256, v11\n"
    "s_waitcnt lgkmcnt(0)\n"
    "v_pk_add_f32 v[114:115], v[32:33], v[34:35]\n"
    "v_pk_add_f32 v[116:117], v[36:37], v[38:39]\n"
    "v_pk_add_f32 v[114:115], v[114:115], v[116:117]\n"
    "v_fmac_f32 v32, 0xbe800000, v114\n"
    "v_fmac_f32 v34, 0xbe800000, v114\n"
    "v_fmac_f32 v36, 0xbe800000, v114\n"
    "v_fmac_f32 v38, 0xbe800000, v114\n"
    "v_fmac_f32 v112, 0xbe800000, v114\n"
    "v_mul_f32 v116, v32, v32\n"
    "v_fmac_f32 v116, v34, v34\n"
    "v_fmac_f32 v116, v36, v36\n"
    "v_fmac_f32 v116, v38, v38\n"
    "v_fmac_f32 v115, 0x43100000, v116\n"
    "v_mov_b32 v116, 0x3727c5ac\n"
    "v_fmac_f32 v116, 0x3ae38e39, v115\n"
    "v_rsq_f32 v116, v116\n"
    "s_nop 0\n"
    "v_mul_f32 v114, v112, v116\n"
    "v_pk_fma_f32 v[80:81], v[80:81], v[116:117], v[114:115] op_sel_hi:[1,0,0]\n"
    "v_pk_fma_f32 v[82:83], v[82:83], v[116:117], v[114:115] op_sel_hi:[1,0,0]\n"
    "v_pk_fma_f32 v[84:85], v[84:85], v[116:117], v[114:115] op_sel_hi:[1,0,0]\n"
    "v_pk_fma_f32 v[86:87], v[86:87], v[116:117], v[114:115] op_sel_hi:[1,0,0]\n"
    "v_pk_fma_f32 v[88:89], v[88:89], v[116:117], v[114:115] op_sel_hi:[1,0,0]\n"
    "v_pk_fma_f32 v[90:91], v[90:91], v[116:117], v[114:115] op_sel_hi:[1,0,0]\n"
    "v_pk_fma_f32 v[92:93], v[92:93], v[116:117], v[114:115] op_sel_hi:[1,0,0]\n"
    "v_pk_fma_f32 v[94:95], v[94:95], v[116:117], v[114:115] op_sel_hi:[1,0,0]\n"
    "v_pk_fma_f32 v[96:97], v[96:97], v[116:117], v[114:115] op_sel_hi:[1,0,0]\n"
    "v_pk_fma_f32 v[98:99], v[98:99], v[116:117], v[114:115] op_sel_hi:[1,0,0]\n"
    "v_cvt_pk_bf16_f32 v100, v80, v81\n"
    "v_cvt_pk_bf16_f32 v101, v82, v83\n"
    "v_cvt_pk_bf16_f32 v102, v84, v85\n"
    "v_cvt_pk_bf16_f32 v103, v86, v87\n"
    "v_cvt_pk_bf16_f32 v104, v88, v89\n"
    "v_cvt_pk_bf16_f32 v105, v90, v91\n"
    "v_cvt_pk_bf16_f32 v106, v92, v93\n"
    "v_cvt_pk_bf16_f32 v107, v94, v95\n"
    "v_cvt_pk_bf16_f32 v108, v96, v97\n"
    "v_cvt_pk_bf16_f32 v109, v98, v99\n"
    "v_permlane16_swap_b32 v100, v102\n"
    "v_permlane16_swap_b32 v101, v103\n"
    "buffer_store_dwordx4 v[100:103], v4, s[44:47], s77 offen sc1\n"
    "v_permlane16_swap_b32 v104, v106\n"
    "v_permlane16_swap_b32 v105, v107\n"
    "buffer_store_dwordx4 v[104:107], v4, s[44:47], s78 offen sc1\n"
    "v_permlane16_swap_b32 v108, v110\n"
    "v_permlane16_swap_b32 v109, v111\n"
    "buffer_store_dwordx4 v[108:111], v5, s[44:47], s77 offen sc1\n"
    "s_cmp_eq_u32 s69, 64\n"
    "s_cbranch_scc1 .LrMID_fin_%=\n"
    "s_waitcnt vmcnt(6)\n"
    "v_add_u32 v120, v122, v1\n"
    "v_cmp_gt_i32 vcc, s69, v120\n"
    "s_cbranch_vccnz .LrMID_idle_%=\n"
    "buffer_load_dwordx4 v[32:35], v2, s[36:39], s71 offen sc1\n"
    "buffer_load_dwordx4 v[36:39], v2, s[36:39], s71 offen offset:512 sc1\n"
    "buffer_load_dwordx4 v[40:43], v2, s[36:39], s71 offen offset:2048 sc1\n"
    "buffer_load_dwordx4 v[44:47], v2, s[36:39], s71 offen offset:2560 sc1\n"
    "buffer_load_dwordx4 v[48:51], v2, s[36:39], s72 offen sc1\n"
    "buffer_load_dwordx4 v[52:55], v2, s[36:39], s72 offen offset:512 sc1\n"
    "buffer_load_dwordx4 v[56:59], v2, s[36:39], s72 offen offset:2048 sc1\n"
    "buffer_load_dwordx4 v[60:63], v2, s[36:39], s72 offen offset:2560 sc1\n"
    "buffer_load_dwordx4 v[64:67], v3, s[36:39], s71 offen sc1\n"
    "buffer_load_dwordx4 v[68:71], v6, s[40:43], s73 offen sc1\n"
    "buffer_load_dwordx4 v[72:75], v6, s[40:43], s74 offen sc1\n"
    "buffer_load_dwordx4 v[76:79], v7, s[40:43], s73 offen sc1\n"
    "s_waitcnt vmcnt(12)\n"
    "v_mov_b32 v14, s68\n"
    "s_mov_b64 exec, s[64:65]\n"
    "buffer_store_dword v14, v13, s[36:39], 0 offen sc1\n"
    "s_mov_b64 exec, -1\n"
    "s_branch .LrMID_top_%=\n"
    ".LrMID_LOOPE_%=:\n"
    ".LrMID_fin_%=:\n"
    "s_waitcnt vmcnt(0)\n"
    "v_mov_b32 v14, s68\n"
    "s_mov_b64 exec, s[64:65]\n"
    "buffer_store_dword v14, v13, s[36:39], 0 offen sc1\n"
    "s_mov_b64 exec, -1\n"
    "s_waitcnt vmcnt(0)\n"
    "s_branch .LrMID_FIN_%=\n"
    ".LrMID_idle_%=:\n"
    "v_add_u32 v120, v122, v1\n"
    "v_cmp_gt_i32 vcc, s69, v120\n"
    "s_nop 0\n"
    "s_or_b64 exec, vcc, 1\n"
    "buffer_atomic_smax v122, v0, s[36:39], 0 offen sc0 sc1\n"
    "s_mov_b64 exec, -1\n"
    "s_waitcnt vmcnt(1)\n"
    "v_mov_b32 v14, s68\n"
    "s_mov_b64 exec, s[64:65]\n"
    "buffer_store_dword v14, v13, s[36:39], 0 offen sc1\n"
    "s_mov_b64 exec, -1\n"
    "s_waitcnt vmcnt(1)\n"
    "s_branch .LrMID_block1_%=\n"
    ".LrMID_block_%=:\n"
    "v_add_u32 v120, v122, v1\n"
    "v_cmp_gt_i32 vcc, s69, v120\n"
    "s_nop 0\n"
    "s_or_b64 exec, vcc, 1\n"
    "buffer_atomic_smax v122, v0, s[36:39], 0 offen sc0 sc1\n"
    "s_mov_b64 exec, -1\n"
    "s_waitcnt vmcnt(0)\n"
    ".LrMID_block1_%=:\n"
    "v_add_u32 v120, v122, v1\n"
    "v_cmp_gt_i32 vcc, s69, v120\n"
    "s_cbranch_vccz .LrMID_go_%=\n"
    "s_sleep 2\n"
    "s_sub_u32 s70, s70, 1\n"
    "s_cbranch_scc0 .LrMID_block_%=\n"
    "s_mov_b32 s70, 0\n"
    ".LrMID_go_%=:\n"
    "s_and_b32 s71, s69, 7\n"
    "s_mul_i32 s71, s71, 147456\n"
    "s_add_u32 s71, s71, s52\n"
    "s_add_u32 s72, s71, 4096\n"
    "s_and_b32 s73, s69, s55\n"
    "s_mul_i32 s73, s73, s54\n"
    "s_add_u32 s73, s73, s53\n"
    "s_add_u32 s74, s73, s56\n"
    "buffer_load_dwordx4 v[32:35], v2, s[36:39], s71 offen sc1\n"
    "buffer_load_dwordx4 v[36:39], v2, s[36:39], s71 offen offset:512 sc1\n"
    "buffer_load_dwordx4 v[40:43], v2, s[36:39], s71 offen offset:2048 sc1\n"
    "buffer_load_dwordx4 v[44:47], v2, s[36:39], s71 offen offset:2560 sc1\n"
    "buffer_load_dwordx4 v[48:51], v2, s[36:39], s72 offen sc1\n"
    "buffer_load_dwordx4 v[52:55], v2, s[36:39], s72 offen offset:512 sc1\n"
    "buffer_load_dwordx4 v[56:59], v2, s[36:39], s72 offen offset:2048 sc1\n"
    "buffer_load_dwordx4 v[60:63], v2, s[36:39], s72 offen offset:2560 sc1\n"
    "buffer_load_dwordx4 v[64:67], v3, s[36:39], s71 offen sc1\n"
    "buffer_load_dwordx4 v[68:71], v6, s[40:43], s73 offen sc1\n"
    "buffer_load_dwordx4 v[72:75], v6, s[40:43], s74 offen sc1\n"
    "buffer_load_dwordx4 v[76:79], v7, s[40:43], s73 offen sc1\n"
    "buffer_load_dword v121, v7, s[40:43], s73 offen\n"
    "s_branch .LrMID_top_%=\n"
    ".LrMID_FIN_%=:\n"
  );
}
DEVI void role_fin(const Params& p, int q, u32 dbg) {
  v16u sa, sb, va, vb;
  red_args<2>(p, 1, q, dbg, sa, sb, va, vb);
  RUN_RED(
    "s_nop 4\n"
    "s_mov_b32 s69, 0\n"
    "v_mov_b32 v122, -1\n"
    "s_mov_b32 s70, 262144\n"
    "s_branch .LrFIN_block_%=\n"
    ".LrFIN_LOOPB_%=:\n"
    ".LrFIN_top_%=:\n"
    "s_mov_b32 s68, s69\n"
    "s_add_u32 s69, s68, 1\n"
    "s_and_b32 s75, s68, s58\n"
    "s_mul_i32 s77, s75, 36864\n"
    "s_add_u32 s77, s77, s59\n"
    "s_add_u32 s78, s77, s60\n"
    "s_and_b32 s71, s69, 7\n"
    "s_mul_i32 s71, s71, 147456\n"
    "s_add_u32 s71, s71, s52\n"
    "s_add_u32 s72, s71, 4096\n"
    "s_and_b32 s73, s69, s55\n"
    "s_mul_i32 s73, s73, s54\n"
    "s_add_u32 s73, s73, s53\n"
    "s_add_u32 s74, s73, s56\n"
    "s_waitcnt vmcnt(11)\n"
    "v_mfma_f32_16x16x32_bf16 v[80:83], v[16:19], v[32:35], 0\n"
    "s_waitcnt vmcnt(10)\n"
    "v_mfma_f32_16x16x32_bf16 v[80:83], v[20:23], v[36:39], v[80:83]\n"
    "s_waitcnt vmcnt(9)\n"
    "v_mfma_f32_16x16x32_bf16 v[84:87], v[16:19], v[40:43], 0\n"
    "s_waitcnt vmcnt(8)\n"
    "v_mfma_f32_16x16x32_bf16 v[84:87], v[20:23], v[44:47], v[84:87]\n"
    "s_waitcnt vmcnt(7)\n"
    "v_mfma_f32_16x16x32_bf16 v[88:91], v[16:19], v[48:51], 0\n"
    "s_waitcnt vmcnt(6)\n"
    "v_mfma_f32_16x16x32_bf16 v[88:91], v[20:23], v[52:55], v[88:91]\n"
    "s_waitcnt vmcnt(5)\n"
    "v_mfma_f32_16x16x32_bf16 v[92:95], v[16:19], v[56:59], 0\n"
    "s_waitcnt vmcnt(4)\n"
    "v_mfma_f32_16x16x32_bf16 v[92:95], v[20:23], v[60:63], v[92:95]\n"
    "s_waitcnt vmcnt(3)\n"
    "v_mfma_f32_16x16x32_bf16 v[96:99], v[16:19], v[64:67], 0\n"
    "s_waitcnt vmcnt(2)\n"
    "v_mfma_f32_16x16x32_bf16 v[80:83], v[24:27], v[68:71], v[80:83]\n"
    "v_mfma_f32_16x16x32_bf16 v[84:87], v[28:31], v[68:71], v[84:87]\n"
    "s_waitcnt vmcnt(1)\n"
    "v_mfma_f32_16x16x32_bf16 v[88:91], v[24:27], v[72:75], v[88:91]\n"
    "v_mfma_f32_16x16x32_bf16 v[92:95], v[28:31], v[72:75], v[92:95]\n"
    "s_waitcnt vmcnt(0)\n"
    "v_mfma_f32_16x16x32_bf16 v[96:99], v[24:27], v[76:79], v[96:99]\n"
    "v_add_u32 v120, v122, v1\n"
    "v_cmp_gt_i32 vcc, s69, v120\n"
    "s_nop 0\n"
    "s_or_b64 exec, vcc, 1\n"
    "buffer_atomic_smax v122, v0, s[36:39], 0 offen sc0 sc1\n"
    "s_mov_b64 exec, -1\n"
    "v_mov_b32 v14, s68\n"
    "s_mov_b64 exec, s[64:65]\n"
    "buffer_store_dword v14, v13, s[36:39], 0 offen sc1\n"
    "s_mov_b64 exec, -1\n"
    "v_cvt_pk_bf16_f32 v100, v80, v81\n"
    "v_cvt_pk_bf16_f32 v101, v82, v83\n"
    "v_cvt_pk_bf16_f32 v102, v84, v85\n"
    "v_cvt_pk_bf16_f32 v103, v86, v87\n"
    "v_cvt_pk_bf16_f32 v104, v88, v89\n"
    "v_cvt_pk_bf16_f32 v105, v90, v91\n"
    "v_cvt_pk_bf16_f32 v106, v92, v93\n"
    "v_cvt_pk_bf16_f32 v107, v94, v95\n"
    "v_cvt_pk_bf16_f32 v108, v96, v97\n"
    "v_cvt_pk_bf16_f32 v109, v98, v99\n"
    "v_permlane16_swap_b32 v100, v102\n"
    "v_permlane16_swap_b32 v101, v103\n"
    "buffer_store_dwordx4 v[100:103], v4, s[44:47], s77 offen\n"
    "v_permlane16_swap_b32 v104, v106\n"
    "v_permlane16_swap_b32 v105, v107\n"
    "buffer_store_dwordx4 v[104:107], v4, s[44:47], s78 offen\n"
    "v_permlane16_swap_b32 v108, v110\n"
    "v_permlane16_swap_b32 v109, v111\n"
    "buffer_store_dwordx4 v[108:111], v5, s[44:47], s77 offen\n"
    "s_cmp_eq_u32 s69, 64\n"
    "s_cbranch_scc1 .LrFIN_fin_%=\n"
    "s_waitcnt vmcnt(4)\n"
    "v_add_u32 v120, v122, v1\n"
    "v_cmp_gt_i32 vcc, s69, v120\n"
    "s_cbranch_vccnz .LrFIN_idle_%=\n"
    "buffer_load_dwordx4 v[32:35], v2, s[36:39], s71 offen sc1\n"
    "buffer_load_dwordx4 v[36:39], v2, s[36:39], s71 offen offset:512 sc1\n"
    "buffer_load_dwordx4 v[40:43], v2, s[36:39], s71 offen offset:2048 sc1\n"
    "buffer_load_dwordx4 v[44:47], v2, s[36:39], s71 offen offset:2560 sc1\n"
    "buffer_load_dwordx4 v[48:51], v2, s[36:39], s72 offen sc1\n"
    "buffer_load_dwordx4 v[52:55], v2, s[36:39], s72 offen offset:512 sc1\n"
    "buffer_load_dwordx4 v[56:59], v2, s[36:39], s72 offen offset:2048 sc1\n"
    "buffer_load_dwordx4 v[60:63], v2, s[36:39], s72 offen offset:2560 sc1\n"
    "buffer_load_dwordx4 v[64:67], v3, s[36:39], s71 offen sc1\n"
    "buffer_load_dwordx4 v[68:71], v6, s[40:43], s73 offen sc1\n"
    "buffer_load_dwordx4 v[72:75], v6, s[40:43], s74 offen sc1\n"
    "buffer_load_dwordx4 v[76:79], v7, s[40:43], s73 offen sc1\n"
    "s_branch .LrFIN_top_%=\n"
    ".LrFIN_LOOPE_%=:\n"
    ".LrFIN_fin_%=:\n"
    "s_waitcnt vmcnt(0)\n"
    "s_branch .LrFIN_FIN_%=\n"
    ".LrFIN_idle_%=:\n"
    ".LrFIN_block_%=:\n"
    "v_add_u32 v120, v122, v1\n"
    "v_cmp_gt_i32 vcc, s69, v120\n"
    "s_nop 0\n"
    "s_or_b64 exec, vcc, 1\n"
    "buffer_atomic_smax v122, v0, s[36:39], 0 offen sc0 sc1\n"
    "s_mov_b64 exec, -1\n"
    "s_waitcnt vmcnt(0)\n"
    ".LrFIN_block1_%=:\n"
    "v_add_u32 v120, v122, v1\n"
    "v_cmp_gt_i32 vcc, s69, v120\n"
    "s_cbranch_vccz .LrFIN_go_%=\n"
    "s_sleep 2\n"
    "s_sub_u32 s70, s70, 1\n"
    "s_cbranch_scc0 .LrFIN_block_%=\n"
    "s_mov_b32 s70, 0\n"
    ".LrFIN_go_%=:\n"
    "s_and_b32 s71, s69, 7\n"
    "s_mul_i32 s71, s71, 147456\n"
    "s_add_u32 s71, s71, s52\n"
    "s_add_u32 s72, s71, 4096\n"
    "s_and_b32 s73, s69, s55\n"
    "s_mul_i32 s73, s73, s54\n"
    "s_add_u32 s73, s73, s53\n"
    "s_add_u32 s74, s73, s56\n"
    "buffer_load_dwordx4 v[32:35], v2, s[36:39], s71 offen sc1\n"
    "buffer_load_dwordx4 v[36:39], v2, s[36:39], s71 offen offset:512 sc1\n"
    "buffer_load_dwordx4 v[40:43], v2, s[36:39], s71 offen offset:2048 sc1\n"
    "buffer_load_dwordx4 v[44:47], v2, s[36:39], s71 offen offset:2560 sc1\n"
    "buffer_load_dwordx4 v[48:51], v2, s[36:39], s72 offen sc1\n"
    "buffer_load_dwordx4 v[52:55], v2, s[36:39], s72 offen offset:512 sc1\n"
    "buffer_load_dwordx4 v[56:59], v2, s[36:39], s72 offen offset:2048 sc1\n"
    "buffer_load_dwordx4 v[60:63], v2, s[36:39], s72 offen offset:2560 sc1\n"
    "buffer_load_dwordx4 v[64:67], v3, s[36:39], s71 offen sc1\n"
    "buffer_load_dwordx4 v[68:71], v6, s[40:43], s73 offen sc1\n"
    "buffer_load_dwordx4 v[72:75], v6, s[40:43], s74 offen sc1\n"
    "buffer_load_dwordx4 v[76:79], v7, s[40:43], s73 offen sc1\n"
    "s_branch .LrFIN_top_%=\n"
    ".LrFIN_FIN_%=:\n"
  );
}

__global__ void __launch_bounds__(256, 1) mega(Params p) {
  extern __shared__ __attribute__((aligned(16))) unsigned char smem[];
  const rsrc_t rw = __builtin_amdgcn_make_buffer_rsrc(p.ws, 0, WS_END, 0x00020000);
  if (threadIdx.x == 0) {
    const int x0 = (int)(__builtin_amdgcn_s_getreg(20 | (3 << 11)) & 7u);
    int rid = 0;
    for (int k = 0; k < 8; ++k) {
      const int x = (x0 + k) & 7;
      const u32 t = __hip_atomic_fetch_add((u32*)(p.ws + TICK + x * 128), 1u, __ATOMIC_RELAXED, __HIP_MEMORY_SCOPE_AGENT) + 1u;
      if (t < 32u) {
        rid = x * 32 + (int)t;
        if (PLACE == 1) {
          const int u = (int)t >> 1, jj = 2 * (x & 1) + ((int)t & 1);
          const int sl = (x & 2) == 0 ? (u < 8 ? u : (u < 14 ? SL_UP + u - 8 : SL_REDA + u - 14)) : (u < 13 ? SL_P + u : SL_DOWN + u - 13);
          rid = ((x & 4) + jj) * 32 + sl;
        }
        break;
      }
    }
    *(volatile int*)smem = rid;
  }
  __syncthreads();
  const int rid = (int)rfl(*(volatile u32*)smem);
  __syncthreads();
  const int L = rid >> 7, j = (rid >> 5) & 3, sl = rid & 31;
  const u32 dbg = OFF_DBG + (u32)rid * 66 * 256;
  auto on = [&](int r) { return ((p.role_mask >> (2 + 8 * L + r)) & 1) != 0; };
  STAMP(65, 8);
  if (sl < SL_P) { delay_start(L ? DL_R1 : 0); if (on(1)) role_rec(p, L, j, sl, dbg, smem); }
  else if (sl < SL_ATT) { delay_start(L ? DL_R1 : 0); if (on(0)) role_p(p, L, j, sl - SL_P, dbg); }
  else if (sl < SL_WO) {
    if (p.role_mask & 2) hot_init(p, rw, L, 2 * j + sl - SL_ATT);
    if (on(2)) role_att(p, L, j, sl - SL_ATT, dbg);
  }
  else if (sl < SL_UP) { delay_start(L ? DL_W1 : DL_W0); if (on(3)) role_wo(p, L, j, sl - SL_WO, dbg); }
  else if (sl < SL_DOWN) { delay_start(L ? DL_U1 : DL_U0); if (on(5)) role_up(p, L, j, sl - SL_UP, dbg); }
  else if (sl < SL_REDA) { delay_start(L ? DL_D1 : DL_D0); if (on(6)) role_down(p, L, j, sl - SL_DOWN, dbg); }
  else if (sl == SL_REDF && L == 1) {
    if (p.role_mask & 1) role_ln0(p, j, dbg);
    if (on(7)) role_fin(p, j, dbg);
  } else {
    v16u sa, sb, va, vb;
    if (sl == SL_REDA) {
      if (p.role_mask & 2) hot_init(p, rw, L, 8 + j);
      red_args<1>(p, L, j, dbg, sa, sb, va, vb);
    } else {
      red_args<2>(p, 0, j, dbg, sa, sb, va, vb);
    }
    if (on(sl == SL_REDA ? 4 : 7)) role_mid(sa, sb, va, vb);
  }
}

static void fill_params(Params& p, void* const* d_in, void* d_out, void* d_ws) {
  p.x = (const u16*)d_in[0]; p.mem0 = (const u16*)d_in[1];
  p.Wq = (const u16*)d_in[2]; p.Wk = (const u16*)d_in[3]; p.Wv = (const u16*)d_in[4]; p.Wo = (const u16*)d_in[5];
  p.w1 = (const u16*)d_in[8]; p.w3 = (const u16*)d_in[9]; p.w2 = (const u16*)d_in[10];
  p.out = (u16*)d_out; p.ws = (unsigned char*)d_ws;
}

extern "C" void kernel_launch(void* const* d_in, const int* in_sizes, int n_in, void* d_out, int out_size, void* d_ws,
                              size_t ws_size, hipStream_t stream) {
  Params p;
  fill_params(p, d_in, d_out, d_ws);
  (void)hipFuncSetAttribute((const void*)mega, hipFuncAttributeMaxDynamicSharedMemorySize, LDS_BYTES);
  (void)hipMemsetAsync(d_ws, 0xFF, CTRL_BYTES, stream);
  p.role_mask = 0x3FFFF; p.unused = 0;
  mega<<<256, 256, LDS_BYTES, stream>>>(p);
}
```

```cpp
#include <hip/hip_runtime.h>
#include <stdint.h>
#include <type_traits>
#include <utility>

typedef unsigned short u16;
typedef unsigned int u32;
typedef short bf16x8 __attribute__((ext_vector_type(8)));
typedef short bf16x4 __attribute__((ext_vector_type(4)));
typedef float f32x4 __attribute__((ext_vector_type(4)));
typedef float f32x2 __attribute__((ext_vector_type(2)));
typedef __bf16 bf2_t __attribute__((ext_vector_type(2)));
typedef unsigned v16u __attribute__((ext_vector_type(16)));
typedef unsigned v8u __attribute__((ext_vector_type(8)));
typedef unsigned v4u __attribute__((ext_vector_type(4)));
typedef unsigned v2u __attribute__((ext_vector_type(2)));
typedef __amdgpu_buffer_rsrc_t rsrc_t;
#define DEVI __device__ __forceinline__

static constexpr int NSEG = 64;
static constexpr int PLACE = 0, RQ = 8, RO = 8, RP = 8, RH = 8, RX = 8, RX2 = 16, LN_B = 12, LN_C = 36, PS_ATT = 2, PS_RED = 2, DL_R1 = 6, DL_W0 = 4, DL_W1 = 12, DL_U0 = 6, DL_U1 = 14, DL_D0 = 9, DL_D1 = 16;

static constexpr u32 PARW = 0, TICK = 1024;
static constexpr u32 CHUNK = 16384;
static constexpr u32 MBOX = CHUNK;
static constexpr u32 INITF = 5 * CHUNK;
static constexpr u32 CTRL_BYTES = 6 * CHUNK + 8192;
static constexpr u32 HOT = 6 * CHUNK, HOT2 = 2u << 20;
static constexpr u32 SLOT = 4u << 20;
static constexpr u32 PGA = SLOT, PGB = SLOT + (2u << 20);
static constexpr int cmax(int a, int b) { return a > b ? a : b; }
static constexpr int NSLOT = cmax(cmax(cmax(RQ, RO), cmax(RP, RH)), cmax(RX, RX2));
DEVI u32 pga(int L, int blk) { return PGA + (u32)blk * CHUNK + (u32)L * 8192; }
DEVI u32 pgb(int L, int blk) { return PGB + (u32)blk * CHUNK + (u32)(1 - L) * 8192; }
static constexpr int B_QKV = 0, B_KVM = 96;
static constexpr int B_O = 0, B_H = 32, B_PA = 68, B_PF = 88, B_XN2 = 108, B_X1 = 113, B_XN = 118, B_X = 123;
static constexpr u32 OFF_XN0 = SLOT * (1 + NSLOT);
static constexpr u32 OFF_DBG = OFF_XN0 + 320 * CHUNK;
static constexpr u32 WS_END = OFF_DBG + 256u * 66 * 256;
DEVI u32 kb_off(int kb) { return (u32)((kb >> 3) * CHUNK + (kb & 7) * 1024); }
static constexpr u32 OOB = 0x80000000u;
static constexpr int LDS_BYTES = 163840;
static constexpr unsigned SPIN_LIMIT = 1u << 18;
static constexpr int SL_REC = 0, SL_P = 8, SL_ATT = 16, SL_WO = 18, SL_UP = 21, SL_DOWN = 27, SL_REDA = 30, SL_REDF = 31;
DEVI u32 mbox(int L, int j, int role, int slot) { return MBOX + (u32)(j * CHUNK + L * 8192 + role * 256 + 4 * slot); }
DEVI u32 mbox_att(int L, int j, int hl, int slot) { return mbox(L, j, SL_ATT + (hl >> 2), 16 * (hl & 3) + slot); }

struct Params {
  const u16* x; const u16* mem0; const u16* Wq; const u16* Wk; const u16* Wv; const u16* Wo;
  const u16* w1; const u16* w3; const u16* w2; u16* out; unsigned char* ws;
  int role_mask; int unused;
};

template <class F, int... I> DEVI void static_for_impl(F&& f, std::integer_sequence<int, I...>) { (f(std::integral_constant<int, I>{}), ...); }
template <int N, class F> DEVI void static_for(F&& f) { static_for_impl(f, std::make_integer_sequence<int, N>{}); }
DEVI float bflo(u32 d) { return __uint_as_float(d << 16); }
DEVI float bfhi(u32 d) { return __uint_as_float(d & 0xffff0000u); }
DEVI u32 pk2(float a, float b) {
  f32x2 f = {a, b};
  bf2_t r = __builtin_convertvector(f, bf2_t);
  return __builtin_bit_cast(u32, r);
}
DEVI bf16x8 as_frag(v4u v) { return __builtin_bit_cast(bf16x8, v); }
DEVI bf16x4 as_frag4(v2u v) { return __builtin_bit_cast(bf16x4, v); }
DEVI f32x4 mfma32(v4u a, v4u b, f32x4 c) { return __builtin_amdgcn_mfma_f32_16x16x32_bf16(as_frag(a), as_frag(b), c, 0, 0, 0); }
DEVI f32x4 mfma16(v2u a, v2u b, f32x4 c) { return __builtin_amdgcn_mfma_f32_16x16x16bf16_1k(as_frag4(a), as_frag4(b), c, 0, 0, 0); }
DEVI v4u ld16(rsrc_t r, u32 voff, u32 soff) { return __builtin_amdgcn_raw_buffer_load_b128(r, voff, soff, 16); }
DEVI v2u ld8(rsrc_t r, u32 voff, u32 soff) { return __builtin_amdgcn_raw_buffer_load_b64(r, voff, soff, 16); }
DEVI u32 ld4(rsrc_t r, u32 voff, u32 soff) { return __builtin_amdgcn_raw_buffer_load_b32(r, voff, soff, 16); }
DEVI void st16(rsrc_t r, v4u v, u32 voff, u32 soff) { __builtin_amdgcn_raw_buffer_store_b128(v, r, voff, soff, 16); }
DEVI void st8(rsrc_t r, v2u v, u32 voff, u32 soff) { __builtin_amdgcn_raw_buffer_store_b64(v, r, voff, soff, 16); }
DEVI void st4(rsrc_t r, u32 v, u32 voff, u32 soff) { __builtin_amdgcn_raw_buffer_store_b32(v, r, voff, soff, 16); }
DEVI void drain() { asm volatile("s_waitcnt vmcnt(0)" ::: "memory"); }
DEVI void cbar() { asm volatile("" ::: "memory"); }
DEVI void sbar() { __builtin_amdgcn_sched_barrier(0); }
DEVI u32 rfl(u32 v) { return (u32)__builtin_amdgcn_readfirstlane((int)v); }
DEVI float xmax16_32(float x) {
  u32 a = __float_as_uint(x);
  auto r = __builtin_amdgcn_permlane16_swap(a, a, false, false);
  x = fmaxf(__uint_as_float(r[0]), __uint_as_float(r[1]));
  a = __float_as_uint(x);
  auto q = __builtin_amdgcn_permlane32_swap(a, a, false, false);
  return fmaxf(__uint_as_float(q[0]), __uint_as_float(q[1]));
}
DEVI float xsum16_32(float x) {
  u32 a = __float_as_uint(x);
  auto r = __builtin_amdgcn_permlane16_swap(a, a, false, false);
  x = __uint_as_float(r[0]) + __uint_as_float(r[1]);
  a = __float_as_uint(x);
  auto q = __builtin_amdgcn_permlane32_swap(a, a, false, false);
  return __uint_as_float(q[0]) + __uint_as_float(q[1]);
}
DEVI float ror8(float x) { return __int_as_float(__builtin_amdgcn_update_dpp(0, __float_as_int(x), 0x128, 0xf, 0xf, false)); }
DEVI v4u swap16(v2u a, v2u b) {
  auto s0 = __builtin_amdgcn_permlane16_swap(a.x, b.x, false, false);
  auto s1 = __builtin_amdgcn_permlane16_swap(a.y, b.y, false, false);
  return (v4u){s0[0], s1[0], s0[1], s1[1]};
}

#define STAMP(row, slot) do { } while (0)

DEVI bool flags_ok(int v, int bias, int s) {
  asm volatile("" : "+v"(v));
  return __all(v + bias >= s);
}
template <int SLP> DEVI void wait_flags(rsrc_t r, u32 addr, int bias, int s, unsigned& budget) {
  for (;;) {
    cbar();
    const int v = (int)ld4(r, addr, 0);
    if (flags_ok(v, bias, s) || budget == 0) break;
    --budget;
    __builtin_amdgcn_s_sleep(SLP);
  }
  cbar();
}
DEVI void publish(rsrc_t r, u32 flag, int s, int lane) {
  drain();
  if (lane == 0) st4(r, (u32)s, flag, 0);
  cbar();
}
DEVI void delay_start(int us) {
  if (us == 0) return;
  const unsigned long long t0 = __builtin_amdgcn_s_memrealtime();
  while (__builtin_amdgcn_s_memrealtime() - t0 < (unsigned long long)us * 100u) __builtin_amdgcn_s_sleep(16);
}
DEVI v4u make_srd(const void* p, u32 bytes) {
  const unsigned long long a = (unsigned long long)p;
  return (v4u){rfl((u32)a), rfl((u32)(a >> 32) & 0xffffu), bytes, 0x00020000u};
}
DEVI v16u cat4(v4u a, v4u b, v4u c, v4u d) {
  return (v16u){a.x, a.y, a.z, a.w, b.x, b.y, b.z, b.w, c.x, c.y, c.z, c.w, d.x, d.y, d.z, d.w};
}
DEVI u32 wswz(int j, int g) { return (u32)(4 * ((j >> 1) & 1) ^ 2 * (g & 1)); }
DEVI u32 tr_base(int lane, int chunk) {
  const int g = lane >> 4, q = (lane >> 2) & 3;
  return (u32)(g * 1024 + q * 128) + (((u32)chunk ^ wswz(q, g)) << 4);
}

#define RUN_ASM(TEXT) do { for (int i_ = 0; i_ < 16; ++i_) { sa[i_] = rfl(sa[i_]); sb[i_] = rfl(sb[i_]); } \
    asm volatile(TEXT "s_endpgm\n" :: "{s[36:51]}"(sa), "{s[52:67]}"(sb), "{v[0:15]}"(va) : "v255", "a255", "s99", "vcc", "memory"); \
    __builtin_unreachable(); } while (0)

#define GEMM_DECL v4u g_w0, g_w1, g_w2; u32 g_ldw, g_in_base, g_in_stride, g_in_mask, g_out_base, g_out_kb, g_out_mask, g_flag, g_paddr; int g_pbias, g_pinit = -1
#define GEMM_ARGS g_w0, g_w1, g_w2, g_ldw, g_in_base, g_in_stride, g_in_mask, g_out_base, g_out_kb, g_out_mask, g_flag, g_paddr, g_pbias, g_pinit
DEVI void gemm_args(v4u g_w0, v4u g_w1, v4u g_w2, u32 g_ldw, u32 g_in_base, u32 g_in_stride, u32 g_in_mask, u32 g_out_base, u32 g_out_kb,
                    u32 g_out_mask, u32 g_flag, u32 g_paddr, int g_pbias, int g_pinit, const Params& p, u32 dbg, v16u& sa, v16u& sb, v16u& va) {
  const int tid = threadIdx.x, lane = tid & 63, w = (int)rfl(tid >> 6);
  const int c = lane & 15, gg = lane >> 4, pp = lane & 3;
  const unsigned long long fm = __ballot(g_flag != OOB);
  sa = cat4(make_srd(p.ws, WS_END), g_w0, g_w1, g_w2);
  sb = (v16u){g_in_base, g_in_stride, g_in_mask, g_out_base, SLOT, g_out_mask, 0, (u32)w * 1024, dbg + w * 64, 0, (u32)fm, (u32)(fm >> 32), 0, 0, 0, 0};
  va[0] = lane * 16;
  va[1] = lane * 16 + w * 2048;
  va[2] = g_paddr;
  va[3] = (u32)g_pbias;
  va[4] = kb_off((int)g_out_kb + 8 + 2 * (w >> 1)) + (u32)((gg & 1) * 1024 + ((2 * (w & 1) + (gg >> 1)) * 16 + c) * 16);
  va[5] = (u32)(8 * w + (lane >> 3)) * g_ldw + ((((u32)lane & 7) ^ wswz(lane >> 3, w)) << 4);
  va[6] = (u32)(w >> 1) * 4096 + tr_base(lane, 4 * (w & 1) + pp);
  va[7] = 2 * 4096 + tr_base(lane, 2 * w + (pp >> 1)) + 8 * (pp & 1);
  for (int i = 8; i < 16; ++i) va[i] = 0;
  va[11] = (u32)g_pinit;
  va[14] = g_flag;
  va[15] = kb_off((int)g_out_kb + 2 * w) + lane * 16;
}

DEVI void role_p(const Params& p, int L, int j, int i, u32 dbg) {
  const int lane = threadIdx.x & 63, w = threadIdx.x >> 6, h = 8 * j + i;
  const u32 wb = 576u * 4096 * 2 - h * 256;
  GEMM_DECL;
  g_w0 = make_srd(p.Wq + h * 128, wb); g_w1 = make_srd(p.Wk + h * 128, wb); g_w2 = make_srd(p.Wv + h * 128, wb);
  g_ldw = 8192;
  g_in_base = L ? pgb(1, B_XN) : OFF_XN0; g_in_stride = L ? SLOT : 5 * CHUNK; g_in_mask = L ? RX2 - 1 : 63;
  g_out_base = pga(L, B_QKV + 3 * h); g_out_kb = 0; g_out_mask = RQ - 1;
  g_flag = lane == 0 ? mbox(L, j, SL_REC + i, w) : (lane == 1 ? mbox_att(L, j, i, 8 + w) : OOB);
  g_paddr = mbox(L, j, SL_P + i, lane);
  g_pbias = lane < (L ? 16 : 48) ? 0 : (lane == 48 ? RQ : 1000);
  g_pinit = lane < 16 || lane >= 48 ? -1 : (lane < 32 ? LN_B - 1 : LN_C - 1);
  v16u sa, sb, va;
  gemm_args(GEMM_ARGS, p, dbg, sa, sb, va);
  RUN_ASM(
    "s_nop 4\n"
    "v_add_u32 v8, s59, v0\n"
    "s_add_u32 m0, s59, 0\n"
    "s_mov_b32 s74, 0\n"
    "buffer_load_dwordx4 v5, s[40:43], s74 offen lds\n"
    "s_add_u32 m0, s59, 4096\n"
    "s_mov_b32 s74, 128\n"
    "buffer_load_dwordx4 v5, s[40:43], s74 offen lds\n"
    "s_add_u32 m0, s59, 8192\n"
    "s_mov_b32 s74, 0\n"
    "buffer_load_dwordx4 v5, s[44:47], s74 offen lds\n"
    "s_add_u32 m0, s59, 12288\n"
    "s_mov_b32 s74, 128\n"
    "buffer_load_dwordx4 v5, s[44:47], s74 offen lds\n"
    "s_add_u32 m0, s59, 16384\n"
    "s_mov_b32 s74, 0\n"
    "buffer_load_dwordx4 v5, s[48:51], s74 offen lds\n"
    "s_add_u32 m0, s59, 20480\n"
    "s_mov_b32 s74, 128\n"
    "buffer_load_dwordx4 v5, s[48:51], s74 offen lds\n"
    "s_add_u32 m0, s59, 24576\n"
    "s_mov_b32 s74, 262144\n"
    "buffer_load_dwordx4 v5, s[40:43], s74 offen lds\n"
    "s_add_u32 m0, s59, 28672\n"
    "s_mov_b32 s74, 262272\n"
    "buffer_load_dwordx4 v5, s[40:43], s74 offen lds\n"
    "s_add_u32 m0, s59, 32768\n"
    "s_mov_b32 s74, 262144\n"
    "buffer_load_dwordx4 v5, s[44:47], s74 offen lds\n"
    "s_add_u32 m0, s59, 36864\n"
    "s_mov_b32 s74, 262272\n"
    "buffer_load_dwordx4 v5, s[44:47], s74 offen lds\n"
    "s_add_u32 m0, s59, 40960\n"
    "s_mov_b32 s74, 262144\n"
    "buffer_load_dwordx4 v5, s[48:51], s74 offen lds\n"
    "s_add_u32 m0, s59, 45056\n"
    "s_mov_b32 s74, 262272\n"
    "buffer_load_dwordx4 v5, s[48:51], s74 offen lds\n"
    "s_add_u32 m0, s59, 49152\n"
    "s_mov_b32 s74, 524288\n"
    "buffer_load_dwordx4 v5, s[40:43], s74 offen lds\n"
    "s_add_u32 m0, s59, 53248\n"
    "s_mov_b32 s74, 524416\n"
    "buffer_load_dwordx4 v5, s[40:43], s74 offen lds\n"
    "s_add_u32 m0, s59, 57344\n"
    "s_mov_b32 s74, 524288\n"
    "buffer_load_dwordx4 v5, s[44:47], s74 offen lds\n"
    "s_add_u32 m0, s59, 61440\n"
    "s_mov_b32 s74, 524416\n"
    "buffer_load_dwordx4 v5, s[44:47], s74 offen lds\n"
    "s_add_u32 m0, s59, 65536\n"
    "s_mov_b32 s74, 524288\n"
    "buffer_load_dwordx4 v5, s[48:51], s74 offen lds\n"
    "s_add_u32 m0, s59, 69632\n"
    "s_mov_b32 s74, 524416\n"
    "buffer_load_dwordx4 v5, s[48:51], s74 offen lds\n"
    "s_add_u32 m0, s59, 73728\n"
    "s_mov_b32 s74, 786432\n"
    "buffer_load_dwordx4 v5, s[40:43], s74 offen lds\n"
    "s_add_u32 m0, s59, 77824\n"
    "s_mov_b32 s74, 786560\n"
    "buffer_load_dwordx4 v5, s[40:43], s74 offen lds\n"
    "s_add_u32 m0, s59, 81920\n"
    "s_mov_b32 s74, 786432\n"
    "buffer_load_dwordx4 v5, s[44:47], s74 offen lds\n"
    "s_add_u32 m0, s59, 86016\n"
    "s_mov_b32 s74, 786560\n"
    "buffer_load_dwordx4 v5, s[44:47], s74 offen lds\n"
    "s_add_u32 m0, s59, 90112\n"
    "s_mov_b32 s74, 786432\n"
    "buffer_load_dwordx4 v5, s[48:51], s74 offen lds\n"
    "s_add_u32 m0, s59, 94208\n"
    "s_mov_b32 s74, 786560\n"
    "buffer_load_dwordx4 v5, s[48:51], s74 offen lds\n"
    "s_add_u32 m0, s59, 98304\n"
    "s_mov_b32 s74, 1048576\n"
    "buffer_load_dwordx4 v5, s[40:43], s74 offen lds\n"
    "s_add_u32 m0, s59, 102400\n"
    "s_mov_b32 s74, 1048704\n"
    "buffer_load_dwordx4 v5, s[40:43], s74 offen lds\n"
    "s_add_u32 m0, s59, 106496\n"
    "s_mov_b32 s74, 1048576\n"
    "buffer_load_dwordx4 v5, s[44:47], s74 offen lds\n"
    "s_add_u32 m0, s59, 110592\n"
    "s_mov_b32 s74, 1048704\n"
    "buffer_load_dwordx4 v5, s[44:47], s74 offen lds\n"
    "s_add_u32 m0, s59, 114688\n"
    "s_mov_b32 s74, 1048576\n"
    "buffer_load_dwordx4 v5, s[48:51], s74 offen lds\n"
    "s_add_u32 m0, s59, 118784\n"
    "s_mov_b32 s74, 1048704\n"
    "buffer_load_dwordx4 v5, s[48:51], s74 offen lds\n"
    "v_add_u32 v16, 49152, v6\n"
    "v_add_u32 v17, 98304, v6\n"
    "v_add_u32 v18, 49152, v7\n"
    "v_add_u32 v19, 98304, v7\n"
    "s_waitcnt vmcnt(24) lgkmcnt(0)\n"
    "s_barrier\n"
    "s_add_u32 m0, s59, 122880\n"
    "s_mov_b32 s74, 1310720\n"
    "buffer_load_dwordx4 v5, s[40:43], s74 offen lds\n"
    "s_add_u32 m0, s59, 126976\n"
    "s_mov_b32 s74, 1310848\n"
    "buffer_load_dwordx4 v5, s[40:43], s74 offen lds\n"
    "s_add_u32 m0, s59, 131072\n"
    "s_mov_b32 s74, 1310720\n"
    "buffer_load_dwordx4 v5, s[44:47], s74 offen lds\n"
    "s_add_u32 m0, s59, 135168\n"
    "s_mov_b32 s74, 1310848\n"
    "buffer_load_dwordx4 v5, s[44:47], s74 offen lds\n"
    "s_add_u32 m0, s59, 139264\n"
    "s_mov_b32 s74, 1310720\n"
    "buffer_load_dwordx4 v5, s[48:51], s74 offen lds\n"
    "s_add_u32 m0, s59, 143360\n"
    "s_mov_b32 s74, 1310848\n"
    "buffer_load_dwordx4 v5, s[48:51], s74 offen lds\n"
    "ds_read_b64_tr_b16 a[0:1], v6 offset:8192\n"
    "ds_read_b64_tr_b16 a[2:3], v6 offset:8704\n"
    "ds_read_b64_tr_b16 a[4:5], v6 offset:8200\n"
    "ds_read_b64_tr_b16 a[6:7], v6 offset:8712\n"
    "ds_read_b64_tr_b16 a[8:9], v6 offset:16384\n"
    "ds_read_b64_tr_b16 a[10:11], v6 offset:16896\n"
    "ds_read_b64_tr_b16 a[12:13], v6 offset:16392\n"
    "ds_read_b64_tr_b16 a[14:15], v6 offset:16904\n"
    "ds_read_b64_tr_b16 a[16:17], v6 offset:0\n"
    "ds_read_b64_tr_b16 a[18:19], v6 offset:512\n"
    "ds_read_b64_tr_b16 a[20:21], v6 offset:8\n"
    "ds_read_b64_tr_b16 a[22:23], v6 offset:520\n"
    "s_waitcnt vmcnt(24) lgkmcnt(0)\n"
    "s_barrier\n"
    "s_add_u32 m0, s59, 0\n"
    "s_mov_b32 s74, 1572864\n"
    "buffer_load_dwordx4 v5, s[40:43], s74 offen lds\n"
    "s_add_u32 m0, s59, 4096\n"
    "s_mov_b32 s74, 1572992\n"
    "buffer_load_dwordx4 v5, s[40:43], s74 offen lds\n"
    "s_add_u32 m0, s59, 8192\n"
    "s_mov_b32 s74, 1572864\n"
    "buffer_load_dwordx4 v5, s[44:47], s74 offen lds\n"
    "s_add_u32 m0, s59, 12288\n"
    "s_mov_b32 s74, 1572992\n"
    "buffer_load_dwordx4 v5, s[44:47], s74 offen lds\n"
    "s_add_u32 m0, s59, 16384\n"
    "s_mov_b32 s74, 1572864\n"
    "buffer_load_dwordx4 v5, s[48:51], s74 offen lds\n"
    "s_add_u32 m0, s59, 20480\n"
    "s_mov_b32 s74, 1572992\n"
    "buffer_load_dwordx4 v5, s[48:51], s74 offen lds\n"
    "ds_read_b64_tr_b16 a[24:25], v6 offset:32768\n"
    "ds_read_b64_tr_b16 a[26:27], v6 offset:33280\n"
    "ds_read_b64_tr_b16 a[28:29], v6 offset:32776\n"
    "ds_read_b64_tr_b16 a[30:31], v6 offset:33288\n"
    "ds_read_b64_tr_b16 a[32:33], v6 offset:40960\n"
    "ds_read_b64_tr_b16 a[34:35], v6 offset:41472\n"
    "ds_read_b64_tr_b16 a[36:37], v6 offset:40968\n"
    "ds_read_b64_tr_b16 a[38:39], v6 offset:41480\n"
    "ds_read_b64_tr_b16 a[40:41], v6 offset:24576\n"
    "ds_read_b64_tr_b16 a[42:43], v6 offset:25088\n"
    "ds_read_b64_tr_b16 a[44:45], v6 offset:24584\n"
    "ds_read_b64_tr_b16 a[46:47], v6 offset:25096\n"
    "s_waitcnt vmcnt(24) lgkmcnt(0)\n"
    "s_barrier\n"
    "s_add_u32 m0, s59, 24576\n"
    "s_mov_b32 s74, 1835008\n"
    "buffer_load_dwordx4 v5, s[40:43], s74 offen lds\n"
    "s_add_u32 m0, s59, 28672\n"
    "s_mov_b32 s74, 1835136\n"
    "buffer_load_dwordx4 v5, s[40:43], s74 offen lds\n"
    "s_add_u32 m0, s59, 32768\n"
    "s_mov_b32 s74, 1835008\n"
    "buffer_load_dwordx4 v5, s[44:47], s74 offen lds\n"
    "s_add_u32 m0, s59, 36864\n"
    "s_mov_b32 s74, 1835136\n"
    "buffer_load_dwordx4 v5, s[44:47], s74 offen lds\n"
    "s_add_u32 m0, s59, 40960\n"
    "s_mov_b32 s74, 1835008\n"
    "buffer_load_dwordx4 v5, s[48:51], s74 offen lds\n"
    "s_add_u32 m0, s59, 45056\n"
    "s_mov_b32 s74, 1835136\n"
    "buffer_load_dwordx4 v5, s[48:51], s74 offen lds\n"
    "ds_read_b64_tr_b16 a[48:49], v16 offset:8192\n"
    "ds_read_b64_tr_b16 a[50:51], v16 offset:8704\n"
    "ds_read_b64_tr_b16 a[52:53], v16 offset:8200\n"
    "ds_read_b64_tr_b16 a[54:55], v16 offset:8712\n"
    "ds_read_b64_tr_b16 a[56:57], v16 offset:16384\n"
    "ds_read_b64_tr_b16 a[58:59], v16 offset:16896\n"
    "ds_read_b64_tr_b16 a[60:61], v16 offset:16392\n"
    "ds_read_b64_tr_b16 a[62:63], v16 offset:16904\n"
    "ds_read_b64_tr_b16 a[64:65], v16 offset:0\n"
    "ds_read_b64_tr_b16 a[66:67], v16 offset:512\n"
    "ds_read_b64_tr_b16 a[68:69], v16 offset:8\n"
    "ds_read_b64_tr_b16 a[70:71], v16 offset:520\n"
    "s_waitcnt vmcnt(24) lgkmcnt(0)\n"
    "s_barrier\n"
    "s_add_u32 m0, s59, 49152\n"
    "s_mov_b32 s74, 2097152\n"
    "buffer_load_dwordx4 v5, s[40:43], s74 offen lds\n"
    "s_add_u32 m0, s59, 53248\n"
    "s_mov_b32 s74, 2097280\n"
    "buffer_load_dwordx4 v5, s[40:43], s74 offen lds\n"
    "s_add_u32 m0, s59, 57344\n"
    "s_mov_b32 s74, 2097152\n"
    "buffer_load_dwordx4 v5, s[44:47], s74 offen lds\n"
    "s_add_u32 m0, s59, 61440\n"
    "s_mov_b32 s74, 2097280\n"
    "buffer_load_dwordx4 v5, s[44:47], s74 offen lds\n"
    "s_add_u32 m0, s59, 65536\n"
    "s_mov_b32 s74, 2097152\n"
    "buffer_load_dwordx4 v5, s[48:51], s74 offen lds\n"
    "s_add_u32 m0, s59, 69632\n"
    "s_mov_b32 s74, 2097280\n"
    "buffer_load_dwordx4 v5, s[48:51], s74 offen lds\n"
    "ds_read_b64_tr_b16 a[72:73], v16 offset:32768\n"
    "ds_read_b64_tr_b16 a[74:75], v16 offset:33280\n"
    "ds_read_b64_tr_b16 a[76:77], v16 offset:32776\n"
    "ds_read_b64_tr_b16 a[78:79], v16 offset:33288\n"
    "ds_read_b64_tr_b16 a[80:81], v16 offset:40960\n"
    "ds_read_b64_tr_b16 a[82:83], v16 offset:41472\n"
    "ds_read_b64_tr_b16 a[84:85], v16 offset:40968\n"
    "ds_read_b64_tr_b16 a[86:87], v16 offset:41480\n"
    "ds_read_b64_tr_b16 a[88:89], v16 offset:24576\n"
    "ds_read_b64_tr_b16 a[90:91], v16 offset:25088\n"
    "ds_read_b64_tr_b16 a[92:93], v16 offset:24584\n"
    "ds_read_b64_tr_b16 a[94:95], v16 offset:25096\n"
    "s_waitcnt vmcnt(24) lgkmcnt(0)\n"
    "s_barrier\n"
    "s_add_u32 m0, s59, 73728\n"
    "s_mov_b32 s74, 2359296\n"
    "buffer_load_dwordx4 v5, s[40:43], s74 offen lds\n"
    "s_add_u32 m0, s59, 77824\n"
    "s_mov_b32 s74, 2359424\n"
    "buffer_load_dwordx4 v5, s[40:43], s74 offen lds\n"
    "s_add_u32 m0, s59, 81920\n"
    "s_mov_b32 s74, 2359296\n"
    "buffer_load_dwordx4 v5, s[44:47], s74 offen lds\n"
    "s_add_u32 m0, s59, 86016\n"
    "s_mov_b32 s74, 2359424\n"
    "buffer_load_dwordx4 v5, s[44:47], s74 offen lds\n"
    "s_add_u32 m0, s59, 90112\n"
    "s_mov_b32 s74, 2359296\n"
    "buffer_load_dwordx4 v5, s[48:51], s74 offen lds\n"
    "s_add_u32 m0, s59, 94208\n"
    "s_mov_b32 s74, 2359424\n"
    "buffer_load_dwordx4 v5, s[48:51], s74 offen lds\n"
    "ds_read_b64_tr_b16 a[96:97], v17 offset:8192\n"
    "ds_read_b64_tr_b16 a[98:99], v17 offset:8704\n"
    "ds_read_b64_tr_b16 a[100:101], v17 offset:8200\n"
    "ds_read_b64_tr_b16 a[102:103], v17 offset:8712\n"
    "ds_read_b64_tr_b16 a[104:105], v17 offset:16384\n"
    "ds_read_b64_tr_b16 a[106:107], v17 offset:16896\n"
    "ds_read_b64_tr_b16 a[108:109], v17 offset:16392\n"
    "ds_read_b64_tr_b16 a[110:111], v17 offset:16904\n"
    "ds_read_b64_tr_b16 a[112:113], v17 offset:0\n"
    "ds_read_b64_tr_b16 a[114:115], v17 offset:512\n"
    "ds_read_b64_tr_b16 a[116:117], v17 offset:8\n"
    "ds_read_b64_tr_b16 a[118:119], v17 offset:520\n"
    "s_waitcnt vmcnt(24) lgkmcnt(0)\n"
    "s_barrier\n"
    "s_add_u32 m0, s59, 98304\n"
    "s_mov_b32 s74, 2621440\n"
    "buffer_load_dwordx4 v5, s[40:43], s74 offen lds\n"
    "s_add_u32 m0, s59, 102400\n"
    "s_mov_b32 s74, 2621568\n"
    "buffer_load_dwordx4 v5, s[40:43], s74 offen lds\n"
    "s_add_u32 m0, s59, 106496\n"
    "s_mov_b32 s74, 2621440\n"
    "buffer_load_dwordx4 v5, s[44:47], s74 offen lds\n"
    "s_add_u32 m0, s59, 110592\n"
    "s_mov_b32 s74, 2621568\n"
    "buffer_load_dwordx4 v5, s[44:47], s74 offen lds\n"
    "s_add_u32 m0, s59, 114688\n"
    "s_mov_b32 s74, 2621440\n"
    "buffer_load_dwordx4 v5, s[48:51], s74 offen lds\n"
    "s_add_u32 m0, s59, 118784\n"
    "s_mov_b32 s74, 2621568\n"
    "buffer_load_dwordx4 v5, s[48:51], s74 offen lds\n"
    "ds_read_b64_tr_b16 a[120:121], v17 offset:32768\n"
    "ds_read_b64_tr_b16 a[122:123], v17 offset:33280\n"
    "ds_read_b64_tr_b16 a[124:125], v17 offset:32776\n"
    "ds_read_b64_tr_b16 a[126:127], v17 offset:33288\n"
    "ds_read_b64_tr_b16 a[128:129], v17 offset:40960\n"
    "ds_read_b64_tr_b16 a[130:131], v17 offset:41472\n"
    "ds_read_b64_tr_b16 a[132:133], v17 offset:40968\n"
    "ds_read_b64_tr_b16 a[134:135], v17 offset:41480\n"
    "ds_read_b64_tr_b16 a[136:137], v17 offset:24576\n"
    "ds_read_b64_tr_b16 a[138:139], v17 offset:25088\n"
    "ds_read_b64_tr_b16 a[140:141], v17 offset:24584\n"
    "ds_read_b64_tr_b16 a[142:143], v17 offset:25096\n"
    "s_waitcnt vmcnt(24) lgkmcnt(0)\n"
    "s_barrier\n"
    "s_add_u32 m0, s59, 122880\n"
    "s_mov_b32 s74, 2883584\n"
    "buffer_load_dwordx4 v5, s[40:43], s74 offen lds\n"
    "s_add_u32 m0, s59, 126976\n"
    "s_mov_b32 s74, 2883712\n"
    "buffer_load_dwordx4 v5, s[40:43], s74 offen lds\n"
    "s_add_u32 m0, s59, 131072\n"
    "s_mov_b32 s74, 2883584\n"
    "buffer_load_dwordx4 v5, s[44:47], s74 offen lds\n"
    "s_add_u32 m0, s59, 135168\n"
    "s_mov_b32 s74, 2883712\n"
    "buffer_load_dwordx4 v5, s[44:47], s74 offen lds\n"
    "s_add_u32 m0, s59, 139264\n"
    "s_mov_b32 s74, 2883584\n"
    "buffer_load_dwordx4 v5, s[48:51], s74 offen lds\n"
    "s_add_u32 m0, s59, 143360\n"
    "s_mov_b32 s74, 2883712\n"
    "buffer_load_dwordx4 v5, s[48:51], s74 offen lds\n"
    "ds_read_b64_tr_b16 a[144:145], v6 offset:8192\n"
    "ds_read_b64_tr_b16 a[146:147], v6 offset:8704\n"
    "ds_read_b64_tr_b16 a[148:149], v6 offset:8200\n"
    "ds_read_b64_tr_b16 a[150:151], v6 offset:8712\n"
    "ds_read_b64_tr_b16 a[152:153], v6 offset:16384\n"
    "ds_read_b64_tr_b16 a[154:155], v6 offset:16896\n"
    "ds_read_b64_tr_b16 a[156:157], v6 offset:16392\n"
    "ds_read_b64_tr_b16 a[158:159], v6 offset:16904\n"
    "ds_read_b64_tr_b16 a[160:161], v6 offset:0\n"
    "ds_read_b64_tr_b16 a[162:163], v6 offset:512\n"
    "ds_read_b64_tr_b16 a[164:165], v6 offset:8\n"
    "ds_read_b64_tr_b16 a[166:167], v6 offset:520\n"
    "s_waitcnt vmcnt(24) lgkmcnt(0)\n"
    "s_barrier\n"
    "s_add_u32 m0, s59, 0\n"
    "s_mov_b32 s74, 3145728\n"
    "buffer_load_dwordx4 v5, s[40:43], s74 offen lds\n"
    "s_add_u32 m0, s59, 4096\n"
    "s_mov_b32 s74, 3145856\n"
    "buffer_load_dwordx4 v5, s[40:43], s74 offen lds\n"
    "s_add_u32 m0, s59, 8192\n"
    "s_mov_b32 s74, 3145728\n"
    "buffer_load_dwordx4 v5, s[44:47], s74 offen lds\n"
    "s_add_u32 m0, s59, 12288\n"
    "s_mov_b32 s74, 3145856\n"
    "buffer_load_dwordx4 v5, s[44:47], s74 offen lds\n"
    "s_add_u32 m0, s59, 16384\n"
    "s_mov_b32 s74, 3145728\n"
    "buffer_load_dwordx4 v5, s[48:51], s74 offen lds\n"
    "s_add_u32 m0, s59, 20480\n"
    "s_mov_b32 s74, 3145856\n"
    "buffer_load_dwordx4 v5, s[48:51], s74 offen lds\n"
    "ds_read_b64_tr_b16 a[168:169], v6 offset:32768\n"
    "ds_read_b64_tr_b16 a[170:171], v6 offset:33280\n"
    "ds_read_b64_tr_b16 a[172:173], v6 offset:32776\n"
    "ds_read_b64_tr_b16 a[174:175], v6 offset:33288\n"
    "ds_read_b64_tr_b16 a[176:177], v6 offset:40960\n"
    "ds_read_b64_tr_b16 a[178:179], v6 offset:41472\n"
    "ds_read_b64_tr_b16 a[180:181], v6 offset:40968\n"
    "ds_read_b64_tr_b16 a[182:183], v6 offset:41480\n"
    "ds_read_b64_tr_b16 a[184:185], v6 offset:24576\n"
    "ds_read_b64_tr_b16 a[186:187], v6 offset:25088\n"
    "ds_read_b64_tr_b16 a[188:189], v6 offset:24584\n"
    "ds_read_b64_tr_b16 a[190:191], v6 offset:25096\n"
    "s_waitcnt vmcnt(24) lgkmcnt(0)\n"
    "s_barrier\n"
    "s_add_u32 m0, s59, 24576\n"
    "s_mov_b32 s74, 3407872\n"
    "buffer_load_dwordx4 v5, s[40:43], s74 offen lds\n"
    "s_add_u32 m0, s59, 28672\n"
    "s_mov_b32 s74, 3408000\n"
    "buffer_load_dwordx4 v5, s[40:43], s74 offen lds\n"
    "s_add_u32 m0, s59, 32768\n"
    "s_mov_b32 s74, 3407872\n"
    "buffer_load_dwordx4 v5, s[44:47], s74 offen lds\n"
    "s_add_u32 m0, s59, 36864\n"
    "s_mov_b32 s74, 3408000\n"
    "buffer_load_dwordx4 v5, s[44:47], s74 offen lds\n"
    "s_add_u32 m0, s59, 40960\n"
    "s_mov_b32 s74, 3407872\n"
    "buffer_load_dwordx4 v5, s[48:51], s74 offen lds\n"
    "s_add_u32 m0, s59, 45056\n"
    "s_mov_b32 s74, 3408000\n"
    "buffer_load_dwordx4 v5, s[48:51], s74 offen lds\n"
    "ds_read_b64_tr_b16 a[192:193], v16 offset:8192\n"
    "ds_read_b64_tr_b16 a[194:195], v16 offset:8704\n"
    "ds_read_b64_tr_b16 a[196:197], v16 offset:8200\n"
    "ds_read_b64_tr_b16 a[198:199], v16 offset:8712\n"
    "ds_read_b64_tr_b16 a[200:201], v16 offset:16384\n"
    "ds_read_b64_tr_b16 a[202:203], v16 offset:16896\n"
    "ds_read_b64_tr_b16 a[204:205], v16 offset:16392\n"
    "ds_read_b64_tr_b16 a[206:207], v16 offset:16904\n"
    "ds_read_b64_tr_b16 a[208:209], v16 offset:0\n"
    "ds_read_b64_tr_b16 a[210:211], v16 offset:512\n"
    "ds_read_b64_tr_b16 a[212:213], v16 offset:8\n"
    "ds_read_b64_tr_b16 a[214:215], v16 offset:520\n"
    "s_waitcnt vmcnt(24) lgkmcnt(0)\n"
    "s_barrier\n"
    "s_add_u32 m0, s59, 49152\n"
    "s_mov_b32 s74, 3670016\n"
    "buffer_load_dwordx4 v5, s[40:43], s74 offen lds\n"
    "s_add_u32 m0, s59, 53248\n"
    "s_mov_b32 s74, 3670144\n"
    "buffer_load_dwordx4 v5, s[40:43], s74 offen lds\n"
    "s_add_u32 m0, s59, 57344\n"
    "s_mov_b32 s74, 3670016\n"
    "buffer_load_dwordx4 v5, s[44:47], s74 offen lds\n"
    "s_add_u32 m0, s59, 61440\n"
    "s_mov_b32 s74, 3670144\n"
    "buffer_load_dwordx4 v5, s[44:47], s74 offen lds\n"
    "s_add_u32 m0, s59, 65536\n"
    "s_mov_b32 s74, 3670016\n"
    "buffer_load_dwordx4 v5, s[48:51], s74 offen lds\n"
    "s_add_u32 m0, s59, 69632\n"
    "s_mov_b32 s74, 3670144\n"
    "buffer_load_dwordx4 v5, s[48:51], s74 offen lds\n"
    "ds_read_b64_tr_b16 a[216:217], v16 offset:32768\n"
    "ds_read_b64_tr_b16 a[218:219], v16 offset:33280\n"
    "ds_read_b64_tr_b16 a[220:221], v16 offset:32776\n"
    "ds_read_b64_tr_b16 a[222:223], v16 offset:33288\n"
    "ds_read_b64_tr_b16 a[224:225], v16 offset:40960\n"
    "ds_read_b64_tr_b16 a[226:227], v16 offset:41472\n"
    "ds_read_b64_tr_b16 a[228:229], v16 offset:40968\n"
    "ds_read_b64_tr_b16 a[230:231], v16 offset:41480\n"
    "ds_read_b64_tr_b16 a[232:233], v16 offset:24576\n"
    "ds_read_b64_tr_b16 a[234:235], v16 offset:25088\n"
    "ds_read_b64_tr_b16 a[236:237], v16 offset:24584\n"
    "ds_read_b64_tr_b16 a[238:239], v16 offset:25096\n"
    "s_waitcnt vmcnt(24) lgkmcnt(0)\n"
    "s_barrier\n"
    "s_add_u32 m0, s59, 73728\n"
    "s_mov_b32 s74, 3932160\n"
    "buffer_load_dwordx4 v5, s[40:43], s74 offen lds\n"
    "s_add_u32 m0, s59, 77824\n"
    "s_mov_b32 s74, 3932288\n"
    "buffer_load_dwordx4 v5, s[40:43], s74 offen lds\n"
    "s_add_u32 m0, s59, 81920\n"
    "s_mov_b32 s74, 3932160\n"
    "buffer_load_dwordx4 v5, s[44:47], s74 offen lds\n"
    "s_add_u32 m0, s59, 86016\n"
    "s_mov_b32 s74, 3932288\n"
    "buffer_load_dwordx4 v5, s[44:47], s74 offen lds\n"
    "s_add_u32 m0, s59, 90112\n"
    "s_mov_b32 s74, 3932160\n"
    "buffer_load_dwordx4 v5, s[48:51], s74 offen lds\n"
    "s_add_u32 m0, s59, 94208\n"
    "s_mov_b32 s74, 3932288\n"
    "buffer_load_dwordx4 v5, s[48:51], s74 offen lds\n"
    "ds_read_b64_tr_b16 a[240:241], v17 offset:8192\n"
    "ds_read_b64_tr_b16 a[242:243], v17 offset:8704\n"
    "ds_read_b64_tr_b16 a[244:245], v17 offset:8200\n"
    "ds_read_b64_tr_b16 a[246:247], v17 offset:8712\n"
    "ds_read_b64_tr_b16 a[248:249], v17 offset:16384\n"
    "ds_read_b64_tr_b16 a[250:251], v17 offset:16896\n"
    "ds_read_b64_tr_b16 a[252:253], v17 offset:16392\n"
    "ds_read_b64_tr_b16 a[254:255], v17 offset:16904\n"
    "ds_read_b64_tr_b16 v[80:81], v17 offset:0\n"
    "ds_read_b64_tr_b16 v[82:83], v17 offset:512\n"
    "ds_read_b64_tr_b16 v[84:85], v17 offset:8\n"
    "ds_read_b64_tr_b16 v[86:87], v17 offset:520\n"
    "s_waitcnt vmcnt(24) lgkmcnt(0)\n"
    "s_barrier\n"
    "s_add_u32 m0, s59, 98304\n"
    "s_mov_b32 s74, 4194304\n"
    "buffer_load_dwordx4 v5, s[40:43], s74 offen lds\n"
    "s_add_u32 m0, s59, 102400\n"
    "s_mov_b32 s74, 4194432\n"
    "buffer_load_dwordx4 v5, s[40:43], s74 offen lds\n"
    "s_add_u32 m0, s59, 106496\n"
    "s_mov_b32 s74, 4194304\n"
    "buffer_load_dwordx4 v5, s[44:47], s74 offen lds\n"
    "s_add_u32 m0, s59, 110592\n"
    "s_mov_b32 s74, 4194432\n"
    "buffer_load_dwordx4 v5, s[44:47], s74 offen lds\n"
    "s_add_u32 m0, s59, 114688\n"
    "s_mov_b32 s74, 4194304\n"
    "buffer_load_dwordx4 v5, s[48:51], s74 offen lds\n"
    "s_add_u32 m0, s59, 118784\n"
    "s_mov_b32 s74, 4194432\n"
    "buffer_load_dwordx4 v5, s[48:51], s74 offen lds\n"
    "ds_read_b64_tr_b16 v[88:89], v17 offset:32768\n"
    "ds_read_b64_tr_b16 v[90:91], v17 offset:33280\n"
    "ds_read_b64_tr_b16 v[92:93], v17 offset:32776\n"
    "ds_read_b64_tr_b16 v[94:95], v17 offset:33288\n"
    "ds_read_b64_tr_b16 v[96:97], v17 offset:40960\n"
    "ds_read_b64_tr_b16 v[98:99], v17 offset:41472\n"
    "ds_read_b64_tr_b16 v[100:101], v17 offset:40968\n"
    "ds_read_b64_tr_b16 v[102:103], v17 offset:41480\n"
    "ds_read_b64_tr_b16 v[104:105], v17 offset:24576\n"
    "ds_read_b64_tr_b16 v[106:107], v17 offset:25088\n"
    "ds_read_b64_tr_b16 v[108:109], v17 offset:24584\n"
    "ds_read_b64_tr_b16 v[110:111], v17 offset:25096\n"
    "s_waitcnt vmcnt(24) lgkmcnt(0)\n"
    "s_barrier\n"
    "s_add_u32 m0, s59, 122880\n"
    "s_mov_b32 s74, 4456448\n"
    "buffer_load_dwordx4 v5, s[40:43], s74 offen lds\n"
    "s_add_u32 m0, s59, 126976\n"
    "s_mov_b32 s74, 4456576\n"
    "buffer_load_dwordx4 v5, s[40:43], s74 offen lds\n"
    "s_add_u32 m0, s59, 131072\n"
    "s_mov_b32 s74, 4456448\n"
    "buffer_load_dwordx4 v5, s[44:47], s74 offen lds\n"
    "s_add_u32 m0, s59, 135168\n"
    "s_mov_b32 s74, 4456576\n"
    "buffer_load_dwordx4 v5, s[44:47], s74 offen lds\n"
    "s_add_u32 m0, s59, 139264\n"
    "s_mov_b32 s74, 4456448\n"
    "buffer_load_dwordx4 v5, s[48:51], s74 offen lds\n"
    "s_add_u32 m0, s59, 143360\n"
    "s_mov_b32 s74, 4456576\n"
    "buffer_load_dwordx4 v5, s[48:51], s74 offen lds\n"
    "ds_read_b64_tr_b16 v[112:113], v6 offset:8192\n"
    "ds_read_b64_tr_b16 v[114:115], v6 offset:8704\n"
    "ds_read_b64_tr_b16 v[116:117], v6 offset:8200\n"
    "ds_read_b64_tr_b16 v[118:119], v6 offset:8712\n"
    "ds_read_b64_tr_b16 v[120:121], v6 offset:16384\n"
    "ds_read_b64_tr_b16 v[122:123], v6 offset:16896\n"
    "ds_read_b64_tr_b16 v[124:125], v6 offset:16392\n"
    "ds_read_b64_tr_b16 v[126:127], v6 offset:16904\n"
    "ds_read_b64_tr_b16 v[128:129], v6 offset:0\n"
    "ds_read_b64_tr_b16 v[130:131], v6 offset:512\n"
    "ds_read_b64_tr_b16 v[132:133], v6 offset:8\n"
    "ds_read_b64_tr_b16 v[134:135], v6 offset:520\n"
    "s_waitcnt vmcnt(24) lgkmcnt(0)\n"
    "s_barrier\n"
    "ds_read_b64_tr_b16 v[136:137], v6 offset:32768\n"
    "ds_read_b64_tr_b16 v[138:139], v6 offset:33280\n"
    "ds_read_b64_tr_b16 v[140:141], v6 offset:32776\n"
    "ds_read_b64_tr_b16 v[142:143], v6 offset:33288\n"
    "ds_read_b64_tr_b16 v[144:145], v6 offset:40960\n"
    "ds_read_b64_tr_b16 v[146:147], v6 offset:41472\n"
    "ds_read_b64_tr_b16 v[148:149], v6 offset:40968\n"
    "ds_read_b64_tr_b16 v[150:151], v6 offset:41480\n"
    "ds_read_b64_tr_b16 v[152:153], v6 offset:24576\n"
    "ds_read_b64_tr_b16 v[154:155], v6 offset:25088\n"
    "ds_read_b64_tr_b16 v[156:157], v6 offset:24584\n"
    "ds_read_b64_tr_b16 v[158:159], v6 offset:25096\n"
    "s_waitcnt vmcnt(18) lgkmcnt(0)\n"
    "s_barrier\n"
    "ds_read_b64_tr_b16 v[160:161], v16 offset:8192\n"
    "ds_read_b64_tr_b16 v[162:163], v16 offset:8704\n"
    "ds_read_b64_tr_b16 v[164:165], v16 offset:8200\n"
    "ds_read_b64_tr_b16 v[166:167], v16 offset:8712\n"
    "ds_read_b64_tr_b16 v[168:169], v16 offset:16384\n"
    "ds_read_b64_tr_b16 v[170:171], v16 offset:16896\n"
    "ds_read_b64_tr_b16 v[172:173], v16 offset:16392\n"
    "ds_read_b64_tr_b16 v[174:175], v16 offset:16904\n"
    "ds_read_b64_tr_b16 v[176:177], v16 offset:0\n"
    "ds_read_b64_tr_b16 v[178:179], v16 offset:512\n"
    "ds_read_b64_tr_b16 v[180:181], v16 offset:8\n"
    "ds_read_b64_tr_b16 v[182:183], v16 offset:520\n"
    "s_waitcnt vmcnt(12) lgkmcnt(0)\n"
    "s_barrier\n"
    "ds_read_b64_tr_b16 v[184:185], v16 offset:32768\n"
    "ds_read_b64_tr_b16 v[186:187], v16 offset:33280\n"
    "ds_read_b64_tr_b16 v[188:189], v16 offset:32776\n"
    "ds_read_b64_tr_b16 v[190:191], v16 offset:33288\n"
    "ds_read_b64_tr_b16 v[192:193], v16 offset:40960\n"
    "ds_read_b64_tr_b16 v[194:195], v16 offset:41472\n"
    "ds_read_b64_tr_b16 v[196:197], v16 offset:40968\n"
    "ds_read_b64_tr_b16 v[198:199], v16 offset:41480\n"
    "ds_read_b64_tr_b16 v[200:201], v16 offset:24576\n"
    "ds_read_b64_tr_b16 v[202:203], v16 offset:25088\n"
    "ds_read_b64_tr_b16 v[204:205], v16 offset:24584\n"
    "ds_read_b64_tr_b16 v[206:207], v16 offset:25096\n"
    "s_waitcnt vmcnt(6) lgkmcnt(0)\n"
    "s_barrier\n"
    "ds_read_b64_tr_b16 v[208:209], v17 offset:8192\n"
    "ds_read_b64_tr_b16 v[210:211], v17 offset:8704\n"
    "ds_read_b64_tr_b16 v[212:213], v17 offset:8200\n"
    "ds_read_b64_tr_b16 v[214:215], v17 offset:8712\n"
    "ds_read_b64_tr_b16 v[216:217], v17 offset:16384\n"
    "ds_read_b64_tr_b16 v[218:219], v17 offset:16896\n"
    "ds_read_b64_tr_b16 v[220:221], v17 offset:16392\n"
    "ds_read_b64_tr_b16 v[222:223], v17 offset:16904\n"
    "ds_read_b64_tr_b16 v[224:225], v17 offset:0\n"
    "ds_read_b64_tr_b16 v[226:227], v17 offset:512\n"
    "ds_read_b64_tr_b16 v[228:229], v17 offset:8\n"
    "ds_read_b64_tr_b16 v[230:231], v17 offset:520\n"
    "s_waitcnt vmcnt(0) lgkmcnt(0)\n"
    "s_barrier\n"
    "ds_read_b64_tr_b16 v[232:233], v17 offset:32768\n"
    "ds_read_b64_tr_b16 v[234:235], v17 offset:33280\n"
    "ds_read_b64_tr_b16 v[236:237], v17 offset:32776\n"
    "ds_read_b64_tr_b16 v[238:239], v17 offset:33288\n"
    "ds_read_b64_tr_b16 v[240:241], v17 offset:40960\n"
    "ds_read_b64_tr_b16 v[242:243], v17 offset:41472\n"
    "ds_read_b64_tr_b16 v[244:245], v17 offset:40968\n"
    "ds_read_b64_tr_b16 v[246:247], v17 offset:41480\n"
    "ds_read_b64_tr_b16 v[248:249], v17 offset:24576\n"
    "ds_read_b64_tr_b16 v[250:251], v17 offset:25088\n"
    "ds_read_b64_tr_b16 v[252:253], v17 offset:24584\n"
    "ds_read_b64_tr_b16 v[254:255], v17 offset:25096\n"
    "s_waitcnt lgkmcnt(0)\n"
    "s_barrier\n"
    "s_mov_b32 s68, 0\n"
    "s_mov_b32 s71, 0\n"
    "s_mov_b32 s78, 65536\n"
    "v_add_u32 v9, s78, v0\n"
    "s_mov_b32 s64, 0xbfb8aa3b\n"
    "s_mov_b32 s72, 262144\n"
    ".LgP_par_%=:\n"
    "buffer_load_dword v12, off, s[36:39], 0 sc1\n"
    "s_waitcnt vmcnt(0)\n"
    "v_readfirstlane_b32 s74, v12\n"
    "s_nop 0\n"
    "s_cmp_ge_i32 s74, 0\n"
    "s_cbranch_scc1 .LgP_par1_%=\n"
    "s_sleep 4\n"
    "s_sub_u32 s72, s72, 1\n"
    "s_cbranch_scc0 .LgP_par_%=\n"
    "s_mov_b32 s72, 0\n"
    ".LgP_par1_%=:\n"
    "s_and_b32 s74, s74, 1\n"
    "s_lshl_b32 s74, s74, 13\n"
    "s_add_u32 s36, s36, s74\n"
    "s_addc_u32 s37, s37, 0\n"
    "s_branch .LgP_block_%=\n"
    ".LgP_LOOPB_%=:\n"
    ".LgP_head_%=:\n"
    "v_mfma_f32_16x16x32_bf16 v[48:51], v[16:19], a[8:11], 0\n"
    "s_waitcnt lgkmcnt(2)\n"
    "s_sub_u32 s74, s68, 1\n"
    "v_mfma_f32_16x16x32_bf16 v[56:59], v[16:19], a[12:15], 0\n"
    "ds_read_b128 v[16:19], v9 offset:0\n"
    "v_cvt_pk_bf16_f32 v40, v40, v41\n"
    "v_mfma_f32_16x16x32_bf16 v[52:55], v[20:23], a[8:11], 0\n"
    "s_waitcnt lgkmcnt(2)\n"
    "v_cvt_pk_bf16_f32 v41, v42, v43\n"
    "v_cvt_pk_bf16_f32 v42, v44, v45\n"
    "v_mfma_f32_16x16x32_bf16 v[60:63], v[20:23], a[12:15], 0\n"
    "ds_read_b128 v[20:23], v9 offset:2048\n"
    "v_cvt_pk_bf16_f32 v43, v46, v47\n"
    "v_mfma_f32_16x16x32_bf16 v[48:51], v[24:27], a[32:35], v[48:51]\n"
    "s_waitcnt lgkmcnt(2)\n"
    "v_cvt_pk_bf16_f32 v72, v72, v73\n"
    "v_cvt_pk_bf16_f32 v73, v74, v75\n"
    "v_mfma_f32_16x16x32_bf16 v[56:59], v[24:27], a[36:39], v[56:59]\n"
    "ds_read_b128 v[24:27], v9 offset:1024\n"
    "v_cvt_pk_bf16_f32 v74, v76, v77\n"
    "v_mfma_f32_16x16x32_bf16 v[52:55], v[28:31], a[32:35], v[52:55]\n"
    "s_waitcnt lgkmcnt(2)\n"
    "v_cvt_pk_bf16_f32 v75, v78, v79\n"
    "v_mfma_f32_16x16x32_bf16 v[60:63], v[28:31], a[36:39], v[60:63]\n"
    "ds_read_b128 v[28:31], v9 offset:3072\n"
    "v_mfma_f32_16x16x32_bf16 v[32:35], a[0:3], v[16:19], 0\n"
    "s_and_b32 s76, s71, s54\n"
    "buffer_store_dwordx4 v[40:43], v1, s[36:39], s79 offen offset:1024 sc1\n"
    "v_mfma_f32_16x16x32_bf16 v[36:39], a[4:7], v[16:19], 0\n"
    "s_mul_i32 s76, s76, s53\n"
    "buffer_store_dwordx4 v[72:75], v1, s[36:39], s73 offen offset:1024 sc1\n"
    "v_mfma_f32_16x16x32_bf16 v[64:67], a[16:19], v[16:19], 0\n"
    "s_waitcnt lgkmcnt(2)\n"
    "s_add_u32 s76, s76, s52\n"
    "v_mfma_f32_16x16x32_bf16 v[68:71], a[20:23], v[16:19], 0\n"
    "ds_read_b128 v[16:19], v9 offset:4096\n"
    "v_mfma_f32_16x16x32_bf16 v[32:35], a[24:27], v[20:23], v[32:35]\n"
    "s_and_b32 s77, s71, 1\n"
    "v_mfma_f32_16x16x32_bf16 v[36:39], a[28:31], v[20:23], v[36:39]\n"
    "s_mul_i32 s77, s77, 65536\n"
    "v_mfma_f32_16x16x32_bf16 v[64:67], a[40:43], v[20:23], v[64:67]\n"
    "s_waitcnt lgkmcnt(2)\n"
    "s_add_u32 s77, s77, s59\n"
    "v_mfma_f32_16x16x32_bf16 v[68:71], a[44:47], v[20:23], v[68:71]\n"
    "ds_read_b128 v[20:23], v9 offset:5120\n"
    "v_mfma_f32_16x16x32_bf16 v[40:43], a[0:3], v[24:27], 0\n"
    "v_mfma_f32_16x16x32_bf16 v[44:47], a[4:7], v[24:27], 0\n"
    "v_mfma_f32_16x16x32_bf16 v[72:75], a[16:19], v[24:27], 0\n"
    "s_waitcnt lgkmcnt(2)\n"
    "v_mfma_f32_16x16x32_bf16 v[76:79], a[20:23], v[24:27], 0\n"
    "ds_read_b128 v[24:27], v9 offset:6144\n"
    "v_mfma_f32_16x16x32_bf16 v[40:43], a[24:27], v[28:31], v[40:43]\n"
    "v_mfma_f32_16x16x32_bf16 v[44:47], a[28:31], v[28:31], v[44:47]\n"
    "v_mfma_f32_16x16x32_bf16 v[72:75], a[40:43], v[28:31], v[72:75]\n"
    "s_waitcnt lgkmcnt(2)\n"
    "v_mfma_f32_16x16x32_bf16 v[76:79], a[44:47], v[28:31], v[76:79]\n"
    "ds_read_b128 v[28:31], v9 offset:7168\n"
    "v_mfma_f32_16x16x32_bf16 v[32:35], a[48:51], v[16:19], v[32:35]\n"
    "v_mfma_f32_16x16x32_bf16 v[36:39], a[52:55], v[16:19], v[36:39]\n"
    "v_mfma_f32_16x16x32_bf16 v[48:51], v[16:19], a[56:59], v[48:51]\n"
    "v_mfma_f32_16x16x32_bf16 v[56:59], v[16:19], a[60:63], v[56:59]\n"
    "v_mfma_f32_16x16x32_bf16 v[64:67], a[64:67], v[16:19], v[64:67]\n"
    "s_waitcnt lgkmcnt(2)\n"
    "v_mfma_f32_16x16x32_bf16 v[68:71], a[68:71], v[16:19], v[68:71]\n"
    "ds_read_b128 v[16:19], v9 offset:8192\n"
    "v_mfma_f32_16x16x32_bf16 v[40:43], a[48:51], v[20:23], v[40:43]\n"
    "v_mfma_f32_16x16x32_bf16 v[44:47], a[52:55], v[20:23], v[44:47]\n"
    "v_mfma_f32_16x16x32_bf16 v[52:55], v[20:23], a[56:59], v[52:55]\n"
    "v_mfma_f32_16x16x32_bf16 v[60:63], v[20:23], a[60:63], v[60:63]\n"
    "v_mfma_f32_16x16x32_bf16 v[72:75], a[64:67], v[20:23], v[72:75]\n"
    "s_waitcnt lgkmcnt(2)\n"
    "v_mfma_f32_16x16x32_bf16 v[76:79], a[68:71], v[20:23], v[76:79]\n"
    "ds_read_b128 v[20:23], v9 offset:9216\n"
    "v_mfma_f32_16x16x32_bf16 v[32:35], a[72:75], v[24:27], v[32:35]\n"
    "v_mfma_f32_16x16x32_bf16 v[36:39], a[76:79], v[24:27], v[36:39]\n"
    "v_mfma_f32_16x16x32_bf16 v[48:51], v[24:27], a[80:83], v[48:51]\n"
    "v_mfma_f32_16x16x32_bf16 v[56:59], v[24:27], a[84:87], v[56:59]\n"
    "v_mfma_f32_16x16x32_bf16 v[64:67], a[88:91], v[24:27], v[64:67]\n"
    "s_waitcnt lgkmcnt(2)\n"
    "v_mfma_f32_16x16x32_bf16 v[68:71], a[92:95], v[24:27], v[68:71]\n"
    "ds_read_b128 v[24:27], v9 offset:10240\n"
    "v_mfma_f32_16x16x32_bf16 v[40:43], a[72:75], v[28:31], v[40:43]\n"
    "v_mfma_f32_16x16x32_bf16 v[44:47], a[76:79], v[28:31], v[44:47]\n"
    "v_mfma_f32_16x16x32_bf16 v[52:55], v[28:31], a[80:83], v[52:55]\n"
    "v_mfma_f32_16x16x32_bf16 v[60:63], v[28:31], a[84:87], v[60:63]\n"
    "v_mfma_f32_16x16x32_bf16 v[72:75], a[88:91], v[28:31], v[72:75]\n"
    "s_waitcnt lgkmcnt(2)\n"
    "v_mfma_f32_16x16x32_bf16 v[76:79], a[92:95], v[28:31], v[76:79]\n"
    "ds_read_b128 v[28:31], v9 offset:11264\n"
    "v_mfma_f32_16x16x32_bf16 v[32:35], a[96:99], v[16:19], v[32:35]\n"
    "v_mov_b32 v12, s74\n"
    "v_mfma_f32_16x16x32_bf16 v[36:39], a[100:103], v[16:19], v[36:39]\n"
    "s_waitcnt vmcnt(0)\n"
    "v_add_u32 v13, v11, v3\n"
    "v_mfma_f32_16x16x32_bf16 v[48:51], v[16:19], a[104:107], v[48:51]\n"
    "buffer_store_dword v12, v14, s[36:39], 0 offen sc1\n"
    "v_cmp_gt_i32 vcc, s71, v13\n"
    "v_mfma_f32_16x16x32_bf16 v[56:59], v[16:19], a[108:111], v[56:59]\n"
    "v_mfma_f32_16x16x32_bf16 v[64:67], a[112:115], v[16:19], v[64:67]\n"
    "s_waitcnt lgkmcnt(2)\n"
    "v_mfma_f32_16x16x32_bf16 v[68:71], a[116:119], v[16:19], v[68:71]\n"
    "ds_read_b128 v[16:19], v9 offset:12288\n"
    ".LgP_mid_%=:\n"
    "s_cbranch_vccnz .LgP_chk0_%=\n"
    "v_mfma_f32_16x16x32_bf16 v[40:43], a[96:99], v[20:23], v[40:43]\n"
    "s_add_u32 m0, s77, s59\n"
    "s_add_u32 s77, s77, 8192\n"
    "buffer_load_dwordx4 v1, s[36:39], s76 offen sc1 lds\n"
    "v_mfma_f32_16x16x32_bf16 v[44:47], a[100:103], v[20:23], v[44:47]\n"
    "s_mov_b32 s70, 1\n"
    "v_mfma_f32_16x16x32_bf16 v[52:55], v[20:23], a[104:107], v[52:55]\n"
    "s_and_b32 s73, s68, s57\n"
    "v_mfma_f32_16x16x32_bf16 v[60:63], v[20:23], a[108:111], v[60:63]\n"
    "buffer_load_dwordx4 v1, s[36:39], s76 offen offset:1024 sc1 lds\n"
    "s_add_u32 s76, s76, 16384\n"
    "v_mfma_f32_16x16x32_bf16 v[72:75], a[112:115], v[20:23], v[72:75]\n"
    "s_waitcnt lgkmcnt(2)\n"
    "s_mul_i32 s73, s73, s56\n"
    "v_mfma_f32_16x16x32_bf16 v[76:79], a[116:119], v[20:23], v[76:79]\n"
    "ds_read_b128 v[20:23], v9 offset:13312\n"
    "v_mfma_f32_16x16x32_bf16 v[32:35], a[120:123], v[24:27], v[32:35]\n"
    "s_add_u32 s73, s73, s55\n"
    "v_mfma_f32_16x16x32_bf16 v[36:39], a[124:127], v[24:27], v[36:39]\n"
    "s_add_u32 s79, s73, 16384\n"
    "v_mfma_f32_16x16x32_bf16 v[48:51], v[24:27], a[128:131], v[48:51]\n"
    "s_add_u32 m0, s77, s59\n"
    "s_add_u32 s77, s77, 8192\n"
    "buffer_load_dwordx4 v1, s[36:39], s76 offen sc1 lds\n"
    "v_mfma_f32_16x16x32_bf16 v[56:59], v[24:27], a[132:135], v[56:59]\n"
    "s_add_u32 s80, s73, 32768\n"
    "v_mfma_f32_16x16x32_bf16 v[64:67], a[136:139], v[24:27], v[64:67]\n"
    "s_waitcnt lgkmcnt(2)\n"
    "v_mfma_f32_16x16x32_bf16 v[68:71], a[140:143], v[24:27], v[68:71]\n"
    "ds_read_b128 v[24:27], v9 offset:14336\n"
    "v_mfma_f32_16x16x32_bf16 v[40:43], a[120:123], v[28:31], v[40:43]\n"
    "v_mfma_f32_16x16x32_bf16 v[44:47], a[124:127], v[28:31], v[44:47]\n"
    "buffer_load_dwordx4 v1, s[36:39], s76 offen offset:1024 sc1 lds\n"
    "s_add_u32 s76, s76, 16384\n"
    "v_mfma_f32_16x16x32_bf16 v[52:55], v[28:31], a[128:131], v[52:55]\n"
    "v_mfma_f32_16x16x32_bf16 v[60:63], v[28:31], a[132:135], v[60:63]\n"
    "v_mfma_f32_16x16x32_bf16 v[72:75], a[136:139], v[28:31], v[72:75]\n"
    "s_waitcnt lgkmcnt(2)\n"
    "v_mfma_f32_16x16x32_bf16 v[76:79], a[140:143], v[28:31], v[76:79]\n"
    "ds_read_b128 v[28:31], v9 offset:15360\n"
    "v_mfma_f32_16x16x32_bf16 v[32:35], a[144:147], v[16:19], v[32:35]\n"
    "s_add_u32 m0, s77, s59\n"
    "s_add_u32 s77, s77, 8192\n"
    "buffer_load_dwordx4 v1, s[36:39], s76 offen sc1 lds\n"
    "v_mfma_f32_16x16x32_bf16 v[36:39], a[148:151], v[16:19], v[36:39]\n"
    "v_mfma_f32_16x16x32_bf16 v[48:51], v[16:19], a[152:155], v[48:51]\n"
    "v_mfma_f32_16x16x32_bf16 v[56:59], v[16:19], a[156:159], v[56:59]\n"
    "v_mfma_f32_16x16x32_bf16 v[64:67], a[160:163], v[16:19], v[64:67]\n"
    "s_waitcnt lgkmcnt(2)\n"
    "buffer_load_dwordx4 v1, s[36:39], s76 offen offset:1024 sc1 lds\n"
    "s_add_u32 s76, s76, 16384\n"
    "v_mfma_f32_16x16x32_bf16 v[68:71], a[164:167], v[16:19], v[68:71]\n"
    "ds_read_b128 v[16:19], v9 offset:16384\n"
    "v_mfma_f32_16x16x32_bf16 v[40:43], a[144:147], v[20:23], v[40:43]\n"
    "v_mfma_f32_16x16x32_bf16 v[44:47], a[148:151], v[20:23], v[44:47]\n"
    "v_mfma_f32_16x16x32_bf16 v[52:55], v[20:23], a[152:155], v[52:55]\n"
    "v_mfma_f32_16x16x32_bf16 v[60:63], v[20:23], a[156:159], v[60:63]\n"
    "s_add_u32 m0, s77, s59\n"
    "s_add_u32 s77, s77, 8192\n"
    "buffer_load_dwordx4 v1, s[36:39], s76 offen sc1 lds\n"
    "v_mfma_f32_16x16x32_bf16 v[72:75], a[160:163], v[20:23], v[72:75]\n"
    "s_waitcnt lgkmcnt(2)\n"
    "v_mfma_f32_16x16x32_bf16 v[76:79], a[164:167], v[20:23], v[76:79]\n"
    "ds_read_b128 v[20:23], v9 offset:17408\n"
    "v_mfma_f32_16x16x32_bf16 v[32:35], a[168:171], v[24:27], v[32:35]\n"
    "v_mfma_f32_16x16x32_bf16 v[36:39], a[172:175], v[24:27], v[36:39]\n"
    "v_mfma_f32_16x16x32_bf16 v[48:51], v[24:27], a[176:179], v[48:51]\n"
    "buffer_load_dwordx4 v1, s[36:39], s76 offen offset:1024 sc1 lds\n"
    "s_add_u32 s76, s76, 16384\n"
    "v_mfma_f32_16x16x32_bf16 v[56:59], v[24:27], a[180:183], v[56:59]\n"
    "v_mfma_f32_16x16x32_bf16 v[64:67], a[184:187], v[24:27], v[64:67]\n"
    "s_waitcnt lgkmcnt(2)\n"
    "v_mfma_f32_16x16x32_bf16 v[68:71], a[188:191], v[24:27], v[68:71]\n"
    "ds_read_b128 v[24:27], v9 offset:18432\n"
    "v_mfma_f32_16x16x32_bf16 v[40:43], a[168:171], v[28:31], v[40:43]\n"
    "v_mfma_f32_16x16x32_bf16 v[44:47], a[172:175], v[28:31], v[44:47]\n"
    "s_mov_b32 m0, s77\n"
    "s_nop 0\n"
    "buffer_load_dwordx4 v8, s[36:39], s76 offen sc1 lds\n"
    "v_mfma_f32_16x16x32_bf16 v[52:55], v[28:31], a[176:179], v[52:55]\n"
    "v_mfma_f32_16x16x32_bf16 v[60:63], v[28:31], a[180:183], v[60:63]\n"
    "v_mfma_f32_16x16x32_bf16 v[72:75], a[184:187], v[28:31], v[72:75]\n"
    "s_waitcnt lgkmcnt(2)\n"
    "v_mfma_f32_16x16x32_bf16 v[76:79], a[188:191], v[28:31], v[76:79]\n"
    "ds_read_b128 v[28:31], v9 offset:19456\n"
    "v_mfma_f32_16x16x32_bf16 v[32:35], a[192:195], v[16:19], v[32:35]\n"
    ".LgP_seg0_%=:\n"
    "s_cmp_lg_u32 s70, 0\n"
    "s_cbranch_scc0 .LgP_chk1_%=\n"
    ".LgP_plain1_%=:\n"
    "v_mfma_f32_16x16x32_bf16 v[36:39], a[196:199], v[16:19], v[36:39]\n"
    "v_mfma_f32_16x16x32_bf16 v[48:51], v[16:19], a[200:203], v[48:51]\n"
    "v_mfma_f32_16x16x32_bf16 v[56:59], v[16:19], a[204:207], v[56:59]\n"
    "v_mfma_f32_16x16x32_bf16 v[64:67], a[208:211], v[16:19], v[64:67]\n"
    "s_waitcnt lgkmcnt(2)\n"
    "v_mfma_f32_16x16x32_bf16 v[68:71], a[212:215], v[16:19], v[68:71]\n"
    "ds_read_b128 v[16:19], v9 offset:20480\n"
    "v_mfma_f32_16x16x32_bf16 v[40:43], a[192:195], v[20:23], v[40:43]\n"
    "v_mfma_f32_16x16x32_bf16 v[44:47], a[196:199], v[20:23], v[44:47]\n"
    "v_mfma_f32_16x16x32_bf16 v[52:55], v[20:23], a[200:203], v[52:55]\n"
    "v_mfma_f32_16x16x32_bf16 v[60:63], v[20:23], a[204:207], v[60:63]\n"
    "v_mfma_f32_16x16x32_bf16 v[72:75], a[208:211], v[20:23], v[72:75]\n"
    "s_waitcnt lgkmcnt(2)\n"
    "v_mfma_f32_16x16x32_bf16 v[76:79], a[212:215], v[20:23], v[76:79]\n"
    "ds_read_b128 v[20:23], v9 offset:21504\n"
    "v_mfma_f32_16x16x32_bf16 v[32:35], a[216:219], v[24:27], v[32:35]\n"
    "v_mfma_f32_16x16x32_bf16 v[36:39], a[220:223], v[24:27], v[36:39]\n"
    "v_mfma_f32_16x16x32_bf16 v[48:51], v[24:27], a[224:227], v[48:51]\n"
    "v_mfma_f32_16x16x32_bf16 v[56:59], v[24:27], a[228:231], v[56:59]\n"
    "v_mfma_f32_16x16x32_bf16 v[64:67], a[232:235], v[24:27], v[64:67]\n"
    "s_waitcnt lgkmcnt(2)\n"
    "v_mfma_f32_16x16x32_bf16 v[68:71], a[236:239], v[24:27], v[68:71]\n"
    "ds_read_b128 v[24:27], v9 offset:22528\n"
    "v_mfma_f32_16x16x32_bf16 v[40:43], a[216:219], v[28:31], v[40:43]\n"
    "v_mfma_f32_16x16x32_bf16 v[44:47], a[220:223], v[28:31], v[44:47]\n"
    "v_mfma_f32_16x16x32_bf16 v[52:55], v[28:31], a[224:227], v[52:55]\n"
    "v_mfma_f32_16x16x32_bf16 v[60:63], v[28:31], a[228:231], v[60:63]\n"
    "v_mfma_f32_16x16x32_bf16 v[72:75], a[232:235], v[28:31], v[72:75]\n"
    "s_waitcnt lgkmcnt(2)\n"
    "v_mfma_f32_16x16x32_bf16 v[76:79], a[236:239], v[28:31], v[76:79]\n"
    "ds_read_b128 v[28:31], v9 offset:23552\n"
    "v_mfma_f32_16x16x32_bf16 v[32:35], a[240:243], v[16:19], v[32:35]\n"
    "v_mfma_f32_16x16x32_bf16 v[36:39], a[244:247], v[16:19], v[36:39]\n"
    "v_mfma_f32_16x16x32_bf16 v[48:51], v[16:19], a[248:251], v[48:51]\n"
    "v_mfma_f32_16x16x32_bf16 v[56:59], v[16:19], a[252:255], v[56:59]\n"
    "v_mfma_f32_16x16x32_bf16 v[64:67], v[80:83], v[16:19], v[64:67]\n"
    "s_waitcnt lgkmcnt(2)\n"
    "v_mfma_f32_16x16x32_bf16 v[68:71], v[84:87], v[16:19], v[68:71]\n"
    "ds_read_b128 v[16:19], v9 offset:24576\n"
    "v_mfma_f32_16x16x32_bf16 v[40:43], a[240:243], v[20:23], v[40:43]\n"
    "v_mfma_f32_16x16x32_bf16 v[44:47], a[244:247], v[20:23], v[44:47]\n"
    "v_mfma_f32_16x16x32_bf16 v[52:55], v[20:23], a[248:251], v[52:55]\n"
    "v_mfma_f32_16x16x32_bf16 v[60:63], v[20:23], a[252:255], v[60:63]\n"
    "v_mfma_f32_16x16x32_bf16 v[72:75], v[80:83], v[20:23], v[72:75]\n"
    "s_waitcnt lgkmcnt(2)\n"
    "v_mfma_f32_16x16x32_bf16 v[76:79], v[84:87], v[20:23], v[76:79]\n"
    "ds_read_b128 v[20:23], v9 offset:25600\n"
    "v_mfma_f32_16x16x32_bf16 v[32:35], v[88:91], v[24:27], v[32:35]\n"
    "v_mfma_f32_16x16x32_bf16 v[36:39], v[92:95], v[24:27], v[36:39]\n"
    "v_mfma_f32_16x16x32_bf16 v[48:51], v[24:27], v[96:99], v[48:51]\n"
    "v_mfma_f32_16x16x32_bf16 v[56:59], v[24:27], v[100:103], v[56:59]\n"
    "v_mfma_f32_16x16x32_bf16 v[64:67], v[104:107], v[24:27], v[64:67]\n"
    "s_waitcnt lgkmcnt(2)\n"
    "v_mfma_f32_16x16x32_bf16 v[68:71], v[108:111], v[24:27], v[68:71]\n"
    "ds_read_b128 v[24:27], v9 offset:26624\n"
    "v_mfma_f32_16x16x32_bf16 v[40:43], v[88:91], v[28:31], v[40:43]\n"
    "v_mfma_f32_16x16x32_bf16 v[44:47], v[92:95], v[28:31], v[44:47]\n"
    ".LgP_seg1_%=:\n"
    "s_cmp_lg_u32 s70, 0\n"
    "s_cbranch_scc0 .LgP_chk2_%=\n"
    ".LgP_plain2_%=:\n"
    "v_mfma_f32_16x16x32_bf16 v[52:55], v[28:31], v[96:99], v[52:55]\n"
    "v_mfma_f32_16x16x32_bf16 v[60:63], v[28:31], v[100:103], v[60:63]\n"
    "v_mfma_f32_16x16x32_bf16 v[72:75], v[104:107], v[28:31], v[72:75]\n"
    "s_waitcnt lgkmcnt(2)\n"
    "v_mfma_f32_16x16x32_bf16 v[76:79], v[108:111], v[28:31], v[76:79]\n"
    "ds_read_b128 v[28:31], v9 offset:27648\n"
    "v_mfma_f32_16x16x32_bf16 v[32:35], v[112:115], v[16:19], v[32:35]\n"
    "v_mfma_f32_16x16x32_bf16 v[36:39], v[116:119], v[16:19], v[36:39]\n"
    "v_mfma_f32_16x16x32_bf16 v[48:51], v[16:19], v[120:123], v[48:51]\n"
    "v_mfma_f32_16x16x32_bf16 v[56:59], v[16:19], v[124:127], v[56:59]\n"
    "v_mfma_f32_16x16x32_bf16 v[64:67], v[128:131], v[16:19], v[64:67]\n"
    "s_waitcnt lgkmcnt(2)\n"
    "v_mfma_f32_16x16x32_bf16 v[68:71], v[132:135], v[16:19], v[68:71]\n"
    "ds_read_b128 v[16:19], v9 offset:28672\n"
    "v_mfma_f32_16x16x32_bf16 v[40:43], v[112:115], v[20:23], v[40:43]\n"
    "v_mfma_f32_16x16x32_bf16 v[44:47], v[116:119], v[20:23], v[44:47]\n"
    "v_mfma_f32_16x16x32_bf16 v[52:55], v[20:23], v[120:123], v[52:55]\n"
    "v_mfma_f32_16x16x32_bf16 v[60:63], v[20:23], v[124:127], v[60:63]\n"
    "v_mfma_f32_16x16x32_bf16 v[72:75], v[128:131], v[20:23], v[72:75]\n"
    "s_waitcnt lgkmcnt(2)\n"
    "v_mfma_f32_16x16x32_bf16 v[76:79], v[132:135], v[20:23], v[76:79]\n"
    "ds_read_b128 v[20:23], v9 offset:29696\n"
    "v_mfma_f32_16x16x32_bf16 v[32:35], v[136:139], v[24:27], v[32:35]\n"
    "v_mfma_f32_16x16x32_bf16 v[36:39], v[140:143], v[24:27], v[36:39]\n"
    "v_mfma_f32_16x16x32_bf16 v[48:51], v[24:27], v[144:147], v[48:51]\n"
    "v_mfma_f32_16x16x32_bf16 v[56:59], v[24:27], v[148:151], v[56:59]\n"
    "v_mfma_f32_16x16x32_bf16 v[64:67], v[152:155], v[24:27], v[64:67]\n"
    "s_waitcnt lgkmcnt(2)\n"
    "v_mfma_f32_16x16x32_bf16 v[68:71], v[156:159], v[24:27], v[68:71]\n"
    "ds_read_b128 v[24:27], v9 offset:30720\n"
    "v_mfma_f32_16x16x32_bf16 v[40:43], v[136:139], v[28:31], v[40:43]\n"
    "v_mfma_f32_16x16x32_bf16 v[44:47], v[140:143], v[28:31], v[44:47]\n"
    "v_mfma_f32_16x16x32_bf16 v[52:55], v[28:31], v[144:147], v[52:55]\n"
    "v_mfma_f32_16x16x32_bf16 v[60:63], v[28:31], v[148:151], v[60:63]\n"
    "v_mfma_f32_16x16x32_bf16 v[72:75], v[152:155], v[28:31], v[72:75]\n"
    "s_waitcnt lgkmcnt(2)\n"
    "v_mfma_f32_16x16x32_bf16 v[76:79], v[156:159], v[28:31], v[76:79]\n"
    "ds_read_b128 v[28:31], v9 offset:31744\n"
    "v_mfma_f32_16x16x32_bf16 v[32:35], v[160:163], v[16:19], v[32:35]\n"
    "v_mfma_f32_16x16x32_bf16 v[36:39], v[164:167], v[16:19], v[36:39]\n"
    "v_mfma_f32_16x16x32_bf16 v[48:51], v[16:19], v[168:171], v[48:51]\n"
    "v_mfma_f32_16x16x32_bf16 v[56:59], v[16:19], v[172:175], v[56:59]\n"
    "v_mfma_f32_16x16x32_bf16 v[64:67], v[176:179], v[16:19], v[64:67]\n"
    "s_waitcnt lgkmcnt(2)\n"
    "v_mfma_f32_16x16x32_bf16 v[68:71], v[180:183], v[16:19], v[68:71]\n"
    "ds_read_b128 v[16:19], v9 offset:32768\n"
    "v_mfma_f32_16x16x32_bf16 v[40:43], v[160:163], v[20:23], v[40:43]\n"
    "v_mfma_f32_16x16x32_bf16 v[44:47], v[164:167], v[20:23], v[44:47]\n"
    "v_mfma_f32_16x16x32_bf16 v[52:55], v[20:23], v[168:171], v[52:55]\n"
    "v_mfma_f32_16x16x32_bf16 v[60:63], v[20:23], v[172:175], v[60:63]\n"
    "v_mfma_f32_16x16x32_bf16 v[72:75], v[176:179], v[20:23], v[72:75]\n"
    "s_waitcnt lgkmcnt(2)\n"
    "v_mfma_f32_16x16x32_bf16 v[76:79], v[180:183], v[20:23], v[76:79]\n"
    "ds_read_b128 v[20:23], v9 offset:33792\n"
    "v_mfma_f32_16x16x32_bf16 v[32:35], v[184:187], v[24:27], v[32:35]\n"
    "v_mfma_f32_16x16x32_bf16 v[36:39], v[188:191], v[24:27], v[36:39]\n"
    "v_mfma_f32_16x16x32_bf16 v[48:51], v[24:27], v[192:195], v[48:51]\n"
    "v_mfma_f32_16x16x32_bf16 v[56:59], v[24:27], v[196:199], v[56:59]\n"
    ".LgP_seg2_%=:\n"
    "s_cmp_lg_u32 s70, 1\n"
    "s_cbranch_scc1 .LgP_slow_%=\n"
    "v_mfma_f32_16x16x32_bf16 v[64:67], v[200:203], v[24:27], v[64:67]\n"
    "s_waitcnt lgkmcnt(2)\n"
    "s_mov_b32 s68, s71\n"
    "v_mfma_f32_16x16x32_bf16 v[68:71], v[204:207], v[24:27], v[68:71]\n"
    "ds_read_b128 v[24:27], v9 offset:34816\n"
    "v_mfma_f32_16x16x32_bf16 v[40:43], v[184:187], v[28:31], v[40:43]\n"
    "s_add_u32 s71, s68, 1\n"
    "v_mfma_f32_16x16x32_bf16 v[44:47], v[188:191], v[28:31], v[44:47]\n"
    "s_cmp_eq_u32 s71, 64\n"
    "v_mfma_f32_16x16x32_bf16 v[52:55], v[28:31], v[192:195], v[52:55]\n"
    "s_cselect_b32 s70, 2, 0\n"
    "v_mfma_f32_16x16x32_bf16 v[60:63], v[28:31], v[196:199], v[60:63]\n"
    "v_mfma_f32_16x16x32_bf16 v[72:75], v[200:203], v[28:31], v[72:75]\n"
    "s_waitcnt lgkmcnt(2)\n"
    "v_mfma_f32_16x16x32_bf16 v[76:79], v[204:207], v[28:31], v[76:79]\n"
    "ds_read_b128 v[28:31], v9 offset:35840\n"
    "v_mfma_f32_16x16x32_bf16 v[48:51], v[16:19], v[216:219], v[48:51]\n"
    "s_waitcnt lgkmcnt(2)\n"
    "v_mfma_f32_16x16x32_bf16 v[56:59], v[16:19], v[220:223], v[56:59]\n"
    "ds_read_b128 v[16:19], v9 offset:32768\n"
    "v_mfma_f32_16x16x32_bf16 v[52:55], v[20:23], v[216:219], v[52:55]\n"
    "s_waitcnt lgkmcnt(2)\n"
    "v_mfma_f32_16x16x32_bf16 v[60:63], v[20:23], v[220:223], v[60:63]\n"
    "ds_read_b128 v[20:23], v9 offset:34816\n"
    "v_mfma_f32_16x16x32_bf16 v[48:51], v[24:27], v[240:243], v[48:51]\n"
    "s_waitcnt lgkmcnt(2)\n"
    "v_mfma_f32_16x16x32_bf16 v[56:59], v[24:27], v[244:247], v[56:59]\n"
    "ds_read_b128 v[24:27], v9 offset:33792\n"
    "v_mfma_f32_16x16x32_bf16 v[52:55], v[28:31], v[240:243], v[52:55]\n"
    "s_waitcnt lgkmcnt(2)\n"
    "v_mfma_f32_16x16x32_bf16 v[60:63], v[28:31], v[244:247], v[60:63]\n"
    "ds_read_b128 v[28:31], v9 offset:35840\n"
    "v_add_u32 v13, v11, v3\n"
    "v_mfma_f32_16x16x32_bf16 v[32:35], v[208:211], v[16:19], v[32:35]\n"
    "v_cmp_gt_i32 vcc, s71, v13\n"
    "v_mfma_f32_16x16x32_bf16 v[36:39], v[212:215], v[16:19], v[36:39]\n"
    "v_cvt_pk_bf16_f32 v48, v48, v49\n"
    "v_cvt_pk_bf16_f32 v49, v50, v51\n"
    "v_mfma_f32_16x16x32_bf16 v[64:67], v[224:227], v[16:19], v[64:67]\n"
    "s_waitcnt lgkmcnt(2)\n"
    "v_cvt_pk_bf16_f32 v50, v52, v53\n"
    "v_cvt_pk_bf16_f32 v51, v54, v55\n"
    "v_xor_b32 v9, s78, v9\n"
    "v_mfma_f32_16x16x32_bf16 v[68:71], v[228:231], v[16:19], v[68:71]\n"
    "s_waitcnt vmcnt(0) lgkmcnt(0)\n"
    "s_barrier\n"
    "ds_read_b128 v[16:19], v9 offset:0\n"
    "v_cvt_pk_bf16_f32 v56, v56, v57\n"
    "v_mfma_f32_16x16x32_bf16 v[32:35], v[232:235], v[20:23], v[32:35]\n"
    "s_or_b64 exec, vcc, 1\n"
    "buffer_atomic_smax v11, v2, s[36:39], 0 offen sc0 sc1\n"
    "s_mov_b64 exec, -1\n"
    "v_cvt_pk_bf16_f32 v57, v58, v59\n"
    "v_cvt_pk_bf16_f32 v58, v60, v61\n"
    "v_mfma_f32_16x16x32_bf16 v[36:39], v[236:239], v[20:23], v[36:39]\n"
    "v_cvt_pk_bf16_f32 v59, v62, v63\n"
    "v_mfma_f32_16x16x32_bf16 v[64:67], v[248:251], v[20:23], v[64:67]\n"
    "buffer_store_dwordx4 v[48:51], v1, s[36:39], s80 offen sc1\n"
    "v_mfma_f32_16x16x32_bf16 v[68:71], v[252:255], v[20:23], v[68:71]\n"
    "ds_read_b128 v[20:23], v9 offset:1024\n"
    "v_mfma_f32_16x16x32_bf16 v[40:43], v[208:211], v[24:27], v[40:43]\n"
    "buffer_store_dwordx4 v[56:59], v1, s[36:39], s80 offen offset:1024 sc1\n"
    "v_mfma_f32_16x16x32_bf16 v[44:47], v[212:215], v[24:27], v[44:47]\n"
    "v_cvt_pk_bf16_f32 v32, v32, v33\n"
    "v_cvt_pk_bf16_f32 v33, v34, v35\n"
    "v_mfma_f32_16x16x32_bf16 v[72:75], v[224:227], v[24:27], v[72:75]\n"
    "v_cvt_pk_bf16_f32 v34, v36, v37\n"
    "v_cvt_pk_bf16_f32 v35, v38, v39\n"
    "v_mfma_f32_16x16x32_bf16 v[76:79], v[228:231], v[24:27], v[76:79]\n"
    "ds_read_b128 v[24:27], v9 offset:2048\n"
    "v_cvt_pk_bf16_f32 v64, v64, v65\n"
    "v_mfma_f32_16x16x32_bf16 v[40:43], v[232:235], v[28:31], v[40:43]\n"
    "v_cvt_pk_bf16_f32 v65, v66, v67\n"
    "v_cvt_pk_bf16_f32 v66, v68, v69\n"
    "v_mfma_f32_16x16x32_bf16 v[44:47], v[236:239], v[28:31], v[44:47]\n"
    "v_cvt_pk_bf16_f32 v67, v70, v71\n"
    "v_mfma_f32_16x16x32_bf16 v[72:75], v[248:251], v[28:31], v[72:75]\n"
    "s_waitcnt lgkmcnt(2)\n"
    "buffer_store_dwordx4 v[32:35], v1, s[36:39], s79 offen sc1\n"
    "v_mfma_f32_16x16x32_bf16 v[76:79], v[252:255], v[28:31], v[76:79]\n"
    "ds_read_b128 v[28:31], v9 offset:3072\n"
    "buffer_store_dwordx4 v[64:67], v1, s[36:39], s73 offen sc1\n"
    "s_branch .LgP_head_%=\n"
    ".LgP_LOOPE_%=:\n"
    ".LgP_done_%=:\n"
    "s_waitcnt vmcnt(0)\n"
    "s_mov_b32 s74, 63\n"
    "v_mov_b32 v12, s74\n"
    "s_mov_b64 exec, s[62:63]\n"
    "buffer_store_dword v12, v14, s[36:39], 0 offen sc1\n"
    "s_mov_b64 exec, -1\n"
    "s_waitcnt vmcnt(0)\n"
    "s_branch .LgP_FIN_%=\n"
    ".LgP_chk0_%=:\n"
    "s_or_b64 exec, vcc, 1\n"
    "buffer_atomic_smax v11, v2, s[36:39], 0 offen sc0 sc1\n"
    "s_mov_b64 exec, -1\n"
    "v_mfma_f32_16x16x32_bf16 v[40:43], a[96:99], v[20:23], v[40:43]\n"
    "s_and_b32 s73, s68, s57\n"
    "v_mfma_f32_16x16x32_bf16 v[44:47], a[100:103], v[20:23], v[44:47]\n"
    "s_mul_i32 s73, s73, s56\n"
    "v_mfma_f32_16x16x32_bf16 v[52:55], v[20:23], a[104:107], v[52:55]\n"
    "s_add_u32 s73, s73, s55\n"
    "v_mfma_f32_16x16x32_bf16 v[60:63], v[20:23], a[108:111], v[60:63]\n"
    "s_add_u32 s79, s73, 16384\n"
    "v_mfma_f32_16x16x32_bf16 v[72:75], a[112:115], v[20:23], v[72:75]\n"
    "s_waitcnt lgkmcnt(2)\n"
    "s_add_u32 s80, s73, 32768\n"
    "v_mfma_f32_16x16x32_bf16 v[76:79], a[116:119], v[20:23], v[76:79]\n"
    "ds_read_b128 v[20:23], v9 offset:13312\n"
    "v_mfma_f32_16x16x32_bf16 v[32:35], a[120:123], v[24:27], v[32:35]\n"
    "v_mfma_f32_16x16x32_bf16 v[36:39], a[124:127], v[24:27], v[36:39]\n"
    "v_mfma_f32_16x16x32_bf16 v[48:51], v[24:27], a[128:131], v[48:51]\n"
    "v_mfma_f32_16x16x32_bf16 v[56:59], v[24:27], a[132:135], v[56:59]\n"
    "v_mfma_f32_16x16x32_bf16 v[64:67], a[136:139], v[24:27], v[64:67]\n"
    "s_waitcnt lgkmcnt(2)\n"
    "v_mfma_f32_16x16x32_bf16 v[68:71], a[140:143], v[24:27], v[68:71]\n"
    "ds_read_b128 v[24:27], v9 offset:14336\n"
    "v_mfma_f32_16x16x32_bf16 v[40:43], a[120:123], v[28:31], v[40:43]\n"
    "v_mfma_f32_16x16x32_bf16 v[44:47], a[124:127], v[28:31], v[44:47]\n"
    "v_mfma_f32_16x16x32_bf16 v[52:55], v[28:31], a[128:131], v[52:55]\n"
    "v_mfma_f32_16x16x32_bf16 v[60:63], v[28:31], a[132:135], v[60:63]\n"
    "v_mfma_f32_16x16x32_bf16 v[72:75], a[136:139], v[28:31], v[72:75]\n"
    "s_waitcnt lgkmcnt(2)\n"
    "v_mfma_f32_16x16x32_bf16 v[76:79], a[140:143], v[28:31], v[76:79]\n"
    "ds_read_b128 v[28:31], v9 offset:15360\n"
    "v_mfma_f32_16x16x32_bf16 v[32:35], a[144:147], v[16:19], v[32:35]\n"
    "v_mfma_f32_16x16x32_bf16 v[36:39], a[148:151], v[16:19], v[36:39]\n"
    "v_mfma_f32_16x16x32_bf16 v[48:51], v[16:19], a[152:155], v[48:51]\n"
    "v_mfma_f32_16x16x32_bf16 v[56:59], v[16:19], a[156:159], v[56:59]\n"
    "v_mfma_f32_16x16x32_bf16 v[64:67], a[160:163], v[16:19], v[64:67]\n"
    "s_waitcnt lgkmcnt(2)\n"
    "v_mfma_f32_16x16x32_bf16 v[68:71], a[164:167], v[16:19], v[68:71]\n"
    "ds_read_b128 v[16:19], v9 offset:16384\n"
    "v_mfma_f32_16x16x32_bf16 v[40:43], a[144:147], v[20:23], v[40:43]\n"
    "v_mfma_f32_16x16x32_bf16 v[44:47], a[148:151], v[20:23], v[44:47]\n"
    "v_mfma_f32_16x16x32_bf16 v[52:55], v[20:23], a[152:155], v[52:55]\n"
    "v_mfma_f32_16x16x32_bf16 v[60:63], v[20:23], a[156:159], v[60:63]\n"
    "v_mfma_f32_16x16x32_bf16 v[72:75], a[160:163], v[20:23], v[72:75]\n"
    "s_waitcnt lgkmcnt(2)\n"
    "v_mfma_f32_16x16x32_bf16 v[76:79], a[164:167], v[20:23], v[76:79]\n"
    "ds_read_b128 v[20:23], v9 offset:17408\n"
    "v_mfma_f32_16x16x32_bf16 v[32:35], a[168:171], v[24:27], v[32:35]\n"
    "v_mfma_f32_16x16x32_bf16 v[36:39], a[172:175], v[24:27], v[36:39]\n"
    "v_mfma_f32_16x16x32_bf16 v[48:51], v[24:27], a[176:179], v[48:51]\n"
    "v_mfma_f32_16x16x32_bf16 v[56:59], v[24:27], a[180:183], v[56:59]\n"
    "v_mfma_f32_16x16x32_bf16 v[64:67], a[184:187], v[24:27], v[64:67]\n"
    "s_waitcnt lgkmcnt(2)\n"
    "v_mfma_f32_16x16x32_bf16 v[68:71], a[188:191], v[24:27], v[68:71]\n"
    "ds_read_b128 v[24:27], v9 offset:18432\n"
    "v_mfma_f32_16x16x32_bf16 v[40:43], a[168:171], v[28:31], v[40:43]\n"
    "v_mfma_f32_16x16x32_bf16 v[44:47], a[172:175], v[28:31], v[44:47]\n"
    "v_mfma_f32_16x16x32_bf16 v[52:55], v[28:31], a[176:179], v[52:55]\n"
    "v_mfma_f32_16x16x32_bf16 v[60:63], v[28:31], a[180:183], v[60:63]\n"
    "v_mfma_f32_16x16x32_bf16 v[72:75], a[184:187], v[28:31], v[72:75]\n"
    "s_waitcnt lgkmcnt(2)\n"
    "v_mfma_f32_16x16x32_bf16 v[76:79], a[188:191], v[28:31], v[76:79]\n"
    "ds_read_b128 v[28:31], v9 offset:19456\n"
    "v_mfma_f32_16x16x32_bf16 v[32:35], a[192:195], v[16:19], v[32:35]\n"
    "s_branch .LgP_seg0_%=\n"
    ".LgP_chk1_%=:\n"
    "s_waitcnt vmcnt(0)\n"
    "v_add_u32 v13, v11, v3\n"
    "v_cmp_gt_i32 vcc, s71, v13\n"
    "s_cbranch_vccz .LgP_dma1_%=\n"
    "s_or_b64 exec, vcc, 1\n"
    "buffer_atomic_smax v11, v2, s[36:39], 0 offen sc0 sc1\n"
    "s_mov_b64 exec, -1\n"
    "s_branch .LgP_plain1_%=\n"
    ".LgP_dma1_%=:\n"
    "s_mov_b32 s70, 1\n"
    "v_mfma_f32_16x16x32_bf16 v[36:39], a[196:199], v[16:19], v[36:39]\n"
    "s_add_u32 m0, s77, s59\n"
    "s_add_u32 s77, s77, 8192\n"
    "buffer_load_dwordx4 v1, s[36:39], s76 offen sc1 lds\n"
    "v_mfma_f32_16x16x32_bf16 v[48:51], v[16:19], a[200:203], v[48:51]\n"
    "v_mfma_f32_16x16x32_bf16 v[56:59], v[16:19], a[204:207], v[56:59]\n"
    "v_mfma_f32_16x16x32_bf16 v[64:67], a[208:211], v[16:19], v[64:67]\n"
    "s_waitcnt lgkmcnt(2)\n"
    "buffer_load_dwordx4 v1, s[36:39], s76 offen offset:1024 sc1 lds\n"
    "s_add_u32 s76, s76, 16384\n"
    "v_mfma_f32_16x16x32_bf16 v[68:71], a[212:215], v[16:19], v[68:71]\n"
    "ds_read_b128 v[16:19], v9 offset:20480\n"
    "v_mfma_f32_16x16x32_bf16 v[40:43], a[192:195], v[20:23], v[40:43]\n"
    "v_mfma_f32_16x16x32_bf16 v[44:47], a[196:199], v[20:23], v[44:47]\n"
    "v_mfma_f32_16x16x32_bf16 v[52:55], v[20:23], a[200:203], v[52:55]\n"
    "v_mfma_f32_16x16x32_bf16 v[60:63], v[20:23], a[204:207], v[60:63]\n"
    "s_add_u32 m0, s77, s59\n"
    "s_add_u32 s77, s77, 8192\n"
    "buffer_load_dwordx4 v1, s[36:39], s76 offen sc1 lds\n"
    "v_mfma_f32_16x16x32_bf16 v[72:75], a[208:211], v[20:23], v[72:75]\n"
    "s_waitcnt lgkmcnt(2)\n"
    "v_mfma_f32_16x16x32_bf16 v[76:79], a[212:215], v[20:23], v[76:79]\n"
    "ds_read_b128 v[20:23], v9 offset:21504\n"
    "v_mfma_f32_16x16x32_bf16 v[32:35], a[216:219], v[24:27], v[32:35]\n"
    "v_mfma_f32_16x16x32_bf16 v[36:39], a[220:223], v[24:27], v[36:39]\n"
    "v_mfma_f32_16x16x32_bf16 v[48:51], v[24:27], a[224:227], v[48:51]\n"
    "buffer_load_dwordx4 v1, s[36:39], s76 offen offset:1024 sc1 lds\n"
    "s_add_u32 s76, s76, 16384\n"
    "v_mfma_f32_16x16x32_bf16 v[56:59], v[24:27], a[228:231], v[56:59]\n"
    "v_mfma_f32_16x16x32_bf16 v[64:67], a[232:235], v[24:27], v[64:67]\n"
    "s_waitcnt lgkmcnt(2)\n"
    "v_mfma_f32_16x16x32_bf16 v[68:71], a[236:239], v[24:27], v[68:71]\n"
    "ds_read_b128 v[24:27], v9 offset:22528\n"
    "v_mfma_f32_16x16x32_bf16 v[40:43], a[216:219], v[28:31], v[40:43]\n"
    "v_mfma_f32_16x16x32_bf16 v[44:47], a[220:223], v[28:31], v[44:47]\n"
    "s_add_u32 m0, s77, s59\n"
    "s_add_u32 s77, s77, 8192\n"
    "buffer_load_dwordx4 v1, s[36:39], s76 offen sc1 lds\n"
    "v_mfma_f32_16x16x32_bf16 v[52:55], v[28:31], a[224:227], v[52:55]\n"
    "v_mfma_f32_16x16x32_bf16 v[60:63], v[28:31], a[228:231], v[60:63]\n"
    "v_mfma_f32_16x16x32_bf16 v[72:75], a[232:235], v[28:31], v[72:75]\n"
    "s_waitcnt lgkmcnt(2)\n"
    "v_mfma_f32_16x16x32_bf16 v[76:79], a[236:239], v[28:31], v[76:79]\n"
    "ds_read_b128 v[28:31], v9 offset:23552\n"
    "v_mfma_f32_16x16x32_bf16 v[32:35], a[240:243], v[16:19], v[32:35]\n"
    "buffer_load_dwordx4 v1, s[36:39], s76 offen offset:1024 sc1 lds\n"
    "s_add_u32 s76, s76, 16384\n"
    "v_mfma_f32_16x16x32_bf16 v[36:39], a[244:247], v[16:19], v[36:39]\n"
    "v_mfma_f32_16x16x32_bf16 v[48:51], v[16:19], a[248:251], v[48:51]\n"
    "v_mfma_f32_16x16x32_bf16 v[56:59], v[16:19], a[252:255], v[56:59]\n"
    "v_mfma_f32_16x16x32_bf16 v[64:67], v[80:83], v[16:19], v[64:67]\n"
    "s_waitcnt lgkmcnt(2)\n"
    "s_add_u32 m0, s77, s59\n"
    "s_add_u32 s77, s77, 8192\n"
    "buffer_load_dwordx4 v1, s[36:39], s76 offen sc1 lds\n"
    "v_mfma_f32_16x16x32_bf16 v[68:71], v[84:87], v[16:19], v[68:71]\n"
    "ds_read_b128 v[16:19], v9 offset:24576\n"
    "v_mfma_f32_16x16x32_bf16 v[40:43], a[240:243], v[20:23], v[40:43]\n"
    "v_mfma_f32_16x16x32_bf16 v[44:47], a[244:247], v[20:23], v[44:47]\n"
    "v_mfma_f32_16x16x32_bf16 v[52:55], v[20:23], a[248:251], v[52:55]\n"
    "v_mfma_f32_16x16x32_bf16 v[60:63], v[20:23], a[252:255], v[60:63]\n"
    "buffer_load_dwordx4 v1, s[36:39], s76 offen offset:1024 sc1 lds\n"
    "s_add_u32 s76, s76, 16384\n"
    "v_mfma_f32_16x16x32_bf16 v[72:75], v[80:83], v[20:23], v[72:75]\n"
    "s_waitcnt lgkmcnt(2)\n"
    "v_mfma_f32_16x16x32_bf16 v[76:79], v[84:87], v[20:23], v[76:79]\n"
    "ds_read_b128 v[20:23], v9 offset:25600\n"
    "v_mfma_f32_16x16x32_bf16 v[32:35], v[88:91], v[24:27], v[32:35]\n"
    "v_mfma_f32_16x16x32_bf16 v[36:39], v[92:95], v[24:27], v[36:39]\n"
    "v_mfma_f32_16x16x32_bf16 v[48:51], v[24:27], v[96:99], v[48:51]\n"
    "s_mov_b32 m0, s77\n"
    "s_nop 0\n"
    "buffer_load_dwordx4 v8, s[36:39], s76 offen sc1 lds\n"
    "v_mfma_f32_16x16x32_bf16 v[56:59], v[24:27], v[100:103], v[56:59]\n"
    "v_mfma_f32_16x16x32_bf16 v[64:67], v[104:107], v[24:27], v[64:67]\n"
    "s_waitcnt lgkmcnt(2)\n"
    "v_mfma_f32_16x16x32_bf16 v[68:71], v[108:111], v[24:27], v[68:71]\n"
    "ds_read_b128 v[24:27], v9 offset:26624\n"
    "v_mfma_f32_16x16x32_bf16 v[40:43], v[88:91], v[28:31], v[40:43]\n"
    "v_mfma_f32_16x16x32_bf16 v[44:47], v[92:95], v[28:31], v[44:47]\n"
    "s_branch .LgP_seg1_%=\n"
    ".LgP_chk2_%=:\n"
    "s_waitcnt vmcnt(0)\n"
    "v_add_u32 v13, v11, v3\n"
    "v_cmp_gt_i32 vcc, s71, v13\n"
    "s_cbranch_vccz .LgP_dma2_%=\n"
    "s_or_b64 exec, vcc, 1\n"
    "buffer_atomic_smax v11, v2, s[36:39], 0 offen sc0 sc1\n"
    "s_mov_b64 exec, -1\n"
    "s_branch .LgP_plain2_%=\n"
    ".LgP_dma2_%=:\n"
    "s_mov_b32 s70, 2\n"
    "v_mfma_f32_16x16x32_bf16 v[52:55], v[28:31], v[96:99], v[52:55]\n"
    "s_add_u32 m0, s77, s59\n"
    "s_add_u32 s77, s77, 8192\n"
    "buffer_load_dwordx4 v1, s[36:39], s76 offen sc1 lds\n"
    "v_mfma_f32_16x16x32_bf16 v[60:63], v[28:31], v[100:103], v[60:63]\n"
    "v_mfma_f32_16x16x32_bf16 v[72:75], v[104:107], v[28:31], v[72:75]\n"
    "s_waitcnt lgkmcnt(2)\n"
    "v_mfma_f32_16x16x32_bf16 v[76:79], v[108:111], v[28:31], v[76:79]\n"
    "ds_read_b128 v[28:31], v9 offset:27648\n"
    "v_mfma_f32_16x16x32_bf16 v[32:35], v[112:115], v[16:19], v[32:35]\n"
    "v_mfma_f32_16x16x32_bf16 v[36:39], v[116:119], v[16:19], v[36:39]\n"
    "buffer_load_dwordx4 v1, s[36:39], s76 offen offset:1024 sc1 lds\n"
    "s_add_u32 s76, s76, 16384\n"
    "v_mfma_f32_16x16x32_bf16 v[48:51], v[16:19], v[120:123], v[48:51]\n"
    "v_mfma_f32_16x16x32_bf16 v[56:59], v[16:19], v[124:127], v[56:59]\n"
    "v_mfma_f32_16x16x32_bf16 v[64:67], v[128:131], v[16:19], v[64:67]\n"
    "s_waitcnt lgkmcnt(2)\n"
    "v_mfma_f32_16x16x32_bf16 v[68:71], v[132:135], v[16:19], v[68:71]\n"
    "ds_read_b128 v[16:19], v9 offset:28672\n"
    "v_mfma_f32_16x16x32_bf16 v[40:43], v[112:115], v[20:23], v[40:43]\n"
    "s_add_u32 m0, s77, s59\n"
    "s_add_u32 s77, s77, 8192\n"
    "buffer_load_dwordx4 v1, s[36:39], s76 offen sc1 lds\n"
    "v_mfma_f32_16x16x32_bf16 v[44:47], v[116:119], v[20:23], v[44:47]\n"
    "v_mfma_f32_16x16x32_bf16 v[52:55], v[20:23], v[120:123], v[52:55]\n"
    "v_mfma_f32_16x16x32_bf16 v[60:63], v[20:23], v[124:127], v[60:63]\n"
    "v_mfma_f32_16x16x32_bf16 v[72:75], v[128:131], v[20:23], v[72:75]\n"
    "s_waitcnt lgkmcnt(2)\n"
    "buffer_load_dwordx4 v1, s[36:39], s76 offen offset:1024 sc1 lds\n"
    "s_add_u32 s76, s76, 16384\n"
    "v_mfma_f32_16x16x32_bf16 v[76:79], v[132:135], v[20:23], v[76:79]\n"
    "ds_read_b128 v[20:23], v9 offset:29696\n"
    "v_mfma_f32_16x16x32_bf16 v[32:35], v[136:139], v[24:27], v[32:35]\n"
    "v_mfma_f32_16x16x32_bf16 v[36:39], v[140:143], v[24:27], v[36:39]\n"
    "v_mfma_f32_16x16x32_bf16 v[48:51], v[24:27], v[144:147], v[48:51]\n"
    "v_mfma_f32_16x16x32_bf16 v[56:59], v[24:27], v[148:151], v[56:59]\n"
    "s_add_u32 m0, s77, s59\n"
    "s_add_u32 s77, s77, 8192\n"
    "buffer_load_dwordx4 v1, s[36:39], s76 offen sc1 lds\n"
    "v_mfma_f32_16x16x32_bf16 v[64:67], v[152:155], v[24:27], v[64:67]\n"
    "s_waitcnt lgkmcnt(2)\n"
    "v_mfma_f32_16x16x32_bf16 v[68:71], v[156:159], v[24:27], v[68:71]\n"
    "ds_read_b128 v[24:27], v9 offset:30720\n"
    "v_mfma_f32_16x16x32_bf16 v[40:43], v[136:139], v[28:31], v[40:43]\n"
    "v_mfma_f32_16x16x32_bf16 v[44:47], v[140:143], v[28:31], v[44:47]\n"
    "v_mfma_f32_16x16x32_bf16 v[52:55], v[28:31], v[144:147], v[52:55]\n"
    "buffer_load_dwordx4 v1, s[36:39], s76 offen offset:1024 sc1 lds\n"
    "s_add_u32 s76, s76, 16384\n"
    "v_mfma_f32_16x16x32_bf16 v[60:63], v[28:31], v[148:151], v[60:63]\n"
    "v_mfma_f32_16x16x32_bf16 v[72:75], v[152:155], v[28:31], v[72:75]\n"
    "s_waitcnt lgkmcnt(2)\n"
    "v_mfma_f32_16x16x32_bf16 v[76:79], v[156:159], v[28:31], v[76:79]\n"
    "ds_read_b128 v[28:31], v9 offset:31744\n"
    "v_mfma_f32_16x16x32_bf16 v[32:35], v[160:163], v[16:19], v[32:35]\n"
    "v_mfma_f32_16x16x32_bf16 v[36:39], v[164:167], v[16:19], v[36:39]\n"
    "s_add_u32 m0, s77, s59\n"
    "s_add_u32 s77, s77, 8192\n"
    "buffer_load_dwordx4 v1, s[36:39], s76 offen sc1 lds\n"
    "v_mfma_f32_16x16x32_bf16 v[48:51], v[16:19], v[168:171], v[48:51]\n"
    "v_mfma_f32_16x16x32_bf16 v[56:59], v[16:19], v[172:175], v[56:59]\n"
    "v_mfma_f32_16x16x32_bf16 v[64:67], v[176:179], v[16:19], v[64:67]\n"
    "s_waitcnt lgkmcnt(2)\n"
    "v_mfma_f32_16x16x32_bf16 v[68:71], v[180:183], v[16:19], v[68:71]\n"
    "ds_read_b128 v[16:19], v9 offset:32768\n"
    "v_mfma_f32_16x16x32_bf16 v[40:43], v[160:163], v[20:23], v[40:43]\n"
    "buffer_load_dwordx4 v1, s[36:39], s76 offen offset:1024 sc1 lds\n"
    "s_add_u32 s76, s76, 16384\n"
    "v_mfma_f32_16x16x32_bf16 v[44:47], v[164:167], v[20:23], v[44:47]\n"
    "v_mfma_f32_16x16x32_bf16 v[52:55], v[20:23], v[168:171], v[52:55]\n"
    "v_mfma_f32_16x16x32_bf16 v[60:63], v[20:23], v[172:175], v[60:63]\n"
    "v_mfma_f32_16x16x32_bf16 v[72:75], v[176:179], v[20:23], v[72:75]\n"
    "s_waitcnt lgkmcnt(2)\n"
    "s_mov_b32 m0, s77\n"
    "s_nop 0\n"
    "buffer_load_dwordx4 v8, s[36:39], s76 offen sc1 lds\n"
    "v_mfma_f32_16x16x32_bf16 v[76:79], v[180:183], v[20:23], v[76:79]\n"
    "ds_read_b128 v[20:23], v9 offset:33792\n"
    "v_mfma_f32_16x16x32_bf16 v[32:35], v[184:187], v[24:27], v[32:35]\n"
    "v_mfma_f32_16x16x32_bf16 v[36:39], v[188:191], v[24:27], v[36:39]\n"
    "v_mfma_f32_16x16x32_bf16 v[48:51], v[24:27], v[192:195], v[48:51]\n"
    "v_mfma_f32_16x16x32_bf16 v[56:59], v[24:27], v[196:199], v[56:59]\n"
    "s_branch .LgP_seg2_%=\n"
    ".LgP_slow_%=:\n"
    "s_cmp_lg_u32 s70, 0\n"
    "s_cbranch_scc1 .LgP_nolast_%=\n"
    "s_waitcnt vmcnt(0)\n"
    "v_add_u32 v13, v11, v3\n"
    "v_cmp_gt_i32 vcc, s71, v13\n"
    "s_nop 0\n"
    "s_or_b64 exec, vcc, 1\n"
    "buffer_atomic_smax v11, v2, s[36:39], 0 offen sc0 sc1\n"
    "s_mov_b64 exec, -1\n"
    ".LgP_nolast_%=:\n"
    "v_mfma_f32_16x16x32_bf16 v[64:67], v[200:203], v[24:27], v[64:67]\n"
    "s_waitcnt lgkmcnt(2)\n"
    "v_mfma_f32_16x16x32_bf16 v[68:71], v[204:207], v[24:27], v[68:71]\n"
    "ds_read_b128 v[24:27], v9 offset:34816\n"
    "v_mfma_f32_16x16x32_bf16 v[40:43], v[184:187], v[28:31], v[40:43]\n"
    "v_mfma_f32_16x16x32_bf16 v[44:47], v[188:191], v[28:31], v[44:47]\n"
    "v_mfma_f32_16x16x32_bf16 v[52:55], v[28:31], v[192:195], v[52:55]\n"
    "v_mfma_f32_16x16x32_bf16 v[60:63], v[28:31], v[196:199], v[60:63]\n"
    "v_mfma_f32_16x16x32_bf16 v[72:75], v[200:203], v[28:31], v[72:75]\n"
    "s_waitcnt lgkmcnt(2)\n"
    "v_mfma_f32_16x16x32_bf16 v[76:79], v[204:207], v[28:31], v[76:79]\n"
    "ds_read_b128 v[28:31], v9 offset:35840\n"
    "v_mfma_f32_16x16x32_bf16 v[48:51], v[16:19], v[216:219], v[48:51]\n"
    "s_waitcnt lgkmcnt(2)\n"
    "v_mfma_f32_16x16x32_bf16 v[56:59], v[16:19], v[220:223], v[56:59]\n"
    "ds_read_b128 v[16:19], v9 offset:32768\n"
    "v_mfma_f32_16x16x32_bf16 v[52:55], v[20:23], v[216:219], v[52:55]\n"
    "s_waitcnt lgkmcnt(2)\n"
    "v_mfma_f32_16x16x32_bf16 v[60:63], v[20:23], v[220:223], v[60:63]\n"
    "ds_read_b128 v[20:23], v9 offset:34816\n"
    "v_mfma_f32_16x16x32_bf16 v[48:51], v[24:27], v[240:243], v[48:51]\n"
    "s_waitcnt lgkmcnt(2)\n"
    "v_mfma_f32_16x16x32_bf16 v[56:59], v[24:27], v[244:247], v[56:59]\n"
    "ds_read_b128 v[24:27], v9 offset:33792\n"
    "v_mfma_f32_16x16x32_bf16 v[52:55], v[28:31], v[240:243], v[52:55]\n"
    "s_waitcnt lgkmcnt(2)\n"
    "v_mfma_f32_16x16x32_bf16 v[60:63], v[28:31], v[244:247], v[60:63]\n"
    "ds_read_b128 v[28:31], v9 offset:35840\n"
    "v_mfma_f32_16x16x32_bf16 v[32:35], v[208:211], v[16:19], v[32:35]\n"
    "v_mfma_f32_16x16x32_bf16 v[36:39], v[212:215], v[16:19], v[36:39]\n"
    "v_cvt_pk_bf16_f32 v48, v48, v49\n"
    "v_cvt_pk_bf16_f32 v49, v50, v51\n"
    "v_mfma_f32_16x16x32_bf16 v[64:67], v[224:227], v[16:19], v[64:67]\n"
    "s_waitcnt lgkmcnt(2)\n"
    "v_cvt_pk_bf16_f32 v50, v52, v53\n"
    "v_cvt_pk_bf16_f32 v51, v54, v55\n"
    "v_mfma_f32_16x16x32_bf16 v[68:71], v[228:231], v[16:19], v[68:71]\n"
    "v_cvt_pk_bf16_f32 v56, v56, v57\n"
    "v_mfma_f32_16x16x32_bf16 v[32:35], v[232:235], v[20:23], v[32:35]\n"
    "v_cvt_pk_bf16_f32 v57, v58, v59\n"
    "v_cvt_pk_bf16_f32 v58, v60, v61\n"
    "v_mfma_f32_16x16x32_bf16 v[36:39], v[236:239], v[20:23], v[36:39]\n"
    "v_cvt_pk_bf16_f32 v59, v62, v63\n"
    "v_mfma_f32_16x16x32_bf16 v[64:67], v[248:251], v[20:23], v[64:67]\n"
    "s_waitcnt lgkmcnt(1)\n"
    "buffer_store_dwordx4 v[48:51], v1, s[36:39], s80 offen sc1\n"
    "v_mfma_f32_16x16x32_bf16 v[68:71], v[252:255], v[20:23], v[68:71]\n"
    "v_mfma_f32_16x16x32_bf16 v[40:43], v[208:211], v[24:27], v[40:43]\n"
    "buffer_store_dwordx4 v[56:59], v1, s[36:39], s80 offen offset:1024 sc1\n"
    "v_mfma_f32_16x16x32_bf16 v[44:47], v[212:215], v[24:27], v[44:47]\n"
    "v_cvt_pk_bf16_f32 v32, v32, v33\n"
    "v_cvt_pk_bf16_f32 v33, v34, v35\n"
    "v_mfma_f32_16x16x32_bf16 v[72:75], v[224:227], v[24:27], v[72:75]\n"
    "s_waitcnt lgkmcnt(0)\n"
    "v_cvt_pk_bf16_f32 v34, v36, v37\n"
    "v_cvt_pk_bf16_f32 v35, v38, v39\n"
    "v_mfma_f32_16x16x32_bf16 v[76:79], v[228:231], v[24:27], v[76:79]\n"
    "v_cvt_pk_bf16_f32 v64, v64, v65\n"
    "v_mfma_f32_16x16x32_bf16 v[40:43], v[232:235], v[28:31], v[40:43]\n"
    "v_cvt_pk_bf16_f32 v65, v66, v67\n"
    "v_cvt_pk_bf16_f32 v66, v68, v69\n"
    "v_mfma_f32_16x16x32_bf16 v[44:47], v[236:239], v[28:31], v[44:47]\n"
    "v_cvt_pk_bf16_f32 v67, v70, v71\n"
    "v_mfma_f32_16x16x32_bf16 v[72:75], v[248:251], v[28:31], v[72:75]\n"
    "buffer_store_dwordx4 v[32:35], v1, s[36:39], s79 offen sc1\n"
    "v_mfma_f32_16x16x32_bf16 v[76:79], v[252:255], v[28:31], v[76:79]\n"
    "buffer_store_dwordx4 v[64:67], v1, s[36:39], s73 offen sc1\n"
    "v_cvt_pk_bf16_f32 v40, v40, v41\n"
    "v_cvt_pk_bf16_f32 v41, v42, v43\n"
    "v_cvt_pk_bf16_f32 v42, v44, v45\n"
    "v_cvt_pk_bf16_f32 v43, v46, v47\n"
    "buffer_store_dwordx4 v[40:43], v1, s[36:39], s79 offen offset:1024 sc1\n"
    "v_cvt_pk_bf16_f32 v72, v72, v73\n"
    "v_cvt_pk_bf16_f32 v73, v74, v75\n"
    "v_cvt_pk_bf16_f32 v74, v76, v77\n"
    "v_cvt_pk_bf16_f32 v75, v78, v79\n"
    "buffer_store_dwordx4 v[72:75], v1, s[36:39], s73 offen offset:1024 sc1\n"
    "s_mov_b32 s68, s71\n"
    "s_cmp_eq_u32 s70, 0\n"
    "s_cbranch_scc1 .LgP_idle_%=\n"
    "s_cmp_eq_u32 s68, 64\n"
    "s_cbranch_scc1 .LgP_done_%=\n"
    "s_waitcnt vmcnt(6)\n"
    ".LgP_bar_%=:\n"
    "v_xor_b32 v9, s78, v9\n"
    "s_add_u32 s71, s68, 1\n"
    "s_cmp_eq_u32 s71, 64\n"
    "s_cselect_b32 s70, 2, 0\n"
    "s_barrier\n"
    "ds_read_b128 v[16:19], v9 offset:0\n"
    "ds_read_b128 v[20:23], v9 offset:1024\n"
    "ds_read_b128 v[24:27], v9 offset:2048\n"
    "ds_read_b128 v[28:31], v9 offset:3072\n"
    "v_add_u32 v13, v11, v3\n"
    "v_cmp_gt_i32 vcc, s71, v13\n"
    "s_nop 0\n"
    "s_or_b64 exec, vcc, 1\n"
    "buffer_atomic_smax v11, v2, s[36:39], 0 offen sc0 sc1\n"
    "s_mov_b64 exec, -1\n"
    "s_waitcnt lgkmcnt(3)\n"
    "v_mfma_f32_16x16x32_bf16 v[48:51], v[16:19], a[8:11], 0\n"
    "s_waitcnt lgkmcnt(2)\n"
    "s_sub_u32 s74, s68, 1\n"
    "v_mfma_f32_16x16x32_bf16 v[56:59], v[16:19], a[12:15], 0\n"
    "ds_read_b128 v[16:19], v9 offset:0\n"
    "v_mfma_f32_16x16x32_bf16 v[52:55], v[20:23], a[8:11], 0\n"
    "s_waitcnt lgkmcnt(2)\n"
    "s_and_b32 s76, s71, s54\n"
    "v_mfma_f32_16x16x32_bf16 v[60:63], v[20:23], a[12:15], 0\n"
    "ds_read_b128 v[20:23], v9 offset:2048\n"
    "v_mfma_f32_16x16x32_bf16 v[48:51], v[24:27], a[32:35], v[48:51]\n"
    "s_waitcnt lgkmcnt(2)\n"
    "s_mul_i32 s76, s76, s53\n"
    "v_mfma_f32_16x16x32_bf16 v[56:59], v[24:27], a[36:39], v[56:59]\n"
    "ds_read_b128 v[24:27], v9 offset:1024\n"
    "v_mfma_f32_16x16x32_bf16 v[52:55], v[28:31], a[32:35], v[52:55]\n"
    "s_waitcnt lgkmcnt(2)\n"
    "s_add_u32 s76, s76, s52\n"
    "v_mfma_f32_16x16x32_bf16 v[60:63], v[28:31], a[36:39], v[60:63]\n"
    "ds_read_b128 v[28:31], v9 offset:3072\n"
    "v_mfma_f32_16x16x32_bf16 v[32:35], a[0:3], v[16:19], 0\n"
    "s_and_b32 s77, s71, 1\n"
    "v_mfma_f32_16x16x32_bf16 v[36:39], a[4:7], v[16:19], 0\n"
    "s_mul_i32 s77, s77, 65536\n"
    "v_mfma_f32_16x16x32_bf16 v[64:67], a[16:19], v[16:19], 0\n"
    "s_waitcnt lgkmcnt(2)\n"
    "s_add_u32 s77, s77, s59\n"
    "v_mfma_f32_16x16x32_bf16 v[68:71], a[20:23], v[16:19], 0\n"
    "ds_read_b128 v[16:19], v9 offset:4096\n"
    "v_mfma_f32_16x16x32_bf16 v[32:35], a[24:27], v[20:23], v[32:35]\n"
    "v_mfma_f32_16x16x32_bf16 v[36:39], a[28:31], v[20:23], v[36:39]\n"
    "v_mfma_f32_16x16x32_bf16 v[64:67], a[40:43], v[20:23], v[64:67]\n"
    "s_waitcnt lgkmcnt(2)\n"
    "v_mfma_f32_16x16x32_bf16 v[68:71], a[44:47], v[20:23], v[68:71]\n"
    "ds_read_b128 v[20:23], v9 offset:5120\n"
    "v_mfma_f32_16x16x32_bf16 v[40:43], a[0:3], v[24:27], 0\n"
    "v_mfma_f32_16x16x32_bf16 v[44:47], a[4:7], v[24:27], 0\n"
    "v_mfma_f32_16x16x32_bf16 v[72:75], a[16:19], v[24:27], 0\n"
    "s_waitcnt lgkmcnt(2)\n"
    "v_mfma_f32_16x16x32_bf16 v[76:79], a[20:23], v[24:27], 0\n"
    "ds_read_b128 v[24:27], v9 offset:6144\n"
    "v_mfma_f32_16x16x32_bf16 v[40:43], a[24:27], v[28:31], v[40:43]\n"
    "v_mfma_f32_16x16x32_bf16 v[44:47], a[28:31], v[28:31], v[44:47]\n"
    "v_mfma_f32_16x16x32_bf16 v[72:75], a[40:43], v[28:31], v[72:75]\n"
    "s_waitcnt lgkmcnt(2)\n"
    "v_mfma_f32_16x16x32_bf16 v[76:79], a[44:47], v[28:31], v[76:79]\n"
    "ds_read_b128 v[28:31], v9 offset:7168\n"
    "v_mfma_f32_16x16x32_bf16 v[32:35], a[48:51], v[16:19], v[32:35]\n"
    "v_mfma_f32_16x16x32_bf16 v[36:39], a[52:55], v[16:19], v[36:39]\n"
    "v_mfma_f32_16x16x32_bf16 v[48:51], v[16:19], a[56:59], v[48:51]\n"
    "v_mfma_f32_16x16x32_bf16 v[56:59], v[16:19], a[60:63], v[56:59]\n"
    "v_mfma_f32_16x16x32_bf16 v[64:67], a[64:67], v[16:19], v[64:67]\n"
    "s_waitcnt lgkmcnt(2)\n"
    "v_mfma_f32_16x16x32_bf16 v[68:71], a[68:71], v[16:19], v[68:71]\n"
    "ds_read_b128 v[16:19], v9 offset:8192\n"
    "v_mfma_f32_16x16x32_bf16 v[40:43], a[48:51], v[20:23], v[40:43]\n"
    "v_mfma_f32_16x16x32_bf16 v[44:47], a[52:55], v[20:23], v[44:47]\n"
    "v_mfma_f32_16x16x32_bf16 v[52:55], v[20:23], a[56:59], v[52:55]\n"
    "v_mfma_f32_16x16x32_bf16 v[60:63], v[20:23], a[60:63], v[60:63]\n"
    "v_mfma_f32_16x16x32_bf16 v[72:75], a[64:67], v[20:23], v[72:75]\n"
    "s_waitcnt lgkmcnt(2)\n"
    "v_mfma_f32_16x16x32_bf16 v[76:79], a[68:71], v[20:23], v[76:79]\n"
    "ds_read_b128 v[20:23], v9 offset:9216\n"
    "v_mfma_f32_16x16x32_bf16 v[32:35], a[72:75], v[24:27], v[32:35]\n"
    "v_mfma_f32_16x16x32_bf16 v[36:39], a[76:79], v[24:27], v[36:39]\n"
    "v_mfma_f32_16x16x32_bf16 v[48:51], v[24:27], a[80:83], v[48:51]\n"
    "v_mfma_f32_16x16x32_bf16 v[56:59], v[24:27], a[84:87], v[56:59]\n"
    "v_mfma_f32_16x16x32_bf16 v[64:67], a[88:91], v[24:27], v[64:67]\n"
    "s_waitcnt lgkmcnt(2)\n"
    "v_mfma_f32_16x16x32_bf16 v[68:71], a[92:95], v[24:27], v[68:71]\n"
    "ds_read_b128 v[24:27], v9 offset:10240\n"
    "v_mfma_f32_16x16x32_bf16 v[40:43], a[72:75], v[28:31], v[40:43]\n"
    "v_mfma_f32_16x16x32_bf16 v[44:47], a[76:79], v[28:31], v[44:47]\n"
    "v_mfma_f32_16x16x32_bf16 v[52:55], v[28:31], a[80:83], v[52:55]\n"
    "v_mfma_f32_16x16x32_bf16 v[60:63], v[28:31], a[84:87], v[60:63]\n"
    "v_mfma_f32_16x16x32_bf16 v[72:75], a[88:91], v[28:31], v[72:75]\n"
    "s_waitcnt lgkmcnt(2)\n"
    "v_mfma_f32_16x16x32_bf16 v[76:79], a[92:95], v[28:31], v[76:79]\n"
    "ds_read_b128 v[28:31], v9 offset:11264\n"
    "v_mfma_f32_16x16x32_bf16 v[32:35], a[96:99], v[16:19], v[32:35]\n"
    "v_mov_b32 v12, s74\n"
    "v_mfma_f32_16x16x32_bf16 v[36:39], a[100:103], v[16:19], v[36:39]\n"
    "s_waitcnt vmcnt(0)\n"
    "v_add_u32 v13, v11, v3\n"
    "v_mfma_f32_16x16x32_bf16 v[48:51], v[16:19], a[104:107], v[48:51]\n"
    "buffer_store_dword v12, v14, s[36:39], 0 offen sc1\n"
    "v_cmp_gt_i32 vcc, s71, v13\n"
    "v_mfma_f32_16x16x32_bf16 v[56:59], v[16:19], a[108:111], v[56:59]\n"
    "v_mfma_f32_16x16x32_bf16 v[64:67], a[112:115], v[16:19], v[64:67]\n"
    "s_waitcnt lgkmcnt(2)\n"
    "v_mfma_f32_16x16x32_bf16 v[68:71], a[116:119], v[16:19], v[68:71]\n"
    "ds_read_b128 v[16:19], v9 offset:12288\n"
    "s_branch .LgP_mid_%=\n"
    ".LgP_idle_%=:\n"
    "s_waitcnt vmcnt(6)\n"
    "v_add_u32 v13, v11, v3\n"
    "v_cmp_gt_i32 vcc, s71, v13\n"
    "s_cbranch_vccnz .LgP_idle2_%=\n"
    "s_and_b32 s76, s71, s54\n"
    "s_mul_i32 s76, s76, s53\n"
    "s_add_u32 s76, s76, s52\n"
    "s_and_b32 s77, s71, 1\n"
    "s_mul_i32 s77, s77, 65536\n"
    "s_add_u32 s77, s77, s59\n"
    "s_add_u32 m0, s77, s59\n"
    "s_add_u32 s77, s77, 8192\n"
    "buffer_load_dwordx4 v1, s[36:39], s76 offen sc1 lds\n"
    "buffer_load_dwordx4 v1, s[36:39], s76 offen offset:1024 sc1 lds\n"
    "s_add_u32 s76, s76, 16384\n"
    "s_add_u32 m0, s77, s59\n"
    "s_add_u32 s77, s77, 8192\n"
    "buffer_load_dwordx4 v1, s[36:39], s76 offen sc1 lds\n"
    "buffer_load_dwordx4 v1, s[36:39], s76 offen offset:1024 sc1 lds\n"
    "s_add_u32 s76, s76, 16384\n"
    "s_add_u32 m0, s77, s59\n"
    "s_add_u32 s77, s77, 8192\n"
    "buffer_load_dwordx4 v1, s[36:39], s76 offen sc1 lds\n"
    "buffer_load_dwordx4 v1, s[36:39], s76 offen offset:1024 sc1 lds\n"
    "s_add_u32 s76, s76, 16384\n"
    "s_add_u32 m0, s77, s59\n"
    "s_add_u32 s77, s77, 8192\n"
    "buffer_load_dwordx4 v1, s[36:39], s76 offen sc1 lds\n"
    "buffer_load_dwordx4 v1, s[36:39], s76 offen offset:1024 sc1 lds\n"
    "s_add_u32 s76, s76, 16384\n"
    "s_mov_b32 m0, s77\n"
    "s_nop 0\n"
    "buffer_load_dwordx4 v8, s[36:39], s76 offen sc1 lds\n"
    "s_waitcnt vmcnt(9)\n"
    "s_sub_u32 s74, s68, 1\n"
    "v_mov_b32 v12, s74\n"
    "s_mov_b64 exec, s[62:63]\n"
    "buffer_store_dword v12, v14, s[36:39], 0 offen sc1\n"
    "s_mov_b64 exec, -1\n"
    "s_waitcnt vmcnt(1)\n"
    "s_branch .LgP_bar_%=\n"
    ".LgP_idle2_%=:\n"
    "s_or_b64 exec, vcc, 1\n"
    "buffer_atomic_smax v11, v2, s[36:39], 0 offen sc0 sc1\n"
    "s_mov_b64 exec, -1\n"
    "s_waitcnt vmcnt(1)\n"
    "s_sub_u32 s74, s68, 1\n"
    "v_mov_b32 v12, s74\n"
    "s_mov_b64 exec, s[62:63]\n"
    "buffer_store_dword v12, v14, s[36:39], 0 offen sc1\n"
    "s_mov_b64 exec, -1\n"
    "s_waitcnt vmcnt(1)\n"
    "s_branch .LgP_block1_%=\n"
    ".LgP_block_%=:\n"
    "v_add_u32 v13, v11, v3\n"
    "v_cmp_gt_i32 vcc, s71, v13\n"
    "s_nop 0\n"
    "s_or_b64 exec, vcc, 1\n"
    "buffer_atomic_smax v11, v2, s[36:39], 0 offen sc0 sc1\n"
    "s_mov_b64 exec, -1\n"
    "s_waitcnt vmcnt(0)\n"
    ".LgP_block1_%=:\n"
    "v_add_u32 v13, v11, v3\n"
    "v_cmp_gt_i32 vcc, s71, v13\n"
    "s_cbranch_vccz .LgP_go_%=\n"
    "s_sleep 4\n"
    "s_sub_u32 s72, s72, 1\n"
    "s_cbranch_scc0 .LgP_block_%=\n"
    "s_mov_b32 s72, 0\n"
    ".LgP_go_%=:\n"
    "s_and_b32 s76, s71, s54\n"
    "s_mul_i32 s76, s76, s53\n"
    "s_add_u32 s76, s76, s52\n"
    "s_and_b32 s77, s71, 1\n"
    "s_mul_i32 s77, s77, 65536\n"
    "s_add_u32 s77, s77, s59\n"
    "s_add_u32 m0, s77, s59\n"
    "s_add_u32 s77, s77, 8192\n"
    "buffer_load_dwordx4 v1, s[36:39], s76 offen sc1 lds\n"
    "buffer_load_dwordx4 v1, s[36:39], s76 offen offset:1024 sc1 lds\n"
    "s_add_u32 s76, s76, 16384\n"
    "s_add_u32 m0, s77, s59\n"
    "s_add_u32 s77, s77, 8192\n"
    "buffer_load_dwordx4 v1, s[36:39], s76 offen sc1 lds\n"
    "buffer_load_dwordx4 v1, s[36:39], s76 offen offset:1024 sc1 lds\n"
    "s_add_u32 s76, s76, 16384\n"
    "s_add_u32 m0, s77, s59\n"
    "s_add_u32 s77, s77, 8192\n"
    "buffer_load_dwordx4 v1, s[36:39], s76 offen sc1 lds\n"
    "buffer_load_dwordx4 v1, s[36:39], s76 offen offset:1024 sc1 lds\n"
    "s_add_u32 s76, s76, 16384\n"
    "s_add_u32 m0, s77, s59\n"
    "s_add_u32 s77, s77, 8192\n"
    "buffer_load_dwordx4 v1, s[36:39], s76 offen sc1 lds\n"
    "buffer_load_dwordx4 v1, s[36:39], s76 offen offset:1024 sc1 lds\n"
    "s_add_u32 s76, s76, 16384\n"
    "s_mov_b32 m0, s77\n"
    "s_nop 0\n"
    "buffer_load_dwordx4 v8, s[36:39], s76 offen sc1 lds\n"
    "s_waitcnt vmcnt(0)\n"
    "s_branch .LgP_bar_%=\n"
    ".LgP_FIN_%=:\n"
  );
}
DEVI void role_up(const Params& p, int L, int j, int i, u32 dbg) {
  const int lane = threadIdx.x & 63, w = threadIdx.x >> 6, col = (6 * j + i) * 192;
  const u32 wb = 576u * 4608 * 2 - col * 2;
  GEMM_DECL;
  g_w0 = make_srd(p.w1 + col, wb); g_w1 = make_srd(p.w3 + col, wb); g_w2 = g_w1;
  g_ldw = 9216;
  g_in_base = pgb(L, B_XN2); g_in_stride = SLOT; g_in_mask = RX - 1;
  g_out_base = pgb(L, B_H + 9 * j); g_out_kb = 12 * i; g_out_mask = RH - 1;
  g_flag = lane < 3 ? mbox(L, j, SL_DOWN + lane, 4 * i + w) : OOB;
  g_paddr = mbox(L, j, SL_UP + i, lane);
  g_pbias = lane < 16 ? 0 : (lane < 19 ? RH : 1000);
  v16u sa, sb, va;
  gemm_args(GEMM_ARGS, p, dbg, sa, sb, va);
  RUN_ASM(
    "s_nop 4\n"
    "v_add_u32 v8, s59, v0\n"
    "s_add_u32 m0, s59, 0\n"
    "s_mov_b32 s74, 0\n"
    "buffer_load_dwordx4 v5, s[40:43], s74 offen lds\n"
    "s_add_u32 m0, s59, 4096\n"
    "s_mov_b32 s74, 128\n"
    "buffer_load_dwordx4 v5, s[40:43], s74 offen lds\n"
    "s_add_u32 m0, s59, 8192\n"
    "s_mov_b32 s74, 256\n"
    "buffer_load_dwordx4 v5, s[40:43], s74 offen lds\n"
    "s_add_u32 m0, s59, 12288\n"
    "s_mov_b32 s74, 0\n"
    "buffer_load_dwordx4 v5, s[44:47], s74 offen lds\n"
    "s_add_u32 m0, s59, 16384\n"
    "s_mov_b32 s74, 128\n"
    "buffer_load_dwordx4 v5, s[44:47], s74 offen lds\n"
    "s_add_u32 m0, s59, 20480\n"
    "s_mov_b32 s74, 256\n"
    "buffer_load_dwordx4 v5, s[44:47], s74 offen lds\n"
    "s_add_u32 m0, s59, 24576\n"
    "s_mov_b32 s74, 294912\n"
    "buffer_load_dwordx4 v5, s[40:43], s74 offen lds\n"
    "s_add_u32 m0, s59, 28672\n"
    "s_mov_b32 s74, 295040\n"
    "buffer_load_dwordx4 v5, s[40:43], s74 offen lds\n"
    "s_add_u32 m0, s59, 32768\n"
    "s_mov_b32 s74, 295168\n"
    "buffer_load_dwordx4 v5, s[40:43], s74 offen lds\n"
    "s_add_u32 m0, s59, 36864\n"
    "s_mov_b32 s74, 294912\n"
    "buffer_load_dwordx4 v5, s[44:47], s74 offen lds\n"
    "s_add_u32 m0, s59, 40960\n"
    "s_mov_b32 s74, 295040\n"
    "buffer_load_dwordx4 v5, s[44:47], s74 offen lds\n"
    "s_add_u32 m0, s59, 45056\n"
    "s_mov_b32 s74, 295168\n"
    "buffer_load_dwordx4 v5, s[44:47], s74 offen lds\n"
    "s_add_u32 m0, s59, 49152\n"
    "s_mov_b32 s74, 589824\n"
    "buffer_load_dwordx4 v5, s[40:43], s74 offen lds\n"
    "s_add_u32 m0, s59, 53248\n"
    "s_mov_b32 s74, 589952\n"
    "buffer_load_dwordx4 v5, s[40:43], s74 offen lds\n"
    "s_add_u32 m0, s59, 57344\n"
    "s_mov_b32 s74, 590080\n"
    "buffer_load_dwordx4 v5, s[40:43], s74 offen lds\n"
    "s_add_u32 m0, s59, 61440\n"
    "s_mov_b32 s74, 589824\n"
    "buffer_load_dwordx4 v5, s[44:47], s74 offen lds\n"
    "s_add_u32 m0, s59, 65536\n"
    "s_mov_b32 s74, 589952\n"
    "buffer_load_dwordx4 v5, s[44:47], s74 offen lds\n"
    "s_add_u32 m0, s59, 69632\n"
    "s_mov_b32 s74, 590080\n"
    "buffer_load_dwordx4 v5, s[44:47], s74 offen lds\n"
    "s_add_u32 m0, s59, 73728\n"
    "s_mov_b32 s74, 884736\n"
    "buffer_load_dwordx4 v5, s[40:43], s74 offen lds\n"
    "s_add_u32 m0, s59, 77824\n"
    "s_mov_b32 s74, 884864\n"
    "buffer_load_dwordx4 v5, s[40:43], s74 offen lds\n"
    "s_add_u32 m0, s59, 81920\n"
    "s_mov_b32 s74, 884992\n"
    "buffer_load_dwordx4 v5, s[40:43], s74 offen lds\n"
    "s_add_u32 m0, s59, 86016\n"
    "s_mov_b32 s74, 884736\n"
    "buffer_load_dwordx4 v5, s[44:47], s74 offen lds\n"
    "s_add_u32 m0, s59, 90112\n"
    "s_mov_b32 s74, 884864\n"
    "buffer_load_dwordx4 v5, s[44:47], s74 offen lds\n"
    "s_add_u32 m0, s59, 94208\n"
    "s_mov_b32 s74, 884992\n"
    "buffer_load_dwordx4 v5, s[44:47], s74 offen lds\n"
    "s_add_u32 m0, s59, 98304\n"
    "s_mov_b32 s74, 1179648\n"
    "buffer_load_dwordx4 v5, s[40:43], s74 offen lds\n"
    "s_add_u32 m0, s59, 102400\n"
    "s_mov_b32 s74, 1179776\n"
    "buffer_load_dwordx4 v5, s[40:43], s74 offen lds\n"
    "s_add_u32 m0, s59, 106496\n"
    "s_mov_b32 s74, 1179904\n"
    "buffer_load_dwordx4 v5, s[40:43], s74 offen lds\n"
    "s_add_u32 m0, s59, 110592\n"
    "s_mov_b32 s74, 1179648\n"
    "buffer_load_dwordx4 v5, s[44:47], s74 offen lds\n"
    "s_add_u32 m0, s59, 114688\n"
    "s_mov_b32 s74, 1179776\n"
    "buffer_load_dwordx4 v5, s[44:47], s74 offen lds\n"
    "s_add_u32 m0, s59, 118784\n"
    "s_mov_b32 s74, 1179904\n"
    "buffer_load_dwordx4 v5, s[44:47], s74 offen lds\n"
    "v_add_u32 v16, 49152, v6\n"
    "v_add_u32 v17, 98304, v6\n"
    "v_add_u32 v18, 49152, v7\n"
    "v_add_u32 v19, 98304, v7\n"
    "s_waitcnt vmcnt(24) lgkmcnt(0)\n"
    "s_barrier\n"
    "s_add_u32 m0, s59, 122880\n"
    "s_mov_b32 s74, 1474560\n"
    "buffer_load_dwordx4 v5, s[40:43], s74 offen lds\n"
    "s_add_u32 m0, s59, 126976\n"
    "s_mov_b32 s74, 1474688\n"
    "buffer_load_dwordx4 v5, s[40:43], s74 offen lds\n"
    "s_add_u32 m0, s59, 131072\n"
    "s_mov_b32 s74, 1474816\n"
    "buffer_load_dwordx4 v5, s[40:43], s74 offen lds\n"
    "s_add_u32 m0, s59, 135168\n"
    "s_mov_b32 s74, 1474560\n"
    "buffer_load_dwordx4 v5, s[44:47], s74 offen lds\n"
    "s_add_u32 m0, s59, 139264\n"
    "s_mov_b32 s74, 1474688\n"
    "buffer_load_dwordx4 v5, s[44:47], s74 offen lds\n"
    "s_add_u32 m0, s59, 143360\n"
    "s_mov_b32 s74, 1474816\n"
    "buffer_load_dwordx4 v5, s[44:47], s74 offen lds\n"
    "ds_read_b64_tr_b16 a[0:1], v6 offset:0\n"
    "ds_read_b64_tr_b16 a[2:3], v6 offset:512\n"
    "ds_read_b64_tr_b16 a[4:5], v6 offset:8\n"
    "ds_read_b64_tr_b16 a[6:7], v6 offset:520\n"
    "ds_read_b64_tr_b16 a[8:9], v7 offset:0\n"
    "ds_read_b64_tr_b16 a[10:11], v7 offset:512\n"
    "ds_read_b64_tr_b16 a[12:13], v6 offset:12288\n"
    "ds_read_b64_tr_b16 a[14:15], v6 offset:12800\n"
    "ds_read_b64_tr_b16 a[16:17], v6 offset:12296\n"
    "ds_read_b64_tr_b16 a[18:19], v6 offset:12808\n"
    "ds_read_b64_tr_b16 a[20:21], v7 offset:12288\n"
    "ds_read_b64_tr_b16 a[22:23], v7 offset:12800\n"
    "s_waitcnt vmcnt(24) lgkmcnt(0)\n"
    "s_barrier\n"
    "s_add_u32 m0, s59, 0\n"
    "s_mov_b32 s74, 1769472\n"
    "buffer_load_dwordx4 v5, s[40:43], s74 offen lds\n"
    "s_add_u32 m0, s59, 4096\n"
    "s_mov_b32 s74, 1769600\n"
    "buffer_load_dwordx4 v5, s[40:43], s74 offen lds\n"
    "s_add_u32 m0, s59, 8192\n"
    "s_mov_b32 s74, 1769728\n"
    "buffer_load_dwordx4 v5, s[40:43], s74 offen lds\n"
    "s_add_u32 m0, s59, 12288\n"
    "s_mov_b32 s74, 1769472\n"
    "buffer_load_dwordx4 v5, s[44:47], s74 offen lds\n"
    "s_add_u32 m0, s59, 16384\n"
    "s_mov_b32 s74, 1769600\n"
    "buffer_load_dwordx4 v5, s[44:47], s74 offen lds\n"
    "s_add_u32 m0, s59, 20480\n"
    "s_mov_b32 s74, 1769728\n"
    "buffer_load_dwordx4 v5, s[44:47], s74 offen lds\n"
    "ds_read_b64_tr_b16 a[24:25], v6 offset:24576\n"
    "ds_read_b64_tr_b16 a[26:27], v6 offset:25088\n"
    "ds_read_b64_tr_b16 a[28:29], v6 offset:24584\n"
    "ds_read_b64_tr_b16 a[30:31], v6 offset:25096\n"
    "ds_read_b64_tr_b16 a[32:33], v7 offset:24576\n"
    "ds_read_b64_tr_b16 a[34:35], v7 offset:25088\n"
    "ds_read_b64_tr_b16 a[36:37], v6 offset:36864\n"
    "ds_read_b64_tr_b16 a[38:39], v6 offset:37376\n"
    "ds_read_b64_tr_b16 a[40:41], v6 offset:36872\n"
    "ds_read_b64_tr_b16 a[42:43], v6 offset:37384\n"
    "ds_read_b64_tr_b16 a[44:45], v7 offset:36864\n"
    "ds_read_b64_tr_b16 a[46:47], v7 offset:37376\n"
    "s_waitcnt vmcnt(24) lgkmcnt(0)\n"
    "s_barrier\n"
    "s_add_u32 m0, s59, 24576\n"
    "s_mov_b32 s74, 2064384\n"
    "buffer_load_dwordx4 v5, s[40:43], s74 offen lds\n"
    "s_add_u32 m0, s59, 28672\n"
    "s_mov_b32 s74, 2064512\n"
    "buffer_load_dwordx4 v5, s[40:43], s74 offen lds\n"
    "s_add_u32 m0, s59, 32768\n"
    "s_mov_b32 s74, 2064640\n"
    "buffer_load_dwordx4 v5, s[40:43], s74 offen lds\n"
    "s_add_u32 m0, s59, 36864\n"
    "s_mov_b32 s74, 2064384\n"
    "buffer_load_dwordx4 v5, s[44:47], s74 offen lds\n"
    "s_add_u32 m0, s59, 40960\n"
    "s_mov_b32 s74, 2064512\n"
    "buffer_load_dwordx4 v5, s[44:47], s74 offen lds\n"
    "s_add_u32 m0, s59, 45056\n"
    "s_mov_b32 s74, 2064640\n"
    "buffer_load_dwordx4 v5, s[44:47], s74 offen lds\n"
    "ds_read_b64_tr_b16 a[48:49], v16 offset:0\n"
    "ds_read_b64_tr_b16 a[50:51], v16 offset:512\n"
    "ds_read_b64_tr_b16 a[52:53], v16 offset:8\n"
    "ds_read_b64_tr_b16 a[54:55], v16 offset:520\n"
    "ds_read_b64_tr_b16 a[56:57], v18 offset:0\n"
    "ds_read_b64_tr_b16 a[58:59], v18 offset:512\n"
    "ds_read_b64_tr_b16 a[60:61], v16 offset:12288\n"
    "ds_read_b64_tr_b16 a[62:63], v16 offset:12800\n"
    "ds_read_b64_tr_b16 a[64:65], v16 offset:12296\n"
    "ds_read_b64_tr_b16 a[66:67], v16 offset:12808\n"
    "ds_read_b64_tr_b16 a[68:69], v18 offset:12288\n"
    "ds_read_b64_tr_b16 a[70:71], v18 offset:12800\n"
    "s_waitcnt vmcnt(24) lgkmcnt(0)\n"
    "s_barrier\n"
    "s_add_u32 m0, s59, 49152\n"
    "s_mov_b32 s74, 2359296\n"
    "buffer_load_dwordx4 v5, s[40:43], s74 offen lds\n"
    "s_add_u32 m0, s59, 53248\n"
    "s_mov_b32 s74, 2359424\n"
    "buffer_load_dwordx4 v5, s[40:43], s74 offen lds\n"
    "s_add_u32 m0, s59, 57344\n"
    "s_mov_b32 s74, 2359552\n"
    "buffer_load_dwordx4 v5, s[40:43], s74 offen lds\n"
    "s_add_u32 m0, s59, 61440\n"
    "s_mov_b32 s74, 2359296\n"
    "buffer_load_dwordx4 v5, s[44:47], s74 offen lds\n"
    "s_add_u32 m0, s59, 65536\n"
    "s_mov_b32 s74, 2359424\n"
    "buffer_load_dwordx4 v5, s[44:47], s74 offen lds\n"
    "s_add_u32 m0, s59, 69632\n"
    "s_mov_b32 s74, 2359552\n"
    "buffer_load_dwordx4 v5, s[44:47], s74 offen lds\n"
    "ds_read_b64_tr_b16 a[72:73], v16 offset:24576\n"
    "ds_read_b64_tr_b16 a[74:75], v16 offset:25088\n"
    "ds_read_b64_tr_b16 a[76:77], v16 offset:24584\n"
    "ds_read_b64_tr_b16 a[78:79], v16 offset:25096\n"
    "ds_read_b64_tr_b16 a[80:81], v18 offset:24576\n"
    "ds_read_b64_tr_b16 a[82:83], v18 offset:25088\n"
    "ds_read_b64_tr_b16 a[84:85], v16 offset:36864\n"
    "ds_read_b64_tr_b16 a[86:87], v16 offset:37376\n"
    "ds_read_b64_tr_b16 a[88:89], v16 offset:36872\n"
    "ds_read_b64_tr_b16 a[90:91], v16 offset:37384\n"
    "ds_read_b64_tr_b16 a[92:93], v18 offset:36864\n"
    "ds_read_b64_tr_b16 a[94:95], v18 offset:37376\n"
    "s_waitcnt vmcnt(24) lgkmcnt(0)\n"
    "s_barrier\n"
    "s_add_u32 m0, s59, 73728\n"
    "s_mov_b32 s74, 2654208\n"
    "buffer_load_dwordx4 v5, s[40:43], s74 offen lds\n"
    "s_add_u32 m0, s59, 77824\n"
    "s_mov_b32 s74, 2654336\n"
    "buffer_load_dwordx4 v5, s[40:43], s74 offen lds\n"
    "s_add_u32 m0, s59, 81920\n"
    "s_mov_b32 s74, 2654464\n"
    "buffer_load_dwordx4 v5, s[40:43], s74 offen lds\n"
    "s_add_u32 m0, s59, 86016\n"
    "s_mov_b32 s74, 2654208\n"
    "buffer_load_dwordx4 v5, s[44:47], s74 offen lds\n"
    "s_add_u32 m0, s59, 90112\n"
    "s_mov_b32 s74, 2654336\n"
    "buffer_load_dwordx4 v5, s[44:47], s74 offen lds\n"
    "s_add_u32 m0, s59, 94208\n"
    "s_mov_b32 s74, 2654464\n"
    "buffer_load_dwordx4 v5, s[44:47], s74 offen lds\n"
    "ds_read_b64_tr_b16 a[96:97], v17 offset:0\n"
    "ds_read_b64_tr_b16 a[98:99], v17 offset:512\n"
    "ds_read_b64_tr_b16 a[100:101], v17 offset:8\n"
    "ds_read_b64_tr_b16 a[102:103], v17 offset:520\n"
    "ds_read_b64_tr_b16 a[104:105], v19 offset:0\n"
    "ds_read_b64_tr_b16 a[106:107], v19 offset:512\n"
    "ds_read_b64_tr_b16 a[108:109], v17 offset:12288\n"
    "ds_read_b64_tr_b16 a[110:111], v17 offset:12800\n"
    "ds_read_b64_tr_b16 a[112:113], v17 offset:12296\n"
    "ds_read_b64_tr_b16 a[114:115], v17 offset:12808\n"
    "ds_read_b64_tr_b16 a[116:117], v19 offset:12288\n"
    "ds_read_b64_tr_b16 a[118:119], v19 offset:12800\n"
    "s_waitcnt vmcnt(24) lgkmcnt(0)\n"
    "s_barrier\n"
    "s_add_u32 m0, s59, 98304\n"
    "s_mov_b32 s74, 2949120\n"
    "buffer_load_dwordx4 v5, s[40:43], s74 offen lds\n"
    "s_add_u32 m0, s59, 102400\n"
    "s_mov_b32 s74, 2949248\n"
    "buffer_load_dwordx4 v5, s[40:43], s74 offen lds\n"
    "s_add_u32 m0, s59, 106496\n"
    "s_mov_b32 s74, 2949376\n"
    "buffer_load_dwordx4 v5, s[40:43], s74 offen lds\n"
    "s_add_u32 m0, s59, 110592\n"
    "s_mov_b32 s74, 2949120\n"
    "buffer_load_dwordx4 v5, s[44:47], s74 offen lds\n"
    "s_add_u32 m0, s59, 114688\n"
    "s_mov_b32 s74, 2949248\n"
    "buffer_load_dwordx4 v5, s[44:47], s74 offen lds\n"
    "s_add_u32 m0, s59, 118784\n"
    "s_mov_b32 s74, 2949376\n"
    "buffer_load_dwordx4 v5, s[44:47], s74 offen lds\n"
    "ds_read_b64_tr_b16 a[120:121], v17 offset:24576\n"
    "ds_read_b64_tr_b16 a[122:123], v17 offset:25088\n"
    "ds_read_b64_tr_b16 a[124:125], v17 offset:24584\n"
    "ds_read_b64_tr_b16 a[126:127], v17 offset:25096\n"
    "ds_read_b64_tr_b16 a[128:129], v19 offset:24576\n"
    "ds_read_b64_tr_b16 a[130:131], v19 offset:25088\n"
    "ds_read_b64_tr_b16 a[132:133], v17 offset:36864\n"
    "ds_read_b64_tr_b16 a[134:135], v17 offset:37376\n"
    "ds_read_b64_tr_b16 a[136:137], v17 offset:36872\n"
    "ds_read_b64_tr_b16 a[138:139], v17 offset:37384\n"
    "ds_read_b64_tr_b16 a[140:141], v19 offset:36864\n"
    "ds_read_b64_tr_b16 a[142:143], v19 offset:37376\n"
    "s_waitcnt vmcnt(24) lgkmcnt(0)\n"
    "s_barrier\n"
    "s_add_u32 m0, s59, 122880\n"
    "s_mov_b32 s74, 3244032\n"
    "buffer_load_dwordx4 v5, s[40:43], s74 offen lds\n"
    "s_add_u32 m0, s59, 126976\n"
    "s_mov_b32 s74, 3244160\n"
    "buffer_load_dwordx4 v5, s[40:43], s74 offen lds\n"
    "s_add_u32 m0, s59, 131072\n"
    "s_mov_b32 s74, 3244288\n"
    "buffer_load_dwordx4 v5, s[40:43], s74 offen lds\n"
    "s_add_u32 m0, s59, 135168\n"
    "s_mov_b32 s74, 3244032\n"
    "buffer_load_dwordx4 v5, s[44:47], s74 offen lds\n"
    "s_add_u32 m0, s59, 139264\n"
    "s_mov_b32 s74, 3244160\n"
    "buffer_load_dwordx4 v5, s[44:47], s74 offen lds\n"
    "s_add_u32 m0, s59, 143360\n"
    "s_mov_b32 s74, 3244288\n"
    "buffer_load_dwordx4 v5, s[44:47], s74 offen lds\n"
    "ds_read_b64_tr_b16 a[144:145], v6 offset:0\n"
    "ds_read_b64_tr_b16 a[146:147], v6 offset:512\n"
    "ds_read_b64_tr_b16 a[148:149], v6 offset:8\n"
    "ds_read_b64_tr_b16 a[150:151], v6 offset:520\n"
    "ds_read_b64_tr_b16 a[152:153], v7 offset:0\n"
    "ds_read_b64_tr_b16 a[154:155], v7 offset:512\n"
    "ds_read_b64_tr_b16 a[156:157], v6 offset:12288\n"
    "ds_read_b64_tr_b16 a[158:159], v6 offset:12800\n"
    "ds_read_b64_tr_b16 a[160:161], v6 offset:12296\n"
    "ds_read_b64_tr_b16 a[162:163], v6 offset:12808\n"
    "ds_read_b64_tr_b16 a[164:165], v7 offset:12288\n"
    "ds_read_b64_tr_b16 a[166:167], v7 offset:12800\n"
    "s_waitcnt vmcnt(24) lgkmcnt(0)\n"
    "s_barrier\n"
    "s_add_u32 m0, s59, 0\n"
    "s_mov_b32 s74, 3538944\n"
    "buffer_load_dwordx4 v5, s[40:43], s74 offen lds\n"
    "s_add_u32 m0, s59, 4096\n"
    "s_mov_b32 s74, 3539072\n"
    "buffer_load_dwordx4 v5, s[40:43], s74 offen lds\n"
    "s_add_u32 m0, s59, 8192\n"
    "s_mov_b32 s74, 3539200\n"
    "buffer_load_dwordx4 v5, s[40:43], s74 offen lds\n"
    "s_add_u32 m0, s59, 12288\n"
    "s_mov_b32 s74, 3538944\n"
    "buffer_load_dwordx4 v5, s[44:47], s74 offen lds\n"
    "s_add_u32 m0, s59, 16384\n"
    "s_mov_b32 s74, 3539072\n"
    "buffer_load_dwordx4 v5, s[44:47], s74 offen lds\n"
    "s_add_u32 m0, s59, 20480\n"
    "s_mov_b32 s74, 3539200\n"
    "buffer_load_dwordx4 v5, s[44:47], s74 offen lds\n"
    "ds_read_b64_tr_b16 a[168:169], v6 offset:24576\n"
    "ds_read_b64_tr_b16 a[170:171], v6 offset:25088\n"
    "ds_read_b64_tr_b16 a[172:173], v6 offset:24584\n"
    "ds_read_b64_tr_b16 a[174:175], v6 offset:25096\n"
    "ds_read_b64_tr_b16 a[176:177], v7 offset:24576\n"
    "ds_read_b64_tr_b16 a[178:179], v7 offset:25088\n"
    "ds_read_b64_tr_b16 a[180:181], v6 offset:36864\n"
    "ds_read_b64_tr_b16 a[182:183], v6 offset:37376\n"
    "ds_read_b64_tr_b16 a[184:185], v6 offset:36872\n"
    "ds_read_b64_tr_b16 a[186:187], v6 offset:37384\n"
    "ds_read_b64_tr_b16 a[188:189], v7 offset:36864\n"
    "ds_read_b64_tr_b16 a[190:191], v7 offset:37376\n"
    "s_waitcnt vmcnt(24) lgkmcnt(0)\n"
    "s_barrier\n"
    "s_add_u32 m0, s59, 24576\n"
    "s_mov_b32 s74, 3833856\n"
    "buffer_load_dwordx4 v5, s[40:43], s74 offen lds\n"
    "s_add_u32 m0, s59, 28672\n"
    "s_mov_b32 s74, 3833984\n"
    "buffer_load_dwordx4 v5, s[40:43], s74 offen lds\n"
    "s_add_u32 m0, s59, 32768\n"
    "s_mov_b32 s74, 3834112\n"
    "buffer_load_dwordx4 v5, s[40:43], s74 offen lds\n"
    "s_add_u32 m0, s59, 36864\n"
    "s_mov_b32 s74, 3833856\n"
    "buffer_load_dwordx4 v5, s[44:47], s74 offen lds\n"
    "s_add_u32 m0, s59, 40960\n"
    "s_mov_b32 s74, 3833984\n"
    "buffer_load_dwordx4 v5, s[44:47], s74 offen lds\n"
    "s_add_u32 m0, s59, 45056\n"
    "s_mov_b32 s74, 3834112\n"
    "buffer_load_dwordx4 v5, s[44:47], s74 offen lds\n"
    "ds_read_b64_tr_b16 a[192:193], v16 offset:0\n"
    "ds_read_b64_tr_b16 a[194:195], v16 offset:512\n"
    "ds_read_b64_tr_b16 a[196:197], v16 offset:8\n"
    "ds_read_b64_tr_b16 a[198:199], v16 offset:520\n"
    "ds_read_b64_tr_b16 a[200:201], v18 offset:0\n"
    "ds_read_b64_tr_b16 a[202:203], v18 offset:512\n"
    "ds_read_b64_tr_b16 a[204:205], v16 offset:12288\n"
    "ds_read_b64_tr_b16 a[206:207], v16 offset:12800\n"
    "ds_read_b64_tr_b16 a[208:209], v16 offset:12296\n"
    "ds_read_b64_tr_b16 a[210:211], v16 offset:12808\n"
    "ds_read_b64_tr_b16 a[212:213], v18 offset:12288\n"
    "ds_read_b64_tr_b16 a[214:215], v18 offset:12800\n"
    "s_waitcnt vmcnt(24) lgkmcnt(0)\n"
    "s_barrier\n"
    "s_add_u32 m0, s59, 49152\n"
    "s_mov_b32 s74, 4128768\n"
    "buffer_load_dwordx4 v5, s[40:43], s74 offen lds\n"
    "s_add_u32 m0, s59, 53248\n"
    "s_mov_b32 s74, 4128896\n"
    "buffer_load_dwordx4 v5, s[40:43], s74 offen lds\n"
    "s_add_u32 m0, s59, 57344\n"
    "s_mov_b32 s74, 4129024\n"
    "buffer_load_dwordx4 v5, s[40:43], s74 offen lds\n"
    "s_add_u32 m0, s59, 61440\n"
    "s_mov_b32 s74, 4128768\n"
    "buffer_load_dwordx4 v5, s[44:47], s74 offen lds\n"
    "s_add_u32 m0, s59, 65536\n"
    "s_mov_b32 s74, 4128896\n"
    "buffer_load_dwordx4 v5, s[44:47], s74 offen lds\n"
    "s_add_u32 m0, s59, 69632\n"
    "s_mov_b32 s74, 4129024\n"
    "buffer_load_dwordx4 v5, s[44:47], s74 offen lds\n"
    "ds_read_b64_tr_b16 a[216:217], v16 offset:24576\n"
    "ds_read_b64_tr_b16 a[218:219], v16 offset:25088\n"
    "ds_read_b64_tr_b16 a[220:221], v16 offset:24584\n"
    "ds_read_b64_tr_b16 a[222:223], v16 offset:25096\n"
    "ds_read_b64_tr_b16 a[224:225], v18 offset:24576\n"
    "ds_read_b64_tr_b16 a[226:227], v18 offset:25088\n"
    "ds_read_b64_tr_b16 a[228:229], v16 offset:36864\n"
    "ds_read_b64_tr_b16 a[230:231], v16 offset:37376\n"
    "ds_read_b64_tr_b16 a[232:233], v16 offset:36872\n"
    "ds_read_b64_tr_b16 a[234:235], v16 offset:37384\n"
    "ds_read_b64_tr_b16 a[236:237], v18 offset:36864\n"
    "ds_read_b64_tr_b16 a[238:239], v18 offset:37376\n"
    "s_waitcnt vmcnt(24) lgkmcnt(0)\n"
    "s_barrier\n"
    "s_add_u32 m0, s59, 73728\n"
    "s_mov_b32 s74, 4423680\n"
    "buffer_load_dwordx4 v5, s[40:43], s74 offen lds\n"
    "s_add_u32 m0, s59, 77824\n"
    "s_mov_b32 s74, 4423808\n"
    "buffer_load_dwordx4 v5, s[40:43], s74 offen lds\n"
    "s_add_u32 m0, s59, 81920\n"
    "s_mov_b32 s74, 4423936\n"
    "buffer_load_dwordx4 v5, s[40:43], s74 offen lds\n"
    "s_add_u32 m0, s59, 86016\n"
    "s_mov_b32 s74, 4423680\n"
    "buffer_load_dwordx4 v5, s[44:47], s74 offen lds\n"
    "s_add_u32 m0, s59, 90112\n"
    "s_mov_b32 s74, 4423808\n"
    "buffer_load_dwordx4 v5, s[44:47], s74 offen lds\n"
    "s_add_u32 m0, s59, 94208\n"
    "s_mov_b32 s74, 4423936\n"
    "buffer_load_dwordx4 v5, s[44:47], s74 offen lds\n"
    "ds_read_b64_tr_b16 a[240:241], v17 offset:0\n"
    "ds_read_b64_tr_b16 a[242:243], v17 offset:512\n"
    "ds_read_b64_tr_b16 a[244:245], v17 offset:8\n"
    "ds_read_b64_tr_b16 a[246:247], v17 offset:520\n"
    "ds_read_b64_tr_b16 a[248:249], v19 offset:0\n"
    "ds_read_b64_tr_b16 a[250:251], v19 offset:512\n"
    "ds_read_b64_tr_b16 a[252:253], v17 offset:12288\n"
    "ds_read_b64_tr_b16 a[254:255], v17 offset:12800\n"
    "ds_read_b64_tr_b16 v[80:81], v17 offset:12296\n"
    "ds_read_b64_tr_b16 v[82:83], v17 offset:12808\n"
    "ds_read_b64_tr_b16 v[84:85], v19 offset:12288\n"
    "ds_read_b64_tr_b16 v[86:87], v19 offset:12800\n"
    "s_waitcnt vmcnt(24) lgkmcnt(0)\n"
    "s_barrier\n"
    "s_add_u32 m0, s59, 98304\n"
    "s_mov_b32 s74, 4718592\n"
    "buffer_load_dwordx4 v5, s[40:43], s74 offen lds\n"
    "s_add_u32 m0, s59, 102400\n"
    "s_mov_b32 s74, 4718720\n"
    "buffer_load_dwordx4 v5, s[40:43], s74 offen lds\n"
    "s_add_u32 m0, s59, 106496\n"
    "s_mov_b32 s74, 4718848\n"
    "buffer_load_dwordx4 v5, s[40:43], s74 offen lds\n"
    "s_add_u32 m0, s59, 110592\n"
    "s_mov_b32 s74, 4718592\n"
    "buffer_load_dwordx4 v5, s[44:47], s74 offen lds\n"
    "s_add_u32 m0, s59, 114688\n"
    "s_mov_b32 s74, 4718720\n"
    "buffer_load_dwordx4 v5, s[44:47], s74 offen lds\n"
    "s_add_u32 m0, s59, 118784\n"
    "s_mov_b32 s74, 4718848\n"
    "buffer_load_dwordx4 v5, s[44:47], s74 offen lds\n"
    "ds_read_b64_tr_b16 v[88:89], v17 offset:24576\n"
    "ds_read_b64_tr_b16 v[90:91], v17 offset:25088\n"
    "ds_read_b64_tr_b16 v[92:93], v17 offset:24584\n"
    "ds_read_b64_tr_b16 v[94:95], v17 offset:25096\n"
    "ds_read_b64_tr_b16 v[96:97], v19 offset:24576\n"
    "ds_read_b64_tr_b16 v[98:99], v19 offset:25088\n"
    "ds_read_b64_tr_b16 v[100:101], v17 offset:36864\n"
    "ds_read_b64_tr_b16 v[102:103], v17 offset:37376\n"
    "ds_read_b64_tr_b16 v[104:105], v17 offset:36872\n"
    "ds_read_b64_tr_b16 v[106:107], v17 offset:37384\n"
    "ds_read_b64_tr_b16 v[108:109], v19 offset:36864\n"
    "ds_read_b64_tr_b16 v[110:111], v19 offset:37376\n"
    "s_waitcnt vmcnt(24) lgkmcnt(0)\n"
    "s_barrier\n"
    "s_add_u32 m0, s59, 122880\n"
    "s_mov_b32 s74, 5013504\n"
    "buffer_load_dwordx4 v5, s[40:43], s74 offen lds\n"
    "s_add_u32 m0, s59, 126976\n"
    "s_mov_b32 s74, 5013632\n"
    "buffer_load_dwordx4 v5, s[40:43], s74 offen lds\n"
    "s_add_u32 m0, s59, 131072\n"
    "s_mov_b32 s74, 5013760\n"
    "buffer_load_dwordx4 v5, s[40:43], s74 offen lds\n"
    "s_add_u32 m0, s59, 135168\n"
    "s_mov_b32 s74, 5013504\n"
    "buffer_load_dwordx4 v5, s[44:47], s74 offen lds\n"
    "s_add_u32 m0, s59, 139264\n"
    "s_mov_b32 s74, 5013632\n"
    "buffer_load_dwordx4 v5, s[44:47], s74 offen lds\n"
    "s_add_u32 m0, s59, 143360\n"
    "s_mov_b32 s74, 5013760\n"
    "buffer_load_dwordx4 v5, s[44:47], s74 offen lds\n"
    "ds_read_b64_tr_b16 v[112:113], v6 offset:0\n"
    "ds_read_b64_tr_b16 v[114:115], v6 offset:512\n"
    "ds_read_b64_tr_b16 v[116:117], v6 offset:8\n"
    "ds_read_b64_tr_b16 v[118:119], v6 offset:520\n"
    "ds_read_b64_tr_b16 v[120:121], v7 offset:0\n"
    "ds_read_b64_tr_b16 v[122:123], v7 offset:512\n"
    "ds_read_b64_tr_b16 v[124:125], v6 offset:12288\n"
    "ds_read_b64_tr_b16 v[126:127], v6 offset:12800\n"
    "ds_read_b64_tr_b16 v[128:129], v6 offset:12296\n"
    "ds_read_b64_tr_b16 v[130:131], v6 offset:12808\n"
    "ds_read_b64_tr_b16 v[132:133], v7 offset:12288\n"
    "ds_read_b64_tr_b16 v[134:135], v7 offset:12800\n"
    "s_waitcnt vmcnt(24) lgkmcnt(0)\n"
    "s_barrier\n"
    "ds_read_b64_tr_b16 v[136:137], v6 offset:24576\n"
    "ds_read_b64_tr_b16 v[138:139], v6 offset:25088\n"
    "ds_read_b64_tr_b16 v[140:141], v6 offset:24584\n"
    "ds_read_b64_tr_b16 v[142:143], v6 offset:25096\n"
    "ds_read_b64_tr_b16 v[144:145], v7 offset:24576\n"
    "ds_read_b64_tr_b16 v[146:147], v7 offset:25088\n"
    "ds_read_b64_tr_b16 v[148:149], v6 offset:36864\n"
    "ds_read_b64_tr_b16 v[150:151], v6 offset:37376\n"
    "ds_read_b64_tr_b16 v[152:153], v6 offset:36872\n"
    "ds_read_b64_tr_b16 v[154:155], v6 offset:37384\n"
    "ds_read_b64_tr_b16 v[156:157], v7 offset:36864\n"
    "ds_read_b64_tr_b16 v[158:159], v7 offset:37376\n"
    "s_waitcnt vmcnt(18) lgkmcnt(0)\n"
    "s_barrier\n"
    "ds_read_b64_tr_b16 v[160:161], v16 offset:0\n"
    "ds_read_b64_tr_b16 v[162:163], v16 offset:512\n"
    "ds_read_b64_tr_b16 v[164:165], v16 offset:8\n"
    "ds_read_b64_tr_b16 v[166:167], v16 offset:520\n"
    "ds_read_b64_tr_b16 v[168:169], v18 offset:0\n"
    "ds_read_b64_tr_b16 v[170:171], v18 offset:512\n"
    "ds_read_b64_tr_b16 v[172:173], v16 offset:12288\n"
    "ds_read_b64_tr_b16 v[174:175], v16 offset:12800\n"
    "ds_read_b64_tr_b16 v[176:177], v16 offset:12296\n"
    "ds_read_b64_tr_b16 v[178:179], v16 offset:12808\n"
    "ds_read_b64_tr_b16 v[180:181], v18 offset:12288\n"
    "ds_read_b64_tr_b16 v[182:183], v18 offset:12800\n"
    "s_waitcnt vmcnt(12) lgkmcnt(0)\n"
    "s_barrier\n"
    "ds_read_b64_tr_b16 v[184:185], v16 offset:24576\n"
    "ds_read_b64_tr_b16 v[186:187], v16 offset:25088\n"
    "ds_read_b64_tr_b16 v[188:189], v16 offset:24584\n"
    "ds_read_b64_tr_b16 v[190:191], v16 offset:25096\n"
    "ds_read_b64_tr_b16 v[192:193], v18 offset:24576\n"
    "ds_read_b64_tr_b16 v[194:195], v18 offset:25088\n"
    "ds_read_b64_tr_b16 v[196:197], v16 offset:36864\n"
    "ds_read_b64_tr_b16 v[198:199], v16 offset:37376\n"
    "ds_read_b64_tr_b16 v[200:201], v16 offset:36872\n"
    "ds_read_b64_tr_b16 v[202:203], v16 offset:37384\n"
    "ds_read_b64_tr_b16 v[204:205], v18 offset:36864\n"
    "ds_read_b64_tr_b16 v[206:207], v18 offset:37376\n"
    "s_waitcnt vmcnt(6) lgkmcnt(0)\n"
    "s_barrier\n"
    "ds_read_b64_tr_b16 v[208:209], v17 offset:0\n"
    "ds_read_b64_tr_b16 v[210:211], v17 offset:512\n"
    "ds_read_b64_tr_b16 v[212:213], v17 offset:8\n"
    "ds_read_b64_tr_b16 v[214:215], v17 offset:520\n"
    "ds_read_b64_tr_b16 v[216:217], v19 offset:0\n"
    "ds_read_b64_tr_b16 v[218:219], v19 offset:512\n"
    "ds_read_b64_tr_b16 v[220:221], v17 offset:12288\n"
    "ds_read_b64_tr_b16 v[222:223], v17 offset:12800\n"
    "ds_read_b64_tr_b16 v[224:225], v17 offset:12296\n"
    "ds_read_b64_tr_b16 v[226:227], v17 offset:12808\n"
    "ds_read_b64_tr_b16 v[228:229], v19 offset:12288\n"
    "ds_read_b64_tr_b16 v[230:231], v19 offset:12800\n"
    "s_waitcnt vmcnt(0) lgkmcnt(0)\n"
    "s_barrier\n"
    "ds_read_b64_tr_b16 v[232:233], v17 offset:24576\n"
    "ds_read_b64_tr_b16 v[234:235], v17 offset:25088\n"
    "ds_read_b64_tr_b16 v[236:237], v17 offset:24584\n"
    "ds_read_b64_tr_b16 v[238:239], v17 offset:25096\n"
    "ds_read_b64_tr_b16 v[240:241], v19 offset:24576\n"
    "ds_read_b64_tr_b16 v[242:243], v19 offset:25088\n"
    "ds_read_b64_tr_b16 v[244:245], v17 offset:36864\n"
    "ds_read_b64_tr_b16 v[246:247], v17 offset:37376\n"
    "ds_read_b64_tr_b16 v[248:249], v17 offset:36872\n"
    "ds_read_b64_tr_b16 v[250:251], v17 offset:37384\n"
    "ds_read_b64_tr_b16 v[252:253], v19 offset:36864\n"
    "ds_read_b64_tr_b16 v[254:255], v19 offset:37376\n"
    "s_waitcnt lgkmcnt(0)\n"
    "s_barrier\n"
    "s_mov_b32 s68, 0\n"
    "s_mov_b32 s71, 0\n"
    "s_mov_b32 s78, 65536\n"
    "v_add_u32 v9, s78, v0\n"
    "s_mov_b32 s64, 0xbfb8aa3b\n"
    "s_mov_b32 s72, 262144\n"
    ".LgUP_par_%=:\n"
    "buffer_load_dword v12, off, s[36:39], 0 sc1\n"
    "s_waitcnt vmcnt(0)\n"
    "v_readfirstlane_b32 s74, v12\n"
    "s_nop 0\n"
    "s_cmp_ge_i32 s74, 0\n"
    "s_cbranch_scc1 .LgUP_par1_%=\n"
    "s_sleep 4\n"
    "s_sub_u32 s72, s72, 1\n"
    "s_cbranch_scc0 .LgUP_par_%=\n"
    "s_mov_b32 s72, 0\n"
    ".LgUP_par1_%=:\n"
    "s_and_b32 s74, s74, 1\n"
    "s_lshl_b32 s74, s74, 13\n"
    "s_add_u32 s36, s36, s74\n"
    "s_addc_u32 s37, s37, 0\n"
    "s_branch .LgUP_block_%=\n"
    ".LgUP_LOOPB_%=:\n"
    ".LgUP_head_%=:\n"
    "v_mfma_f32_16x16x32_bf16 v[48:51], a[8:11], v[16:19], 0\n"
    "s_waitcnt lgkmcnt(2)\n"
    "s_sub_u32 s74, s68, 1\n"
    "v_mfma_f32_16x16x32_bf16 v[72:75], a[20:23], v[16:19], 0\n"
    "ds_read_b128 v[16:19], v9 offset:4096\n"
    "v_mul_f32 v64, v40, v64\n"
    "v_mfma_f32_16x16x32_bf16 v[52:55], a[8:11], v[20:23], 0\n"
    "s_waitcnt lgkmcnt(2)\n"
    "v_mul_f32 v65, v41, v65\n"
    "v_mul_f32 v66, v42, v66\n"
    "v_mfma_f32_16x16x32_bf16 v[76:79], a[20:23], v[20:23], 0\n"
    "ds_read_b128 v[20:23], v9 offset:5120\n"
    "v_mul_f32 v67, v43, v67\n"
    "v_mfma_f32_16x16x32_bf16 v[48:51], a[32:35], v[24:27], v[48:51]\n"
    "s_waitcnt lgkmcnt(2)\n"
    "v_mul_f32 v68, v44, v68\n"
    "v_mul_f32 v69, v45, v69\n"
    "v_mfma_f32_16x16x32_bf16 v[72:75], a[44:47], v[24:27], v[72:75]\n"
    "ds_read_b128 v[24:27], v9 offset:6144\n"
    "v_mul_f32 v70, v46, v70\n"
    "v_mfma_f32_16x16x32_bf16 v[52:55], a[32:35], v[28:31], v[52:55]\n"
    "s_waitcnt lgkmcnt(2)\n"
    "v_mul_f32 v71, v47, v71\n"
    "v_mul_f32 v40, 0xbfb8aa3b, v40\n"
    "v_mfma_f32_16x16x32_bf16 v[76:79], a[44:47], v[28:31], v[76:79]\n"
    "ds_read_b128 v[28:31], v9 offset:7168\n"
    "v_mul_f32 v41, 0xbfb8aa3b, v41\n"
    "v_mfma_f32_16x16x32_bf16 v[48:51], a[56:59], v[16:19], v[48:51]\n"
    "s_waitcnt lgkmcnt(2)\n"
    "v_exp_f32 v40, v40\n"
    "v_mfma_f32_16x16x32_bf16 v[72:75], a[68:71], v[16:19], v[72:75]\n"
    "ds_read_b128 v[16:19], v9 offset:8192\n"
    "v_mul_f32 v42, 0xbfb8aa3b, v42\n"
    "v_mfma_f32_16x16x32_bf16 v[52:55], a[56:59], v[20:23], v[52:55]\n"
    "s_waitcnt lgkmcnt(2)\n"
    "v_exp_f32 v41, v41\n"
    "v_mfma_f32_16x16x32_bf16 v[76:79], a[68:71], v[20:23], v[76:79]\n"
    "ds_read_b128 v[20:23], v9 offset:9216\n"
    "v_mul_f32 v43, 0xbfb8aa3b, v43\n"
    "v_mfma_f32_16x16x32_bf16 v[48:51], a[80:83], v[24:27], v[48:51]\n"
    "s_waitcnt lgkmcnt(2)\n"
    "v_exp_f32 v42, v42\n"
    "v_mfma_f32_16x16x32_bf16 v[72:75], a[92:95], v[24:27], v[72:75]\n"
    "ds_read_b128 v[24:27], v9 offset:10240\n"
    "v_mul_f32 v44, 0xbfb8aa3b, v44\n"
    "v_mfma_f32_16x16x32_bf16 v[52:55], a[80:83], v[28:31], v[52:55]\n"
    "s_waitcnt lgkmcnt(2)\n"
    "v_exp_f32 v43, v43\n"
    "v_mfma_f32_16x16x32_bf16 v[76:79], a[92:95], v[28:31], v[76:79]\n"
    "ds_read_b128 v[28:31], v9 offset:11264\n"
    "v_mul_f32 v45, 0xbfb8aa3b, v45\n"
    "v_mfma_f32_16x16x32_bf16 v[48:51], a[104:107], v[16:19], v[48:51]\n"
    "s_waitcnt lgkmcnt(2)\n"
    "v_exp_f32 v44, v44\n"
    "v_mfma_f32_16x16x32_bf16 v[72:75], a[116:119], v[16:19], v[72:75]\n"
    "ds_read_b128 v[16:19], v9 offset:12288\n"
    "v_mul_f32 v46, 0xbfb8aa3b, v46\n"
    "v_mfma_f32_16x16x32_bf16 v[52:55], a[104:107], v[20:23], v[52:55]\n"
    "s_waitcnt lgkmcnt(2)\n"
    "v_exp_f32 v45, v45\n"
    "v_mfma_f32_16x16x32_bf16 v[76:79], a[116:119], v[20:23], v[76:79]\n"
    "ds_read_b128 v[20:23], v9 offset:13312\n"
    "v_mul_f32 v47, 0xbfb8aa3b, v47\n"
    "v_mfma_f32_16x16x32_bf16 v[48:51], a[128:131], v[24:27], v[48:51]\n"
    "s_waitcnt lgkmcnt(2)\n"
    "v_exp_f32 v46, v46\n"
    "v_mfma_f32_16x16x32_bf16 v[72:75], a[140:143], v[24:27], v[72:75]\n"
    "ds_read_b128 v[24:27], v9 offset:14336\n"
    "v_add_f32 v40, 1.0, v40\n"
    "v_mfma_f32_16x16x32_bf16 v[52:55], a[128:131], v[28:31], v[52:55]\n"
    "s_waitcnt lgkmcnt(2)\n"
    "v_exp_f32 v47, v47\n"
    "v_mfma_f32_16x16x32_bf16 v[76:79], a[140:143], v[28:31], v[76:79]\n"
    "ds_read_b128 v[28:31], v9 offset:15360\n"
    "v_add_f32 v41, 1.0, v41\n"
    "v_mfma_f32_16x16x32_bf16 v[48:51], a[152:155], v[16:19], v[48:51]\n"
    "s_waitcnt lgkmcnt(2)\n"
    "v_rcp_f32 v40, v40\n"
    "v_mfma_f32_16x16x32_bf16 v[72:75], a[164:167], v[16:19], v[72:75]\n"
    "ds_read_b128 v[16:19], v9 offset:16384\n"
    "v_add_f32 v42, 1.0, v42\n"
    "v_mfma_f32_16x16x32_bf16 v[52:55], a[152:155], v[20:23], v[52:55]\n"
    "s_waitcnt lgkmcnt(2)\n"
    "v_rcp_f32 v41, v41\n"
    "v_mfma_f32_16x16x32_bf16 v[76:79], a[164:167], v[20:23], v[76:79]\n"
    "ds_read_b128 v[20:23], v9 offset:17408\n"
    "v_add_f32 v43, 1.0, v43\n"
    "v_mfma_f32_16x16x32_bf16 v[48:51], a[176:179], v[24:27], v[48:51]\n"
    "s_waitcnt lgkmcnt(2)\n"
    "v_rcp_f32 v42, v42\n"
    "v_mfma_f32_16x16x32_bf16 v[72:75], a[188:191], v[24:27], v[72:75]\n"
    "ds_read_b128 v[24:27], v9 offset:18432\n"
    "v_add_f32 v44, 1.0, v44\n"
    "v_mfma_f32_16x16x32_bf16 v[52:55], a[176:179], v[28:31], v[52:55]\n"
    "s_waitcnt lgkmcnt(2)\n"
    "v_rcp_f32 v43, v43\n"
    "v_mfma_f32_16x16x32_bf16 v[76:79], a[188:191], v[28:31], v[76:79]\n"
    "ds_read_b128 v[28:31], v9 offset:19456\n"
    "v_add_f32 v45, 1.0, v45\n"
    "v_mfma_f32_16x16x32_bf16 v[48:51], a[200:203], v[16:19], v[48:51]\n"
    "s_waitcnt lgkmcnt(2)\n"
    "v_rcp_f32 v44, v44\n"
    "v_mfma_f32_16x16x32_bf16 v[72:75], a[212:215], v[16:19], v[72:75]\n"
    "ds_read_b128 v[16:19], v9 offset:20480\n"
    "v_add_f32 v46, 1.0, v46\n"
    "v_mfma_f32_16x16x32_bf16 v[52:55], a[200:203], v[20:23], v[52:55]\n"
    "s_waitcnt lgkmcnt(2)\n"
    "v_rcp_f32 v45, v45\n"
    "v_mfma_f32_16x16x32_bf16 v[76:79], a[212:215], v[20:23], v[76:79]\n"
    "ds_read_b128 v[20:23], v9 offset:21504\n"
    "v_add_f32 v47, 1.0, v47\n"
    "v_mfma_f32_16x16x32_bf16 v[48:51], a[224:227], v[24:27], v[48:51]\n"
    "s_waitcnt lgkmcnt(2)\n"
    "v_rcp_f32 v46, v46\n"
    "v_mfma_f32_16x16x32_bf16 v[72:75], a[236:239], v[24:27], v[72:75]\n"
    "ds_read_b128 v[24:27], v9 offset:22528\n"
    "v_mul_f32 v40, v40, v64\n"
    "v_mfma_f32_16x16x32_bf16 v[52:55], a[224:227], v[28:31], v[52:55]\n"
    "s_waitcnt lgkmcnt(2)\n"
    "v_rcp_f32 v47, v47\n"
    "v_mfma_f32_16x16x32_bf16 v[76:79], a[236:239], v[28:31], v[76:79]\n"
    "ds_read_b128 v[28:31], v9 offset:23552\n"
    "v_mul_f32 v41, v41, v65\n"
    "v_mfma_f32_16x16x32_bf16 v[48:51], a[248:251], v[16:19], v[48:51]\n"
    "s_waitcnt lgkmcnt(2)\n"
    "v_mul_f32 v42, v42, v66\n"
    "v_mul_f32 v43, v43, v67\n"
    "v_mfma_f32_16x16x32_bf16 v[72:75], v[84:87], v[16:19], v[72:75]\n"
    "ds_read_b128 v[16:19], v9 offset:24576\n"
    "v_mul_f32 v44, v44, v68\n"
    "v_mfma_f32_16x16x32_bf16 v[52:55], a[248:251], v[20:23], v[52:55]\n"
    "s_waitcnt lgkmcnt(2)\n"
    "v_mul_f32 v45, v45, v69\n"
    "v_mul_f32 v46, v46, v70\n"
    "v_mfma_f32_16x16x32_bf16 v[76:79], v[84:87], v[20:23], v[76:79]\n"
    "ds_read_b128 v[20:23], v9 offset:25600\n"
    "v_mul_f32 v47, v47, v71\n"
    "v_mfma_f32_16x16x32_bf16 v[48:51], v[96:99], v[24:27], v[48:51]\n"
    "s_waitcnt lgkmcnt(2)\n"
    "v_cvt_pk_bf16_f32 v40, v40, v41\n"
    "v_cvt_pk_bf16_f32 v41, v42, v43\n"
    "v_mfma_f32_16x16x32_bf16 v[72:75], v[108:111], v[24:27], v[72:75]\n"
    "ds_read_b128 v[24:27], v9 offset:26624\n"
    "v_cvt_pk_bf16_f32 v42, v44, v45\n"
    "v_mfma_f32_16x16x32_bf16 v[52:55], v[96:99], v[28:31], v[52:55]\n"
    "s_waitcnt lgkmcnt(2)\n"
    "v_cvt_pk_bf16_f32 v43, v46, v47\n"
    "v_mfma_f32_16x16x32_bf16 v[76:79], v[108:111], v[28:31], v[76:79]\n"
    "ds_read_b128 v[28:31], v9 offset:27648\n"
    "v_mfma_f32_16x16x32_bf16 v[48:51], v[120:123], v[16:19], v[48:51]\n"
    "s_waitcnt lgkmcnt(2)\n"
    "s_and_b32 s76, s71, s54\n"
    "buffer_store_dwordx4 v[40:43], v15, s[36:39], s73 offen offset:1024 sc1\n"
    "v_mfma_f32_16x16x32_bf16 v[72:75], v[132:135], v[16:19], v[72:75]\n"
    "ds_read_b128 v[16:19], v9 offset:28672\n"
    "v_mfma_f32_16x16x32_bf16 v[52:55], v[120:123], v[20:23], v[52:55]\n"
    "s_waitcnt lgkmcnt(2)\n"
    "s_mul_i32 s76, s76, s53\n"
    "v_mfma_f32_16x16x32_bf16 v[76:79], v[132:135], v[20:23], v[76:79]\n"
    "ds_read_b128 v[20:23], v9 offset:29696\n"
    "v_mfma_f32_16x16x32_bf16 v[48:51], v[144:147], v[24:27], v[48:51]\n"
    "s_waitcnt lgkmcnt(2)\n"
    "s_add_u32 s76, s76, s52\n"
    "v_mfma_f32_16x16x32_bf16 v[72:75], v[156:159], v[24:27], v[72:75]\n"
    "ds_read_b128 v[24:27], v9 offset:30720\n"
    "v_mfma_f32_16x16x32_bf16 v[52:55], v[144:147], v[28:31], v[52:55]\n"
    "s_waitcnt lgkmcnt(2)\n"
    "s_and_b32 s77, s71, 1\n"
    "v_mfma_f32_16x16x32_bf16 v[76:79], v[156:159], v[28:31], v[76:79]\n"
    "ds_read_b128 v[28:31], v9 offset:31744\n"
    "v_mfma_f32_16x16x32_bf16 v[48:51], v[168:171], v[16:19], v[48:51]\n"
    "s_waitcnt lgkmcnt(2)\n"
    "s_mul_i32 s77, s77, 65536\n"
    "v_mfma_f32_16x16x32_bf16 v[72:75], v[180:183], v[16:19], v[72:75]\n"
    "ds_read_b128 v[16:19], v9 offset:32768\n"
    "v_mfma_f32_16x16x32_bf16 v[52:55], v[168:171], v[20:23], v[52:55]\n"
    "s_waitcnt lgkmcnt(2)\n"
    "s_add_u32 s77, s77, s59\n"
    "v_mfma_f32_16x16x32_bf16 v[76:79], v[180:183], v[20:23], v[76:79]\n"
    "ds_read_b128 v[20:23], v9 offset:33792\n"
    "v_mfma_f32_16x16x32_bf16 v[48:51], v[192:195], v[24:27], v[48:51]\n"
    "s_waitcnt lgkmcnt(2)\n"
    "v_mfma_f32_16x16x32_bf16 v[72:75], v[204:207], v[24:27], v[72:75]\n"
    "ds_read_b128 v[24:27], v9 offset:34816\n"
    "v_mfma_f32_16x16x32_bf16 v[52:55], v[192:195], v[28:31], v[52:55]\n"
    "s_waitcnt lgkmcnt(2)\n"
    "v_mfma_f32_16x16x32_bf16 v[76:79], v[204:207], v[28:31], v[76:79]\n"
    "ds_read_b128 v[28:31], v9 offset:35840\n"
    "v_mfma_f32_16x16x32_bf16 v[48:51], v[216:219], v[16:19], v[48:51]\n"
    "s_waitcnt lgkmcnt(2)\n"
    "v_mfma_f32_16x16x32_bf16 v[72:75], v[228:231], v[16:19], v[72:75]\n"
    "ds_read_b128 v[16:19], v9 offset:0\n"
    "v_mfma_f32_16x16x32_bf16 v[52:55], v[216:219], v[20:23], v[52:55]\n"
    "s_waitcnt lgkmcnt(2)\n"
    "v_mfma_f32_16x16x32_bf16 v[76:79], v[228:231], v[20:23], v[76:79]\n"
    "ds_read_b128 v[20:23], v9 offset:2048\n"
    "v_mfma_f32_16x16x32_bf16 v[48:51], v[240:243], v[24:27], v[48:51]\n"
    "s_waitcnt lgkmcnt(2)\n"
    "v_mfma_f32_16x16x32_bf16 v[72:75], v[252:255], v[24:27], v[72:75]\n"
    "ds_read_b128 v[24:27], v9 offset:4096\n"
    "v_mfma_f32_16x16x32_bf16 v[52:55], v[240:243], v[28:31], v[52:55]\n"
    "s_waitcnt lgkmcnt(2)\n"
    "v_mfma_f32_16x16x32_bf16 v[76:79], v[252:255], v[28:31], v[76:79]\n"
    "ds_read_b128 v[28:31], v9 offset:6144\n"
    "v_mfma_f32_16x16x32_bf16 v[32:35], a[0:3], v[16:19], 0\n"
    "v_mfma_f32_16x16x32_bf16 v[36:39], a[4:7], v[16:19], 0\n"
    "v_mul_f32 v72, v48, v72\n"
    "v_mul_f32 v73, v49, v73\n"
    "v_mfma_f32_16x16x32_bf16 v[56:59], a[12:15], v[16:19], 0\n"
    "s_waitcnt lgkmcnt(2)\n"
    "v_mul_f32 v74, v50, v74\n"
    "v_mul_f32 v75, v51, v75\n"
    "v_mfma_f32_16x16x32_bf16 v[60:63], a[16:19], v[16:19], 0\n"
    "ds_read_b128 v[16:19], v9 offset:8192\n"
    "v_mul_f32 v76, v52, v76\n"
    "v_mfma_f32_16x16x32_bf16 v[32:35], a[24:27], v[20:23], v[32:35]\n"
    "v_mul_f32 v77, v53, v77\n"
    "v_mul_f32 v78, v54, v78\n"
    "v_mfma_f32_16x16x32_bf16 v[36:39], a[28:31], v[20:23], v[36:39]\n"
    "v_mul_f32 v79, v55, v79\n"
    "v_mul_f32 v48, 0xbfb8aa3b, v48\n"
    "v_mfma_f32_16x16x32_bf16 v[56:59], a[36:39], v[20:23], v[56:59]\n"
    "s_waitcnt lgkmcnt(2)\n"
    "v_exp_f32 v48, v48\n"
    "v_mfma_f32_16x16x32_bf16 v[60:63], a[40:43], v[20:23], v[60:63]\n"
    "ds_read_b128 v[20:23], v9 offset:10240\n"
    "v_mul_f32 v49, 0xbfb8aa3b, v49\n"
    "v_mfma_f32_16x16x32_bf16 v[32:35], a[48:51], v[24:27], v[32:35]\n"
    "v_exp_f32 v49, v49\n"
    "v_mfma_f32_16x16x32_bf16 v[36:39], a[52:55], v[24:27], v[36:39]\n"
    "v_mul_f32 v50, 0xbfb8aa3b, v50\n"
    "v_mul_f32 v51, 0xbfb8aa3b, v51\n"
    "v_mfma_f32_16x16x32_bf16 v[56:59], a[60:63], v[24:27], v[56:59]\n"
    "s_waitcnt lgkmcnt(2)\n"
    "v_exp_f32 v50, v50\n"
    "v_mfma_f32_16x16x32_bf16 v[60:63], a[64:67], v[24:27], v[60:63]\n"
    "ds_read_b128 v[24:27], v9 offset:12288\n"
    "v_mul_f32 v52, 0xbfb8aa3b, v52\n"
    "v_mfma_f32_16x16x32_bf16 v[32:35], a[72:75], v[28:31], v[32:35]\n"
    "v_exp_f32 v51, v51\n"
    "v_mfma_f32_16x16x32_bf16 v[36:39], a[76:79], v[28:31], v[36:39]\n"
    "v_exp_f32 v52, v52\n"
    "v_mov_b32 v12, s74\n"
    "v_mfma_f32_16x16x32_bf16 v[56:59], a[84:87], v[28:31], v[56:59]\n"
    "s_waitcnt lgkmcnt(2)\n"
    "s_waitcnt vmcnt(0)\n"
    "v_mul_f32 v53, 0xbfb8aa3b, v53\n"
    "v_mul_f32 v54, 0xbfb8aa3b, v54\n"
    "v_add_u32 v13, v11, v3\n"
    "v_mfma_f32_16x16x32_bf16 v[60:63], a[88:91], v[28:31], v[60:63]\n"
    "ds_read_b128 v[28:31], v9 offset:14336\n"
    "buffer_store_dword v12, v14, s[36:39], 0 offen sc1\n"
    "v_mul_f32 v55, 0xbfb8aa3b, v55\n"
    "v_cmp_gt_i32 vcc, s71, v13\n"
    "v_mfma_f32_16x16x32_bf16 v[32:35], a[96:99], v[16:19], v[32:35]\n"
    "v_exp_f32 v53, v53\n"
    "v_mfma_f32_16x16x32_bf16 v[36:39], a[100:103], v[16:19], v[36:39]\n"
    "v_exp_f32 v54, v54\n"
    "v_mfma_f32_16x16x32_bf16 v[56:59], a[108:111], v[16:19], v[56:59]\n"
    "s_waitcnt lgkmcnt(2)\n"
    "v_exp_f32 v55, v55\n"
    ".LgUP_mid_%=:\n"
    "s_cbranch_vccnz .LgUP_chk0_%=\n"
    "v_mfma_f32_16x16x32_bf16 v[60:63], a[112:115], v[16:19], v[60:63]\n"
    "ds_read_b128 v[16:19], v9 offset:16384\n"
    "v_add_f32 v48, 1.0, v48\n"
    "v_mfma_f32_16x16x32_bf16 v[32:35], a[120:123], v[20:23], v[32:35]\n"
    "s_add_u32 m0, s77, s59\n"
    "s_add_u32 s77, s77, 8192\n"
    "buffer_load_dwordx4 v1, s[36:39], s76 offen sc1 lds\n"
    "v_rcp_f32 v48, v48\n"
    "v_mfma_f32_16x16x32_bf16 v[36:39], a[124:127], v[20:23], v[36:39]\n"
    "s_mov_b32 s70, 1\n"
    "v_add_f32 v49, 1.0, v49\n"
    "v_add_f32 v50, 1.0, v50\n"
    "v_mfma_f32_16x16x32_bf16 v[56:59], a[132:135], v[20:23], v[56:59]\n"
    "s_waitcnt lgkmcnt(2)\n"
    "buffer_load_dwordx4 v1, s[36:39], s76 offen offset:1024 sc1 lds\n"
    "s_add_u32 s76, s76, 16384\n"
    "v_rcp_f32 v49, v49\n"
    "v_mfma_f32_16x16x32_bf16 v[60:63], a[136:139], v[20:23], v[60:63]\n"
    "ds_read_b128 v[20:23], v9 offset:18432\n"
    "v_add_f32 v51, 1.0, v51\n"
    "v_mfma_f32_16x16x32_bf16 v[32:35], a[144:147], v[24:27], v[32:35]\n"
    "s_and_b32 s73, s68, s57\n"
    "v_rcp_f32 v50, v50\n"
    "v_mfma_f32_16x16x32_bf16 v[36:39], a[148:151], v[24:27], v[36:39]\n"
    "s_mul_i32 s73, s73, s56\n"
    "v_rcp_f32 v51, v51\n"
    "v_mfma_f32_16x16x32_bf16 v[56:59], a[156:159], v[24:27], v[56:59]\n"
    "s_waitcnt lgkmcnt(2)\n"
    "s_add_u32 m0, s77, s59\n"
    "s_add_u32 s77, s77, 8192\n"
    "buffer_load_dwordx4 v1, s[36:39], s76 offen sc1 lds\n"
    "v_add_f32 v52, 1.0, v52\n"
    "v_add_f32 v53, 1.0, v53\n"
    "v_mfma_f32_16x16x32_bf16 v[60:63], a[160:163], v[24:27], v[60:63]\n"
    "ds_read_b128 v[24:27], v9 offset:20480\n"
    "v_add_f32 v54, 1.0, v54\n"
    "v_mfma_f32_16x16x32_bf16 v[32:35], a[168:171], v[28:31], v[32:35]\n"
    "s_add_u32 s73, s73, s55\n"
    "v_rcp_f32 v52, v52\n"
    "v_mfma_f32_16x16x32_bf16 v[36:39], a[172:175], v[28:31], v[36:39]\n"
    "buffer_load_dwordx4 v1, s[36:39], s76 offen offset:1024 sc1 lds\n"
    "s_add_u32 s76, s76, 16384\n"
    "v_rcp_f32 v53, v53\n"
    "v_mfma_f32_16x16x32_bf16 v[56:59], a[180:183], v[28:31], v[56:59]\n"
    "s_waitcnt lgkmcnt(2)\n"
    "v_rcp_f32 v54, v54\n"
    "v_mfma_f32_16x16x32_bf16 v[60:63], a[184:187], v[28:31], v[60:63]\n"
    "ds_read_b128 v[28:31], v9 offset:22528\n"
    "v_add_f32 v55, 1.0, v55\n"
    "v_mfma_f32_16x16x32_bf16 v[32:35], a[192:195], v[16:19], v[32:35]\n"
    "v_rcp_f32 v55, v55\n"
    "v_mfma_f32_16x16x32_bf16 v[36:39], a[196:199], v[16:19], v[36:39]\n"
    "s_add_u32 m0, s77, s59\n"
    "s_add_u32 s77, s77, 8192\n"
    "buffer_load_dwordx4 v1, s[36:39], s76 offen sc1 lds\n"
    "v_mul_f32 v48, v48, v72\n"
    "v_mul_f32 v49, v49, v73\n"
    "v_mfma_f32_16x16x32_bf16 v[56:59], a[204:207], v[16:19], v[56:59]\n"
    "s_waitcnt lgkmcnt(2)\n"
    "v_mul_f32 v50, v50, v74\n"
    "v_mul_f32 v51, v51, v75\n"
    "v_mfma_f32_16x16x32_bf16 v[60:63], a[208:211], v[16:19], v[60:63]\n"
    "ds_read_b128 v[16:19], v9 offset:24576\n"
    "v_mul_f32 v52, v52, v76\n"
    "v_mfma_f32_16x16x32_bf16 v[32:35], a[216:219], v[20:23], v[32:35]\n"
    "buffer_load_dwordx4 v1, s[36:39], s76 offen offset:1024 sc1 lds\n"
    "s_add_u32 s76, s76, 16384\n"
    "v_mul_f32 v53, v53, v77\n"
    "v_mul_f32 v54, v54, v78\n"
    "v_mfma_f32_16x16x32_bf16 v[36:39], a[220:223], v[20:23], v[36:39]\n"
    "v_mul_f32 v55, v55, v79\n"
    "v_cvt_pk_bf16_f32 v48, v48, v49\n"
    "v_mfma_f32_16x16x32_bf16 v[56:59], a[228:231], v[20:23], v[56:59]\n"
    "s_waitcnt lgkmcnt(2)\n"
    "v_cvt_pk_bf16_f32 v49, v50, v51\n"
    "v_cvt_pk_bf16_f32 v50, v52, v53\n"
    "v_mfma_f32_16x16x32_bf16 v[60:63], a[232:235], v[20:23], v[60:63]\n"
    "ds_read_b128 v[20:23], v9 offset:26624\n"
    "v_cvt_pk_bf16_f32 v51, v54, v55\n"
    "v_mfma_f32_16x16x32_bf16 v[32:35], a[240:243], v[24:27], v[32:35]\n"
    "s_add_u32 m0, s77, s59\n"
    "s_add_u32 s77, s77, 8192\n"
    "buffer_load_dwordx4 v1, s[36:39], s76 offen sc1 lds\n"
    "s_nop 0\n"
    "v_mfma_f32_16x16x32_bf16 v[36:39], a[244:247], v[24:27], v[36:39]\n"
    "v_permlane16_swap_b32 v48, v50\n"
    "v_mfma_f32_16x16x32_bf16 v[56:59], a[252:255], v[24:27], v[56:59]\n"
    "s_waitcnt lgkmcnt(2)\n"
    "buffer_load_dwordx4 v1, s[36:39], s76 offen offset:1024 sc1 lds\n"
    "s_add_u32 s76, s76, 16384\n"
    "v_permlane16_swap_b32 v49, v51\n"
    "v_mfma_f32_16x16x32_bf16 v[60:63], v[80:83], v[24:27], v[60:63]\n"
    "ds_read_b128 v[24:27], v9 offset:28672\n"
    "v_mfma_f32_16x16x32_bf16 v[32:35], v[88:91], v[28:31], v[32:35]\n"
    "v_mfma_f32_16x16x32_bf16 v[36:39], v[92:95], v[28:31], v[36:39]\n"
    "v_mfma_f32_16x16x32_bf16 v[56:59], v[100:103], v[28:31], v[56:59]\n"
    "s_waitcnt lgkmcnt(2)\n"
    "s_mov_b32 m0, s77\n"
    "s_nop 0\n"
    "buffer_load_dwordx4 v8, s[36:39], s76 offen sc1 lds\n"
    "v_mfma_f32_16x16x32_bf16 v[60:63], v[104:107], v[28:31], v[60:63]\n"
    "ds_read_b128 v[28:31], v9 offset:30720\n"
    "v_mfma_f32_16x16x32_bf16 v[32:35], v[112:115], v[16:19], v[32:35]\n"
    "v_mfma_f32_16x16x32_bf16 v[36:39], v[116:119], v[16:19], v[36:39]\n"
    "v_mfma_f32_16x16x32_bf16 v[56:59], v[124:127], v[16:19], v[56:59]\n"
    "s_waitcnt lgkmcnt(2)\n"
    ".LgUP_seg0_%=:\n"
    "s_cmp_lg_u32 s70, 0\n"
    "s_cbranch_scc0 .LgUP_chk1_%=\n"
    ".LgUP_plain1_%=:\n"
    "v_mfma_f32_16x16x32_bf16 v[60:63], v[128:131], v[16:19], v[60:63]\n"
    "ds_read_b128 v[16:19], v9 offset:32768\n"
    "v_mfma_f32_16x16x32_bf16 v[32:35], v[136:139], v[20:23], v[32:35]\n"
    "v_mfma_f32_16x16x32_bf16 v[36:39], v[140:143], v[20:23], v[36:39]\n"
    "v_mfma_f32_16x16x32_bf16 v[56:59], v[148:151], v[20:23], v[56:59]\n"
    "s_waitcnt lgkmcnt(2)\n"
    "v_mfma_f32_16x16x32_bf16 v[60:63], v[152:155], v[20:23], v[60:63]\n"
    "ds_read_b128 v[20:23], v9 offset:34816\n"
    "v_mfma_f32_16x16x32_bf16 v[32:35], v[160:163], v[24:27], v[32:35]\n"
    "v_mfma_f32_16x16x32_bf16 v[36:39], v[164:167], v[24:27], v[36:39]\n"
    "v_mfma_f32_16x16x32_bf16 v[56:59], v[172:175], v[24:27], v[56:59]\n"
    "s_waitcnt lgkmcnt(2)\n"
    "v_mfma_f32_16x16x32_bf16 v[60:63], v[176:179], v[24:27], v[60:63]\n"
    "ds_read_b128 v[24:27], v9 offset:1024\n"
    "v_mfma_f32_16x16x32_bf16 v[32:35], v[184:187], v[28:31], v[32:35]\n"
    "v_mfma_f32_16x16x32_bf16 v[36:39], v[188:191], v[28:31], v[36:39]\n"
    "v_mfma_f32_16x16x32_bf16 v[56:59], v[196:199], v[28:31], v[56:59]\n"
    "s_waitcnt lgkmcnt(2)\n"
    "v_mfma_f32_16x16x32_bf16 v[60:63], v[200:203], v[28:31], v[60:63]\n"
    "ds_read_b128 v[28:31], v9 offset:3072\n"
    "v_mfma_f32_16x16x32_bf16 v[32:35], v[208:211], v[16:19], v[32:35]\n"
    "v_mfma_f32_16x16x32_bf16 v[36:39], v[212:215], v[16:19], v[36:39]\n"
    "v_mfma_f32_16x16x32_bf16 v[56:59], v[220:223], v[16:19], v[56:59]\n"
    "s_waitcnt lgkmcnt(2)\n"
    "v_mfma_f32_16x16x32_bf16 v[60:63], v[224:227], v[16:19], v[60:63]\n"
    "ds_read_b128 v[16:19], v9 offset:5120\n"
    "v_mfma_f32_16x16x32_bf16 v[32:35], v[232:235], v[20:23], v[32:35]\n"
    "v_mfma_f32_16x16x32_bf16 v[36:39], v[236:239], v[20:23], v[36:39]\n"
    "v_mfma_f32_16x16x32_bf16 v[56:59], v[244:247], v[20:23], v[56:59]\n"
    "s_waitcnt lgkmcnt(2)\n"
    "v_mfma_f32_16x16x32_bf16 v[60:63], v[248:251], v[20:23], v[60:63]\n"
    "ds_read_b128 v[20:23], v9 offset:7168\n"
    "v_mfma_f32_16x16x32_bf16 v[40:43], a[0:3], v[24:27], 0\n"
    "v_mfma_f32_16x16x32_bf16 v[44:47], a[4:7], v[24:27], 0\n"
    "v_mul_f32 v56, v32, v56\n"
    "v_mul_f32 v57, v33, v57\n"
    "v_mfma_f32_16x16x32_bf16 v[64:67], a[12:15], v[24:27], 0\n"
    "s_waitcnt lgkmcnt(2)\n"
    "v_mul_f32 v58, v34, v58\n"
    "v_mul_f32 v59, v35, v59\n"
    "v_mfma_f32_16x16x32_bf16 v[68:71], a[16:19], v[24:27], 0\n"
    "ds_read_b128 v[24:27], v9 offset:9216\n"
    "v_mul_f32 v60, v36, v60\n"
    "v_mfma_f32_16x16x32_bf16 v[40:43], a[24:27], v[28:31], v[40:43]\n"
    "v_mul_f32 v61, v37, v61\n"
    "v_mul_f32 v62, v38, v62\n"
    "v_mfma_f32_16x16x32_bf16 v[44:47], a[28:31], v[28:31], v[44:47]\n"
    "v_mul_f32 v63, v39, v63\n"
    "v_mul_f32 v32, 0xbfb8aa3b, v32\n"
    "v_mfma_f32_16x16x32_bf16 v[64:67], a[36:39], v[28:31], v[64:67]\n"
    "s_waitcnt lgkmcnt(2)\n"
    "v_exp_f32 v32, v32\n"
    "v_mfma_f32_16x16x32_bf16 v[68:71], a[40:43], v[28:31], v[68:71]\n"
    "ds_read_b128 v[28:31], v9 offset:11264\n"
    "v_mul_f32 v33, 0xbfb8aa3b, v33\n"
    "v_mfma_f32_16x16x32_bf16 v[40:43], a[48:51], v[16:19], v[40:43]\n"
    "v_exp_f32 v33, v33\n"
    ".LgUP_seg1_%=:\n"
    "s_cmp_lg_u32 s70, 0\n"
    "s_cbranch_scc0 .LgUP_chk2_%=\n"
    ".LgUP_plain2_%=:\n"
    "v_mfma_f32_16x16x32_bf16 v[44:47], a[52:55], v[16:19], v[44:47]\n"
    "v_mul_f32 v34, 0xbfb8aa3b, v34\n"
    "v_mul_f32 v35, 0xbfb8aa3b, v35\n"
    "v_mfma_f32_16x16x32_bf16 v[64:67], a[60:63], v[16:19], v[64:67]\n"
    "s_waitcnt lgkmcnt(2)\n"
    "v_exp_f32 v34, v34\n"
    "v_mfma_f32_16x16x32_bf16 v[68:71], a[64:67], v[16:19], v[68:71]\n"
    "ds_read_b128 v[16:19], v9 offset:13312\n"
    "v_mul_f32 v36, 0xbfb8aa3b, v36\n"
    "v_mfma_f32_16x16x32_bf16 v[40:43], a[72:75], v[20:23], v[40:43]\n"
    "v_exp_f32 v35, v35\n"
    "v_mfma_f32_16x16x32_bf16 v[44:47], a[76:79], v[20:23], v[44:47]\n"
    "v_exp_f32 v36, v36\n"
    "v_mfma_f32_16x16x32_bf16 v[64:67], a[84:87], v[20:23], v[64:67]\n"
    "s_waitcnt lgkmcnt(2)\n"
    "v_mul_f32 v37, 0xbfb8aa3b, v37\n"
    "v_mul_f32 v38, 0xbfb8aa3b, v38\n"
    "v_mfma_f32_16x16x32_bf16 v[68:71], a[88:91], v[20:23], v[68:71]\n"
    "ds_read_b128 v[20:23], v9 offset:15360\n"
    "v_mul_f32 v39, 0xbfb8aa3b, v39\n"
    "v_mfma_f32_16x16x32_bf16 v[40:43], a[96:99], v[24:27], v[40:43]\n"
    "v_exp_f32 v37, v37\n"
    "v_mfma_f32_16x16x32_bf16 v[44:47], a[100:103], v[24:27], v[44:47]\n"
    "v_exp_f32 v38, v38\n"
    "v_mfma_f32_16x16x32_bf16 v[64:67], a[108:111], v[24:27], v[64:67]\n"
    "s_waitcnt lgkmcnt(2)\n"
    "v_exp_f32 v39, v39\n"
    "v_mfma_f32_16x16x32_bf16 v[68:71], a[112:115], v[24:27], v[68:71]\n"
    "ds_read_b128 v[24:27], v9 offset:17408\n"
    "v_add_f32 v32, 1.0, v32\n"
    "v_mfma_f32_16x16x32_bf16 v[40:43], a[120:123], v[28:31], v[40:43]\n"
    "v_rcp_f32 v32, v32\n"
    "v_mfma_f32_16x16x32_bf16 v[44:47], a[124:127], v[28:31], v[44:47]\n"
    "v_add_f32 v33, 1.0, v33\n"
    "v_add_f32 v34, 1.0, v34\n"
    "v_mfma_f32_16x16x32_bf16 v[64:67], a[132:135], v[28:31], v[64:67]\n"
    "s_waitcnt lgkmcnt(2)\n"
    "v_rcp_f32 v33, v33\n"
    "v_mfma_f32_16x16x32_bf16 v[68:71], a[136:139], v[28:31], v[68:71]\n"
    "ds_read_b128 v[28:31], v9 offset:19456\n"
    "v_add_f32 v35, 1.0, v35\n"
    "v_mfma_f32_16x16x32_bf16 v[40:43], a[144:147], v[16:19], v[40:43]\n"
    "v_rcp_f32 v34, v34\n"
    "v_mfma_f32_16x16x32_bf16 v[44:47], a[148:151], v[16:19], v[44:47]\n"
    "v_rcp_f32 v35, v35\n"
    "v_mfma_f32_16x16x32_bf16 v[64:67], a[156:159], v[16:19], v[64:67]\n"
    "s_waitcnt lgkmcnt(2)\n"
    "v_add_f32 v36, 1.0, v36\n"
    "v_add_f32 v37, 1.0, v37\n"
    "v_mfma_f32_16x16x32_bf16 v[68:71], a[160:163], v[16:19], v[68:71]\n"
    "ds_read_b128 v[16:19], v9 offset:21504\n"
    "v_add_f32 v38, 1.0, v38\n"
    "v_mfma_f32_16x16x32_bf16 v[40:43], a[168:171], v[20:23], v[40:43]\n"
    "v_rcp_f32 v36, v36\n"
    "v_mfma_f32_16x16x32_bf16 v[44:47], a[172:175], v[20:23], v[44:47]\n"
    "v_rcp_f32 v37, v37\n"
    "v_mfma_f32_16x16x32_bf16 v[64:67], a[180:183], v[20:23], v[64:67]\n"
    "s_waitcnt lgkmcnt(2)\n"
    "v_rcp_f32 v38, v38\n"
    "v_mfma_f32_16x16x32_bf16 v[68:71], a[184:187], v[20:23], v[68:71]\n"
    "ds_read_b128 v[20:23], v9 offset:23552\n"
    "v_add_f32 v39, 1.0, v39\n"
    "v_mfma_f32_16x16x32_bf16 v[40:43], a[192:195], v[24:27], v[40:43]\n"
    "v_rcp_f32 v39, v39\n"
    "v_mfma_f32_16x16x32_bf16 v[44:47], a[196:199], v[24:27], v[44:47]\n"
    "v_mul_f32 v32, v32, v56\n"
    "v_mul_f32 v33, v33, v57\n"
    "v_mfma_f32_16x16x32_bf16 v[64:67], a[204:207], v[24:27], v[64:67]\n"
    "s_waitcnt lgkmcnt(2)\n"
    "v_mul_f32 v34, v34, v58\n"
    "v_mul_f32 v35, v35, v59\n"
    "v_mfma_f32_16x16x32_bf16 v[68:71], a[208:211], v[24:27], v[68:71]\n"
    "ds_read_b128 v[24:27], v9 offset:25600\n"
    "v_mul_f32 v36, v36, v60\n"
    "v_mfma_f32_16x16x32_bf16 v[40:43], a[216:219], v[28:31], v[40:43]\n"
    "v_mul_f32 v37, v37, v61\n"
    "v_mul_f32 v38, v38, v62\n"
    "v_mfma_f32_16x16x32_bf16 v[44:47], a[220:223], v[28:31], v[44:47]\n"
    "v_mul_f32 v39, v39, v63\n"
    "v_cvt_pk_bf16_f32 v32, v32, v33\n"
    "v_mfma_f32_16x16x32_bf16 v[64:67], a[228:231], v[28:31], v[64:67]\n"
    "s_waitcnt lgkmcnt(2)\n"
    "v_cvt_pk_bf16_f32 v33, v34, v35\n"
    "v_cvt_pk_bf16_f32 v34, v36, v37\n"
    "v_mfma_f32_16x16x32_bf16 v[68:71], a[232:235], v[28:31], v[68:71]\n"
    "ds_read_b128 v[28:31], v9 offset:27648\n"
    "v_cvt_pk_bf16_f32 v35, v38, v39\n"
    ".LgUP_seg2_%=:\n"
    "s_cmp_lg_u32 s70, 1\n"
    "s_cbranch_scc1 .LgUP_slow_%=\n"
    "v_mfma_f32_16x16x32_bf16 v[40:43], a[240:243], v[16:19], v[40:43]\n"
    "s_mov_b32 s68, s71\n"
    "buffer_store_dwordx4 v[48:51], v4, s[36:39], s73 offen sc1\n"
    "v_mfma_f32_16x16x32_bf16 v[44:47], a[244:247], v[16:19], v[44:47]\n"
    "s_add_u32 s71, s68, 1\n"
    "v_mfma_f32_16x16x32_bf16 v[64:67], a[252:255], v[16:19], v[64:67]\n"
    "s_waitcnt lgkmcnt(2)\n"
    "s_cmp_eq_u32 s71, 64\n"
    "buffer_store_dwordx4 v[32:35], v15, s[36:39], s73 offen sc1\n"
    "v_mfma_f32_16x16x32_bf16 v[68:71], v[80:83], v[16:19], v[68:71]\n"
    "ds_read_b128 v[16:19], v9 offset:29696\n"
    "v_mfma_f32_16x16x32_bf16 v[40:43], v[88:91], v[20:23], v[40:43]\n"
    "s_cselect_b32 s70, 2, 0\n"
    "v_mfma_f32_16x16x32_bf16 v[44:47], v[92:95], v[20:23], v[44:47]\n"
    "v_mfma_f32_16x16x32_bf16 v[64:67], v[100:103], v[20:23], v[64:67]\n"
    "s_waitcnt lgkmcnt(2)\n"
    "v_mfma_f32_16x16x32_bf16 v[68:71], v[104:107], v[20:23], v[68:71]\n"
    "ds_read_b128 v[20:23], v9 offset:31744\n"
    "v_mfma_f32_16x16x32_bf16 v[40:43], v[112:115], v[24:27], v[40:43]\n"
    "v_mfma_f32_16x16x32_bf16 v[44:47], v[116:119], v[24:27], v[44:47]\n"
    "v_mfma_f32_16x16x32_bf16 v[64:67], v[124:127], v[24:27], v[64:67]\n"
    "s_waitcnt lgkmcnt(2)\n"
    "v_mfma_f32_16x16x32_bf16 v[68:71], v[128:131], v[24:27], v[68:71]\n"
    "ds_read_b128 v[24:27], v9 offset:33792\n"
    "v_mfma_f32_16x16x32_bf16 v[40:43], v[136:139], v[28:31], v[40:43]\n"
    "v_mfma_f32_16x16x32_bf16 v[44:47], v[140:143], v[28:31], v[44:47]\n"
    "v_mfma_f32_16x16x32_bf16 v[64:67], v[148:151], v[28:31], v[64:67]\n"
    "s_waitcnt lgkmcnt(2)\n"
    "v_mfma_f32_16x16x32_bf16 v[68:71], v[152:155], v[28:31], v[68:71]\n"
    "ds_read_b128 v[28:31], v9 offset:35840\n"
    "v_add_u32 v13, v11, v3\n"
    "v_mfma_f32_16x16x32_bf16 v[40:43], v[160:163], v[16:19], v[40:43]\n"
    "v_cmp_gt_i32 vcc, s71, v13\n"
    "v_mfma_f32_16x16x32_bf16 v[44:47], v[164:167], v[16:19], v[44:47]\n"
    "v_mfma_f32_16x16x32_bf16 v[64:67], v[172:175], v[16:19], v[64:67]\n"
    "s_waitcnt lgkmcnt(2)\n"
    "v_xor_b32 v9, s78, v9\n"
    "v_mfma_f32_16x16x32_bf16 v[68:71], v[176:179], v[16:19], v[68:71]\n"
    "s_waitcnt vmcnt(2) lgkmcnt(0)\n"
    "s_barrier\n"
    "ds_read_b128 v[16:19], v9 offset:0\n"
    "v_mfma_f32_16x16x32_bf16 v[40:43], v[184:187], v[20:23], v[40:43]\n"
    "s_or_b64 exec, vcc, 1\n"
    "buffer_atomic_smax v11, v2, s[36:39], 0 offen sc0 sc1\n"
    "s_mov_b64 exec, -1\n"
    "v_mfma_f32_16x16x32_bf16 v[44:47], v[188:191], v[20:23], v[44:47]\n"
    "v_mfma_f32_16x16x32_bf16 v[64:67], v[196:199], v[20:23], v[64:67]\n"
    "v_mfma_f32_16x16x32_bf16 v[68:71], v[200:203], v[20:23], v[68:71]\n"
    "ds_read_b128 v[20:23], v9 offset:1024\n"
    "v_mfma_f32_16x16x32_bf16 v[40:43], v[208:211], v[24:27], v[40:43]\n"
    "v_mfma_f32_16x16x32_bf16 v[44:47], v[212:215], v[24:27], v[44:47]\n"
    "v_mfma_f32_16x16x32_bf16 v[64:67], v[220:223], v[24:27], v[64:67]\n"
    "v_mfma_f32_16x16x32_bf16 v[68:71], v[224:227], v[24:27], v[68:71]\n"
    "ds_read_b128 v[24:27], v9 offset:2048\n"
    "v_mfma_f32_16x16x32_bf16 v[40:43], v[232:235], v[28:31], v[40:43]\n"
    "v_mfma_f32_16x16x32_bf16 v[44:47], v[236:239], v[28:31], v[44:47]\n"
    "v_mfma_f32_16x16x32_bf16 v[64:67], v[244:247], v[28:31], v[64:67]\n"
    "s_waitcnt lgkmcnt(2)\n"
    "v_mfma_f32_16x16x32_bf16 v[68:71], v[248:251], v[28:31], v[68:71]\n"
    "ds_read_b128 v[28:31], v9 offset:3072\n"
    "s_branch .LgUP_head_%=\n"
    ".LgUP_LOOPE_%=:\n"
    ".LgUP_done_%=:\n"
    "s_waitcnt vmcnt(0)\n"
    "s_mov_b32 s74, 63\n"
    "v_mov_b32 v12, s74\n"
    "s_mov_b64 exec, s[62:63]\n"
    "buffer_store_dword v12, v14, s[36:39], 0 offen sc1\n"
    "s_mov_b64 exec, -1\n"
    "s_waitcnt vmcnt(0)\n"
    "s_branch .LgUP_FIN_%=\n"
    ".LgUP_chk0_%=:\n"
    "s_or_b64 exec, vcc, 1\n"
    "buffer_atomic_smax v11, v2, s[36:39], 0 offen sc0 sc1\n"
    "s_mov_b64 exec, -1\n"
    "v_mfma_f32_16x16x32_bf16 v[60:63], a[112:115], v[16:19], v[60:63]\n"
    "ds_read_b128 v[16:19], v9 offset:16384\n"
    "v_add_f32 v48, 1.0, v48\n"
    "v_mfma_f32_16x16x32_bf16 v[32:35], a[120:123], v[20:23], v[32:35]\n"
    "s_and_b32 s73, s68, s57\n"
    "v_rcp_f32 v48, v48\n"
    "v_mfma_f32_16x16x32_bf16 v[36:39], a[124:127], v[20:23], v[36:39]\n"
    "s_mul_i32 s73, s73, s56\n"
    "v_add_f32 v49, 1.0, v49\n"
    "v_add_f32 v50, 1.0, v50\n"
    "v_mfma_f32_16x16x32_bf16 v[56:59], a[132:135], v[20:23], v[56:59]\n"
    "s_waitcnt lgkmcnt(2)\n"
    "v_rcp_f32 v49, v49\n"
    "v_mfma_f32_16x16x32_bf16 v[60:63], a[136:139], v[20:23], v[60:63]\n"
    "ds_read_b128 v[20:23], v9 offset:18432\n"
    "v_add_f32 v51, 1.0, v51\n"
    "v_mfma_f32_16x16x32_bf16 v[32:35], a[144:147], v[24:27], v[32:35]\n"
    "s_add_u32 s73, s73, s55\n"
    "v_rcp_f32 v50, v50\n"
    "v_mfma_f32_16x16x32_bf16 v[36:39], a[148:151], v[24:27], v[36:39]\n"
    "v_rcp_f32 v51, v51\n"
    "v_mfma_f32_16x16x32_bf16 v[56:59], a[156:159], v[24:27], v[56:59]\n"
    "s_waitcnt lgkmcnt(2)\n"
    "v_add_f32 v52, 1.0, v52\n"
    "v_add_f32 v53, 1.0, v53\n"
    "v_mfma_f32_16x16x32_bf16 v[60:63], a[160:163], v[24:27], v[60:63]\n"
    "ds_read_b128 v[24:27], v9 offset:20480\n"
    "v_add_f32 v54, 1.0, v54\n"
    "v_mfma_f32_16x16x32_bf16 v[32:35], a[168:171], v[28:31], v[32:35]\n"
    "v_rcp_f32 v52, v52\n"
    "v_mfma_f32_16x16x32_bf16 v[36:39], a[172:175], v[28:31], v[36:39]\n"
    "v_rcp_f32 v53, v53\n"
    "v_mfma_f32_16x16x32_bf16 v[56:59], a[180:183], v[28:31], v[56:59]\n"
    "s_waitcnt lgkmcnt(2)\n"
    "v_rcp_f32 v54, v54\n"
    "v_mfma_f32_16x16x32_bf16 v[60:63], a[184:187], v[28:31], v[60:63]\n"
    "ds_read_b128 v[28:31], v9 offset:22528\n"
    "v_add_f32 v55, 1.0, v55\n"
    "v_mfma_f32_16x16x32_bf16 v[32:35], a[192:195], v[16:19], v[32:35]\n"
    "v_rcp_f32 v55, v55\n"
    "v_mfma_f32_16x16x32_bf16 v[36:39], a[196:199], v[16:19], v[36:39]\n"
    "v_mul_f32 v48, v48, v72\n"
    "v_mul_f32 v49, v49, v73\n"
    "v_mfma_f32_16x16x32_bf16 v[56:59], a[204:207], v[16:19], v[56:59]\n"
    "s_waitcnt lgkmcnt(2)\n"
    "v_mul_f32 v50, v50, v74\n"
    "v_mul_f32 v51, v51, v75\n"
    "v_mfma_f32_16x16x32_bf16 v[60:63], a[208:211], v[16:19], v[60:63]\n"
    "ds_read_b128 v[16:19], v9 offset:24576\n"
    "v_mul_f32 v52, v52, v76\n"
    "v_mfma_f32_16x16x32_bf16 v[32:35], a[216:219], v[20:23], v[32:35]\n"
    "v_mul_f32 v53, v53, v77\n"
    "v_mul_f32 v54, v54, v78\n"
    "v_mfma_f32_16x16x32_bf16 v[36:39], a[220:223], v[20:23], v[36:39]\n"
    "v_mul_f32 v55, v55, v79\n"
    "v_cvt_pk_bf16_f32 v48, v48, v49\n"
    "v_mfma_f32_16x16x32_bf16 v[56:59], a[228:231], v[20:23], v[56:59]\n"
    "s_waitcnt lgkmcnt(2)\n"
    "v_cvt_pk_bf16_f32 v49, v50, v51\n"
    "v_cvt_pk_bf16_f32 v50, v52, v53\n"
    "v_mfma_f32_16x16x32_bf16 v[60:63], a[232:235], v[20:23], v[60:63]\n"
    "ds_read_b128 v[20:23], v9 offset:26624\n"
    "v_cvt_pk_bf16_f32 v51, v54, v55\n"
    "v_mfma_f32_16x16x32_bf16 v[32:35], a[240:243], v[24:27], v[32:35]\n"
    "s_nop 0\n"
    "v_mfma_f32_16x16x32_bf16 v[36:39], a[244:247], v[24:27], v[36:39]\n"
    "v_permlane16_swap_b32 v48, v50\n"
    "v_mfma_f32_16x16x32_bf16 v[56:59], a[252:255], v[24:27], v[56:59]\n"
    "s_waitcnt lgkmcnt(2)\n"
    "v_permlane16_swap_b32 v49, v51\n"
    "v_mfma_f32_16x16x32_bf16 v[60:63], v[80:83], v[24:27], v[60:63]\n"
    "ds_read_b128 v[24:27], v9 offset:28672\n"
    "v_mfma_f32_16x16x32_bf16 v[32:35], v[88:91], v[28:31], v[32:35]\n"
    "v_mfma_f32_16x16x32_bf16 v[36:39], v[92:95], v[28:31], v[36:39]\n"
    "v_mfma_f32_16x16x32_bf16 v[56:59], v[100:103], v[28:31], v[56:59]\n"
    "s_waitcnt lgkmcnt(2)\n"
    "v_mfma_f32_16x16x32_bf16 v[60:63], v[104:107], v[28:31], v[60:63]\n"
    "ds_read_b128 v[28:31], v9 offset:30720\n"
    "v_mfma_f32_16x16x32_bf16 v[32:35], v[112:115], v[16:19], v[32:35]\n"
    "v_mfma_f32_16x16x32_bf16 v[36:39], v[116:119], v[16:19], v[36:39]\n"
    "v_mfma_f32_16x16x32_bf16 v[56:59], v[124:127], v[16:19], v[56:59]\n"
    "s_waitcnt lgkmcnt(2)\n"
    "s_branch .LgUP_seg0_%=\n"
    ".LgUP_chk1_%=:\n"
    "s_waitcnt vmcnt(0)\n"
    "v_add_u32 v13, v11, v3\n"
    "v_cmp_gt_i32 vcc, s71, v13\n"
    "s_cbranch_vccz .LgUP_dma1_%=\n"
    "s_or_b64 exec, vcc, 1\n"
    "buffer_atomic_smax v11, v2, s[36:39], 0 offen sc0 sc1\n"
    "s_mov_b64 exec, -1\n"
    "s_branch .LgUP_plain1_%=\n"
    ".LgUP_dma1_%=:\n"
    "s_mov_b32 s70, 1\n"
    "v_mfma_f32_16x16x32_bf16 v[60:63], v[128:131], v[16:19], v[60:63]\n"
    "ds_read_b128 v[16:19], v9 offset:32768\n"
    "v_mfma_f32_16x16x32_bf16 v[32:35], v[136:139], v[20:23], v[32:35]\n"
    "s_add_u32 m0, s77, s59\n"
    "s_add_u32 s77, s77, 8192\n"
    "buffer_load_dwordx4 v1, s[36:39], s76 offen sc1 lds\n"
    "v_mfma_f32_16x16x32_bf16 v[36:39], v[140:143], v[20:23], v[36:39]\n"
    "v_mfma_f32_16x16x32_bf16 v[56:59], v[148:151], v[20:23], v[56:59]\n"
    "s_waitcnt lgkmcnt(2)\n"
    "buffer_load_dwordx4 v1, s[36:39], s76 offen offset:1024 sc1 lds\n"
    "s_add_u32 s76, s76, 16384\n"
    "v_mfma_f32_16x16x32_bf16 v[60:63], v[152:155], v[20:23], v[60:63]\n"
    "ds_read_b128 v[20:23], v9 offset:34816\n"
    "v_mfma_f32_16x16x32_bf16 v[32:35], v[160:163], v[24:27], v[32:35]\n"
    "v_mfma_f32_16x16x32_bf16 v[36:39], v[164:167], v[24:27], v[36:39]\n"
    "s_add_u32 m0, s77, s59\n"
    "s_add_u32 s77, s77, 8192\n"
    "buffer_load_dwordx4 v1, s[36:39], s76 offen sc1 lds\n"
    "v_mfma_f32_16x16x32_bf16 v[56:59], v[172:175], v[24:27], v[56:59]\n"
    "s_waitcnt lgkmcnt(2)\n"
    "v_mfma_f32_16x16x32_bf16 v[60:63], v[176:179], v[24:27], v[60:63]\n"
    "ds_read_b128 v[24:27], v9 offset:1024\n"
    "v_mfma_f32_16x16x32_bf16 v[32:35], v[184:187], v[28:31], v[32:35]\n"
    "v_mfma_f32_16x16x32_bf16 v[36:39], v[188:191], v[28:31], v[36:39]\n"
    "buffer_load_dwordx4 v1, s[36:39], s76 offen offset:1024 sc1 lds\n"
    "s_add_u32 s76, s76, 16384\n"
    "v_mfma_f32_16x16x32_bf16 v[56:59], v[196:199], v[28:31], v[56:59]\n"
    "s_waitcnt lgkmcnt(2)\n"
    "v_mfma_f32_16x16x32_bf16 v[60:63], v[200:203], v[28:31], v[60:63]\n"
    "ds_read_b128 v[28:31], v9 offset:3072\n"
    "v_mfma_f32_16x16x32_bf16 v[32:35], v[208:211], v[16:19], v[32:35]\n"
    "s_add_u32 m0, s77, s59\n"
    "s_add_u32 s77, s77, 8192\n"
    "buffer_load_dwordx4 v1, s[36:39], s76 offen sc1 lds\n"
    "v_mfma_f32_16x16x32_bf16 v[36:39], v[212:215], v[16:19], v[36:39]\n"
    "v_mfma_f32_16x16x32_bf16 v[56:59], v[220:223], v[16:19], v[56:59]\n"
    "s_waitcnt lgkmcnt(2)\n"
    "buffer_load_dwordx4 v1, s[36:39], s76 offen offset:1024 sc1 lds\n"
    "s_add_u32 s76, s76, 16384\n"
    "v_mfma_f32_16x16x32_bf16 v[60:63], v[224:227], v[16:19], v[60:63]\n"
    "ds_read_b128 v[16:19], v9 offset:5120\n"
    "v_mfma_f32_16x16x32_bf16 v[32:35], v[232:235], v[20:23], v[32:35]\n"
    "v_mfma_f32_16x16x32_bf16 v[36:39], v[236:239], v[20:23], v[36:39]\n"
    "v_mfma_f32_16x16x32_bf16 v[56:59], v[244:247], v[20:23], v[56:59]\n"
    "s_waitcnt lgkmcnt(2)\n"
    "s_add_u32 m0, s77, s59\n"
    "s_add_u32 s77, s77, 8192\n"
    "buffer_load_dwordx4 v1, s[36:39], s76 offen sc1 lds\n"
    "v_mfma_f32_16x16x32_bf16 v[60:63], v[248:251], v[20:23], v[60:63]\n"
    "ds_read_b128 v[20:23], v9 offset:7168\n"
    "v_mfma_f32_16x16x32_bf16 v[40:43], a[0:3], v[24:27], 0\n"
    "v_mfma_f32_16x16x32_bf16 v[44:47], a[4:7], v[24:27], 0\n"
    "buffer_load_dwordx4 v1, s[36:39], s76 offen offset:1024 sc1 lds\n"
    "s_add_u32 s76, s76, 16384\n"
    "v_mul_f32 v56, v32, v56\n"
    "v_mul_f32 v57, v33, v57\n"
    "v_mfma_f32_16x16x32_bf16 v[64:67], a[12:15], v[24:27], 0\n"
    "s_waitcnt lgkmcnt(2)\n"
    "v_mul_f32 v58, v34, v58\n"
    "v_mul_f32 v59, v35, v59\n"
    "v_mfma_f32_16x16x32_bf16 v[68:71], a[16:19], v[24:27], 0\n"
    "ds_read_b128 v[24:27], v9 offset:9216\n"
    "v_mul_f32 v60, v36, v60\n"
    "v_mfma_f32_16x16x32_bf16 v[40:43], a[24:27], v[28:31], v[40:43]\n"
    "s_mov_b32 m0, s77\n"
    "s_nop 0\n"
    "buffer_load_dwordx4 v8, s[36:39], s76 offen sc1 lds\n"
    "v_mul_f32 v61, v37, v61\n"
    "v_mul_f32 v62, v38, v62\n"
    "v_mfma_f32_16x16x32_bf16 v[44:47], a[28:31], v[28:31], v[44:47]\n"
    "v_mul_f32 v63, v39, v63\n"
    "v_mul_f32 v32, 0xbfb8aa3b, v32\n"
    "v_mfma_f32_16x16x32_bf16 v[64:67], a[36:39], v[28:31], v[64:67]\n"
    "s_waitcnt lgkmcnt(2)\n"
    "v_exp_f32 v32, v32\n"
    "v_mfma_f32_16x16x32_bf16 v[68:71], a[40:43], v[28:31], v[68:71]\n"
    "ds_read_b128 v[28:31], v9 offset:11264\n"
    "v_mul_f32 v33, 0xbfb8aa3b, v33\n"
    "v_mfma_f32_16x16x32_bf16 v[40:43], a[48:51], v[16:19], v[40:43]\n"
    "v_exp_f32 v33, v33\n"
    "s_branch .LgUP_seg1_%=\n"
    ".LgUP_chk2_%=:\n"
    "s_waitcnt vmcnt(0)\n"
    "v_add_u32 v13, v11, v3\n"
    "v_cmp_gt_i32 vcc, s71, v13\n"
    "s_cbranch_vccz .LgUP_dma2_%=\n"
    "s_or_b64 exec, vcc, 1\n"
    "buffer_atomic_smax v11, v2, s[36:39], 0 offen sc0 sc1\n"
    "s_mov_b64 exec, -1\n"
    "s_branch .LgUP_plain2_%=\n"
    ".LgUP_dma2_%=:\n"
    "s_mov_b32 s70, 2\n"
    "v_mfma_f32_16x16x32_bf16 v[44:47], a[52:55], v[16:19], v[44:47]\n"
    "s_add_u32 m0, s77, s59\n"
    "s_add_u32 s77, s77, 8192\n"
    "buffer_load_dwordx4 v1, s[36:39], s76 offen sc1 lds\n"
    "v_mul_f32 v34, 0xbfb8aa3b, v34\n"
    "v_mul_f32 v35, 0xbfb8aa3b, v35\n"
    "v_mfma_f32_16x16x32_bf16 v[64:67], a[60:63], v[16:19], v[64:67]\n"
    "s_waitcnt lgkmcnt(2)\n"
    "v_exp_f32 v34, v34\n"
    "v_mfma_f32_16x16x32_bf16 v[68:71], a[64:67], v[16:19], v[68:71]\n"
    "ds_read_b128 v[16:19], v9 offset:13312\n"
    "v_mul_f32 v36, 0xbfb8aa3b, v36\n"
    "v_mfma_f32_16x16x32_bf16 v[40:43], a[72:75], v[20:23], v[40:43]\n"
    "buffer_load_dwordx4 v1, s[36:39], s76 offen offset:1024 sc1 lds\n"
    "s_add_u32 s76, s76, 16384\n"
    "v_exp_f32 v35, v35\n"
    "v_mfma_f32_16x16x32_bf16 v[44:47], a[76:79], v[20:23], v[44:47]\n"
    "v_exp_f32 v36, v36\n"
    "v_mfma_f32_16x16x32_bf16 v[64:67], a[84:87], v[20:23], v[64:67]\n"
    "s_waitcnt lgkmcnt(2)\n"
    "v_mul_f32 v37, 0xbfb8aa3b, v37\n"
    "v_mul_f32 v38, 0xbfb8aa3b, v38\n"
    "v_mfma_f32_16x16x32_bf16 v[68:71], a[88:91], v[20:23], v[68:71]\n"
    "ds_read_b128 v[20:23], v9 offset:15360\n"
    "v_mul_f32 v39, 0xbfb8aa3b, v39\n"
    "v_mfma_f32_16x16x32_bf16 v[40:43], a[96:99], v[24:27], v[40:43]\n"
    "s_add_u32 m0, s77, s59\n"
    "s_add_u32 s77, s77, 8192\n"
    "buffer_load_dwordx4 v1, s[36:39], s76 offen sc1 lds\n"
    "v_exp_f32 v37, v37\n"
    "v_mfma_f32_16x16x32_bf16 v[44:47], a[100:103], v[24:27], v[44:47]\n"
    "v_exp_f32 v38, v38\n"
    "v_mfma_f32_16x16x32_bf16 v[64:67], a[108:111], v[24:27], v[64:67]\n"
    "s_waitcnt lgkmcnt(2)\n"
    "buffer_load_dwordx4 v1, s[36:39], s76 offen offset:1024 sc1 lds\n"
    "s_add_u32 s76, s76, 16384\n"
    "v_exp_f32 v39, v39\n"
    "v_mfma_f32_16x16x32_bf16 v[68:71], a[112:115], v[24:27], v[68:71]\n"
    "ds_read_b128 v[24:27], v9 offset:17408\n"
    "v_add_f32 v32, 1.0, v32\n"
    "v_mfma_f32_16x16x32_bf16 v[40:43], a[120:123], v[28:31], v[40:43]\n"
    "v_rcp_f32 v32, v32\n"
    "v_mfma_f32_16x16x32_bf16 v[44:47], a[124:127], v[28:31], v[44:47]\n"
    "v_add_f32 v33, 1.0, v33\n"
    "v_add_f32 v34, 1.0, v34\n"
    "v_mfma_f32_16x16x32_bf16 v[64:67], a[132:135], v[28:31], v[64:67]\n"
    "s_waitcnt lgkmcnt(2)\n"
    "s_add_u32 m0, s77, s59\n"
    "s_add_u32 s77, s77, 8192\n"
    "buffer_load_dwordx4 v1, s[36:39], s76 offen sc1 lds\n"
    "v_rcp_f32 v33, v33\n"
    "v_mfma_f32_16x16x32_bf16 v[68:71], a[136:139], v[28:31], v[68:71]\n"
    "ds_read_b128 v[28:31], v9 offset:19456\n"
    "v_add_f32 v35, 1.0, v35\n"
    "v_mfma_f32_16x16x32_bf16 v[40:43], a[144:147], v[16:19], v[40:43]\n"
    "v_rcp_f32 v34, v34\n"
    "v_mfma_f32_16x16x32_bf16 v[44:47], a[148:151], v[16:19], v[44:47]\n"
    "buffer_load_dwordx4 v1, s[36:39], s76 offen offset:1024 sc1 lds\n"
    "s_add_u32 s76, s76, 16384\n"
    "v_rcp_f32 v35, v35\n"
    "v_mfma_f32_16x16x32_bf16 v[64:67], a[156:159], v[16:19], v[64:67]\n"
    "s_waitcnt lgkmcnt(2)\n"
    "v_add_f32 v36, 1.0, v36\n"
    "v_add_f32 v37, 1.0, v37\n"
    "v_mfma_f32_16x16x32_bf16 v[68:71], a[160:163], v[16:19], v[68:71]\n"
    "ds_read_b128 v[16:19], v9 offset:21504\n"
    "v_add_f32 v38, 1.0, v38\n"
    "v_mfma_f32_16x16x32_bf16 v[40:43], a[168:171], v[20:23], v[40:43]\n"
    "v_rcp_f32 v36, v36\n"
    "v_mfma_f32_16x16x32_bf16 v[44:47], a[172:175], v[20:23], v[44:47]\n"
    "s_add_u32 m0, s77, s59\n"
    "s_add_u32 s77, s77, 8192\n"
    "buffer_load_dwordx4 v1, s[36:39], s76 offen sc1 lds\n"
    "v_rcp_f32 v37, v37\n"
    "v_mfma_f32_16x16x32_bf16 v[64:67], a[180:183], v[20:23], v[64:67]\n"
    "s_waitcnt lgkmcnt(2)\n"
    "v_rcp_f32 v38, v38\n"
    "v_mfma_f32_16x16x32_bf16 v[68:71], a[184:187], v[20:23], v[68:71]\n"
    "ds_read_b128 v[20:23], v9 offset:23552\n"
    "v_add_f32 v39, 1.0, v39\n"
    "v_mfma_f32_16x16x32_bf16 v[40:43], a[192:195], v[24:27], v[40:43]\n"
    "buffer_load_dwordx4 v1, s[36:39], s76 offen offset:1024 sc1 lds\n"
    "s_add_u32 s76, s76, 16384\n"
    "v_rcp_f32 v39, v39\n"
    "v_mfma_f32_16x16x32_bf16 v[44:47], a[196:199], v[24:27], v[44:47]\n"
    "v_mul_f32 v32, v32, v56\n"
    "v_mul_f32 v33, v33, v57\n"
    "v_mfma_f32_16x16x32_bf16 v[64:67], a[204:207], v[24:27], v[64:67]\n"
    "s_waitcnt lgkmcnt(2)\n"
    "v_mul_f32 v34, v34, v58\n"
    "v_mul_f32 v35, v35, v59\n"
    "v_mfma_f32_16x16x32_bf16 v[68:71], a[208:211], v[24:27], v[68:71]\n"
    "ds_read_b128 v[24:27], v9 offset:25600\n"
    "v_mul_f32 v36, v36, v60\n"
    "v_mfma_f32_16x16x32_bf16 v[40:43], a[216:219], v[28:31], v[40:43]\n"
    "s_mov_b32 m0, s77\n"
    "s_nop 0\n"
    "buffer_load_dwordx4 v8, s[36:39], s76 offen sc1 lds\n"
    "v_mul_f32 v37, v37, v61\n"
    "v_mul_f32 v38, v38, v62\n"
    "v_mfma_f32_16x16x32_bf16 v[44:47], a[220:223], v[28:31], v[44:47]\n"
    "v_mul_f32 v39, v39, v63\n"
    "v_cvt_pk_bf16_f32 v32, v32, v33\n"
    "v_mfma_f32_16x16x32_bf16 v[64:67], a[228:231], v[28:31], v[64:67]\n"
    "s_waitcnt lgkmcnt(2)\n"
    "v_cvt_pk_bf16_f32 v33, v34, v35\n"
    "v_cvt_pk_bf16_f32 v34, v36, v37\n"
    "v_mfma_f32_16x16x32_bf16 v[68:71], a[232:235], v[28:31], v[68:71]\n"
    "ds_read_b128 v[28:31], v9 offset:27648\n"
    "v_cvt_pk_bf16_f32 v35, v38, v39\n"
    "s_branch .LgUP_seg2_%=\n"
    ".LgUP_slow_%=:\n"
    "s_cmp_lg_u32 s70, 0\n"
    "s_cbranch_scc1 .LgUP_nolast_%=\n"
    "s_waitcnt vmcnt(0)\n"
    "v_add_u32 v13, v11, v3\n"
    "v_cmp_gt_i32 vcc, s71, v13\n"
    "s_nop 0\n"
    "s_or_b64 exec, vcc, 1\n"
    "buffer_atomic_smax v11, v2, s[36:39], 0 offen sc0 sc1\n"
    "s_mov_b64 exec, -1\n"
    ".LgUP_nolast_%=:\n"
    "v_mfma_f32_16x16x32_bf16 v[40:43], a[240:243], v[16:19], v[40:43]\n"
    "buffer_store_dwordx4 v[48:51], v4, s[36:39], s73 offen sc1\n"
    "v_mfma_f32_16x16x32_bf16 v[44:47], a[244:247], v[16:19], v[44:47]\n"
    "v_mfma_f32_16x16x32_bf16 v[64:67], a[252:255], v[16:19], v[64:67]\n"
    "s_waitcnt lgkmcnt(2)\n"
    "buffer_store_dwordx4 v[32:35], v15, s[36:39], s73 offen sc1\n"
    "v_mfma_f32_16x16x32_bf16 v[68:71], v[80:83], v[16:19], v[68:71]\n"
    "ds_read_b128 v[16:19], v9 offset:29696\n"
    "v_mfma_f32_16x16x32_bf16 v[40:43], v[88:91], v[20:23], v[40:43]\n"
    "v_mfma_f32_16x16x32_bf16 v[44:47], v[92:95], v[20:23], v[44:47]\n"
    "v_mfma_f32_16x16x32_bf16 v[64:67], v[100:103], v[20:23], v[64:67]\n"
    "s_waitcnt lgkmcnt(2)\n"
    "v_mfma_f32_16x16x32_bf16 v[68:71], v[104:107], v[20:23], v[68:71]\n"
    "ds_read_b128 v[20:23], v9 offset:31744\n"
    "v_mfma_f32_16x16x32_bf16 v[40:43], v[112:115], v[24:27], v[40:43]\n"
    "v_mfma_f32_16x16x32_bf16 v[44:47], v[116:119], v[24:27], v[44:47]\n"
    "v_mfma_f32_16x16x32_bf16 v[64:67], v[124:127], v[24:27], v[64:67]\n"
    "s_waitcnt lgkmcnt(2)\n"
    "v_mfma_f32_16x16x32_bf16 v[68:71], v[128:131], v[24:27], v[68:71]\n"
    "ds_read_b128 v[24:27], v9 offset:33792\n"
    "v_mfma_f32_16x16x32_bf16 v[40:43], v[136:139], v[28:31], v[40:43]\n"
    "v_mfma_f32_16x16x32_bf16 v[44:47], v[140:143], v[28:31], v[44:47]\n"
    "v_mfma_f32_16x16x32_bf16 v[64:67], v[148:151], v[28:31], v[64:67]\n"
    "s_waitcnt lgkmcnt(2)\n"
    "v_mfma_f32_16x16x32_bf16 v[68:71], v[152:155], v[28:31], v[68:71]\n"
    "ds_read_b128 v[28:31], v9 offset:35840\n"
    "v_mfma_f32_16x16x32_bf16 v[40:43], v[160:163], v[16:19], v[40:43]\n"
    "v_mfma_f32_16x16x32_bf16 v[44:47], v[164:167], v[16:19], v[44:47]\n"
    "v_mfma_f32_16x16x32_bf16 v[64:67], v[172:175], v[16:19], v[64:67]\n"
    "s_waitcnt lgkmcnt(2)\n"
    "v_mfma_f32_16x16x32_bf16 v[68:71], v[176:179], v[16:19], v[68:71]\n"
    "v_mfma_f32_16x16x32_bf16 v[40:43], v[184:187], v[20:23], v[40:43]\n"
    "v_mfma_f32_16x16x32_bf16 v[44:47], v[188:191], v[20:23], v[44:47]\n"
    "v_mfma_f32_16x16x32_bf16 v[64:67], v[196:199], v[20:23], v[64:67]\n"
    "s_waitcnt lgkmcnt(1)\n"
    "v_mfma_f32_16x16x32_bf16 v[68:71], v[200:203], v[20:23], v[68:71]\n"
    "v_mfma_f32_16x16x32_bf16 v[40:43], v[208:211], v[24:27], v[40:43]\n"
    "v_mfma_f32_16x16x32_bf16 v[44:47], v[212:215], v[24:27], v[44:47]\n"
    "v_mfma_f32_16x16x32_bf16 v[64:67], v[220:223], v[24:27], v[64:67]\n"
    "s_waitcnt lgkmcnt(0)\n"
    "v_mfma_f32_16x16x32_bf16 v[68:71], v[224:227], v[24:27], v[68:71]\n"
    "v_mfma_f32_16x16x32_bf16 v[40:43], v[232:235], v[28:31], v[40:43]\n"
    "v_mfma_f32_16x16x32_bf16 v[44:47], v[236:239], v[28:31], v[44:47]\n"
    "v_mfma_f32_16x16x32_bf16 v[64:67], v[244:247], v[28:31], v[64:67]\n"
    "v_mfma_f32_16x16x32_bf16 v[68:71], v[248:251], v[28:31], v[68:71]\n"
    "s_nop 3\n"
    "v_pk_mul_f32 v[64:65], v[40:41], v[64:65]\n"
    "v_pk_mul_f32 v[66:67], v[42:43], v[66:67]\n"
    "s_nop 1\n"
    "v_pk_mul_f32 v[68:69], v[44:45], v[68:69]\n"
    "v_pk_mul_f32 v[70:71], v[46:47], v[70:71]\n"
    "v_pk_mul_f32 v[40:41], v[40:41], s[64:65] op_sel_hi:[1,0]\n"
    "v_pk_mul_f32 v[42:43], v[42:43], s[64:65] op_sel_hi:[1,0]\n"
    "v_pk_mul_f32 v[44:45], v[44:45], s[64:65] op_sel_hi:[1,0]\n"
    "v_pk_mul_f32 v[46:47], v[46:47], s[64:65] op_sel_hi:[1,0]\n"
    "v_exp_f32 v40, v40\n"
    "v_exp_f32 v41, v41\n"
    "v_exp_f32 v42, v42\n"
    "v_exp_f32 v43, v43\n"
    "v_exp_f32 v44, v44\n"
    "v_exp_f32 v45, v45\n"
    "v_exp_f32 v46, v46\n"
    "v_exp_f32 v47, v47\n"
    "v_pk_add_f32 v[40:41], v[40:41], 1.0 op_sel_hi:[1,0]\n"
    "v_pk_add_f32 v[42:43], v[42:43], 1.0 op_sel_hi:[1,0]\n"
    "v_pk_add_f32 v[44:45], v[44:45], 1.0 op_sel_hi:[1,0]\n"
    "v_pk_add_f32 v[46:47], v[46:47], 1.0 op_sel_hi:[1,0]\n"
    "v_rcp_f32 v40, v40\n"
    "v_rcp_f32 v41, v41\n"
    "v_rcp_f32 v42, v42\n"
    "v_rcp_f32 v43, v43\n"
    "v_rcp_f32 v44, v44\n"
    "v_rcp_f32 v45, v45\n"
    "v_rcp_f32 v46, v46\n"
    "v_rcp_f32 v47, v47\n"
    "v_pk_mul_f32 v[40:41], v[40:41], v[64:65]\n"
    "v_pk_mul_f32 v[42:43], v[42:43], v[66:67]\n"
    "v_pk_mul_f32 v[44:45], v[44:45], v[68:69]\n"
    "v_pk_mul_f32 v[46:47], v[46:47], v[70:71]\n"
    "v_cvt_pk_bf16_f32 v40, v40, v41\n"
    "v_cvt_pk_bf16_f32 v41, v42, v43\n"
    "v_cvt_pk_bf16_f32 v42, v44, v45\n"
    "v_cvt_pk_bf16_f32 v43, v46, v47\n"
    "buffer_store_dwordx4 v[40:43], v15, s[36:39], s73 offen offset:1024 sc1\n"
    "s_mov_b32 s68, s71\n"
    "s_cmp_eq_u32 s70, 0\n"
    "s_cbranch_scc1 .LgUP_idle_%=\n"
    "s_cmp_eq_u32 s68, 64\n"
    "s_cbranch_scc1 .LgUP_done_%=\n"
    "s_waitcnt vmcnt(3)\n"
    ".LgUP_bar_%=:\n"
    "v_xor_b32 v9, s78, v9\n"
    "s_add_u32 s71, s68, 1\n"
    "s_cmp_eq_u32 s71, 64\n"
    "s_cselect_b32 s70, 2, 0\n"
    "s_barrier\n"
    "ds_read_b128 v[16:19], v9 offset:0\n"
    "ds_read_b128 v[20:23], v9 offset:1024\n"
    "ds_read_b128 v[24:27], v9 offset:2048\n"
    "ds_read_b128 v[28:31], v9 offset:3072\n"
    "v_add_u32 v13, v11, v3\n"
    "v_cmp_gt_i32 vcc, s71, v13\n"
    "s_nop 0\n"
    "s_or_b64 exec, vcc, 1\n"
    "buffer_atomic_smax v11, v2, s[36:39], 0 offen sc0 sc1\n"
    "s_mov_b64 exec, -1\n"
    "s_waitcnt lgkmcnt(3)\n"
    "v_mfma_f32_16x16x32_bf16 v[48:51], a[8:11], v[16:19], 0\n"
    "s_waitcnt lgkmcnt(2)\n"
    "s_sub_u32 s74, s68, 1\n"
    "v_mfma_f32_16x16x32_bf16 v[72:75], a[20:23], v[16:19], 0\n"
    "ds_read_b128 v[16:19], v9 offset:4096\n"
    "v_mfma_f32_16x16x32_bf16 v[52:55], a[8:11], v[20:23], 0\n"
    "s_waitcnt lgkmcnt(2)\n"
    "s_and_b32 s76, s71, s54\n"
    "v_mfma_f32_16x16x32_bf16 v[76:79], a[20:23], v[20:23], 0\n"
    "ds_read_b128 v[20:23], v9 offset:5120\n"
    "v_mfma_f32_16x16x32_bf16 v[48:51], a[32:35], v[24:27], v[48:51]\n"
    "s_waitcnt lgkmcnt(2)\n"
    "s_mul_i32 s76, s76, s53\n"
    "v_mfma_f32_16x16x32_bf16 v[72:75], a[44:47], v[24:27], v[72:75]\n"
    "ds_read_b128 v[24:27], v9 offset:6144\n"
    "v_mfma_f32_16x16x32_bf16 v[52:55], a[32:35], v[28:31], v[52:55]\n"
    "s_waitcnt lgkmcnt(2)\n"
    "s_add_u32 s76, s76, s52\n"
    "v_mfma_f32_16x16x32_bf16 v[76:79], a[44:47], v[28:31], v[76:79]\n"
    "ds_read_b128 v[28:31], v9 offset:7168\n"
    "v_mfma_f32_16x16x32_bf16 v[48:51], a[56:59], v[16:19], v[48:51]\n"
    "s_waitcnt lgkmcnt(2)\n"
    "s_and_b32 s77, s71, 1\n"
    "v_mfma_f32_16x16x32_bf16 v[72:75], a[68:71], v[16:19], v[72:75]\n"
    "ds_read_b128 v[16:19], v9 offset:8192\n"
    "v_mfma_f32_16x16x32_bf16 v[52:55], a[56:59], v[20:23], v[52:55]\n"
    "s_waitcnt lgkmcnt(2)\n"
    "s_mul_i32 s77, s77, 65536\n"
    "v_mfma_f32_16x16x32_bf16 v[76:79], a[68:71], v[20:23], v[76:79]\n"
    "ds_read_b128 v[20:23], v9 offset:9216\n"
    "v_mfma_f32_16x16x32_bf16 v[48:51], a[80:83], v[24:27], v[48:51]\n"
    "s_waitcnt lgkmcnt(2)\n"
    "s_add_u32 s77, s77, s59\n"
    "v_mfma_f32_16x16x32_bf16 v[72:75], a[92:95], v[24:27], v[72:75]\n"
    "ds_read_b128 v[24:27], v9 offset:10240\n"
    "v_mfma_f32_16x16x32_bf16 v[52:55], a[80:83], v[28:31], v[52:55]\n"
    "s_waitcnt lgkmcnt(2)\n"
    "v_mfma_f32_16x16x32_bf16 v[76:79], a[92:95], v[28:31], v[76:79]\n"
    "ds_read_b128 v[28:31], v9 offset:11264\n"
    "v_mfma_f32_16x16x32_bf16 v[48:51], a[104:107], v[16:19], v[48:51]\n"
    "s_waitcnt lgkmcnt(2)\n"
    "v_mfma_f32_16x16x32_bf16 v[72:75], a[116:119], v[16:19], v[72:75]\n"
    "ds_read_b128 v[16:19], v9 offset:12288\n"
    "v_mfma_f32_16x16x32_bf16 v[52:55], a[104:107], v[20:23], v[52:55]\n"
    "s_waitcnt lgkmcnt(2)\n"
    "v_mfma_f32_16x16x32_bf16 v[76:79], a[116:119], v[20:23], v[76:79]\n"
    "ds_read_b128 v[20:23], v9 offset:13312\n"
    "v_mfma_f32_16x16x32_bf16 v[48:51], a[128:131], v[24:27], v[48:51]\n"
    "s_waitcnt lgkmcnt(2)\n"
    "v_mfma_f32_16x16x32_bf16 v[72:75], a[140:143], v[24:27], v[72:75]\n"
    "ds_read_b128 v[24:27], v9 offset:14336\n"
    "v_mfma_f32_16x16x32_bf16 v[52:55], a[128:131], v[28:31], v[52:55]\n"
    "s_waitcnt lgkmcnt(2)\n"
    "v_mfma_f32_16x16x32_bf16 v[76:79], a[140:143], v[28:31], v[76:79]\n"
    "ds_read_b128 v[28:31], v9 offset:15360\n"
    "v_mfma_f32_16x16x32_bf16 v[48:51], a[152:155], v[16:19], v[48:51]\n"
    "s_waitcnt lgkmcnt(2)\n"
    "v_mfma_f32_16x16x32_bf16 v[72:75], a[164:167], v[16:19], v[72:75]\n"
    "ds_read_b128 v[16:19], v9 offset:16384\n"
    "v_mfma_f32_16x16x32_bf16 v[52:55], a[152:155], v[20:23], v[52:55]\n"
    "s_waitcnt lgkmcnt(2)\n"
    "v_mfma_f32_16x16x32_bf16 v[76:79], a[164:167], v[20:23], v[76:79]\n"
    "ds_read_b128 v[20:23], v9 offset:17408\n"
    "v_mfma_f32_16x16x32_bf16 v[48:51], a[176:179], v[24:27], v[48:51]\n"
    "s_waitcnt lgkmcnt(2)\n"
    "v_mfma_f32_16x16x32_bf16 v[72:75], a[188:191], v[24:27], v[72:75]\n"
    "ds_read_b128 v[24:27], v9 offset:18432\n"
    "v_mfma_f32_16x16x32_bf16 v[52:55], a[176:179], v[28:31], v[52:55]\n"
    "s_waitcnt lgkmcnt(2)\n"
    "v_mfma_f32_16x16x32_bf16 v[76:79], a[188:191], v[28:31], v[76:79]\n"
    "ds_read_b128 v[28:31], v9 offset:19456\n"
    "v_mfma_f32_16x16x32_bf16 v[48:51], a[200:203], v[16:19], v[48:51]\n"
    "s_waitcnt lgkmcnt(2)\n"
    "v_mfma_f32_16x16x32_bf16 v[72:75], a[212:215], v[16:19], v[72:75]\n"
    "ds_read_b128 v[16:19], v9 offset:20480\n"
    "v_mfma_f32_16x16x32_bf16 v[52:55], a[200:203], v[20:23], v[52:55]\n"
    "s_waitcnt lgkmcnt(2)\n"
    "v_mfma_f32_16x16x32_bf16 v[76:79], a[212:215], v[20:23], v[76:79]\n"
    "ds_read_b128 v[20:23], v9 offset:21504\n"
    "v_mfma_f32_16x16x32_bf16 v[48:51], a[224:227], v[24:27], v[48:51]\n"
    "s_waitcnt lgkmcnt(2)\n"
    "v_mfma_f32_16x16x32_bf16 v[72:75], a[236:239], v[24:27], v[72:75]\n"
    "ds_read_b128 v[24:27], v9 offset:22528\n"
    "v_mfma_f32_16x16x32_bf16 v[52:55], a[224:227], v[28:31], v[52:55]\n"
    "s_waitcnt lgkmcnt(2)\n"
    "v_mfma_f32_16x16x32_bf16 v[76:79], a[236:239], v[28:31], v[76:79]\n"
    "ds_read_b128 v[28:31], v9 offset:23552\n"
    "v_mfma_f32_16x16x32_bf16 v[48:51], a[248:251], v[16:19], v[48:51]\n"
    "s_waitcnt lgkmcnt(2)\n"
    "v_mfma_f32_16x16x32_bf16 v[72:75], v[84:87], v[16:19], v[72:75]\n"
    "ds_read_b128 v[16:19], v9 offset:24576\n"
    "v_mfma_f32_16x16x32_bf16 v[52:55], a[248:251], v[20:23], v[52:55]\n"
    "s_waitcnt lgkmcnt(2)\n"
    "v_mfma_f32_16x16x32_bf16 v[76:79], v[84:87], v[20:23], v[76:79]\n"
    "ds_read_b128 v[20:23], v9 offset:25600\n"
    "v_mfma_f32_16x16x32_bf16 v[48:51], v[96:99], v[24:27], v[48:51]\n"
    "s_waitcnt lgkmcnt(2)\n"
    "v_mfma_f32_16x16x32_bf16 v[72:75], v[108:111], v[24:27], v[72:75]\n"
    "ds_read_b128 v[24:27], v9 offset:26624\n"
    "v_mfma_f32_16x16x32_bf16 v[52:55], v[96:99], v[28:31], v[52:55]\n"
    "s_waitcnt lgkmcnt(2)\n"
    "v_mfma_f32_16x16x32_bf16 v[76:79], v[108:111], v[28:31], v[76:79]\n"
    "ds_read_b128 v[28:31], v9 offset:27648\n"
    "v_mfma_f32_16x16x32_bf16 v[48:51], v[120:123], v[16:19], v[48:51]\n"
    "s_waitcnt lgkmcnt(2)\n"
    "v_mfma_f32_16x16x32_bf16 v[72:75], v[132:135], v[16:19], v[72:75]\n"
    "ds_read_b128 v[16:19], v9 offset:28672\n"
    "v_mfma_f32_16x16x32_bf16 v[52:55], v[120:123], v[20:23], v[52:55]\n"
    "s_waitcnt lgkmcnt(2)\n"
    "v_mfma_f32_16x16x32_bf16 v[76:79], v[132:135], v[20:23], v[76:79]\n"
    "ds_read_b128 v[20:23], v9 offset:29696\n"
    "v_mfma_f32_16x16x32_bf16 v[48:51], v[144:147], v[24:27], v[48:51]\n"
    "s_waitcnt lgkmcnt(2)\n"
    "v_mfma_f32_16x16x32_bf16 v[72:75], v[156:159], v[24:27], v[72:75]\n"
    "ds_read_b128 v[24:27], v9 offset:30720\n"
    "v_mfma_f32_16x16x32_bf16 v[52:55], v[144:147], v[28:31], v[52:55]\n"
    "s_waitcnt lgkmcnt(2)\n"
    "v_mfma_f32_16x16x32_bf16 v[76:79], v[156:159], v[28:31], v[76:79]\n"
    "ds_read_b128 v[28:31], v9 offset:31744\n"
    "v_mfma_f32_16x16x32_bf16 v[48:51], v[168:171], v[16:19], v[48:51]\n"
    "s_waitcnt lgkmcnt(2)\n"
    "v_mfma_f32_16x16x32_bf16 v[72:75], v[180:183], v[16:19], v[72:75]\n"
    "ds_read_b128 v[16:19], v9 offset:32768\n"
    "v_mfma_f32_16x16x32_bf16 v[52:55], v[168:171], v[20:23], v[52:55]\n"
    "s_waitcnt lgkmcnt(2)\n"
    "v_mfma_f32_16x16x32_bf16 v[76:79], v[180:183], v[20:23], v[76:79]\n"
    "ds_read_b128 v[20:23], v9 offset:33792\n"
    "v_mfma_f32_16x16x32_bf16 v[48:51], v[192:195], v[24:27], v[48:51]\n"
    "s_waitcnt lgkmcnt(2)\n"
    "v_mfma_f32_16x16x32_bf16 v[72:75], v[204:207], v[24:27], v[72:75]\n"
    "ds_read_b128 v[24:27], v9 offset:34816\n"
    "v_mfma_f32_16x16x32_bf16 v[52:55], v[192:195], v[28:31], v[52:55]\n"
    "s_waitcnt lgkmcnt(2)\n"
    "v_mfma_f32_16x16x32_bf16 v[76:79], v[204:207], v[28:31], v[76:79]\n"
    "ds_read_b128 v[28:31], v9 offset:35840\n"
    "v_mfma_f32_16x16x32_bf16 v[48:51], v[216:219], v[16:19], v[48:51]\n"
    "s_waitcnt lgkmcnt(2)\n"
    "v_mfma_f32_16x16x32_bf16 v[72:75], v[228:231], v[16:19], v[72:75]\n"
    "ds_read_b128 v[16:19], v9 offset:0\n"
    "v_mfma_f32_16x16x32_bf16 v[52:55], v[216:219], v[20:23], v[52:55]\n"
    "s_waitcnt lgkmcnt(2)\n"
    "v_mfma_f32_16x16x32_bf16 v[76:79], v[228:231], v[20:23], v[76:79]\n"
    "ds_read_b128 v[20:23], v9 offset:2048\n"
    "v_mfma_f32_16x16x32_bf16 v[48:51], v[240:243], v[24:27], v[48:51]\n"
    "s_waitcnt lgkmcnt(2)\n"
    "v_mfma_f32_16x16x32_bf16 v[72:75], v[252:255], v[24:27], v[72:75]\n"
    "ds_read_b128 v[24:27], v9 offset:4096\n"
    "v_mfma_f32_16x16x32_bf16 v[52:55], v[240:243], v[28:31], v[52:55]\n"
    "s_waitcnt lgkmcnt(2)\n"
    "v_mfma_f32_16x16x32_bf16 v[76:79], v[252:255], v[28:31], v[76:79]\n"
    "ds_read_b128 v[28:31], v9 offset:6144\n"
    "v_mfma_f32_16x16x32_bf16 v[32:35], a[0:3], v[16:19], 0\n"
    "v_mfma_f32_16x16x32_bf16 v[36:39], a[4:7], v[16:19], 0\n"
    "v_mul_f32 v72, v48, v72\n"
    "v_mul_f32 v73, v49, v73\n"
    "v_mfma_f32_16x16x32_bf16 v[56:59], a[12:15], v[16:19], 0\n"
    "s_waitcnt lgkmcnt(2)\n"
    "v_mul_f32 v74, v50, v74\n"
    "v_mul_f32 v75, v51, v75\n"
    "v_mfma_f32_16x16x32_bf16 v[60:63], a[16:19], v[16:19], 0\n"
    "ds_read_b128 v[16:19], v9 offset:8192\n"
    "v_mul_f32 v76, v52, v76\n"
    "v_mfma_f32_16x16x32_bf16 v[32:35], a[24:27], v[20:23], v[32:35]\n"
    "v_mul_f32 v77, v53, v77\n"
    "v_mul_f32 v78, v54, v78\n"
    "v_mfma_f32_16x16x32_bf16 v[36:39], a[28:31], v[20:23], v[36:39]\n"
    "v_mul_f32 v79, v55, v79\n"
    "v_mul_f32 v48, 0xbfb8aa3b, v48\n"
    "v_mfma_f32_16x16x32_bf16 v[56:59], a[36:39], v[20:23], v[56:59]\n"
    "s_waitcnt lgkmcnt(2)\n"
    "v_exp_f32 v48, v48\n"
    "v_mfma_f32_16x16x32_bf16 v[60:63], a[40:43], v[20:23], v[60:63]\n"
    "ds_read_b128 v[20:23], v9 offset:10240\n"
    "v_mul_f32 v49, 0xbfb8aa3b, v49\n"
    "v_mfma_f32_16x16x32_bf16 v[32:35], a[48:51], v[24:27], v[32:35]\n"
    "v_exp_f32 v49, v49\n"
    "v_mfma_f32_16x16x32_bf16 v[36:39], a[52:55], v[24:27], v[36:39]\n"
    "v_mul_f32 v50, 0xbfb8aa3b, v50\n"
    "v_mul_f32 v51, 0xbfb8aa3b, v51\n"
    "v_mfma_f32_16x16x32_bf16 v[56:59], a[60:63], v[24:27], v[56:59]\n"
    "s_waitcnt lgkmcnt(2)\n"
    "v_exp_f32 v50, v50\n"
    "v_mfma_f32_16x16x32_bf16 v[60:63], a[64:67], v[24:27], v[60:63]\n"
    "ds_read_b128 v[24:27], v9 offset:12288\n"
    "v_mul_f32 v52, 0xbfb8aa3b, v52\n"
    "v_mfma_f32_16x16x32_bf16 v[32:35], a[72:75], v[28:31], v[32:35]\n"
    "v_exp_f32 v51, v51\n"
    "v_mfma_f32_16x16x32_bf16 v[36:39], a[76:79], v[28:31], v[36:39]\n"
    "v_exp_f32 v52, v52\n"
    "v_mov_b32 v12, s74\n"
    "v_mfma_f32_16x16x32_bf16 v[56:59], a[84:87], v[28:31], v[56:59]\n"
    "s_waitcnt lgkmcnt(2)\n"
    "s_waitcnt vmcnt(0)\n"
    "v_mul_f32 v53, 0xbfb8aa3b, v53\n"
    "v_mul_f32 v54, 0xbfb8aa3b, v54\n"
    "v_add_u32 v13, v11, v3\n"
    "v_mfma_f32_16x16x32_bf16 v[60:63], a[88:91], v[28:31], v[60:63]\n"
    "ds_read_b128 v[28:31], v9 offset:14336\n"
    "buffer_store_dword v12, v14, s[36:39], 0 offen sc1\n"
    "v_mul_f32 v55, 0xbfb8aa3b, v55\n"
    "v_cmp_gt_i32 vcc, s71, v13\n"
    "v_mfma_f32_16x16x32_bf16 v[32:35], a[96:99], v[16:19], v[32:35]\n"
    "v_exp_f32 v53, v53\n"
    "v_mfma_f32_16x16x32_bf16 v[36:39], a[100:103], v[16:19], v[36:39]\n"
    "v_exp_f32 v54, v54\n"
    "v_mfma_f32_16x16x32_bf16 v[56:59], a[108:111], v[16:19], v[56:59]\n"
    "s_waitcnt lgkmcnt(2)\n"
    "v_exp_f32 v55, v55\n"
    "s_branch .LgUP_mid_%=\n"
    ".LgUP_idle_%=:\n"
    "s_waitcnt vmcnt(3)\n"
    "v_add_u32 v13, v11, v3\n"
    "v_cmp_gt_i32 vcc, s71, v13\n"
    "s_cbranch_vccnz .LgUP_idle2_%=\n"
    "s_and_b32 s76, s71, s54\n"
    "s_mul_i32 s76, s76, s53\n"
    "s_add_u32 s76, s76, s52\n"
    "s_and_b32 s77, s71, 1\n"
    "s_mul_i32 s77, s77, 65536\n"
    "s_add_u32 s77, s77, s59\n"
    "s_add_u32 m0, s77, s59\n"
    "s_add_u32 s77, s77, 8192\n"
    "buffer_load_dwordx4 v1, s[36:39], s76 offen sc1 lds\n"
    "buffer_load_dwordx4 v1, s[36:39], s76 offen offset:1024 sc1 lds\n"
    "s_add_u32 s76, s76, 16384\n"
    "s_add_u32 m0, s77, s59\n"
    "s_add_u32 s77, s77, 8192\n"
    "buffer_load_dwordx4 v1, s[36:39], s76 offen sc1 lds\n"
    "buffer_load_dwordx4 v1, s[36:39], s76 offen offset:1024 sc1 lds\n"
    "s_add_u32 s76, s76, 16384\n"
    "s_add_u32 m0, s77, s59\n"
    "s_add_u32 s77, s77, 8192\n"
    "buffer_load_dwordx4 v1, s[36:39], s76 offen sc1 lds\n"
    "buffer_load_dwordx4 v1, s[36:39], s76 offen offset:1024 sc1 lds\n"
    "s_add_u32 s76, s76, 16384\n"
    "s_add_u32 m0, s77, s59\n"
    "s_add_u32 s77, s77, 8192\n"
    "buffer_load_dwordx4 v1, s[36:39], s76 offen sc1 lds\n"
    "buffer_load_dwordx4 v1, s[36:39], s76 offen offset:1024 sc1 lds\n"
    "s_add_u32 s76, s76, 16384\n"
    "s_mov_b32 m0, s77\n"
    "s_nop 0\n"
    "buffer_load_dwordx4 v8, s[36:39], s76 offen sc1 lds\n"
    "s_waitcnt vmcnt(9)\n"
    "s_sub_u32 s74, s68, 1\n"
    "v_mov_b32 v12, s74\n"
    "s_mov_b64 exec, s[62:63]\n"
    "buffer_store_dword v12, v14, s[36:39], 0 offen sc1\n"
    "s_mov_b64 exec, -1\n"
    "s_waitcnt vmcnt(1)\n"
    "s_branch .LgUP_bar_%=\n"
    ".LgUP_idle2_%=:\n"
    "s_or_b64 exec, vcc, 1\n"
    "buffer_atomic_smax v11, v2, s[36:39], 0 offen sc0 sc1\n"
    "s_mov_b64 exec, -1\n"
    "s_waitcnt vmcnt(1)\n"
    "s_sub_u32 s74, s68, 1\n"
    "v_mov_b32 v12, s74\n"
    "s_mov_b64 exec, s[62:63]\n"
    "buffer_store_dword v12, v14, s[36:39], 0 offen sc1\n"
    "s_mov_b64 exec, -1\n"
    "s_waitcnt vmcnt(1)\n"
    "s_branch .LgUP_block1_%=\n"
    ".LgUP_block_%=:\n"
    "v_add_u32 v13, v11, v3\n"
    "v_cmp_gt_i32 vcc, s71, v13\n"
    "s_nop 0\n"
    "s_or_b64 exec, vcc, 1\n"
    "buffer_atomic_smax v11, v2, s[36:39], 0 offen sc0 sc1\n"
    "s_mov_b64 exec, -1\n"
    "s_waitcnt vmcnt(0)\n"
    ".LgUP_block1_%=:\n"
    "v_add_u32 v13, v11, v3\n"
    "v_cmp_gt_i32 vcc, s71, v13\n"
    "s_cbranch_vccz .LgUP_go_%=\n"
    "s_sleep 4\n"
    "s_sub_u32 s72, s72, 1\n"
    "s_cbranch_scc0 .LgUP_block_%=\n"
    "s_mov_b32 s72, 0\n"
    ".LgUP_go_%=:\n"
    "s_and_b32 s76, s71, s54\n"
    "s_mul_i32 s76, s76, s53\n"
    "s_add_u32 s76, s76, s52\n"
    "s_and_b32 s77, s71, 1\n"
    "s_mul_i32 s77, s77, 65536\n"
    "s_add_u32 s77, s77, s59\n"
    "s_add_u32 m0, s77, s59\n"
    "s_add_u32 s77, s77, 8192\n"
    "buffer_load_dwordx4 v1, s[36:39], s76 offen sc1 lds\n"
    "buffer_load_dwordx4 v1, s[36:39], s76 offen offset:1024 sc1 lds\n"
    "s_add_u32 s76, s76, 16384\n"
    "s_add_u32 m0, s77, s59\n"
    "s_add_u32 s77, s77, 8192\n"
    "buffer_load_dwordx4 v1, s[36:39], s76 offen sc1 lds\n"
    "buffer_load_dwordx4 v1, s[36:39], s76 offen offset:1024 sc1 lds\n"
    "s_add_u32 s76, s76, 16384\n"
    "s_add_u32 m0, s77, s59\n"
    "s_add_u32 s77, s77, 8192\n"
    "buffer_load_dwordx4 v1, s[36:39], s76 offen sc1 lds\n"
    "buffer_load_dwordx4 v1, s[36:39], s76 offen offset:1024 sc1 lds\n"
    "s_add_u32 s76, s76, 16384\n"
    "s_add_u32 m0, s77, s59\n"
    "s_add_u32 s77, s77, 8192\n"
    "buffer_load_dwordx4 v1, s[36:39], s76 offen sc1 lds\n"
    "buffer_load_dwordx4 v1, s[36:39], s76 offen offset:1024 sc1 lds\n"
    "s_add_u32 s76, s76, 16384\n"
    "s_mov_b32 m0, s77\n"
    "s_nop 0\n"
    "buffer_load_dwordx4 v8, s[36:39], s76 offen sc1 lds\n"
    "s_waitcnt vmcnt(0)\n"
    "s_branch .LgUP_bar_%=\n"
    ".LgUP_FIN_%=:\n"
  );
}
DEVI void role_wo(const Params& p, int L, int j, int i, u32 dbg) {
  const int lane = threadIdx.x & 63, w = threadIdx.x >> 6, e0 = j * 1024 * 576 + i * 192;
  GEMM_DECL;
  g_w0 = make_srd(p.Wo + e0, 4096u * 576 * 2 - e0 * 2); g_w1 = g_w0; g_w2 = g_w0;
  g_ldw = 1152;
  g_in_base = pgb(L, B_O + 8 * j); g_in_stride = SLOT; g_in_mask = RO - 1;
  g_out_base = pgb(L, B_PA + 5 * j); g_out_kb = 12 * i; g_out_mask = RP - 1;
  g_flag = lane < 4 ? mbox(L, lane, SL_REDA, 12 * j + 4 * i + w) : ((lane < 12 && w == 0) ? mbox_att(L, j, lane - 4, 4 + i) : OOB);
  g_paddr = mbox(L, j, SL_WO + i, lane);
  g_pbias = lane < 8 ? 0 : (lane < 24 ? RP : 1000);
  v16u sa, sb, va;
  gemm_args(GEMM_ARGS, p, dbg, sa, sb, va);
  RUN_ASM(
    "s_nop 4\n"
    "v_add_u32 v8, s59, v0\n"
    "s_add_u32 m0, s59, 0\n"
    "s_mov_b32 s74, 0\n"
    "buffer_load_dwordx4 v5, s[40:43], s74 offen lds\n"
    "s_add_u32 m0, s59, 4096\n"
    "s_mov_b32 s74, 128\n"
    "buffer_load_dwordx4 v5, s[40:43], s74 offen lds\n"
    "s_add_u32 m0, s59, 8192\n"
    "s_mov_b32 s74, 256\n"
    "buffer_load_dwordx4 v5, s[40:43], s74 offen lds\n"
    "s_add_u32 m0, s59, 12288\n"
    "s_mov_b32 s74, 36864\n"
    "buffer_load_dwordx4 v5, s[40:43], s74 offen lds\n"
    "s_add_u32 m0, s59, 16384\n"
    "s_mov_b32 s74, 36992\n"
    "buffer_load_dwordx4 v5, s[40:43], s74 offen lds\n"
    "s_add_u32 m0, s59, 20480\n"
    "s_mov_b32 s74, 37120\n"
    "buffer_load_dwordx4 v5, s[40:43], s74 offen lds\n"
    "s_add_u32 m0, s59, 24576\n"
    "s_mov_b32 s74, 73728\n"
    "buffer_load_dwordx4 v5, s[40:43], s74 offen lds\n"
    "s_add_u32 m0, s59, 28672\n"
    "s_mov_b32 s74, 73856\n"
    "buffer_load_dwordx4 v5, s[40:43], s74 offen lds\n"
    "s_add_u32 m0, s59, 32768\n"
    "s_mov_b32 s74, 73984\n"
    "buffer_load_dwordx4 v5, s[40:43], s74 offen lds\n"
    "s_add_u32 m0, s59, 36864\n"
    "s_mov_b32 s74, 110592\n"
    "buffer_load_dwordx4 v5, s[40:43], s74 offen lds\n"
    "s_add_u32 m0, s59, 40960\n"
    "s_mov_b32 s74, 110720\n"
    "buffer_load_dwordx4 v5, s[40:43], s74 offen lds\n"
    "s_add_u32 m0, s59, 45056\n"
    "s_mov_b32 s74, 110848\n"
    "buffer_load_dwordx4 v5, s[40:43], s74 offen lds\n"
    "s_add_u32 m0, s59, 49152\n"
    "s_mov_b32 s74, 147456\n"
    "buffer_load_dwordx4 v5, s[40:43], s74 offen lds\n"
    "s_add_u32 m0, s59, 53248\n"
    "s_mov_b32 s74, 147584\n"
    "buffer_load_dwordx4 v5, s[40:43], s74 offen lds\n"
    "s_add_u32 m0, s59, 57344\n"
    "s_mov_b32 s74, 147712\n"
    "buffer_load_dwordx4 v5, s[40:43], s74 offen lds\n"
    "s_add_u32 m0, s59, 61440\n"
    "s_mov_b32 s74, 184320\n"
    "buffer_load_dwordx4 v5, s[40:43], s74 offen lds\n"
    "s_add_u32 m0, s59, 65536\n"
    "s_mov_b32 s74, 184448\n"
    "buffer_load_dwordx4 v5, s[40:43], s74 offen lds\n"
    "s_add_u32 m0, s59, 69632\n"
    "s_mov_b32 s74, 184576\n"
    "buffer_load_dwordx4 v5, s[40:43], s74 offen lds\n"
    "s_add_u32 m0, s59, 73728\n"
    "s_mov_b32 s74, 221184\n"
    "buffer_load_dwordx4 v5, s[40:43], s74 offen lds\n"
    "s_add_u32 m0, s59, 77824\n"
    "s_mov_b32 s74, 221312\n"
    "buffer_load_dwordx4 v5, s[40:43], s74 offen lds\n"
    "s_add_u32 m0, s59, 81920\n"
    "s_mov_b32 s74, 221440\n"
    "buffer_load_dwordx4 v5, s[40:43], s74 offen lds\n"
    "s_add_u32 m0, s59, 86016\n"
    "s_mov_b32 s74, 258048\n"
    "buffer_load_dwordx4 v5, s[40:43], s74 offen lds\n"
    "s_add_u32 m0, s59, 90112\n"
    "s_mov_b32 s74, 258176\n"
    "buffer_load_dwordx4 v5, s[40:43], s74 offen lds\n"
    "s_add_u32 m0, s59, 94208\n"
    "s_mov_b32 s74, 258304\n"
    "buffer_load_dwordx4 v5, s[40:43], s74 offen lds\n"
    "s_add_u32 m0, s59, 98304\n"
    "s_mov_b32 s74, 294912\n"
    "buffer_load_dwordx4 v5, s[40:43], s74 offen lds\n"
    "s_add_u32 m0, s59, 102400\n"
    "s_mov_b32 s74, 295040\n"
    "buffer_load_dwordx4 v5, s[40:43], s74 offen lds\n"
    "s_add_u32 m0, s59, 106496\n"
    "s_mov_b32 s74, 295168\n"
    "buffer_load_dwordx4 v5, s[40:43], s74 offen lds\n"
    "s_add_u32 m0, s59, 110592\n"
    "s_mov_b32 s74, 331776\n"
    "buffer_load_dwordx4 v5, s[40:43], s74 offen lds\n"
    "s_add_u32 m0, s59, 114688\n"
    "s_mov_b32 s74, 331904\n"
    "buffer_load_dwordx4 v5, s[40:43], s74 offen lds\n"
    "s_add_u32 m0, s59, 118784\n"
    "s_mov_b32 s74, 332032\n"
    "buffer_load_dwordx4 v5, s[40:43], s74 offen lds\n"
    "v_add_u32 v16, 49152, v6\n"
    "v_add_u32 v17, 98304, v6\n"
    "v_add_u32 v18, 49152, v7\n"
    "v_add_u32 v19, 98304, v7\n"
    "s_waitcnt vmcnt(24) lgkmcnt(0)\n"
    "s_barrier\n"
    "s_add_u32 m0, s59, 122880\n"
    "s_mov_b32 s74, 368640\n"
    "buffer_load_dwordx4 v5, s[40:43], s74 offen lds\n"
    "s_add_u32 m0, s59, 126976\n"
    "s_mov_b32 s74, 368768\n"
    "buffer_load_dwordx4 v5, s[40:43], s74 offen lds\n"
    "s_add_u32 m0, s59, 131072\n"
    "s_mov_b32 s74, 368896\n"
    "buffer_load_dwordx4 v5, s[40:43], s74 offen lds\n"
    "s_add_u32 m0, s59, 135168\n"
    "s_mov_b32 s74, 405504\n"
    "buffer_load_dwordx4 v5, s[40:43], s74 offen lds\n"
    "s_add_u32 m0, s59, 139264\n"
    "s_mov_b32 s74, 405632\n"
    "buffer_load_dwordx4 v5, s[40:43], s74 offen lds\n"
    "s_add_u32 m0, s59, 143360\n"
    "s_mov_b32 s74, 405760\n"
    "buffer_load_dwordx4 v5, s[40:43], s74 offen lds\n"
    "ds_read_b64_tr_b16 a[0:1], v6 offset:0\n"
    "ds_read_b64_tr_b16 a[2:3], v6 offset:512\n"
    "ds_read_b64_tr_b16 a[4:5], v6 offset:8\n"
    "ds_read_b64_tr_b16 a[6:7], v6 offset:520\n"
    "ds_read_b64_tr_b16 a[8:9], v7 offset:0\n"
    "ds_read_b64_tr_b16 a[10:11], v7 offset:512\n"
    "ds_read_b64_tr_b16 a[12:13], v6 offset:12288\n"
    "ds_read_b64_tr_b16 a[14:15], v6 offset:12800\n"
    "ds_read_b64_tr_b16 a[16:17], v6 offset:12296\n"
    "ds_read_b64_tr_b16 a[18:19], v6 offset:12808\n"
    "ds_read_b64_tr_b16 a[20:21], v7 offset:12288\n"
    "ds_read_b64_tr_b16 a[22:23], v7 offset:12800\n"
    "s_waitcnt vmcnt(24) lgkmcnt(0)\n"
    "s_barrier\n"
    "s_add_u32 m0, s59, 0\n"
    "s_mov_b32 s74, 442368\n"
    "buffer_load_dwordx4 v5, s[40:43], s74 offen lds\n"
    "s_add_u32 m0, s59, 4096\n"
    "s_mov_b32 s74, 442496\n"
    "buffer_load_dwordx4 v5, s[40:43], s74 offen lds\n"
    "s_add_u32 m0, s59, 8192\n"
    "s_mov_b32 s74, 442624\n"
    "buffer_load_dwordx4 v5, s[40:43], s74 offen lds\n"
    "s_add_u32 m0, s59, 12288\n"
    "s_mov_b32 s74, 479232\n"
    "buffer_load_dwordx4 v5, s[40:43], s74 offen lds\n"
    "s_add_u32 m0, s59, 16384\n"
    "s_mov_b32 s74, 479360\n"
    "buffer_load_dwordx4 v5, s[40:43], s74 offen lds\n"
    "s_add_u32 m0, s59, 20480\n"
    "s_mov_b32 s74, 479488\n"
    "buffer_load_dwordx4 v5, s[40:43], s74 offen lds\n"
    "ds_read_b64_tr_b16 a[24:25], v6 offset:24576\n"
    "ds_read_b64_tr_b16 a[26:27], v6 offset:25088\n"
    "ds_read_b64_tr_b16 a[28:29], v6 offset:24584\n"
    "ds_read_b64_tr_b16 a[30:31], v6 offset:25096\n"
    "ds_read_b64_tr_b16 a[32:33], v7 offset:24576\n"
    "ds_read_b64_tr_b16 a[34:35], v7 offset:25088\n"
    "ds_read_b64_tr_b16 a[36:37], v6 offset:36864\n"
    "ds_read_b64_tr_b16 a[38:39], v6 offset:37376\n"
    "ds_read_b64_tr_b16 a[40:41], v6 offset:36872\n"
    "ds_read_b64_tr_b16 a[42:43], v6 offset:37384\n"
    "ds_read_b64_tr_b16 a[44:45], v7 offset:36864\n"
    "ds_read_b64_tr_b16 a[46:47], v7 offset:37376\n"
    "s_waitcnt vmcnt(24) lgkmcnt(0)\n"
    "s_barrier\n"
    "s_add_u32 m0, s59, 24576\n"
    "s_mov_b32 s74, 516096\n"
    "buffer_load_dwordx4 v5, s[40:43], s74 offen lds\n"
    "s_add_u32 m0, s59, 28672\n"
    "s_mov_b32 s74, 516224\n"
    "buffer_load_dwordx4 v5, s[40:43], s74 offen lds\n"
    "s_add_u32 m0, s59, 32768\n"
    "s_mov_b32 s74, 516352\n"
    "buffer_load_dwordx4 v5, s[40:43], s74 offen lds\n"
    "s_add_u32 m0, s59, 36864\n"
    "s_mov_b32 s74, 552960\n"
    "buffer_load_dwordx4 v5, s[40:43], s74 offen lds\n"
    "s_add_u32 m0, s59, 40960\n"
    "s_mov_b32 s74, 553088\n"
    "buffer_load_dwordx4 v5, s[40:43], s74 offen lds\n"
    "s_add_u32 m0, s59, 45056\n"
    "s_mov_b32 s74, 553216\n"
    "buffer_load_dwordx4 v5, s[40:43], s74 offen lds\n"
    "ds_read_b64_tr_b16 a[48:49], v16 offset:0\n"
    "ds_read_b64_tr_b16 a[50:51], v16 offset:512\n"
    "ds_read_b64_tr_b16 a[52:53], v16 offset:8\n"
    "ds_read_b64_tr_b16 a[54:55], v16 offset:520\n"
    "ds_read_b64_tr_b16 a[56:57], v18 offset:0\n"
    "ds_read_b64_tr_b16 a[58:59], v18 offset:512\n"
    "ds_read_b64_tr_b16 a[60:61], v16 offset:12288\n"
    "ds_read_b64_tr_b16 a[62:63], v16 offset:12800\n"
    "ds_read_b64_tr_b16 a[64:65], v16 offset:12296\n"
    "ds_read_b64_tr_b16 a[66:67], v16 offset:12808\n"
    "ds_read_b64_tr_b16 a[68:69], v18 offset:12288\n"
    "ds_read_b64_tr_b16 a[70:71], v18 offset:12800\n"
    "s_waitcnt vmcnt(24) lgkmcnt(0)\n"
    "s_barrier\n"
    "s_add_u32 m0, s59, 49152\n"
    "s_mov_b32 s74, 589824\n"
    "buffer_load_dwordx4 v5, s[40:43], s74 offen lds\n"
    "s_add_u32 m0, s59, 53248\n"
    "s_mov_b32 s74, 589952\n"
    "buffer_load_dwordx4 v5, s[40:43], s74 offen lds\n"
    "s_add_u32 m0, s59, 57344\n"
    "s_mov_b32 s74, 590080\n"
    "buffer_load_dwordx4 v5, s[40:43], s74 offen lds\n"
    "s_add_u32 m0, s59, 61440\n"
    "s_mov_b32 s74, 626688\n"
    "buffer_load_dwordx4 v5, s[40:43], s74 offen lds\n"
    "s_add_u32 m0, s59, 65536\n"
    "s_mov_b32 s74, 626816\n"
    "buffer_load_dwordx4 v5, s[40:43], s74 offen lds\n"
    "s_add_u32 m0, s59, 69632\n"
    "s_mov_b32 s74, 626944\n"
    "buffer_load_dwordx4 v5, s[40:43], s74 offen lds\n"
    "ds_read_b64_tr_b16 a[72:73], v16 offset:24576\n"
    "ds_read_b64_tr_b16 a[74:75], v16 offset:25088\n"
    "ds_read_b64_tr_b16 a[76:77], v16 offset:24584\n"
    "ds_read_b64_tr_b16 a[78:79], v16 offset:25096\n"
    "ds_read_b64_tr_b16 a[80:81], v18 offset:24576\n"
    "ds_read_b64_tr_b16 a[82:83], v18 offset:25088\n"
    "ds_read_b64_tr_b16 a[84:85], v16 offset:36864\n"
    "ds_read_b64_tr_b16 a[86:87], v16 offset:37376\n"
    "ds_read_b64_tr_b16 a[88:89], v16 offset:36872\n"
    "ds_read_b64_tr_b16 a[90:91], v16 offset:37384\n"
    "ds_read_b64_tr_b16 a[92:93], v18 offset:36864\n"
    "ds_read_b64_tr_b16 a[94:95], v18 offset:37376\n"
    "s_waitcnt vmcnt(24) lgkmcnt(0)\n"
    "s_barrier\n"
    "s_add_u32 m0, s59, 73728\n"
    "s_mov_b32 s74, 663552\n"
    "buffer_load_dwordx4 v5, s[40:43], s74 offen lds\n"
    "s_add_u32 m0, s59, 77824\n"
    "s_mov_b32 s74, 663680\n"
    "buffer_load_dwordx4 v5, s[40:43], s74 offen lds\n"
    "s_add_u32 m0, s59, 81920\n"
    "s_mov_b32 s74, 663808\n"
    "buffer_load_dwordx4 v5, s[40:43], s74 offen lds\n"
    "s_add_u32 m0, s59, 86016\n"
    "s_mov_b32 s74, 700416\n"
    "buffer_load_dwordx4 v5, s[40:43], s74 offen lds\n"
    "s_add_u32 m0, s59, 90112\n"
    "s_mov_b32 s74, 700544\n"
    "buffer_load_dwordx4 v5, s[40:43], s74 offen lds\n"
    "s_add_u32 m0, s59, 94208\n"
    "s_mov_b32 s74, 700672\n"
    "buffer_load_dwordx4 v5, s[40:43], s74 offen lds\n"
    "ds_read_b64_tr_b16 a[96:97], v17 offset:0\n"
    "ds_read_b64_tr_b16 a[98:99], v17 offset:512\n"
    "ds_read_b64_tr_b16 a[100:101], v17 offset:8\n"
    "ds_read_b64_tr_b16 a[102:103], v17 offset:520\n"
    "ds_read_b64_tr_b16 a[104:105], v19 offset:0\n"
    "ds_read_b64_tr_b16 a[106:107], v19 offset:512\n"
    "ds_read_b64_tr_b16 a[108:109], v17 offset:12288\n"
    "ds_read_b64_tr_b16 a[110:111], v17 offset:12800\n"
    "ds_read_b64_tr_b16 a[112:113], v17 offset:12296\n"
    "ds_read_b64_tr_b16 a[114:115], v17 offset:12808\n"
    "ds_read_b64_tr_b16 a[116:117], v19 offset:12288\n"
    "ds_read_b64_tr_b16 a[118:119], v19 offset:12800\n"
    "s_waitcnt vmcnt(24) lgkmcnt(0)\n"
    "s_barrier\n"
    "s_add_u32 m0, s59, 98304\n"
    "s_mov_b32 s74, 737280\n"
    "buffer_load_dwordx4 v5, s[40:43], s74 offen lds\n"
    "s_add_u32 m0, s59, 102400\n"
    "s_mov_b32 s74, 737408\n"
    "buffer_load_dwordx4 v5, s[40:43], s74 offen lds\n"
    "s_add_u32 m0, s59, 106496\n"
    "s_mov_b32 s74, 737536\n"
    "buffer_load_dwordx4 v5, s[40:43], s74 offen lds\n"
    "s_add_u32 m0, s59, 110592\n"
    "s_mov_b32 s74, 774144\n"
    "buffer_load_dwordx4 v5, s[40:43], s74 offen lds\n"
    "s_add_u32 m0, s59, 114688\n"
    "s_mov_b32 s74, 774272\n"
    "buffer_load_dwordx4 v5, s[40:43], s74 offen lds\n"
    "s_add_u32 m0, s59, 118784\n"
    "s_mov_b32 s74, 774400\n"
    "buffer_load_dwordx4 v5, s[40:43], s74 offen lds\n"
    "ds_read_b64_tr_b16 a[120:121], v17 offset:24576\n"
    "ds_read_b64_tr_b16 a[122:123], v17 offset:25088\n"
    "ds_read_b64_tr_b16 a[124:125], v17 offset:24584\n"
    "ds_read_b64_tr_b16 a[126:127], v17 offset:25096\n"
    "ds_read_b64_tr_b16 a[128:129], v19 offset:24576\n"
    "ds_read_b64_tr_b16 a[130:131], v19 offset:25088\n"
    "ds_read_b64_tr_b16 a[132:133], v17 offset:36864\n"
    "ds_read_b64_tr_b16 a[134:135], v17 offset:37376\n"
    "ds_read_b64_tr_b16 a[136:137], v17 offset:36872\n"
    "ds_read_b64_tr_b16 a[138:139], v17 offset:37384\n"
    "ds_read_b64_tr_b16 a[140:141], v19 offset:36864\n"
    "ds_read_b64_tr_b16 a[142:143], v19 offset:37376\n"
    "s_waitcnt vmcnt(24) lgkmcnt(0)\n"
    "s_barrier\n"
    "s_add_u32 m0, s59, 122880\n"
    "s_mov_b32 s74, 811008\n"
    "buffer_load_dwordx4 v5, s[40:43], s74 offen lds\n"
    "s_add_u32 m0, s59, 126976\n"
    "s_mov_b32 s74, 811136\n"
    "buffer_load_dwordx4 v5, s[40:43], s74 offen lds\n"
    "s_add_u32 m0, s59, 131072\n"
    "s_mov_b32 s74, 811264\n"
    "buffer_load_dwordx4 v5, s[40:43], s74 offen lds\n"
    "s_add_u32 m0, s59, 135168\n"
    "s_mov_b32 s74, 847872\n"
    "buffer_load_dwordx4 v5, s[40:43], s74 offen lds\n"
    "s_add_u32 m0, s59, 139264\n"
    "s_mov_b32 s74, 848000\n"
    "buffer_load_dwordx4 v5, s[40:43], s74 offen lds\n"
    "s_add_u32 m0, s59, 143360\n"
    "s_mov_b32 s74, 848128\n"
    "buffer_load_dwordx4 v5, s[40:43], s74 offen lds\n"
    "ds_read_b64_tr_b16 a[144:145], v6 offset:0\n"
    "ds_read_b64_tr_b16 a[146:147], v6 offset:512\n"
    "ds_read_b64_tr_b16 a[148:149], v6 offset:8\n"
    "ds_read_b64_tr_b16 a[150:151], v6 offset:520\n"
    "ds_read_b64_tr_b16 a[152:153], v7 offset:0\n"
    "ds_read_b64_tr_b16 a[154:155], v7 offset:512\n"
    "ds_read_b64_tr_b16 a[156:157], v6 offset:12288\n"
    "ds_read_b64_tr_b16 a[158:159], v6 offset:12800\n"
    "ds_read_b64_tr_b16 a[160:161], v6 offset:12296\n"
    "ds_read_b64_tr_b16 a[162:163], v6 offset:12808\n"
    "ds_read_b64_tr_b16 a[164:165], v7 offset:12288\n"
    "ds_read_b64_tr_b16 a[166:167], v7 offset:12800\n"
    "s_waitcnt vmcnt(24) lgkmcnt(0)\n"
    "s_barrier\n"
    "s_add_u32 m0, s59, 0\n"
    "s_mov_b32 s74, 884736\n"
    "buffer_load_dwordx4 v5, s[40:43], s74 offen lds\n"
    "s_add_u32 m0, s59, 4096\n"
    "s_mov_b32 s74, 884864\n"
    "buffer_load_dwordx4 v5, s[40:43], s74 offen lds\n"
    "s_add_u32 m0, s59, 8192\n"
    "s_mov_b32 s74, 884992\n"
    "buffer_load_dwordx4 v5, s[40:43], s74 offen lds\n"
    "s_add_u32 m0, s59, 12288\n"
    "s_mov_b32 s74, 921600\n"
    "buffer_load_dwordx4 v5, s[40:43], s74 offen lds\n"
    "s_add_u32 m0, s59, 16384\n"
    "s_mov_b32 s74, 921728\n"
    "buffer_load_dwordx4 v5, s[40:43], s74 offen lds\n"
    "s_add_u32 m0, s59, 20480\n"
    "s_mov_b32 s74, 921856\n"
    "buffer_load_dwordx4 v5, s[40:43], s74 offen lds\n"
    "ds_read_b64_tr_b16 a[168:169], v6 offset:24576\n"
    "ds_read_b64_tr_b16 a[170:171], v6 offset:25088\n"
    "ds_read_b64_tr_b16 a[172:173], v6 offset:24584\n"
    "ds_read_b64_tr_b16 a[174:175], v6 offset:25096\n"
    "ds_read_b64_tr_b16 a[176:177], v7 offset:24576\n"
    "ds_read_b64_tr_b16 a[178:179], v7 offset:25088\n"
    "ds_read_b64_tr_b16 a[180:181], v6 offset:36864\n"
    "ds_read_b64_tr_b16 a[182:183], v6 offset:37376\n"
    "ds_read_b64_tr_b16 a[184:185], v6 offset:36872\n"
    "ds_read_b64_tr_b16 a[186:187], v6 offset:37384\n"
    "ds_read_b64_tr_b16 a[188:189], v7 offset:36864\n"
    "ds_read_b64_tr_b16 a[190:191], v7 offset:37376\n"
    "s_waitcnt vmcnt(24) lgkmcnt(0)\n"
    "s_barrier\n"
    "s_add_u32 m0, s59, 24576\n"
    "s_mov_b32 s74, 958464\n"
    "buffer_load_dwordx4 v5, s[40:43], s74 offen lds\n"
    "s_add_u32 m0, s59, 28672\n"
    "s_mov_b32 s74, 958592\n"
    "buffer_load_dwordx4 v5, s[40:43], s74 offen lds\n"
    "s_add_u32 m0, s59, 32768\n"
    "s_mov_b32 s74, 958720\n"
    "buffer_load_dwordx4 v5, s[40:43], s74 offen lds\n"
    "s_add_u32 m0, s59, 36864\n"
    "s_mov_b32 s74, 995328\n"
    "buffer_load_dwordx4 v5, s[40:43], s74 offen lds\n"
    "s_add_u32 m0, s59, 40960\n"
    "s_mov_b32 s74, 995456\n"
    "buffer_load_dwordx4 v5, s[40:43], s74 offen lds\n"
    "s_add_u32 m0, s59, 45056\n"
    "s_mov_b32 s74, 995584\n"
    "buffer_load_dwordx4 v5, s[40:43], s74 offen lds\n"
    "ds_read_b64_tr_b16 a[192:193], v16 offset:0\n"
    "ds_read_b64_tr_b16 a[194:195], v16 offset:512\n"
    "ds_read_b64_tr_b16 a[196:197], v16 offset:8\n"
    "ds_read_b64_tr_b16 a[198:199], v16 offset:520\n"
    "ds_read_b64_tr_b16 a[200:201], v18 offset:0\n"
    "ds_read_b64_tr_b16 a[202:203], v18 offset:512\n"
    "ds_read_b64_tr_b16 a[204:205], v16 offset:12288\n"
    "ds_read_b64_tr_b16 a[206:207], v16 offset:12800\n"
    "ds_read_b64_tr_b16 a[208:209], v16 offset:12296\n"
    "ds_read_b64_tr_b16 a[210:211], v16 offset:12808\n"
    "ds_read_b64_tr_b16 a[212:213], v18 offset:12288\n"
    "ds_read_b64_tr_b16 a[214:215], v18 offset:12800\n"
    "s_waitcnt vmcnt(24) lgkmcnt(0)\n"
    "s_barrier\n"
    "s_add_u32 m0, s59, 49152\n"
    "s_mov_b32 s74, 1032192\n"
    "buffer_load_dwordx4 v5, s[40:43], s74 offen lds\n"
    "s_add_u32 m0, s59, 53248\n"
    "s_mov_b32 s74, 1032320\n"
    "buffer_load_dwordx4 v5, s[40:43], s74 offen lds\n"
    "s_add_u32 m0, s59, 57344\n"
    "s_mov_b32 s74, 1032448\n"
    "buffer_load_dwordx4 v5, s[40:43], s74 offen lds\n"
    "s_add_u32 m0, s59, 61440\n"
    "s_mov_b32 s74, 1069056\n"
    "buffer_load_dwordx4 v5, s[40:43], s74 offen lds\n"
    "s_add_u32 m0, s59, 65536\n"
    "s_mov_b32 s74, 1069184\n"
    "buffer_load_dwordx4 v5, s[40:43], s74 offen lds\n"
    "s_add_u32 m0, s59, 69632\n"
    "s_mov_b32 s74, 1069312\n"
    "buffer_load_dwordx4 v5, s[40:43], s74 offen lds\n"
    "ds_read_b64_tr_b16 a[216:217], v16 offset:24576\n"
    "ds_read_b64_tr_b16 a[218:219], v16 offset:25088\n"
    "ds_read_b64_tr_b16 a[220:221], v16 offset:24584\n"
    "ds_read_b64_tr_b16 a[222:223], v16 offset:25096\n"
    "ds_read_b64_tr_b16 a[224:225], v18 offset:24576\n"
    "ds_read_b64_tr_b16 a[226:227], v18 offset:25088\n"
    "ds_read_b64_tr_b16 a[228:229], v16 offset:36864\n"
    "ds_read_b64_tr_b16 a[230:231], v16 offset:37376\n"
    "ds_read_b64_tr_b16 a[232:233], v16 offset:36872\n"
    "ds_read_b64_tr_b16 a[234:235], v16 offset:37384\n"
    "ds_read_b64_tr_b16 a[236:237], v18 offset:36864\n"
    "ds_read_b64_tr_b16 a[238:239], v18 offset:37376\n"
    "s_waitcnt vmcnt(24) lgkmcnt(0)\n"
    "s_barrier\n"
    "s_add_u32 m0, s59, 73728\n"
    "s_mov_b32 s74, 1105920\n"
    "buffer_load_dwordx4 v5, s[40:43], s74 offen lds\n"
    "s_add_u32 m0, s59, 77824\n"
    "s_mov_b32 s74, 1106048\n"
    "buffer_load_dwordx4 v5, s[40:43], s74 offen lds\n"
    "s_add_u32 m0, s59, 81920\n"
    "s_mov_b32 s74, 1106176\n"
    "buffer_load_dwordx4 v5, s[40:43], s74 offen lds\n"
    "s_add_u32 m0, s59, 86016\n"
    "s_mov_b32 s74, 1142784\n"
    "buffer_load_dwordx4 v5, s[40:43], s74 offen lds\n"
    "s_add_u32 m0, s59, 90112\n"
    "s_mov_b32 s74, 1142912\n"
    "buffer_load_dwordx4 v5, s[40:43], s74 offen lds\n"
    "s_add_u32 m0, s59, 94208\n"
    "s_mov_b32 s74, 1143040\n"
    "buffer_load_dwordx4 v5, s[40:43], s74 offen lds\n"
    "ds_read_b64_tr_b16 a[240:241], v17 offset:0\n"
    "ds_read_b64_tr_b16 a[242:243], v17 offset:512\n"
    "ds_read_b64_tr_b16 a[244:245], v17 offset:8\n"
    "ds_read_b64_tr_b16 a[246:247], v17 offset:520\n"
    "ds_read_b64_tr_b16 a[248:249], v19 offset:0\n"
    "ds_read_b64_tr_b16 a[250:251], v19 offset:512\n"
    "ds_read_b64_tr_b16 a[252:253], v17 offset:12288\n"
    "ds_read_b64_tr_b16 a[254:255], v17 offset:12800\n"
    "ds_read_b64_tr_b16 v[128:129], v17 offset:12296\n"
    "ds_read_b64_tr_b16 v[130:131], v17 offset:12808\n"
    "ds_read_b64_tr_b16 v[132:133], v19 offset:12288\n"
    "ds_read_b64_tr_b16 v[134:135], v19 offset:12800\n"
    "s_waitcnt vmcnt(24) lgkmcnt(0)\n"
    "s_barrier\n"
    "ds_read_b64_tr_b16 v[136:137], v17 offset:24576\n"
    "ds_read_b64_tr_b16 v[138:139], v17 offset:25088\n"
    "ds_read_b64_tr_b16 v[140:141], v17 offset:24584\n"
    "ds_read_b64_tr_b16 v[142:143], v17 offset:25096\n"
    "ds_read_b64_tr_b16 v[144:145], v19 offset:24576\n"
    "ds_read_b64_tr_b16 v[146:147], v19 offset:25088\n"
    "ds_read_b64_tr_b16 v[148:149], v17 offset:36864\n"
    "ds_read_b64_tr_b16 v[150:151], v17 offset:37376\n"
    "ds_read_b64_tr_b16 v[152:153], v17 offset:36872\n"
    "ds_read_b64_tr_b16 v[154:155], v17 offset:37384\n"
    "ds_read_b64_tr_b16 v[156:157], v19 offset:36864\n"
    "ds_read_b64_tr_b16 v[158:159], v19 offset:37376\n"
    "s_waitcnt vmcnt(18) lgkmcnt(0)\n"
    "s_barrier\n"
    "ds_read_b64_tr_b16 v[160:161], v6 offset:0\n"
    "ds_read_b64_tr_b16 v[162:163], v6 offset:512\n"
    "ds_read_b64_tr_b16 v[164:165], v6 offset:8\n"
    "ds_read_b64_tr_b16 v[166:167], v6 offset:520\n"
    "ds_read_b64_tr_b16 v[168:169], v7 offset:0\n"
    "ds_read_b64_tr_b16 v[170:171], v7 offset:512\n"
    "ds_read_b64_tr_b16 v[172:173], v6 offset:12288\n"
    "ds_read_b64_tr_b16 v[174:175], v6 offset:12800\n"
    "ds_read_b64_tr_b16 v[176:177], v6 offset:12296\n"
    "ds_read_b64_tr_b16 v[178:179], v6 offset:12808\n"
    "ds_read_b64_tr_b16 v[180:181], v7 offset:12288\n"
    "ds_read_b64_tr_b16 v[182:183], v7 offset:12800\n"
    "s_waitcnt vmcnt(12) lgkmcnt(0)\n"
    "s_barrier\n"
    "ds_read_b64_tr_b16 v[184:185], v6 offset:24576\n"
    "ds_read_b64_tr_b16 v[186:187], v6 offset:25088\n"
    "ds_read_b64_tr_b16 v[188:189], v6 offset:24584\n"
    "ds_read_b64_tr_b16 v[190:191], v6 offset:25096\n"
    "ds_read_b64_tr_b16 v[192:193], v7 offset:24576\n"
    "ds_read_b64_tr_b16 v[194:195], v7 offset:25088\n"
    "ds_read_b64_tr_b16 v[196:197], v6 offset:36864\n"
    "ds_read_b64_tr_b16 v[198:199], v6 offset:37376\n"
    "ds_read_b64_tr_b16 v[200:201], v6 offset:36872\n"
    "ds_read_b64_tr_b16 v[202:203], v6 offset:37384\n"
    "ds_read_b64_tr_b16 v[204:205], v7 offset:36864\n"
    "ds_read_b64_tr_b16 v[206:207], v7 offset:37376\n"
    "s_waitcnt vmcnt(6) lgkmcnt(0)\n"
    "s_barrier\n"
    "ds_read_b64_tr_b16 v[208:209], v16 offset:0\n"
    "ds_read_b64_tr_b16 v[210:211], v16 offset:512\n"
    "ds_read_b64_tr_b16 v[212:213], v16 offset:8\n"
    "ds_read_b64_tr_b16 v[214:215], v16 offset:520\n"
    "ds_read_b64_tr_b16 v[216:217], v18 offset:0\n"
    "ds_read_b64_tr_b16 v[218:219], v18 offset:512\n"
    "ds_read_b64_tr_b16 v[220:221], v16 offset:12288\n"
    "ds_read_b64_tr_b16 v[222:223], v16 offset:12800\n"
    "ds_read_b64_tr_b16 v[224:225], v16 offset:12296\n"
    "ds_read_b64_tr_b16 v[226:227], v16 offset:12808\n"
    "ds_read_b64_tr_b16 v[228:229], v18 offset:12288\n"
    "ds_read_b64_tr_b16 v[230:231], v18 offset:12800\n"
    "s_waitcnt vmcnt(0) lgkmcnt(0)\n"
    "s_barrier\n"
    "ds_read_b64_tr_b16 v[232:233], v16 offset:24576\n"
    "ds_read_b64_tr_b16 v[234:235], v16 offset:25088\n"
    "ds_read_b64_tr_b16 v[236:237], v16 offset:24584\n"
    "ds_read_b64_tr_b16 v[238:239], v16 offset:25096\n"
    "ds_read_b64_tr_b16 v[240:241], v18 offset:24576\n"
    "ds_read_b64_tr_b16 v[242:243], v18 offset:25088\n"
    "ds_read_b64_tr_b16 v[244:245], v16 offset:36864\n"
    "ds_read_b64_tr_b16 v[246:247], v16 offset:37376\n"
    "ds_read_b64_tr_b16 v[248:249], v16 offset:36872\n"
    "ds_read_b64_tr_b16 v[250:251], v16 offset:37384\n"
    "ds_read_b64_tr_b16 v[252:253], v18 offset:36864\n"
    "ds_read_b64_tr_b16 v[254:255], v18 offset:37376\n"
    "s_waitcnt lgkmcnt(0)\n"
    "s_barrier\n"
    "s_mov_b32 s68, 0\n"
    "s_mov_b32 s71, 0\n"
    "s_mov_b32 s78, 65536\n"
    "v_add_u32 v9, s78, v0\n"
    "s_mov_b32 s64, 0xbfb8aa3b\n"
    "s_mov_b32 s72, 262144\n"
    ".LgWO_par_%=:\n"
    "buffer_load_dword v12, off, s[36:39], 0 sc1\n"
    "s_waitcnt vmcnt(0)\n"
    "v_readfirstlane_b32 s74, v12\n"
    "s_nop 0\n"
    "s_cmp_ge_i32 s74, 0\n"
    "s_cbranch_scc1 .LgWO_par1_%=\n"
    "s_sleep 4\n"
    "s_sub_u32 s72, s72, 1\n"
    "s_cbranch_scc0 .LgWO_par_%=\n"
    "s_mov_b32 s72, 0\n"
    ".LgWO_par1_%=:\n"
    "s_and_b32 s74, s74, 1\n"
    "s_lshl_b32 s74, s74, 13\n"
    "s_add_u32 s36, s36, s74\n"
    "s_addc_u32 s37, s37, 0\n"
    "s_branch .LgWO_block_%=\n"
    ".LgWO_LOOPB_%=:\n"
    ".LgWO_head_%=:\n"
    "v_mfma_f32_16x16x32_bf16 v[64:67], a[8:11], v[16:19], 0\n"
    "s_waitcnt lgkmcnt(6)\n"
    "ds_read_b128 v[16:19], v9 offset:0\n"
    "v_mfma_f32_16x16x32_bf16 v[68:71], a[8:11], v[20:23], 0\n"
    "s_waitcnt lgkmcnt(6)\n"
    "ds_read_b128 v[20:23], v9 offset:2048\n"
    "v_mfma_f32_16x16x32_bf16 v[64:67], a[20:23], v[24:27], v[64:67]\n"
    "s_waitcnt lgkmcnt(6)\n"
    "ds_read_b128 v[24:27], v9 offset:4096\n"
    "v_mfma_f32_16x16x32_bf16 v[68:71], a[20:23], v[28:31], v[68:71]\n"
    "s_waitcnt lgkmcnt(6)\n"
    "ds_read_b128 v[28:31], v9 offset:6144\n"
    "v_mfma_f32_16x16x32_bf16 v[64:67], a[32:35], v[32:35], v[64:67]\n"
    "s_waitcnt lgkmcnt(6)\n"
    "ds_read_b128 v[32:35], v9 offset:1024\n"
    "v_mfma_f32_16x16x32_bf16 v[68:71], a[32:35], v[36:39], v[68:71]\n"
    "s_waitcnt lgkmcnt(6)\n"
    "ds_read_b128 v[36:39], v9 offset:3072\n"
    "v_mfma_f32_16x16x32_bf16 v[64:67], a[44:47], v[40:43], v[64:67]\n"
    "s_waitcnt lgkmcnt(6)\n"
    "ds_read_b128 v[40:43], v9 offset:5120\n"
    "v_mfma_f32_16x16x32_bf16 v[68:71], a[44:47], v[44:47], v[68:71]\n"
    "s_waitcnt lgkmcnt(6)\n"
    "ds_read_b128 v[44:47], v9 offset:7168\n"
    "v_mfma_f32_16x16x32_bf16 v[48:51], a[0:3], v[16:19], 0\n"
    "s_waitcnt lgkmcnt(6)\n"
    "v_cvt_pk_bf16_f32 v56, v56, v57\n"
    "v_cvt_pk_bf16_f32 v57, v58, v59\n"
    "v_mfma_f32_16x16x32_bf16 v[52:55], a[4:7], v[16:19], 0\n"
    "ds_read_b128 v[16:19], v9 offset:8192\n"
    "v_cvt_pk_bf16_f32 v58, v60, v61\n"
    "v_mfma_f32_16x16x32_bf16 v[48:51], a[12:15], v[20:23], v[48:51]\n"
    "s_waitcnt lgkmcnt(6)\n"
    "v_cvt_pk_bf16_f32 v59, v62, v63\n"
    "v_mfma_f32_16x16x32_bf16 v[52:55], a[16:19], v[20:23], v[52:55]\n"
    "ds_read_b128 v[20:23], v9 offset:9216\n"
    "v_mfma_f32_16x16x32_bf16 v[48:51], a[24:27], v[24:27], v[48:51]\n"
    "s_waitcnt lgkmcnt(6)\n"
    "s_sub_u32 s74, s68, 1\n"
    "buffer_store_dwordx4 v[56:59], v15, s[36:39], s73 offen offset:1024 sc1\n"
    "v_mfma_f32_16x16x32_bf16 v[52:55], a[28:31], v[24:27], v[52:55]\n"
    "ds_read_b128 v[24:27], v9 offset:10240\n"
    "v_mfma_f32_16x16x32_bf16 v[48:51], a[36:39], v[28:31], v[48:51]\n"
    "s_waitcnt lgkmcnt(6)\n"
    "s_and_b32 s76, s71, s54\n"
    "v_mfma_f32_16x16x32_bf16 v[52:55], a[40:43], v[28:31], v[52:55]\n"
    "ds_read_b128 v[28:31], v9 offset:11264\n"
    "v_mfma_f32_16x16x32_bf16 v[56:59], a[0:3], v[32:35], 0\n"
    "s_waitcnt lgkmcnt(6)\n"
    "s_mul_i32 s76, s76, s53\n"
    "v_mfma_f32_16x16x32_bf16 v[60:63], a[4:7], v[32:35], 0\n"
    "ds_read_b128 v[32:35], v9 offset:12288\n"
    "v_mfma_f32_16x16x32_bf16 v[56:59], a[12:15], v[36:39], v[56:59]\n"
    "s_waitcnt lgkmcnt(6)\n"
    "s_add_u32 s76, s76, s52\n"
    "v_mfma_f32_16x16x32_bf16 v[60:63], a[16:19], v[36:39], v[60:63]\n"
    "ds_read_b128 v[36:39], v9 offset:13312\n"
    "v_mfma_f32_16x16x32_bf16 v[56:59], a[24:27], v[40:43], v[56:59]\n"
    "s_waitcnt lgkmcnt(6)\n"
    "s_and_b32 s77, s71, 1\n"
    "v_mfma_f32_16x16x32_bf16 v[60:63], a[28:31], v[40:43], v[60:63]\n"
    "ds_read_b128 v[40:43], v9 offset:14336\n"
    "v_mfma_f32_16x16x32_bf16 v[56:59], a[36:39], v[44:47], v[56:59]\n"
    "s_waitcnt lgkmcnt(6)\n"
    "s_mul_i32 s77, s77, 65536\n"
    "v_mfma_f32_16x16x32_bf16 v[60:63], a[40:43], v[44:47], v[60:63]\n"
    "ds_read_b128 v[44:47], v9 offset:15360\n"
    "v_mfma_f32_16x16x32_bf16 v[48:51], a[48:51], v[16:19], v[48:51]\n"
    "s_add_u32 s77, s77, s59\n"
    "v_mfma_f32_16x16x32_bf16 v[52:55], a[52:55], v[16:19], v[52:55]\n"
    "s_waitcnt lgkmcnt(6)\n"
    "v_mfma_f32_16x16x32_bf16 v[64:67], a[56:59], v[16:19], v[64:67]\n"
    "ds_read_b128 v[16:19], v9 offset:16384\n"
    "v_mfma_f32_16x16x32_bf16 v[56:59], a[48:51], v[20:23], v[56:59]\n"
    "v_mfma_f32_16x16x32_bf16 v[60:63], a[52:55], v[20:23], v[60:63]\n"
    "s_waitcnt lgkmcnt(6)\n"
    "v_mfma_f32_16x16x32_bf16 v[68:71], a[56:59], v[20:23], v[68:71]\n"
    "ds_read_b128 v[20:23], v9 offset:17408\n"
    "v_mfma_f32_16x16x32_bf16 v[48:51], a[60:63], v[24:27], v[48:51]\n"
    "v_mfma_f32_16x16x32_bf16 v[52:55], a[64:67], v[24:27], v[52:55]\n"
    "s_waitcnt lgkmcnt(6)\n"
    "v_mfma_f32_16x16x32_bf16 v[64:67], a[68:71], v[24:27], v[64:67]\n"
    "ds_read_b128 v[24:27], v9 offset:18432\n"
    "v_mfma_f32_16x16x32_bf16 v[56:59], a[60:63], v[28:31], v[56:59]\n"
    "v_mfma_f32_16x16x32_bf16 v[60:63], a[64:67], v[28:31], v[60:63]\n"
    "s_waitcnt lgkmcnt(6)\n"
    "v_mfma_f32_16x16x32_bf16 v[68:71], a[68:71], v[28:31], v[68:71]\n"
    "ds_read_b128 v[28:31], v9 offset:19456\n"
    "v_mfma_f32_16x16x32_bf16 v[48:51], a[72:75], v[32:35], v[48:51]\n"
    "v_mfma_f32_16x16x32_bf16 v[52:55], a[76:79], v[32:35], v[52:55]\n"
    "s_waitcnt lgkmcnt(6)\n"
    "v_mfma_f32_16x16x32_bf16 v[64:67], a[80:83], v[32:35], v[64:67]\n"
    "ds_read_b128 v[32:35], v9 offset:20480\n"
    "v_mfma_f32_16x16x32_bf16 v[56:59], a[72:75], v[36:39], v[56:59]\n"
    "v_mfma_f32_16x16x32_bf16 v[60:63], a[76:79], v[36:39], v[60:63]\n"
    "s_waitcnt lgkmcnt(6)\n"
    "v_mfma_f32_16x16x32_bf16 v[68:71], a[80:83], v[36:39], v[68:71]\n"
    "ds_read_b128 v[36:39], v9 offset:21504\n"
    "v_mfma_f32_16x16x32_bf16 v[48:51], a[84:87], v[40:43], v[48:51]\n"
    "v_mov_b32 v12, s74\n"
    "v_mfma_f32_16x16x32_bf16 v[52:55], a[88:91], v[40:43], v[52:55]\n"
    "s_waitcnt lgkmcnt(6)\n"
    "s_waitcnt vmcnt(0)\n"
    "v_add_u32 v13, v11, v3\n"
    "v_mfma_f32_16x16x32_bf16 v[64:67], a[92:95], v[40:43], v[64:67]\n"
    "ds_read_b128 v[40:43], v9 offset:22528\n"
    "buffer_store_dword v12, v14, s[36:39], 0 offen sc1\n"
    "v_cmp_gt_i32 vcc, s71, v13\n"
    "v_mfma_f32_16x16x32_bf16 v[56:59], a[84:87], v[44:47], v[56:59]\n"
    "v_add_u32 v10, 49152, v9\n"
    "v_mfma_f32_16x16x32_bf16 v[60:63], a[88:91], v[44:47], v[60:63]\n"
    "s_waitcnt lgkmcnt(6)\n"
    "v_mfma_f32_16x16x32_bf16 v[68:71], a[92:95], v[44:47], v[68:71]\n"
    "ds_read_b128 v[44:47], v9 offset:23552\n"
    ".LgWO_mid_%=:\n"
    "s_cbranch_vccnz .LgWO_chk0_%=\n"
    "v_mfma_f32_16x16x32_bf16 v[48:51], a[96:99], v[16:19], v[48:51]\n"
    "s_add_u32 m0, s77, s59\n"
    "s_add_u32 s77, s77, 8192\n"
    "buffer_load_dwordx4 v1, s[36:39], s76 offen sc1 lds\n"
    "v_mfma_f32_16x16x32_bf16 v[52:55], a[100:103], v[16:19], v[52:55]\n"
    "s_waitcnt lgkmcnt(6)\n"
    "buffer_load_dwordx4 v1, s[36:39], s76 offen offset:1024 sc1 lds\n"
    "s_add_u32 s76, s76, 16384\n"
    "v_mfma_f32_16x16x32_bf16 v[64:67], a[104:107], v[16:19], v[64:67]\n"
    "ds_read_b128 v[16:19], v9 offset:24576\n"
    "v_mfma_f32_16x16x32_bf16 v[56:59], a[96:99], v[20:23], v[56:59]\n"
    "s_mov_b32 s70, 1\n"
    "v_mfma_f32_16x16x32_bf16 v[60:63], a[100:103], v[20:23], v[60:63]\n"
    "s_waitcnt lgkmcnt(6)\n"
    "s_add_u32 m0, s77, s59\n"
    "s_add_u32 s77, s77, 8192\n"
    "buffer_load_dwordx4 v1, s[36:39], s76 offen sc1 lds\n"
    "v_mfma_f32_16x16x32_bf16 v[68:71], a[104:107], v[20:23], v[68:71]\n"
    "ds_read_b128 v[20:23], v9 offset:25600\n"
    "v_mfma_f32_16x16x32_bf16 v[48:51], a[108:111], v[24:27], v[48:51]\n"
    "buffer_load_dwordx4 v1, s[36:39], s76 offen offset:1024 sc1 lds\n"
    "s_add_u32 s76, s76, 16384\n"
    "v_mfma_f32_16x16x32_bf16 v[52:55], a[112:115], v[24:27], v[52:55]\n"
    "s_waitcnt lgkmcnt(6)\n"
    "s_and_b32 s73, s68, s57\n"
    "v_mfma_f32_16x16x32_bf16 v[64:67], a[116:119], v[24:27], v[64:67]\n"
    "ds_read_b128 v[24:27], v9 offset:26624\n"
    "v_mfma_f32_16x16x32_bf16 v[56:59], a[108:111], v[28:31], v[56:59]\n"
    "s_add_u32 m0, s77, s59\n"
    "s_add_u32 s77, s77, 8192\n"
    "buffer_load_dwordx4 v1, s[36:39], s76 offen sc1 lds\n"
    "v_mfma_f32_16x16x32_bf16 v[60:63], a[112:115], v[28:31], v[60:63]\n"
    "s_waitcnt lgkmcnt(6)\n"
    "buffer_load_dwordx4 v1, s[36:39], s76 offen offset:1024 sc1 lds\n"
    "s_add_u32 s76, s76, 16384\n"
    "v_mfma_f32_16x16x32_bf16 v[68:71], a[116:119], v[28:31], v[68:71]\n"
    "ds_read_b128 v[28:31], v9 offset:27648\n"
    "v_mfma_f32_16x16x32_bf16 v[48:51], a[120:123], v[32:35], v[48:51]\n"
    "s_mul_i32 s73, s73, s56\n"
    "v_mfma_f32_16x16x32_bf16 v[52:55], a[124:127], v[32:35], v[52:55]\n"
    "s_waitcnt lgkmcnt(6)\n"
    "s_add_u32 m0, s77, s59\n"
    "s_add_u32 s77, s77, 8192\n"
    "buffer_load_dwordx4 v1, s[36:39], s76 offen sc1 lds\n"
    "v_mfma_f32_16x16x32_bf16 v[64:67], a[128:131], v[32:35], v[64:67]\n"
    "ds_read_b128 v[32:35], v9 offset:28672\n"
    "v_mfma_f32_16x16x32_bf16 v[56:59], a[120:123], v[36:39], v[56:59]\n"
    "buffer_load_dwordx4 v1, s[36:39], s76 offen offset:1024 sc1 lds\n"
    "s_add_u32 s76, s76, 16384\n"
    "v_mfma_f32_16x16x32_bf16 v[60:63], a[124:127], v[36:39], v[60:63]\n"
    "s_waitcnt lgkmcnt(6)\n"
    "s_add_u32 s73, s73, s55\n"
    "v_mfma_f32_16x16x32_bf16 v[68:71], a[128:131], v[36:39], v[68:71]\n"
    "ds_read_b128 v[36:39], v9 offset:29696\n"
    "v_mfma_f32_16x16x32_bf16 v[48:51], a[132:135], v[40:43], v[48:51]\n"
    "s_add_u32 m0, s77, s59\n"
    "s_add_u32 s77, s77, 8192\n"
    "buffer_load_dwordx4 v1, s[36:39], s76 offen sc1 lds\n"
    "v_mfma_f32_16x16x32_bf16 v[52:55], a[136:139], v[40:43], v[52:55]\n"
    "s_waitcnt lgkmcnt(6)\n"
    "v_mfma_f32_16x16x32_bf16 v[64:67], a[140:143], v[40:43], v[64:67]\n"
    "ds_read_b128 v[40:43], v9 offset:30720\n"
    "v_mfma_f32_16x16x32_bf16 v[56:59], a[132:135], v[44:47], v[56:59]\n"
    "buffer_load_dwordx4 v1, s[36:39], s76 offen offset:1024 sc1 lds\n"
    "s_add_u32 s76, s76, 16384\n"
    "v_mfma_f32_16x16x32_bf16 v[60:63], a[136:139], v[44:47], v[60:63]\n"
    "s_waitcnt lgkmcnt(6)\n"
    "s_add_u32 m0, s77, s59\n"
    "s_add_u32 s77, s77, 8192\n"
    "buffer_load_dwordx4 v1, s[36:39], s76 offen sc1 lds\n"
    "v_mfma_f32_16x16x32_bf16 v[68:71], a[140:143], v[44:47], v[68:71]\n"
    "ds_read_b128 v[44:47], v9 offset:31744\n"
    "v_mfma_f32_16x16x32_bf16 v[48:51], a[144:147], v[16:19], v[48:51]\n"
    "v_mfma_f32_16x16x32_bf16 v[52:55], a[148:151], v[16:19], v[52:55]\n"
    "s_waitcnt lgkmcnt(6)\n"
    "buffer_load_dwordx4 v1, s[36:39], s76 offen offset:1024 sc1 lds\n"
    "s_add_u32 s76, s76, 16384\n"
    "v_mfma_f32_16x16x32_bf16 v[64:67], a[152:155], v[16:19], v[64:67]\n"
    "ds_read_b128 v[16:19], v9 offset:32768\n"
    "v_mfma_f32_16x16x32_bf16 v[56:59], a[144:147], v[20:23], v[56:59]\n"
    "s_add_u32 m0, s77, s59\n"
    "s_add_u32 s77, s77, 8192\n"
    "buffer_load_dwordx4 v1, s[36:39], s76 offen sc1 lds\n"
    "v_mfma_f32_16x16x32_bf16 v[60:63], a[148:151], v[20:23], v[60:63]\n"
    "s_waitcnt lgkmcnt(6)\n"
    "v_mfma_f32_16x16x32_bf16 v[68:71], a[152:155], v[20:23], v[68:71]\n"
    "ds_read_b128 v[20:23], v9 offset:33792\n"
    "v_mfma_f32_16x16x32_bf16 v[48:51], a[156:159], v[24:27], v[48:51]\n"
    "buffer_load_dwordx4 v1, s[36:39], s76 offen offset:1024 sc1 lds\n"
    "s_add_u32 s76, s76, 16384\n"
    "v_mfma_f32_16x16x32_bf16 v[52:55], a[160:163], v[24:27], v[52:55]\n"
    "s_waitcnt lgkmcnt(6)\n"
    "s_add_u32 m0, s77, s59\n"
    "s_add_u32 s77, s77, 8192\n"
    "buffer_load_dwordx4 v1, s[36:39], s76 offen sc1 lds\n"
    "v_mfma_f32_16x16x32_bf16 v[64:67], a[164:167], v[24:27], v[64:67]\n"
    "ds_read_b128 v[24:27], v9 offset:34816\n"
    "v_mfma_f32_16x16x32_bf16 v[56:59], a[156:159], v[28:31], v[56:59]\n"
    "v_mfma_f32_16x16x32_bf16 v[60:63], a[160:163], v[28:31], v[60:63]\n"
    "s_waitcnt lgkmcnt(6)\n"
    "buffer_load_dwordx4 v1, s[36:39], s76 offen offset:1024 sc1 lds\n"
    "s_add_u32 s76, s76, 16384\n"
    "v_mfma_f32_16x16x32_bf16 v[68:71], a[164:167], v[28:31], v[68:71]\n"
    "ds_read_b128 v[28:31], v9 offset:35840\n"
    "v_mfma_f32_16x16x32_bf16 v[48:51], a[168:171], v[32:35], v[48:51]\n"
    "v_mfma_f32_16x16x32_bf16 v[52:55], a[172:175], v[32:35], v[52:55]\n"
    "s_waitcnt lgkmcnt(6)\n"
    ".LgWO_seg0_%=:\n"
    "s_cmp_lg_u32 s70, 0\n"
    "s_cbranch_scc0 .LgWO_chk1_%=\n"
    ".LgWO_plain1_%=:\n"
    "v_mfma_f32_16x16x32_bf16 v[64:67], a[176:179], v[32:35], v[64:67]\n"
    "ds_read_b128 v[32:35], v9 offset:36864\n"
    "v_mfma_f32_16x16x32_bf16 v[56:59], a[168:171], v[36:39], v[56:59]\n"
    "v_mfma_f32_16x16x32_bf16 v[60:63], a[172:175], v[36:39], v[60:63]\n"
    "s_waitcnt lgkmcnt(6)\n"
    "v_mfma_f32_16x16x32_bf16 v[68:71], a[176:179], v[36:39], v[68:71]\n"
    "ds_read_b128 v[36:39], v9 offset:37888\n"
    "v_mfma_f32_16x16x32_bf16 v[48:51], a[180:183], v[40:43], v[48:51]\n"
    "v_mfma_f32_16x16x32_bf16 v[52:55], a[184:187], v[40:43], v[52:55]\n"
    "s_waitcnt lgkmcnt(6)\n"
    "v_mfma_f32_16x16x32_bf16 v[64:67], a[188:191], v[40:43], v[64:67]\n"
    "ds_read_b128 v[40:43], v9 offset:38912\n"
    "v_mfma_f32_16x16x32_bf16 v[56:59], a[180:183], v[44:47], v[56:59]\n"
    "v_mfma_f32_16x16x32_bf16 v[60:63], a[184:187], v[44:47], v[60:63]\n"
    "s_waitcnt lgkmcnt(6)\n"
    "v_mfma_f32_16x16x32_bf16 v[68:71], a[188:191], v[44:47], v[68:71]\n"
    "ds_read_b128 v[44:47], v9 offset:39936\n"
    "v_mfma_f32_16x16x32_bf16 v[48:51], a[192:195], v[16:19], v[48:51]\n"
    "v_mfma_f32_16x16x32_bf16 v[52:55], a[196:199], v[16:19], v[52:55]\n"
    "s_waitcnt lgkmcnt(6)\n"
    "v_mfma_f32_16x16x32_bf16 v[64:67], a[200:203], v[16:19], v[64:67]\n"
    "ds_read_b128 v[16:19], v9 offset:40960\n"
    "v_mfma_f32_16x16x32_bf16 v[56:59], a[192:195], v[20:23], v[56:59]\n"
    "v_mfma_f32_16x16x32_bf16 v[60:63], a[196:199], v[20:23], v[60:63]\n"
    "s_waitcnt lgkmcnt(6)\n"
    "v_mfma_f32_16x16x32_bf16 v[68:71], a[200:203], v[20:23], v[68:71]\n"
    "ds_read_b128 v[20:23], v9 offset:41984\n"
    "v_mfma_f32_16x16x32_bf16 v[48:51], a[204:207], v[24:27], v[48:51]\n"
    "v_mfma_f32_16x16x32_bf16 v[52:55], a[208:211], v[24:27], v[52:55]\n"
    "s_waitcnt lgkmcnt(6)\n"
    "v_mfma_f32_16x16x32_bf16 v[64:67], a[212:215], v[24:27], v[64:67]\n"
    "ds_read_b128 v[24:27], v9 offset:43008\n"
    "v_mfma_f32_16x16x32_bf16 v[56:59], a[204:207], v[28:31], v[56:59]\n"
    "v_mfma_f32_16x16x32_bf16 v[60:63], a[208:211], v[28:31], v[60:63]\n"
    "s_waitcnt lgkmcnt(6)\n"
    "v_mfma_f32_16x16x32_bf16 v[68:71], a[212:215], v[28:31], v[68:71]\n"
    "ds_read_b128 v[28:31], v9 offset:44032\n"
    "v_mfma_f32_16x16x32_bf16 v[48:51], a[216:219], v[32:35], v[48:51]\n"
    "v_mfma_f32_16x16x32_bf16 v[52:55], a[220:223], v[32:35], v[52:55]\n"
    "s_waitcnt lgkmcnt(6)\n"
    "v_mfma_f32_16x16x32_bf16 v[64:67], a[224:227], v[32:35], v[64:67]\n"
    "ds_read_b128 v[32:35], v9 offset:45056\n"
    "v_mfma_f32_16x16x32_bf16 v[56:59], a[216:219], v[36:39], v[56:59]\n"
    "v_mfma_f32_16x16x32_bf16 v[60:63], a[220:223], v[36:39], v[60:63]\n"
    "s_waitcnt lgkmcnt(6)\n"
    "v_mfma_f32_16x16x32_bf16 v[68:71], a[224:227], v[36:39], v[68:71]\n"
    "ds_read_b128 v[36:39], v9 offset:46080\n"
    "v_mfma_f32_16x16x32_bf16 v[48:51], a[228:231], v[40:43], v[48:51]\n"
    "v_mfma_f32_16x16x32_bf16 v[52:55], a[232:235], v[40:43], v[52:55]\n"
    "s_waitcnt lgkmcnt(6)\n"
    "v_mfma_f32_16x16x32_bf16 v[64:67], a[236:239], v[40:43], v[64:67]\n"
    "ds_read_b128 v[40:43], v9 offset:47104\n"
    "v_mfma_f32_16x16x32_bf16 v[56:59], a[228:231], v[44:47], v[56:59]\n"
    "v_mfma_f32_16x16x32_bf16 v[60:63], a[232:235], v[44:47], v[60:63]\n"
    "s_waitcnt lgkmcnt(6)\n"
    "v_mfma_f32_16x16x32_bf16 v[68:71], a[236:239], v[44:47], v[68:71]\n"
    "ds_read_b128 v[44:47], v9 offset:48128\n"
    "v_mfma_f32_16x16x32_bf16 v[48:51], a[240:243], v[16:19], v[48:51]\n"
    "v_mfma_f32_16x16x32_bf16 v[52:55], a[244:247], v[16:19], v[52:55]\n"
    "s_waitcnt lgkmcnt(6)\n"
    "v_mfma_f32_16x16x32_bf16 v[64:67], a[248:251], v[16:19], v[64:67]\n"
    "ds_read_b128 v[16:19], v10 offset:0\n"
    "v_mfma_f32_16x16x32_bf16 v[56:59], a[240:243], v[20:23], v[56:59]\n"
    "v_mfma_f32_16x16x32_bf16 v[60:63], a[244:247], v[20:23], v[60:63]\n"
    "s_waitcnt lgkmcnt(6)\n"
    ".LgWO_seg1_%=:\n"
    "s_cmp_lg_u32 s70, 0\n"
    "s_cbranch_scc0 .LgWO_chk2_%=\n"
    ".LgWO_plain2_%=:\n"
    "v_mfma_f32_16x16x32_bf16 v[68:71], a[248:251], v[20:23], v[68:71]\n"
    "ds_read_b128 v[20:23], v10 offset:1024\n"
    "v_mfma_f32_16x16x32_bf16 v[48:51], a[252:255], v[24:27], v[48:51]\n"
    "v_mfma_f32_16x16x32_bf16 v[52:55], v[128:131], v[24:27], v[52:55]\n"
    "s_waitcnt lgkmcnt(6)\n"
    "v_mfma_f32_16x16x32_bf16 v[64:67], v[132:135], v[24:27], v[64:67]\n"
    "ds_read_b128 v[24:27], v10 offset:2048\n"
    "v_mfma_f32_16x16x32_bf16 v[56:59], a[252:255], v[28:31], v[56:59]\n"
    "v_mfma_f32_16x16x32_bf16 v[60:63], v[128:131], v[28:31], v[60:63]\n"
    "s_waitcnt lgkmcnt(6)\n"
    "v_mfma_f32_16x16x32_bf16 v[68:71], v[132:135], v[28:31], v[68:71]\n"
    "ds_read_b128 v[28:31], v10 offset:3072\n"
    "v_mfma_f32_16x16x32_bf16 v[48:51], v[136:139], v[32:35], v[48:51]\n"
    "v_mfma_f32_16x16x32_bf16 v[52:55], v[140:143], v[32:35], v[52:55]\n"
    "s_waitcnt lgkmcnt(6)\n"
    "v_mfma_f32_16x16x32_bf16 v[64:67], v[144:147], v[32:35], v[64:67]\n"
    "ds_read_b128 v[32:35], v10 offset:4096\n"
    "v_mfma_f32_16x16x32_bf16 v[56:59], v[136:139], v[36:39], v[56:59]\n"
    "v_mfma_f32_16x16x32_bf16 v[60:63], v[140:143], v[36:39], v[60:63]\n"
    "s_waitcnt lgkmcnt(6)\n"
    "v_mfma_f32_16x16x32_bf16 v[68:71], v[144:147], v[36:39], v[68:71]\n"
    "ds_read_b128 v[36:39], v10 offset:5120\n"
    "v_mfma_f32_16x16x32_bf16 v[48:51], v[148:151], v[40:43], v[48:51]\n"
    "v_mfma_f32_16x16x32_bf16 v[52:55], v[152:155], v[40:43], v[52:55]\n"
    "s_waitcnt lgkmcnt(6)\n"
    "v_mfma_f32_16x16x32_bf16 v[64:67], v[156:159], v[40:43], v[64:67]\n"
    "ds_read_b128 v[40:43], v10 offset:6144\n"
    "v_mfma_f32_16x16x32_bf16 v[56:59], v[148:151], v[44:47], v[56:59]\n"
    "v_mfma_f32_16x16x32_bf16 v[60:63], v[152:155], v[44:47], v[60:63]\n"
    "s_waitcnt lgkmcnt(6)\n"
    "v_mfma_f32_16x16x32_bf16 v[68:71], v[156:159], v[44:47], v[68:71]\n"
    "ds_read_b128 v[44:47], v10 offset:7168\n"
    "v_mfma_f32_16x16x32_bf16 v[48:51], v[160:163], v[16:19], v[48:51]\n"
    "v_mfma_f32_16x16x32_bf16 v[52:55], v[164:167], v[16:19], v[52:55]\n"
    "s_waitcnt lgkmcnt(6)\n"
    "v_mfma_f32_16x16x32_bf16 v[64:67], v[168:171], v[16:19], v[64:67]\n"
    "ds_read_b128 v[16:19], v10 offset:8192\n"
    "v_mfma_f32_16x16x32_bf16 v[56:59], v[160:163], v[20:23], v[56:59]\n"
    "v_mfma_f32_16x16x32_bf16 v[60:63], v[164:167], v[20:23], v[60:63]\n"
    "s_waitcnt lgkmcnt(6)\n"
    "v_mfma_f32_16x16x32_bf16 v[68:71], v[168:171], v[20:23], v[68:71]\n"
    "ds_read_b128 v[20:23], v10 offset:9216\n"
    "v_mfma_f32_16x16x32_bf16 v[48:51], v[172:175], v[24:27], v[48:51]\n"
    "v_mfma_f32_16x16x32_bf16 v[52:55], v[176:179], v[24:27], v[52:55]\n"
    "s_waitcnt lgkmcnt(6)\n"
    "v_mfma_f32_16x16x32_bf16 v[64:67], v[180:183], v[24:27], v[64:67]\n"
    "ds_read_b128 v[24:27], v10 offset:10240\n"
    "v_mfma_f32_16x16x32_bf16 v[56:59], v[172:175], v[28:31], v[56:59]\n"
    "v_mfma_f32_16x16x32_bf16 v[60:63], v[176:179], v[28:31], v[60:63]\n"
    "s_waitcnt lgkmcnt(6)\n"
    "v_mfma_f32_16x16x32_bf16 v[68:71], v[180:183], v[28:31], v[68:71]\n"
    "ds_read_b128 v[28:31], v10 offset:11264\n"
    "v_mfma_f32_16x16x32_bf16 v[48:51], v[184:187], v[32:35], v[48:51]\n"
    "v_mfma_f32_16x16x32_bf16 v[52:55], v[188:191], v[32:35], v[52:55]\n"
    "s_waitcnt lgkmcnt(6)\n"
    "v_mfma_f32_16x16x32_bf16 v[64:67], v[192:195], v[32:35], v[64:67]\n"
    "ds_read_b128 v[32:35], v10 offset:12288\n"
    "v_mfma_f32_16x16x32_bf16 v[56:59], v[184:187], v[36:39], v[56:59]\n"
    "v_mfma_f32_16x16x32_bf16 v[60:63], v[188:191], v[36:39], v[60:63]\n"
    "s_waitcnt lgkmcnt(6)\n"
    "v_mfma_f32_16x16x32_bf16 v[68:71], v[192:195], v[36:39], v[68:71]\n"
    "ds_read_b128 v[36:39], v10 offset:13312\n"
    "v_mfma_f32_16x16x32_bf16 v[48:51], v[196:199], v[40:43], v[48:51]\n"
    ".LgWO_seg2_%=:\n"
    "s_cmp_lg_u32 s70, 1\n"
    "s_cbranch_scc1 .LgWO_slow_%=\n"
    "v_mfma_f32_16x16x32_bf16 v[52:55], v[200:203], v[40:43], v[52:55]\n"
    "s_waitcnt lgkmcnt(6)\n"
    "s_mov_b32 s68, s71\n"
    "v_mfma_f32_16x16x32_bf16 v[64:67], v[204:207], v[40:43], v[64:67]\n"
    "ds_read_b128 v[40:43], v10 offset:14336\n"
    "v_mfma_f32_16x16x32_bf16 v[56:59], v[196:199], v[44:47], v[56:59]\n"
    "s_add_u32 s71, s68, 1\n"
    "v_mfma_f32_16x16x32_bf16 v[60:63], v[200:203], v[44:47], v[60:63]\n"
    "s_waitcnt lgkmcnt(6)\n"
    "s_cmp_eq_u32 s71, 64\n"
    "v_mfma_f32_16x16x32_bf16 v[68:71], v[204:207], v[44:47], v[68:71]\n"
    "ds_read_b128 v[44:47], v10 offset:15360\n"
    "v_mfma_f32_16x16x32_bf16 v[64:67], v[216:219], v[16:19], v[64:67]\n"
    "s_waitcnt lgkmcnt(6)\n"
    "ds_read_b128 v[16:19], v10 offset:8192\n"
    "v_mfma_f32_16x16x32_bf16 v[68:71], v[216:219], v[20:23], v[68:71]\n"
    "s_waitcnt lgkmcnt(6)\n"
    "ds_read_b128 v[20:23], v10 offset:10240\n"
    "v_mfma_f32_16x16x32_bf16 v[64:67], v[228:231], v[24:27], v[64:67]\n"
    "s_waitcnt lgkmcnt(6)\n"
    "ds_read_b128 v[24:27], v10 offset:12288\n"
    "v_mfma_f32_16x16x32_bf16 v[68:71], v[228:231], v[28:31], v[68:71]\n"
    "s_waitcnt lgkmcnt(6)\n"
    "ds_read_b128 v[28:31], v10 offset:14336\n"
    "v_mfma_f32_16x16x32_bf16 v[64:67], v[240:243], v[32:35], v[64:67]\n"
    "s_waitcnt lgkmcnt(6)\n"
    "ds_read_b128 v[32:35], v10 offset:9216\n"
    "v_mfma_f32_16x16x32_bf16 v[68:71], v[240:243], v[36:39], v[68:71]\n"
    "s_waitcnt lgkmcnt(6)\n"
    "ds_read_b128 v[36:39], v10 offset:11264\n"
    "v_mfma_f32_16x16x32_bf16 v[64:67], v[252:255], v[40:43], v[64:67]\n"
    "s_waitcnt lgkmcnt(6)\n"
    "ds_read_b128 v[40:43], v10 offset:13312\n"
    "s_cselect_b32 s70, 2, 0\n"
    "v_mfma_f32_16x16x32_bf16 v[68:71], v[252:255], v[44:47], v[68:71]\n"
    "s_waitcnt lgkmcnt(6)\n"
    "ds_read_b128 v[44:47], v10 offset:15360\n"
    "v_add_u32 v13, v11, v3\n"
    "v_mfma_f32_16x16x32_bf16 v[48:51], v[208:211], v[16:19], v[48:51]\n"
    "v_cmp_gt_i32 vcc, s71, v13\n"
    "v_mfma_f32_16x16x32_bf16 v[52:55], v[212:215], v[16:19], v[52:55]\n"
    "s_waitcnt lgkmcnt(6)\n"
    "v_cvt_pk_bf16_f32 v64, v64, v65\n"
    "v_cvt_pk_bf16_f32 v65, v66, v67\n"
    "v_mfma_f32_16x16x32_bf16 v[48:51], v[220:223], v[20:23], v[48:51]\n"
    "s_waitcnt lgkmcnt(5)\n"
    "v_cvt_pk_bf16_f32 v66, v68, v69\n"
    "v_cvt_pk_bf16_f32 v67, v70, v71\n"
    "v_xor_b32 v9, s78, v9\n"
    "v_mfma_f32_16x16x32_bf16 v[52:55], v[224:227], v[20:23], v[52:55]\n"
    "s_waitcnt vmcnt(0) lgkmcnt(0)\n"
    "s_barrier\n"
    "ds_read_b128 v[16:19], v9 offset:0\n"
    "s_nop 0\n"
    "v_mfma_f32_16x16x32_bf16 v[48:51], v[232:235], v[24:27], v[48:51]\n"
    "ds_read_b128 v[20:23], v9 offset:1024\n"
    "s_or_b64 exec, vcc, 1\n"
    "buffer_atomic_smax v11, v2, s[36:39], 0 offen sc0 sc1\n"
    "s_mov_b64 exec, -1\n"
    "v_mfma_f32_16x16x32_bf16 v[52:55], v[236:239], v[24:27], v[52:55]\n"
    "ds_read_b128 v[24:27], v9 offset:2048\n"
    "v_mfma_f32_16x16x32_bf16 v[48:51], v[244:247], v[28:31], v[48:51]\n"
    "v_permlane16_swap_b32 v64, v66\n"
    "v_mfma_f32_16x16x32_bf16 v[52:55], v[248:251], v[28:31], v[52:55]\n"
    "ds_read_b128 v[28:31], v9 offset:3072\n"
    "v_mfma_f32_16x16x32_bf16 v[56:59], v[208:211], v[32:35], v[56:59]\n"
    "v_permlane16_swap_b32 v65, v67\n"
    "v_mfma_f32_16x16x32_bf16 v[60:63], v[212:215], v[32:35], v[60:63]\n"
    "ds_read_b128 v[32:35], v9 offset:4096\n"
    "v_cvt_pk_bf16_f32 v48, v48, v49\n"
    "v_mfma_f32_16x16x32_bf16 v[56:59], v[220:223], v[36:39], v[56:59]\n"
    "buffer_store_dwordx4 v[64:67], v4, s[36:39], s73 offen sc1\n"
    "v_cvt_pk_bf16_f32 v49, v50, v51\n"
    "v_cvt_pk_bf16_f32 v50, v52, v53\n"
    "v_mfma_f32_16x16x32_bf16 v[60:63], v[224:227], v[36:39], v[60:63]\n"
    "ds_read_b128 v[36:39], v9 offset:5120\n"
    "v_cvt_pk_bf16_f32 v51, v54, v55\n"
    "v_mfma_f32_16x16x32_bf16 v[56:59], v[232:235], v[40:43], v[56:59]\n"
    "buffer_store_dwordx4 v[48:51], v15, s[36:39], s73 offen sc1\n"
    "v_mfma_f32_16x16x32_bf16 v[60:63], v[236:239], v[40:43], v[60:63]\n"
    "ds_read_b128 v[40:43], v9 offset:6144\n"
    "v_mfma_f32_16x16x32_bf16 v[56:59], v[244:247], v[44:47], v[56:59]\n"
    "s_waitcnt lgkmcnt(6)\n"
    "v_mfma_f32_16x16x32_bf16 v[60:63], v[248:251], v[44:47], v[60:63]\n"
    "ds_read_b128 v[44:47], v9 offset:7168\n"
    "s_branch .LgWO_head_%=\n"
    ".LgWO_LOOPE_%=:\n"
    ".LgWO_done_%=:\n"
    "s_waitcnt vmcnt(0)\n"
    "s_mov_b32 s74, 63\n"
    "v_mov_b32 v12, s74\n"
    "s_mov_b64 exec, s[62:63]\n"
    "buffer_store_dword v12, v14, s[36:39], 0 offen sc1\n"
    "s_mov_b64 exec, -1\n"
    "s_waitcnt vmcnt(0)\n"
    "s_branch .LgWO_FIN_%=\n"
    ".LgWO_chk0_%=:\n"
    "s_or_b64 exec, vcc, 1\n"
    "buffer_atomic_smax v11, v2, s[36:39], 0 offen sc0 sc1\n"
    "s_mov_b64 exec, -1\n"
    "v_mfma_f32_16x16x32_bf16 v[48:51], a[96:99], v[16:19], v[48:51]\n"
    "s_and_b32 s73, s68, s57\n"
    "v_mfma_f32_16x16x32_bf16 v[52:55], a[100:103], v[16:19], v[52:55]\n"
    "s_waitcnt lgkmcnt(6)\n"
    "s_mul_i32 s73, s73, s56\n"
    "v_mfma_f32_16x16x32_bf16 v[64:67], a[104:107], v[16:19], v[64:67]\n"
    "ds_read_b128 v[16:19], v9 offset:24576\n"
    "v_mfma_f32_16x16x32_bf16 v[56:59], a[96:99], v[20:23], v[56:59]\n"
    "s_add_u32 s73, s73, s55\n"
    "v_mfma_f32_16x16x32_bf16 v[60:63], a[100:103], v[20:23], v[60:63]\n"
    "s_waitcnt lgkmcnt(6)\n"
    "v_mfma_f32_16x16x32_bf16 v[68:71], a[104:107], v[20:23], v[68:71]\n"
    "ds_read_b128 v[20:23], v9 offset:25600\n"
    "v_mfma_f32_16x16x32_bf16 v[48:51], a[108:111], v[24:27], v[48:51]\n"
    "v_mfma_f32_16x16x32_bf16 v[52:55], a[112:115], v[24:27], v[52:55]\n"
    "s_waitcnt lgkmcnt(6)\n"
    "v_mfma_f32_16x16x32_bf16 v[64:67], a[116:119], v[24:27], v[64:67]\n"
    "ds_read_b128 v[24:27], v9 offset:26624\n"
    "v_mfma_f32_16x16x32_bf16 v[56:59], a[108:111], v[28:31], v[56:59]\n"
    "v_mfma_f32_16x16x32_bf16 v[60:63], a[112:115], v[28:31], v[60:63]\n"
    "s_waitcnt lgkmcnt(6)\n"
    "v_mfma_f32_16x16x32_bf16 v[68:71], a[116:119], v[28:31], v[68:71]\n"
    "ds_read_b128 v[28:31], v9 offset:27648\n"
    "v_mfma_f32_16x16x32_bf16 v[48:51], a[120:123], v[32:35], v[48:51]\n"
    "v_mfma_f32_16x16x32_bf16 v[52:55], a[124:127], v[32:35], v[52:55]\n"
    "s_waitcnt lgkmcnt(6)\n"
    "v_mfma_f32_16x16x32_bf16 v[64:67], a[128:131], v[32:35], v[64:67]\n"
    "ds_read_b128 v[32:35], v9 offset:28672\n"
    "v_mfma_f32_16x16x32_bf16 v[56:59], a[120:123], v[36:39], v[56:59]\n"
    "v_mfma_f32_16x16x32_bf16 v[60:63], a[124:127], v[36:39], v[60:63]\n"
    "s_waitcnt lgkmcnt(6)\n"
    "v_mfma_f32_16x16x32_bf16 v[68:71], a[128:131], v[36:39], v[68:71]\n"
    "ds_read_b128 v[36:39], v9 offset:29696\n"
    "v_mfma_f32_16x16x32_bf16 v[48:51], a[132:135], v[40:43], v[48:51]\n"
    "v_mfma_f32_16x16x32_bf16 v[52:55], a[136:139], v[40:43], v[52:55]\n"
    "s_waitcnt lgkmcnt(6)\n"
    "v_mfma_f32_16x16x32_bf16 v[64:67], a[140:143], v[40:43], v[64:67]\n"
    "ds_read_b128 v[40:43], v9 offset:30720\n"
    "v_mfma_f32_16x16x32_bf16 v[56:59], a[132:135], v[44:47], v[56:59]\n"
    "v_mfma_f32_16x16x32_bf16 v[60:63], a[136:139], v[44:47], v[60:63]\n"
    "s_waitcnt lgkmcnt(6)\n"
    "v_mfma_f32_16x16x32_bf16 v[68:71], a[140:143], v[44:47], v[68:71]\n"
    "ds_read_b128 v[44:47], v9 offset:31744\n"
    "v_mfma_f32_16x16x32_bf16 v[48:51], a[144:147], v[16:19], v[48:51]\n"
    "v_mfma_f32_16x16x32_bf16 v[52:55], a[148:151], v[16:19], v[52:55]\n"
    "s_waitcnt lgkmcnt(6)\n"
    "v_mfma_f32_16x16x32_bf16 v[64:67], a[152:155], v[16:19], v[64:67]\n"
    "ds_read_b128 v[16:19], v9 offset:32768\n"
    "v_mfma_f32_16x16x32_bf16 v[56:59], a[144:147], v[20:23], v[56:59]\n"
    "v_mfma_f32_16x16x32_bf16 v[60:63], a[148:151], v[20:23], v[60:63]\n"
    "s_waitcnt lgkmcnt(6)\n"
    "v_mfma_f32_16x16x32_bf16 v[68:71], a[152:155], v[20:23], v[68:71]\n"
    "ds_read_b128 v[20:23], v9 offset:33792\n"
    "v_mfma_f32_16x16x32_bf16 v[48:51], a[156:159], v[24:27], v[48:51]\n"
    "v_mfma_f32_16x16x32_bf16 v[52:55], a[160:163], v[24:27], v[52:55]\n"
    "s_waitcnt lgkmcnt(6)\n"
    "v_mfma_f32_16x16x32_bf16 v[64:67], a[164:167], v[24:27], v[64:67]\n"
    "ds_read_b128 v[24:27], v9 offset:34816\n"
    "v_mfma_f32_16x16x32_bf16 v[56:59], a[156:159], v[28:31], v[56:59]\n"
    "v_mfma_f32_16x16x32_bf16 v[60:63], a[160:163], v[28:31], v[60:63]\n"
    "s_waitcnt lgkmcnt(6)\n"
    "v_mfma_f32_16x16x32_bf16 v[68:71], a[164:167], v[28:31], v[68:71]\n"
    "ds_read_b128 v[28:31], v9 offset:35840\n"
    "v_mfma_f32_16x16x32_bf16 v[48:51], a[168:171], v[32:35], v[48:51]\n"
    "v_mfma_f32_16x16x32_bf16 v[52:55], a[172:175], v[32:35], v[52:55]\n"
    "s_waitcnt lgkmcnt(6)\n"
    "s_branch .LgWO_seg0_%=\n"
    ".LgWO_chk1_%=:\n"
    "s_waitcnt vmcnt(0)\n"
    "v_add_u32 v13, v11, v3\n"
    "v_cmp_gt_i32 vcc, s71, v13\n"
    "s_cbranch_vccz .LgWO_dma1_%=\n"
    "s_or_b64 exec, vcc, 1\n"
    "buffer_atomic_smax v11, v2, s[36:39], 0 offen sc0 sc1\n"
    "s_mov_b64 exec, -1\n"
    "s_branch .LgWO_plain1_%=\n"
    ".LgWO_dma1_%=:\n"
    "s_mov_b32 s70, 1\n"
    "v_mfma_f32_16x16x32_bf16 v[64:67], a[176:179], v[32:35], v[64:67]\n"
    "ds_read_b128 v[32:35], v9 offset:36864\n"
    "v_mfma_f32_16x16x32_bf16 v[56:59], a[168:171], v[36:39], v[56:59]\n"
    "s_add_u32 m0, s77, s59\n"
    "s_add_u32 s77, s77, 8192\n"
    "buffer_load_dwordx4 v1, s[36:39], s76 offen sc1 lds\n"
    "v_mfma_f32_16x16x32_bf16 v[60:63], a[172:175], v[36:39], v[60:63]\n"
    "s_waitcnt lgkmcnt(6)\n"
    "buffer_load_dwordx4 v1, s[36:39], s76 offen offset:1024 sc1 lds\n"
    "s_add_u32 s76, s76, 16384\n"
    "v_mfma_f32_16x16x32_bf16 v[68:71], a[176:179], v[36:39], v[68:71]\n"
    "ds_read_b128 v[36:39], v9 offset:37888\n"
    "v_mfma_f32_16x16x32_bf16 v[48:51], a[180:183], v[40:43], v[48:51]\n"
    "v_mfma_f32_16x16x32_bf16 v[52:55], a[184:187], v[40:43], v[52:55]\n"
    "s_waitcnt lgkmcnt(6)\n"
    "s_add_u32 m0, s77, s59\n"
    "s_add_u32 s77, s77, 8192\n"
    "buffer_load_dwordx4 v1, s[36:39], s76 offen sc1 lds\n"
    "v_mfma_f32_16x16x32_bf16 v[64:67], a[188:191], v[40:43], v[64:67]\n"
    "ds_read_b128 v[40:43], v9 offset:38912\n"
    "v_mfma_f32_16x16x32_bf16 v[56:59], a[180:183], v[44:47], v[56:59]\n"
    "buffer_load_dwordx4 v1, s[36:39], s76 offen offset:1024 sc1 lds\n"
    "s_add_u32 s76, s76, 16384\n"
    "v_mfma_f32_16x16x32_bf16 v[60:63], a[184:187], v[44:47], v[60:63]\n"
    "s_waitcnt lgkmcnt(6)\n"
    "v_mfma_f32_16x16x32_bf16 v[68:71], a[188:191], v[44:47], v[68:71]\n"
    "ds_read_b128 v[44:47], v9 offset:39936\n"
    "v_mfma_f32_16x16x32_bf16 v[48:51], a[192:195], v[16:19], v[48:51]\n"
    "s_add_u32 m0, s77, s59\n"
    "s_add_u32 s77, s77, 8192\n"
    "buffer_load_dwordx4 v1, s[36:39], s76 offen sc1 lds\n"
    "v_mfma_f32_16x16x32_bf16 v[52:55], a[196:199], v[16:19], v[52:55]\n"
    "s_waitcnt lgkmcnt(6)\n"
    "buffer_load_dwordx4 v1, s[36:39], s76 offen offset:1024 sc1 lds\n"
    "s_add_u32 s76, s76, 16384\n"
    "v_mfma_f32_16x16x32_bf16 v[64:67], a[200:203], v[16:19], v[64:67]\n"
    "ds_read_b128 v[16:19], v9 offset:40960\n"
    "v_mfma_f32_16x16x32_bf16 v[56:59], a[192:195], v[20:23], v[56:59]\n"
    "v_mfma_f32_16x16x32_bf16 v[60:63], a[196:199], v[20:23], v[60:63]\n"
    "s_waitcnt lgkmcnt(6)\n"
    "s_add_u32 m0, s77, s59\n"
    "s_add_u32 s77, s77, 8192\n"
    "buffer_load_dwordx4 v1, s[36:39], s76 offen sc1 lds\n"
    "v_mfma_f32_16x16x32_bf16 v[68:71], a[200:203], v[20:23], v[68:71]\n"
    "ds_read_b128 v[20:23], v9 offset:41984\n"
    "v_mfma_f32_16x16x32_bf16 v[48:51], a[204:207], v[24:27], v[48:51]\n"
    "buffer_load_dwordx4 v1, s[36:39], s76 offen offset:1024 sc1 lds\n"
    "s_add_u32 s76, s76, 16384\n"
    "v_mfma_f32_16x16x32_bf16 v[52:55], a[208:211], v[24:27], v[52:55]\n"
    "s_waitcnt lgkmcnt(6)\n"
    "v_mfma_f32_16x16x32_bf16 v[64:67], a[212:215], v[24:27], v[64:67]\n"
    "ds_read_b128 v[24:27], v9 offset:43008\n"
    "v_mfma_f32_16x16x32_bf16 v[56:59], a[204:207], v[28:31], v[56:59]\n"
    "s_add_u32 m0, s77, s59\n"
    "s_add_u32 s77, s77, 8192\n"
    "buffer_load_dwordx4 v1, s[36:39], s76 offen sc1 lds\n"
    "v_mfma_f32_16x16x32_bf16 v[60:63], a[208:211], v[28:31], v[60:63]\n"
    "s_waitcnt lgkmcnt(6)\n"
    "v_mfma_f32_16x16x32_bf16 v[68:71], a[212:215], v[28:31], v[68:71]\n"
    "ds_read_b128 v[28:31], v9 offset:44032\n"
    "v_mfma_f32_16x16x32_bf16 v[48:51], a[216:219], v[32:35], v[48:51]\n"
    "buffer_load_dwordx4 v1, s[36:39], s76 offen offset:1024 sc1 lds\n"
    "s_add_u32 s76, s76, 16384\n"
    "v_mfma_f32_16x16x32_bf16 v[52:55], a[220:223], v[32:35], v[52:55]\n"
    "s_waitcnt lgkmcnt(6)\n"
    "s_add_u32 m0, s77, s59\n"
    "s_add_u32 s77, s77, 8192\n"
    "buffer_load_dwordx4 v1, s[36:39], s76 offen sc1 lds\n"
    "v_mfma_f32_16x16x32_bf16 v[64:67], a[224:227], v[32:35], v[64:67]\n"
    "ds_read_b128 v[32:35], v9 offset:45056\n"
    "v_mfma_f32_16x16x32_bf16 v[56:59], a[216:219], v[36:39], v[56:59]\n"
    "v_mfma_f32_16x16x32_bf16 v[60:63], a[220:223], v[36:39], v[60:63]\n"
    "s_waitcnt lgkmcnt(6)\n"
    "buffer_load_dwordx4 v1, s[36:39], s76 offen offset:1024 sc1 lds\n"
    "s_add_u32 s76, s76, 16384\n"
    "v_mfma_f32_16x16x32_bf16 v[68:71], a[224:227], v[36:39], v[68:71]\n"
    "ds_read_b128 v[36:39], v9 offset:46080\n"
    "v_mfma_f32_16x16x32_bf16 v[48:51], a[228:231], v[40:43], v[48:51]\n"
    "s_add_u32 m0, s77, s59\n"
    "s_add_u32 s77, s77, 8192\n"
    "buffer_load_dwordx4 v1, s[36:39], s76 offen sc1 lds\n"
    "v_mfma_f32_16x16x32_bf16 v[52:55], a[232:235], v[40:43], v[52:55]\n"
    "s_waitcnt lgkmcnt(6)\n"
    "v_mfma_f32_16x16x32_bf16 v[64:67], a[236:239], v[40:43], v[64:67]\n"
    "ds_read_b128 v[40:43], v9 offset:47104\n"
    "v_mfma_f32_16x16x32_bf16 v[56:59], a[228:231], v[44:47], v[56:59]\n"
    "buffer_load_dwordx4 v1, s[36:39], s76 offen offset:1024 sc1 lds\n"
    "s_add_u32 s76, s76, 16384\n"
    "v_mfma_f32_16x16x32_bf16 v[60:63], a[232:235], v[44:47], v[60:63]\n"
    "s_waitcnt lgkmcnt(6)\n"
    "s_add_u32 m0, s77, s59\n"
    "s_add_u32 s77, s77, 8192\n"
    "buffer_load_dwordx4 v1, s[36:39], s76 offen sc1 lds\n"
    "v_mfma_f32_16x16x32_bf16 v[68:71], a[236:239], v[44:47], v[68:71]\n"
    "ds_read_b128 v[44:47], v9 offset:48128\n"
    "v_mfma_f32_16x16x32_bf16 v[48:51], a[240:243], v[16:19], v[48:51]\n"
    "v_mfma_f32_16x16x32_bf16 v[52:55], a[244:247], v[16:19], v[52:55]\n"
    "s_waitcnt lgkmcnt(6)\n"
    "buffer_load_dwordx4 v1, s[36:39], s76 offen offset:1024 sc1 lds\n"
    "s_add_u32 s76, s76, 16384\n"
    "v_mfma_f32_16x16x32_bf16 v[64:67], a[248:251], v[16:19], v[64:67]\n"
    "ds_read_b128 v[16:19], v10 offset:0\n"
    "v_mfma_f32_16x16x32_bf16 v[56:59], a[240:243], v[20:23], v[56:59]\n"
    "v_mfma_f32_16x16x32_bf16 v[60:63], a[244:247], v[20:23], v[60:63]\n"
    "s_waitcnt lgkmcnt(6)\n"
    "s_branch .LgWO_seg1_%=\n"
    ".LgWO_chk2_%=:\n"
    "s_waitcnt vmcnt(0)\n"
    "v_add_u32 v13, v11, v3\n"
    "v_cmp_gt_i32 vcc, s71, v13\n"
    "s_cbranch_vccz .LgWO_dma2_%=\n"
    "s_or_b64 exec, vcc, 1\n"
    "buffer_atomic_smax v11, v2, s[36:39], 0 offen sc0 sc1\n"
    "s_mov_b64 exec, -1\n"
    "s_branch .LgWO_plain2_%=\n"
    ".LgWO_dma2_%=:\n"
    "s_mov_b32 s70, 2\n"
    "v_mfma_f32_16x16x32_bf16 v[68:71], a[248:251], v[20:23], v[68:71]\n"
    "ds_read_b128 v[20:23], v10 offset:1024\n"
    "v_mfma_f32_16x16x32_bf16 v[48:51], a[252:255], v[24:27], v[48:51]\n"
    "s_add_u32 m0, s77, s59\n"
    "s_add_u32 s77, s77, 8192\n"
    "buffer_load_dwordx4 v1, s[36:39], s76 offen sc1 lds\n"
    "v_mfma_f32_16x16x32_bf16 v[52:55], v[128:131], v[24:27], v[52:55]\n"
    "s_waitcnt lgkmcnt(6)\n"
    "buffer_load_dwordx4 v1, s[36:39], s76 offen offset:1024 sc1 lds\n"
    "s_add_u32 s76, s76, 16384\n"
    "v_mfma_f32_16x16x32_bf16 v[64:67], v[132:135], v[24:27], v[64:67]\n"
    "ds_read_b128 v[24:27], v10 offset:2048\n"
    "v_mfma_f32_16x16x32_bf16 v[56:59], a[252:255], v[28:31], v[56:59]\n"
    "v_mfma_f32_16x16x32_bf16 v[60:63], v[128:131], v[28:31], v[60:63]\n"
    "s_waitcnt lgkmcnt(6)\n"
    "s_add_u32 m0, s77, s59\n"
    "s_add_u32 s77, s77, 8192\n"
    "buffer_load_dwordx4 v1, s[36:39], s76 offen sc1 lds\n"
    "v_mfma_f32_16x16x32_bf16 v[68:71], v[132:135], v[28:31], v[68:71]\n"
    "ds_read_b128 v[28:31], v10 offset:3072\n"
    "v_mfma_f32_16x16x32_bf16 v[48:51], v[136:139], v[32:35], v[48:51]\n"
    "buffer_load_dwordx4 v1, s[36:39], s76 offen offset:1024 sc1 lds\n"
    "s_add_u32 s76, s76, 16384\n"
    "v_mfma_f32_16x16x32_bf16 v[52:55], v[140:143], v[32:35], v[52:55]\n"
    "s_waitcnt lgkmcnt(6)\n"
    "v_mfma_f32_16x16x32_bf16 v[64:67], v[144:147], v[32:35], v[64:67]\n"
    "ds_read_b128 v[32:35], v10 offset:4096\n"
    "v_mfma_f32_16x16x32_bf16 v[56:59], v[136:139], v[36:39], v[56:59]\n"
    "s_add_u32 m0, s77, s59\n"
    "s_add_u32 s77, s77, 8192\n"
    "buffer_load_dwordx4 v1, s[36:39], s76 offen sc1 lds\n"
    "v_mfma_f32_16x16x32_bf16 v[60:63], v[140:143], v[36:39], v[60:63]\n"
    "s_waitcnt lgkmcnt(6)\n"
    "buffer_load_dwordx4 v1, s[36:39], s76 offen offset:1024 sc1 lds\n"
    "s_add_u32 s76, s76, 16384\n"
    "v_mfma_f32_16x16x32_bf16 v[68:71], v[144:147], v[36:39], v[68:71]\n"
    "ds_read_b128 v[36:39], v10 offset:5120\n"
    "v_mfma_f32_16x16x32_bf16 v[48:51], v[148:151], v[40:43], v[48:51]\n"
    "v_mfma_f32_16x16x32_bf16 v[52:55], v[152:155], v[40:43], v[52:55]\n"
    "s_waitcnt lgkmcnt(6)\n"
    "s_add_u32 m0, s77, s59\n"
    "s_add_u32 s77, s77, 8192\n"
    "buffer_load_dwordx4 v1, s[36:39], s76 offen sc1 lds\n"
    "v_mfma_f32_16x16x32_bf16 v[64:67], v[156:159], v[40:43], v[64:67]\n"
    "ds_read_b128 v[40:43], v10 offset:6144\n"
    "v_mfma_f32_16x16x32_bf16 v[56:59], v[148:151], v[44:47], v[56:59]\n"
    "buffer_load_dwordx4 v1, s[36:39], s76 offen offset:1024 sc1 lds\n"
    "s_add_u32 s76, s76, 16384\n"
    "v_mfma_f32_16x16x32_bf16 v[60:63], v[152:155], v[44:47], v[60:63]\n"
    "s_waitcnt lgkmcnt(6)\n"
    "v_mfma_f32_16x16x32_bf16 v[68:71], v[156:159], v[44:47], v[68:71]\n"
    "ds_read_b128 v[44:47], v10 offset:7168\n"
    "v_mfma_f32_16x16x32_bf16 v[48:51], v[160:163], v[16:19], v[48:51]\n"
    "s_add_u32 m0, s77, s59\n"
    "s_add_u32 s77, s77, 8192\n"
    "buffer_load_dwordx4 v1, s[36:39], s76 offen sc1 lds\n"
    "v_mfma_f32_16x16x32_bf16 v[52:55], v[164:167], v[16:19], v[52:55]\n"
    "s_waitcnt lgkmcnt(6)\n"
    "buffer_load_dwordx4 v1, s[36:39], s76 offen offset:1024 sc1 lds\n"
    "s_add_u32 s76, s76, 16384\n"
    "v_mfma_f32_16x16x32_bf16 v[64:67], v[168:171], v[16:19], v[64:67]\n"
    "ds_read_b128 v[16:19], v10 offset:8192\n"
    "v_mfma_f32_16x16x32_bf16 v[56:59], v[160:163], v[20:23], v[56:59]\n"
    "v_mfma_f32_16x16x32_bf16 v[60:63], v[164:167], v[20:23], v[60:63]\n"
    "s_waitcnt lgkmcnt(6)\n"
    "s_add_u32 m0, s77, s59\n"
    "s_add_u32 s77, s77, 8192\n"
    "buffer_load_dwordx4 v1, s[36:39], s76 offen sc1 lds\n"
    "v_mfma_f32_16x16x32_bf16 v[68:71], v[168:171], v[20:23], v[68:71]\n"
    "ds_read_b128 v[20:23], v10 offset:9216\n"
    "v_mfma_f32_16x16x32_bf16 v[48:51], v[172:175], v[24:27], v[48:51]\n"
    "buffer_load_dwordx4 v1, s[36:39], s76 offen offset:1024 sc1 lds\n"
    "s_add_u32 s76, s76, 16384\n"
    "v_mfma_f32_16x16x32_bf16 v[52:55], v[176:179], v[24:27], v[52:55]\n"
    "s_waitcnt lgkmcnt(6)\n"
    "v_mfma_f32_16x16x32_bf16 v[64:67], v[180:183], v[24:27], v[64:67]\n"
    "ds_read_b128 v[24:27], v10 offset:10240\n"
    "v_mfma_f32_16x16x32_bf16 v[56:59], v[172:175], v[28:31], v[56:59]\n"
    "s_add_u32 m0, s77, s59\n"
    "s_add_u32 s77, s77, 8192\n"
    "buffer_load_dwordx4 v1, s[36:39], s76 offen sc1 lds\n"
    "v_mfma_f32_16x16x32_bf16 v[60:63], v[176:179], v[28:31], v[60:63]\n"
    "s_waitcnt lgkmcnt(6)\n"
    "buffer_load_dwordx4 v1, s[36:39], s76 offen offset:1024 sc1 lds\n"
    "s_add_u32 s76, s76, 16384\n"
    "v_mfma_f32_16x16x32_bf16 v[68:71], v[180:183], v[28:31], v[68:71]\n"
    "ds_read_b128 v[28:31], v10 offset:11264\n"
    "v_mfma_f32_16x16x32_bf16 v[48:51], v[184:187], v[32:35], v[48:51]\n"
    "v_mfma_f32_16x16x32_bf16 v[52:55], v[188:191], v[32:35], v[52:55]\n"
    "s_waitcnt lgkmcnt(6)\n"
    "s_add_u32 m0, s77, s59\n"
    "s_add_u32 s77, s77, 8192\n"
    "buffer_load_dwordx4 v1, s[36:39], s76 offen sc1 lds\n"
    "v_mfma_f32_16x16x32_bf16 v[64:67], v[192:195], v[32:35], v[64:67]\n"
    "ds_read_b128 v[32:35], v10 offset:12288\n"
    "v_mfma_f32_16x16x32_bf16 v[56:59], v[184:187], v[36:39], v[56:59]\n"
    "buffer_load_dwordx4 v1, s[36:39], s76 offen offset:1024 sc1 lds\n"
    "s_add_u32 s76, s76, 16384\n"
    "v_mfma_f32_16x16x32_bf16 v[60:63], v[188:191], v[36:39], v[60:63]\n"
    "s_waitcnt lgkmcnt(6)\n"
    "v_mfma_f32_16x16x32_bf16 v[68:71], v[192:195], v[36:39], v[68:71]\n"
    "ds_read_b128 v[36:39], v10 offset:13312\n"
    "v_mfma_f32_16x16x32_bf16 v[48:51], v[196:199], v[40:43], v[48:51]\n"
    "s_branch .LgWO_seg2_%=\n"
    ".LgWO_slow_%=:\n"
    "s_cmp_lg_u32 s70, 0\n"
    "s_cbranch_scc1 .LgWO_nolast_%=\n"
    "s_waitcnt vmcnt(0)\n"
    "v_add_u32 v13, v11, v3\n"
    "v_cmp_gt_i32 vcc, s71, v13\n"
    "s_nop 0\n"
    "s_or_b64 exec, vcc, 1\n"
    "buffer_atomic_smax v11, v2, s[36:39], 0 offen sc0 sc1\n"
    "s_mov_b64 exec, -1\n"
    ".LgWO_nolast_%=:\n"
    "v_mfma_f32_16x16x32_bf16 v[52:55], v[200:203], v[40:43], v[52:55]\n"
    "s_waitcnt lgkmcnt(6)\n"
    "v_mfma_f32_16x16x32_bf16 v[64:67], v[204:207], v[40:43], v[64:67]\n"
    "ds_read_b128 v[40:43], v10 offset:14336\n"
    "v_mfma_f32_16x16x32_bf16 v[56:59], v[196:199], v[44:47], v[56:59]\n"
    "v_mfma_f32_16x16x32_bf16 v[60:63], v[200:203], v[44:47], v[60:63]\n"
    "s_waitcnt lgkmcnt(6)\n"
    "v_mfma_f32_16x16x32_bf16 v[68:71], v[204:207], v[44:47], v[68:71]\n"
    "ds_read_b128 v[44:47], v10 offset:15360\n"
    "v_mfma_f32_16x16x32_bf16 v[64:67], v[216:219], v[16:19], v[64:67]\n"
    "s_waitcnt lgkmcnt(6)\n"
    "ds_read_b128 v[16:19], v10 offset:8192\n"
    "v_mfma_f32_16x16x32_bf16 v[68:71], v[216:219], v[20:23], v[68:71]\n"
    "s_waitcnt lgkmcnt(6)\n"
    "ds_read_b128 v[20:23], v10 offset:10240\n"
    "v_mfma_f32_16x16x32_bf16 v[64:67], v[228:231], v[24:27], v[64:67]\n"
    "s_waitcnt lgkmcnt(6)\n"
    "ds_read_b128 v[24:27], v10 offset:12288\n"
    "v_mfma_f32_16x16x32_bf16 v[68:71], v[228:231], v[28:31], v[68:71]\n"
    "s_waitcnt lgkmcnt(6)\n"
    "ds_read_b128 v[28:31], v10 offset:14336\n"
    "v_mfma_f32_16x16x32_bf16 v[64:67], v[240:243], v[32:35], v[64:67]\n"
    "s_waitcnt lgkmcnt(6)\n"
    "ds_read_b128 v[32:35], v10 offset:9216\n"
    "v_mfma_f32_16x16x32_bf16 v[68:71], v[240:243], v[36:39], v[68:71]\n"
    "s_waitcnt lgkmcnt(6)\n"
    "ds_read_b128 v[36:39], v10 offset:11264\n"
    "v_mfma_f32_16x16x32_bf16 v[64:67], v[252:255], v[40:43], v[64:67]\n"
    "s_waitcnt lgkmcnt(6)\n"
    "ds_read_b128 v[40:43], v10 offset:13312\n"
    "v_mfma_f32_16x16x32_bf16 v[68:71], v[252:255], v[44:47], v[68:71]\n"
    "s_waitcnt lgkmcnt(6)\n"
    "ds_read_b128 v[44:47], v10 offset:15360\n"
    "v_mfma_f32_16x16x32_bf16 v[48:51], v[208:211], v[16:19], v[48:51]\n"
    "v_mfma_f32_16x16x32_bf16 v[52:55], v[212:215], v[16:19], v[52:55]\n"
    "s_waitcnt lgkmcnt(6)\n"
    "v_cvt_pk_bf16_f32 v64, v64, v65\n"
    "v_cvt_pk_bf16_f32 v65, v66, v67\n"
    "v_mfma_f32_16x16x32_bf16 v[48:51], v[220:223], v[20:23], v[48:51]\n"
    "s_waitcnt lgkmcnt(5)\n"
    "v_cvt_pk_bf16_f32 v66, v68, v69\n"
    "v_cvt_pk_bf16_f32 v67, v70, v71\n"
    "v_mfma_f32_16x16x32_bf16 v[52:55], v[224:227], v[20:23], v[52:55]\n"
    "s_nop 0\n"
    "v_mfma_f32_16x16x32_bf16 v[48:51], v[232:235], v[24:27], v[48:51]\n"
    "v_mfma_f32_16x16x32_bf16 v[52:55], v[236:239], v[24:27], v[52:55]\n"
    "s_waitcnt lgkmcnt(4)\n"
    "v_mfma_f32_16x16x32_bf16 v[48:51], v[244:247], v[28:31], v[48:51]\n"
    "s_waitcnt lgkmcnt(3)\n"
    "v_permlane16_swap_b32 v64, v66\n"
    "v_mfma_f32_16x16x32_bf16 v[52:55], v[248:251], v[28:31], v[52:55]\n"
    "v_mfma_f32_16x16x32_bf16 v[56:59], v[208:211], v[32:35], v[56:59]\n"
    "s_waitcnt lgkmcnt(2)\n"
    "v_permlane16_swap_b32 v65, v67\n"
    "v_mfma_f32_16x16x32_bf16 v[60:63], v[212:215], v[32:35], v[60:63]\n"
    "v_cvt_pk_bf16_f32 v48, v48, v49\n"
    "v_mfma_f32_16x16x32_bf16 v[56:59], v[220:223], v[36:39], v[56:59]\n"
    "s_waitcnt lgkmcnt(1)\n"
    "buffer_store_dwordx4 v[64:67], v4, s[36:39], s73 offen sc1\n"
    "v_cvt_pk_bf16_f32 v49, v50, v51\n"
    "v_cvt_pk_bf16_f32 v50, v52, v53\n"
    "v_mfma_f32_16x16x32_bf16 v[60:63], v[224:227], v[36:39], v[60:63]\n"
    "v_cvt_pk_bf16_f32 v51, v54, v55\n"
    "v_mfma_f32_16x16x32_bf16 v[56:59], v[232:235], v[40:43], v[56:59]\n"
    "s_waitcnt lgkmcnt(0)\n"
    "buffer_store_dwordx4 v[48:51], v15, s[36:39], s73 offen sc1\n"
    "v_mfma_f32_16x16x32_bf16 v[60:63], v[236:239], v[40:43], v[60:63]\n"
    "v_mfma_f32_16x16x32_bf16 v[56:59], v[244:247], v[44:47], v[56:59]\n"
    "v_mfma_f32_16x16x32_bf16 v[60:63], v[248:251], v[44:47], v[60:63]\n"
    "s_nop 3\n"
    "v_cvt_pk_bf16_f32 v56, v56, v57\n"
    "v_cvt_pk_bf16_f32 v57, v58, v59\n"
    "s_nop 1\n"
    "v_cvt_pk_bf16_f32 v58, v60, v61\n"
    "v_cvt_pk_bf16_f32 v59, v62, v63\n"
    "buffer_store_dwordx4 v[56:59], v15, s[36:39], s73 offen offset:1024 sc1\n"
    "s_mov_b32 s68, s71\n"
    "s_cmp_eq_u32 s70, 0\n"
    "s_cbranch_scc1 .LgWO_idle_%=\n"
    "s_cmp_eq_u32 s68, 64\n"
    "s_cbranch_scc1 .LgWO_done_%=\n"
    "s_waitcnt vmcnt(3)\n"
    ".LgWO_bar_%=:\n"
    "v_xor_b32 v9, s78, v9\n"
    "s_add_u32 s71, s68, 1\n"
    "s_cmp_eq_u32 s71, 64\n"
    "s_cselect_b32 s70, 2, 0\n"
    "s_barrier\n"
    "ds_read_b128 v[16:19], v9 offset:0\n"
    "ds_read_b128 v[20:23], v9 offset:1024\n"
    "ds_read_b128 v[24:27], v9 offset:2048\n"
    "ds_read_b128 v[28:31], v9 offset:3072\n"
    "ds_read_b128 v[32:35], v9 offset:4096\n"
    "ds_read_b128 v[36:39], v9 offset:5120\n"
    "ds_read_b128 v[40:43], v9 offset:6144\n"
    "ds_read_b128 v[44:47], v9 offset:7168\n"
    "v_add_u32 v13, v11, v3\n"
    "v_cmp_gt_i32 vcc, s71, v13\n"
    "s_nop 0\n"
    "s_or_b64 exec, vcc, 1\n"
    "buffer_atomic_smax v11, v2, s[36:39], 0 offen sc0 sc1\n"
    "s_mov_b64 exec, -1\n"
    "s_waitcnt lgkmcnt(7)\n"
    "v_mfma_f32_16x16x32_bf16 v[64:67], a[8:11], v[16:19], 0\n"
    "s_waitcnt lgkmcnt(6)\n"
    "ds_read_b128 v[16:19], v9 offset:0\n"
    "v_mfma_f32_16x16x32_bf16 v[68:71], a[8:11], v[20:23], 0\n"
    "s_waitcnt lgkmcnt(6)\n"
    "ds_read_b128 v[20:23], v9 offset:2048\n"
    "v_mfma_f32_16x16x32_bf16 v[64:67], a[20:23], v[24:27], v[64:67]\n"
    "s_waitcnt lgkmcnt(6)\n"
    "ds_read_b128 v[24:27], v9 offset:4096\n"
    "v_mfma_f32_16x16x32_bf16 v[68:71], a[20:23], v[28:31], v[68:71]\n"
    "s_waitcnt lgkmcnt(6)\n"
    "ds_read_b128 v[28:31], v9 offset:6144\n"
    "v_mfma_f32_16x16x32_bf16 v[64:67], a[32:35], v[32:35], v[64:67]\n"
    "s_waitcnt lgkmcnt(6)\n"
    "ds_read_b128 v[32:35], v9 offset:1024\n"
    "v_mfma_f32_16x16x32_bf16 v[68:71], a[32:35], v[36:39], v[68:71]\n"
    "s_waitcnt lgkmcnt(6)\n"
    "ds_read_b128 v[36:39], v9 offset:3072\n"
    "v_mfma_f32_16x16x32_bf16 v[64:67], a[44:47], v[40:43], v[64:67]\n"
    "s_waitcnt lgkmcnt(6)\n"
    "ds_read_b128 v[40:43], v9 offset:5120\n"
    "v_mfma_f32_16x16x32_bf16 v[68:71], a[44:47], v[44:47], v[68:71]\n"
    "s_waitcnt lgkmcnt(6)\n"
    "ds_read_b128 v[44:47], v9 offset:7168\n"
    "v_mfma_f32_16x16x32_bf16 v[48:51], a[0:3], v[16:19], 0\n"
    "s_waitcnt lgkmcnt(6)\n"
    "s_sub_u32 s74, s68, 1\n"
    "v_mfma_f32_16x16x32_bf16 v[52:55], a[4:7], v[16:19], 0\n"
    "ds_read_b128 v[16:19], v9 offset:8192\n"
    "v_mfma_f32_16x16x32_bf16 v[48:51], a[12:15], v[20:23], v[48:51]\n"
    "s_waitcnt lgkmcnt(6)\n"
    "s_and_b32 s76, s71, s54\n"
    "v_mfma_f32_16x16x32_bf16 v[52:55], a[16:19], v[20:23], v[52:55]\n"
    "ds_read_b128 v[20:23], v9 offset:9216\n"
    "v_mfma_f32_16x16x32_bf16 v[48:51], a[24:27], v[24:27], v[48:51]\n"
    "s_waitcnt lgkmcnt(6)\n"
    "s_mul_i32 s76, s76, s53\n"
    "v_mfma_f32_16x16x32_bf16 v[52:55], a[28:31], v[24:27], v[52:55]\n"
    "ds_read_b128 v[24:27], v9 offset:10240\n"
    "v_mfma_f32_16x16x32_bf16 v[48:51], a[36:39], v[28:31], v[48:51]\n"
    "s_waitcnt lgkmcnt(6)\n"
    "s_add_u32 s76, s76, s52\n"
    "v_mfma_f32_16x16x32_bf16 v[52:55], a[40:43], v[28:31], v[52:55]\n"
    "ds_read_b128 v[28:31], v9 offset:11264\n"
    "v_mfma_f32_16x16x32_bf16 v[56:59], a[0:3], v[32:35], 0\n"
    "s_waitcnt lgkmcnt(6)\n"
    "s_and_b32 s77, s71, 1\n"
    "v_mfma_f32_16x16x32_bf16 v[60:63], a[4:7], v[32:35], 0\n"
    "ds_read_b128 v[32:35], v9 offset:12288\n"
    "v_mfma_f32_16x16x32_bf16 v[56:59], a[12:15], v[36:39], v[56:59]\n"
    "s_waitcnt lgkmcnt(6)\n"
    "s_mul_i32 s77, s77, 65536\n"
    "v_mfma_f32_16x16x32_bf16 v[60:63], a[16:19], v[36:39], v[60:63]\n"
    "ds_read_b128 v[36:39], v9 offset:13312\n"
    "v_mfma_f32_16x16x32_bf16 v[56:59], a[24:27], v[40:43], v[56:59]\n"
    "s_waitcnt lgkmcnt(6)\n"
    "s_add_u32 s77, s77, s59\n"
    "v_mfma_f32_16x16x32_bf16 v[60:63], a[28:31], v[40:43], v[60:63]\n"
    "ds_read_b128 v[40:43], v9 offset:14336\n"
    "v_mfma_f32_16x16x32_bf16 v[56:59], a[36:39], v[44:47], v[56:59]\n"
    "s_waitcnt lgkmcnt(6)\n"
    "v_mfma_f32_16x16x32_bf16 v[60:63], a[40:43], v[44:47], v[60:63]\n"
    "ds_read_b128 v[44:47], v9 offset:15360\n"
    "v_mfma_f32_16x16x32_bf16 v[48:51], a[48:51], v[16:19], v[48:51]\n"
    "v_mfma_f32_16x16x32_bf16 v[52:55], a[52:55], v[16:19], v[52:55]\n"
    "s_waitcnt lgkmcnt(6)\n"
    "v_mfma_f32_16x16x32_bf16 v[64:67], a[56:59], v[16:19], v[64:67]\n"
    "ds_read_b128 v[16:19], v9 offset:16384\n"
    "v_mfma_f32_16x16x32_bf16 v[56:59], a[48:51], v[20:23], v[56:59]\n"
    "v_mfma_f32_16x16x32_bf16 v[60:63], a[52:55], v[20:23], v[60:63]\n"
    "s_waitcnt lgkmcnt(6)\n"
    "v_mfma_f32_16x16x32_bf16 v[68:71], a[56:59], v[20:23], v[68:71]\n"
    "ds_read_b128 v[20:23], v9 offset:17408\n"
    "v_mfma_f32_16x16x32_bf16 v[48:51], a[60:63], v[24:27], v[48:51]\n"
    "v_mfma_f32_16x16x32_bf16 v[52:55], a[64:67], v[24:27], v[52:55]\n"
    "s_waitcnt lgkmcnt(6)\n"
    "v_mfma_f32_16x16x32_bf16 v[64:67], a[68:71], v[24:27], v[64:67]\n"
    "ds_read_b128 v[24:27], v9 offset:18432\n"
    "v_mfma_f32_16x16x32_bf16 v[56:59], a[60:63], v[28:31], v[56:59]\n"
    "v_mfma_f32_16x16x32_bf16 v[60:63], a[64:67], v[28:31], v[60:63]\n"
    "s_waitcnt lgkmcnt(6)\n"
    "v_mfma_f32_16x16x32_bf16 v[68:71], a[68:71], v[28:31], v[68:71]\n"
    "ds_read_b128 v[28:31], v9 offset:19456\n"
    "v_mfma_f32_16x16x32_bf16 v[48:51], a[72:75], v[32:35], v[48:51]\n"
    "v_mfma_f32_16x16x32_bf16 v[52:55], a[76:79], v[32:35], v[52:55]\n"
    "s_waitcnt lgkmcnt(6)\n"
    "v_mfma_f32_16x16x32_bf16 v[64:67], a[80:83], v[32:35], v[64:67]\n"
    "ds_read_b128 v[32:35], v9 offset:20480\n"
    "v_mfma_f32_16x16x32_bf16 v[56:59], a[72:75], v[36:39], v[56:59]\n"
    "v_mfma_f32_16x16x32_bf16 v[60:63], a[76:79], v[36:39], v[60:63]\n"
    "s_waitcnt lgkmcnt(6)\n"
    "v_mfma_f32_16x16x32_bf16 v[68:71], a[80:83], v[36:39], v[68:71]\n"
    "ds_read_b128 v[36:39], v9 offset:21504\n"
    "v_mfma_f32_16x16x32_bf16 v[48:51], a[84:87], v[40:43], v[48:51]\n"
    "v_mov_b32 v12, s74\n"
    "v_mfma_f32_16x16x32_bf16 v[52:55], a[88:91], v[40:43], v[52:55]\n"
    "s_waitcnt lgkmcnt(6)\n"
    "s_waitcnt vmcnt(0)\n"
    "v_add_u32 v13, v11, v3\n"
    "v_mfma_f32_16x16x32_bf16 v[64:67], a[92:95], v[40:43], v[64:67]\n"
    "ds_read_b128 v[40:43], v9 offset:22528\n"
    "buffer_store_dword v12, v14, s[36:39], 0 offen sc1\n"
    "v_cmp_gt_i32 vcc, s71, v13\n"
    "v_mfma_f32_16x16x32_bf16 v[56:59], a[84:87], v[44:47], v[56:59]\n"
    "v_add_u32 v10, 49152, v9\n"
    "v_mfma_f32_16x16x32_bf16 v[60:63], a[88:91], v[44:47], v[60:63]\n"
    "s_waitcnt lgkmcnt(6)\n"
    "v_mfma_f32_16x16x32_bf16 v[68:71], a[92:95], v[44:47], v[68:71]\n"
    "ds_read_b128 v[44:47], v9 offset:23552\n"
    "s_branch .LgWO_mid_%=\n"
    ".LgWO_idle_%=:\n"
    "s_waitcnt vmcnt(3)\n"
    "v_add_u32 v13, v11, v3\n"
    "v_cmp_gt_i32 vcc, s71, v13\n"
    "s_cbranch_vccnz .LgWO_idle2_%=\n"
    "s_and_b32 s76, s71, s54\n"
    "s_mul_i32 s76, s76, s53\n"
    "s_add_u32 s76, s76, s52\n"
    "s_and_b32 s77, s71, 1\n"
    "s_mul_i32 s77, s77, 65536\n"
    "s_add_u32 s77, s77, s59\n"
    "s_add_u32 m0, s77, s59\n"
    "s_add_u32 s77, s77, 8192\n"
    "buffer_load_dwordx4 v1, s[36:39], s76 offen sc1 lds\n"
    "buffer_load_dwordx4 v1, s[36:39], s76 offen offset:1024 sc1 lds\n"
    "s_add_u32 s76, s76, 16384\n"
    "s_add_u32 m0, s77, s59\n"
    "s_add_u32 s77, s77, 8192\n"
    "buffer_load_dwordx4 v1, s[36:39], s76 offen sc1 lds\n"
    "buffer_load_dwordx4 v1, s[36:39], s76 offen offset:1024 sc1 lds\n"
    "s_add_u32 s76, s76, 16384\n"
    "s_add_u32 m0, s77, s59\n"
    "s_add_u32 s77, s77, 8192\n"
    "buffer_load_dwordx4 v1, s[36:39], s76 offen sc1 lds\n"
    "buffer_load_dwordx4 v1, s[36:39], s76 offen offset:1024 sc1 lds\n"
    "s_add_u32 s76, s76, 16384\n"
    "s_add_u32 m0, s77, s59\n"
    "s_add_u32 s77, s77, 8192\n"
    "buffer_load_dwordx4 v1, s[36:39], s76 offen sc1 lds\n"
    "buffer_load_dwordx4 v1, s[36:39], s76 offen offset:1024 sc1 lds\n"
    "s_add_u32 s76, s76, 16384\n"
    "s_add_u32 m0, s77, s59\n"
    "s_add_u32 s77, s77, 8192\n"
    "buffer_load_dwordx4 v1, s[36:39], s76 offen sc1 lds\n"
    "buffer_load_dwordx4 v1, s[36:39], s76 offen offset:1024 sc1 lds\n"
    "s_add_u32 s76, s76, 16384\n"
    "s_add_u32 m0, s77, s59\n"
    "s_add_u32 s77, s77, 8192\n"
    "buffer_load_dwordx4 v1, s[36:39], s76 offen sc1 lds\n"
    "buffer_load_dwordx4 v1, s[36:39], s76 offen offset:1024 sc1 lds\n"
    "s_add_u32 s76, s76, 16384\n"
    "s_add_u32 m0, s77, s59\n"
    "s_add_u32 s77, s77, 8192\n"
    "buffer_load_dwordx4 v1, s[36:39], s76 offen sc1 lds\n"
    "buffer_load_dwordx4 v1, s[36:39], s76 offen offset:1024 sc1 lds\n"
    "s_add_u32 s76, s76, 16384\n"
    "s_add_u32 m0, s77, s59\n"
    "s_add_u32 s77, s77, 8192\n"
    "buffer_load_dwordx4 v1, s[36:39], s76 offen sc1 lds\n"
    "buffer_load_dwordx4 v1, s[36:39], s76 offen offset:1024 sc1 lds\n"
    "s_add_u32 s76, s76, 16384\n"
    "s_waitcnt vmcnt(16)\n"
    "s_sub_u32 s74, s68, 1\n"
    "v_mov_b32 v12, s74\n"
    "s_mov_b64 exec, s[62:63]\n"
    "buffer_store_dword v12, v14, s[36:39], 0 offen sc1\n"
    "s_mov_b64 exec, -1\n"
    "s_waitcnt vmcnt(1)\n"
    "s_branch .LgWO_bar_%=\n"
    ".LgWO_idle2_%=:\n"
    "s_or_b64 exec, vcc, 1\n"
    "buffer_atomic_smax v11, v2, s[36:39], 0 offen sc0 sc1\n"
    "s_mov_b64 exec, -1\n"
    "s_waitcnt vmcnt(1)\n"
    "s_sub_u32 s74, s68, 1\n"
    "v_mov_b32 v12, s74\n"
    "s_mov_b64 exec, s[62:63]\n"
    "buffer_store_dword v12, v14, s[36:39], 0 offen sc1\n"
    "s_mov_b64 exec, -1\n"
    "s_waitcnt vmcnt(1)\n"
    "s_branch .LgWO_block1_%=\n"
    ".LgWO_block_%=:\n"
    "v_add_u32 v13, v11, v3\n"
    "v_cmp_gt_i32 vcc, s71, v13\n"
    "s_nop 0\n"
    "s_or_b64 exec, vcc, 1\n"
    "buffer_atomic_smax v11, v2, s[36:39], 0 offen sc0 sc1\n"
    "s_mov_b64 exec, -1\n"
    "s_waitcnt vmcnt(0)\n"
    ".LgWO_block1_%=:\n"
    "v_add_u32 v13, v11, v3\n"
    "v_cmp_gt_i32 vcc, s71, v13\n"
    "s_cbranch_vccz .LgWO_go_%=\n"
    "s_sleep 4\n"
    "s_sub_u32 s72, s72, 1\n"
    "s_cbranch_scc0 .LgWO_block_%=\n"
    "s_mov_b32 s72, 0\n"
    ".LgWO_go_%=:\n"
    "s_and_b32 s76, s71, s54\n"
    "s_mul_i32 s76, s76, s53\n"
    "s_add_u32 s76, s76, s52\n"
    "s_and_b32 s77, s71, 1\n"
    "s_mul_i32 s77, s77, 65536\n"
    "s_add_u32 s77, s77, s59\n"
    "s_add_u32 m0, s77, s59\n"
    "s_add_u32 s77, s77, 8192\n"
    "buffer_load_dwordx4 v1, s[36:39], s76 offen sc1 lds\n"
    "buffer_load_dwordx4 v1, s[36:39], s76 offen offset:1024 sc1 lds\n"
    "s_add_u32 s76, s76, 16384\n"
    "s_add_u32 m0, s77, s59\n"
    "s_add_u32 s77, s77, 8192\n"
    "buffer_load_dwordx4 v1, s[36:39], s76 offen sc1 lds\n"
    "buffer_load_dwordx4 v1, s[36:39], s76 offen offset:1024 sc1 lds\n"
    "s_add_u32 s76, s76, 16384\n"
    "s_add_u32 m0, s77, s59\n"
    "s_add_u32 s77, s77, 8192\n"
    "buffer_load_dwordx4 v1, s[36:39], s76 offen sc1 lds\n"
    "buffer_load_dwordx4 v1, s[36:39], s76 offen offset:1024 sc1 lds\n"
    "s_add_u32 s76, s76, 16384\n"
    "s_add_u32 m0, s77, s59\n"
    "s_add_u32 s77, s77, 8192\n"
    "buffer_load_dwordx4 v1, s[36:39], s76 offen sc1 lds\n"
    "buffer_load_dwordx4 v1, s[36:39], s76 offen offset:1024 sc1 lds\n"
    "s_add_u32 s76, s76, 16384\n"
    "s_add_u32 m0, s77, s59\n"
    "s_add_u32 s77, s77, 8192\n"
    "buffer_load_dwordx4 v1, s[36:39], s76 offen sc1 lds\n"
    "buffer_load_dwordx4 v1, s[36:39], s76 offen offset:1024 sc1 lds\n"
    "s_add_u32 s76, s76, 16384\n"
    "s_add_u32 m0, s77, s59\n"
    "s_add_u32 s77, s77, 8192\n"
    "buffer_load_dwordx4 v1, s[36:39], s76 offen sc1 lds\n"
    "buffer_load_dwordx4 v1, s[36:39], s76 offen offset:1024 sc1 lds\n"
    "s_add_u32 s76, s76, 16384\n"
    "s_add_u32 m0, s77, s59\n"
    "s_add_u32 s77, s77, 8192\n"
    "buffer_load_dwordx4 v1, s[36:39], s76 offen sc1 lds\n"
    "buffer_load_dwordx4 v1, s[36:39], s76 offen offset:1024 sc1 lds\n"
    "s_add_u32 s76, s76, 16384\n"
    "s_add_u32 m0, s77, s59\n"
    "s_add_u32 s77, s77, 8192\n"
    "buffer_load_dwordx4 v1, s[36:39], s76 offen sc1 lds\n"
    "buffer_load_dwordx4 v1, s[36:39], s76 offen offset:1024 sc1 lds\n"
    "s_add_u32 s76, s76, 16384\n"
    "s_waitcnt vmcnt(0)\n"
    "s_branch .LgWO_bar_%=\n"
    ".LgWO_FIN_%=:\n"
  );
}
DEVI void role_down(const Params& p, int L, int j, int i, u32 dbg) {
  const int lane = threadIdx.x & 63, w = threadIdx.x >> 6, e0 = j * 1152 * 576 + i * 192;
  GEMM_DECL;
  g_w0 = make_srd(p.w2 + e0, 4608u * 576 * 2 - e0 * 2); g_w1 = g_w0; g_w2 = g_w0;
  g_ldw = 1152;
  g_in_base = pgb(L, B_H + 9 * j); g_in_stride = SLOT; g_in_mask = RH - 1;
  g_out_base = pgb(L, B_PF + 5 * j); g_out_kb = 12 * i; g_out_mask = RP - 1;
  g_flag = lane < 4 ? mbox(L, lane, SL_REDF, 12 * j + 4 * i + w) : ((lane < 10 && w == 0) ? mbox(L, j, SL_UP + lane - 4, 16 + i) : OOB);
  g_paddr = mbox(L, j, SL_DOWN + i, lane);
  g_pbias = lane < 24 ? 0 : (lane < 40 ? RP : 1000);
  v16u sa, sb, va;
  gemm_args(GEMM_ARGS, p, dbg, sa, sb, va);
  RUN_ASM(
    "s_nop 4\n"
    "v_add_u32 v8, s59, v0\n"
    "s_add_u32 m0, s59, 0\n"
    "s_mov_b32 s74, 0\n"
    "buffer_load_dwordx4 v5, s[40:43], s74 offen lds\n"
    "s_add_u32 m0, s59, 4096\n"
    "s_mov_b32 s74, 128\n"
    "buffer_load_dwordx4 v5, s[40:43], s74 offen lds\n"
    "s_add_u32 m0, s59, 8192\n"
    "s_mov_b32 s74, 256\n"
    "buffer_load_dwordx4 v5, s[40:43], s74 offen lds\n"
    "s_add_u32 m0, s59, 12288\n"
    "s_mov_b32 s74, 36864\n"
    "buffer_load_dwordx4 v5, s[40:43], s74 offen lds\n"
    "s_add_u32 m0, s59, 16384\n"
    "s_mov_b32 s74, 36992\n"
    "buffer_load_dwordx4 v5, s[40:43], s74 offen lds\n"
    "s_add_u32 m0, s59, 20480\n"
    "s_mov_b32 s74, 37120\n"
    "buffer_load_dwordx4 v5, s[40:43], s74 offen lds\n"
    "s_add_u32 m0, s59, 24576\n"
    "s_mov_b32 s74, 73728\n"
    "buffer_load_dwordx4 v5, s[40:43], s74 offen lds\n"
    "s_add_u32 m0, s59, 28672\n"
    "s_mov_b32 s74, 73856\n"
    "buffer_load_dwordx4 v5, s[40:43], s74 offen lds\n"
    "s_add_u32 m0, s59, 32768\n"
    "s_mov_b32 s74, 73984\n"
    "buffer_load_dwordx4 v5, s[40:43], s74 offen lds\n"
    "s_add_u32 m0, s59, 36864\n"
    "s_mov_b32 s74, 110592\n"
    "buffer_load_dwordx4 v5, s[40:43], s74 offen lds\n"
    "s_add_u32 m0, s59, 40960\n"
    "s_mov_b32 s74, 110720\n"
    "buffer_load_dwordx4 v5, s[40:43], s74 offen lds\n"
    "s_add_u32 m0, s59, 45056\n"
    "s_mov_b32 s74, 110848\n"
    "buffer_load_dwordx4 v5, s[40:43], s74 offen lds\n"
    "s_add_u32 m0, s59, 49152\n"
    "s_mov_b32 s74, 147456\n"
    "buffer_load_dwordx4 v5, s[40:43], s74 offen lds\n"
    "s_add_u32 m0, s59, 53248\n"
    "s_mov_b32 s74, 147584\n"
    "buffer_load_dwordx4 v5, s[40:43], s74 offen lds\n"
    "s_add_u32 m0, s59, 57344\n"
    "s_mov_b32 s74, 147712\n"
    "buffer_load_dwordx4 v5, s[40:43], s74 offen lds\n"
    "s_add_u32 m0, s59, 61440\n"
    "s_mov_b32 s74, 184320\n"
    "buffer_load_dwordx4 v5, s[40:43], s74 offen lds\n"
    "s_add_u32 m0, s59, 65536\n"
    "s_mov_b32 s74, 184448\n"
    "buffer_load_dwordx4 v5, s[40:43], s74 offen lds\n"
    "s_add_u32 m0, s59, 69632\n"
    "s_mov_b32 s74, 184576\n"
    "buffer_load_dwordx4 v5, s[40:43], s74 offen lds\n"
    "s_add_u32 m0, s59, 73728\n"
    "s_mov_b32 s74, 221184\n"
    "buffer_load_dwordx4 v5, s[40:43], s74 offen lds\n"
    "s_add_u32 m0, s59, 77824\n"
    "s_mov_b32 s74, 221312\n"
    "buffer_load_dwordx4 v5, s[40:43], s74 offen lds\n"
    "s_add_u32 m0, s59, 81920\n"
    "s_mov_b32 s74, 221440\n"
    "buffer_load_dwordx4 v5, s[40:43], s74 offen lds\n"
    "s_add_u32 m0, s59, 86016\n"
    "s_mov_b32 s74, 258048\n"
    "buffer_load_dwordx4 v5, s[40:43], s74 offen lds\n"
    "s_add_u32 m0, s59, 90112\n"
    "s_mov_b32 s74, 258176\n"
    "buffer_load_dwordx4 v5, s[40:43], s74 offen lds\n"
    "s_add_u32 m0, s59, 94208\n"
    "s_mov_b32 s74, 258304\n"
    "buffer_load_dwordx4 v5, s[40:43], s74 offen lds\n"
    "s_add_u32 m0, s59, 98304\n"
    "s_mov_b32 s74, 294912\n"
    "buffer_load_dwordx4 v5, s[40:43], s74 offen lds\n"
    "s_add_u32 m0, s59, 102400\n"
    "s_mov_b32 s74, 295040\n"
    "buffer_load_dwordx4 v5, s[40:43], s74 offen lds\n"
    "s_add_u32 m0, s59, 106496\n"
    "s_mov_b32 s74, 295168\n"
    "buffer_load_dwordx4 v5, s[40:43], s74 offen lds\n"
    "s_add_u32 m0, s59, 110592\n"
    "s_mov_b32 s74, 331776\n"
    "buffer_load_dwordx4 v5, s[40:43], s74 offen lds\n"
    "s_add_u32 m0, s59, 114688\n"
    "s_mov_b32 s74, 331904\n"
    "buffer_load_dwordx4 v5, s[40:43], s74 offen lds\n"
    "s_add_u32 m0, s59, 118784\n"
    "s_mov_b32 s74, 332032\n"
    "buffer_load_dwordx4 v5, s[40:43], s74 offen lds\n"
    "v_add_u32 v16, 49152, v6\n"
    "v_add_u32 v17, 98304, v6\n"
    "v_add_u32 v18, 49152, v7\n"
    "v_add_u32 v19, 98304, v7\n"
    "s_waitcnt vmcnt(24) lgkmcnt(0)\n"
    "s_barrier\n"
    "s_add_u32 m0, s59, 122880\n"
    "s_mov_b32 s74, 368640\n"
    "buffer_load_dwordx4 v5, s[40:43], s74 offen lds\n"
    "s_add_u32 m0, s59, 126976\n"
    "s_mov_b32 s74, 368768\n"
    "buffer_load_dwordx4 v5, s[40:43], s74 offen lds\n"
    "s_add_u32 m0, s59, 131072\n"
    "s_mov_b32 s74, 368896\n"
    "buffer_load_dwordx4 v5, s[40:43], s74 offen lds\n"
    "s_add_u32 m0, s59, 135168\n"
    "s_mov_b32 s74, 405504\n"
    "buffer_load_dwordx4 v5, s[40:43], s74 offen lds\n"
    "s_add_u32 m0, s59, 139264\n"
    "s_mov_b32 s74, 405632\n"
    "buffer_load_dwordx4 v5, s[40:43], s74 offen lds\n"
    "s_add_u32 m0, s59, 143360\n"
    "s_mov_b32 s74, 405760\n"
    "buffer_load_dwordx4 v5, s[40:43], s74 offen lds\n"
    "ds_read_b64_tr_b16 a[0:1], v6 offset:0\n"
    "ds_read_b64_tr_b16 a[2:3], v6 offset:512\n"
    "ds_read_b64_tr_b16 a[4:5], v6 offset:8\n"
    "ds_read_b64_tr_b16 a[6:7], v6 offset:520\n"
    "ds_read_b64_tr_b16 a[8:9], v7 offset:0\n"
    "ds_read_b64_tr_b16 a[10:11], v7 offset:512\n"
    "ds_read_b64_tr_b16 a[12:13], v6 offset:12288\n"
    "ds_read_b64_tr_b16 a[14:15], v6 offset:12800\n"
    "ds_read_b64_tr_b16 a[16:17], v6 offset:12296\n"
    "ds_read_b64_tr_b16 a[18:19], v6 offset:12808\n"
    "ds_read_b64_tr_b16 a[20:21], v7 offset:12288\n"
    "ds_read_b64_tr_b16 a[22:23], v7 offset:12800\n"
    "s_waitcnt vmcnt(24) lgkmcnt(0)\n"
    "s_barrier\n"
    "s_add_u32 m0, s59, 0\n"
    "s_mov_b32 s74, 442368\n"
    "buffer_load_dwordx4 v5, s[40:43], s74 offen lds\n"
    "s_add_u32 m0, s59, 4096\n"
    "s_mov_b32 s74, 442496\n"
    "buffer_load_dwordx4 v5, s[40:43], s74 offen lds\n"
    "s_add_u32 m0, s59, 8192\n"
    "s_mov_b32 s74, 442624\n"
    "buffer_load_dwordx4 v5, s[40:43], s74 offen lds\n"
    "s_add_u32 m0, s59, 12288\n"
    "s_mov_b32 s74, 479232\n"
    "buffer_load_dwordx4 v5, s[40:43], s74 offen lds\n"
    "s_add_u32 m0, s59, 16384\n"
    "s_mov_b32 s74, 479360\n"
    "buffer_load_dwordx4 v5, s[40:43], s74 offen lds\n"
    "s_add_u32 m0, s59, 20480\n"
    "s_mov_b32 s74, 479488\n"
    "buffer_load_dwordx4 v5, s[40:43], s74 offen lds\n"
    "ds_read_b64_tr_b16 a[24:25], v6 offset:24576\n"
    "ds_read_b64_tr_b16 a[26:27], v6 offset:25088\n"
    "ds_read_b64_tr_b16 a[28:29], v6 offset:24584\n"
    "ds_read_b64_tr_b16 a[30:31], v6 offset:25096\n"
    "ds_read_b64_tr_b16 a[32:33], v7 offset:24576\n"
    "ds_read_b64_tr_b16 a[34:35], v7 offset:25088\n"
    "ds_read_b64_tr_b16 a[36:37], v6 offset:36864\n"
    "ds_read_b64_tr_b16 a[38:39], v6 offset:37376\n"
    "ds_read_b64_tr_b16 a[40:41], v6 offset:36872\n"
    "ds_read_b64_tr_b16 a[42:43], v6 offset:37384\n"
    "ds_read_b64_tr_b16 a[44:45], v7 offset:36864\n"
    "ds_read_b64_tr_b16 a[46:47], v7 offset:37376\n"
    "s_waitcnt vmcnt(24) lgkmcnt(0)\n"
    "s_barrier\n"
    "s_add_u32 m0, s59, 24576\n"
    "s_mov_b32 s74, 516096\n"
    "buffer_load_dwordx4 v5, s[40:43], s74 offen lds\n"
    "s_add_u32 m0, s59, 28672\n"
    "s_mov_b32 s74, 516224\n"
    "buffer_load_dwordx4 v5, s[40:43], s74 offen lds\n"
    "s_add_u32 m0, s59, 32768\n"
    "s_mov_b32 s74, 516352\n"
    "buffer_load_dwordx4 v5, s[40:43], s74 offen lds\n"
    "s_add_u32 m0, s59, 36864\n"
    "s_mov_b32 s74, 552960\n"
    "buffer_load_dwordx4 v5, s[40:43], s74 offen lds\n"
    "s_add_u32 m0, s59, 40960\n"
    "s_mov_b32 s74, 553088\n"
    "buffer_load_dwordx4 v5, s[40:43], s74 offen lds\n"
    "s_add_u32 m0, s59, 45056\n"
    "s_mov_b32 s74, 553216\n"
    "buffer_load_dwordx4 v5, s[40:43], s74 offen lds\n"
    "ds_read_b64_tr_b16 a[48:49], v16 offset:0\n"
    "ds_read_b64_tr_b16 a[50:51], v16 offset:512\n"
    "ds_read_b64_tr_b16 a[52:53], v16 offset:8\n"
    "ds_read_b64_tr_b16 a[54:55], v16 offset:520\n"
    "ds_read_b64_tr_b16 a[56:57], v18 offset:0\n"
    "ds_read_b64_tr_b16 a[58:59], v18 offset:512\n"
    "ds_read_b64_tr_b16 a[60:61], v16 offset:12288\n"
    "ds_read_b64_tr_b16 a[62:63], v16 offset:12800\n"
    "ds_read_b64_tr_b16 a[64:65], v16 offset:12296\n"
    "ds_read_b64_tr_b16 a[66:67], v16 offset:12808\n"
    "ds_read_b64_tr_b16 a[68:69], v18 offset:12288\n"
    "ds_read_b64_tr_b16 a[70:71], v18 offset:12800\n"
    "s_waitcnt vmcnt(24) lgkmcnt(0)\n"
    "s_barrier\n"
    "s_add_u32 m0, s59, 49152\n"
    "s_mov_b32 s74, 589824\n"
    "buffer_load_dwordx4 v5, s[40:43], s74 offen lds\n"
    "s_add_u32 m0, s59, 53248\n"
    "s_mov_b32 s74, 589952\n"
    "buffer_load_dwordx4 v5, s[40:43], s74 offen lds\n"
    "s_add_u32 m0, s59, 57344\n"
    "s_mov_b32 s74, 590080\n"
    "buffer_load_dwordx4 v5, s[40:43], s74 offen lds\n"
    "s_add_u32 m0, s59, 61440\n"
    "s_mov_b32 s74, 626688\n"
    "buffer_load_dwordx4 v5, s[40:43], s74 offen lds\n"
    "s_add_u32 m0, s59, 65536\n"
    "s_mov_b32 s74, 626816\n"
    "buffer_load_dwordx4 v5, s[40:43], s74 offen lds\n"
    "s_add_u32 m0, s59, 69632\n"
    "s_mov_b32 s74, 626944\n"
    "buffer_load_dwordx4 v5, s[40:43], s74 offen lds\n"
    "ds_read_b64_tr_b16 a[72:73], v16 offset:24576\n"
    "ds_read_b64_tr_b16 a[74:75], v16 offset:25088\n"
    "ds_read_b64_tr_b16 a[76:77], v16 offset:24584\n"
    "ds_read_b64_tr_b16 a[78:79], v16 offset:25096\n"
    "ds_read_b64_tr_b16 a[80:81], v18 offset:24576\n"
    "ds_read_b64_tr_b16 a[82:83], v18 offset:25088\n"
    "ds_read_b64_tr_b16 a[84:85], v16 offset:36864\n"
    "ds_read_b64_tr_b16 a[86:87], v16 offset:37376\n"
    "ds_read_b64_tr_b16 a[88:89], v16 offset:36872\n"
    "ds_read_b64_tr_b16 a[90:91], v16 offset:37384\n"
    "ds_read_b64_tr_b16 a[92:93], v18 offset:36864\n"
    "ds_read_b64_tr_b16 a[94:95], v18 offset:37376\n"
    "s_waitcnt vmcnt(24) lgkmcnt(0)\n"
    "s_barrier\n"
    "s_add_u32 m0, s59, 73728\n"
    "s_mov_b32 s74, 663552\n"
    "buffer_load_dwordx4 v5, s[40:43], s74 offen lds\n"
    "s_add_u32 m0, s59, 77824\n"
    "s_mov_b32 s74, 663680\n"
    "buffer_load_dwordx4 v5, s[40:43], s74 offen lds\n"
    "s_add_u32 m0, s59, 81920\n"
    "s_mov_b32 s74, 663808\n"
    "buffer_load_dwordx4 v5, s[40:43], s74 offen lds\n"
    "s_add_u32 m0, s59, 86016\n"
    "s_mov_b32 s74, 700416\n"
    "buffer_load_dwordx4 v5, s[40:43], s74 offen lds\n"
    "s_add_u32 m0, s59, 90112\n"
    "s_mov_b32 s74, 700544\n"
    "buffer_load_dwordx4 v5, s[40:43], s74 offen lds\n"
    "s_add_u32 m0, s59, 94208\n"
    "s_mov_b32 s74, 700672\n"
    "buffer_load_dwordx4 v5, s[40:43], s74 offen lds\n"
    "ds_read_b64_tr_b16 a[96:97], v17 offset:0\n"
    "ds_read_b64_tr_b16 a[98:99], v17 offset:512\n"
    "ds_read_b64_tr_b16 a[100:101], v17 offset:8\n"
    "ds_read_b64_tr_b16 a[102:103], v17 offset:520\n"
    "ds_read_b64_tr_b16 a[104:105], v19 offset:0\n"
    "ds_read_b64_tr_b16 a[106:107], v19 offset:512\n"
    "ds_read_b64_tr_b16 a[108:109], v17 offset:12288\n"
    "ds_read_b64_tr_b16 a[110:111], v17 offset:12800\n"
    "ds_read_b64_tr_b16 a[112:113], v17 offset:12296\n"
    "ds_read_b64_tr_b16 a[114:115], v17 offset:12808\n"
    "ds_read_b64_tr_b16 a[116:117], v19 offset:12288\n"
    "ds_read_b64_tr_b16 a[118:119], v19 offset:12800\n"
    "s_waitcnt vmcnt(24) lgkmcnt(0)\n"
    "s_barrier\n"
    "s_add_u32 m0, s59, 98304\n"
    "s_mov_b32 s74, 737280\n"
    "buffer_load_dwordx4 v5, s[40:43], s74 offen lds\n"
    "s_add_u32 m0, s59, 102400\n"
    "s_mov_b32 s74, 737408\n"
    "buffer_load_dwordx4 v5, s[40:43], s74 offen lds\n"
    "s_add_u32 m0, s59, 106496\n"
    "s_mov_b32 s74, 737536\n"
    "buffer_load_dwordx4 v5, s[40:43], s74 offen lds\n"
    "s_add_u32 m0, s59, 110592\n"
    "s_mov_b32 s74, 774144\n"
    "buffer_load_dwordx4 v5, s[40:43], s74 offen lds\n"
    "s_add_u32 m0, s59, 114688\n"
    "s_mov_b32 s74, 774272\n"
    "buffer_load_dwordx4 v5, s[40:43], s74 offen lds\n"
    "s_add_u32 m0, s59, 118784\n"
    "s_mov_b32 s74, 774400\n"
    "buffer_load_dwordx4 v5, s[40:43], s74 offen lds\n"
    "ds_read_b64_tr_b16 a[120:121], v17 offset:24576\n"
    "ds_read_b64_tr_b16 a[122:123], v17 offset:25088\n"
    "ds_read_b64_tr_b16 a[124:125], v17 offset:24584\n"
    "ds_read_b64_tr_b16 a[126:127], v17 offset:25096\n"
    "ds_read_b64_tr_b16 a[128:129], v19 offset:24576\n"
    "ds_read_b64_tr_b16 a[130:131], v19 offset:25088\n"
    "ds_read_b64_tr_b16 a[132:133], v17 offset:36864\n"
    "ds_read_b64_tr_b16 a[134:135], v17 offset:37376\n"
    "ds_read_b64_tr_b16 a[136:137], v17 offset:36872\n"
    "ds_read_b64_tr_b16 a[138:139], v17 offset:37384\n"
    "ds_read_b64_tr_b16 a[140:141], v19 offset:36864\n"
    "ds_read_b64_tr_b16 a[142:143], v19 offset:37376\n"
    "s_waitcnt vmcnt(24) lgkmcnt(0)\n"
    "s_barrier\n"
    "s_add_u32 m0, s59, 122880\n"
    "s_mov_b32 s74, 811008\n"
    "buffer_load_dwordx4 v5, s[40:43], s74 offen lds\n"
    "s_add_u32 m0, s59, 126976\n"
    "s_mov_b32 s74, 811136\n"
    "buffer_load_dwordx4 v5, s[40:43], s74 offen lds\n"
    "s_add_u32 m0, s59, 131072\n"
    "s_mov_b32 s74, 811264\n"
    "buffer_load_dwordx4 v5, s[40:43], s74 offen lds\n"
    "s_add_u32 m0, s59, 135168\n"
    "s_mov_b32 s74, 847872\n"
    "buffer_load_dwordx4 v5, s[40:43], s74 offen lds\n"
    "s_add_u32 m0, s59, 139264\n"
    "s_mov_b32 s74, 848000\n"
    "buffer_load_dwordx4 v5, s[40:43], s74 offen lds\n"
    "s_add_u32 m0, s59, 143360\n"
    "s_mov_b32 s74, 848128\n"
    "buffer_load_dwordx4 v5, s[40:43], s74 offen lds\n"
    "ds_read_b64_tr_b16 a[144:145], v6 offset:0\n"
    "ds_read_b64_tr_b16 a[146:147], v6 offset:512\n"
    "ds_read_b64_tr_b16 a[148:149], v6 offset:8\n"
    "ds_read_b64_tr_b16 a[150:151], v6 offset:520\n"
    "ds_read_b64_tr_b16 a[152:153], v7 offset:0\n"
    "ds_read_b64_tr_b16 a[154:155], v7 offset:512\n"
    "ds_read_b64_tr_b16 a[156:157], v6 offset:12288\n"
    "ds_read_b64_tr_b16 a[158:159], v6 offset:12800\n"
    "ds_read_b64_tr_b16 a[160:161], v6 offset:12296\n"
    "ds_read_b64_tr_b16 a[162:163], v6 offset:12808\n"
    "ds_read_b64_tr_b16 a[164:165], v7 offset:12288\n"
    "ds_read_b64_tr_b16 a[166:167], v7 offset:12800\n"
    "s_waitcnt vmcnt(24) lgkmcnt(0)\n"
    "s_barrier\n"
    "s_add_u32 m0, s59, 0\n"
    "s_mov_b32 s74, 884736\n"
    "buffer_load_dwordx4 v5, s[40:43], s74 offen lds\n"
    "s_add_u32 m0, s59, 4096\n"
    "s_mov_b32 s74, 884864\n"
    "buffer_load_dwordx4 v5, s[40:43], s74 offen lds\n"
    "s_add_u32 m0, s59, 8192\n"
    "s_mov_b32 s74, 884992\n"
    "buffer_load_dwordx4 v5, s[40:43], s74 offen lds\n"
    "s_add_u32 m0, s59, 12288\n"
    "s_mov_b32 s74, 921600\n"
    "buffer_load_dwordx4 v5, s[40:43], s74 offen lds\n"
    "s_add_u32 m0, s59, 16384\n"
    "s_mov_b32 s74, 921728\n"
    "buffer_load_dwordx4 v5, s[40:43], s74 offen lds\n"
    "s_add_u32 m0, s59, 20480\n"
    "s_mov_b32 s74, 921856\n"
    "buffer_load_dwordx4 v5, s[40:43], s74 offen lds\n"
    "ds_read_b64_tr_b16 a[168:169], v6 offset:24576\n"
    "ds_read_b64_tr_b16 a[170:171], v6 offset:25088\n"
    "ds_read_b64_tr_b16 a[172:173], v6 offset:24584\n"
    "ds_read_b64_tr_b16 a[174:175], v6 offset:25096\n"
    "ds_read_b64_tr_b16 a[176:177], v7 offset:24576\n"
    "ds_read_b64_tr_b16 a[178:179], v7 offset:25088\n"
    "ds_read_b64_tr_b16 a[180:181], v6 offset:36864\n"
    "ds_read_b64_tr_b16 a[182:183], v6 offset:37376\n"
    "ds_read_b64_tr_b16 a[184:185], v6 offset:36872\n"
    "ds_read_b64_tr_b16 a[186:187], v6 offset:37384\n"
    "ds_read_b64_tr_b16 a[188:189], v7 offset:36864\n"
    "ds_read_b64_tr_b16 a[190:191], v7 offset:37376\n"
    "s_waitcnt vmcnt(24) lgkmcnt(0)\n"
    "s_barrier\n"
    "s_add_u32 m0, s59, 24576\n"
    "s_mov_b32 s74, 958464\n"
    "buffer_load_dwordx4 v5, s[40:43], s74 offen lds\n"
    "s_add_u32 m0, s59, 28672\n"
    "s_mov_b32 s74, 958592\n"
    "buffer_load_dwordx4 v5, s[40:43], s74 offen lds\n"
    "s_add_u32 m0, s59, 32768\n"
    "s_mov_b32 s74, 958720\n"
    "buffer_load_dwordx4 v5, s[40:43], s74 offen lds\n"
    "s_add_u32 m0, s59, 36864\n"
    "s_mov_b32 s74, 995328\n"
    "buffer_load_dwordx4 v5, s[40:43], s74 offen lds\n"
    "s_add_u32 m0, s59, 40960\n"
    "s_mov_b32 s74, 995456\n"
    "buffer_load_dwordx4 v5, s[40:43], s74 offen lds\n"
    "s_add_u32 m0, s59, 45056\n"
    "s_mov_b32 s74, 995584\n"
    "buffer_load_dwordx4 v5, s[40:43], s74 offen lds\n"
    "ds_read_b64_tr_b16 a[192:193], v16 offset:0\n"
    "ds_read_b64_tr_b16 a[194:195], v16 offset:512\n"
    "ds_read_b64_tr_b16 a[196:197], v16 offset:8\n"
    "ds_read_b64_tr_b16 a[198:199], v16 offset:520\n"
    "ds_read_b64_tr_b16 a[200:201], v18 offset:0\n"
    "ds_read_b64_tr_b16 a[202:203], v18 offset:512\n"
    "ds_read_b64_tr_b16 a[204:205], v16 offset:12288\n"
    "ds_read_b64_tr_b16 a[206:207], v16 offset:12800\n"
    "ds_read_b64_tr_b16 a[208:209], v16 offset:12296\n"
    "ds_read_b64_tr_b16 a[210:211], v16 offset:12808\n"
    "ds_read_b64_tr_b16 a[212:213], v18 offset:12288\n"
    "ds_read_b64_tr_b16 a[214:215], v18 offset:12800\n"
    "s_waitcnt vmcnt(24) lgkmcnt(0)\n"
    "s_barrier\n"
    "s_add_u32 m0, s59, 49152\n"
    "s_mov_b32 s74, 1032192\n"
    "buffer_load_dwordx4 v5, s[40:43], s74 offen lds\n"
    "s_add_u32 m0, s59, 53248\n"
    "s_mov_b32 s74, 1032320\n"
    "buffer_load_dwordx4 v5, s[40:43], s74 offen lds\n"
    "s_add_u32 m0, s59, 57344\n"
    "s_mov_b32 s74, 1032448\n"
    "buffer_load_dwordx4 v5, s[40:43], s74 offen lds\n"
    "s_add_u32 m0, s59, 61440\n"
    "s_mov_b32 s74, 1069056\n"
    "buffer_load_dwordx4 v5, s[40:43], s74 offen lds\n"
    "s_add_u32 m0, s59, 65536\n"
    "s_mov_b32 s74, 1069184\n"
    "buffer_load_dwordx4 v5, s[40:43], s74 offen lds\n"
    "s_add_u32 m0, s59, 69632\n"
    "s_mov_b32 s74, 1069312\n"
    "buffer_load_dwordx4 v5, s[40:43], s74 offen lds\n"
    "ds_read_b64_tr_b16 a[216:217], v16 offset:24576\n"
    "ds_read_b64_tr_b16 a[218:219], v16 offset:25088\n"
    "ds_read_b64_tr_b16 a[220:221], v16 offset:24584\n"
    "ds_read_b64_tr_b16 a[222:223], v16 offset:25096\n"
    "ds_read_b64_tr_b16 a[224:225], v18 offset:24576\n"
    "ds_read_b64_tr_b16 a[226:227], v18 offset:25088\n"
    "ds_read_b64_tr_b16 a[228:229], v16 offset:36864\n"
    "ds_read_b64_tr_b16 a[230:231], v16 offset:37376\n"
    "ds_read_b64_tr_b16 a[232:233], v16 offset:36872\n"
    "ds_read_b64_tr_b16 a[234:235], v16 offset:37384\n"
    "ds_read_b64_tr_b16 a[236:237], v18 offset:36864\n"
    "ds_read_b64_tr_b16 a[238:239], v18 offset:37376\n"
    "s_waitcnt vmcnt(24) lgkmcnt(0)\n"
    "s_barrier\n"
    "s_add_u32 m0, s59, 73728\n"
    "s_mov_b32 s74, 1105920\n"
    "buffer_load_dwordx4 v5, s[40:43], s74 offen lds\n"
    "s_add_u32 m0, s59, 77824\n"
    "s_mov_b32 s74, 1106048\n"
    "buffer_load_dwordx4 v5, s[40:43], s74 offen lds\n"
    "s_add_u32 m0, s59, 81920\n"
    "s_mov_b32 s74, 1106176\n"
    "buffer_load_dwordx4 v5, s[40:43], s74 offen lds\n"
    "s_add_u32 m0, s59, 86016\n"
    "s_mov_b32 s74, 1142784\n"
    "buffer_load_dwordx4 v5, s[40:43], s74 offen lds\n"
    "s_add_u32 m0, s59, 90112\n"
    "s_mov_b32 s74, 1142912\n"
    "buffer_load_dwordx4 v5, s[40:43], s74 offen lds\n"
    "s_add_u32 m0, s59, 94208\n"
    "s_mov_b32 s74, 1143040\n"
    "buffer_load_dwordx4 v5, s[40:43], s74 offen lds\n"
    "ds_read_b64_tr_b16 a[240:241], v17 offset:0\n"
    "ds_read_b64_tr_b16 a[242:243], v17 offset:512\n"
    "ds_read_b64_tr_b16 a[244:245], v17 offset:8\n"
    "ds_read_b64_tr_b16 a[246:247], v17 offset:520\n"
    "ds_read_b64_tr_b16 a[248:249], v19 offset:0\n"
    "ds_read_b64_tr_b16 a[250:251], v19 offset:512\n"
    "ds_read_b64_tr_b16 a[252:253], v17 offset:12288\n"
    "ds_read_b64_tr_b16 a[254:255], v17 offset:12800\n"
    "ds_read_b64_tr_b16 v[80:81], v17 offset:12296\n"
    "ds_read_b64_tr_b16 v[82:83], v17 offset:12808\n"
    "ds_read_b64_tr_b16 v[84:85], v19 offset:12288\n"
    "ds_read_b64_tr_b16 v[86:87], v19 offset:12800\n"
    "s_waitcnt vmcnt(24) lgkmcnt(0)\n"
    "s_barrier\n"
    "s_add_u32 m0, s59, 98304\n"
    "s_mov_b32 s74, 1179648\n"
    "buffer_load_dwordx4 v5, s[40:43], s74 offen lds\n"
    "s_add_u32 m0, s59, 102400\n"
    "s_mov_b32 s74, 1179776\n"
    "buffer_load_dwordx4 v5, s[40:43], s74 offen lds\n"
    "s_add_u32 m0, s59, 106496\n"
    "s_mov_b32 s74, 1179904\n"
    "buffer_load_dwordx4 v5, s[40:43], s74 offen lds\n"
    "s_add_u32 m0, s59, 110592\n"
    "s_mov_b32 s74, 1216512\n"
    "buffer_load_dwordx4 v5, s[40:43], s74 offen lds\n"
    "s_add_u32 m0, s59, 114688\n"
    "s_mov_b32 s74, 1216640\n"
    "buffer_load_dwordx4 v5, s[40:43], s74 offen lds\n"
    "s_add_u32 m0, s59, 118784\n"
    "s_mov_b32 s74, 1216768\n"
    "buffer_load_dwordx4 v5, s[40:43], s74 offen lds\n"
    "ds_read_b64_tr_b16 v[88:89], v17 offset:24576\n"
    "ds_read_b64_tr_b16 v[90:91], v17 offset:25088\n"
    "ds_read_b64_tr_b16 v[92:93], v17 offset:24584\n"
    "ds_read_b64_tr_b16 v[94:95], v17 offset:25096\n"
    "ds_read_b64_tr_b16 v[96:97], v19 offset:24576\n"
    "ds_read_b64_tr_b16 v[98:99], v19 offset:25088\n"
    "ds_read_b64_tr_b16 v[100:101], v17 offset:36864\n"
    "ds_read_b64_tr_b16 v[102:103], v17 offset:37376\n"
    "ds_read_b64_tr_b16 v[104:105], v17 offset:36872\n"
    "ds_read_b64_tr_b16 v[106:107], v17 offset:37384\n"
    "ds_read_b64_tr_b16 v[108:109], v19 offset:36864\n"
    "ds_read_b64_tr_b16 v[110:111], v19 offset:37376\n"
    "s_waitcnt vmcnt(24) lgkmcnt(0)\n"
    "s_barrier\n"
    "s_add_u32 m0, s59, 122880\n"
    "s_mov_b32 s74, 1253376\n"
    "buffer_load_dwordx4 v5, s[40:43], s74 offen lds\n"
    "s_add_u32 m0, s59, 126976\n"
    "s_mov_b32 s74, 1253504\n"
    "buffer_load_dwordx4 v5, s[40:43], s74 offen lds\n"
    "s_add_u32 m0, s59, 131072\n"
    "s_mov_b32 s74, 1253632\n"
    "buffer_load_dwordx4 v5, s[40:43], s74 offen lds\n"
    "s_add_u32 m0, s59, 135168\n"
    "s_mov_b32 s74, 1290240\n"
    "buffer_load_dwordx4 v5, s[40:43], s74 offen lds\n"
    "s_add_u32 m0, s59, 139264\n"
    "s_mov_b32 s74, 1290368\n"
    "buffer_load_dwordx4 v5, s[40:43], s74 offen lds\n"
    "s_add_u32 m0, s59, 143360\n"
    "s_mov_b32 s74, 1290496\n"
    "buffer_load_dwordx4 v5, s[40:43], s74 offen lds\n"
    "ds_read_b64_tr_b16 v[112:113], v6 offset:0\n"
    "ds_read_b64_tr_b16 v[114:115], v6 offset:512\n"
    "ds_read_b64_tr_b16 v[116:117], v6 offset:8\n"
    "ds_read_b64_tr_b16 v[118:119], v6 offset:520\n"
    "ds_read_b64_tr_b16 v[120:121], v7 offset:0\n"
    "ds_read_b64_tr_b16 v[122:123], v7 offset:512\n"
    "ds_read_b64_tr_b16 v[124:125], v6 offset:12288\n"
    "ds_read_b64_tr_b16 v[126:127], v6 offset:12800\n"
    "ds_read_b64_tr_b16 v[128:129], v6 offset:12296\n"
    "ds_read_b64_tr_b16 v[130:131], v6 offset:12808\n"
    "ds_read_b64_tr_b16 v[132:133], v7 offset:12288\n"
    "ds_read_b64_tr_b16 v[134:135], v7 offset:12800\n"
    "s_waitcnt vmcnt(24) lgkmcnt(0)\n"
    "s_barrier\n"
    "ds_read_b64_tr_b16 v[136:137], v6 offset:24576\n"
    "ds_read_b64_tr_b16 v[138:139], v6 offset:25088\n"
    "ds_read_b64_tr_b16 v[140:141], v6 offset:24584\n"
    "ds_read_b64_tr_b16 v[142:143], v6 offset:25096\n"
    "ds_read_b64_tr_b16 v[144:145], v7 offset:24576\n"
    "ds_read_b64_tr_b16 v[146:147], v7 offset:25088\n"
    "ds_read_b64_tr_b16 v[148:149], v6 offset:36864\n"
    "ds_read_b64_tr_b16 v[150:151], v6 offset:37376\n"
    "ds_read_b64_tr_b16 v[152:153], v6 offset:36872\n"
    "ds_read_b64_tr_b16 v[154:155], v6 offset:37384\n"
    "ds_read_b64_tr_b16 v[156:157], v7 offset:36864\n"
    "ds_read_b64_tr_b16 v[158:159], v7 offset:37376\n"
    "s_waitcnt vmcnt(18) lgkmcnt(0)\n"
    "s_barrier\n"
    "ds_read_b64_tr_b16 v[160:161], v16 offset:0\n"
    "ds_read_b64_tr_b16 v[162:163], v16 offset:512\n"
    "ds_read_b64_tr_b16 v[164:165], v16 offset:8\n"
    "ds_read_b64_tr_b16 v[166:167], v16 offset:520\n"
    "ds_read_b64_tr_b16 v[168:169], v18 offset:0\n"
    "ds_read_b64_tr_b16 v[170:171], v18 offset:512\n"
    "ds_read_b64_tr_b16 v[172:173], v16 offset:12288\n"
    "ds_read_b64_tr_b16 v[174:175], v16 offset:12800\n"
    "ds_read_b64_tr_b16 v[176:177], v16 offset:12296\n"
    "ds_read_b64_tr_b16 v[178:179], v16 offset:12808\n"
    "ds_read_b64_tr_b16 v[180:181], v18 offset:12288\n"
    "ds_read_b64_tr_b16 v[182:183], v18 offset:12800\n"
    "s_waitcnt vmcnt(12) lgkmcnt(0)\n"
    "s_barrier\n"
    "ds_read_b64_tr_b16 v[184:185], v16 offset:24576\n"
    "ds_read_b64_tr_b16 v[186:187], v16 offset:25088\n"
    "ds_read_b64_tr_b16 v[188:189], v16 offset:24584\n"
    "ds_read_b64_tr_b16 v[190:191], v16 offset:25096\n"
    "ds_read_b64_tr_b16 v[192:193], v18 offset:24576\n"
    "ds_read_b64_tr_b16 v[194:195], v18 offset:25088\n"
    "ds_read_b64_tr_b16 v[196:197], v16 offset:36864\n"
    "ds_read_b64_tr_b16 v[198:199], v16 offset:37376\n"
    "ds_read_b64_tr_b16 v[200:201], v16 offset:36872\n"
    "ds_read_b64_tr_b16 v[202:203], v16 offset:37384\n"
    "ds_read_b64_tr_b16 v[204:205], v18 offset:36864\n"
    "ds_read_b64_tr_b16 v[206:207], v18 offset:37376\n"
    "s_waitcnt vmcnt(6) lgkmcnt(0)\n"
    "s_barrier\n"
    "ds_read_b64_tr_b16 v[208:209], v17 offset:0\n"
    "ds_read_b64_tr_b16 v[210:211], v17 offset:512\n"
    "ds_read_b64_tr_b16 v[212:213], v17 offset:8\n"
    "ds_read_b64_tr_b16 v[214:215], v17 offset:520\n"
    "ds_read_b64_tr_b16 v[216:217], v19 offset:0\n"
    "ds_read_b64_tr_b16 v[218:219], v19 offset:512\n"
    "ds_read_b64_tr_b16 v[220:221], v17 offset:12288\n"
    "ds_read_b64_tr_b16 v[222:223], v17 offset:12800\n"
    "ds_read_b64_tr_b16 v[224:225], v17 offset:12296\n"
    "ds_read_b64_tr_b16 v[226:227], v17 offset:12808\n"
    "ds_read_b64_tr_b16 v[228:229], v19 offset:12288\n"
    "ds_read_b64_tr_b16 v[230:231], v19 offset:12800\n"
    "s_waitcnt vmcnt(0) lgkmcnt(0)\n"
    "s_barrier\n"
    "ds_read_b64_tr_b16 v[232:233], v17 offset:24576\n"
    "ds_read_b64_tr_b16 v[234:235], v17 offset:25088\n"
    "ds_read_b64_tr_b16 v[236:237], v17 offset:24584\n"
    "ds_read_b64_tr_b16 v[238:239], v17 offset:25096\n"
    "ds_read_b64_tr_b16 v[240:241], v19 offset:24576\n"
    "ds_read_b64_tr_b16 v[242:243], v19 offset:25088\n"
    "ds_read_b64_tr_b16 v[244:245], v17 offset:36864\n"
    "ds_read_b64_tr_b16 v[246:247], v17 offset:37376\n"
    "ds_read_b64_tr_b16 v[248:249], v17 offset:36872\n"
    "ds_read_b64_tr_b16 v[250:251], v17 offset:37384\n"
    "ds_read_b64_tr_b16 v[252:253], v19 offset:36864\n"
    "ds_read_b64_tr_b16 v[254:255], v19 offset:37376\n"
    "s_waitcnt lgkmcnt(0)\n"
    "s_barrier\n"
    "s_mov_b32 s68, 0\n"
    "s_mov_b32 s71, 0\n"
    "s_mov_b32 s78, 81920\n"
    "v_add_u32 v9, s78, v0\n"
    "s_mov_b32 s64, 0xbfb8aa3b\n"
    "s_mov_b32 s72, 262144\n"
    ".LgDOWN_par_%=:\n"
    "buffer_load_dword v12, off, s[36:39], 0 sc1\n"
    "s_waitcnt vmcnt(0)\n"
    "v_readfirstlane_b32 s74, v12\n"
    "s_nop 0\n"
    "s_cmp_ge_i32 s74, 0\n"
    "s_cbranch_scc1 .LgDOWN_par1_%=\n"
    "s_sleep 4\n"
    "s_sub_u32 s72, s72, 1\n"
    "s_cbranch_scc0 .LgDOWN_par_%=\n"
    "s_mov_b32 s72, 0\n"
    ".LgDOWN_par1_%=:\n"
    "s_and_b32 s74, s74, 1\n"
    "s_lshl_b32 s74, s74, 13\n"
    "s_add_u32 s36, s36, s74\n"
    "s_addc_u32 s37, s37, 0\n"
    "s_branch .LgDOWN_block_%=\n"
    ".LgDOWN_LOOPB_%=:\n"
    ".LgDOWN_head_%=:\n"
    "v_mfma_f32_16x16x32_bf16 v[64:67], a[8:11], v[16:19], 0\n"
    "s_waitcnt lgkmcnt(6)\n"
    "ds_read_b128 v[16:19], v9 offset:0\n"
    "v_mfma_f32_16x16x32_bf16 v[68:71], a[8:11], v[20:23], 0\n"
    "s_waitcnt lgkmcnt(6)\n"
    "ds_read_b128 v[20:23], v9 offset:2048\n"
    "v_mfma_f32_16x16x32_bf16 v[64:67], a[20:23], v[24:27], v[64:67]\n"
    "s_waitcnt lgkmcnt(6)\n"
    "ds_read_b128 v[24:27], v9 offset:4096\n"
    "v_mfma_f32_16x16x32_bf16 v[68:71], a[20:23], v[28:31], v[68:71]\n"
    "s_waitcnt lgkmcnt(6)\n"
    "ds_read_b128 v[28:31], v9 offset:6144\n"
    "v_mfma_f32_16x16x32_bf16 v[64:67], a[32:35], v[32:35], v[64:67]\n"
    "s_waitcnt lgkmcnt(6)\n"
    "ds_read_b128 v[32:35], v9 offset:1024\n"
    "v_mfma_f32_16x16x32_bf16 v[68:71], a[32:35], v[36:39], v[68:71]\n"
    "s_waitcnt lgkmcnt(6)\n"
    "ds_read_b128 v[36:39], v9 offset:3072\n"
    "v_mfma_f32_16x16x32_bf16 v[64:67], a[44:47], v[40:43], v[64:67]\n"
    "s_waitcnt lgkmcnt(6)\n"
    "ds_read_b128 v[40:43], v9 offset:5120\n"
    "v_mfma_f32_16x16x32_bf16 v[68:71], a[44:47], v[44:47], v[68:71]\n"
    "s_waitcnt lgkmcnt(6)\n"
    "ds_read_b128 v[44:47], v9 offset:7168\n"
    "v_mfma_f32_16x16x32_bf16 v[48:51], a[0:3], v[16:19], 0\n"
    "s_waitcnt lgkmcnt(6)\n"
    "v_cvt_pk_bf16_f32 v56, v56, v57\n"
    "v_cvt_pk_bf16_f32 v57, v58, v59\n"
    "v_mfma_f32_16x16x32_bf16 v[52:55], a[4:7], v[16:19], 0\n"
    "ds_read_b128 v[16:19], v9 offset:8192\n"
    "v_cvt_pk_bf16_f32 v58, v60, v61\n"
    "v_mfma_f32_16x16x32_bf16 v[48:51], a[12:15], v[20:23], v[48:51]\n"
    "s_waitcnt lgkmcnt(6)\n"
    "v_cvt_pk_bf16_f32 v59, v62, v63\n"
    "v_mfma_f32_16x16x32_bf16 v[52:55], a[16:19], v[20:23], v[52:55]\n"
    "ds_read_b128 v[20:23], v9 offset:9216\n"
    "v_mfma_f32_16x16x32_bf16 v[48:51], a[24:27], v[24:27], v[48:51]\n"
    "s_waitcnt lgkmcnt(6)\n"
    "s_sub_u32 s74, s68, 1\n"
    "buffer_store_dwordx4 v[56:59], v15, s[36:39], s73 offen offset:1024 sc1\n"
    "v_mfma_f32_16x16x32_bf16 v[52:55], a[28:31], v[24:27], v[52:55]\n"
    "ds_read_b128 v[24:27], v9 offset:10240\n"
    "v_mfma_f32_16x16x32_bf16 v[48:51], a[36:39], v[28:31], v[48:51]\n"
    "s_waitcnt lgkmcnt(6)\n"
    "s_and_b32 s76, s71, s54\n"
    "v_mfma_f32_16x16x32_bf16 v[52:55], a[40:43], v[28:31], v[52:55]\n"
    "ds_read_b128 v[28:31], v9 offset:11264\n"
    "v_mfma_f32_16x16x32_bf16 v[56:59], a[0:3], v[32:35], 0\n"
    "s_waitcnt lgkmcnt(6)\n"
    "s_mul_i32 s76, s76, s53\n"
    "v_mfma_f32_16x16x32_bf16 v[60:63], a[4:7], v[32:35], 0\n"
    "ds_read_b128 v[32:35], v9 offset:12288\n"
    "v_mfma_f32_16x16x32_bf16 v[56:59], a[12:15], v[36:39], v[56:59]\n"
    "s_waitcnt lgkmcnt(6)\n"
    "s_add_u32 s76, s76, s52\n"
    "v_mfma_f32_16x16x32_bf16 v[60:63], a[16:19], v[36:39], v[60:63]\n"
    "ds_read_b128 v[36:39], v9 offset:13312\n"
    "v_mfma_f32_16x16x32_bf16 v[56:59], a[24:27], v[40:43], v[56:59]\n"
    "s_waitcnt lgkmcnt(6)\n"
    "s_and_b32 s77, s71, 1\n"
    "v_mfma_f32_16x16x32_bf16 v[60:63], a[28:31], v[40:43], v[60:63]\n"
    "ds_read_b128 v[40:43], v9 offset:14336\n"
    "v_mfma_f32_16x16x32_bf16 v[56:59], a[36:39], v[44:47], v[56:59]\n"
    "s_waitcnt lgkmcnt(6)\n"
    "s_mul_i32 s77, s77, 81920\n"
    "v_mfma_f32_16x16x32_bf16 v[60:63], a[40:43], v[44:47], v[60:63]\n"
    "ds_read_b128 v[44:47], v9 offset:15360\n"
    "v_mfma_f32_16x16x32_bf16 v[48:51], a[48:51], v[16:19], v[48:51]\n"
    "s_add_u32 s77, s77, s59\n"
    "v_mfma_f32_16x16x32_bf16 v[52:55], a[52:55], v[16:19], v[52:55]\n"
    "s_waitcnt lgkmcnt(6)\n"
    "v_mfma_f32_16x16x32_bf16 v[64:67], a[56:59], v[16:19], v[64:67]\n"
    "ds_read_b128 v[16:19], v9 offset:16384\n"
    "v_mfma_f32_16x16x32_bf16 v[56:59], a[48:51], v[20:23], v[56:59]\n"
    "v_mfma_f32_16x16x32_bf16 v[60:63], a[52:55], v[20:23], v[60:63]\n"
    "s_waitcnt lgkmcnt(6)\n"
    "v_mfma_f32_16x16x32_bf16 v[68:71], a[56:59], v[20:23], v[68:71]\n"
    "ds_read_b128 v[20:23], v9 offset:17408\n"
    "v_mfma_f32_16x16x32_bf16 v[48:51], a[60:63], v[24:27], v[48:51]\n"
    "v_mfma_f32_16x16x32_bf16 v[52:55], a[64:67], v[24:27], v[52:55]\n"
    "s_waitcnt lgkmcnt(6)\n"
    "v_mfma_f32_16x16x32_bf16 v[64:67], a[68:71], v[24:27], v[64:67]\n"
    "ds_read_b128 v[24:27], v9 offset:18432\n"
    "v_mfma_f32_16x16x32_bf16 v[56:59], a[60:63], v[28:31], v[56:59]\n"
    "v_mfma_f32_16x16x32_bf16 v[60:63], a[64:67], v[28:31], v[60:63]\n"
    "s_waitcnt lgkmcnt(6)\n"
    "v_mfma_f32_16x16x32_bf16 v[68:71], a[68:71], v[28:31], v[68:71]\n"
    "ds_read_b128 v[28:31], v9 offset:19456\n"
    "v_mfma_f32_16x16x32_bf16 v[48:51], a[72:75], v[32:35], v[48:51]\n"
    "v_mfma_f32_16x16x32_bf16 v[52:55], a[76:79], v[32:35], v[52:55]\n"
    "s_waitcnt lgkmcnt(6)\n"
    "v_mfma_f32_16x16x32_bf16 v[64:67], a[80:83], v[32:35], v[64:67]\n"
    "ds_read_b128 v[32:35], v9 offset:20480\n"
    "v_mfma_f32_16x16x32_bf16 v[56:59], a[72:75], v[36:39], v[56:59]\n"
    "v_mfma_f32_16x16x32_bf16 v[60:63], a[76:79], v[36:39], v[60:63]\n"
    "s_waitcnt lgkmcnt(6)\n"
    "v_mfma_f32_16x16x32_bf16 v[68:71], a[80:83], v[36:39], v[68:71]\n"
    "ds_read_b128 v[36:39], v9 offset:21504\n"
    "v_mfma_f32_16x16x32_bf16 v[48:51], a[84:87], v[40:43], v[48:51]\n"
    "v_mfma_f32_16x16x32_bf16 v[52:55], a[88:91], v[40:43], v[52:55]\n"
    "s_waitcnt lgkmcnt(6)\n"
    "v_mfma_f32_16x16x32_bf16 v[64:67], a[92:95], v[40:43], v[64:67]\n"
    "ds_read_b128 v[40:43], v9 offset:22528\n"
    "v_mfma_f32_16x16x32_bf16 v[56:59], a[84:87], v[44:47], v[56:59]\n"
    "v_mfma_f32_16x16x32_bf16 v[60:63], a[88:91], v[44:47], v[60:63]\n"
    "s_waitcnt lgkmcnt(6)\n"
    "v_mfma_f32_16x16x32_bf16 v[68:71], a[92:95], v[44:47], v[68:71]\n"
    "ds_read_b128 v[44:47], v9 offset:23552\n"
    "v_mfma_f32_16x16x32_bf16 v[48:51], a[96:99], v[16:19], v[48:51]\n"
    "v_mov_b32 v12, s74\n"
    "v_mfma_f32_16x16x32_bf16 v[52:55], a[100:103], v[16:19], v[52:55]\n"
    "s_waitcnt lgkmcnt(6)\n"
    "s_waitcnt vmcnt(0)\n"
    "v_add_u32 v13, v11, v3\n"
    "v_mfma_f32_16x16x32_bf16 v[64:67], a[104:107], v[16:19], v[64:67]\n"
    "ds_read_b128 v[16:19], v9 offset:24576\n"
    "buffer_store_dword v12, v14, s[36:39], 0 offen sc1\n"
    "v_cmp_gt_i32 vcc, s71, v13\n"
    "v_mfma_f32_16x16x32_bf16 v[56:59], a[96:99], v[20:23], v[56:59]\n"
    "v_add_u32 v10, 49152, v9\n"
    "v_mfma_f32_16x16x32_bf16 v[60:63], a[100:103], v[20:23], v[60:63]\n"
    "s_waitcnt lgkmcnt(6)\n"
    "v_mfma_f32_16x16x32_bf16 v[68:71], a[104:107], v[20:23], v[68:71]\n"
    "ds_read_b128 v[20:23], v9 offset:25600\n"
    ".LgDOWN_mid_%=:\n"
    "s_cbranch_vccnz .LgDOWN_chk0_%=\n"
    "v_mfma_f32_16x16x32_bf16 v[48:51], a[108:111], v[24:27], v[48:51]\n"
    "s_add_u32 m0, s77, s59\n"
    "s_add_u32 s77, s77, 8192\n"
    "buffer_load_dwordx4 v1, s[36:39], s76 offen sc1 lds\n"
    "v_mfma_f32_16x16x32_bf16 v[52:55], a[112:115], v[24:27], v[52:55]\n"
    "s_waitcnt lgkmcnt(6)\n"
    "buffer_load_dwordx4 v1, s[36:39], s76 offen offset:1024 sc1 lds\n"
    "s_add_u32 s76, s76, 16384\n"
    "v_mfma_f32_16x16x32_bf16 v[64:67], a[116:119], v[24:27], v[64:67]\n"
    "ds_read_b128 v[24:27], v9 offset:26624\n"
    "v_mfma_f32_16x16x32_bf16 v[56:59], a[108:111], v[28:31], v[56:59]\n"
    "s_mov_b32 s70, 1\n"
    "v_mfma_f32_16x16x32_bf16 v[60:63], a[112:115], v[28:31], v[60:63]\n"
    "s_waitcnt lgkmcnt(6)\n"
    "s_add_u32 m0, s77, s59\n"
    "s_add_u32 s77, s77, 8192\n"
    "buffer_load_dwordx4 v1, s[36:39], s76 offen sc1 lds\n"
    "v_mfma_f32_16x16x32_bf16 v[68:71], a[116:119], v[28:31], v[68:71]\n"
    "ds_read_b128 v[28:31], v9 offset:27648\n"
    "v_mfma_f32_16x16x32_bf16 v[48:51], a[120:123], v[32:35], v[48:51]\n"
    "buffer_load_dwordx4 v1, s[36:39], s76 offen offset:1024 sc1 lds\n"
    "s_add_u32 s76, s76, 16384\n"
    "v_mfma_f32_16x16x32_bf16 v[52:55], a[124:127], v[32:35], v[52:55]\n"
    "s_waitcnt lgkmcnt(6)\n"
    "s_and_b32 s73, s68, s57\n"
    "v_mfma_f32_16x16x32_bf16 v[64:67], a[128:131], v[32:35], v[64:67]\n"
    "ds_read_b128 v[32:35], v9 offset:28672\n"
    "v_mfma_f32_16x16x32_bf16 v[56:59], a[120:123], v[36:39], v[56:59]\n"
    "s_add_u32 m0, s77, s59\n"
    "s_add_u32 s77, s77, 8192\n"
    "buffer_load_dwordx4 v1, s[36:39], s76 offen sc1 lds\n"
    "v_mfma_f32_16x16x32_bf16 v[60:63], a[124:127], v[36:39], v[60:63]\n"
    "s_waitcnt lgkmcnt(6)\n"
    "buffer_load_dwordx4 v1, s[36:39], s76 offen offset:1024 sc1 lds\n"
    "s_add_u32 s76, s76, 16384\n"
    "v_mfma_f32_16x16x32_bf16 v[68:71], a[128:131], v[36:39], v[68:71]\n"
    "ds_read_b128 v[36:39], v9 offset:29696\n"
    "v_mfma_f32_16x16x32_bf16 v[48:51], a[132:135], v[40:43], v[48:51]\n"
    "s_mul_i32 s73, s73, s56\n"
    "v_mfma_f32_16x16x32_bf16 v[52:55], a[136:139], v[40:43], v[52:55]\n"
    "s_waitcnt lgkmcnt(6)\n"
    "s_add_u32 m0, s77, s59\n"
    "s_add_u32 s77, s77, 8192\n"
    "buffer_load_dwordx4 v1, s[36:39], s76 offen sc1 lds\n"
    "v_mfma_f32_16x16x32_bf16 v[64:67], a[140:143], v[40:43], v[64:67]\n"
    "ds_read_b128 v[40:43], v9 offset:30720\n"
    "v_mfma_f32_16x16x32_bf16 v[56:59], a[132:135], v[44:47], v[56:59]\n"
    "buffer_load_dwordx4 v1, s[36:39], s76 offen offset:1024 sc1 lds\n"
    "s_add_u32 s76, s76, 16384\n"
    "v_mfma_f32_16x16x32_bf16 v[60:63], a[136:139], v[44:47], v[60:63]\n"
    "s_waitcnt lgkmcnt(6)\n"
    "s_add_u32 s73, s73, s55\n"
    "v_mfma_f32_16x16x32_bf16 v[68:71], a[140:143], v[44:47], v[68:71]\n"
    "ds_read_b128 v[44:47], v9 offset:31744\n"
    "v_mfma_f32_16x16x32_bf16 v[48:51], a[144:147], v[16:19], v[48:51]\n"
    "s_add_u32 m0, s77, s59\n"
    "s_add_u32 s77, s77, 8192\n"
    "buffer_load_dwordx4 v1, s[36:39], s76 offen sc1 lds\n"
    "v_mfma_f32_16x16x32_bf16 v[52:55], a[148:151], v[16:19], v[52:55]\n"
    "s_waitcnt lgkmcnt(6)\n"
    "v_mfma_f32_16x16x32_bf16 v[64:67], a[152:155], v[16:19], v[64:67]\n"
    "ds_read_b128 v[16:19], v9 offset:32768\n"
    "v_mfma_f32_16x16x32_bf16 v[56:59], a[144:147], v[20:23], v[56:59]\n"
    "buffer_load_dwordx4 v1, s[36:39], s76 offen offset:1024 sc1 lds\n"
    "s_add_u32 s76, s76, 16384\n"
    "v_mfma_f32_16x16x32_bf16 v[60:63], a[148:151], v[20:23], v[60:63]\n"
    "s_waitcnt lgkmcnt(6)\n"
    "s_add_u32 m0, s77, s59\n"
    "s_add_u32 s77, s77, 8192\n"
    "buffer_load_dwordx4 v1, s[36:39], s76 offen sc1 lds\n"
    "v_mfma_f32_16x16x32_bf16 v[68:71], a[152:155], v[20:23], v[68:71]\n"
    "ds_read_b128 v[20:23], v9 offset:33792\n"
    "v_mfma_f32_16x16x32_bf16 v[48:51], a[156:159], v[24:27], v[48:51]\n"
    "v_mfma_f32_16x16x32_bf16 v[52:55], a[160:163], v[24:27], v[52:55]\n"
    "s_waitcnt lgkmcnt(6)\n"
    "buffer_load_dwordx4 v1, s[36:39], s76 offen offset:1024 sc1 lds\n"
    "s_add_u32 s76, s76, 16384\n"
    "v_mfma_f32_16x16x32_bf16 v[64:67], a[164:167], v[24:27], v[64:67]\n"
    "ds_read_b128 v[24:27], v9 offset:34816\n"
    "v_mfma_f32_16x16x32_bf16 v[56:59], a[156:159], v[28:31], v[56:59]\n"
    "s_add_u32 m0, s77, s59\n"
    "s_add_u32 s77, s77, 8192\n"
    "buffer_load_dwordx4 v1, s[36:39], s76 offen sc1 lds\n"
    "v_mfma_f32_16x16x32_bf16 v[60:63], a[160:163], v[28:31], v[60:63]\n"
    "s_waitcnt lgkmcnt(6)\n"
    "v_mfma_f32_16x16x32_bf16 v[68:71], a[164:167], v[28:31], v[68:71]\n"
    "ds_read_b128 v[28:31], v9 offset:35840\n"
    "v_mfma_f32_16x16x32_bf16 v[48:51], a[168:171], v[32:35], v[48:51]\n"
    "buffer_load_dwordx4 v1, s[36:39], s76 offen offset:1024 sc1 lds\n"
    "s_add_u32 s76, s76, 16384\n"
    "v_mfma_f32_16x16x32_bf16 v[52:55], a[172:175], v[32:35], v[52:55]\n"
    "s_waitcnt lgkmcnt(6)\n"
    "s_add_u32 m0, s77, s59\n"
    "s_add_u32 s77, s77, 8192\n"
    "buffer_load_dwordx4 v1, s[36:39], s76 offen sc1 lds\n"
    "v_mfma_f32_16x16x32_bf16 v[64:67], a[176:179], v[32:35], v[64:67]\n"
    "ds_read_b128 v[32:35], v9 offset:36864\n"
    "v_mfma_f32_16x16x32_bf16 v[56:59], a[168:171], v[36:39], v[56:59]\n"
    "v_mfma_f32_16x16x32_bf16 v[60:63], a[172:175], v[36:39], v[60:63]\n"
    "s_waitcnt lgkmcnt(6)\n"
    "buffer_load_dwordx4 v1, s[36:39], s76 offen offset:1024 sc1 lds\n"
    "s_add_u32 s76, s76, 16384\n"
    "v_mfma_f32_16x16x32_bf16 v[68:71], a[176:179], v[36:39], v[68:71]\n"
    "ds_read_b128 v[36:39], v9 offset:37888\n"
    "v_mfma_f32_16x16x32_bf16 v[48:51], a[180:183], v[40:43], v[48:51]\n"
    "s_add_u32 m0, s77, s59\n"
    "s_add_u32 s77, s77, 8192\n"
    "buffer_load_dwordx4 v1, s[36:39], s76 offen sc1 lds\n"
    "v_mfma_f32_16x16x32_bf16 v[52:55], a[184:187], v[40:43], v[52:55]\n"
    "s_waitcnt lgkmcnt(6)\n"
    "v_mfma_f32_16x16x32_bf16 v[64:67], a[188:191], v[40:43], v[64:67]\n"
    "ds_read_b128 v[40:43], v9 offset:38912\n"
    "v_mfma_f32_16x16x32_bf16 v[56:59], a[180:183], v[44:47], v[56:59]\n"
    "buffer_load_dwordx4 v1, s[36:39], s76 offen offset:1024 sc1 lds\n"
    "s_add_u32 s76, s76, 16384\n"
    "v_mfma_f32_16x16x32_bf16 v[60:63], a[184:187], v[44:47], v[60:63]\n"
    "s_waitcnt lgkmcnt(6)\n"
    "v_mfma_f32_16x16x32_bf16 v[68:71], a[188:191], v[44:47], v[68:71]\n"
    "ds_read_b128 v[44:47], v9 offset:39936\n"
    "v_mfma_f32_16x16x32_bf16 v[48:51], a[192:195], v[16:19], v[48:51]\n"
    ".LgDOWN_seg0_%=:\n"
    "s_cmp_lg_u32 s70, 0\n"
    "s_cbranch_scc0 .LgDOWN_chk1_%=\n"
    ".LgDOWN_plain1_%=:\n"
    "v_mfma_f32_16x16x32_bf16 v[52:55], a[196:199], v[16:19], v[52:55]\n"
    "s_waitcnt lgkmcnt(6)\n"
    "v_mfma_f32_16x16x32_bf16 v[64:67], a[200:203], v[16:19], v[64:67]\n"
    "ds_read_b128 v[16:19], v9 offset:40960\n"
    "v_mfma_f32_16x16x32_bf16 v[56:59], a[192:195], v[20:23], v[56:59]\n"
    "v_mfma_f32_16x16x32_bf16 v[60:63], a[196:199], v[20:23], v[60:63]\n"
    "s_waitcnt lgkmcnt(6)\n"
    "v_mfma_f32_16x16x32_bf16 v[68:71], a[200:203], v[20:23], v[68:71]\n"
    "ds_read_b128 v[20:23], v9 offset:41984\n"
    "v_mfma_f32_16x16x32_bf16 v[48:51], a[204:207], v[24:27], v[48:51]\n"
    "v_mfma_f32_16x16x32_bf16 v[52:55], a[208:211], v[24:27], v[52:55]\n"
    "s_waitcnt lgkmcnt(6)\n"
    "v_mfma_f32_16x16x32_bf16 v[64:67], a[212:215], v[24:27], v[64:67]\n"
    "ds_read_b128 v[24:27], v9 offset:43008\n"
    "v_mfma_f32_16x16x32_bf16 v[56:59], a[204:207], v[28:31], v[56:59]\n"
    "v_mfma_f32_16x16x32_bf16 v[60:63], a[208:211], v[28:31], v[60:63]\n"
    "s_waitcnt lgkmcnt(6)\n"
    "v_mfma_f32_16x16x32_bf16 v[68:71], a[212:215], v[28:31], v[68:71]\n"
    "ds_read_b128 v[28:31], v9 offset:44032\n"
    "v_mfma_f32_16x16x32_bf16 v[48:51], a[216:219], v[32:35], v[48:51]\n"
    "v_mfma_f32_16x16x32_bf16 v[52:55], a[220:223], v[32:35], v[52:55]\n"
    "s_waitcnt lgkmcnt(6)\n"
    "v_mfma_f32_16x16x32_bf16 v[64:67], a[224:227], v[32:35], v[64:67]\n"
    "ds_read_b128 v[32:35], v9 offset:45056\n"
    "v_mfma_f32_16x16x32_bf16 v[56:59], a[216:219], v[36:39], v[56:59]\n"
    "v_mfma_f32_16x16x32_bf16 v[60:63], a[220:223], v[36:39], v[60:63]\n"
    "s_waitcnt lgkmcnt(6)\n"
    "v_mfma_f32_16x16x32_bf16 v[68:71], a[224:227], v[36:39], v[68:71]\n"
    "ds_read_b128 v[36:39], v9 offset:46080\n"
    "v_mfma_f32_16x16x32_bf16 v[48:51], a[228:231], v[40:43], v[48:51]\n"
    "v_mfma_f32_16x16x32_bf16 v[52:55], a[232:235], v[40:43], v[52:55]\n"
    "s_waitcnt lgkmcnt(6)\n"
    "v_mfma_f32_16x16x32_bf16 v[64:67], a[236:239], v[40:43], v[64:67]\n"
    "ds_read_b128 v[40:43], v9 offset:47104\n"
    "v_mfma_f32_16x16x32_bf16 v[56:59], a[228:231], v[44:47], v[56:59]\n"
    "v_mfma_f32_16x16x32_bf16 v[60:63], a[232:235], v[44:47], v[60:63]\n"
    "s_waitcnt lgkmcnt(6)\n"
    "v_mfma_f32_16x16x32_bf16 v[68:71], a[236:239], v[44:47], v[68:71]\n"
    "ds_read_b128 v[44:47], v9 offset:48128\n"
    "v_mfma_f32_16x16x32_bf16 v[48:51], a[240:243], v[16:19], v[48:51]\n"
    "v_mfma_f32_16x16x32_bf16 v[52:55], a[244:247], v[16:19], v[52:55]\n"
    "s_waitcnt lgkmcnt(6)\n"
    "v_mfma_f32_16x16x32_bf16 v[64:67], a[248:251], v[16:19], v[64:67]\n"
    "ds_read_b128 v[16:19], v10 offset:0\n"
    "v_mfma_f32_16x16x32_bf16 v[56:59], a[240:243], v[20:23], v[56:59]\n"
    "v_mfma_f32_16x16x32_bf16 v[60:63], a[244:247], v[20:23], v[60:63]\n"
    "s_waitcnt lgkmcnt(6)\n"
    "v_mfma_f32_16x16x32_bf16 v[68:71], a[248:251], v[20:23], v[68:71]\n"
    "ds_read_b128 v[20:23], v10 offset:1024\n"
    "v_mfma_f32_16x16x32_bf16 v[48:51], a[252:255], v[24:27], v[48:51]\n"
    "v_mfma_f32_16x16x32_bf16 v[52:55], v[80:83], v[24:27], v[52:55]\n"
    "s_waitcnt lgkmcnt(6)\n"
    "v_mfma_f32_16x16x32_bf16 v[64:67], v[84:87], v[24:27], v[64:67]\n"
    "ds_read_b128 v[24:27], v10 offset:2048\n"
    "v_mfma_f32_16x16x32_bf16 v[56:59], a[252:255], v[28:31], v[56:59]\n"
    "v_mfma_f32_16x16x32_bf16 v[60:63], v[80:83], v[28:31], v[60:63]\n"
    "s_waitcnt lgkmcnt(6)\n"
    "v_mfma_f32_16x16x32_bf16 v[68:71], v[84:87], v[28:31], v[68:71]\n"
    "ds_read_b128 v[28:31], v10 offset:3072\n"
    "v_mfma_f32_16x16x32_bf16 v[48:51], v[88:91], v[32:35], v[48:51]\n"
    "v_mfma_f32_16x16x32_bf16 v[52:55], v[92:95], v[32:35], v[52:55]\n"
    "s_waitcnt lgkmcnt(6)\n"
    "v_mfma_f32_16x16x32_bf16 v[64:67], v[96:99], v[32:35], v[64:67]\n"
    "ds_read_b128 v[32:35], v10 offset:4096\n"
    "v_mfma_f32_16x16x32_bf16 v[56:59], v[88:91], v[36:39], v[56:59]\n"
    "v_mfma_f32_16x16x32_bf16 v[60:63], v[92:95], v[36:39], v[60:63]\n"
    "s_waitcnt lgkmcnt(6)\n"
    "v_mfma_f32_16x16x32_bf16 v[68:71], v[96:99], v[36:39], v[68:71]\n"
    "ds_read_b128 v[36:39], v10 offset:5120\n"
    "v_mfma_f32_16x16x32_bf16 v[48:51], v[100:103], v[40:43], v[48:51]\n"
    "v_mfma_f32_16x16x32_bf16 v[52:55], v[104:107], v[40:43], v[52:55]\n"
    "s_waitcnt lgkmcnt(6)\n"
    ".LgDOWN_seg1_%=:\n"
    "s_cmp_lg_u32 s70, 0\n"
    "s_cbranch_scc0 .LgDOWN_chk2_%=\n"
    ".LgDOWN_plain2_%=:\n"
    "v_mfma_f32_16x16x32_bf16 v[64:67], v[108:111], v[40:43], v[64:67]\n"
    "ds_read_b128 v[40:43], v10 offset:6144\n"
    "v_mfma_f32_16x16x32_bf16 v[56:59], v[100:103], v[44:47], v[56:59]\n"
    "v_mfma_f32_16x16x32_bf16 v[60:63], v[104:107], v[44:47], v[60:63]\n"
    "s_waitcnt lgkmcnt(6)\n"
    "v_mfma_f32_16x16x32_bf16 v[68:71], v[108:111], v[44:47], v[68:71]\n"
    "ds_read_b128 v[44:47], v10 offset:7168\n"
    "v_mfma_f32_16x16x32_bf16 v[48:51], v[112:115], v[16:19], v[48:51]\n"
    "v_mfma_f32_16x16x32_bf16 v[52:55], v[116:119], v[16:19], v[52:55]\n"
    "s_waitcnt lgkmcnt(6)\n"
    "v_mfma_f32_16x16x32_bf16 v[64:67], v[120:123], v[16:19], v[64:67]\n"
    "ds_read_b128 v[16:19], v10 offset:8192\n"
    "v_mfma_f32_16x16x32_bf16 v[56:59], v[112:115], v[20:23], v[56:59]\n"
    "v_mfma_f32_16x16x32_bf16 v[60:63], v[116:119], v[20:23], v[60:63]\n"
    "s_waitcnt lgkmcnt(6)\n"
    "v_mfma_f32_16x16x32_bf16 v[68:71], v[120:123], v[20:23], v[68:71]\n"
    "ds_read_b128 v[20:23], v10 offset:9216\n"
    "v_mfma_f32_16x16x32_bf16 v[48:51], v[124:127], v[24:27], v[48:51]\n"
    "v_mfma_f32_16x16x32_bf16 v[52:55], v[128:131], v[24:27], v[52:55]\n"
    "s_waitcnt lgkmcnt(6)\n"
    "v_mfma_f32_16x16x32_bf16 v[64:67], v[132:135], v[24:27], v[64:67]\n"
    "ds_read_b128 v[24:27], v10 offset:10240\n"
    "v_mfma_f32_16x16x32_bf16 v[56:59], v[124:127], v[28:31], v[56:59]\n"
    "v_mfma_f32_16x16x32_bf16 v[60:63], v[128:131], v[28:31], v[60:63]\n"
    "s_waitcnt lgkmcnt(6)\n"
    "v_mfma_f32_16x16x32_bf16 v[68:71], v[132:135], v[28:31], v[68:71]\n"
    "ds_read_b128 v[28:31], v10 offset:11264\n"
    "v_mfma_f32_16x16x32_bf16 v[48:51], v[136:139], v[32:35], v[48:51]\n"
    "v_mfma_f32_16x16x32_bf16 v[52:55], v[140:143], v[32:35], v[52:55]\n"
    "s_waitcnt lgkmcnt(6)\n"
    "v_mfma_f32_16x16x32_bf16 v[64:67], v[144:147], v[32:35], v[64:67]\n"
    "ds_read_b128 v[32:35], v10 offset:12288\n"
    "v_mfma_f32_16x16x32_bf16 v[56:59], v[136:139], v[36:39], v[56:59]\n"
    "v_mfma_f32_16x16x32_bf16 v[60:63], v[140:143], v[36:39], v[60:63]\n"
    "s_waitcnt lgkmcnt(6)\n"
    "v_mfma_f32_16x16x32_bf16 v[68:71], v[144:147], v[36:39], v[68:71]\n"
    "ds_read_b128 v[36:39], v10 offset:13312\n"
    "v_mfma_f32_16x16x32_bf16 v[48:51], v[148:151], v[40:43], v[48:51]\n"
    "v_mfma_f32_16x16x32_bf16 v[52:55], v[152:155], v[40:43], v[52:55]\n"
    "s_waitcnt lgkmcnt(6)\n"
    "v_mfma_f32_16x16x32_bf16 v[64:67], v[156:159], v[40:43], v[64:67]\n"
    "ds_read_b128 v[40:43], v10 offset:14336\n"
    "v_mfma_f32_16x16x32_bf16 v[56:59], v[148:151], v[44:47], v[56:59]\n"
    "v_mfma_f32_16x16x32_bf16 v[60:63], v[152:155], v[44:47], v[60:63]\n"
    "s_waitcnt lgkmcnt(6)\n"
    "v_mfma_f32_16x16x32_bf16 v[68:71], v[156:159], v[44:47], v[68:71]\n"
    "ds_read_b128 v[44:47], v10 offset:15360\n"
    "v_mfma_f32_16x16x32_bf16 v[48:51], v[160:163], v[16:19], v[48:51]\n"
    "v_mfma_f32_16x16x32_bf16 v[52:55], v[164:167], v[16:19], v[52:55]\n"
    "s_waitcnt lgkmcnt(6)\n"
    "v_mfma_f32_16x16x32_bf16 v[64:67], v[168:171], v[16:19], v[64:67]\n"
    "ds_read_b128 v[16:19], v10 offset:16384\n"
    "v_mfma_f32_16x16x32_bf16 v[56:59], v[160:163], v[20:23], v[56:59]\n"
    "v_mfma_f32_16x16x32_bf16 v[60:63], v[164:167], v[20:23], v[60:63]\n"
    "s_waitcnt lgkmcnt(6)\n"
    "v_mfma_f32_16x16x32_bf16 v[68:71], v[168:171], v[20:23], v[68:71]\n"
    "ds_read_b128 v[20:23], v10 offset:17408\n"
    "v_mfma_f32_16x16x32_bf16 v[48:51], v[172:175], v[24:27], v[48:51]\n"
    "v_mfma_f32_16x16x32_bf16 v[52:55], v[176:179], v[24:27], v[52:55]\n"
    "s_waitcnt lgkmcnt(6)\n"
    "v_mfma_f32_16x16x32_bf16 v[64:67], v[180:183], v[24:27], v[64:67]\n"
    "ds_read_b128 v[24:27], v10 offset:18432\n"
    "v_mfma_f32_16x16x32_bf16 v[56:59], v[172:175], v[28:31], v[56:59]\n"
    "v_mfma_f32_16x16x32_bf16 v[60:63], v[176:179], v[28:31], v[60:63]\n"
    "s_waitcnt lgkmcnt(6)\n"
    "v_mfma_f32_16x16x32_bf16 v[68:71], v[180:183], v[28:31], v[68:71]\n"
    "ds_read_b128 v[28:31], v10 offset:19456\n"
    "v_mfma_f32_16x16x32_bf16 v[48:51], v[184:187], v[32:35], v[48:51]\n"
    "v_mfma_f32_16x16x32_bf16 v[52:55], v[188:191], v[32:35], v[52:55]\n"
    "s_waitcnt lgkmcnt(6)\n"
    "v_mfma_f32_16x16x32_bf16 v[64:67], v[192:195], v[32:35], v[64:67]\n"
    "ds_read_b128 v[32:35], v10 offset:20480\n"
    "v_mfma_f32_16x16x32_bf16 v[56:59], v[184:187], v[36:39], v[56:59]\n"
    ".LgDOWN_seg2_%=:\n"
    "s_cmp_lg_u32 s70, 1\n"
    "s_cbranch_scc1 .LgDOWN_slow_%=\n"
    "v_mfma_f32_16x16x32_bf16 v[60:63], v[188:191], v[36:39], v[60:63]\n"
    "s_waitcnt lgkmcnt(6)\n"
    "s_mov_b32 s68, s71\n"
    "v_mfma_f32_16x16x32_bf16 v[68:71], v[192:195], v[36:39], v[68:71]\n"
    "ds_read_b128 v[36:39], v10 offset:21504\n"
    "v_mfma_f32_16x16x32_bf16 v[48:51], v[196:199], v[40:43], v[48:51]\n"
    "s_add_u32 s71, s68, 1\n"
    "v_mfma_f32_16x16x32_bf16 v[52:55], v[200:203], v[40:43], v[52:55]\n"
    "s_waitcnt lgkmcnt(6)\n"
    "s_cmp_eq_u32 s71, 64\n"
    "v_mfma_f32_16x16x32_bf16 v[64:67], v[204:207], v[40:43], v[64:67]\n"
    "ds_read_b128 v[40:43], v10 offset:22528\n"
    "v_mfma_f32_16x16x32_bf16 v[56:59], v[196:199], v[44:47], v[56:59]\n"
    "s_cselect_b32 s70, 2, 0\n"
    "v_mfma_f32_16x16x32_bf16 v[60:63], v[200:203], v[44:47], v[60:63]\n"
    "s_waitcnt lgkmcnt(6)\n"
    "v_mfma_f32_16x16x32_bf16 v[68:71], v[204:207], v[44:47], v[68:71]\n"
    "ds_read_b128 v[44:47], v10 offset:23552\n"
    "v_mfma_f32_16x16x32_bf16 v[64:67], v[216:219], v[16:19], v[64:67]\n"
    "s_waitcnt lgkmcnt(6)\n"
    "ds_read_b128 v[16:19], v10 offset:16384\n"
    "v_mfma_f32_16x16x32_bf16 v[68:71], v[216:219], v[20:23], v[68:71]\n"
    "s_waitcnt lgkmcnt(6)\n"
    "ds_read_b128 v[20:23], v10 offset:18432\n"
    "v_mfma_f32_16x16x32_bf16 v[64:67], v[228:231], v[24:27], v[64:67]\n"
    "s_waitcnt lgkmcnt(6)\n"
    "ds_read_b128 v[24:27], v10 offset:20480\n"
    "v_mfma_f32_16x16x32_bf16 v[68:71], v[228:231], v[28:31], v[68:71]\n"
    "s_waitcnt lgkmcnt(6)\n"
    "ds_read_b128 v[28:31], v10 offset:22528\n"
    "v_mfma_f32_16x16x32_bf16 v[64:67], v[240:243], v[32:35], v[64:67]\n"
    "s_waitcnt lgkmcnt(6)\n"
    "ds_read_b128 v[32:35], v10 offset:17408\n"
    "v_mfma_f32_16x16x32_bf16 v[68:71], v[240:243], v[36:39], v[68:71]\n"
    "s_waitcnt lgkmcnt(6)\n"
    "ds_read_b128 v[36:39], v10 offset:19456\n"
    "v_mfma_f32_16x16x32_bf16 v[64:67], v[252:255], v[40:43], v[64:67]\n"
    "s_waitcnt lgkmcnt(6)\n"
    "ds_read_b128 v[40:43], v10 offset:21504\n"
    "v_mfma_f32_16x16x32_bf16 v[68:71], v[252:255], v[44:47], v[68:71]\n"
    "s_waitcnt lgkmcnt(6)\n"
    "ds_read_b128 v[44:47], v10 offset:23552\n"
    "v_add_u32 v13, v11, v3\n"
    "v_mfma_f32_16x16x32_bf16 v[48:51], v[208:211], v[16:19], v[48:51]\n"
    "v_cmp_gt_i32 vcc, s71, v13\n"
    "v_mfma_f32_16x16x32_bf16 v[52:55], v[212:215], v[16:19], v[52:55]\n"
    "s_waitcnt lgkmcnt(6)\n"
    "v_cvt_pk_bf16_f32 v64, v64, v65\n"
    "v_cvt_pk_bf16_f32 v65, v66, v67\n"
    "v_mfma_f32_16x16x32_bf16 v[48:51], v[220:223], v[20:23], v[48:51]\n"
    "s_waitcnt lgkmcnt(5)\n"
    "v_cvt_pk_bf16_f32 v66, v68, v69\n"
    "v_cvt_pk_bf16_f32 v67, v70, v71\n"
    "v_xor_b32 v9, s78, v9\n"
    "v_mfma_f32_16x16x32_bf16 v[52:55], v[224:227], v[20:23], v[52:55]\n"
    "s_waitcnt vmcnt(0) lgkmcnt(0)\n"
    "s_barrier\n"
    "ds_read_b128 v[16:19], v9 offset:0\n"
    "s_nop 0\n"
    "v_mfma_f32_16x16x32_bf16 v[48:51], v[232:235], v[24:27], v[48:51]\n"
    "ds_read_b128 v[20:23], v9 offset:1024\n"
    "s_or_b64 exec, vcc, 1\n"
    "buffer_atomic_smax v11, v2, s[36:39], 0 offen sc0 sc1\n"
    "s_mov_b64 exec, -1\n"
    "v_mfma_f32_16x16x32_bf16 v[52:55], v[236:239], v[24:27], v[52:55]\n"
    "ds_read_b128 v[24:27], v9 offset:2048\n"
    "v_mfma_f32_16x16x32_bf16 v[48:51], v[244:247], v[28:31], v[48:51]\n"
    "v_permlane16_swap_b32 v64, v66\n"
    "v_mfma_f32_16x16x32_bf16 v[52:55], v[248:251], v[28:31], v[52:55]\n"
    "ds_read_b128 v[28:31], v9 offset:3072\n"
    "v_mfma_f32_16x16x32_bf16 v[56:59], v[208:211], v[32:35], v[56:59]\n"
    "v_permlane16_swap_b32 v65, v67\n"
    "v_mfma_f32_16x16x32_bf16 v[60:63], v[212:215], v[32:35], v[60:63]\n"
    "ds_read_b128 v[32:35], v9 offset:4096\n"
    "v_cvt_pk_bf16_f32 v48, v48, v49\n"
    "v_mfma_f32_16x16x32_bf16 v[56:59], v[220:223], v[36:39], v[56:59]\n"
    "buffer_store_dwordx4 v[64:67], v4, s[36:39], s73 offen sc1\n"
    "v_cvt_pk_bf16_f32 v49, v50, v51\n"
    "v_cvt_pk_bf16_f32 v50, v52, v53\n"
    "v_mfma_f32_16x16x32_bf16 v[60:63], v[224:227], v[36:39], v[60:63]\n"
    "ds_read_b128 v[36:39], v9 offset:5120\n"
    "v_cvt_pk_bf16_f32 v51, v54, v55\n"
    "v_mfma_f32_16x16x32_bf16 v[56:59], v[232:235], v[40:43], v[56:59]\n"
    "buffer_store_dwordx4 v[48:51], v15, s[36:39], s73 offen sc1\n"
    "v_mfma_f32_16x16x32_bf16 v[60:63], v[236:239], v[40:43], v[60:63]\n"
    "ds_read_b128 v[40:43], v9 offset:6144\n"
    "v_mfma_f32_16x16x32_bf16 v[56:59], v[244:247], v[44:47], v[56:59]\n"
    "s_waitcnt lgkmcnt(6)\n"
    "v_mfma_f32_16x16x32_bf16 v[60:63], v[248:251], v[44:47], v[60:63]\n"
    "ds_read_b128 v[44:47], v9 offset:7168\n"
    "s_branch .LgDOWN_head_%=\n"
    ".LgDOWN_LOOPE_%=:\n"
    ".LgDOWN_done_%=:\n"
    "s_waitcnt vmcnt(0)\n"
    "s_mov_b32 s74, 63\n"
    "v_mov_b32 v12, s74\n"
    "s_mov_b64 exec, s[62:63]\n"
    "buffer_store_dword v12, v14, s[36:39], 0 offen sc1\n"
    "s_mov_b64 exec, -1\n"
    "s_waitcnt vmcnt(0)\n"
    "s_branch .LgDOWN_FIN_%=\n"
    ".LgDOWN_chk0_%=:\n"
    "s_or_b64 exec, vcc, 1\n"
    "buffer_atomic_smax v11, v2, s[36:39], 0 offen sc0 sc1\n"
    "s_mov_b64 exec, -1\n"
    "v_mfma_f32_16x16x32_bf16 v[48:51], a[108:111], v[24:27], v[48:51]\n"
    "s_and_b32 s73, s68, s57\n"
    "v_mfma_f32_16x16x32_bf16 v[52:55], a[112:115], v[24:27], v[52:55]\n"
    "s_waitcnt lgkmcnt(6)\n"
    "s_mul_i32 s73, s73, s56\n"
    "v_mfma_f32_16x16x32_bf16 v[64:67], a[116:119], v[24:27], v[64:67]\n"
    "ds_read_b128 v[24:27], v9 offset:26624\n"
    "v_mfma_f32_16x16x32_bf16 v[56:59], a[108:111], v[28:31], v[56:59]\n"
    "s_add_u32 s73, s73, s55\n"
    "v_mfma_f32_16x16x32_bf16 v[60:63], a[112:115], v[28:31], v[60:63]\n"
    "s_waitcnt lgkmcnt(6)\n"
    "v_mfma_f32_16x16x32_bf16 v[68:71], a[116:119], v[28:31], v[68:71]\n"
    "ds_read_b128 v[28:31], v9 offset:27648\n"
    "v_mfma_f32_16x16x32_bf16 v[48:51], a[120:123], v[32:35], v[48:51]\n"
    "v_mfma_f32_16x16x32_bf16 v[52:55], a[124:127], v[32:35], v[52:55]\n"
    "s_waitcnt lgkmcnt(6)\n"
    "v_mfma_f32_16x16x32_bf16 v[64:67], a[128:131], v[32:35], v[64:67]\n"
    "ds_read_b128 v[32:35], v9 offset:28672\n"
    "v_mfma_f32_16x16x32_bf16 v[56:59], a[120:123], v[36:39], v[56:59]\n"
    "v_mfma_f32_16x16x32_bf16 v[60:63], a[124:127], v[36:39], v[60:63]\n"
    "s_waitcnt lgkmcnt(6)\n"
    "v_mfma_f32_16x16x32_bf16 v[68:71], a[128:131], v[36:39], v[68:71]\n"
    "ds_read_b128 v[36:39], v9 offset:29696\n"
    "v_mfma_f32_16x16x32_bf16 v[48:51], a[132:135], v[40:43], v[48:51]\n"
    "v_mfma_f32_16x16x32_bf16 v[52:55], a[136:139], v[40:43], v[52:55]\n"
    "s_waitcnt lgkmcnt(6)\n"
    "v_mfma_f32_16x16x32_bf16 v[64:67], a[140:143], v[40:43], v[64:67]\n"
    "ds_read_b128 v[40:43], v9 offset:30720\n"
    "v_mfma_f32_16x16x32_bf16 v[56:59], a[132:135], v[44:47], v[56:59]\n"
    "v_mfma_f32_16x16x32_bf16 v[60:63], a[136:139], v[44:47], v[60:63]\n"
    "s_waitcnt lgkmcnt(6)\n"
    "v_mfma_f32_16x16x32_bf16 v[68:71], a[140:143], v[44:47], v[68:71]\n"
    "ds_read_b128 v[44:47], v9 offset:31744\n"
    "v_mfma_f32_16x16x32_bf16 v[48:51], a[144:147], v[16:19], v[48:51]\n"
    "v_mfma_f32_16x16x32_bf16 v[52:55], a[148:151], v[16:19], v[52:55]\n"
    "s_waitcnt lgkmcnt(6)\n"
    "v_mfma_f32_16x16x32_bf16 v[64:67], a[152:155], v[16:19], v[64:67]\n"
    "ds_read_b128 v[16:19], v9 offset:32768\n"
    "v_mfma_f32_16x16x32_bf16 v[56:59], a[144:147], v[20:23], v[56:59]\n"
    "v_mfma_f32_16x16x32_bf16 v[60:63], a[148:151], v[20:23], v[60:63]\n"
    "s_waitcnt lgkmcnt(6)\n"
    "v_mfma_f32_16x16x32_bf16 v[68:71], a[152:155], v[20:23], v[68:71]\n"
    "ds_read_b128 v[20:23], v9 offset:33792\n"
    "v_mfma_f32_16x16x32_bf16 v[48:51], a[156:159], v[24:27], v[48:51]\n"
    "v_mfma_f32_16x16x32_bf16 v[52:55], a[160:163], v[24:27], v[52:55]\n"
    "s_waitcnt lgkmcnt(6)\n"
    "v_mfma_f32_16x16x32_bf16 v[64:67], a[164:167], v[24:27], v[64:67]\n"
    "ds_read_b128 v[24:27], v9 offset:34816\n"
    "v_mfma_f32_16x16x32_bf16 v[56:59], a[156:159], v[28:31], v[56:59]\n"
    "v_mfma_f32_16x16x32_bf16 v[60:63], a[160:163], v[28:31], v[60:63]\n"
    "s_waitcnt lgkmcnt(6)\n"
    "v_mfma_f32_16x16x32_bf16 v[68:71], a[164:167], v[28:31], v[68:71]\n"
    "ds_read_b128 v[28:31], v9 offset:35840\n"
    "v_mfma_f32_16x16x32_bf16 v[48:51], a[168:171], v[32:35], v[48:51]\n"
    "v_mfma_f32_16x16x32_bf16 v[52:55], a[172:175], v[32:35], v[52:55]\n"
    "s_waitcnt lgkmcnt(6)\n"
    "v_mfma_f32_16x16x32_bf16 v[64:67], a[176:179], v[32:35], v[64:67]\n"
    "ds_read_b128 v[32:35], v9 offset:36864\n"
    "v_mfma_f32_16x16x32_bf16 v[56:59], a[168:171], v[36:39], v[56:59]\n"
    "v_mfma_f32_16x16x32_bf16 v[60:63], a[172:175], v[36:39], v[60:63]\n"
    "s_waitcnt lgkmcnt(6)\n"
    "v_mfma_f32_16x16x32_bf16 v[68:71], a[176:179], v[36:39], v[68:71]\n"
    "ds_read_b128 v[36:39], v9 offset:37888\n"
    "v_mfma_f32_16x16x32_bf16 v[48:51], a[180:183], v[40:43], v[48:51]\n"
    "v_mfma_f32_16x16x32_bf16 v[52:55], a[184:187], v[40:43], v[52:55]\n"
    "s_waitcnt lgkmcnt(6)\n"
    "v_mfma_f32_16x16x32_bf16 v[64:67], a[188:191], v[40:43], v[64:67]\n"
    "ds_read_b128 v[40:43], v9 offset:38912\n"
    "v_mfma_f32_16x16x32_bf16 v[56:59], a[180:183], v[44:47], v[56:59]\n"
    "v_mfma_f32_16x16x32_bf16 v[60:63], a[184:187], v[44:47], v[60:63]\n"
    "s_waitcnt lgkmcnt(6)\n"
    "v_mfma_f32_16x16x32_bf16 v[68:71], a[188:191], v[44:47], v[68:71]\n"
    "ds_read_b128 v[44:47], v9 offset:39936\n"
    "v_mfma_f32_16x16x32_bf16 v[48:51], a[192:195], v[16:19], v[48:51]\n"
    "s_branch .LgDOWN_seg0_%=\n"
    ".LgDOWN_chk1_%=:\n"
    "s_waitcnt vmcnt(0)\n"
    "v_add_u32 v13, v11, v3\n"
    "v_cmp_gt_i32 vcc, s71, v13\n"
    "s_cbranch_vccz .LgDOWN_dma1_%=\n"
    "s_or_b64 exec, vcc, 1\n"
    "buffer_atomic_smax v11, v2, s[36:39], 0 offen sc0 sc1\n"
    "s_mov_b64 exec, -1\n"
    "s_branch .LgDOWN_plain1_%=\n"
    ".LgDOWN_dma1_%=:\n"
    "s_mov_b32 s70, 1\n"
    "v_mfma_f32_16x16x32_bf16 v[52:55], a[196:199], v[16:19], v[52:55]\n"
    "s_waitcnt lgkmcnt(6)\n"
    "s_add_u32 m0, s77, s59\n"
    "s_add_u32 s77, s77, 8192\n"
    "buffer_load_dwordx4 v1, s[36:39], s76 offen sc1 lds\n"
    "v_mfma_f32_16x16x32_bf16 v[64:67], a[200:203], v[16:19], v[64:67]\n"
    "ds_read_b128 v[16:19], v9 offset:40960\n"
    "v_mfma_f32_16x16x32_bf16 v[56:59], a[192:195], v[20:23], v[56:59]\n"
    "buffer_load_dwordx4 v1, s[36:39], s76 offen offset:1024 sc1 lds\n"
    "s_add_u32 s76, s76, 16384\n"
    "v_mfma_f32_16x16x32_bf16 v[60:63], a[196:199], v[20:23], v[60:63]\n"
    "s_waitcnt lgkmcnt(6)\n"
    "v_mfma_f32_16x16x32_bf16 v[68:71], a[200:203], v[20:23], v[68:71]\n"
    "ds_read_b128 v[20:23], v9 offset:41984\n"
    "v_mfma_f32_16x16x32_bf16 v[48:51], a[204:207], v[24:27], v[48:51]\n"
    "s_add_u32 m0, s77, s59\n"
    "s_add_u32 s77, s77, 8192\n"
    "buffer_load_dwordx4 v1, s[36:39], s76 offen sc1 lds\n"
    "v_mfma_f32_16x16x32_bf16 v[52:55], a[208:211], v[24:27], v[52:55]\n"
    "s_waitcnt lgkmcnt(6)\n"
    "buffer_load_dwordx4 v1, s[36:39], s76 offen offset:1024 sc1 lds\n"
    "s_add_u32 s76, s76, 16384\n"
    "v_mfma_f32_16x16x32_bf16 v[64:67], a[212:215], v[24:27], v[64:67]\n"
    "ds_read_b128 v[24:27], v9 offset:43008\n"
    "v_mfma_f32_16x16x32_bf16 v[56:59], a[204:207], v[28:31], v[56:59]\n"
    "v_mfma_f32_16x16x32_bf16 v[60:63], a[208:211], v[28:31], v[60:63]\n"
    "s_waitcnt lgkmcnt(6)\n"
    "s_add_u32 m0, s77, s59\n"
    "s_add_u32 s77, s77, 8192\n"
    "buffer_load_dwordx4 v1, s[36:39], s76 offen sc1 lds\n"
    "v_mfma_f32_16x16x32_bf16 v[68:71], a[212:215], v[28:31], v[68:71]\n"
    "ds_read_b128 v[28:31], v9 offset:44032\n"
    "v_mfma_f32_16x16x32_bf16 v[48:51], a[216:219], v[32:35], v[48:51]\n"
    "buffer_load_dwordx4 v1, s[36:39], s76 offen offset:1024 sc1 lds\n"
    "s_add_u32 s76, s76, 16384\n"
    "v_mfma_f32_16x16x32_bf16 v[52:55], a[220:223], v[32:35], v[52:55]\n"
    "s_waitcnt lgkmcnt(6)\n"
    "v_mfma_f32_16x16x32_bf16 v[64:67], a[224:227], v[32:35], v[64:67]\n"
    "ds_read_b128 v[32:35], v9 offset:45056\n"
    "v_mfma_f32_16x16x32_bf16 v[56:59], a[216:219], v[36:39], v[56:59]\n"
    "s_add_u32 m0, s77, s59\n"
    "s_add_u32 s77, s77, 8192\n"
    "buffer_load_dwordx4 v1, s[36:39], s76 offen sc1 lds\n"
    "v_mfma_f32_16x16x32_bf16 v[60:63], a[220:223], v[36:39], v[60:63]\n"
    "s_waitcnt lgkmcnt(6)\n"
    "buffer_load_dwordx4 v1, s[36:39], s76 offen offset:1024 sc1 lds\n"
    "s_add_u32 s76, s76, 16384\n"
    "v_mfma_f32_16x16x32_bf16 v[68:71], a[224:227], v[36:39], v[68:71]\n"
    "ds_read_b128 v[36:39], v9 offset:46080\n"
    "v_mfma_f32_16x16x32_bf16 v[48:51], a[228:231], v[40:43], v[48:51]\n"
    "v_mfma_f32_16x16x32_bf16 v[52:55], a[232:235], v[40:43], v[52:55]\n"
    "s_waitcnt lgkmcnt(6)\n"
    "s_add_u32 m0, s77, s59\n"
    "s_add_u32 s77, s77, 8192\n"
    "buffer_load_dwordx4 v1, s[36:39], s76 offen sc1 lds\n"
    "v_mfma_f32_16x16x32_bf16 v[64:67], a[236:239], v[40:43], v[64:67]\n"
    "ds_read_b128 v[40:43], v9 offset:47104\n"
    "v_mfma_f32_16x16x32_bf16 v[56:59], a[228:231], v[44:47], v[56:59]\n"
    "v_mfma_f32_16x16x32_bf16 v[60:63], a[232:235], v[44:47], v[60:63]\n"
    "s_waitcnt lgkmcnt(6)\n"
    "buffer_load_dwordx4 v1, s[36:39], s76 offen offset:1024 sc1 lds\n"
    "s_add_u32 s76, s76, 16384\n"
    "v_mfma_f32_16x16x32_bf16 v[68:71], a[236:239], v[44:47], v[68:71]\n"
    "ds_read_b128 v[44:47], v9 offset:48128\n"
    "v_mfma_f32_16x16x32_bf16 v[48:51], a[240:243], v[16:19], v[48:51]\n"
    "s_add_u32 m0, s77, s59\n"
    "s_add_u32 s77, s77, 8192\n"
    "buffer_load_dwordx4 v1, s[36:39], s76 offen sc1 lds\n"
    "v_mfma_f32_16x16x32_bf16 v[52:55], a[244:247], v[16:19], v[52:55]\n"
    "s_waitcnt lgkmcnt(6)\n"
    "v_mfma_f32_16x16x32_bf16 v[64:67], a[248:251], v[16:19], v[64:67]\n"
    "ds_read_b128 v[16:19], v10 offset:0\n"
    "v_mfma_f32_16x16x32_bf16 v[56:59], a[240:243], v[20:23], v[56:59]\n"
    "buffer_load_dwordx4 v1, s[36:39], s76 offen offset:1024 sc1 lds\n"
    "s_add_u32 s76, s76, 16384\n"
    "v_mfma_f32_16x16x32_bf16 v[60:63], a[244:247], v[20:23], v[60:63]\n"
    "s_waitcnt lgkmcnt(6)\n"
    "s_add_u32 m0, s77, s59\n"
    "s_add_u32 s77, s77, 8192\n"
    "buffer_load_dwordx4 v1, s[36:39], s76 offen sc1 lds\n"
    "v_mfma_f32_16x16x32_bf16 v[68:71], a[248:251], v[20:23], v[68:71]\n"
    "ds_read_b128 v[20:23], v10 offset:1024\n"
    "v_mfma_f32_16x16x32_bf16 v[48:51], a[252:255], v[24:27], v[48:51]\n"
    "v_mfma_f32_16x16x32_bf16 v[52:55], v[80:83], v[24:27], v[52:55]\n"
    "s_waitcnt lgkmcnt(6)\n"
    "buffer_load_dwordx4 v1, s[36:39], s76 offen offset:1024 sc1 lds\n"
    "s_add_u32 s76, s76, 16384\n"
    "v_mfma_f32_16x16x32_bf16 v[64:67], v[84:87], v[24:27], v[64:67]\n"
    "ds_read_b128 v[24:27], v10 offset:2048\n"
    "v_mfma_f32_16x16x32_bf16 v[56:59], a[252:255], v[28:31], v[56:59]\n"
    "s_add_u32 m0, s77, s59\n"
    "s_add_u32 s77, s77, 8192\n"
    "buffer_load_dwordx4 v1, s[36:39], s76 offen sc1 lds\n"
    "v_mfma_f32_16x16x32_bf16 v[60:63], v[80:83], v[28:31], v[60:63]\n"
    "s_waitcnt lgkmcnt(6)\n"
    "v_mfma_f32_16x16x32_bf16 v[68:71], v[84:87], v[28:31], v[68:71]\n"
    "ds_read_b128 v[28:31], v10 offset:3072\n"
    "v_mfma_f32_16x16x32_bf16 v[48:51], v[88:91], v[32:35], v[48:51]\n"
    "buffer_load_dwordx4 v1, s[36:39], s76 offen offset:1024 sc1 lds\n"
    "s_add_u32 s76, s76, 16384\n"
    "v_mfma_f32_16x16x32_bf16 v[52:55], v[92:95], v[32:35], v[52:55]\n"
    "s_waitcnt lgkmcnt(6)\n"
    "s_add_u32 m0, s77, s59\n"
    "s_add_u32 s77, s77, 8192\n"
    "buffer_load_dwordx4 v1, s[36:39], s76 offen sc1 lds\n"
    "v_mfma_f32_16x16x32_bf16 v[64:67], v[96:99], v[32:35], v[64:67]\n"
    "ds_read_b128 v[32:35], v10 offset:4096\n"
    "v_mfma_f32_16x16x32_bf16 v[56:59], v[88:91], v[36:39], v[56:59]\n"
    "v_mfma_f32_16x16x32_bf16 v[60:63], v[92:95], v[36:39], v[60:63]\n"
    "s_waitcnt lgkmcnt(6)\n"
    "buffer_load_dwordx4 v1, s[36:39], s76 offen offset:1024 sc1 lds\n"
    "s_add_u32 s76, s76, 16384\n"
    "v_mfma_f32_16x16x32_bf16 v[68:71], v[96:99], v[36:39], v[68:71]\n"
    "ds_read_b128 v[36:39], v10 offset:5120\n"
    "v_mfma_f32_16x16x32_bf16 v[48:51], v[100:103], v[40:43], v[48:51]\n"
    "v_mfma_f32_16x16x32_bf16 v[52:55], v[104:107], v[40:43], v[52:55]\n"
    "s_waitcnt lgkmcnt(6)\n"
    "s_branch .LgDOWN_seg1_%=\n"
    ".LgDOWN_chk2_%=:\n"
    "s_waitcnt vmcnt(0)\n"
    "v_add_u32 v13, v11, v3\n"
    "v_cmp_gt_i32 vcc, s71, v13\n"
    "s_cbranch_vccz .LgDOWN_dma2_%=\n"
    "s_or_b64 exec, vcc, 1\n"
    "buffer_atomic_smax v11, v2, s[36:39], 0 offen sc0 sc1\n"
    "s_mov_b64 exec, -1\n"
    "s_branch .LgDOWN_plain2_%=\n"
    ".LgDOWN_dma2_%=:\n"
    "s_mov_b32 s70, 2\n"
    "v_mfma_f32_16x16x32_bf16 v[64:67], v[108:111], v[40:43], v[64:67]\n"
    "ds_read_b128 v[40:43], v10 offset:6144\n"
    "v_mfma_f32_16x16x32_bf16 v[56:59], v[100:103], v[44:47], v[56:59]\n"
    "s_add_u32 m0, s77, s59\n"
    "s_add_u32 s77, s77, 8192\n"
    "buffer_load_dwordx4 v1, s[36:39], s76 offen sc1 lds\n"
    "v_mfma_f32_16x16x32_bf16 v[60:63], v[104:107], v[44:47], v[60:63]\n"
    "s_waitcnt lgkmcnt(6)\n"
    "buffer_load_dwordx4 v1, s[36:39], s76 offen offset:1024 sc1 lds\n"
    "s_add_u32 s76, s76, 16384\n"
    "v_mfma_f32_16x16x32_bf16 v[68:71], v[108:111], v[44:47], v[68:71]\n"
    "ds_read_b128 v[44:47], v10 offset:7168\n"
    "v_mfma_f32_16x16x32_bf16 v[48:51], v[112:115], v[16:19], v[48:51]\n"
    "v_mfma_f32_16x16x32_bf16 v[52:55], v[116:119], v[16:19], v[52:55]\n"
    "s_waitcnt lgkmcnt(6)\n"
    "s_add_u32 m0, s77, s59\n"
    "s_add_u32 s77, s77, 8192\n"
    "buffer_load_dwordx4 v1, s[36:39], s76 offen sc1 lds\n"
    "v_mfma_f32_16x16x32_bf16 v[64:67], v[120:123], v[16:19], v[64:67]\n"
    "ds_read_b128 v[16:19], v10 offset:8192\n"
    "v_mfma_f32_16x16x32_bf16 v[56:59], v[112:115], v[20:23], v[56:59]\n"
    "buffer_load_dwordx4 v1, s[36:39], s76 offen offset:1024 sc1 lds\n"
    "s_add_u32 s76, s76, 16384\n"
    "v_mfma_f32_16x16x32_bf16 v[60:63], v[116:119], v[20:23], v[60:63]\n"
    "s_waitcnt lgkmcnt(6)\n"
    "v_mfma_f32_16x16x32_bf16 v[68:71], v[120:123], v[20:23], v[68:71]\n"
    "ds_read_b128 v[20:23], v10 offset:9216\n"
    "v_mfma_f32_16x16x32_bf16 v[48:51], v[124:127], v[24:27], v[48:51]\n"
    "s_add_u32 m0, s77, s59\n"
    "s_add_u32 s77, s77, 8192\n"
    "buffer_load_dwordx4 v1, s[36:39], s76 offen sc1 lds\n"
    "v_mfma_f32_16x16x32_bf16 v[52:55], v[128:131], v[24:27], v[52:55]\n"
    "s_waitcnt lgkmcnt(6)\n"
    "buffer_load_dwordx4 v1, s[36:39], s76 offen offset:1024 sc1 lds\n"
    "s_add_u32 s76, s76, 16384\n"
    "v_mfma_f32_16x16x32_bf16 v[64:67], v[132:135], v[24:27], v[64:67]\n"
    "ds_read_b128 v[24:27], v10 offset:10240\n"
    "v_mfma_f32_16x16x32_bf16 v[56:59], v[124:127], v[28:31], v[56:59]\n"
    "v_mfma_f32_16x16x32_bf16 v[60:63], v[128:131], v[28:31], v[60:63]\n"
    "s_waitcnt lgkmcnt(6)\n"
    "s_add_u32 m0, s77, s59\n"
    "s_add_u32 s77, s77, 8192\n"
    "buffer_load_dwordx4 v1, s[36:39], s76 offen sc1 lds\n"
    "v_mfma_f32_16x16x32_bf16 v[68:71], v[132:135], v[28:31], v[68:71]\n"
    "ds_read_b128 v[28:31], v10 offset:11264\n"
    "v_mfma_f32_16x16x32_bf16 v[48:51], v[136:139], v[32:35], v[48:51]\n"
    "buffer_load_dwordx4 v1, s[36:39], s76 offen offset:1024 sc1 lds\n"
    "s_add_u32 s76, s76, 16384\n"
    "v_mfma_f32_16x16x32_bf16 v[52:55], v[140:143], v[32:35], v[52:55]\n"
    "s_waitcnt lgkmcnt(6)\n"
    "v_mfma_f32_16x16x32_bf16 v[64:67], v[144:147], v[32:35], v[64:67]\n"
    "ds_read_b128 v[32:35], v10 offset:12288\n"
    "v_mfma_f32_16x16x32_bf16 v[56:59], v[136:139], v[36:39], v[56:59]\n"
    "s_add_u32 m0, s77, s59\n"
    "s_add_u32 s77, s77, 8192\n"
    "buffer_load_dwordx4 v1, s[36:39], s76 offen sc1 lds\n"
    "v_mfma_f32_16x16x32_bf16 v[60:63], v[140:143], v[36:39], v[60:63]\n"
    "s_waitcnt lgkmcnt(6)\n"
    "v_mfma_f32_16x16x32_bf16 v[68:71], v[144:147], v[36:39], v[68:71]\n"
    "ds_read_b128 v[36:39], v10 offset:13312\n"
    "v_mfma_f32_16x16x32_bf16 v[48:51], v[148:151], v[40:43], v[48:51]\n"
    "buffer_load_dwordx4 v1, s[36:39], s76 offen offset:1024 sc1 lds\n"
    "s_add_u32 s76, s76, 16384\n"
    "v_mfma_f32_16x16x32_bf16 v[52:55], v[152:155], v[40:43], v[52:55]\n"
    "s_waitcnt lgkmcnt(6)\n"
    "s_add_u32 m0, s77, s59\n"
    "s_add_u32 s77, s77, 8192\n"
    "buffer_load_dwordx4 v1, s[36:39], s76 offen sc1 lds\n"
    "v_mfma_f32_16x16x32_bf16 v[64:67], v[156:159], v[40:43], v[64:67]\n"
    "ds_read_b128 v[40:43], v10 offset:14336\n"
    "v_mfma_f32_16x16x32_bf16 v[56:59], v[148:151], v[44:47], v[56:59]\n"
    "v_mfma_f32_16x16x32_bf16 v[60:63], v[152:155], v[44:47], v[60:63]\n"
    "s_waitcnt lgkmcnt(6)\n"
    "buffer_load_dwordx4 v1, s[36:39], s76 offen offset:1024 sc1 lds\n"
    "s_add_u32 s76, s76, 16384\n"
    "v_mfma_f32_16x16x32_bf16 v[68:71], v[156:159], v[44:47], v[68:71]\n"
    "ds_read_b128 v[44:47], v10 offset:15360\n"
    "v_mfma_f32_16x16x32_bf16 v[48:51], v[160:163], v[16:19], v[48:51]\n"
    "s_add_u32 m0, s77, s59\n"
    "s_add_u32 s77, s77, 8192\n"
    "buffer_load_dwordx4 v1, s[36:39], s76 offen sc1 lds\n"
    "v_mfma_f32_16x16x32_bf16 v[52:55], v[164:167], v[16:19], v[52:55]\n"
    "s_waitcnt lgkmcnt(6)\n"
    "v_mfma_f32_16x16x32_bf16 v[64:67], v[168:171], v[16:19], v[64:67]\n"
    "ds_read_b128 v[16:19], v10 offset:16384\n"
    "v_mfma_f32_16x16x32_bf16 v[56:59], v[160:163], v[20:23], v[56:59]\n"
    "buffer_load_dwordx4 v1, s[36:39], s76 offen offset:1024 sc1 lds\n"
    "s_add_u32 s76, s76, 16384\n"
    "v_mfma_f32_16x16x32_bf16 v[60:63], v[164:167], v[20:23], v[60:63]\n"
    "s_waitcnt lgkmcnt(6)\n"
    "s_add_u32 m0, s77, s59\n"
    "s_add_u32 s77, s77, 8192\n"
    "buffer_load_dwordx4 v1, s[36:39], s76 offen sc1 lds\n"
    "v_mfma_f32_16x16x32_bf16 v[68:71], v[168:171], v[20:23], v[68:71]\n"
    "ds_read_b128 v[20:23], v10 offset:17408\n"
    "v_mfma_f32_16x16x32_bf16 v[48:51], v[172:175], v[24:27], v[48:51]\n"
    "v_mfma_f32_16x16x32_bf16 v[52:55], v[176:179], v[24:27], v[52:55]\n"
    "s_waitcnt lgkmcnt(6)\n"
    "buffer_load_dwordx4 v1, s[36:39], s76 offen offset:1024 sc1 lds\n"
    "s_add_u32 s76, s76, 16384\n"
    "v_mfma_f32_16x16x32_bf16 v[64:67], v[180:183], v[24:27], v[64:67]\n"
    "ds_read_b128 v[24:27], v10 offset:18432\n"
    "v_mfma_f32_16x16x32_bf16 v[56:59], v[172:175], v[28:31], v[56:59]\n"
    "s_add_u32 m0, s77, s59\n"
    "s_add_u32 s77, s77, 8192\n"
    "buffer_load_dwordx4 v1, s[36:39], s76 offen sc1 lds\n"
    "v_mfma_f32_16x16x32_bf16 v[60:63], v[176:179], v[28:31], v[60:63]\n"
    "s_waitcnt lgkmcnt(6)\n"
    "v_mfma_f32_16x16x32_bf16 v[68:71], v[180:183], v[28:31], v[68:71]\n"
    "ds_read_b128 v[28:31], v10 offset:19456\n"
    "v_mfma_f32_16x16x32_bf16 v[48:51], v[184:187], v[32:35], v[48:51]\n"
    "buffer_load_dwordx4 v1, s[36:39], s76 offen offset:1024 sc1 lds\n"
    "s_add_u32 s76, s76, 16384\n"
    "v_mfma_f32_16x16x32_bf16 v[52:55], v[188:191], v[32:35], v[52:55]\n"
    "s_waitcnt lgkmcnt(6)\n"
    "v_mfma_f32_16x16x32_bf16 v[64:67], v[192:195], v[32:35], v[64:67]\n"
    "ds_read_b128 v[32:35], v10 offset:20480\n"
    "v_mfma_f32_16x16x32_bf16 v[56:59], v[184:187], v[36:39], v[56:59]\n"
    "s_branch .LgDOWN_seg2_%=\n"
    ".LgDOWN_slow_%=:\n"
    "s_cmp_lg_u32 s70, 0\n"
    "s_cbranch_scc1 .LgDOWN_nolast_%=\n"
    "s_waitcnt vmcnt(0)\n"
    "v_add_u32 v13, v11, v3\n"
    "v_cmp_gt_i32 vcc, s71, v13\n"
    "s_nop 0\n"
    "s_or_b64 exec, vcc, 1\n"
    "buffer_atomic_smax v11, v2, s[36:39], 0 offen sc0 sc1\n"
    "s_mov_b64 exec, -1\n"
    ".LgDOWN_nolast_%=:\n"
    "v_mfma_f32_16x16x32_bf16 v[60:63], v[188:191], v[36:39], v[60:63]\n"
    "s_waitcnt lgkmcnt(6)\n"
    "v_mfma_f32_16x16x32_bf16 v[68:71], v[192:195], v[36:39], v[68:71]\n"
    "ds_read_b128 v[36:39], v10 offset:21504\n"
    "v_mfma_f32_16x16x32_bf16 v[48:51], v[196:199], v[40:43], v[48:51]\n"
    "v_mfma_f32_16x16x32_bf16 v[52:55], v[200:203], v[40:43], v[52:55]\n"
    "s_waitcnt lgkmcnt(6)\n"
    "v_mfma_f32_16x16x32_bf16 v[64:67], v[204:207], v[40:43], v[64:67]\n"
    "ds_read_b128 v[40:43], v10 offset:22528\n"
    "v_mfma_f32_16x16x32_bf16 v[56:59], v[196:199], v[44:47], v[56:59]\n"
    "v_mfma_f32_16x16x32_bf16 v[60:63], v[200:203], v[44:47], v[60:63]\n"
    "s_waitcnt lgkmcnt(6)\n"
    "v_mfma_f32_16x16x32_bf16 v[68:71], v[204:207], v[44:47], v[68:71]\n"
    "ds_read_b128 v[44:47], v10 offset:23552\n"
    "v_mfma_f32_16x16x32_bf16 v[64:67], v[216:219], v[16:19], v[64:67]\n"
    "s_waitcnt lgkmcnt(6)\n"
    "ds_read_b128 v[16:19], v10 offset:16384\n"
    "v_mfma_f32_16x16x32_bf16 v[68:71], v[216:219], v[20:23], v[68:71]\n"
    "s_waitcnt lgkmcnt(6)\n"
    "ds_read_b128 v[20:23], v10 offset:18432\n"
    "v_mfma_f32_16x16x32_bf16 v[64:67], v[228:231], v[24:27], v[64:67]\n"
    "s_waitcnt lgkmcnt(6)\n"
    "ds_read_b128 v[24:27], v10 offset:20480\n"
    "v_mfma_f32_16x16x32_bf16 v[68:71], v[228:231], v[28:31], v[68:71]\n"
    "s_waitcnt lgkmcnt(6)\n"
    "ds_read_b128 v[28:31], v10 offset:22528\n"
    "v_mfma_f32_16x16x32_bf16 v[64:67], v[240:243], v[32:35], v[64:67]\n"
    "s_waitcnt lgkmcnt(6)\n"
    "ds_read_b128 v[32:35], v10 offset:17408\n"
    "v_mfma_f32_16x16x32_bf16 v[68:71], v[240:243], v[36:39], v[68:71]\n"
    "s_waitcnt lgkmcnt(6)\n"
    "ds_read_b128 v[36:39], v10 offset:19456\n"
    "v_mfma_f32_16x16x32_bf16 v[64:67], v[252:255], v[40:43], v[64:67]\n"
    "s_waitcnt lgkmcnt(6)\n"
    "ds_read_b128 v[40:43], v10 offset:21504\n"
    "v_mfma_f32_16x16x32_bf16 v[68:71], v[252:255], v[44:47], v[68:71]\n"
    "s_waitcnt lgkmcnt(6)\n"
    "ds_read_b128 v[44:47], v10 offset:23552\n"
    "v_mfma_f32_16x16x32_bf16 v[48:51], v[208:211], v[16:19], v[48:51]\n"
    "v_mfma_f32_16x16x32_bf16 v[52:55], v[212:215], v[16:19], v[52:55]\n"
    "s_waitcnt lgkmcnt(6)\n"
    "v_cvt_pk_bf16_f32 v64, v64, v65\n"
    "v_cvt_pk_bf16_f32 v65, v66, v67\n"
    "v_mfma_f32_16x16x32_bf16 v[48:51], v[220:223], v[20:23], v[48:51]\n"
    "s_waitcnt lgkmcnt(5)\n"
    "v_cvt_pk_bf16_f32 v66, v68, v69\n"
    "v_cvt_pk_bf16_f32 v67, v70, v71\n"
    "v_mfma_f32_16x16x32_bf16 v[52:55], v[224:227], v[20:23], v[52:55]\n"
    "s_nop 0\n"
    "v_mfma_f32_16x16x32_bf16 v[48:51], v[232:235], v[24:27], v[48:51]\n"
    "v_mfma_f32_16x16x32_bf16 v[52:55], v[236:239], v[24:27], v[52:55]\n"
    "s_waitcnt lgkmcnt(4)\n"
    "v_mfma_f32_16x16x32_bf16 v[48:51], v[244:247], v[28:31], v[48:51]\n"
    "s_waitcnt lgkmcnt(3)\n"
    "v_permlane16_swap_b32 v64, v66\n"
    "v_mfma_f32_16x16x32_bf16 v[52:55], v[248:251], v[28:31], v[52:55]\n"
    "v_mfma_f32_16x16x32_bf16 v[56:59], v[208:211], v[32:35], v[56:59]\n"
    "s_waitcnt lgkmcnt(2)\n"
    "v_permlane16_swap_b32 v65, v67\n"
    "v_mfma_f32_16x16x32_bf16 v[60:63], v[212:215], v[32:35], v[60:63]\n"
    "v_cvt_pk_bf16_f32 v48, v48, v49\n"
    "v_mfma_f32_16x16x32_bf16 v[56:59], v[220:223], v[36:39], v[56:59]\n"
    "s_waitcnt lgkmcnt(1)\n"
    "buffer_store_dwordx4 v[64:67], v4, s[36:39], s73 offen sc1\n"
    "v_cvt_pk_bf16_f32 v49, v50, v51\n"
    "v_cvt_pk_bf16_f32 v50, v52, v53\n"
    "v_mfma_f32_16x16x32_bf16 v[60:63], v[224:227], v[36:39], v[60:63]\n"
    "v_cvt_pk_bf16_f32 v51, v54, v55\n"
    "v_mfma_f32_16x16x32_bf16 v[56:59], v[232:235], v[40:43], v[56:59]\n"
    "s_waitcnt lgkmcnt(0)\n"
    "buffer_store_dwordx4 v[48:51], v15, s[36:39], s73 offen sc1\n"
    "v_mfma_f32_16x16x32_bf16 v[60:63], v[236:239], v[40:43], v[60:63]\n"
    "v_mfma_f32_16x16x32_bf16 v[56:59], v[244:247], v[44:47], v[56:59]\n"
    "v_mfma_f32_16x16x32_bf16 v[60:63], v[248:251], v[44:47], v[60:63]\n"
    "s_nop 3\n"
    "v_cvt_pk_bf16_f32 v56, v56, v57\n"
    "v_cvt_pk_bf16_f32 v57, v58, v59\n"
    "s_nop 1\n"
    "v_cvt_pk_bf16_f32 v58, v60, v61\n"
    "v_cvt_pk_bf16_f32 v59, v62, v63\n"
    "buffer_store_dwordx4 v[56:59], v15, s[36:39], s73 offen offset:1024 sc1\n"
    "s_mov_b32 s68, s71\n"
    "s_cmp_eq_u32 s70, 0\n"
    "s_cbranch_scc1 .LgDOWN_idle_%=\n"
    "s_cmp_eq_u32 s68, 64\n"
    "s_cbranch_scc1 .LgDOWN_done_%=\n"
    "s_waitcnt vmcnt(3)\n"
    ".LgDOWN_bar_%=:\n"
    "v_xor_b32 v9, s78, v9\n"
    "s_add_u32 s71, s68, 1\n"
    "s_cmp_eq_u32 s71, 64\n"
    "s_cselect_b32 s70, 2, 0\n"
    "s_barrier\n"
    "ds_read_b128 v[16:19], v9 offset:0\n"
    "ds_read_b128 v[20:23], v9 offset:1024\n"
    "ds_read_b128 v[24:27], v9 offset:2048\n"
    "ds_read_b128 v[28:31], v9 offset:3072\n"
    "ds_read_b128 v[32:35], v9 offset:4096\n"
    "ds_read_b128 v[36:39], v9 offset:5120\n"
    "ds_read_b128 v[40:43], v9 offset:6144\n"
    "ds_read_b128 v[44:47], v9 offset:7168\n"
    "v_add_u32 v13, v11, v3\n"
    "v_cmp_gt_i32 vcc, s71, v13\n"
    "s_nop 0\n"
    "s_or_b64 exec, vcc, 1\n"
    "buffer_atomic_smax v11, v2, s[36:39], 0 offen sc0 sc1\n"
    "s_mov_b64 exec, -1\n"
    "s_waitcnt lgkmcnt(7)\n"
    "v_mfma_f32_16x16x32_bf16 v[64:67], a[8:11], v[16:19], 0\n"
    "s_waitcnt lgkmcnt(6)\n"
    "ds_read_b128 v[16:19], v9 offset:0\n"
    "v_mfma_f32_16x16x32_bf16 v[68:71], a[8:11], v[20:23], 0\n"
    "s_waitcnt lgkmcnt(6)\n"
    "ds_read_b128 v[20:23], v9 offset:2048\n"
    "v_mfma_f32_16x16x32_bf16 v[64:67], a[20:23], v[24:27], v[64:67]\n"
    "s_waitcnt lgkmcnt(6)\n"
    "ds_read_b128 v[24:27], v9 offset:4096\n"
    "v_mfma_f32_16x16x32_bf16 v[68:71], a[20:23], v[28:31], v[68:71]\n"
    "s_waitcnt lgkmcnt(6)\n"
    "ds_read_b128 v[28:31], v9 offset:6144\n"
    "v_mfma_f32_16x16x32_bf16 v[64:67], a[32:35], v[32:35], v[64:67]\n"
    "s_waitcnt lgkmcnt(6)\n"
    "ds_read_b128 v[32:35], v9 offset:1024\n"
    "v_mfma_f32_16x16x32_bf16 v[68:71], a[32:35], v[36:39], v[68:71]\n"
    "s_waitcnt lgkmcnt(6)\n"
    "ds_read_b128 v[36:39], v9 offset:3072\n"
    "v_mfma_f32_16x16x32_bf16 v[64:67], a[44:47], v[40:43], v[64:67]\n"
    "s_waitcnt lgkmcnt(6)\n"
    "ds_read_b128 v[40:43], v9 offset:5120\n"
    "v_mfma_f32_16x16x32_bf16 v[68:71], a[44:47], v[44:47], v[68:71]\n"
    "s_waitcnt lgkmcnt(6)\n"
    "ds_read_b128 v[44:47], v9 offset:7168\n"
    "v_mfma_f32_16x16x32_bf16 v[48:51], a[0:3], v[16:19], 0\n"
    "s_waitcnt lgkmcnt(6)\n"
    "s_sub_u32 s74, s68, 1\n"
    "v_mfma_f32_16x16x32_bf16 v[52:55], a[4:7], v[16:19], 0\n"
    "ds_read_b128 v[16:19], v9 offset:8192\n"
    "v_mfma_f32_16x16x32_bf16 v[48:51], a[12:15], v[20:23], v[48:51]\n"
    "s_waitcnt lgkmcnt(6)\n"
    "s_and_b32 s76, s71, s54\n"
    "v_mfma_f32_16x16x32_bf16 v[52:55], a[16:19], v[20:23], v[52:55]\n"
    "ds_read_b128 v[20:23], v9 offset:9216\n"
    "v_mfma_f32_16x16x32_bf16 v[48:51], a[24:27], v[24:27], v[48:51]\n"
    "s_waitcnt lgkmcnt(6)\n"
    "s_mul_i32 s76, s76, s53\n"
    "v_mfma_f32_16x16x32_bf16 v[52:55], a[28:31], v[24:27], v[52:55]\n"
    "ds_read_b128 v[24:27], v9 offset:10240\n"
    "v_mfma_f32_16x16x32_bf16 v[48:51], a[36:39], v[28:31], v[48:51]\n"
    "s_waitcnt lgkmcnt(6)\n"
    "s_add_u32 s76, s76, s52\n"
    "v_mfma_f32_16x16x32_bf16 v[52:55], a[40:43], v[28:31], v[52:55]\n"
    "ds_read_b128 v[28:31], v9 offset:11264\n"
    "v_mfma_f32_16x16x32_bf16 v[56:59], a[0:3], v[32:35], 0\n"
    "s_waitcnt lgkmcnt(6)\n"
    "s_and_b32 s77, s71, 1\n"
    "v_mfma_f32_16x16x32_bf16 v[60:63], a[4:7], v[32:35], 0\n"
    "ds_read_b128 v[32:35], v9 offset:12288\n"
    "v_mfma_f32_16x16x32_bf16 v[56:59], a[12:15], v[36:39], v[56:59]\n"
    "s_waitcnt lgkmcnt(6)\n"
    "s_mul_i32 s77, s77, 81920\n"
    "v_mfma_f32_16x16x32_bf16 v[60:63], a[16:19], v[36:39], v[60:63]\n"
    "ds_read_b128 v[36:39], v9 offset:13312\n"
    "v_mfma_f32_16x16x32_bf16 v[56:59], a[24:27], v[40:43], v[56:59]\n"
    "s_waitcnt lgkmcnt(6)\n"
    "s_add_u32 s77, s77, s59\n"
    "v_mfma_f32_16x16x32_bf16 v[60:63], a[28:31], v[40:43], v[60:63]\n"
    "ds_read_b128 v[40:43], v9 offset:14336\n"
    "v_mfma_f32_16x16x32_bf16 v[56:59], a[36:39], v[44:47], v[56:59]\n"
    "s_waitcnt lgkmcnt(6)\n"
    "v_mfma_f32_16x16x32_bf16 v[60:63], a[40:43], v[44:47], v[60:63]\n"
    "ds_read_b128 v[44:47], v9 offset:15360\n"
    "v_mfma_f32_16x16x32_bf16 v[48:51], a[48:51], v[16:19], v[48:51]\n"
    "v_mfma_f32_16x16x32_bf16 v[52:55], a[52:55], v[16:19], v[52:55]\n"
    "s_waitcnt lgkmcnt(6)\n"
    "v_mfma_f32_16x16x32_bf16 v[64:67], a[56:59], v[16:19], v[64:67]\n"
    "ds_read_b128 v[16:19], v9 offset:16384\n"
    "v_mfma_f32_16x16x32_bf16 v[56:59], a[48:51], v[20:23], v[56:59]\n"
    "v_mfma_f32_16x16x32_bf16 v[60:63], a[52:55], v[20:23], v[60:63]\n"
    "s_waitcnt lgkmcnt(6)\n"
    "v_mfma_f32_16x16x32_bf16 v[68:71], a[56:59], v[20:23], v[68:71]\n"
    "ds_read_b128 v[20:23], v9 offset:17408\n"
    "v_mfma_f32_16x16x32_bf16 v[48:51], a[60:63], v[24:27], v[48:51]\n"
    "v_mfma_f32_16x16x32_bf16 v[52:55], a[64:67], v[24:27], v[52:55]\n"
    "s_waitcnt lgkmcnt(6)\n"
    "v_mfma_f32_16x16x32_bf16 v[64:67], a[68:71], v[24:27], v[64:67]\n"
    "ds_read_b128 v[24:27], v9 offset:18432\n"
    "v_mfma_f32_16x16x32_bf16 v[56:59], a[60:63], v[28:31], v[56:59]\n"
    "v_mfma_f32_16x16x32_bf16 v[60:63], a[64:67], v[28:31], v[60:63]\n"
    "s_waitcnt lgkmcnt(6)\n"
    "v_mfma_f32_16x16x32_bf16 v[68:71], a[68:71], v[28:31], v[68:71]\n"
    "ds_read_b128 v[28:31], v9 offset:19456\n"
    "v_mfma_f32_16x16x32_bf16 v[48:51], a[72:75], v[32:35], v[48:51]\n"
    "v_mfma_f32_16x16x32_bf16 v[52:55], a[76:79], v[32:35], v[52:55]\n"
    "s_waitcnt lgkmcnt(6)\n"
    "v_mfma_f32_16x16x32_bf16 v[64:67], a[80:83], v[32:35], v[64:67]\n"
    "ds_read_b128 v[32:35], v9 offset:20480\n"
    "v_mfma_f32_16x16x32_bf16 v[56:59], a[72:75], v[36:39], v[56:59]\n"
    "v_mfma_f32_16x16x32_bf16 v[60:63], a[76:79], v[36:39], v[60:63]\n"
    "s_waitcnt lgkmcnt(6)\n"
    "v_mfma_f32_16x16x32_bf16 v[68:71], a[80:83], v[36:39], v[68:71]\n"
    "ds_read_b128 v[36:39], v9 offset:21504\n"
    "v_mfma_f32_16x16x32_bf16 v[48:51], a[84:87], v[40:43], v[48:51]\n"
    "v_mfma_f32_16x16x32_bf16 v[52:55], a[88:91], v[40:43], v[52:55]\n"
    "s_waitcnt lgkmcnt(6)\n"
    "v_mfma_f32_16x16x32_bf16 v[64:67], a[92:95], v[40:43], v[64:67]\n"
    "ds_read_b128 v[40:43], v9 offset:22528\n"
    "v_mfma_f32_16x16x32_bf16 v[56:59], a[84:87], v[44:47], v[56:59]\n"
    "v_mfma_f32_16x16x32_bf16 v[60:63], a[88:91], v[44:47], v[60:63]\n"
    "s_waitcnt lgkmcnt(6)\n"
    "v_mfma_f32_16x16x32_bf16 v[68:71], a[92:95], v[44:47], v[68:71]\n"
    "ds_read_b128 v[44:47], v9 offset:23552\n"
    "v_mfma_f32_16x16x32_bf16 v[48:51], a[96:99], v[16:19], v[48:51]\n"
    "v_mov_b32 v12, s74\n"
    "v_mfma_f32_16x16x32_bf16 v[52:55], a[100:103], v[16:19], v[52:55]\n"
    "s_waitcnt lgkmcnt(6)\n"
    "s_waitcnt vmcnt(0)\n"
    "v_add_u32 v13, v11, v3\n"
    "v_mfma_f32_16x16x32_bf16 v[64:67], a[104:107], v[16:19], v[64:67]\n"
    "ds_read_b128 v[16:19], v9 offset:24576\n"
    "buffer_store_dword v12, v14, s[36:39], 0 offen sc1\n"
    "v_cmp_gt_i32 vcc, s71, v13\n"
    "v_mfma_f32_16x16x32_bf16 v[56:59], a[96:99], v[20:23], v[56:59]\n"
    "v_add_u32 v10, 49152, v9\n"
    "v_mfma_f32_16x16x32_bf16 v[60:63], a[100:103], v[20:23], v[60:63]\n"
    "s_waitcnt lgkmcnt(6)\n"
    "v_mfma_f32_16x16x32_bf16 v[68:71], a[104:107], v[20:23], v[68:71]\n"
    "ds_read_b128 v[20:23], v9 offset:25600\n"
    "s_branch .LgDOWN_mid_%=\n"
    ".LgDOWN_idle_%=:\n"
    "s_waitcnt vmcnt(3)\n"
    "v_add_u32 v13, v11, v3\n"
    "v_cmp_gt_i32 vcc, s71, v13\n"
    "s_cbranch_vccnz .LgDOWN_idle2_%=\n"
    "s_and_b32 s76, s71, s54\n"
    "s_mul_i32 s76, s76, s53\n"
    "s_add_u32 s76, s76, s52\n"
    "s_and_b32 s77, s71, 1\n"
    "s_mul_i32 s77, s77, 81920\n"
    "s_add_u32 s77, s77, s59\n"
    "s_add_u32 m0, s77, s59\n"
    "s_add_u32 s77, s77, 8192\n"
    "buffer_load_dwordx4 v1, s[36:39], s76 offen sc1 lds\n"
    "buffer_load_dwordx4 v1, s[36:39], s76 offen offset:1024 sc1 lds\n"
    "s_add_u32 s76, s76, 16384\n"
    "s_add_u32 m0, s77, s59\n"
    "s_add_u32 s77, s77, 8192\n"
    "buffer_load_dwordx4 v1, s[36:39], s76 offen sc1 lds\n"
    "buffer_load_dwordx4 v1, s[36:39], s76 offen offset:1024 sc1 lds\n"
    "s_add_u32 s76, s76, 16384\n"
    "s_add_u32 m0, s77, s59\n"
    "s_add_u32 s77, s77, 8192\n"
    "buffer_load_dwordx4 v1, s[36:39], s76 offen sc1 lds\n"
    "buffer_load_dwordx4 v1, s[36:39], s76 offen offset:1024 sc1 lds\n"
    "s_add_u32 s76, s76, 16384\n"
    "s_add_u32 m0, s77, s59\n"
    "s_add_u32 s77, s77, 8192\n"
    "buffer_load_dwordx4 v1, s[36:39], s76 offen sc1 lds\n"
    "buffer_load_dwordx4 v1, s[36:39], s76 offen offset:1024 sc1 lds\n"
    "s_add_u32 s76, s76, 16384\n"
    "s_add_u32 m0, s77, s59\n"
    "s_add_u32 s77, s77, 8192\n"
    "buffer_load_dwordx4 v1, s[36:39], s76 offen sc1 lds\n"
    "buffer_load_dwordx4 v1, s[36:39], s76 offen offset:1024 sc1 lds\n"
    "s_add_u32 s76, s76, 16384\n"
    "s_add_u32 m0, s77, s59\n"
    "s_add_u32 s77, s77, 8192\n"
    "buffer_load_dwordx4 v1, s[36:39], s76 offen sc1 lds\n"
    "buffer_load_dwordx4 v1, s[36:39], s76 offen offset:1024 sc1 lds\n"
    "s_add_u32 s76, s76, 16384\n"
    "s_add_u32 m0, s77, s59\n"
    "s_add_u32 s77, s77, 8192\n"
    "buffer_load_dwordx4 v1, s[36:39], s76 offen sc1 lds\n"
    "buffer_load_dwordx4 v1, s[36:39], s76 offen offset:1024 sc1 lds\n"
    "s_add_u32 s76, s76, 16384\n"
    "s_add_u32 m0, s77, s59\n"
    "s_add_u32 s77, s77, 8192\n"
    "buffer_load_dwordx4 v1, s[36:39], s76 offen sc1 lds\n"
    "buffer_load_dwordx4 v1, s[36:39], s76 offen offset:1024 sc1 lds\n"
    "s_add_u32 s76, s76, 16384\n"
    "s_add_u32 m0, s77, s59\n"
    "s_add_u32 s77, s77, 8192\n"
    "buffer_load_dwordx4 v1, s[36:39], s76 offen sc1 lds\n"
    "buffer_load_dwordx4 v1, s[36:39], s76 offen offset:1024 sc1 lds\n"
    "s_add_u32 s76, s76, 16384\n"
    "s_waitcnt vmcnt(18)\n"
    "s_sub_u32 s74, s68, 1\n"
    "v_mov_b32 v12, s74\n"
    "s_mov_b64 exec, s[62:63]\n"
    "buffer_store_dword v12, v14, s[36:39], 0 offen sc1\n"
    "s_mov_b64 exec, -1\n"
    "s_waitcnt vmcnt(1)\n"
    "s_branch .LgDOWN_bar_%=\n"
    ".LgDOWN_idle2_%=:\n"
    "s_or_b64 exec, vcc, 1\n"
    "buffer_atomic_smax v11, v2, s[36:39], 0 offen sc0 sc1\n"
    "s_mov_b64 exec, -1\n"
    "s_waitcnt vmcnt(1)\n"
    "s_sub_u32 s74, s68, 1\n"
    "v_mov_b32 v12, s74\n"
    "s_mov_b64 exec, s[62:63]\n"
    "buffer_store_dword v12, v14, s[36:39], 0 offen sc1\n"
    "s_mov_b64 exec, -1\n"
    "s_waitcnt vmcnt(1)\n"
    "s_branch .LgDOWN_block1_%=\n"
    ".LgDOWN_block_%=:\n"
    "v_add_u32 v13, v11, v3\n"
    "v_cmp_gt_i32 vcc, s71, v13\n"
    "s_nop 0\n"
    "s_or_b64 exec, vcc, 1\n"
    "buffer_atomic_smax v11, v2, s[36:39], 0 offen sc0 sc1\n"
    "s_mov_b64 exec, -1\n"
    "s_waitcnt vmcnt(0)\n"
    ".LgDOWN_block1_%=:\n"
    "v_add_u32 v13, v11, v3\n"
    "v_cmp_gt_i32 vcc, s71, v13\n"
    "s_cbranch_vccz .LgDOWN_go_%=\n"
    "s_sleep 4\n"
    "s_sub_u32 s72, s72, 1\n"
    "s_cbranch_scc0 .LgDOWN_block_%=\n"
    "s_mov_b32 s72, 0\n"
    ".LgDOWN_go_%=:\n"
    "s_and_b32 s76, s71, s54\n"
    "s_mul_i32 s76, s76, s53\n"
    "s_add_u32 s76, s76, s52\n"
    "s_and_b32 s77, s71, 1\n"
    "s_mul_i32 s77, s77, 81920\n"
    "s_add_u32 s77, s77, s59\n"
    "s_add_u32 m0, s77, s59\n"
    "s_add_u32 s77, s77, 8192\n"
    "buffer_load_dwordx4 v1, s[36:39], s76 offen sc1 lds\n"
    "buffer_load_dwordx4 v1, s[36:39], s76 offen offset:1024 sc1 lds\n"
    "s_add_u32 s76, s76, 16384\n"
    "s_add_u32 m0, s77, s59\n"
    "s_add_u32 s77, s77, 8192\n"
    "buffer_load_dwordx4 v1, s[36:39], s76 offen sc1 lds\n"
    "buffer_load_dwordx4 v1, s[36:39], s76 offen offset:1024 sc1 lds\n"
    "s_add_u32 s76, s76, 16384\n"
    "s_add_u32 m0, s77, s59\n"
    "s_add_u32 s77, s77, 8192\n"
    "buffer_load_dwordx4 v1, s[36:39], s76 offen sc1 lds\n"
    "buffer_load_dwordx4 v1, s[36:39], s76 offen offset:1024 sc1 lds\n"
    "s_add_u32 s76, s76, 16384\n"
    "s_add_u32 m0, s77, s59\n"
    "s_add_u32 s77, s77, 8192\n"
    "buffer_load_dwordx4 v1, s[36:39], s76 offen sc1 lds\n"
    "buffer_load_dwordx4 v1, s[36:39], s76 offen offset:1024 sc1 lds\n"
    "s_add_u32 s76, s76, 16384\n"
    "s_add_u32 m0, s77, s59\n"
    "s_add_u32 s77, s77, 8192\n"
    "buffer_load_dwordx4 v1, s[36:39], s76 offen sc1 lds\n"
    "buffer_load_dwordx4 v1, s[36:39], s76 offen offset:1024 sc1 lds\n"
    "s_add_u32 s76, s76, 16384\n"
    "s_add_u32 m0, s77, s59\n"
    "s_add_u32 s77, s77, 8192\n"
    "buffer_load_dwordx4 v1, s[36:39], s76 offen sc1 lds\n"
    "buffer_load_dwordx4 v1, s[36:39], s76 offen offset:1024 sc1 lds\n"
    "s_add_u32 s76, s76, 16384\n"
    "s_add_u32 m0, s77, s59\n"
    "s_add_u32 s77, s77, 8192\n"
    "buffer_load_dwordx4 v1, s[36:39], s76 offen sc1 lds\n"
    "buffer_load_dwordx4 v1, s[36:39], s76 offen offset:1024 sc1 lds\n"
    "s_add_u32 s76, s76, 16384\n"
    "s_add_u32 m0, s77, s59\n"
    "s_add_u32 s77, s77, 8192\n"
    "buffer_load_dwordx4 v1, s[36:39], s76 offen sc1 lds\n"
    "buffer_load_dwordx4 v1, s[36:39], s76 offen offset:1024 sc1 lds\n"
    "s_add_u32 s76, s76, 16384\n"
    "s_add_u32 m0, s77, s59\n"
    "s_add_u32 s77, s77, 8192\n"
    "buffer_load_dwordx4 v1, s[36:39], s76 offen sc1 lds\n"
    "buffer_load_dwordx4 v1, s[36:39], s76 offen offset:1024 sc1 lds\n"
    "s_add_u32 s76, s76, 16384\n"
    "s_waitcnt vmcnt(0)\n"
    "s_branch .LgDOWN_bar_%=\n"
    ".LgDOWN_FIN_%=:\n"
  );
}

DEVI void role_rec(const Params& p, int L, int j, int i, u32 dbg, unsigned char* smem) {
  const int tid = threadIdx.x, lane = tid & 63, w = (int)rfl(tid >> 6), h = 8 * j + i;
  const int c = lane & 15, g = lane >> 4, pp = lane & 3;
  {
    v4u t;
    if (w == 0 || w == 3) {
#pragma unroll
      for (int d = 0; d < 4; ++d) {
        u32 v = 0;
#pragma unroll
        for (int e = 0; e < 2; ++e) {
          const int q = 2 * d + e;
          const bool on = w == 0 ? ((q >> 2) == (c >> 3) && ((4 * g + (q & 3)) & 7) == (c & 7)) : (q == (c & 7));
          v |= on ? 0x3F80u << (16 * e) : 0u;
        }
        t[d] = v;
      }
    } else if (w == 1) {
      t.x = (u32)(((g & 1) * 16 + c) * 8);
      t.y = (u32)(g * 128 + (c & 7) * 16 + (c >> 3) * 8);
      t.z = c < 8 ? (u32)(g * 128 + c * 16) : OOB;
      t.w = (u32)(g * 32768 + c * 16);
    } else {
      t.x = (u32)(g * 16384 + c * 16);
      t.y = g < 2 ? (u32)(c * 16 + g * 8) : OOB;
      t.z = (u32)((g >> 1) * 128 + (c & 7) * 16 + (2 * (g & 1) + (c >> 3)) * 32768);
      t.w = lane < 32 ? (u32)lane * 64 : OOB;
    }
    *(v4u*)(smem + 12288 + tid * 16) = t;
    asm volatile("s_waitcnt lgkmcnt(0)" ::: "memory");
  }
  const u32 wb = 576u * 4096 * 2 - h * 256;
  v16u sa = cat4(make_srd(p.ws, WS_END), make_srd(p.Wq + h * 128, wb), make_srd(p.Wk + h * 128, wb), make_srd(p.Wv + h * 128, wb));
  const v4u so = make_srd(p.Wo + h * 128 * 576, 128u * 1152);
  const int unit = w == 0 ? h : ((w == 1 && i == 0) ? 32 + j : -1);
  const u32 hot = HOT + L * 8192, hot2 = HOT2 + (1 - L) * 8192, img = hot2 + j * 2 * CHUNK;
  v16u sb = {so.x, so.y, so.z, so.w,
             pga(L, B_QKV + 3 * h + 1) + w * 2048, pga(L, B_KVM + h) + w * 1536, mbox_att(L, j, i, w), mbox(L, j, SL_REC + i, w),
             (u32)w * 1024, dbg + w * 64, INITF + L * 8192, hot + 8 * w * CHUNK + h * 256,
             unit < 0 ? OOB : hot + unit * CHUNK, unit < 0 ? OOB : hot2 + (unit >> 5) * CHUNK + (unit & 31) * 256, img, hot + (32 + w) * CHUNK + h * 256};
  const int jr = lane >> 3;
  const u32 row = (u32)(16 * (w >> 1) + 8 * ((jr >> 1) & 1) + 4 * (w & 1) + 2 * (jr >> 2) + (jr & 1));
  const u32 ch = (((u32)lane & 7) ^ wswz(jr, w)) << 4;
  v16u va = {0, 0, 0, 0, 0, 0, 0, 0, 0, 0, 0, 0, 0, 0, 0, 0};
  va[0] = lane * 16;
  va[1] = 16384 + w * 2048 + lane * 16;
  va[2] = (u32)((c >> 3) * 0xAAAA) | 0x4444u;
  const int qq = (lane >> 2) & 3;
  va[3] = (u32)((g >> 1) * 36864 + 32768 + (g & 1) * 2048 + (4 * (qq >> 1) + (qq & 1)) * 128 + ((2 * w + (pp >> 1)) << 4) + 8 * (pp & 1));
  va[4] = row * 8192 + ch;
  va[5] = row * 1152 + ch;
  va[6] = (u32)(w >> 1) * 4096 + tr_base(lane, 4 * (w & 1) + pp);
  va[7] = (u32)w * 8192 + tr_base(lane, 4 * (qq >> 1) + (pp >> 1)) + 8 * (pp & 1);
  va[8] = hot2 + 32 * CHUNK;
  va[9] = (u32)(h * 64 + 4 * w);
  va[10] = w >= 2 ? hot + h * CHUNK + (w - 2) * 4096 : OOB;
  va[11] = w == 3 ? hot + (32 + j) * CHUNK + i * 1024 : (w == 2 ? img + i * 1024 : (i == 1 ? img + CHUNK : OOB));
  va[12] = w == 3 ? ~0u : 0u;
  va[13] = w == 3 ? 0u : ~0u;
  RUN_ASM(
    "s_nop 4\n"
    "v_mov_b32 v48, v8\n"
    "v_mov_b32 v49, v9\n"
    "v_mov_b32 v50, v10\n"
    "v_mov_b32 v51, v11\n"
    "v_mov_b32 v52, v12\n"
    "v_mov_b32 v53, v13\n"
    "s_add_u32 m0, s60, 16384\n"
    "s_mov_b32 s73, 0\n"
    "buffer_load_dwordx4 v5, s[52:55], s73 offen lds\n"
    "s_add_u32 m0, s60, 20480\n"
    "s_mov_b32 s73, 128\n"
    "buffer_load_dwordx4 v5, s[52:55], s73 offen lds\n"
    "s_add_u32 m0, s60, 24576\n"
    "s_mov_b32 s73, 256\n"
    "buffer_load_dwordx4 v5, s[52:55], s73 offen lds\n"
    "s_add_u32 m0, s60, 28672\n"
    "s_mov_b32 s73, 384\n"
    "buffer_load_dwordx4 v5, s[52:55], s73 offen lds\n"
    "s_add_u32 m0, s60, 32768\n"
    "s_mov_b32 s73, 512\n"
    "buffer_load_dwordx4 v5, s[52:55], s73 offen lds\n"
    "s_add_u32 m0, s60, 36864\n"
    "s_mov_b32 s73, 640\n"
    "buffer_load_dwordx4 v5, s[52:55], s73 offen lds\n"
    "s_add_u32 m0, s60, 40960\n"
    "s_mov_b32 s73, 768\n"
    "buffer_load_dwordx4 v5, s[52:55], s73 offen lds\n"
    "s_add_u32 m0, s60, 45056\n"
    "s_mov_b32 s73, 896\n"
    "buffer_load_dwordx4 v5, s[52:55], s73 offen lds\n"
    "s_add_u32 m0, s60, 49152\n"
    "s_mov_b32 s73, 1024\n"
    "buffer_load_dwordx4 v5, s[52:55], s73 offen lds\n"
    "s_add_u32 m0, s60, 53248\n"
    "s_mov_b32 s73, 36864\n"
    "buffer_load_dwordx4 v5, s[52:55], s73 offen lds\n"
    "s_add_u32 m0, s60, 57344\n"
    "s_mov_b32 s73, 36992\n"
    "buffer_load_dwordx4 v5, s[52:55], s73 offen lds\n"
    "s_add_u32 m0, s60, 61440\n"
    "s_mov_b32 s73, 37120\n"
    "buffer_load_dwordx4 v5, s[52:55], s73 offen lds\n"
    "s_add_u32 m0, s60, 65536\n"
    "s_mov_b32 s73, 37248\n"
    "buffer_load_dwordx4 v5, s[52:55], s73 offen lds\n"
    "s_add_u32 m0, s60, 69632\n"
    "s_mov_b32 s73, 37376\n"
    "buffer_load_dwordx4 v5, s[52:55], s73 offen lds\n"
    "s_add_u32 m0, s60, 73728\n"
    "s_mov_b32 s73, 37504\n"
    "buffer_load_dwordx4 v5, s[52:55], s73 offen lds\n"
    "s_add_u32 m0, s60, 77824\n"
    "s_mov_b32 s73, 37632\n"
    "buffer_load_dwordx4 v5, s[52:55], s73 offen lds\n"
    "s_add_u32 m0, s60, 81920\n"
    "s_mov_b32 s73, 37760\n"
    "buffer_load_dwordx4 v5, s[52:55], s73 offen lds\n"
    "s_add_u32 m0, s60, 86016\n"
    "s_mov_b32 s73, 37888\n"
    "buffer_load_dwordx4 v5, s[52:55], s73 offen lds\n"
    "s_add_u32 m0, s60, 90112\n"
    "s_mov_b32 s73, 73728\n"
    "buffer_load_dwordx4 v5, s[52:55], s73 offen lds\n"
    "s_add_u32 m0, s60, 94208\n"
    "s_mov_b32 s73, 73856\n"
    "buffer_load_dwordx4 v5, s[52:55], s73 offen lds\n"
    "s_add_u32 m0, s60, 98304\n"
    "s_mov_b32 s73, 73984\n"
    "buffer_load_dwordx4 v5, s[52:55], s73 offen lds\n"
    "s_add_u32 m0, s60, 102400\n"
    "s_mov_b32 s73, 74112\n"
    "buffer_load_dwordx4 v5, s[52:55], s73 offen lds\n"
    "s_add_u32 m0, s60, 106496\n"
    "s_mov_b32 s73, 74240\n"
    "buffer_load_dwordx4 v5, s[52:55], s73 offen lds\n"
    "s_add_u32 m0, s60, 110592\n"
    "s_mov_b32 s73, 74368\n"
    "buffer_load_dwordx4 v5, s[52:55], s73 offen lds\n"
    "s_add_u32 m0, s60, 114688\n"
    "s_mov_b32 s73, 74496\n"
    "buffer_load_dwordx4 v5, s[52:55], s73 offen lds\n"
    "s_add_u32 m0, s60, 118784\n"
    "s_mov_b32 s73, 74624\n"
    "buffer_load_dwordx4 v5, s[52:55], s73 offen lds\n"
    "s_add_u32 m0, s60, 122880\n"
    "s_mov_b32 s73, 74752\n"
    "buffer_load_dwordx4 v5, s[52:55], s73 offen lds\n"
    "s_add_u32 m0, s60, 126976\n"
    "s_mov_b32 s73, 110592\n"
    "buffer_load_dwordx4 v5, s[52:55], s73 offen lds\n"
    "s_add_u32 m0, s60, 131072\n"
    "s_mov_b32 s73, 110720\n"
    "buffer_load_dwordx4 v5, s[52:55], s73 offen lds\n"
    "s_add_u32 m0, s60, 135168\n"
    "s_mov_b32 s73, 110848\n"
    "buffer_load_dwordx4 v5, s[52:55], s73 offen lds\n"
    "s_add_u32 m0, s60, 139264\n"
    "s_mov_b32 s73, 110976\n"
    "buffer_load_dwordx4 v5, s[52:55], s73 offen lds\n"
    "s_add_u32 m0, s60, 143360\n"
    "s_mov_b32 s73, 111104\n"
    "buffer_load_dwordx4 v5, s[52:55], s73 offen lds\n"
    "s_add_u32 m0, s60, 147456\n"
    "s_mov_b32 s73, 111232\n"
    "buffer_load_dwordx4 v5, s[52:55], s73 offen lds\n"
    "s_add_u32 m0, s60, 151552\n"
    "s_mov_b32 s73, 111360\n"
    "buffer_load_dwordx4 v5, s[52:55], s73 offen lds\n"
    "s_add_u32 m0, s60, 155648\n"
    "s_mov_b32 s73, 111488\n"
    "buffer_load_dwordx4 v5, s[52:55], s73 offen lds\n"
    "s_add_u32 m0, s60, 159744\n"
    "s_mov_b32 s73, 111616\n"
    "buffer_load_dwordx4 v5, s[52:55], s73 offen lds\n"
    "s_waitcnt vmcnt(18) lgkmcnt(0)\n"
    "s_barrier\n"
    "v_add_u32 v8, 16384, v7\n"
    "v_xor_b32 v9, 32, v8\n"
    "v_xor_b32 v10, 320, v8\n"
    "v_xor_b32 v11, 32, v10\n"
    "v_add_u32 v12, 16384, v3\n"
    "v_xor_b32 v13, 320, v12\n"
    "ds_read_b64_tr_b16 v[112:113], v8 offset:0\n"
    "ds_read_b64_tr_b16 v[114:115], v8 offset:512\n"
    "ds_read_b64_tr_b16 v[116:117], v8 offset:36864\n"
    "ds_read_b64_tr_b16 v[118:119], v8 offset:37376\n"
    "ds_read_b64_tr_b16 v[144:145], v10 offset:0\n"
    "ds_read_b64_tr_b16 v[146:147], v10 offset:512\n"
    "ds_read_b64_tr_b16 v[148:149], v10 offset:36864\n"
    "ds_read_b64_tr_b16 v[150:151], v10 offset:37376\n"
    "s_waitcnt lgkmcnt(6)\n"
    "ds_read_b64_tr_b16 v[128:129], v9 offset:0\n"
    "ds_read_b64_tr_b16 v[130:131], v9 offset:512\n"
    "ds_read_b64_tr_b16 v[132:133], v9 offset:36864\n"
    "ds_read_b64_tr_b16 v[134:135], v9 offset:37376\n"
    "ds_read_b64_tr_b16 v[152:153], v11 offset:0\n"
    "ds_read_b64_tr_b16 v[154:155], v11 offset:512\n"
    "ds_read_b64_tr_b16 v[156:157], v11 offset:36864\n"
    "ds_read_b64_tr_b16 v[158:159], v11 offset:37376\n"
    "s_waitcnt lgkmcnt(6)\n"
    "ds_read_b64_tr_b16 v[160:161], v8 offset:4096\n"
    "ds_read_b64_tr_b16 v[162:163], v8 offset:4608\n"
    "ds_read_b64_tr_b16 v[164:165], v8 offset:40960\n"
    "ds_read_b64_tr_b16 v[166:167], v8 offset:41472\n"
    "ds_read_b64_tr_b16 v[176:177], v10 offset:4096\n"
    "ds_read_b64_tr_b16 v[178:179], v10 offset:4608\n"
    "ds_read_b64_tr_b16 v[180:181], v10 offset:40960\n"
    "ds_read_b64_tr_b16 v[182:183], v10 offset:41472\n"
    "s_waitcnt lgkmcnt(6)\n"
    "ds_read_b64_tr_b16 v[168:169], v9 offset:4096\n"
    "ds_read_b64_tr_b16 v[170:171], v9 offset:4608\n"
    "ds_read_b64_tr_b16 v[172:173], v9 offset:40960\n"
    "ds_read_b64_tr_b16 v[174:175], v9 offset:41472\n"
    "ds_read_b64_tr_b16 v[184:185], v11 offset:4096\n"
    "ds_read_b64_tr_b16 v[186:187], v11 offset:4608\n"
    "ds_read_b64_tr_b16 v[188:189], v11 offset:40960\n"
    "ds_read_b64_tr_b16 v[190:191], v11 offset:41472\n"
    "s_waitcnt lgkmcnt(6)\n"
    "v_xor_b32 v8, 32, v12\n"
    "v_xor_b32 v9, 32, v13\n"
    "ds_read_b64_tr_b16 v[192:193], v12 offset:0\n"
    "ds_read_b64_tr_b16 v[194:195], v8 offset:1024\n"
    "ds_read_b64_tr_b16 v[196:197], v13 offset:0\n"
    "ds_read_b64_tr_b16 v[198:199], v9 offset:1024\n"
    "s_waitcnt vmcnt(0) lgkmcnt(0)\n"
    "s_barrier\n"
    "s_add_u32 m0, s60, 16384\n"
    "s_mov_b32 s73, 0\n"
    "buffer_load_dwordx4 v4, s[40:43], s73 offen lds\n"
    "s_add_u32 m0, s60, 20480\n"
    "s_mov_b32 s73, 128\n"
    "buffer_load_dwordx4 v4, s[40:43], s73 offen lds\n"
    "s_add_u32 m0, s60, 24576\n"
    "s_mov_b32 s73, 0\n"
    "buffer_load_dwordx4 v4, s[44:47], s73 offen lds\n"
    "s_add_u32 m0, s60, 28672\n"
    "s_mov_b32 s73, 128\n"
    "buffer_load_dwordx4 v4, s[44:47], s73 offen lds\n"
    "s_add_u32 m0, s60, 32768\n"
    "s_mov_b32 s73, 262144\n"
    "buffer_load_dwordx4 v4, s[40:43], s73 offen lds\n"
    "s_add_u32 m0, s60, 36864\n"
    "s_mov_b32 s73, 262272\n"
    "buffer_load_dwordx4 v4, s[40:43], s73 offen lds\n"
    "s_add_u32 m0, s60, 40960\n"
    "s_mov_b32 s73, 262144\n"
    "buffer_load_dwordx4 v4, s[44:47], s73 offen lds\n"
    "s_add_u32 m0, s60, 45056\n"
    "s_mov_b32 s73, 262272\n"
    "buffer_load_dwordx4 v4, s[44:47], s73 offen lds\n"
    "s_add_u32 m0, s60, 49152\n"
    "s_mov_b32 s73, 524288\n"
    "buffer_load_dwordx4 v4, s[40:43], s73 offen lds\n"
    "s_add_u32 m0, s60, 53248\n"
    "s_mov_b32 s73, 524416\n"
    "buffer_load_dwordx4 v4, s[40:43], s73 offen lds\n"
    "s_add_u32 m0, s60, 57344\n"
    "s_mov_b32 s73, 524288\n"
    "buffer_load_dwordx4 v4, s[44:47], s73 offen lds\n"
    "s_add_u32 m0, s60, 61440\n"
    "s_mov_b32 s73, 524416\n"
    "buffer_load_dwordx4 v4, s[44:47], s73 offen lds\n"
    "s_add_u32 m0, s60, 65536\n"
    "s_mov_b32 s73, 786432\n"
    "buffer_load_dwordx4 v4, s[40:43], s73 offen lds\n"
    "s_add_u32 m0, s60, 69632\n"
    "s_mov_b32 s73, 786560\n"
    "buffer_load_dwordx4 v4, s[40:43], s73 offen lds\n"
    "s_add_u32 m0, s60, 73728\n"
    "s_mov_b32 s73, 786432\n"
    "buffer_load_dwordx4 v4, s[44:47], s73 offen lds\n"
    "s_add_u32 m0, s60, 77824\n"
    "s_mov_b32 s73, 786560\n"
    "buffer_load_dwordx4 v4, s[44:47], s73 offen lds\n"
    "v_add_u32 v8, 90112, v7\n"
    "v_xor_b32 v9, 32, v8\n"
    "v_xor_b32 v10, 320, v8\n"
    "v_xor_b32 v11, 32, v10\n"
    "v_add_u32 v12, 90112, v3\n"
    "v_xor_b32 v13, 320, v12\n"
    "ds_read_b64_tr_b16 v[120:121], v8 offset:0\n"
    "ds_read_b64_tr_b16 v[122:123], v8 offset:512\n"
    "ds_read_b64_tr_b16 v[124:125], v8 offset:36864\n"
    "ds_read_b64_tr_b16 v[126:127], v8 offset:37376\n"
    "ds_read_b64_tr_b16 v[208:209], v10 offset:0\n"
    "ds_read_b64_tr_b16 v[210:211], v10 offset:512\n"
    "ds_read_b64_tr_b16 v[212:213], v10 offset:36864\n"
    "ds_read_b64_tr_b16 v[214:215], v10 offset:37376\n"
    "s_waitcnt lgkmcnt(6)\n"
    "ds_read_b64_tr_b16 v[136:137], v9 offset:0\n"
    "ds_read_b64_tr_b16 v[138:139], v9 offset:512\n"
    "ds_read_b64_tr_b16 v[140:141], v9 offset:36864\n"
    "ds_read_b64_tr_b16 v[142:143], v9 offset:37376\n"
    "ds_read_b64_tr_b16 v[216:217], v11 offset:0\n"
    "ds_read_b64_tr_b16 v[218:219], v11 offset:512\n"
    "ds_read_b64_tr_b16 v[220:221], v11 offset:36864\n"
    "ds_read_b64_tr_b16 v[222:223], v11 offset:37376\n"
    "s_waitcnt lgkmcnt(6)\n"
    "ds_read_b64_tr_b16 v[224:225], v8 offset:4096\n"
    "ds_read_b64_tr_b16 v[226:227], v8 offset:4608\n"
    "ds_read_b64_tr_b16 v[228:229], v8 offset:40960\n"
    "ds_read_b64_tr_b16 v[230:231], v8 offset:41472\n"
    "ds_read_b64_tr_b16 v[240:241], v10 offset:4096\n"
    "ds_read_b64_tr_b16 v[242:243], v10 offset:4608\n"
    "ds_read_b64_tr_b16 v[244:245], v10 offset:40960\n"
    "ds_read_b64_tr_b16 v[246:247], v10 offset:41472\n"
    "s_waitcnt lgkmcnt(6)\n"
    "ds_read_b64_tr_b16 v[232:233], v9 offset:4096\n"
    "ds_read_b64_tr_b16 v[234:235], v9 offset:4608\n"
    "ds_read_b64_tr_b16 v[236:237], v9 offset:40960\n"
    "ds_read_b64_tr_b16 v[238:239], v9 offset:41472\n"
    "ds_read_b64_tr_b16 v[248:249], v11 offset:4096\n"
    "ds_read_b64_tr_b16 v[250:251], v11 offset:4608\n"
    "ds_read_b64_tr_b16 v[252:253], v11 offset:40960\n"
    "ds_read_b64_tr_b16 v[254:255], v11 offset:41472\n"
    "s_waitcnt lgkmcnt(6)\n"
    "v_xor_b32 v8, 32, v12\n"
    "v_xor_b32 v9, 32, v13\n"
    "ds_read_b64_tr_b16 v[200:201], v12 offset:0\n"
    "ds_read_b64_tr_b16 v[202:203], v8 offset:1024\n"
    "ds_read_b64_tr_b16 v[204:205], v13 offset:0\n"
    "ds_read_b64_tr_b16 v[206:207], v9 offset:1024\n"
    "s_waitcnt vmcnt(12) lgkmcnt(0)\n"
    "s_barrier\n"
    "s_add_u32 m0, s60, 81920\n"
    "s_mov_b32 s73, 1048576\n"
    "buffer_load_dwordx4 v4, s[40:43], s73 offen lds\n"
    "s_add_u32 m0, s60, 86016\n"
    "s_mov_b32 s73, 1048704\n"
    "buffer_load_dwordx4 v4, s[40:43], s73 offen lds\n"
    "s_add_u32 m0, s60, 90112\n"
    "s_mov_b32 s73, 1048576\n"
    "buffer_load_dwordx4 v4, s[44:47], s73 offen lds\n"
    "s_add_u32 m0, s60, 94208\n"
    "s_mov_b32 s73, 1048704\n"
    "buffer_load_dwordx4 v4, s[44:47], s73 offen lds\n"
    "s_add_u32 m0, s60, 98304\n"
    "s_mov_b32 s73, 1310720\n"
    "buffer_load_dwordx4 v4, s[40:43], s73 offen lds\n"
    "s_add_u32 m0, s60, 102400\n"
    "s_mov_b32 s73, 1310848\n"
    "buffer_load_dwordx4 v4, s[40:43], s73 offen lds\n"
    "s_add_u32 m0, s60, 106496\n"
    "s_mov_b32 s73, 1310720\n"
    "buffer_load_dwordx4 v4, s[44:47], s73 offen lds\n"
    "s_add_u32 m0, s60, 110592\n"
    "s_mov_b32 s73, 1310848\n"
    "buffer_load_dwordx4 v4, s[44:47], s73 offen lds\n"
    "s_add_u32 m0, s60, 114688\n"
    "s_mov_b32 s73, 1572864\n"
    "buffer_load_dwordx4 v4, s[40:43], s73 offen lds\n"
    "s_add_u32 m0, s60, 118784\n"
    "s_mov_b32 s73, 1572992\n"
    "buffer_load_dwordx4 v4, s[40:43], s73 offen lds\n"
    "s_add_u32 m0, s60, 122880\n"
    "s_mov_b32 s73, 1572864\n"
    "buffer_load_dwordx4 v4, s[44:47], s73 offen lds\n"
    "s_add_u32 m0, s60, 126976\n"
    "s_mov_b32 s73, 1572992\n"
    "buffer_load_dwordx4 v4, s[44:47], s73 offen lds\n"
    "s_add_u32 m0, s60, 131072\n"
    "s_mov_b32 s73, 1835008\n"
    "buffer_load_dwordx4 v4, s[40:43], s73 offen lds\n"
    "s_add_u32 m0, s60, 135168\n"
    "s_mov_b32 s73, 1835136\n"
    "buffer_load_dwordx4 v4, s[40:43], s73 offen lds\n"
    "s_add_u32 m0, s60, 139264\n"
    "s_mov_b32 s73, 1835008\n"
    "buffer_load_dwordx4 v4, s[44:47], s73 offen lds\n"
    "s_add_u32 m0, s60, 143360\n"
    "s_mov_b32 s73, 1835136\n"
    "buffer_load_dwordx4 v4, s[44:47], s73 offen lds\n"
    "s_add_u32 m0, s60, 147456\n"
    "s_mov_b32 s73, 2097152\n"
    "buffer_load_dwordx4 v4, s[40:43], s73 offen lds\n"
    "s_add_u32 m0, s60, 151552\n"
    "s_mov_b32 s73, 2097280\n"
    "buffer_load_dwordx4 v4, s[40:43], s73 offen lds\n"
    "s_add_u32 m0, s60, 155648\n"
    "s_mov_b32 s73, 2097152\n"
    "buffer_load_dwordx4 v4, s[44:47], s73 offen lds\n"
    "s_add_u32 m0, s60, 159744\n"
    "s_mov_b32 s73, 2097280\n"
    "buffer_load_dwordx4 v4, s[44:47], s73 offen lds\n"
    "v_add_u32 v8, 16384, v6\n"
    "ds_read_b64_tr_b16 a[0:1], v8 offset:0\n"
    "ds_read_b64_tr_b16 a[2:3], v8 offset:512\n"
    "ds_read_b64_tr_b16 a[8:9], v8 offset:8\n"
    "ds_read_b64_tr_b16 a[10:11], v8 offset:520\n"
    "ds_read_b64_tr_b16 a[144:145], v8 offset:8192\n"
    "ds_read_b64_tr_b16 a[146:147], v8 offset:8704\n"
    "ds_read_b64_tr_b16 a[152:153], v8 offset:8200\n"
    "ds_read_b64_tr_b16 a[154:155], v8 offset:8712\n"
    "s_waitcnt vmcnt(28) lgkmcnt(0)\n"
    "s_barrier\n"
    "s_add_u32 m0, s60, 16384\n"
    "s_mov_b32 s73, 2359296\n"
    "buffer_load_dwordx4 v4, s[40:43], s73 offen lds\n"
    "s_add_u32 m0, s60, 20480\n"
    "s_mov_b32 s73, 2359424\n"
    "buffer_load_dwordx4 v4, s[40:43], s73 offen lds\n"
    "s_add_u32 m0, s60, 24576\n"
    "s_mov_b32 s73, 2359296\n"
    "buffer_load_dwordx4 v4, s[44:47], s73 offen lds\n"
    "s_add_u32 m0, s60, 28672\n"
    "s_mov_b32 s73, 2359424\n"
    "buffer_load_dwordx4 v4, s[44:47], s73 offen lds\n"
    "v_add_u32 v8, 32768, v6\n"
    "ds_read_b64_tr_b16 a[4:5], v8 offset:0\n"
    "ds_read_b64_tr_b16 a[6:7], v8 offset:512\n"
    "ds_read_b64_tr_b16 a[12:13], v8 offset:8\n"
    "ds_read_b64_tr_b16 a[14:15], v8 offset:520\n"
    "ds_read_b64_tr_b16 a[148:149], v8 offset:8192\n"
    "ds_read_b64_tr_b16 a[150:151], v8 offset:8704\n"
    "ds_read_b64_tr_b16 a[156:157], v8 offset:8200\n"
    "ds_read_b64_tr_b16 a[158:159], v8 offset:8712\n"
    "s_waitcnt vmcnt(28) lgkmcnt(0)\n"
    "s_barrier\n"
    "s_add_u32 m0, s60, 32768\n"
    "s_mov_b32 s73, 2621440\n"
    "buffer_load_dwordx4 v4, s[40:43], s73 offen lds\n"
    "s_add_u32 m0, s60, 36864\n"
    "s_mov_b32 s73, 2621568\n"
    "buffer_load_dwordx4 v4, s[40:43], s73 offen lds\n"
    "s_add_u32 m0, s60, 40960\n"
    "s_mov_b32 s73, 2621440\n"
    "buffer_load_dwordx4 v4, s[44:47], s73 offen lds\n"
    "s_add_u32 m0, s60, 45056\n"
    "s_mov_b32 s73, 2621568\n"
    "buffer_load_dwordx4 v4, s[44:47], s73 offen lds\n"
    "v_add_u32 v8, 49152, v6\n"
    "ds_read_b64_tr_b16 a[16:17], v8 offset:0\n"
    "ds_read_b64_tr_b16 a[18:19], v8 offset:512\n"
    "ds_read_b64_tr_b16 a[24:25], v8 offset:8\n"
    "ds_read_b64_tr_b16 a[26:27], v8 offset:520\n"
    "ds_read_b64_tr_b16 a[160:161], v8 offset:8192\n"
    "ds_read_b64_tr_b16 a[162:163], v8 offset:8704\n"
    "ds_read_b64_tr_b16 a[168:169], v8 offset:8200\n"
    "ds_read_b64_tr_b16 a[170:171], v8 offset:8712\n"
    "s_waitcnt vmcnt(28) lgkmcnt(0)\n"
    "s_barrier\n"
    "s_add_u32 m0, s60, 49152\n"
    "s_mov_b32 s73, 2883584\n"
    "buffer_load_dwordx4 v4, s[40:43], s73 offen lds\n"
    "s_add_u32 m0, s60, 53248\n"
    "s_mov_b32 s73, 2883712\n"
    "buffer_load_dwordx4 v4, s[40:43], s73 offen lds\n"
    "s_add_u32 m0, s60, 57344\n"
    "s_mov_b32 s73, 2883584\n"
    "buffer_load_dwordx4 v4, s[44:47], s73 offen lds\n"
    "s_add_u32 m0, s60, 61440\n"
    "s_mov_b32 s73, 2883712\n"
    "buffer_load_dwordx4 v4, s[44:47], s73 offen lds\n"
    "v_add_u32 v8, 65536, v6\n"
    "ds_read_b64_tr_b16 a[20:21], v8 offset:0\n"
    "ds_read_b64_tr_b16 a[22:23], v8 offset:512\n"
    "ds_read_b64_tr_b16 a[28:29], v8 offset:8\n"
    "ds_read_b64_tr_b16 a[30:31], v8 offset:520\n"
    "ds_read_b64_tr_b16 a[164:165], v8 offset:8192\n"
    "ds_read_b64_tr_b16 a[166:167], v8 offset:8704\n"
    "ds_read_b64_tr_b16 a[172:173], v8 offset:8200\n"
    "ds_read_b64_tr_b16 a[174:175], v8 offset:8712\n"
    "s_waitcnt vmcnt(28) lgkmcnt(0)\n"
    "s_barrier\n"
    "s_add_u32 m0, s60, 65536\n"
    "s_mov_b32 s73, 3145728\n"
    "buffer_load_dwordx4 v4, s[40:43], s73 offen lds\n"
    "s_add_u32 m0, s60, 69632\n"
    "s_mov_b32 s73, 3145856\n"
    "buffer_load_dwordx4 v4, s[40:43], s73 offen lds\n"
    "s_add_u32 m0, s60, 73728\n"
    "s_mov_b32 s73, 3145728\n"
    "buffer_load_dwordx4 v4, s[44:47], s73 offen lds\n"
    "s_add_u32 m0, s60, 77824\n"
    "s_mov_b32 s73, 3145856\n"
    "buffer_load_dwordx4 v4, s[44:47], s73 offen lds\n"
    "v_add_u32 v8, 81920, v6\n"
    "ds_read_b64_tr_b16 a[32:33], v8 offset:0\n"
    "ds_read_b64_tr_b16 a[34:35], v8 offset:512\n"
    "ds_read_b64_tr_b16 a[40:41], v8 offset:8\n"
    "ds_read_b64_tr_b16 a[42:43], v8 offset:520\n"
    "ds_read_b64_tr_b16 a[176:177], v8 offset:8192\n"
    "ds_read_b64_tr_b16 a[178:179], v8 offset:8704\n"
    "ds_read_b64_tr_b16 a[184:185], v8 offset:8200\n"
    "ds_read_b64_tr_b16 a[186:187], v8 offset:8712\n"
    "s_waitcnt vmcnt(28) lgkmcnt(0)\n"
    "s_barrier\n"
    "s_add_u32 m0, s60, 81920\n"
    "s_mov_b32 s73, 3407872\n"
    "buffer_load_dwordx4 v4, s[40:43], s73 offen lds\n"
    "s_add_u32 m0, s60, 86016\n"
    "s_mov_b32 s73, 3408000\n"
    "buffer_load_dwordx4 v4, s[40:43], s73 offen lds\n"
    "s_add_u32 m0, s60, 90112\n"
    "s_mov_b32 s73, 3407872\n"
    "buffer_load_dwordx4 v4, s[44:47], s73 offen lds\n"
    "s_add_u32 m0, s60, 94208\n"
    "s_mov_b32 s73, 3408000\n"
    "buffer_load_dwordx4 v4, s[44:47], s73 offen lds\n"
    "v_add_u32 v8, 98304, v6\n"
    "ds_read_b64_tr_b16 a[36:37], v8 offset:0\n"
    "ds_read_b64_tr_b16 a[38:39], v8 offset:512\n"
    "ds_read_b64_tr_b16 a[44:45], v8 offset:8\n"
    "ds_read_b64_tr_b16 a[46:47], v8 offset:520\n"
    "ds_read_b64_tr_b16 a[180:181], v8 offset:8192\n"
    "ds_read_b64_tr_b16 a[182:183], v8 offset:8704\n"
    "ds_read_b64_tr_b16 a[188:189], v8 offset:8200\n"
    "ds_read_b64_tr_b16 a[190:191], v8 offset:8712\n"
    "s_waitcnt vmcnt(28) lgkmcnt(0)\n"
    "s_barrier\n"
    "s_add_u32 m0, s60, 98304\n"
    "s_mov_b32 s73, 3670016\n"
    "buffer_load_dwordx4 v4, s[40:43], s73 offen lds\n"
    "s_add_u32 m0, s60, 102400\n"
    "s_mov_b32 s73, 3670144\n"
    "buffer_load_dwordx4 v4, s[40:43], s73 offen lds\n"
    "s_add_u32 m0, s60, 106496\n"
    "s_mov_b32 s73, 3670016\n"
    "buffer_load_dwordx4 v4, s[44:47], s73 offen lds\n"
    "s_add_u32 m0, s60, 110592\n"
    "s_mov_b32 s73, 3670144\n"
    "buffer_load_dwordx4 v4, s[44:47], s73 offen lds\n"
    "v_add_u32 v8, 114688, v6\n"
    "ds_read_b64_tr_b16 a[48:49], v8 offset:0\n"
    "ds_read_b64_tr_b16 a[50:51], v8 offset:512\n"
    "ds_read_b64_tr_b16 a[56:57], v8 offset:8\n"
    "ds_read_b64_tr_b16 a[58:59], v8 offset:520\n"
    "ds_read_b64_tr_b16 a[192:193], v8 offset:8192\n"
    "ds_read_b64_tr_b16 a[194:195], v8 offset:8704\n"
    "ds_read_b64_tr_b16 a[200:201], v8 offset:8200\n"
    "ds_read_b64_tr_b16 a[202:203], v8 offset:8712\n"
    "s_waitcnt vmcnt(28) lgkmcnt(0)\n"
    "s_barrier\n"
    "s_add_u32 m0, s60, 114688\n"
    "s_mov_b32 s73, 3932160\n"
    "buffer_load_dwordx4 v4, s[40:43], s73 offen lds\n"
    "s_add_u32 m0, s60, 118784\n"
    "s_mov_b32 s73, 3932288\n"
    "buffer_load_dwordx4 v4, s[40:43], s73 offen lds\n"
    "s_add_u32 m0, s60, 122880\n"
    "s_mov_b32 s73, 3932160\n"
    "buffer_load_dwordx4 v4, s[44:47], s73 offen lds\n"
    "s_add_u32 m0, s60, 126976\n"
    "s_mov_b32 s73, 3932288\n"
    "buffer_load_dwordx4 v4, s[44:47], s73 offen lds\n"
    "v_add_u32 v8, 131072, v6\n"
    "ds_read_b64_tr_b16 a[52:53], v8 offset:0\n"
    "ds_read_b64_tr_b16 a[54:55], v8 offset:512\n"
    "ds_read_b64_tr_b16 a[60:61], v8 offset:8\n"
    "ds_read_b64_tr_b16 a[62:63], v8 offset:520\n"
    "ds_read_b64_tr_b16 a[196:197], v8 offset:8192\n"
    "ds_read_b64_tr_b16 a[198:199], v8 offset:8704\n"
    "ds_read_b64_tr_b16 a[204:205], v8 offset:8200\n"
    "ds_read_b64_tr_b16 a[206:207], v8 offset:8712\n"
    "s_waitcnt vmcnt(28) lgkmcnt(0)\n"
    "s_barrier\n"
    "s_add_u32 m0, s60, 131072\n"
    "s_mov_b32 s73, 4194304\n"
    "buffer_load_dwordx4 v4, s[40:43], s73 offen lds\n"
    "s_add_u32 m0, s60, 135168\n"
    "s_mov_b32 s73, 4194432\n"
    "buffer_load_dwordx4 v4, s[40:43], s73 offen lds\n"
    "s_add_u32 m0, s60, 139264\n"
    "s_mov_b32 s73, 4194304\n"
    "buffer_load_dwordx4 v4, s[44:47], s73 offen lds\n"
    "s_add_u32 m0, s60, 143360\n"
    "s_mov_b32 s73, 4194432\n"
    "buffer_load_dwordx4 v4, s[44:47], s73 offen lds\n"
    "v_add_u32 v8, 147456, v6\n"
    "ds_read_b64_tr_b16 a[64:65], v8 offset:0\n"
    "ds_read_b64_tr_b16 a[66:67], v8 offset:512\n"
    "ds_read_b64_tr_b16 a[72:73], v8 offset:8\n"
    "ds_read_b64_tr_b16 a[74:75], v8 offset:520\n"
    "ds_read_b64_tr_b16 a[208:209], v8 offset:8192\n"
    "ds_read_b64_tr_b16 a[210:211], v8 offset:8704\n"
    "ds_read_b64_tr_b16 a[216:217], v8 offset:8200\n"
    "ds_read_b64_tr_b16 a[218:219], v8 offset:8712\n"
    "s_waitcnt vmcnt(28) lgkmcnt(0)\n"
    "s_barrier\n"
    "s_add_u32 m0, s60, 147456\n"
    "s_mov_b32 s73, 4456448\n"
    "buffer_load_dwordx4 v4, s[40:43], s73 offen lds\n"
    "s_add_u32 m0, s60, 151552\n"
    "s_mov_b32 s73, 4456576\n"
    "buffer_load_dwordx4 v4, s[40:43], s73 offen lds\n"
    "s_add_u32 m0, s60, 155648\n"
    "s_mov_b32 s73, 4456448\n"
    "buffer_load_dwordx4 v4, s[44:47], s73 offen lds\n"
    "s_add_u32 m0, s60, 159744\n"
    "s_mov_b32 s73, 4456576\n"
    "buffer_load_dwordx4 v4, s[44:47], s73 offen lds\n"
    "v_add_u32 v8, 16384, v6\n"
    "ds_read_b64_tr_b16 a[68:69], v8 offset:0\n"
    "ds_read_b64_tr_b16 a[70:71], v8 offset:512\n"
    "ds_read_b64_tr_b16 a[76:77], v8 offset:8\n"
    "ds_read_b64_tr_b16 a[78:79], v8 offset:520\n"
    "ds_read_b64_tr_b16 a[212:213], v8 offset:8192\n"
    "ds_read_b64_tr_b16 a[214:215], v8 offset:8704\n"
    "ds_read_b64_tr_b16 a[220:221], v8 offset:8200\n"
    "ds_read_b64_tr_b16 a[222:223], v8 offset:8712\n"
    "s_waitcnt vmcnt(28) lgkmcnt(0)\n"
    "s_barrier\n"
    "s_add_u32 m0, s60, 16384\n"
    "s_mov_b32 s73, 0\n"
    "buffer_load_dwordx4 v4, s[48:51], s73 offen lds\n"
    "s_add_u32 m0, s60, 20480\n"
    "s_mov_b32 s73, 128\n"
    "buffer_load_dwordx4 v4, s[48:51], s73 offen lds\n"
    "s_add_u32 m0, s60, 24576\n"
    "s_mov_b32 s73, 262144\n"
    "buffer_load_dwordx4 v4, s[48:51], s73 offen lds\n"
    "s_add_u32 m0, s60, 28672\n"
    "s_mov_b32 s73, 262272\n"
    "buffer_load_dwordx4 v4, s[48:51], s73 offen lds\n"
    "v_add_u32 v8, 32768, v6\n"
    "ds_read_b64_tr_b16 a[80:81], v8 offset:0\n"
    "ds_read_b64_tr_b16 a[82:83], v8 offset:512\n"
    "ds_read_b64_tr_b16 a[88:89], v8 offset:8\n"
    "ds_read_b64_tr_b16 a[90:91], v8 offset:520\n"
    "ds_read_b64_tr_b16 a[224:225], v8 offset:8192\n"
    "ds_read_b64_tr_b16 a[226:227], v8 offset:8704\n"
    "ds_read_b64_tr_b16 a[232:233], v8 offset:8200\n"
    "ds_read_b64_tr_b16 a[234:235], v8 offset:8712\n"
    "s_waitcnt vmcnt(28) lgkmcnt(0)\n"
    "s_barrier\n"
    "s_add_u32 m0, s60, 32768\n"
    "s_mov_b32 s73, 524288\n"
    "buffer_load_dwordx4 v4, s[48:51], s73 offen lds\n"
    "s_add_u32 m0, s60, 36864\n"
    "s_mov_b32 s73, 524416\n"
    "buffer_load_dwordx4 v4, s[48:51], s73 offen lds\n"
    "s_add_u32 m0, s60, 40960\n"
    "s_mov_b32 s73, 786432\n"
    "buffer_load_dwordx4 v4, s[48:51], s73 offen lds\n"
    "s_add_u32 m0, s60, 45056\n"
    "s_mov_b32 s73, 786560\n"
    "buffer_load_dwordx4 v4, s[48:51], s73 offen lds\n"
    "v_add_u32 v8, 49152, v6\n"
    "ds_read_b64_tr_b16 a[84:85], v8 offset:0\n"
    "ds_read_b64_tr_b16 a[86:87], v8 offset:512\n"
    "ds_read_b64_tr_b16 a[92:93], v8 offset:8\n"
    "ds_read_b64_tr_b16 a[94:95], v8 offset:520\n"
    "ds_read_b64_tr_b16 a[228:229], v8 offset:8192\n"
    "ds_read_b64_tr_b16 a[230:231], v8 offset:8704\n"
    "ds_read_b64_tr_b16 a[236:237], v8 offset:8200\n"
    "ds_read_b64_tr_b16 a[238:239], v8 offset:8712\n"
    "s_waitcnt vmcnt(28) lgkmcnt(0)\n"
    "s_barrier\n"
    "s_add_u32 m0, s60, 49152\n"
    "s_mov_b32 s73, 1048576\n"
    "buffer_load_dwordx4 v4, s[48:51], s73 offen lds\n"
    "s_add_u32 m0, s60, 53248\n"
    "s_mov_b32 s73, 1048704\n"
    "buffer_load_dwordx4 v4, s[48:51], s73 offen lds\n"
    "s_add_u32 m0, s60, 57344\n"
    "s_mov_b32 s73, 1310720\n"
    "buffer_load_dwordx4 v4, s[48:51], s73 offen lds\n"
    "s_add_u32 m0, s60, 61440\n"
    "s_mov_b32 s73, 1310848\n"
    "buffer_load_dwordx4 v4, s[48:51], s73 offen lds\n"
    "v_add_u32 v8, 65536, v6\n"
    "ds_read_b64_tr_b16 a[96:97], v8 offset:0\n"
    "ds_read_b64_tr_b16 a[98:99], v8 offset:512\n"
    "ds_read_b64_tr_b16 a[104:105], v8 offset:8\n"
    "ds_read_b64_tr_b16 a[106:107], v8 offset:520\n"
    "ds_read_b64_tr_b16 a[240:241], v8 offset:8192\n"
    "ds_read_b64_tr_b16 a[242:243], v8 offset:8704\n"
    "ds_read_b64_tr_b16 a[248:249], v8 offset:8200\n"
    "ds_read_b64_tr_b16 a[250:251], v8 offset:8712\n"
    "s_waitcnt vmcnt(28) lgkmcnt(0)\n"
    "s_barrier\n"
    "s_add_u32 m0, s60, 65536\n"
    "s_mov_b32 s73, 1572864\n"
    "buffer_load_dwordx4 v4, s[48:51], s73 offen lds\n"
    "s_add_u32 m0, s60, 69632\n"
    "s_mov_b32 s73, 1572992\n"
    "buffer_load_dwordx4 v4, s[48:51], s73 offen lds\n"
    "s_add_u32 m0, s60, 73728\n"
    "s_mov_b32 s73, 1835008\n"
    "buffer_load_dwordx4 v4, s[48:51], s73 offen lds\n"
    "s_add_u32 m0, s60, 77824\n"
    "s_mov_b32 s73, 1835136\n"
    "buffer_load_dwordx4 v4, s[48:51], s73 offen lds\n"
    "v_add_u32 v8, 81920, v6\n"
    "ds_read_b64_tr_b16 a[100:101], v8 offset:0\n"
    "ds_read_b64_tr_b16 a[102:103], v8 offset:512\n"
    "ds_read_b64_tr_b16 a[108:109], v8 offset:8\n"
    "ds_read_b64_tr_b16 a[110:111], v8 offset:520\n"
    "ds_read_b64_tr_b16 a[244:245], v8 offset:8192\n"
    "ds_read_b64_tr_b16 a[246:247], v8 offset:8704\n"
    "ds_read_b64_tr_b16 a[252:253], v8 offset:8200\n"
    "ds_read_b64_tr_b16 a[254:255], v8 offset:8712\n"
    "s_waitcnt vmcnt(28) lgkmcnt(0)\n"
    "s_barrier\n"
    "s_add_u32 m0, s60, 81920\n"
    "s_mov_b32 s73, 2097152\n"
    "buffer_load_dwordx4 v4, s[48:51], s73 offen lds\n"
    "s_add_u32 m0, s60, 86016\n"
    "s_mov_b32 s73, 2097280\n"
    "buffer_load_dwordx4 v4, s[48:51], s73 offen lds\n"
    "s_add_u32 m0, s60, 90112\n"
    "s_mov_b32 s73, 2359296\n"
    "buffer_load_dwordx4 v4, s[48:51], s73 offen lds\n"
    "s_add_u32 m0, s60, 94208\n"
    "s_mov_b32 s73, 2359424\n"
    "buffer_load_dwordx4 v4, s[48:51], s73 offen lds\n"
    "v_add_u32 v8, 98304, v6\n"
    "ds_read_b64_tr_b16 a[112:113], v8 offset:0\n"
    "ds_read_b64_tr_b16 a[114:115], v8 offset:512\n"
    "ds_read_b64_tr_b16 a[120:121], v8 offset:8\n"
    "ds_read_b64_tr_b16 a[122:123], v8 offset:520\n"
    "ds_read_b64_tr_b16 v[80:81], v8 offset:8192\n"
    "ds_read_b64_tr_b16 v[82:83], v8 offset:8704\n"
    "ds_read_b64_tr_b16 v[88:89], v8 offset:8200\n"
    "ds_read_b64_tr_b16 v[90:91], v8 offset:8712\n"
    "s_waitcnt vmcnt(28) lgkmcnt(0)\n"
    "s_barrier\n"
    "s_add_u32 m0, s60, 98304\n"
    "s_mov_b32 s73, 2621440\n"
    "buffer_load_dwordx4 v4, s[48:51], s73 offen lds\n"
    "s_add_u32 m0, s60, 102400\n"
    "s_mov_b32 s73, 2621568\n"
    "buffer_load_dwordx4 v4, s[48:51], s73 offen lds\n"
    "s_add_u32 m0, s60, 106496\n"
    "s_mov_b32 s73, 2883584\n"
    "buffer_load_dwordx4 v4, s[48:51], s73 offen lds\n"
    "s_add_u32 m0, s60, 110592\n"
    "s_mov_b32 s73, 2883712\n"
    "buffer_load_dwordx4 v4, s[48:51], s73 offen lds\n"
    "v_add_u32 v8, 114688, v6\n"
    "ds_read_b64_tr_b16 a[116:117], v8 offset:0\n"
    "ds_read_b64_tr_b16 a[118:119], v8 offset:512\n"
    "ds_read_b64_tr_b16 a[124:125], v8 offset:8\n"
    "ds_read_b64_tr_b16 a[126:127], v8 offset:520\n"
    "ds_read_b64_tr_b16 v[84:85], v8 offset:8192\n"
    "ds_read_b64_tr_b16 v[86:87], v8 offset:8704\n"
    "ds_read_b64_tr_b16 v[92:93], v8 offset:8200\n"
    "ds_read_b64_tr_b16 v[94:95], v8 offset:8712\n"
    "s_waitcnt vmcnt(28) lgkmcnt(0)\n"
    "s_barrier\n"
    "s_add_u32 m0, s60, 114688\n"
    "s_mov_b32 s73, 3145728\n"
    "buffer_load_dwordx4 v4, s[48:51], s73 offen lds\n"
    "s_add_u32 m0, s60, 118784\n"
    "s_mov_b32 s73, 3145856\n"
    "buffer_load_dwordx4 v4, s[48:51], s73 offen lds\n"
    "s_add_u32 m0, s60, 122880\n"
    "s_mov_b32 s73, 3407872\n"
    "buffer_load_dwordx4 v4, s[48:51], s73 offen lds\n"
    "s_add_u32 m0, s60, 126976\n"
    "s_mov_b32 s73, 3408000\n"
    "buffer_load_dwordx4 v4, s[48:51], s73 offen lds\n"
    "v_add_u32 v8, 131072, v6\n"
    "ds_read_b64_tr_b16 a[128:129], v8 offset:0\n"
    "ds_read_b64_tr_b16 a[130:131], v8 offset:512\n"
    "ds_read_b64_tr_b16 a[136:137], v8 offset:8\n"
    "ds_read_b64_tr_b16 a[138:139], v8 offset:520\n"
    "ds_read_b64_tr_b16 v[96:97], v8 offset:8192\n"
    "ds_read_b64_tr_b16 v[98:99], v8 offset:8704\n"
    "ds_read_b64_tr_b16 v[104:105], v8 offset:8200\n"
    "ds_read_b64_tr_b16 v[106:107], v8 offset:8712\n"
    "s_waitcnt vmcnt(28) lgkmcnt(0)\n"
    "s_barrier\n"
    "s_add_u32 m0, s60, 131072\n"
    "s_mov_b32 s73, 3670016\n"
    "buffer_load_dwordx4 v4, s[48:51], s73 offen lds\n"
    "s_add_u32 m0, s60, 135168\n"
    "s_mov_b32 s73, 3670144\n"
    "buffer_load_dwordx4 v4, s[48:51], s73 offen lds\n"
    "s_add_u32 m0, s60, 139264\n"
    "s_mov_b32 s73, 3932160\n"
    "buffer_load_dwordx4 v4, s[48:51], s73 offen lds\n"
    "s_add_u32 m0, s60, 143360\n"
    "s_mov_b32 s73, 3932288\n"
    "buffer_load_dwordx4 v4, s[48:51], s73 offen lds\n"
    "v_add_u32 v8, 147456, v6\n"
    "ds_read_b64_tr_b16 a[132:133], v8 offset:0\n"
    "ds_read_b64_tr_b16 a[134:135], v8 offset:512\n"
    "ds_read_b64_tr_b16 a[140:141], v8 offset:8\n"
    "ds_read_b64_tr_b16 a[142:143], v8 offset:520\n"
    "ds_read_b64_tr_b16 v[100:101], v8 offset:8192\n"
    "ds_read_b64_tr_b16 v[102:103], v8 offset:8704\n"
    "ds_read_b64_tr_b16 v[108:109], v8 offset:8200\n"
    "ds_read_b64_tr_b16 v[110:111], v8 offset:8712\n"
    "s_waitcnt vmcnt(28) lgkmcnt(0)\n"
    "s_barrier\n"
    "s_add_u32 m0, s60, 147456\n"
    "s_mov_b32 s73, 4194304\n"
    "buffer_load_dwordx4 v4, s[48:51], s73 offen lds\n"
    "s_add_u32 m0, s60, 151552\n"
    "s_mov_b32 s73, 4194432\n"
    "buffer_load_dwordx4 v4, s[48:51], s73 offen lds\n"
    "s_add_u32 m0, s60, 155648\n"
    "s_mov_b32 s73, 4456448\n"
    "buffer_load_dwordx4 v4, s[48:51], s73 offen lds\n"
    "s_add_u32 m0, s60, 159744\n"
    "s_mov_b32 s73, 4456576\n"
    "buffer_load_dwordx4 v4, s[48:51], s73 offen lds\n"
    "v_add_u32 v8, 16384, v6\n"
    "ds_read_b64_tr_b16 v[16:17], v8 offset:0\n"
    "ds_read_b64_tr_b16 v[18:19], v8 offset:512\n"
    "ds_read_b64_tr_b16 v[24:25], v8 offset:8\n"
    "ds_read_b64_tr_b16 v[26:27], v8 offset:520\n"
    "ds_read_b64_tr_b16 v[20:21], v8 offset:8192\n"
    "ds_read_b64_tr_b16 v[22:23], v8 offset:8704\n"
    "ds_read_b64_tr_b16 v[28:29], v8 offset:8200\n"
    "ds_read_b64_tr_b16 v[30:31], v8 offset:8712\n"
    "s_waitcnt vmcnt(28) lgkmcnt(0)\n"
    "s_barrier\n"
    "v_add_u32 v14, 0, v1\n"
    "ds_write_b128 v14, v[112:115] offset:0\n"
    "ds_write_b128 v14, v[116:119] offset:1024\n"
    "ds_write_b128 v14, v[24:27] offset:8192\n"
    "ds_write_b128 v14, v[28:31] offset:9216\n"
    "v_add_u32 v8, 32768, v6\n"
    "ds_read_b64_tr_b16 v[32:33], v8 offset:0\n"
    "ds_read_b64_tr_b16 v[34:35], v8 offset:512\n"
    "ds_read_b64_tr_b16 v[40:41], v8 offset:8\n"
    "ds_read_b64_tr_b16 v[42:43], v8 offset:520\n"
    "ds_read_b64_tr_b16 v[36:37], v8 offset:8192\n"
    "ds_read_b64_tr_b16 v[38:39], v8 offset:8704\n"
    "ds_read_b64_tr_b16 v[44:45], v8 offset:8200\n"
    "ds_read_b64_tr_b16 v[46:47], v8 offset:8712\n"
    "s_waitcnt vmcnt(24) lgkmcnt(0)\n"
    "s_barrier\n"
    "v_mov_b64 v[112:113], v[16:17]\n"
    "v_mov_b64 v[114:115], v[18:19]\n"
    "v_mov_b64 v[116:117], v[20:21]\n"
    "v_mov_b64 v[118:119], v[22:23]\n"
    "v_add_u32 v14, 16384, v1\n"
    "ds_write_b128 v14, v[120:123] offset:0\n"
    "ds_write_b128 v14, v[124:127] offset:1024\n"
    "ds_write_b128 v14, v[40:43] offset:8192\n"
    "ds_write_b128 v14, v[44:47] offset:9216\n"
    "v_add_u32 v8, 49152, v6\n"
    "ds_read_b64_tr_b16 v[16:17], v8 offset:0\n"
    "ds_read_b64_tr_b16 v[18:19], v8 offset:512\n"
    "ds_read_b64_tr_b16 v[24:25], v8 offset:8\n"
    "ds_read_b64_tr_b16 v[26:27], v8 offset:520\n"
    "ds_read_b64_tr_b16 v[20:21], v8 offset:8192\n"
    "ds_read_b64_tr_b16 v[22:23], v8 offset:8704\n"
    "ds_read_b64_tr_b16 v[28:29], v8 offset:8200\n"
    "ds_read_b64_tr_b16 v[30:31], v8 offset:8712\n"
    "s_waitcnt vmcnt(20) lgkmcnt(0)\n"
    "s_barrier\n"
    "v_mov_b64 v[120:121], v[32:33]\n"
    "v_mov_b64 v[122:123], v[34:35]\n"
    "v_mov_b64 v[124:125], v[36:37]\n"
    "v_mov_b64 v[126:127], v[38:39]\n"
    "v_add_u32 v14, 32768, v1\n"
    "ds_write_b128 v14, v[128:131] offset:0\n"
    "ds_write_b128 v14, v[132:135] offset:1024\n"
    "ds_write_b128 v14, v[24:27] offset:8192\n"
    "ds_write_b128 v14, v[28:31] offset:9216\n"
    "v_add_u32 v8, 65536, v6\n"
    "ds_read_b64_tr_b16 v[32:33], v8 offset:0\n"
    "ds_read_b64_tr_b16 v[34:35], v8 offset:512\n"
    "ds_read_b64_tr_b16 v[40:41], v8 offset:8\n"
    "ds_read_b64_tr_b16 v[42:43], v8 offset:520\n"
    "ds_read_b64_tr_b16 v[36:37], v8 offset:8192\n"
    "ds_read_b64_tr_b16 v[38:39], v8 offset:8704\n"
    "ds_read_b64_tr_b16 v[44:45], v8 offset:8200\n"
    "ds_read_b64_tr_b16 v[46:47], v8 offset:8712\n"
    "s_waitcnt vmcnt(16) lgkmcnt(0)\n"
    "s_barrier\n"
    "v_mov_b64 v[128:129], v[16:17]\n"
    "v_mov_b64 v[130:131], v[18:19]\n"
    "v_mov_b64 v[132:133], v[20:21]\n"
    "v_mov_b64 v[134:135], v[22:23]\n"
    "v_add_u32 v14, 49152, v1\n"
    "ds_write_b128 v14, v[136:139] offset:0\n"
    "ds_write_b128 v14, v[140:143] offset:1024\n"
    "ds_write_b128 v14, v[40:43] offset:8192\n"
    "ds_write_b128 v14, v[44:47] offset:9216\n"
    "v_add_u32 v8, 81920, v6\n"
    "ds_read_b64_tr_b16 v[16:17], v8 offset:0\n"
    "ds_read_b64_tr_b16 v[18:19], v8 offset:512\n"
    "ds_read_b64_tr_b16 v[24:25], v8 offset:8\n"
    "ds_read_b64_tr_b16 v[26:27], v8 offset:520\n"
    "ds_read_b64_tr_b16 v[20:21], v8 offset:8192\n"
    "ds_read_b64_tr_b16 v[22:23], v8 offset:8704\n"
    "ds_read_b64_tr_b16 v[28:29], v8 offset:8200\n"
    "ds_read_b64_tr_b16 v[30:31], v8 offset:8712\n"
    "s_waitcnt vmcnt(12) lgkmcnt(0)\n"
    "s_barrier\n"
    "v_mov_b64 v[136:137], v[32:33]\n"
    "v_mov_b64 v[138:139], v[34:35]\n"
    "v_mov_b64 v[140:141], v[36:37]\n"
    "v_mov_b64 v[142:143], v[38:39]\n"
    "v_add_u32 v14, 65536, v1\n"
    "ds_write_b128 v14, v[144:147] offset:0\n"
    "ds_write_b128 v14, v[148:151] offset:1024\n"
    "ds_write_b128 v14, v[24:27] offset:8192\n"
    "ds_write_b128 v14, v[28:31] offset:9216\n"
    "v_add_u32 v8, 98304, v6\n"
    "ds_read_b64_tr_b16 v[32:33], v8 offset:0\n"
    "ds_read_b64_tr_b16 v[34:35], v8 offset:512\n"
    "ds_read_b64_tr_b16 v[40:41], v8 offset:8\n"
    "ds_read_b64_tr_b16 v[42:43], v8 offset:520\n"
    "ds_read_b64_tr_b16 v[36:37], v8 offset:8192\n"
    "ds_read_b64_tr_b16 v[38:39], v8 offset:8704\n"
    "ds_read_b64_tr_b16 v[44:45], v8 offset:8200\n"
    "ds_read_b64_tr_b16 v[46:47], v8 offset:8712\n"
    "s_waitcnt vmcnt(8) lgkmcnt(0)\n"
    "s_barrier\n"
    "v_mov_b64 v[144:145], v[16:17]\n"
    "v_mov_b64 v[146:147], v[18:19]\n"
    "v_mov_b64 v[148:149], v[20:21]\n"
    "v_mov_b64 v[150:151], v[22:23]\n"
    "v_add_u32 v14, 81920, v1\n"
    "ds_write_b128 v14, v[152:155] offset:0\n"
    "ds_write_b128 v14, v[156:159] offset:1024\n"
    "ds_write_b128 v14, v[40:43] offset:8192\n"
    "ds_write_b128 v14, v[44:47] offset:9216\n"
    "v_add_u32 v8, 114688, v6\n"
    "ds_read_b64_tr_b16 v[16:17], v8 offset:0\n"
    "ds_read_b64_tr_b16 v[18:19], v8 offset:512\n"
    "ds_read_b64_tr_b16 v[24:25], v8 offset:8\n"
    "ds_read_b64_tr_b16 v[26:27], v8 offset:520\n"
    "ds_read_b64_tr_b16 v[20:21], v8 offset:8192\n"
    "ds_read_b64_tr_b16 v[22:23], v8 offset:8704\n"
    "ds_read_b64_tr_b16 v[28:29], v8 offset:8200\n"
    "ds_read_b64_tr_b16 v[30:31], v8 offset:8712\n"
    "s_waitcnt vmcnt(4) lgkmcnt(0)\n"
    "s_barrier\n"
    "v_mov_b64 v[152:153], v[32:33]\n"
    "v_mov_b64 v[154:155], v[34:35]\n"
    "v_mov_b64 v[156:157], v[36:37]\n"
    "v_mov_b64 v[158:159], v[38:39]\n"
    "v_add_u32 v14, 98304, v1\n"
    "ds_write_b128 v14, v[160:163] offset:0\n"
    "ds_write_b128 v14, v[164:167] offset:1024\n"
    "ds_write_b128 v14, v[168:171] offset:8192\n"
    "ds_write_b128 v14, v[172:175] offset:9216\n"
    "v_add_u32 v8, 131072, v6\n"
    "ds_read_b64_tr_b16 v[32:33], v8 offset:0\n"
    "ds_read_b64_tr_b16 v[34:35], v8 offset:512\n"
    "ds_read_b64_tr_b16 v[40:41], v8 offset:8\n"
    "ds_read_b64_tr_b16 v[42:43], v8 offset:520\n"
    "ds_read_b64_tr_b16 v[36:37], v8 offset:8192\n"
    "ds_read_b64_tr_b16 v[38:39], v8 offset:8704\n"
    "ds_read_b64_tr_b16 v[44:45], v8 offset:8200\n"
    "ds_read_b64_tr_b16 v[46:47], v8 offset:8712\n"
    "s_waitcnt vmcnt(0) lgkmcnt(0)\n"
    "s_barrier\n"
    "v_mov_b64 v[160:161], v[16:17]\n"
    "v_mov_b64 v[162:163], v[18:19]\n"
    "v_mov_b64 v[164:165], v[20:21]\n"
    "v_mov_b64 v[166:167], v[22:23]\n"
    "v_mov_b64 v[168:169], v[24:25]\n"
    "v_mov_b64 v[170:171], v[26:27]\n"
    "v_mov_b64 v[172:173], v[28:29]\n"
    "v_mov_b64 v[174:175], v[30:31]\n"
    "v_add_u32 v14, 114688, v1\n"
    "ds_write_b128 v14, v[176:179] offset:0\n"
    "ds_write_b128 v14, v[180:183] offset:1024\n"
    "ds_write_b128 v14, v[184:187] offset:8192\n"
    "ds_write_b128 v14, v[188:191] offset:9216\n"
    "v_add_u32 v8, 147456, v6\n"
    "ds_read_b64_tr_b16 v[16:17], v8 offset:0\n"
    "ds_read_b64_tr_b16 v[18:19], v8 offset:512\n"
    "ds_read_b64_tr_b16 v[24:25], v8 offset:8\n"
    "ds_read_b64_tr_b16 v[26:27], v8 offset:520\n"
    "ds_read_b64_tr_b16 v[20:21], v8 offset:8192\n"
    "ds_read_b64_tr_b16 v[22:23], v8 offset:8704\n"
    "ds_read_b64_tr_b16 v[28:29], v8 offset:8200\n"
    "ds_read_b64_tr_b16 v[30:31], v8 offset:8712\n"
    "s_waitcnt lgkmcnt(0)\n"
    "s_barrier\n"
    "v_mov_b64 v[176:177], v[32:33]\n"
    "v_mov_b64 v[178:179], v[34:35]\n"
    "v_mov_b64 v[180:181], v[36:37]\n"
    "v_mov_b64 v[182:183], v[38:39]\n"
    "v_mov_b64 v[184:185], v[40:41]\n"
    "v_mov_b64 v[186:187], v[42:43]\n"
    "v_mov_b64 v[188:189], v[44:45]\n"
    "v_mov_b64 v[190:191], v[46:47]\n"
    "v_add_u32 v14, 131072, v1\n"
    "ds_write_b128 v14, v[192:195] offset:0\n"
    "ds_write_b128 v14, v[196:199] offset:1024\n"
    "ds_write_b128 v14, v[200:203] offset:8192\n"
    "ds_write_b128 v14, v[204:207] offset:9216\n"
    "s_waitcnt lgkmcnt(0)\n"
    "v_mov_b64 v[192:193], v[16:17]\n"
    "v_mov_b64 v[194:195], v[18:19]\n"
    "v_mov_b64 v[196:197], v[20:21]\n"
    "v_mov_b64 v[198:199], v[22:23]\n"
    "v_mov_b64 v[200:201], v[24:25]\n"
    "v_mov_b64 v[202:203], v[26:27]\n"
    "v_mov_b64 v[204:205], v[28:29]\n"
    "v_mov_b64 v[206:207], v[30:31]\n"
    "s_mov_b32 s69, 262144\n"
    ".Lrec_par_%=:\n"
    "buffer_load_dword v9, off, s[36:39], 0 sc1\n"
    "s_waitcnt vmcnt(0)\n"
    "v_readfirstlane_b32 s73, v9\n"
    "s_nop 0\n"
    "s_cmp_ge_i32 s73, 0\n"
    "s_cbranch_scc1 .Lrec_par1_%=\n"
    "s_sleep 4\n"
    "s_sub_u32 s69, s69, 1\n"
    "s_cbranch_scc0 .Lrec_par_%=\n"
    "s_mov_b32 s69, 0\n"
    ".Lrec_par1_%=:\n"
    "s_and_b32 s73, s73, 1\n"
    "s_lshl_b32 s73, s73, 13\n"
    "s_add_u32 s36, s36, s73\n"
    "s_addc_u32 s37, s37, 0\n"
    "s_mov_b32 s84, 0x3e0293ee\n"
    "v_lshlrev_b32 v8, 3, v0\n"
    "v_mov_b32 v9, 0x80000000\n"
    "v_cmp_gt_u32 vcc, 192, v0\n"
    "s_nop 1\n"
    "v_cndmask_b32 v8, v9, v8, vcc\n"
    ".Lrec_init_%=:\n"
    "buffer_load_dword v9, v8, s[36:39], s62 offen sc1\n"
    "s_waitcnt vmcnt(0)\n"
    "v_cmp_gt_i32 vcc, 0, v9\n"
    "s_cbranch_vccz .Lrec_init1_%=\n"
    "s_sleep 2\n"
    "s_sub_u32 s69, s69, 1\n"
    "s_cbranch_scc0 .Lrec_init_%=\n"
    "s_mov_b32 s69, 0\n"
    ".Lrec_init1_%=:\n"
    "v_readfirstlane_b32 s40, v48\n"
    "v_readfirstlane_b32 s41, v49\n"
    "v_readfirstlane_b32 s42, v50\n"
    "v_readfirstlane_b32 s43, v51\n"
    "v_readfirstlane_b32 s44, v52\n"
    "v_readfirstlane_b32 s45, v53\n"
    "s_mov_b32 s82, 1179648\n"
    "s_mov_b32 s48, 1331\n"
    "s_mov_b32 s49, 278\n"
    "s_mov_b32 s46, 0\n"
    "s_mov_b32 s47, 0\n"
    "s_mov_b32 s68, -1\n"
    "s_mov_b32 s81, 0\n"
    "s_mov_b32 s50, s82\n"
    "s_add_u32 s82, s82, 589824\n"
    "s_cmp_eq_u32 s82, 1769472\n"
    "s_cselect_b32 s82, 0, s82\n"
    "s_and_b32 s80, s68, 3\n"
    "s_lshl_b32 s80, s80, 17\n"
    "s_and_b32 s80, s80, s45\n"
    "s_and_b32 s91, s50, s44\n"
    "s_add_u32 s80, s80, s91\n"
    "s_add_u32 s80, s80, s43\n"
    "s_add_u32 s89, s42, s50\n"
    "s_add_u32 s68, s68, 1\n"
    "s_and_b32 s70, s68, 3\n"
    "s_lshl_b32 s70, s70, 17\n"
    "s_add_u32 s70, s70, s66\n"
    "s_add_u32 s71, s70, 4096\n"
    "s_add_u32 s72, s70, 16384\n"
    "s_lshr_b32 s90, s68, 2\n"
    "s_lshl_b32 s90, s90, 14\n"
    "s_and_b32 s91, s68, 3\n"
    "s_lshl_b32 s91, s91, 11\n"
    "s_add_u32 s90, s90, s91\n"
    "s_add_u32 s90, s90, s40\n"
    "s_add_u32 s51, s90, s41\n"
    "s_and_b32 s76, s68, 7\n"
    "s_mul_i32 s76, s76, 4194304\n"
    "s_add_u32 s83, s76, s56\n"
    "s_add_u32 s83, s83, 16384\n"
    "s_add_u32 s76, s76, s57\n"
    "s_add_u32 s77, s63, s82\n"
    "s_add_u32 s86, s77, 65536\n"
    "s_add_u32 s87, s67, s82\n"
    "v_mov_b64 v[40:41], 0\n"
    "v_mov_b64 v[42:43], 0\n"
    "v_mov_b64 v[44:45], 0\n"
    "v_mov_b64 v[46:47], 0\n"
    "v_mov_b64 v[48:49], 0\n"
    "v_mov_b64 v[50:51], 0\n"
    "v_mov_b64 v[52:53], 0\n"
    "v_mov_b64 v[54:55], 0\n"
    "v_mov_b64 v[56:57], 0\n"
    "v_mov_b64 v[58:59], 0\n"
    "v_mov_b64 v[60:61], 0\n"
    "v_mov_b64 v[62:63], 0\n"
    "s_waitcnt lgkmcnt(0)\n"
    "s_barrier\n"
    "s_branch .Lrec_entry_%=\n"
    ".Lrec_LOOPB_%=:\n"
    ".Lrec_top_%=:\n"
    "buffer_load_dwordx4 v[4:7], v0, s[36:39], s70 offen sc1\n"
    "s_nop 3\n"
    "buffer_load_dwordx4 v[8:11], v0, s[36:39], s70 offen offset:1024 sc1\n"
    "s_nop 3\n"
    "buffer_load_dwordx4 v[12:15], v0, s[36:39], s70 offen offset:2048 sc1\n"
    "s_nop 3\n"
    "buffer_load_dwordx4 v[16:19], v0, s[36:39], s70 offen offset:3072 sc1\n"
    "s_nop 3\n"
    "buffer_load_dwordx4 v[20:23], v0, s[36:39], s71 offen sc1\n"
    "s_nop 3\n"
    "buffer_load_dwordx4 v[24:27], v0, s[36:39], s71 offen offset:1024 sc1\n"
    "s_nop 3\n"
    "buffer_load_dwordx4 v[28:31], v0, s[36:39], s71 offen offset:2048 sc1\n"
    "s_nop 3\n"
    "buffer_load_dwordx4 v[32:35], v0, s[36:39], s71 offen offset:3072 sc1\n"
    "s_sleep 1\n"
    "buffer_load_dwordx4 v[36:39], v0, s[36:39], s72 offen sc1\n"
    "s_waitcnt vmcnt(8)\n"
    ".Lrec_start_%=:\n"
    "v_smfmac_f32_16x16x64_bf16 v[40:43], v[4:7], a[0:7], v2\n"
    "v_max3_u32 v3, v4, v5, v6\n"
    "v_smfmac_f32_16x16x64_bf16 v[44:47], v[4:7], a[8:15], v2\n"
    "v_max_u32 v3, v3, v7\n"
    "v_smfmac_f32_16x16x64_bf16 v[48:51], v[4:7], a[144:151], v2\n"
    "v_cmp_eq_u32 vcc, -1, v3\n"
    "v_smfmac_f32_16x16x64_bf16 v[52:55], v[4:7], a[152:159], v2\n"
    "v_smfmac_f32_16x16x64_bf16 v[56:59], v[4:7], v[112:119], v2\n"
    "s_waitcnt vmcnt(7)\n"
    "s_waitcnt lgkmcnt(0)\n"
    "v_smfmac_f32_16x16x64_bf16 v[60:63], v[4:7], v[72:79], v2\n"
    "ds_read_b128 v[72:75], v1 offset:24576\n"
    "v_smfmac_f32_16x16x64_bf16 v[40:43], v[8:11], a[16:23], v2\n"
    "ds_read_b128 v[76:79], v1 offset:25600\n"
    "v_max3_u32 v3, v3, v8, v9\n"
    "v_smfmac_f32_16x16x64_bf16 v[44:47], v[8:11], a[24:31], v2\n"
    "s_cbranch_vccnz .Lrec_retry0_%=\n"
    ".Lrec_back0_%=:\n"
    "v_max3_u32 v3, v3, v10, v11\n"
    "v_smfmac_f32_16x16x64_bf16 v[48:51], v[8:11], a[160:167], v2\n"
    "v_smfmac_f32_16x16x64_bf16 v[52:55], v[8:11], a[168:175], v2\n"
    "v_smfmac_f32_16x16x64_bf16 v[56:59], v[8:11], v[120:127], v2\n"
    "s_waitcnt vmcnt(6)\n"
    "s_waitcnt lgkmcnt(0)\n"
    "v_smfmac_f32_16x16x64_bf16 v[60:63], v[8:11], v[72:79], v2\n"
    "ds_read_b128 v[72:75], v1 offset:40960\n"
    "v_smfmac_f32_16x16x64_bf16 v[40:43], v[12:15], a[32:39], v2\n"
    "ds_read_b128 v[76:79], v1 offset:41984\n"
    "v_max3_u32 v3, v3, v12, v13\n"
    "v_smfmac_f32_16x16x64_bf16 v[44:47], v[12:15], a[40:47], v2\n"
    "buffer_load_dwordx4 v[4:7], v0, s[36:39], s83 offen sc1\n"
    "v_max3_u32 v3, v3, v14, v15\n"
    "v_smfmac_f32_16x16x64_bf16 v[48:51], v[12:15], a[176:183], v2\n"
    "v_smfmac_f32_16x16x64_bf16 v[52:55], v[12:15], a[184:191], v2\n"
    "v_smfmac_f32_16x16x64_bf16 v[56:59], v[12:15], v[128:135], v2\n"
    "s_waitcnt vmcnt(6)\n"
    "s_waitcnt lgkmcnt(0)\n"
    "v_max3_u32 v3, v3, v16, v17\n"
    "v_smfmac_f32_16x16x64_bf16 v[60:63], v[12:15], v[72:79], v2\n"
    "ds_read_b128 v[72:75], v1 offset:57344\n"
    "v_max3_u32 v3, v3, v18, v19\n"
    "v_smfmac_f32_16x16x64_bf16 v[40:43], v[16:19], a[48:55], v2\n"
    "ds_read_b128 v[76:79], v1 offset:58368\n"
    "v_cmp_eq_u32 vcc, -1, v3\n"
    "v_smfmac_f32_16x16x64_bf16 v[44:47], v[16:19], a[56:63], v2\n"
    "v_add_u32 v1, 65536, v1\n"
    "v_smfmac_f32_16x16x64_bf16 v[48:51], v[16:19], a[192:199], v2\n"
    "s_cbranch_vccnz .Lrec_retry1_%=\n"
    ".Lrec_back1_%=:\n"
    "v_smfmac_f32_16x16x64_bf16 v[52:55], v[16:19], a[200:207], v2\n"
    "s_waitcnt vmcnt(5)\n"
    "s_waitcnt lgkmcnt(0)\n"
    "v_max3_u32 v3, v3, v20, v21\n"
    "v_smfmac_f32_16x16x64_bf16 v[60:63], v[16:19], v[72:79], v2\n"
    "ds_read_b128 v[72:75], v1 offset:8192\n"
    "v_max3_u32 v3, v3, v22, v23\n"
    "v_smfmac_f32_16x16x64_bf16 v[40:43], v[20:23], a[64:71], v2\n"
    "ds_read_b128 v[76:79], v1 offset:9216\n"
    "v_smfmac_f32_16x16x64_bf16 v[44:47], v[20:23], a[72:79], v2\n"
    "ds_read_b128 v[12:15], v0 offset:12288\n"
    "v_smfmac_f32_16x16x64_bf16 v[48:51], v[20:23], a[208:215], v2\n"
    "buffer_load_dwordx4 v[8:11], v0, s[36:39], s83 offen offset:1024 sc1\n"
    "v_smfmac_f32_16x16x64_bf16 v[52:55], v[20:23], a[216:223], v2\n"
    "s_waitcnt vmcnt(5)\n"
    "s_waitcnt lgkmcnt(1)\n"
    "v_max3_u32 v3, v3, v24, v25\n"
    "v_smfmac_f32_16x16x64_bf16 v[60:63], v[20:23], v[72:79], v2\n"
    "ds_read_b128 v[72:75], v1 offset:24576\n"
    "v_max3_u32 v3, v3, v26, v27\n"
    "v_smfmac_f32_16x16x64_bf16 v[40:43], v[24:27], a[80:87], v2\n"
    "ds_read_b128 v[76:79], v1 offset:25600\n"
    "v_subrev_u32 v1, 65536, v1\n"
    "v_smfmac_f32_16x16x64_bf16 v[44:47], v[24:27], a[88:95], v2\n"
    "s_waitcnt vmcnt(4)\n"
    "v_max3_u32 v3, v3, v28, v29\n"
    "v_smfmac_f32_16x16x64_bf16 v[48:51], v[24:27], a[224:231], v2\n"
    "s_waitcnt vmcnt(3)\n"
    "v_max3_u32 v3, v3, v30, v31\n"
    "v_smfmac_f32_16x16x64_bf16 v[52:55], v[24:27], a[232:239], v2\n"
    "s_waitcnt lgkmcnt(0)\n"
    "v_max3_u32 v3, v3, v32, v33\n"
    "v_smfmac_f32_16x16x64_bf16 v[60:63], v[24:27], v[72:79], v2\n"
    "ds_read_b128 v[72:75], v0 offset:13312\n"
    "v_max3_u32 v3, v3, v34, v35\n"
    "v_smfmac_f32_16x16x64_bf16 v[40:43], v[28:31], a[96:103], v2\n"
    "s_waitcnt vmcnt(2)\n"
    "v_max3_u32 v3, v3, v36, v37\n"
    "v_smfmac_f32_16x16x64_bf16 v[44:47], v[28:31], a[104:111], v2\n"
    "s_lshr_b32 s73, s60, 1\n"
    "v_max3_u32 v3, v3, v38, v39\n"
    "v_smfmac_f32_16x16x64_bf16 v[40:43], v[32:35], a[112:119], v2\n"
    "v_cmp_eq_u32 vcc, -1, v3\n"
    "v_smfmac_f32_16x16x64_bf16 v[44:47], v[32:35], a[120:127], v2\n"
    "s_waitcnt lgkmcnt(0)\n"
    "s_memrealtime s[28:29]\n"
    "v_add_u32 v79, s60, v0\n"
    "v_smfmac_f32_16x16x64_bf16 v[48:51], v[28:31], a[240:247], v2\n"
    "s_cbranch_vccnz .Lrec_retry2_%=\n"
    ".Lrec_back2_%=:\n"
    "v_xor_b32 v76, 1024, v79\n"
    "v_smfmac_f32_16x16x64_bf16 v[40:43], v[36:39], a[128:135], v2\n"
    "s_memrealtime s[30:31]\n"
    "v_xor_b32 v77, 2048, v79\n"
    "v_smfmac_f32_16x16x64_bf16 v[44:47], v[36:39], a[136:143], v2\n"
    "s_memrealtime s[96:97]\n"
    "v_xor_b32 v78, 3072, v79\n"
    "v_smfmac_f32_16x16x64_bf16 v[52:55], v[28:31], a[248:255], v2\n"
    "s_memtime s[32:33]\n"
    "v_cvt_pk_bf16_f32 v40, v40, v41\n"
    "v_smfmac_f32_16x16x64_bf16 v[48:51], v[32:35], v[80:87], v2\n"
    "s_memrealtime s[98:99]\n"
    "v_cvt_pk_bf16_f32 v41, v42, v43\n"
    "v_smfmac_f32_16x16x64_bf16 v[52:55], v[32:35], v[88:95], v2\n"
    "v_cvt_pk_bf16_f32 v42, v44, v45\n"
    "v_smfmac_f32_16x16x64_bf16 v[48:51], v[36:39], v[96:103], v2\n"
    "v_cvt_pk_bf16_f32 v43, v46, v47\n"
    "v_smfmac_f32_16x16x64_bf16 v[52:55], v[36:39], v[104:111], v2\n"
    "v_lshrrev_b32 v3, 1, v79\n"
    "v_mfma_f32_16x16x32_bf16 v[44:47], v[40:43], v[12:15], 0\n"
    "s_waitcnt lgkmcnt(0)\n"
    "v_cvt_pk_bf16_f32 v48, v48, v49\n"
    "v_cvt_pk_bf16_f32 v49, v50, v51\n"
    "s_add_u32 s28, s28, s30\n"
    "v_cvt_pk_bf16_f32 v50, v52, v53\n"
    "v_cvt_pk_bf16_f32 v51, v54, v55\n"
    "s_add_u32 s96, s96, s98\n"
    "s_add_u32 s28, s28, s96\n"
    "v_mfma_f32_16x16x32_bf16 v[52:55], v[48:51], v[12:15], 0\n"
    "s_add_u32 s30, s28, s49\n"
    "v_cvt_pk_bf16_f32 v40, v44, v45\n"
    "v_cvt_pk_bf16_f32 v41, v46, v47\n"
    "v_mov_b32 v75, s30\n"
    "v_mov_b32_dpp v42, v40 row_ror:8 row_mask:0xf bank_mask:0xf\n"
    "v_mov_b32_dpp v43, v41 row_ror:8 row_mask:0xf bank_mask:0xf\n"
    "buffer_store_dword v75, off, s[36:39], s51 sc1\n"
    "s_nop 0\n"
    "v_mfma_f32_16x16x32_bf16 v[44:47], v[68:71], v[40:43], 0\n"
    "v_cvt_pk_bf16_f32 v48, v52, v53\n"
    "v_cvt_pk_bf16_f32 v49, v54, v55\n"
    "v_mfma_f32_16x16x32_bf16 v[68:71], v[64:67], v[40:43], 0\n"
    "v_mov_b32_dpp v50, v48 row_ror:8 row_mask:0xf bank_mask:0xf\n"
    "v_mov_b32_dpp v51, v49 row_ror:8 row_mask:0xf bank_mask:0xf\n"
    "s_nop 1\n"
    "v_mfma_f32_16x16x32_bf16 v[52:55], v[48:51], v[40:43], 0\n"
    "s_nop 2\n"
    "v_mov_b32_dpp v68, v44 row_ror:8 row_mask:0xf bank_mask:0xc\n"
    "v_mov_b32_dpp v69, v45 row_ror:8 row_mask:0xf bank_mask:0xc\n"
    "v_mov_b32_dpp v70, v46 row_ror:8 row_mask:0xf bank_mask:0xc\n"
    "v_mov_b32_dpp v71, v47 row_ror:8 row_mask:0xf bank_mask:0xc\n"
    "ds_write_b128 v79, v[68:71] offset:0\n"
    "v_mov_b32_dpp v52, v54 row_ror:8 row_mask:0xf bank_mask:0xc\n"
    "v_mov_b32_dpp v53, v55 row_ror:8 row_mask:0xf bank_mask:0xc\n"
    "ds_write_b64 v3, v[52:53] offset:4096\n"
    "v_smfmac_f32_16x16x64_bf16 v[56:59], v[16:19], v[136:143], v2\n"
    "buffer_store_dwordx4 v[48:51], v74, s[36:39], s76 offen sc1\n"
    "v_smfmac_f32_16x16x64_bf16 v[56:59], v[20:23], v[144:151], v2\n"
    "ds_read_b128 v[16:19], v1 offset:0\n"
    "v_smfmac_f32_16x16x64_bf16 v[56:59], v[24:27], v[152:159], v2\n"
    "ds_read_b128 v[20:23], v1 offset:1024\n"
    "v_smfmac_f32_16x16x64_bf16 v[56:59], v[28:31], v[160:167], v2\n"
    "s_waitcnt lgkmcnt(2)\n"
    "s_barrier\n"
    "ds_read_b128 v[64:67], v76 offset:0\n"
    "v_smfmac_f32_16x16x64_bf16 v[60:63], v[28:31], v[168:175], v2\n"
    "ds_read_b128 v[40:43], v77 offset:0\n"
    "v_smfmac_f32_16x16x64_bf16 v[56:59], v[32:35], v[176:183], v2\n"
    "ds_read_b128 v[44:47], v78 offset:0\n"
    "v_smfmac_f32_16x16x64_bf16 v[60:63], v[32:35], v[184:191], v2\n"
    "ds_read_b64 v[12:13], v72 offset:4096\n"
    "ds_read_b64 v[14:15], v72 offset:4608\n"
    "v_smfmac_f32_16x16x64_bf16 v[56:59], v[36:39], v[192:199], v2\n"
    "ds_read_b64 v[52:53], v72 offset:5120\n"
    "ds_read_b64 v[54:55], v72 offset:5632\n"
    "v_smfmac_f32_16x16x64_bf16 v[60:63], v[36:39], v[200:207], v2\n"
    "ds_read_b128 v[24:27], v1 offset:16384\n"
    "s_cmp_eq_u32 s68, 63\n"
    "s_waitcnt lgkmcnt(5)\n"
    "v_add_f32 v68, v68, v64\n"
    "v_add_f32 v69, v69, v65\n"
    "v_add_f32 v70, v70, v66\n"
    "v_add_f32 v71, v71, v67\n"
    "v_pk_add_f32 v[40:41], v[40:41], v[44:45]\n"
    "v_pk_add_f32 v[42:43], v[42:43], v[46:47]\n"
    "s_waitcnt lgkmcnt(1)\n"
    "v_pk_add_f32 v[12:13], v[12:13], v[14:15]\n"
    "v_pk_add_f32 v[52:53], v[52:53], v[54:55]\n"
    "v_pk_add_f32 v[68:69], v[68:69], v[40:41]\n"
    "v_pk_add_f32 v[70:71], v[70:71], v[42:43]\n"
    "v_pk_add_f32 v[12:13], v[12:13], v[52:53]\n"
    "v_max3_f32 v46, v68, v69, v70\n"
    "v_max3_f32 v47, v71, v12, v13\n"
    "v_max_f32 v3, v46, v47\n"
    "v_cvt_pk_bf16_f32 v56, v56, v57\n"
    "v_cvt_pk_bf16_f32 v57, v58, v59\n"
    "v_max_f32_dpp v46, v3, v3 row_ror:8 row_mask:0xf bank_mask:0xf\n"
    "v_max_f32_dpp v47, v3, v3 row_ror:8 row_mask:0xf bank_mask:0xf\n"
    "v_cvt_pk_bf16_f32 v58, v60, v61\n"
    "v_cvt_pk_bf16_f32 v59, v62, v63\n"
    "v_permlane16_swap_b32 v46, v47\n"
    "v_max_f32 v3, v46, v47\n"
    "v_max_f32 v46, v46, v47\n"
    "v_add_u32 v14, s73, v73\n"
    "v_xor_b32 v15, 128, v0\n"
    "v_permlane32_swap_b32 v3, v46\n"
    "v_max_f32 v3, v3, v46\n"
    "v_mul_f32 v46, 0xbe0293ee, v3\n"
    "v_pk_fma_f32 v[68:69], v[68:69], s[84:85], v[46:47] op_sel_hi:[1,0,0]\n"
    "v_pk_fma_f32 v[70:71], v[70:71], s[84:85], v[46:47] op_sel_hi:[1,0,0]\n"
    "v_pk_fma_f32 v[12:13], v[12:13], s[84:85], v[46:47] op_sel_hi:[1,0,0]\n"
    "v_exp_f32 v68, v68\n"
    "v_exp_f32 v69, v69\n"
    "v_exp_f32 v70, v70\n"
    "v_exp_f32 v71, v71\n"
    "v_exp_f32 v12, v12\n"
    "v_cvt_pk_bf16_f32 v40, v68, v69\n"
    "v_cvt_pk_bf16_f32 v41, v70, v71\n"
    "v_exp_f32 v13, v13\n"
    "v_mov_b32_dpp v42, v40 row_ror:8 row_mask:0xf bank_mask:0xf\n"
    "v_mov_b32_dpp v43, v41 row_ror:8 row_mask:0xf bank_mask:0xf\n"
    "v_cvt_pk_bf16_f32 v44, v12, v13\n"
    "v_pk_add_f32 v[68:69], v[68:69], v[70:71]\n"
    "s_waitcnt vmcnt(2)\n"
    "v_mfma_f32_16x16x32_bf16 v[48:51], v[4:7], v[40:43], 0\n"
    "ds_read_b128 v[28:31], v1 offset:17408\n"
    "v_mov_b32_dpp v45, v44 row_ror:8 row_mask:0xf bank_mask:0xf\n"
    "v_add_f32 v3, v12, v13\n"
    "v_mfma_f32_16x16x32_bf16 v[52:55], v[8:11], v[40:43], 0\n"
    "s_cbranch_scc1 .Lrec_fin_%=\n"
    "v_add_f32 v46, v68, v69\n"
    "v_fma_f32 v3, v3, 0.5, v46\n"
    "v_mfma_f32_16x16x16_bf16 v[48:51], v[56:57], v[44:45], v[48:51]\n"
    "ds_read_b128 v[32:35], v1 offset:32768\n"
    "v_add_f32_dpp v46, v3, v3 row_ror:8 row_mask:0xf bank_mask:0xf\n"
    "v_add_f32_dpp v47, v3, v3 row_ror:8 row_mask:0xf bank_mask:0xf\n"
    "v_mfma_f32_16x16x16_bf16 v[52:55], v[58:59], v[44:45], v[52:55]\n"
    "ds_read_b128 v[36:39], v1 offset:33792\n"
    "v_permlane16_swap_b32 v46, v47\n"
    "v_add_f32 v3, v46, v47\n"
    "v_add_f32 v46, v46, v47\n"
    "v_mov_b64 v[64:65], 0\n"
    "v_mov_b64 v[66:67], 0\n"
    "v_permlane32_swap_b32 v3, v46\n"
    "v_add_f32 v3, v3, v46\n"
    "v_rcp_f32 v46, v3\n"
    "v_mov_b32_dpp v48, v52 row_ror:8 row_mask:0xf bank_mask:0xc\n"
    "v_mov_b32_dpp v49, v53 row_ror:8 row_mask:0xf bank_mask:0xc\n"
    "v_mov_b32_dpp v50, v54 row_ror:8 row_mask:0xf bank_mask:0xc\n"
    "v_mov_b32_dpp v51, v55 row_ror:8 row_mask:0xf bank_mask:0xc\n"
    "v_pk_mul_f32 v[48:49], v[48:49], v[46:47] op_sel_hi:[1,0]\n"
    "v_pk_mul_f32 v[50:51], v[50:51], v[46:47] op_sel_hi:[1,0]\n"
    "v_cvt_pk_bf16_f32 v48, v48, v49\n"
    "v_cvt_pk_bf16_f32 v49, v50, v51\n"
    "ds_write_b64 v14, v[48:49] offset:8192\n"
    "buffer_store_dwordx4 v[56:59], v0, s[36:39], s76 offen offset:512 sc1\n"
    "ds_read_b128 v[72:75], v1 offset:49152\n"
    "ds_read_b128 v[76:79], v1 offset:50176\n"
    "v_add_u32 v1, 65536, v1\n"
    "ds_read_b128 v[4:7], v1 offset:0\n"
    "ds_read_b128 v[8:11], v1 offset:1024\n"
    "v_lshlrev_b32 v12, 2, v0\n"
    "s_waitcnt lgkmcnt(4)\n"
    "s_barrier\n"
    "ds_read_b128 v[40:43], v0 offset:8192\n"
    "ds_read_b128 v[44:47], v0 offset:9216\n"
    "ds_read_b128 v[48:51], v15 offset:8192\n"
    "ds_read_b128 v[52:55], v15 offset:9216\n"
    "v_mov_b64 v[56:57], 0\n"
    "v_mov_b64 v[58:59], 0\n"
    "v_mov_b64 v[60:61], 0\n"
    "v_mov_b64 v[62:63], 0\n"
    "s_mov_b32 exec_hi, 0\n"
    "buffer_load_dwordx4 v[12:15], v12, s[36:39], s90 offen sc1\n"
    "s_mov_b32 exec_hi, -1\n"
    "s_waitcnt lgkmcnt(3)\n"
    "v_smfmac_f32_16x16x64_bf16 v[56:59], v[40:43], v[16:23], v2\n"
    "ds_read_b128 v[16:19], v1 offset:16384\n"
    "s_waitcnt lgkmcnt(3)\n"
    "v_smfmac_f32_16x16x64_bf16 v[56:59], v[44:47], v[24:31], v2\n"
    "ds_read_b128 v[20:23], v1 offset:17408\n"
    "v_mov_b64 v[68:69], 0\n"
    "v_smfmac_f32_16x16x64_bf16 v[60:63], v[40:43], v[32:39], v2\n"
    "ds_read_b128 v[24:27], v1 offset:32768\n"
    "v_mov_b64 v[70:71], 0\n"
    "v_smfmac_f32_16x16x64_bf16 v[60:63], v[44:47], v[72:79], v2\n"
    "ds_read_b128 v[28:31], v1 offset:33792\n"
    "s_waitcnt lgkmcnt(5)\n"
    "v_smfmac_f32_16x16x64_bf16 v[56:59], v[48:51], v[4:11], v2\n"
    "ds_read_b128 v[32:35], v1 offset:40960\n"
    "s_waitcnt lgkmcnt(5)\n"
    "v_smfmac_f32_16x16x64_bf16 v[56:59], v[52:55], v[208:215], v2\n"
    "ds_read_b128 v[36:39], v1 offset:41984\n"
    "s_waitcnt lgkmcnt(4)\n"
    "v_smfmac_f32_16x16x64_bf16 v[60:63], v[48:51], v[16:23], v2\n"
    "ds_read_b128 v[72:75], v1 offset:49152\n"
    "v_mov_b32 v3, -1\n"
    "v_smfmac_f32_16x16x64_bf16 v[60:63], v[52:55], v[216:223], v2\n"
    "ds_read_b128 v[76:79], v1 offset:50176\n"
    "ds_read_b128 v[8:11], v0 offset:14336\n"
    "s_waitcnt lgkmcnt(5)\n"
    "v_smfmac_f32_16x16x64_bf16 v[64:67], v[40:43], v[24:31], v2\n"
    "ds_read_b128 v[16:19], v1 offset:57344\n"
    "v_cvt_pk_bf16_f32 v56, v56, v57\n"
    "v_smfmac_f32_16x16x64_bf16 v[64:67], v[44:47], v[224:231], v2\n"
    "ds_read_b128 v[20:23], v1 offset:58368\n"
    "v_cvt_pk_bf16_f32 v57, v58, v59\n"
    "s_waitcnt lgkmcnt(5)\n"
    "v_smfmac_f32_16x16x64_bf16 v[68:71], v[40:43], v[32:39], v2\n"
    "v_add_u32 v1, 65536, v1\n"
    "ds_read_b128 v[24:27], v1 offset:0\n"
    "v_cvt_pk_bf16_f32 v58, v60, v61\n"
    "v_smfmac_f32_16x16x64_bf16 v[68:71], v[44:47], v[232:239], v2\n"
    "ds_read_b128 v[28:31], v1 offset:1024\n"
    "v_cvt_pk_bf16_f32 v59, v62, v63\n"
    "s_waitcnt lgkmcnt(5)\n"
    "v_smfmac_f32_16x16x64_bf16 v[64:67], v[48:51], v[72:79], v2\n"
    "ds_read_b128 v[32:35], v1 offset:8192\n"
    "v_smfmac_f32_16x16x64_bf16 v[64:67], v[52:55], v[240:247], v2\n"
    "ds_read_b128 v[36:39], v1 offset:9216\n"
    "v_permlane16_swap_b32 v56, v58\n"
    "s_waitcnt lgkmcnt(4)\n"
    "v_smfmac_f32_16x16x64_bf16 v[68:71], v[48:51], v[16:23], v2\n"
    "v_permlane16_swap_b32 v57, v59\n"
    "v_smfmac_f32_16x16x64_bf16 v[68:71], v[52:55], v[248:255], v2\n"
    "buffer_store_dwordx4 v[56:59], v8, s[36:39], s77 offen sc1\n"
    "v_cvt_pk_bf16_f32 v64, v64, v65\n"
    "s_waitcnt lgkmcnt(2)\n"
    "v_mfma_f32_16x16x32_bf16 v[4:7], v[40:43], v[24:27], 0\n"
    "v_cvt_pk_bf16_f32 v65, v66, v67\n"
    "v_mfma_f32_16x16x32_bf16 v[4:7], v[48:51], v[28:31], v[4:7]\n"
    "v_cvt_pk_bf16_f32 v66, v68, v69\n"
    "v_cvt_pk_bf16_f32 v67, v70, v71\n"
    "s_waitcnt lgkmcnt(0)\n"
    "v_mfma_f32_16x16x32_bf16 v[4:7], v[44:47], v[32:35], v[4:7]\n"
    "v_permlane16_swap_b32 v64, v66\n"
    "v_mfma_f32_16x16x32_bf16 v[4:7], v[52:55], v[36:39], v[4:7]\n"
    "v_permlane16_swap_b32 v65, v67\n"
    "buffer_store_dwordx4 v[64:67], v8, s[36:39], s86 offen sc1\n"
    "v_subrev_u32 v1, 131072, v1\n"
    "s_nop 4\n"
    "v_cvt_pk_bf16_f32 v4, v4, v5\n"
    "v_cvt_pk_bf16_f32 v5, v6, v7\n"
    "buffer_store_dwordx2 v[4:5], v9, s[36:39], s87 offen sc1\n"
    "s_mov_b64 exec, 1\n"
    "buffer_atomic_smax v3, off, s[36:39], s59 sc0 sc1\n"
    "s_mov_b64 exec, -1\n"
    "ds_read_b128 v[72:75], v0 offset:15360\n"
    "v_mov_b32 v76, v10\n"
    "s_waitcnt vmcnt(4)\n"
    "s_memtime s[96:97]\n"
    "v_mov_b32 v77, s68\n"
    "s_mov_b64 exec, 1\n"
    "buffer_store_dword v77, off, s[36:39], s58 sc1\n"
    "s_mov_b64 exec, -1\n"
    "v_max3_u32 v12, v12, v13, v14\n"
    "v_max_u32 v12, v12, v15\n"
    "s_add_u32 s74, s68, 1\n"
    "s_and_b32 s75, s74, 7\n"
    "v_max_u32_dpp v12, v12, v12 row_ror:8 row_mask:0xf bank_mask:0xf\n"
    "s_mul_i32 s75, s75, 4194304\n"
    "s_add_u32 s75, s75, s56\n"
    "v_max_u32_dpp v12, v12, v12 row_ror:4 row_mask:0xf bank_mask:0xf\n"
    "s_and_b32 s79, s74, 3\n"
    "s_lshl_b32 s79, s79, 17\n"
    "v_max_u32_dpp v12, v12, v12 row_ror:2 row_mask:0xf bank_mask:0xf\n"
    "s_add_u32 s79, s79, s65\n"
    "s_add_u32 s78, s64, s82\n"
    "v_max_u32_dpp v12, v12, v12 row_ror:1 row_mask:0xf bank_mask:0xf\n"
    "s_add_u32 s88, s78, 4096\n"
    "s_mov_b32 s33, s32\n"
    "v_readlane_b32 s95, v12, 0\n"
    "v_readlane_b32 s99, v12, 16\n"
    "s_nop 0\n"
    "s_max_u32 s95, s95, s99\n"
    "s_cmp_eq_u32 s95, -1\n"
    "s_cbranch_scc1 .Lrec_tsretry_%=\n"
    ".Lrec_tsgo_%=:\n"
    "s_sub_u32 s95, s95, s28\n"
    "s_min_u32 s95, s95, 600\n"
    "s_add_u32 s95, s95, 40\n"
    "s_mul_i32 s95, s95, s48\n"
    "s_lshr_b32 s95, s95, 8\n"
    "s_add_u32 s32, s32, s95\n"
    "s_waitcnt lgkmcnt(0)\n"
    "s_sub_u32 s95, s32, s96\n"
    "s_mul_i32 s95, s95, 43\n"
    "s_ashr_i32 s95, s95, 10\n"
    "s_max_i32 s95, s95, 0\n"
    "s_min_i32 s95, s95, 100\n"
    ".Lrec_tsloop_%=:\n"
    "s_sub_u32 s95, s95, 1\n"
    "s_cbranch_scc0 .Lrec_tsloop_%=\n"
    "s_cmp_lg_u32 s60, 0\n"
    "s_cbranch_scc1 .Lrec_nored_%=\n"
    ".Lrec_red_%=:\n"
    "buffer_load_dwordx4 v[4:7], v0, s[36:39], s78 offen sc1\n"
    "buffer_load_dwordx4 v[8:11], v0, s[36:39], s78 offen offset:1024 sc1\n"
    "buffer_load_dwordx4 v[12:15], v0, s[36:39], s78 offen offset:2048 sc1\n"
    "buffer_load_dwordx4 v[16:19], v0, s[36:39], s78 offen offset:3072 sc1\n"
    "buffer_load_dwordx4 v[20:23], v0, s[36:39], s88 offen sc1\n"
    "buffer_load_dwordx4 v[24:27], v0, s[36:39], s88 offen offset:1024 sc1\n"
    "buffer_load_dwordx4 v[28:31], v0, s[36:39], s88 offen offset:2048 sc1\n"
    "buffer_load_dwordx4 v[32:35], v0, s[36:39], s88 offen offset:3072 sc1\n"
    "s_mul_i32 s95, s48, 265\n"
    "s_lshr_b32 s95, s95, 8\n"
    "s_add_u32 s32, s32, s95\n"
    "s_mov_b32 s50, s82\n"
    "s_add_u32 s82, s82, 589824\n"
    "s_cmp_eq_u32 s82, 1769472\n"
    "s_cselect_b32 s82, 0, s82\n"
    "s_and_b32 s80, s68, 3\n"
    "s_lshl_b32 s80, s80, 17\n"
    "s_and_b32 s80, s80, s45\n"
    "s_and_b32 s91, s50, s44\n"
    "s_add_u32 s80, s80, s91\n"
    "s_add_u32 s80, s80, s43\n"
    "s_add_u32 s89, s42, s50\n"
    "s_add_u32 s68, s68, 1\n"
    "s_and_b32 s70, s68, 3\n"
    "s_lshl_b32 s70, s70, 17\n"
    "s_add_u32 s70, s70, s66\n"
    "s_add_u32 s71, s70, 4096\n"
    "s_add_u32 s72, s70, 16384\n"
    "s_lshr_b32 s90, s68, 2\n"
    "s_lshl_b32 s90, s90, 14\n"
    "s_and_b32 s91, s68, 3\n"
    "s_lshl_b32 s91, s91, 11\n"
    "s_add_u32 s90, s90, s91\n"
    "s_add_u32 s90, s90, s40\n"
    "s_add_u32 s51, s90, s41\n"
    "s_and_b32 s76, s68, 7\n"
    "s_mul_i32 s76, s76, 4194304\n"
    "s_add_u32 s83, s76, s56\n"
    "s_add_u32 s83, s83, 16384\n"
    "s_add_u32 s76, s76, s57\n"
    "s_add_u32 s77, s63, s82\n"
    "s_add_u32 s86, s77, 65536\n"
    "s_add_u32 s87, s67, s82\n"
    "v_mov_b64 v[40:41], 0\n"
    "v_mov_b64 v[42:43], 0\n"
    "v_mov_b64 v[44:45], 0\n"
    "v_mov_b64 v[46:47], 0\n"
    "v_mov_b64 v[48:49], 0\n"
    "v_mov_b64 v[50:51], 0\n"
    "v_mov_b64 v[52:53], 0\n"
    "v_mov_b64 v[54:55], 0\n"
    "v_mov_b64 v[56:57], 0\n"
    "v_mov_b64 v[58:59], 0\n"
    "v_mov_b64 v[60:61], 0\n"
    "v_mov_b64 v[62:63], 0\n"
    "s_waitcnt vmcnt(9)\n"
    "buffer_load_dwordx4 v[64:67], v0, s[36:39], s75 offen sc1\n"
    "buffer_load_dwordx4 v[68:71], v0, s[36:39], s75 offen offset:1024 sc1\n"
    "v_readfirstlane_b32 s81, v3\n"
    "s_nop 0\n"
    "s_cmp_ge_i32 s81, s68\n"
    "s_cselect_b32 s81, 1, 0\n"
    "s_sub_u32 s46, s33, s46\n"
    "s_sub_u32 s47, s28, s47\n"
    "v_cvt_f32_u32 v78, s46\n"
    "v_cvt_f32_u32 v79, s47\n"
    "v_rcp_f32 v79, v79\n"
    "s_mov_b32 s46, s33\n"
    "s_mov_b32 s47, s28\n"
    "v_mul_f32 v78, v78, v79\n"
    "v_mov_b32 v77, 0x40c80000\n"
    "v_med3_f32 v78, v78, 4.0, v77\n"
    "v_rcp_f32 v79, v78\n"
    "v_mul_f32 v78, 0x43800000, v78\n"
    "v_mul_f32 v79, 0x44b54000, v79\n"
    "v_cvt_u32_f32 v78, v78\n"
    "v_cvt_u32_f32 v79, v79\n"
    "v_readfirstlane_b32 s48, v78\n"
    "v_readfirstlane_b32 s49, v79\n"
    ".Lrec_rsum_%=:\n"
    "s_waitcnt vmcnt(9)\n"
    "v_mfma_f32_16x16x32_bf16 v[36:39], v[4:7], v[72:75], 0\n"
    "v_max3_u32 v3, v4, v5, v6\n"
    "v_max_u32 v3, v3, v7\n"
    "s_waitcnt vmcnt(8)\n"
    "v_mfma_f32_16x16x32_bf16 v[36:39], v[8:11], v[72:75], v[36:39]\n"
    "v_max3_u32 v3, v3, v8, v9\n"
    "v_max3_u32 v3, v3, v10, v11\n"
    "s_waitcnt vmcnt(7)\n"
    "v_mfma_f32_16x16x32_bf16 v[36:39], v[12:15], v[72:75], v[36:39]\n"
    "v_max3_u32 v3, v3, v12, v13\n"
    "v_max3_u32 v3, v3, v14, v15\n"
    "s_waitcnt vmcnt(6)\n"
    "v_mfma_f32_16x16x32_bf16 v[36:39], v[16:19], v[72:75], v[36:39]\n"
    "v_max3_u32 v3, v3, v16, v17\n"
    "v_max3_u32 v3, v3, v18, v19\n"
    "s_waitcnt vmcnt(5)\n"
    "v_mfma_f32_16x16x32_bf16 v[36:39], v[20:23], v[72:75], v[36:39]\n"
    "v_max3_u32 v3, v3, v20, v21\n"
    "v_max3_u32 v3, v3, v22, v23\n"
    "s_waitcnt vmcnt(4)\n"
    "v_mfma_f32_16x16x32_bf16 v[36:39], v[24:27], v[72:75], v[36:39]\n"
    "v_max3_u32 v3, v3, v24, v25\n"
    "v_max3_u32 v3, v3, v26, v27\n"
    "s_waitcnt vmcnt(3)\n"
    "v_mfma_f32_16x16x32_bf16 v[36:39], v[28:31], v[72:75], v[36:39]\n"
    "v_max3_u32 v3, v3, v28, v29\n"
    "v_max3_u32 v3, v3, v30, v31\n"
    "s_waitcnt vmcnt(2)\n"
    "v_mfma_f32_16x16x32_bf16 v[36:39], v[32:35], v[72:75], v[36:39]\n"
    "v_max3_u32 v3, v3, v32, v33\n"
    "v_max3_u32 v3, v3, v34, v35\n"
    "v_cmp_eq_u32 vcc, -1, v3\n"
    "s_nop 4\n"
    "v_cvt_pk_bf16_f32 v4, v36, v37\n"
    "v_cvt_pk_bf16_f32 v6, v36, v37\n"
    "v_cvt_pk_bf16_f32 v5, v38, v39\n"
    "v_cvt_pk_bf16_f32 v7, v38, v39\n"
    "s_cbranch_vccnz .Lrec_rretry_%=\n"
    ".Lrec_rgo_%=:\n"
    "v_permlane16_swap_b32 v4, v6\n"
    "v_permlane16_swap_b32 v5, v7\n"
    "buffer_store_dwordx4 v[4:7], v76, s[36:39], s79 offen sc1\n"
    ".Lrec_b3_%=:\n"
    ".Lrec_entry_%=:\n"
    "s_memtime s[96:97]\n"
    "ds_read_b128 v[72:75], v1 offset:8192\n"
    "ds_read_b128 v[76:79], v1 offset:9216\n"
    "s_cmp_eq_u32 s81, 0\n"
    "s_cbranch_scc1 .Lrec_waitp_%=\n"
    "s_waitcnt lgkmcnt(0)\n"
    "s_sub_u32 s95, s32, s96\n"
    "s_mul_i32 s95, s95, 43\n"
    "s_ashr_i32 s95, s95, 10\n"
    "s_max_i32 s95, s95, 0\n"
    "s_min_i32 s95, s95, 100\n"
    ".Lrec_gloop_%=:\n"
    "s_sub_u32 s95, s95, 1\n"
    "s_cbranch_scc0 .Lrec_gloop_%=\n"
    "s_branch .Lrec_top_%=\n"
    ".Lrec_LOOPE_%=:\n"
    ".Lrec_fin_%=:\n"
    "buffer_store_dwordx4 v[56:59], v0, s[36:39], s76 offen offset:512 sc1\n"
    "s_waitcnt vmcnt(0) lgkmcnt(0)\n"
    "v_mov_b32 v46, s68\n"
    "s_mov_b64 exec, 1\n"
    "buffer_store_dword v46, off, s[36:39], s58 sc1\n"
    "s_mov_b64 exec, -1\n"
    "s_waitcnt vmcnt(0)\n"
    "s_branch .Lrec_FIN_%=\n"
    ".Lrec_retry_%=:\n"
    "s_waitcnt vmcnt(0) lgkmcnt(0)\n"
    "s_sleep 1\n"
    "buffer_load_dwordx4 v[4:7], v0, s[36:39], s70 offen sc1\n"
    "buffer_load_dwordx4 v[8:11], v0, s[36:39], s70 offen offset:1024 sc1\n"
    "buffer_load_dwordx4 v[12:15], v0, s[36:39], s70 offen offset:2048 sc1\n"
    "buffer_load_dwordx4 v[16:19], v0, s[36:39], s70 offen offset:3072 sc1\n"
    "buffer_load_dwordx4 v[20:23], v0, s[36:39], s71 offen sc1\n"
    "buffer_load_dwordx4 v[24:27], v0, s[36:39], s71 offen offset:1024 sc1\n"
    "buffer_load_dwordx4 v[28:31], v0, s[36:39], s71 offen offset:2048 sc1\n"
    "buffer_load_dwordx4 v[32:35], v0, s[36:39], s71 offen offset:3072 sc1\n"
    "buffer_load_dwordx4 v[36:39], v0, s[36:39], s72 offen sc1\n"
    "ds_read_b128 v[72:75], v1 offset:8192\n"
    "ds_read_b128 v[76:79], v1 offset:9216\n"
    "v_mov_b64 v[40:41], 0\n"
    "v_mov_b64 v[42:43], 0\n"
    "v_mov_b64 v[44:45], 0\n"
    "v_mov_b64 v[46:47], 0\n"
    "v_mov_b64 v[48:49], 0\n"
    "v_mov_b64 v[50:51], 0\n"
    "v_mov_b64 v[52:53], 0\n"
    "v_mov_b64 v[54:55], 0\n"
    "v_mov_b64 v[56:57], 0\n"
    "v_mov_b64 v[58:59], 0\n"
    "v_mov_b64 v[60:61], 0\n"
    "v_mov_b64 v[62:63], 0\n"
    "s_waitcnt vmcnt(0)\n"
    "v_max3_u32 v3, v4, v5, v6\n"
    "v_max3_u32 v3, v3, v7, v8\n"
    "v_max3_u32 v3, v3, v9, v10\n"
    "v_max3_u32 v3, v3, v11, v12\n"
    "v_max3_u32 v3, v3, v13, v14\n"
    "v_max3_u32 v3, v3, v15, v16\n"
    "v_max3_u32 v3, v3, v17, v18\n"
    "v_max3_u32 v3, v3, v19, v20\n"
    "v_max3_u32 v3, v3, v21, v22\n"
    "v_max3_u32 v3, v3, v23, v24\n"
    "v_max3_u32 v3, v3, v25, v26\n"
    "v_max3_u32 v3, v3, v27, v28\n"
    "v_max3_u32 v3, v3, v29, v30\n"
    "v_max3_u32 v3, v3, v31, v32\n"
    "v_max3_u32 v3, v3, v33, v34\n"
    "v_max3_u32 v3, v3, v35, v36\n"
    "v_max3_u32 v3, v3, v37, v38\n"
    "v_max_u32 v3, v3, v39\n"
    "v_cmp_eq_u32 vcc, -1, v3\n"
    "s_cbranch_vccz .Lrec_start_%=\n"
    "s_sub_u32 s69, s69, 1\n"
    "s_cbranch_scc0 .Lrec_retry_%=\n"
    "s_mov_b32 s69, 0\n"
    "s_branch .Lrec_start_%=\n"
    ".Lrec_retry0_%=:\n"
    "s_cmp_eq_u32 s69, 0\n"
    "s_cbranch_scc1 .Lrec_back0_%=\n"
    "s_branch .Lrec_retry_%=\n"
    ".Lrec_retry1_%=:\n"
    "s_cmp_eq_u32 s69, 0\n"
    "s_cbranch_scc1 .Lrec_back1_%=\n"
    "v_subrev_u32 v1, 65536, v1\n"
    "s_branch .Lrec_retry_%=\n"
    ".Lrec_retry2_%=:\n"
    "s_cmp_eq_u32 s69, 0\n"
    "s_cbranch_scc1 .Lrec_back2_%=\n"
    "s_branch .Lrec_retry_%=\n"
    ".Lrec_tsretry_%=:\n"
    "s_mov_b32 s98, 6\n"
    ".Lrec_tsretry1_%=:\n"
    "s_mov_b32 s95, s28\n"
    "s_sub_u32 s98, s98, 1\n"
    "s_cbranch_scc1 .Lrec_tsgo_%=\n"
    "s_sleep 2\n"
    "buffer_load_dwordx4 v[12:15], v11, s[36:39], s90 offen sc1\n"
    "s_waitcnt vmcnt(0)\n"
    "s_memtime s[96:97]\n"
    "v_max3_u32 v12, v12, v13, v14\n"
    "v_max_u32 v12, v12, v15\n"
    "s_nop 1\n"
    "v_max_u32_dpp v12, v12, v12 row_ror:8 row_mask:0xf bank_mask:0xf\n"
    "s_nop 1\n"
    "v_max_u32_dpp v12, v12, v12 row_ror:4 row_mask:0xf bank_mask:0xf\n"
    "s_nop 1\n"
    "v_max_u32_dpp v12, v12, v12 row_ror:2 row_mask:0xf bank_mask:0xf\n"
    "s_nop 1\n"
    "v_max_u32_dpp v12, v12, v12 row_ror:1 row_mask:0xf bank_mask:0xf\n"
    "s_nop 0\n"
    "v_readlane_b32 s95, v12, 0\n"
    "v_readlane_b32 s99, v12, 16\n"
    "s_nop 0\n"
    "s_max_u32 s95, s95, s99\n"
    "s_cmp_eq_u32 s95, -1\n"
    "s_cbranch_scc1 .Lrec_tsretry1_%=\n"
    "s_branch .Lrec_tsgo_%=\n"
    ".Lrec_rretry_%=:\n"
    "s_sub_u32 s69, s69, 1\n"
    "s_cbranch_scc1 .Lrec_rdead_%=\n"
    "s_nop 7\n"
    "buffer_load_dwordx4 v[4:7], v0, s[36:39], s78 offen sc1\n"
    "buffer_load_dwordx4 v[8:11], v0, s[36:39], s78 offen offset:1024 sc1\n"
    "buffer_load_dwordx4 v[12:15], v0, s[36:39], s78 offen offset:2048 sc1\n"
    "buffer_load_dwordx4 v[16:19], v0, s[36:39], s78 offen offset:3072 sc1\n"
    "buffer_load_dwordx4 v[20:23], v0, s[36:39], s88 offen sc1\n"
    "buffer_load_dwordx4 v[24:27], v0, s[36:39], s88 offen offset:1024 sc1\n"
    "buffer_load_dwordx4 v[28:31], v0, s[36:39], s88 offen offset:2048 sc1\n"
    "buffer_load_dwordx4 v[32:35], v0, s[36:39], s88 offen offset:3072 sc1\n"
    "s_waitcnt vmcnt(0)\n"
    "s_branch .Lrec_rsum_%=\n"
    ".Lrec_rdead_%=:\n"
    "s_mov_b32 s69, 0\n"
    "s_nop 7\n"
    "s_branch .Lrec_rgo_%=\n"
    ".Lrec_nored_%=:\n"
    "s_nop 8\n"
    "s_cmp_lt_i32 s64, 0\n"
    "s_cbranch_scc0 .Lrec_red_%=\n"
    "s_mul_i32 s95, s48, 265\n"
    "s_lshr_b32 s95, s95, 8\n"
    "s_add_u32 s32, s32, s95\n"
    "s_mov_b32 s73, s89\n"
    "s_mov_b32 s74, s80\n"
    "s_mov_b32 s50, s82\n"
    "s_add_u32 s82, s82, 589824\n"
    "s_cmp_eq_u32 s82, 1769472\n"
    "s_cselect_b32 s82, 0, s82\n"
    "s_and_b32 s80, s68, 3\n"
    "s_lshl_b32 s80, s80, 17\n"
    "s_and_b32 s80, s80, s45\n"
    "s_and_b32 s91, s50, s44\n"
    "s_add_u32 s80, s80, s91\n"
    "s_add_u32 s80, s80, s43\n"
    "s_add_u32 s89, s42, s50\n"
    "s_add_u32 s68, s68, 1\n"
    "s_and_b32 s70, s68, 3\n"
    "s_lshl_b32 s70, s70, 17\n"
    "s_add_u32 s70, s70, s66\n"
    "s_add_u32 s71, s70, 4096\n"
    "s_add_u32 s72, s70, 16384\n"
    "s_lshr_b32 s90, s68, 2\n"
    "s_lshl_b32 s90, s90, 14\n"
    "s_and_b32 s91, s68, 3\n"
    "s_lshl_b32 s91, s91, 11\n"
    "s_add_u32 s90, s90, s91\n"
    "s_add_u32 s90, s90, s40\n"
    "s_add_u32 s51, s90, s41\n"
    "s_and_b32 s76, s68, 7\n"
    "s_mul_i32 s76, s76, 4194304\n"
    "s_add_u32 s83, s76, s56\n"
    "s_add_u32 s83, s83, 16384\n"
    "s_add_u32 s76, s76, s57\n"
    "s_add_u32 s77, s63, s82\n"
    "s_add_u32 s86, s77, 65536\n"
    "s_add_u32 s87, s67, s82\n"
    "v_mov_b64 v[40:41], 0\n"
    "v_mov_b64 v[42:43], 0\n"
    "v_mov_b64 v[44:45], 0\n"
    "v_mov_b64 v[46:47], 0\n"
    "v_mov_b64 v[48:49], 0\n"
    "v_mov_b64 v[50:51], 0\n"
    "v_mov_b64 v[52:53], 0\n"
    "v_mov_b64 v[54:55], 0\n"
    "v_mov_b64 v[56:57], 0\n"
    "v_mov_b64 v[58:59], 0\n"
    "v_mov_b64 v[60:61], 0\n"
    "v_mov_b64 v[62:63], 0\n"
    "s_sub_u32 s46, s33, s46\n"
    "s_sub_u32 s47, s28, s47\n"
    "v_cvt_f32_u32 v78, s46\n"
    "v_cvt_f32_u32 v79, s47\n"
    "v_rcp_f32 v79, v79\n"
    "s_mov_b32 s46, s33\n"
    "s_mov_b32 s47, s28\n"
    "v_mul_f32 v78, v78, v79\n"
    "v_mov_b32 v77, 0x40c80000\n"
    "v_med3_f32 v78, v78, 4.0, v77\n"
    "v_rcp_f32 v79, v78\n"
    "v_mul_f32 v78, 0x43800000, v78\n"
    "v_mul_f32 v79, 0x44b54000, v79\n"
    "v_cvt_u32_f32 v78, v78\n"
    "v_cvt_u32_f32 v79, v79\n"
    "v_readfirstlane_b32 s48, v78\n"
    "v_readfirstlane_b32 s49, v79\n"
    "s_waitcnt vmcnt(1)\n"
    "buffer_load_dwordx4 v[64:67], v0, s[36:39], s75 offen sc1\n"
    "buffer_load_dwordx4 v[68:71], v0, s[36:39], s75 offen offset:1024 sc1\n"
    "v_readfirstlane_b32 s81, v3\n"
    "s_nop 0\n"
    "s_cmp_ge_i32 s81, s68\n"
    "s_cselect_b32 s81, 1, 0\n"
    "v_mov_b64 v[4:5], -1\n"
    "v_mov_b64 v[6:7], -1\n"
    "buffer_store_dwordx4 v[4:7], v0, s[36:39], s73 offen sc1\n"
    "buffer_store_dwordx4 v[4:7], v0, s[36:39], s73 offen offset:1024 sc1\n"
    "buffer_store_dwordx4 v[4:7], v0, s[36:39], s73 offen offset:2048 sc1\n"
    "buffer_store_dwordx4 v[4:7], v0, s[36:39], s73 offen offset:3072 sc1\n"
    "buffer_store_dwordx4 v[4:7], v0, s[36:39], s74 offen sc1\n"
    "s_branch .Lrec_b3_%=\n"
    ".Lrec_waitp_%=:\n"
    "v_mov_b32 v3, -1\n"
    "s_mov_b64 exec, 1\n"
    "buffer_atomic_smax v3, off, s[36:39], s59 sc0 sc1\n"
    "s_mov_b64 exec, -1\n"
    "s_waitcnt vmcnt(0)\n"
    "v_readfirstlane_b32 s81, v3\n"
    "s_nop 0\n"
    "s_cmp_ge_i32 s81, s68\n"
    "s_cbranch_scc1 .Lrec_waitp1_%=\n"
    "s_sleep 1\n"
    "s_sub_u32 s69, s69, 1\n"
    "s_cbranch_scc0 .Lrec_waitp_%=\n"
    "s_mov_b32 s69, 0\n"
    ".Lrec_waitp1_%=:\n"
    "s_and_b32 s75, s68, 7\n"
    "s_mul_i32 s75, s75, 4194304\n"
    "s_add_u32 s75, s75, s56\n"
    "buffer_load_dwordx4 v[64:67], v0, s[36:39], s75 offen sc1\n"
    "buffer_load_dwordx4 v[68:71], v0, s[36:39], s75 offen offset:1024 sc1\n"
    "s_branch .Lrec_top_%=\n"
    ".Lrec_FIN_%=:\n"
  );
}

DEVI u32 find_shift(rsrc_t rw, bool decide) {
  const int lane = threadIdx.x & 63;
  if (decide) {
    u32 best[2] = {~0u, ~0u};
    const u32 probe = 126 * CHUNK + 8192;
#pragma unroll 1
    for (int r = 0; r < 5; ++r) {
      const u32 a = lane == 0 ? probe + (u32)(r & 1) * 8192 : OOB;
      u32 v = 0;
      const unsigned long long t0 = __builtin_amdgcn_s_memtime();
      asm volatile("s_waitcnt lgkmcnt(0)\n\tbuffer_atomic_or %0, %1, %2, 0 offen sc0 sc1\n\ts_waitcnt vmcnt(0)" : "+v"(v) : "v"(a), "s"(rw) : "memory");
      const u32 dt = (u32)(__builtin_amdgcn_s_memtime() - t0);
      if (r & 1) best[1] = min(best[1], dt); else best[0] = min(best[0], dt);
    }
    if (lane == 0) st4(rw, best[0] < best[1] ? 1u : 0u, PARW, 0);
  }
  unsigned budget = SPIN_LIMIT;
  wait_flags<4>(rw, PARW, 0, 0, budget);
  return (rfl(ld4(rw, PARW, 0)) & 1u) * 8192;
}
DEVI void hot_init(const Params& p, rsrc_t rw, int L, int n, u32 shift) {
  const int tid = threadIdx.x, w = (int)rfl(tid >> 6);
  const u32 hot = HOT + L * 8192 + shift, hot2 = HOT2 + (1 - L) * 8192 + shift;
  const v4u ones = {~0u, ~0u, ~0u, ~0u};
#pragma unroll
  for (int k = 0; k < 9; ++k) {
    st16(rw, ones, tid * 16, hot + (u32)(9 * n + k) * CHUNK);
    st16(rw, ones, tid * 16 + 4096, hot + (u32)(9 * n + k) * CHUNK);
  }
  {
    const u32 b = hot2 + (u32)(4 + n) * 2 * CHUNK;
    st16(rw, ones, tid * 16, b); st16(rw, ones, tid * 16 + 4096, b);
    st16(rw, ones, w == 0 ? (u32)tid * 16 : OOB, b + CHUNK);
  }
  if (n < 8) {
#pragma unroll
    for (int k = 0; k < 2; ++k) {
      st16(rw, ones, tid * 16, hot2 + (u32)(32 + 2 * n + k) * CHUNK);
      st16(rw, ones, tid * 16 + 4096, hot2 + (u32)(32 + 2 * n + k) * CHUNK);
    }
  }
  if (n >= 8) {
    const u32 b = hot2 + (u32)(n - 8) * 2 * CHUNK;
#pragma unroll
    for (int k = 0; k < 3; ++k) {
      const int pc = tid + 256 * k;
      if (pc < 576) {
        v4u v = *(const v4u*)(p.mem0 + (pc & 7) * 576 + (pc >> 3) * 8);
#pragma unroll
        for (int d = 0; d < 4; ++d) v[d] = min(v[d], 0xFFFFFFFEu);
        st16(rw, v, pc < 512 ? (u32)pc * 16 : CHUNK + (u32)(pc - 512) * 16, b);
      }
    }
  }
  drain();
  __syncthreads();
  if (tid == 0) st4(rw, 0u, INITF + L * 8192 + n * 128 + shift, 0);
}

DEVI void role_att(const Params& p, int L, int j, int a, u32 dbg, u32 shift) {
  const int tid = threadIdx.x, lane = tid & 63, w = (int)rfl(tid >> 6);
  const int c = lane & 15, g = lane >> 4;
  const int hl = 4 * a + w, h = 8 * j + hl;
  const v4u ws = make_srd(p.ws + shift, WS_END);
  v16u sa = cat4(ws, ws, ws, ws);
  v16u sb = {pga(L, B_QKV + 3 * h), pga(L, B_KVM + h), pgb(L, B_O + h), 0, dbg + w * 64,
             0, 0, 0, 0, 0, 0, 0, 0, 0, 0, 0};
  v16u va = {0, 0, 0, 0, 0, 0, 0, 0, 0, 0, 0, 0, 0, 0, 0, 0};
  va[0] = lane * 16;
  va[1] = (u32)(g * 128 + (c & 7) * 16);
  va[2] = lane < 3 ? mbox(L, j, SL_WO + lane, hl) : mbox(L, j, SL_P + hl, 48);
  va[4] = mbox_att(L, j, hl, lane & 15);
  va[5] = (u32)(lane < 4 ? 0 : (lane < 7 ? RO : ((lane >> 2) == 2 ? -1 : 1000)));
  va[6] = (u32)(c - 4 * g);
  va[7] = 0;
  RUN_ASM(
    "s_nop 4\n"
    "v_cmp_gt_i32 s[10:11], 0, v6\n"
    "v_cmp_gt_i32 s[12:13], 1, v6\n"
    "v_cmp_gt_i32 s[14:15], 2, v6\n"
    "v_cmp_gt_i32 s[16:17], 3, v6\n"
    "v_mov_b32 v10, 0xff61b1e6\n"
    "s_mov_b32 s18, 0x3e0293ee\n"
    "s_mov_b32 s68, 0\n"
    "v_mov_b32 v8, -1\n"
    "s_mov_b32 s69, 262144\n"
    "s_branch .Latt_waitp_%=\n"
    ".Latt_LOOPB_%=:\n"
    ".Latt_top_%=:\n"
    "s_waitcnt vmcnt(23)\n"
    "v_mfma_f32_16x16x32_bf16 v[160:163], v[16:19], v[48:51], 0\n"
    "v_mfma_f32_16x16x32_bf16 v[164:167], v[16:19], v[52:55], 0\n"
    "v_mfma_f32_16x16x32_bf16 v[168:171], v[20:23], v[52:55], 0\n"
    "s_waitcnt vmcnt(19)\n"
    "v_mfma_f32_16x16x32_bf16 v[160:163], v[24:27], v[56:59], v[160:163]\n"
    "v_mfma_f32_16x16x32_bf16 v[164:167], v[24:27], v[60:63], v[164:167]\n"
    "v_mfma_f32_16x16x32_bf16 v[168:171], v[28:31], v[60:63], v[168:171]\n"
    "s_waitcnt vmcnt(15)\n"
    "v_mfma_f32_16x16x32_bf16 v[160:163], v[32:35], v[64:67], v[160:163]\n"
    "v_mfma_f32_16x16x32_bf16 v[164:167], v[32:35], v[68:71], v[164:167]\n"
    "v_mfma_f32_16x16x32_bf16 v[168:171], v[36:39], v[68:71], v[168:171]\n"
    "s_waitcnt vmcnt(9)\n"
    "v_mfma_f32_16x16x32_bf16 v[160:163], v[40:43], v[72:75], v[160:163]\n"
    "v_mfma_f32_16x16x32_bf16 v[164:167], v[40:43], v[76:79], v[164:167]\n"
    "v_mfma_f32_16x16x32_bf16 v[168:171], v[44:47], v[76:79], v[168:171]\n"
    "v_add_u32 v11, v8, v5\n"
    "v_cmp_gt_i32 vcc, s68, v11\n"
    "s_nop 0\n"
    "s_or_b64 exec, vcc, 1\n"
    "buffer_atomic_smax v8, v4, s[36:39], 0 offen sc0 sc1\n"
    "s_mov_b64 exec, -1\n"
    "s_add_u32 s72, s68, 1\n"
    "s_and_b32 s4, s72, 7\n"
    "s_mul_i32 s4, s4, 4194304\n"
    "s_add_u32 s4, s4, s52\n"
    "s_add_u32 s5, s4, 4096\n"
    "s_add_u32 s6, s4, 16384\n"
    "s_add_u32 s7, s4, 20480\n"
    "s_add_u32 s8, s4, 32768\n"
    "s_add_u32 s9, s4, 36864\n"
    "s_and_b32 s73, s68, 7\n"
    "s_mul_i32 s73, s73, 4194304\n"
    "s_add_u32 s73, s73, s53\n"
    "s_add_u32 s74, s73, 3072\n"
    "s_and_b32 s75, s68, 7\n"
    "s_lshl_b32 s75, s75, 22\n"
    "s_add_u32 s75, s75, s54\n"
    "s_add_u32 s76, s75, 4096\n"
    "v_cndmask_b32 v160, v160, v10, s[10:11]\n"
    "v_cndmask_b32 v161, v161, v10, s[12:13]\n"
    "v_cndmask_b32 v162, v162, v10, s[14:15]\n"
    "v_cndmask_b32 v163, v163, v10, s[16:17]\n"
    "v_cndmask_b32 v168, v168, v10, s[10:11]\n"
    "v_cndmask_b32 v169, v169, v10, s[12:13]\n"
    "v_cndmask_b32 v170, v170, v10, s[14:15]\n"
    "v_cndmask_b32 v171, v171, v10, s[16:17]\n"
    "v_max3_f32 v224, v160, v161, v162\n"
    "v_max3_f32 v226, v164, v165, v166\n"
    "v_max_f32 v224, v224, v163\n"
    "v_max3_f32 v226, v226, v167, v168\n"
    "v_max3_f32 v226, v226, v169, v170\n"
    "v_max_f32 v226, v226, v171\n"
    "v_mov_b32 v238, v224\n"
    "v_mov_b32 v239, v226\n"
    "s_nop 0\n"
    "v_permlane16_swap_b32 v224, v238\n"
    "v_permlane16_swap_b32 v226, v239\n"
    "v_max_f32 v224, v224, v238\n"
    "v_max_f32 v226, v226, v239\n"
    "v_mov_b32 v238, v224\n"
    "v_mov_b32 v239, v226\n"
    "s_nop 0\n"
    "v_permlane32_swap_b32 v224, v238\n"
    "v_permlane32_swap_b32 v226, v239\n"
    "v_max_f32 v224, v224, v238\n"
    "v_max_f32 v226, v226, v239\n"
    "v_mul_f32 v230, 0xbe0293ee, v224\n"
    "v_mul_f32 v232, 0xbe0293ee, v226\n"
    "v_pk_fma_f32 v[160:161], v[160:161], s[18:19], v[230:231] op_sel_hi:[1,0,0]\n"
    "v_pk_fma_f32 v[162:163], v[162:163], s[18:19], v[230:231] op_sel_hi:[1,0,0]\n"
    "v_pk_fma_f32 v[164:165], v[164:165], s[18:19], v[232:233] op_sel_hi:[1,0,0]\n"
    "v_pk_fma_f32 v[166:167], v[166:167], s[18:19], v[232:233] op_sel_hi:[1,0,0]\n"
    "v_pk_fma_f32 v[168:169], v[168:169], s[18:19], v[232:233] op_sel_hi:[1,0,0]\n"
    "v_pk_fma_f32 v[170:171], v[170:171], s[18:19], v[232:233] op_sel_hi:[1,0,0]\n"
    "v_exp_f32 v160, v160\n"
    "v_exp_f32 v161, v161\n"
    "v_exp_f32 v162, v162\n"
    "v_exp_f32 v163, v163\n"
    "v_exp_f32 v164, v164\n"
    "v_exp_f32 v165, v165\n"
    "v_exp_f32 v166, v166\n"
    "v_exp_f32 v167, v167\n"
    "v_exp_f32 v168, v168\n"
    "v_exp_f32 v169, v169\n"
    "v_exp_f32 v170, v170\n"
    "v_exp_f32 v171, v171\n"
    "v_pk_add_f32 v[230:231], v[160:161], v[162:163]\n"
    "v_pk_add_f32 v[232:233], v[164:165], v[166:167]\n"
    "v_pk_add_f32 v[234:235], v[168:169], v[170:171]\n"
    "v_pk_add_f32 v[232:233], v[232:233], v[234:235]\n"
    "v_add_f32 v228, v230, v231\n"
    "v_add_f32 v229, v232, v233\n"
    "v_mov_b32 v238, v228\n"
    "v_mov_b32 v239, v229\n"
    "s_nop 0\n"
    "v_permlane16_swap_b32 v228, v238\n"
    "v_permlane16_swap_b32 v229, v239\n"
    "v_add_f32 v228, v228, v238\n"
    "v_add_f32 v229, v229, v239\n"
    "v_mov_b32 v238, v228\n"
    "v_mov_b32 v239, v229\n"
    "s_nop 0\n"
    "v_permlane32_swap_b32 v228, v238\n"
    "v_permlane32_swap_b32 v229, v239\n"
    "v_add_f32 v228, v228, v238\n"
    "v_add_f32 v229, v229, v239\n"
    "s_waitcnt vmcnt(0)\n"
    ".Latt_pollr_%=:\n"
    "v_add_u32 v11, v8, v5\n"
    "v_cmp_gt_i32 vcc, s68, v11\n"
    "s_nop 0\n"
    "s_and_b32 s71, vcc_lo, 0xff\n"
    "s_cbranch_scc1 .Latt_waitr_%=\n"
    ".Latt_gor_%=:\n"
    "buffer_load_dwordx4 v[128:131], v1, s[36:39], s73 offen sc1\n"
    "buffer_load_dwordx4 v[132:135], v1, s[36:39], s73 offen offset:1536 sc1\n"
    "buffer_load_dwordx4 v[136:139], v1, s[36:39], s74 offen sc1\n"
    "buffer_load_dwordx4 v[140:143], v1, s[36:39], s74 offen offset:1536 sc1\n"
    "buffer_load_dwordx4 v[144:147], v0, s[36:39], s73 offen offset:512 sc1\n"
    "buffer_load_dwordx4 v[148:151], v0, s[36:39], s73 offen offset:2048 sc1\n"
    "buffer_load_dwordx4 v[152:155], v0, s[36:39], s74 offen offset:512 sc1\n"
    "buffer_load_dwordx4 v[156:159], v0, s[36:39], s74 offen offset:2048 sc1\n"
    "s_and_b32 s71, vcc_lo, 0xf00\n"
    "s_cselect_b32 s70, 0, 1\n"
    "s_cselect_b32 s71, 0x80000000, 0\n"
    "v_or_b32 v7, s71, v0\n"
    "s_nop 15\n"
    "buffer_load_dwordx4 v[16:19], v7, s[36:39], s6 offen sc1\n"
    "s_nop 15\n"
    "buffer_load_dwordx4 v[20:23], v7, s[36:39], s6 offen offset:1024 sc1\n"
    "s_nop 15\n"
    "buffer_load_dwordx4 v[24:27], v7, s[36:39], s6 offen offset:2048 sc1\n"
    "s_nop 15\n"
    "buffer_load_dwordx4 v[28:31], v7, s[36:39], s6 offen offset:3072 sc1\n"
    "s_waitcnt vmcnt(11)\n"
    "v_mfma_f32_16x16x32_bf16 v[172:175], v[128:131], v[48:51], 0\n"
    "v_mfma_f32_16x16x32_bf16 v[176:179], v[128:131], v[52:55], 0\n"
    "s_waitcnt vmcnt(10)\n"
    "v_mfma_f32_16x16x32_bf16 v[172:175], v[132:135], v[56:59], v[172:175]\n"
    "v_mfma_f32_16x16x32_bf16 v[176:179], v[132:135], v[60:63], v[176:179]\n"
    "s_waitcnt vmcnt(9)\n"
    "v_mfma_f32_16x16x32_bf16 v[172:175], v[136:139], v[64:67], v[172:175]\n"
    "v_mfma_f32_16x16x32_bf16 v[176:179], v[136:139], v[68:71], v[176:179]\n"
    "s_waitcnt vmcnt(8)\n"
    "v_mfma_f32_16x16x32_bf16 v[172:175], v[140:143], v[72:75], v[172:175]\n"
    "v_mfma_f32_16x16x32_bf16 v[176:179], v[140:143], v[76:79], v[176:179]\n"
    "s_waitcnt vmcnt(4)\n"
    "v_mov_b64 v[80:81], v[144:145]\n"
    "v_mov_b64 v[86:87], v[146:147]\n"
    "v_mov_b64 v[92:93], v[148:149]\n"
    "v_mov_b64 v[98:99], v[150:151]\n"
    "v_mov_b64 v[104:105], v[152:153]\n"
    "v_mov_b64 v[110:111], v[154:155]\n"
    "v_mov_b64 v[116:117], v[156:157]\n"
    "v_mov_b64 v[122:123], v[158:159]\n"
    "v_max3_f32 v230, v172, v173, v174\n"
    "v_max3_f32 v232, v176, v177, v178\n"
    "v_max3_f32 v230, v230, v175, v224\n"
    "v_max3_f32 v232, v232, v179, v226\n"
    "v_mov_b32 v238, v230\n"
    "v_mov_b32 v239, v232\n"
    "s_nop 0\n"
    "v_permlane16_swap_b32 v230, v238\n"
    "v_permlane16_swap_b32 v232, v239\n"
    "v_max_f32 v230, v230, v238\n"
    "v_max_f32 v232, v232, v239\n"
    "v_mul_f32 v230, 0xbe0293ee, v230\n"
    "v_mul_f32 v232, 0xbe0293ee, v232\n"
    "v_pk_fma_f32 v[172:173], v[172:173], s[18:19], v[230:231] op_sel_hi:[1,0,0]\n"
    "v_pk_fma_f32 v[174:175], v[174:175], s[18:19], v[230:231] op_sel_hi:[1,0,0]\n"
    "v_pk_fma_f32 v[176:177], v[176:177], s[18:19], v[232:233] op_sel_hi:[1,0,0]\n"
    "v_pk_fma_f32 v[178:179], v[178:179], s[18:19], v[232:233] op_sel_hi:[1,0,0]\n"
    "v_fma_f32 v224, v224, s18, v230\n"
    "v_fma_f32 v226, v226, s18, v232\n"
    "v_exp_f32 v172, v172\n"
    "v_exp_f32 v173, v173\n"
    "v_exp_f32 v174, v174\n"
    "v_exp_f32 v175, v175\n"
    "buffer_load_dwordx4 v[32:35], v7, s[36:39], s7 offen sc1\n"
    "v_exp_f32 v176, v176\n"
    "v_exp_f32 v177, v177\n"
    "v_exp_f32 v178, v178\n"
    "v_exp_f32 v179, v179\n"
    "buffer_load_dwordx4 v[36:39], v7, s[36:39], s7 offen offset:1024 sc1\n"
    "v_exp_f32 v224, v224\n"
    "v_exp_f32 v226, v226\n"
    "v_pk_add_f32 v[230:231], v[172:173], v[174:175]\n"
    "v_pk_add_f32 v[232:233], v[176:177], v[178:179]\n"
    "v_add_f32 v230, v230, v231\n"
    "v_add_f32 v232, v232, v233\n"
    "v_mov_b32 v238, v230\n"
    "v_mov_b32 v239, v232\n"
    "s_nop 0\n"
    "v_permlane16_swap_b32 v230, v238\n"
    "v_permlane16_swap_b32 v232, v239\n"
    "v_add_f32 v230, v230, v238\n"
    "v_add_f32 v232, v232, v239\n"
    "v_fmac_f32 v230, v228, v224\n"
    "v_fmac_f32 v232, v229, v226\n"
    "v_rcp_f32 v230, v230\n"
    "v_rcp_f32 v232, v232\n"
    "buffer_load_dwordx4 v[40:43], v7, s[36:39], s7 offen offset:2048 sc1\n"
    "v_mul_f32 v224, v224, v230\n"
    "v_mul_f32 v226, v226, v232\n"
    "v_pk_mul_f32 v[164:165], v[164:165], v[226:227] op_sel_hi:[1,0]\n"
    "v_pk_mul_f32 v[166:167], v[166:167], v[226:227] op_sel_hi:[1,0]\n"
    "v_pk_mul_f32 v[168:169], v[168:169], v[226:227] op_sel_hi:[1,0]\n"
    "v_pk_mul_f32 v[170:171], v[170:171], v[226:227] op_sel_hi:[1,0]\n"
    "v_pk_mul_f32 v[176:177], v[176:177], v[232:233] op_sel_hi:[1,0]\n"
    "v_pk_mul_f32 v[178:179], v[178:179], v[232:233] op_sel_hi:[1,0]\n"
    "v_pk_mul_f32 v[172:173], v[172:173], v[230:231] op_sel_hi:[1,0]\n"
    "v_pk_mul_f32 v[174:175], v[174:175], v[230:231] op_sel_hi:[1,0]\n"
    "v_pk_mul_f32 v[160:161], v[160:161], v[224:225] op_sel_hi:[1,0]\n"
    "v_pk_mul_f32 v[162:163], v[162:163], v[224:225] op_sel_hi:[1,0]\n"
    "v_cvt_pk_bf16_f32 v216, v164, v165\n"
    "v_cvt_pk_bf16_f32 v217, v166, v167\n"
    "v_cvt_pk_bf16_f32 v218, v168, v169\n"
    "v_cvt_pk_bf16_f32 v219, v170, v171\n"
    "v_cvt_pk_bf16_f32 v220, v176, v177\n"
    "v_cvt_pk_bf16_f32 v221, v178, v179\n"
    "v_cvt_pk_bf16_f32 v212, v172, v173\n"
    "v_cvt_pk_bf16_f32 v213, v174, v175\n"
    "v_cvt_pk_bf16_f32 v214, v160, v161\n"
    "v_cvt_pk_bf16_f32 v215, v162, v163\n"
    "v_mfma_f32_16x16x32_bf16 v[188:191], v[82:85], v[216:219], 0\n"
    "buffer_load_dwordx4 v[44:47], v7, s[36:39], s7 offen offset:3072 sc1\n"
    "v_mfma_f32_16x16x32_bf16 v[192:195], v[88:91], v[216:219], 0\n"
    "v_mfma_f32_16x16x32_bf16 v[180:183], v[80:83], v[212:215], 0\n"
    "v_mfma_f32_16x16x32_bf16 v[184:187], v[86:89], v[212:215], 0\n"
    "buffer_load_dwordx4 v[48:51], v7, s[36:39], s4 offen sc1\n"
    "v_mfma_f32_16x16x16_bf16 v[188:191], v[80:81], v[220:221], v[188:191]\n"
    "v_mfma_f32_16x16x16_bf16 v[192:195], v[86:87], v[220:221], v[192:195]\n"
    "v_mfma_f32_16x16x32_bf16 v[204:207], v[94:97], v[216:219], 0\n"
    "buffer_load_dwordx4 v[52:55], v7, s[36:39], s4 offen offset:1024 sc1\n"
    "v_mfma_f32_16x16x32_bf16 v[208:211], v[100:103], v[216:219], 0\n"
    "v_cvt_pk_bf16_f32 v180, v180, v181\n"
    "v_cvt_pk_bf16_f32 v181, v182, v183\n"
    "v_mfma_f32_16x16x32_bf16 v[196:199], v[92:95], v[212:215], 0\n"
    "v_cvt_pk_bf16_f32 v182, v184, v185\n"
    "v_cvt_pk_bf16_f32 v183, v186, v187\n"
    "v_mfma_f32_16x16x32_bf16 v[200:203], v[98:101], v[212:215], 0\n"
    "buffer_load_dwordx4 v[56:59], v7, s[36:39], s4 offen offset:2048 sc1\n"
    "buffer_store_dwordx4 v[180:183], v0, s[36:39], s75 offen sc1\n"
    "v_cvt_pk_bf16_f32 v188, v188, v189\n"
    "v_mfma_f32_16x16x16_bf16 v[204:207], v[92:93], v[220:221], v[204:207]\n"
    "v_cvt_pk_bf16_f32 v189, v190, v191\n"
    "v_cvt_pk_bf16_f32 v190, v192, v193\n"
    "v_mfma_f32_16x16x16_bf16 v[208:211], v[98:99], v[220:221], v[208:211]\n"
    "v_cvt_pk_bf16_f32 v191, v194, v195\n"
    "buffer_store_dwordx4 v[188:191], v0, s[36:39], s75 offen offset:1024 sc1\n"
    "s_nop 1\n"
    "v_mfma_f32_16x16x32_bf16 v[188:191], v[106:109], v[216:219], 0\n"
    "buffer_load_dwordx4 v[60:63], v7, s[36:39], s4 offen offset:3072 sc1\n"
    "v_mfma_f32_16x16x32_bf16 v[192:195], v[112:115], v[216:219], 0\n"
    "v_cvt_pk_bf16_f32 v196, v196, v197\n"
    "v_cvt_pk_bf16_f32 v197, v198, v199\n"
    "v_mfma_f32_16x16x32_bf16 v[180:183], v[104:107], v[212:215], 0\n"
    "v_cvt_pk_bf16_f32 v198, v200, v201\n"
    "v_cvt_pk_bf16_f32 v199, v202, v203\n"
    "v_mfma_f32_16x16x32_bf16 v[184:187], v[110:113], v[212:215], 0\n"
    "buffer_load_dwordx4 v[64:67], v7, s[36:39], s5 offen sc1\n"
    "buffer_store_dwordx4 v[196:199], v0, s[36:39], s75 offen offset:2048 sc1\n"
    "v_cvt_pk_bf16_f32 v204, v204, v205\n"
    "v_mfma_f32_16x16x16_bf16 v[188:191], v[104:105], v[220:221], v[188:191]\n"
    "v_cvt_pk_bf16_f32 v205, v206, v207\n"
    "v_cvt_pk_bf16_f32 v206, v208, v209\n"
    "v_mfma_f32_16x16x16_bf16 v[192:195], v[110:111], v[220:221], v[192:195]\n"
    "v_cvt_pk_bf16_f32 v207, v210, v211\n"
    "buffer_store_dwordx4 v[204:207], v0, s[36:39], s75 offen offset:3072 sc1\n"
    "s_nop 1\n"
    "v_mfma_f32_16x16x32_bf16 v[204:207], v[118:121], v[216:219], 0\n"
    "buffer_load_dwordx4 v[68:71], v7, s[36:39], s5 offen offset:1024 sc1\n"
    "v_mfma_f32_16x16x32_bf16 v[208:211], v[124:127], v[216:219], 0\n"
    "v_cvt_pk_bf16_f32 v180, v180, v181\n"
    "v_cvt_pk_bf16_f32 v181, v182, v183\n"
    "v_mfma_f32_16x16x32_bf16 v[196:199], v[116:119], v[212:215], 0\n"
    "v_cvt_pk_bf16_f32 v182, v184, v185\n"
    "v_cvt_pk_bf16_f32 v183, v186, v187\n"
    "v_mfma_f32_16x16x32_bf16 v[200:203], v[122:125], v[212:215], 0\n"
    "buffer_load_dwordx4 v[72:75], v7, s[36:39], s5 offen offset:2048 sc1\n"
    "buffer_store_dwordx4 v[180:183], v0, s[36:39], s76 offen sc1\n"
    "v_cvt_pk_bf16_f32 v188, v188, v189\n"
    "v_mfma_f32_16x16x16_bf16 v[204:207], v[116:117], v[220:221], v[204:207]\n"
    "v_cvt_pk_bf16_f32 v189, v190, v191\n"
    "v_cvt_pk_bf16_f32 v190, v192, v193\n"
    "v_mfma_f32_16x16x16_bf16 v[208:211], v[122:123], v[220:221], v[208:211]\n"
    "v_cvt_pk_bf16_f32 v191, v194, v195\n"
    "buffer_store_dwordx4 v[188:191], v0, s[36:39], s76 offen offset:1024 sc1\n"
    "v_cvt_pk_bf16_f32 v196, v196, v197\n"
    "v_cvt_pk_bf16_f32 v197, v198, v199\n"
    "v_cvt_pk_bf16_f32 v198, v200, v201\n"
    "v_cvt_pk_bf16_f32 v199, v202, v203\n"
    "buffer_store_dwordx4 v[196:199], v0, s[36:39], s76 offen offset:2048 sc1\n"
    "v_cvt_pk_bf16_f32 v204, v204, v205\n"
    "v_cvt_pk_bf16_f32 v205, v206, v207\n"
    "v_cvt_pk_bf16_f32 v206, v208, v209\n"
    "v_cvt_pk_bf16_f32 v207, v210, v211\n"
    "buffer_store_dwordx4 v[204:207], v0, s[36:39], s76 offen offset:3072 sc1\n"
    "buffer_load_dwordx4 v[76:79], v7, s[36:39], s5 offen offset:3072 sc1\n"
    "s_nop 7\n"
    "buffer_load_dwordx4 v[82:85], v7, s[36:39], s8 offen sc1\n"
    "s_nop 7\n"
    "buffer_load_dwordx4 v[88:91], v7, s[36:39], s8 offen offset:1024 sc1\n"
    "s_nop 7\n"
    "buffer_load_dwordx4 v[94:97], v7, s[36:39], s8 offen offset:2048 sc1\n"
    "s_nop 7\n"
    "buffer_load_dwordx4 v[100:103], v7, s[36:39], s8 offen offset:3072 sc1\n"
    "s_nop 7\n"
    "buffer_load_dwordx4 v[106:109], v7, s[36:39], s9 offen sc1\n"
    "s_nop 7\n"
    "buffer_load_dwordx4 v[112:115], v7, s[36:39], s9 offen offset:1024 sc1\n"
    "s_nop 7\n"
    "buffer_load_dwordx4 v[118:121], v7, s[36:39], s9 offen offset:2048 sc1\n"
    "s_nop 7\n"
    "buffer_load_dwordx4 v[124:127], v7, s[36:39], s9 offen offset:3072 sc1\n"
    "s_waitcnt vmcnt(9)\n"
    "v_mov_b32 v9, s68\n"
    "s_mov_b64 exec, 15\n"
    "buffer_store_dword v9, v2, s[36:39], 0 offen sc1\n"
    "s_mov_b64 exec, -1\n"
    "s_add_u32 s68, s68, 1\n"
    "s_cmp_lg_u32 s70, 0\n"
    "s_cbranch_scc1 .Latt_top_%=\n"
    ".Latt_LOOPE_%=:\n"
    "s_cmp_lg_u32 s68, 64\n"
    "s_cbranch_scc1 .Latt_waitp_%=\n"
    "s_waitcnt vmcnt(0)\n"
    "s_branch .Latt_FIN_%=\n"
    ".Latt_waitp_%=:\n"
    "s_sub_u32 s72, s68, 1\n"
    "v_add_u32 v11, v8, v5\n"
    "v_cmp_gt_i32 vcc, s72, v11\n"
    "s_nop 0\n"
    "s_or_b64 exec, vcc, 1\n"
    "buffer_atomic_smax v8, v4, s[36:39], 0 offen sc0 sc1\n"
    "s_mov_b64 exec, -1\n"
    "s_waitcnt vmcnt(0)\n"
    "v_add_u32 v11, v8, v5\n"
    "v_cmp_gt_i32 vcc, s72, v11\n"
    "s_cbranch_vccz .Latt_gop_%=\n"
    "s_sleep 2\n"
    "s_sub_u32 s69, s69, 1\n"
    "s_cbranch_scc0 .Latt_waitp_%=\n"
    "s_mov_b32 s69, 0\n"
    ".Latt_gop_%=:\n"
    "s_and_b32 s4, s68, 7\n"
    "s_mul_i32 s4, s4, 4194304\n"
    "s_add_u32 s4, s4, s52\n"
    "s_add_u32 s5, s4, 4096\n"
    "s_add_u32 s6, s4, 16384\n"
    "s_add_u32 s7, s4, 20480\n"
    "s_add_u32 s8, s4, 32768\n"
    "s_add_u32 s9, s4, 36864\n"
    "buffer_load_dwordx4 v[16:19], v0, s[36:39], s6 offen sc1\n"
    "buffer_load_dwordx4 v[20:23], v0, s[36:39], s6 offen offset:1024 sc1\n"
    "buffer_load_dwordx4 v[24:27], v0, s[36:39], s6 offen offset:2048 sc1\n"
    "buffer_load_dwordx4 v[28:31], v0, s[36:39], s6 offen offset:3072 sc1\n"
    "buffer_load_dwordx4 v[32:35], v0, s[36:39], s7 offen sc1\n"
    "buffer_load_dwordx4 v[36:39], v0, s[36:39], s7 offen offset:1024 sc1\n"
    "buffer_load_dwordx4 v[40:43], v0, s[36:39], s7 offen offset:2048 sc1\n"
    "buffer_load_dwordx4 v[44:47], v0, s[36:39], s7 offen offset:3072 sc1\n"
    "buffer_load_dwordx4 v[48:51], v0, s[36:39], s4 offen sc1\n"
    "buffer_load_dwordx4 v[52:55], v0, s[36:39], s4 offen offset:1024 sc1\n"
    "buffer_load_dwordx4 v[56:59], v0, s[36:39], s4 offen offset:2048 sc1\n"
    "buffer_load_dwordx4 v[60:63], v0, s[36:39], s4 offen offset:3072 sc1\n"
    "buffer_load_dwordx4 v[64:67], v0, s[36:39], s5 offen sc1\n"
    "buffer_load_dwordx4 v[68:71], v0, s[36:39], s5 offen offset:1024 sc1\n"
    "buffer_load_dwordx4 v[72:75], v0, s[36:39], s5 offen offset:2048 sc1\n"
    "buffer_load_dwordx4 v[76:79], v0, s[36:39], s5 offen offset:3072 sc1\n"
    "buffer_load_dwordx4 v[82:85], v0, s[36:39], s8 offen sc1\n"
    "buffer_load_dwordx4 v[88:91], v0, s[36:39], s8 offen offset:1024 sc1\n"
    "buffer_load_dwordx4 v[94:97], v0, s[36:39], s8 offen offset:2048 sc1\n"
    "buffer_load_dwordx4 v[100:103], v0, s[36:39], s8 offen offset:3072 sc1\n"
    "buffer_load_dwordx4 v[106:109], v0, s[36:39], s9 offen sc1\n"
    "buffer_load_dwordx4 v[112:115], v0, s[36:39], s9 offen offset:1024 sc1\n"
    "buffer_load_dwordx4 v[118:121], v0, s[36:39], s9 offen offset:2048 sc1\n"
    "buffer_load_dwordx4 v[124:127], v0, s[36:39], s9 offen offset:3072 sc1\n"
    "s_waitcnt vmcnt(8)\n"
    "s_branch .Latt_top_%=\n"
    ".Latt_waitr_%=:\n"
    "s_sub_u32 s69, s69, 1\n"
    "s_cbranch_scc1 .Latt_deadr_%=\n"
    "s_sleep 2\n"
    "s_or_b64 exec, vcc, 1\n"
    "buffer_atomic_smax v8, v4, s[36:39], 0 offen sc0 sc1\n"
    "s_mov_b64 exec, -1\n"
    "s_waitcnt vmcnt(0)\n"
    "s_branch .Latt_pollr_%=\n"
    ".Latt_deadr_%=:\n"
    "s_mov_b32 s69, 0\n"
    "s_branch .Latt_gor_%=\n"
    ".Latt_FIN_%=:\n"
  );
}

template <int MODE>
DEVI void red_args(const Params& p, int L, int q, u32 dbg, v16u& sa, v16u& sb, v16u& va, v16u& vb) {
  const int tid = threadIdx.x, lane = tid & 63, w = (int)rfl(tid >> 6);
  const int c = lane & 15, g = lane >> 4;
  const bool rowres = MODE == 0 || (MODE == 1 && L == 0);
  const bool fin = MODE == 2 && L == 1;
  const v4u ws = make_srd(p.ws, WS_END), rx = make_srd(p.x, 2048 * 1152), ro = make_srd(p.out, 2048 * 1152);
  u32 ft = OOB;
  if (lane < 32) {
    if (MODE == 1) { if (lane < 24) ft = mbox(L, lane / 6, SL_UP + lane % 6, 4 * q + w); }
    else if (!fin) ft = mbox(MODE == 0 ? 0 : 1, lane >> 3, SL_P + (lane & 7), (MODE == 0 ? 16 * L : 0) + 4 * q + w);
  } else if (MODE != 0) {
    if (lane < 44) ft = mbox(L, (lane - 32) / 3, (MODE == 1 ? SL_WO : SL_DOWN) + (lane - 32) % 3, (MODE == 1 ? 8 : 24) + 4 * q + w);
    else if (lane == 44 && !rowres) ft = MODE == 1 ? mbox(0, q, SL_REDF, 48 + w) : mbox(L, q, SL_REDA, 48 + w);
  }
  const unsigned long long fm = __ballot(ft != OOB);
  sa = cat4(ws, rowres ? rx : ws, fin ? ro : ws, ws);
  sb = (v16u){pgb(L, MODE == 1 ? B_PA : B_PF),
              rowres ? 0u : (MODE == 1 ? pgb(1, B_X) : pgb(L, B_X1)), rowres ? 36864u : SLOT, (u32)(rowres ? 63 : (MODE == 1 ? RX2 - 1 : RX - 1)), rowres ? 128u : 1024u,
              MODE == 1 ? pgb(L, B_X1) : pgb(1, B_X), (u32)((MODE == 0 || fin) ? 63 : (MODE == 1 ? RX - 1 : RX2 - 1)),
              fin ? 0u : (MODE == 0 ? OFF_XN0 : (MODE == 1 ? pgb(L, B_XN2) : pgb(1, B_XN))), fin ? 128u : 4096u,
              rowres ? 0u : ~0u, dbg + w * 64, fin ? 0u : ~0u, (u32)fm, (u32)(fm >> 32), (u32)(L == 0 ? 0 : (L == 1 ? LN_B : LN_C)), (u32)(L == 0 ? LN_B - 1 : (L == 1 ? LN_C - 1 : 63))};
  const int need = w == 0 ? 0x10F : (w == 1 ? 0x23F : (w == 2 ? 0x4F0 : 0xF00));
  const bool in = MODE != 0 && lane < 48 && ((need >> (lane % 12)) & 1);
  const bool back = MODE != 0 && lane >= 48 && lane < 52 && !fin;
  auto frag = [&](int I) { return kb_off(2 * (I >> 1)) + (u32)((I & 1) * 512 + (q >> 1) * 1024 + (c >> 3) * 256 + (8 * (q & 1) + (c & 7)) * 16); };
  auto rowm = [&](int I) { return (u32)((8 * q + (c & 7)) * 1152 + (2 * I + (c >> 3)) * 16); };
  const int I0 = 8 * w + 2 * (g & 1) + (g >> 1);
  const u32 pv = (u32)(q * CHUNK + w * 2048 + lane * 16), pv8 = (u32)(4 * CHUNK + (4 * q + w) * 256 + lane * 16);
  va[0] = mbox(L, q, MODE == 1 ? SL_REDA : SL_REDF, lane);
  va[1] = (u32)(in ? 0 : (back ? (MODE == 1 ? RX : RX2) : 1000));
  va[2] = (u32)g * 5 * CHUNK + frag(8 * w);
  va[3] = (u32)g * 5 * CHUNK + frag(32 + w);
  va[4] = fin ? rowm(I0) : frag(I0);
  va[5] = g == 0 ? (fin ? rowm(32 + w) : frag(32 + w)) : OOB;
  va[6] = rowres ? rowm(I0) : pv;
  va[7] = g == 0 ? (rowres ? rowm(32 + w) : pv8) : OOB;
  va[8] = pv;
  va[9] = g == 0 ? pv8 : OOB;
  va[10] = (u32)(((c & 7) * 4 + w) * 8);
  va[11] = (u32)((c & 7) * 32);
  va[12] = g < 2 ? 0x3F800000u : 0u;
  va[13] = ft; va[14] = 0; va[15] = 0;
  const u32 one = 0x3F80u << (16 * (c & 1));
#pragma unroll
  for (int d = 0; d < 4; ++d) {
    const bool on = ((c & 7) >> 1) == d;
    vb[d] = (on && c < 8) ? one : 0u;
    vb[4 + d] = (on && c >= 8) ? one : 0u;
    vb[8 + d] = (on && g == 2 * (c >> 3)) ? one : 0u;
    vb[12 + d] = (on && g == 2 * (c >> 3) + 1) ? one : 0u;
  }
#pragma unroll
  for (int i = 0; i < 16; ++i) { sa[i] = rfl(sa[i]); sb[i] = rfl(sb[i]); }
}
#define RUN_RED(TEXT) asm volatile(TEXT : "+{s[36:51]}"(sa), "+{s[52:67]}"(sb), "+{v[0:15]}"(va), "+{v[16:31]}"(vb) :: CLOB_RED)
#define CLOB_RED \
  "v32", "v33", "v34", "v35", "v36", "v37", "v38", "v39", "v40", "v41", "v42", "v43", "v44", "v45", "v46", "v47", "v48", "v49", "v50", "v51", "v52", "v53", "v54", "v55", \
  "v56", "v57", "v58", "v59", "v60", "v61", "v62", "v63", "v64", "v65", "v66", "v67", "v68", "v69", "v70", "v71", "v72", "v73", "v74", "v75", "v76", "v77", "v78", "v79", \
  "v80", "v81", "v82", "v83", "v84", "v85", "v86", "v87", "v88", "v89", "v90", "v91", "v92", "v93", "v94", "v95", "v96", "v97", "v98", "v99", "v100", "v101", "v102", "v103", \
  "v104", "v105", "v106", "v107", "v108", "v109", "v110", "v111", "v112", "v113", "v114", "v115", "v116", "v117", "v118", "v119", "v120", "v121", "v122", "v123", "v124", "v125", "v126", "v127", \
  "v128", "v129", "v130", "v131", "v132", "v133", "v134", "v135", "s68", "s69", "s70", "s71", "s72", "s73", "s74", "s75", "s76", "s77", "s78", "s79", "s80", "s81", "s82", "s83", \
  "s84", "s85", "s86", "s87", "s88", "s89", "s90", "s91", "s92", "s93", "s94", "s95", "s96", "s97", "s98", "s99", "vcc", "scc", "memory"
DEVI void role_ln0(const Params& p, int grp, int q, u32 dbg) {
  v16u sa, sb, va, vb;
  red_args<0>(p, grp, q, dbg, sa, sb, va, vb);
  RUN_RED(
    "s_nop 4\n"
    "s_mov_b32 s69, 0\n"
    "v_mov_b32 v122, -1\n"
    "s_mov_b32 s70, 262144\n"
    "s_add_u32 s69, s66, 0\n"
    "s_and_b32 s73, s69, s55\n"
    "s_mul_i32 s73, s73, s54\n"
    "s_add_u32 s73, s73, s53\n"
    "s_add_u32 s74, s73, s56\n"
    "buffer_load_dwordx4 v[40:43], v6, s[40:43], s73 offen sc1\n"
    "buffer_load_dwordx4 v[44:47], v6, s[40:43], s74 offen sc1\n"
    "buffer_load_dwordx4 v[48:51], v7, s[40:43], s73 offen sc1\n"
    "s_add_u32 s69, s66, 1\n"
    "s_and_b32 s73, s69, s55\n"
    "s_mul_i32 s73, s73, s54\n"
    "s_add_u32 s73, s73, s53\n"
    "s_add_u32 s74, s73, s56\n"
    "buffer_load_dwordx4 v[52:55], v6, s[40:43], s73 offen sc1\n"
    "buffer_load_dwordx4 v[56:59], v6, s[40:43], s74 offen sc1\n"
    "buffer_load_dwordx4 v[60:63], v7, s[40:43], s73 offen sc1\n"
    "s_add_u32 s69, s66, 2\n"
    "s_and_b32 s73, s69, s55\n"
    "s_mul_i32 s73, s73, s54\n"
    "s_add_u32 s73, s73, s53\n"
    "s_add_u32 s74, s73, s56\n"
    "buffer_load_dwordx4 v[68:71], v6, s[40:43], s73 offen sc1\n"
    "buffer_load_dwordx4 v[72:75], v6, s[40:43], s74 offen sc1\n"
    "buffer_load_dwordx4 v[76:79], v7, s[40:43], s73 offen sc1\n"
    "s_add_u32 s69, s66, 3\n"
    "s_and_b32 s73, s69, s55\n"
    "s_mul_i32 s73, s73, s54\n"
    "s_add_u32 s73, s73, s53\n"
    "s_add_u32 s74, s73, s56\n"
    "buffer_load_dwordx4 v[124:127], v6, s[40:43], s73 offen sc1\n"
    "buffer_load_dwordx4 v[128:131], v6, s[40:43], s74 offen sc1\n"
    "buffer_load_dwordx4 v[132:135], v7, s[40:43], s73 offen sc1\n"
    "s_sub_u32 s68, s66, 1\n"
    ".LrLN0_par_%=:\n"
    "buffer_load_dword v14, off, s[36:39], 0 sc1\n"
    "s_waitcnt vmcnt(0)\n"
    "v_readfirstlane_b32 s75, v14\n"
    "s_nop 0\n"
    "s_cmp_ge_i32 s75, 0\n"
    "s_cbranch_scc1 .LrLN0_par1_%=\n"
    "s_sleep 4\n"
    "s_sub_u32 s70, s70, 1\n"
    "s_cbranch_scc0 .LrLN0_par_%=\n"
    "s_mov_b32 s70, 0\n"
    ".LrLN0_par1_%=:\n"
    "s_and_b32 s75, s75, 1\n"
    "s_lshl_b32 s75, s75, 13\n"
    "s_add_u32 s36, s36, s75\n"
    "s_addc_u32 s37, s37, 0\n"
    "s_and_b32 s76, s75, s61\n"
    "s_add_u32 s40, s40, s76\n"
    "s_addc_u32 s41, s41, 0\n"
    "s_and_b32 s76, s75, s63\n"
    "s_add_u32 s44, s44, s76\n"
    "s_addc_u32 s45, s45, 0\n"
    ".LrLN0_LOOPB_%=:\n"
    ".LrLN0_top_%=:\n"
    "s_add_u32 s68, s68, 1\n"
    "s_add_u32 s69, s68, 4\n"
    "s_and_b32 s75, s68, s58\n"
    "s_mul_i32 s77, s75, 81920\n"
    "s_add_u32 s77, s77, s59\n"
    "s_add_u32 s78, s77, s60\n"
    "s_and_b32 s73, s69, s55\n"
    "s_mul_i32 s73, s73, s54\n"
    "s_add_u32 s73, s73, s53\n"
    "s_add_u32 s74, s73, s56\n"
    "s_waitcnt vmcnt(27)\n"
    "v_mfma_f32_16x16x32_bf16 v[80:83], v[24:27], v[40:43], 0\n"
    "v_mfma_f32_16x16x32_bf16 v[84:87], v[28:31], v[40:43], 0\n"
    "s_waitcnt vmcnt(26)\n"
    "v_mfma_f32_16x16x32_bf16 v[88:91], v[24:27], v[44:47], 0\n"
    "v_mfma_f32_16x16x32_bf16 v[92:95], v[28:31], v[44:47], 0\n"
    "s_waitcnt vmcnt(25)\n"
    "v_mfma_f32_16x16x32_bf16 v[96:99], v[24:27], v[48:51], 0\n"
    "buffer_load_dwordx4 v[40:43], v6, s[40:43], s73 offen sc1\n"
    "buffer_load_dwordx4 v[44:47], v6, s[40:43], s74 offen sc1\n"
    "buffer_load_dwordx4 v[48:51], v7, s[40:43], s73 offen sc1\n"
    "v_pk_add_f32 v[112:113], v[80:81], v[82:83]\n"
    "v_pk_add_f32 v[114:115], v[84:85], v[86:87]\n"
    "v_pk_add_f32 v[112:113], v[112:113], v[88:89]\n"
    "v_pk_add_f32 v[114:115], v[114:115], v[90:91]\n"
    "v_pk_add_f32 v[112:113], v[112:113], v[92:93]\n"
    "v_pk_add_f32 v[114:115], v[114:115], v[94:95]\n"
    "v_pk_add_f32 v[112:113], v[112:113], v[96:97]\n"
    "v_pk_add_f32 v[114:115], v[114:115], v[98:99]\n"
    "v_pk_add_f32 v[112:113], v[112:113], v[114:115]\n"
    "v_add_f32 v112, v112, v113\n"
    "s_nop 1\n"
    "v_add_f32_dpp v14, v112, v112 row_ror:8 row_mask:0xf bank_mask:0xf\n"
    "v_add_f32_dpp v15, v112, v112 row_ror:8 row_mask:0xf bank_mask:0xf\n"
    "s_nop 1\n"
    "v_permlane16_swap_b32 v14, v15\n"
    "v_add_f32 v112, v14, v15\n"
    "v_add_f32 v14, v14, v15\n"
    "s_nop 1\n"
    "v_permlane32_swap_b32 v112, v14\n"
    "v_add_f32 v112, v112, v14\n"
    "v_mul_f32 v112, 0x3be38e39, v112\n"
    "v_mul_f32 v114, v112, v12\n"
    "v_pk_add_f32 v[80:81], v[80:81], v[112:113] op_sel_hi:[1,0] neg_lo:[0,1] neg_hi:[0,1]\n"
    "v_pk_add_f32 v[82:83], v[82:83], v[112:113] op_sel_hi:[1,0] neg_lo:[0,1] neg_hi:[0,1]\n"
    "v_pk_add_f32 v[84:85], v[84:85], v[112:113] op_sel_hi:[1,0] neg_lo:[0,1] neg_hi:[0,1]\n"
    "v_pk_add_f32 v[86:87], v[86:87], v[112:113] op_sel_hi:[1,0] neg_lo:[0,1] neg_hi:[0,1]\n"
    "v_pk_add_f32 v[88:89], v[88:89], v[112:113] op_sel_hi:[1,0] neg_lo:[0,1] neg_hi:[0,1]\n"
    "v_pk_add_f32 v[90:91], v[90:91], v[112:113] op_sel_hi:[1,0] neg_lo:[0,1] neg_hi:[0,1]\n"
    "v_pk_add_f32 v[92:93], v[92:93], v[112:113] op_sel_hi:[1,0] neg_lo:[0,1] neg_hi:[0,1]\n"
    "v_pk_add_f32 v[94:95], v[94:95], v[112:113] op_sel_hi:[1,0] neg_lo:[0,1] neg_hi:[0,1]\n"
    "v_pk_add_f32 v[96:97], v[96:97], v[114:115] op_sel_hi:[1,0] neg_lo:[0,1] neg_hi:[0,1]\n"
    "v_pk_add_f32 v[98:99], v[98:99], v[114:115] op_sel_hi:[1,0] neg_lo:[0,1] neg_hi:[0,1]\n"
    "v_pk_mul_f32 v[116:117], v[80:81], v[80:81]\n"
    "v_pk_mul_f32 v[118:119], v[82:83], v[82:83]\n"
    "v_pk_fma_f32 v[116:117], v[84:85], v[84:85], v[116:117]\n"
    "v_pk_fma_f32 v[118:119], v[86:87], v[86:87], v[118:119]\n"
    "v_pk_fma_f32 v[116:117], v[88:89], v[88:89], v[116:117]\n"
    "v_pk_fma_f32 v[118:119], v[90:91], v[90:91], v[118:119]\n"
    "v_pk_fma_f32 v[116:117], v[92:93], v[92:93], v[116:117]\n"
    "v_pk_fma_f32 v[118:119], v[94:95], v[94:95], v[118:119]\n"
    "v_pk_fma_f32 v[116:117], v[96:97], v[96:97], v[116:117]\n"
    "v_pk_fma_f32 v[118:119], v[98:99], v[98:99], v[118:119]\n"
    "v_pk_add_f32 v[116:117], v[116:117], v[118:119]\n"
    "v_add_f32 v113, v116, v117\n"
    "s_nop 1\n"
    "v_add_f32_dpp v14, v113, v113 row_ror:8 row_mask:0xf bank_mask:0xf\n"
    "v_add_f32_dpp v15, v113, v113 row_ror:8 row_mask:0xf bank_mask:0xf\n"
    "s_nop 1\n"
    "v_permlane16_swap_b32 v14, v15\n"
    "v_add_f32 v113, v14, v15\n"
    "v_add_f32 v14, v14, v15\n"
    "s_nop 1\n"
    "v_permlane32_swap_b32 v113, v14\n"
    "v_add_f32 v113, v113, v14\n"
    "ds_write_b64 v10, v[112:113]\n"
    "s_waitcnt vmcnt(17)\n"
    "s_add_u32 s75, s66, 1\n"
    "s_min_i32 s75, s75, s68\n"
    "s_sub_u32 s76, s68, 2\n"
    "s_max_i32 s75, s75, s76\n"
    "s_sub_u32 s75, s75, 1\n"
    "v_mov_b32 v14, s75\n"
    "s_mov_b64 exec, s[64:65]\n"
    "buffer_store_dword v14, v13, s[36:39], 0 offen sc1\n"
    "s_mov_b64 exec, -1\n"
    "s_waitcnt lgkmcnt(0)\n"
    "s_barrier\n"
    "ds_read_b128 v[32:35], v11\n"
    "ds_read_b128 v[36:39], v11 offset:16\n"
    "v_xor_b32 v10, 256, v10\n"
    "v_xor_b32 v11, 256, v11\n"
    "s_waitcnt lgkmcnt(0)\n"
    "v_pk_add_f32 v[114:115], v[32:33], v[34:35]\n"
    "v_pk_add_f32 v[116:117], v[36:37], v[38:39]\n"
    "v_pk_add_f32 v[114:115], v[114:115], v[116:117]\n"
    "v_fmac_f32 v32, 0xbe800000, v114\n"
    "v_fmac_f32 v34, 0xbe800000, v114\n"
    "v_fmac_f32 v36, 0xbe800000, v114\n"
    "v_fmac_f32 v38, 0xbe800000, v114\n"
    "v_fmac_f32 v112, 0xbe800000, v114\n"
    "v_mul_f32 v116, v32, v32\n"
    "v_fmac_f32 v116, v34, v34\n"
    "v_fmac_f32 v116, v36, v36\n"
    "v_fmac_f32 v116, v38, v38\n"
    "v_fmac_f32 v115, 0x43100000, v116\n"
    "v_mov_b32 v116, 0x3727c5ac\n"
    "v_fmac_f32 v116, 0x3ae38e39, v115\n"
    "v_rsq_f32 v116, v116\n"
    "s_nop 0\n"
    "v_mul_f32 v114, v112, v116\n"
    "v_pk_fma_f32 v[80:81], v[80:81], v[116:117], v[114:115] op_sel_hi:[1,0,0]\n"
    "v_pk_fma_f32 v[82:83], v[82:83], v[116:117], v[114:115] op_sel_hi:[1,0,0]\n"
    "v_pk_fma_f32 v[84:85], v[84:85], v[116:117], v[114:115] op_sel_hi:[1,0,0]\n"
    "v_pk_fma_f32 v[86:87], v[86:87], v[116:117], v[114:115] op_sel_hi:[1,0,0]\n"
    "v_pk_fma_f32 v[88:89], v[88:89], v[116:117], v[114:115] op_sel_hi:[1,0,0]\n"
    "v_pk_fma_f32 v[90:91], v[90:91], v[116:117], v[114:115] op_sel_hi:[1,0,0]\n"
    "v_pk_fma_f32 v[92:93], v[92:93], v[116:117], v[114:115] op_sel_hi:[1,0,0]\n"
    "v_pk_fma_f32 v[94:95], v[94:95], v[116:117], v[114:115] op_sel_hi:[1,0,0]\n"
    "v_pk_fma_f32 v[96:97], v[96:97], v[116:117], v[114:115] op_sel_hi:[1,0,0]\n"
    "v_pk_fma_f32 v[98:99], v[98:99], v[116:117], v[114:115] op_sel_hi:[1,0,0]\n"
    "v_cvt_pk_bf16_f32 v100, v80, v81\n"
    "v_cvt_pk_bf16_f32 v101, v82, v83\n"
    "v_cvt_pk_bf16_f32 v102, v84, v85\n"
    "v_cvt_pk_bf16_f32 v103, v86, v87\n"
    "v_cvt_pk_bf16_f32 v104, v88, v89\n"
    "v_cvt_pk_bf16_f32 v105, v90, v91\n"
    "v_cvt_pk_bf16_f32 v106, v92, v93\n"
    "v_cvt_pk_bf16_f32 v107, v94, v95\n"
    "v_cvt_pk_bf16_f32 v108, v96, v97\n"
    "v_cvt_pk_bf16_f32 v109, v98, v99\n"
    "v_permlane16_swap_b32 v100, v102\n"
    "v_permlane16_swap_b32 v101, v103\n"
    "buffer_store_dwordx4 v[100:103], v4, s[44:47], s77 offen sc1\n"
    "v_permlane16_swap_b32 v104, v106\n"
    "v_permlane16_swap_b32 v105, v107\n"
    "buffer_store_dwordx4 v[104:107], v4, s[44:47], s78 offen sc1\n"
    "v_permlane16_swap_b32 v108, v110\n"
    "v_permlane16_swap_b32 v109, v111\n"
    "buffer_store_dwordx4 v[108:111], v5, s[44:47], s77 offen sc1\n"
    "s_cmp_eq_u32 s68, s66\n"
    "s_cbranch_scc1 .LrLN0_first_%=\n"
    ".LrLN0_first1_%=:\n"
    "s_add_u32 s68, s68, 1\n"
    "s_add_u32 s69, s68, 4\n"
    "s_and_b32 s75, s68, s58\n"
    "s_mul_i32 s77, s75, 81920\n"
    "s_add_u32 s77, s77, s59\n"
    "s_add_u32 s78, s77, s60\n"
    "s_and_b32 s73, s69, s55\n"
    "s_mul_i32 s73, s73, s54\n"
    "s_add_u32 s73, s73, s53\n"
    "s_add_u32 s74, s73, s56\n"
    "s_waitcnt vmcnt(27)\n"
    "v_mfma_f32_16x16x32_bf16 v[80:83], v[24:27], v[52:55], 0\n"
    "v_mfma_f32_16x16x32_bf16 v[84:87], v[28:31], v[52:55], 0\n"
    "s_waitcnt vmcnt(26)\n"
    "v_mfma_f32_16x16x32_bf16 v[88:91], v[24:27], v[56:59], 0\n"
    "v_mfma_f32_16x16x32_bf16 v[92:95], v[28:31], v[56:59], 0\n"
    "s_waitcnt vmcnt(25)\n"
    "v_mfma_f32_16x16x32_bf16 v[96:99], v[24:27], v[60:63], 0\n"
    "buffer_load_dwordx4 v[52:55], v6, s[40:43], s73 offen sc1\n"
    "buffer_load_dwordx4 v[56:59], v6, s[40:43], s74 offen sc1\n"
    "buffer_load_dwordx4 v[60:63], v7, s[40:43], s73 offen sc1\n"
    "v_pk_add_f32 v[112:113], v[80:81], v[82:83]\n"
    "v_pk_add_f32 v[114:115], v[84:85], v[86:87]\n"
    "v_pk_add_f32 v[112:113], v[112:113], v[88:89]\n"
    "v_pk_add_f32 v[114:115], v[114:115], v[90:91]\n"
    "v_pk_add_f32 v[112:113], v[112:113], v[92:93]\n"
    "v_pk_add_f32 v[114:115], v[114:115], v[94:95]\n"
    "v_pk_add_f32 v[112:113], v[112:113], v[96:97]\n"
    "v_pk_add_f32 v[114:115], v[114:115], v[98:99]\n"
    "v_pk_add_f32 v[112:113], v[112:113], v[114:115]\n"
    "v_add_f32 v112, v112, v113\n"
    "s_nop 1\n"
    "v_add_f32_dpp v14, v112, v112 row_ror:8 row_mask:0xf bank_mask:0xf\n"
    "v_add_f32_dpp v15, v112, v112 row_ror:8 row_mask:0xf bank_mask:0xf\n"
    "s_nop 1\n"
    "v_permlane16_swap_b32 v14, v15\n"
    "v_add_f32 v112, v14, v15\n"
    "v_add_f32 v14, v14, v15\n"
    "s_nop 1\n"
    "v_permlane32_swap_b32 v112, v14\n"
    "v_add_f32 v112, v112, v14\n"
    "v_mul_f32 v112, 0x3be38e39, v112\n"
    "v_mul_f32 v114, v112, v12\n"
    "v_pk_add_f32 v[80:81], v[80:81], v[112:113] op_sel_hi:[1,0] neg_lo:[0,1] neg_hi:[0,1]\n"
    "v_pk_add_f32 v[82:83], v[82:83], v[112:113] op_sel_hi:[1,0] neg_lo:[0,1] neg_hi:[0,1]\n"
    "v_pk_add_f32 v[84:85], v[84:85], v[112:113] op_sel_hi:[1,0] neg_lo:[0,1] neg_hi:[0,1]\n"
    "v_pk_add_f32 v[86:87], v[86:87], v[112:113] op_sel_hi:[1,0] neg_lo:[0,1] neg_hi:[0,1]\n"
    "v_pk_add_f32 v[88:89], v[88:89], v[112:113] op_sel_hi:[1,0] neg_lo:[0,1] neg_hi:[0,1]\n"
    "v_pk_add_f32 v[90:91], v[90:91], v[112:113] op_sel_hi:[1,0] neg_lo:[0,1] neg_hi:[0,1]\n"
    "v_pk_add_f32 v[92:93], v[92:93], v[112:113] op_sel_hi:[1,0] neg_lo:[0,1] neg_hi:[0,1]\n"
    "v_pk_add_f32 v[94:95], v[94:95], v[112:113] op_sel_hi:[1,0] neg_lo:[0,1] neg_hi:[0,1]\n"
    "v_pk_add_f32 v[96:97], v[96:97], v[114:115] op_sel_hi:[1,0] neg_lo:[0,1] neg_hi:[0,1]\n"
    "v_pk_add_f32 v[98:99], v[98:99], v[114:115] op_sel_hi:[1,0] neg_lo:[0,1] neg_hi:[0,1]\n"
    "v_pk_mul_f32 v[116:117], v[80:81], v[80:81]\n"
    "v_pk_mul_f32 v[118:119], v[82:83], v[82:83]\n"
    "v_pk_fma_f32 v[116:117], v[84:85], v[84:85], v[116:117]\n"
    "v_pk_fma_f32 v[118:119], v[86:87], v[86:87], v[118:119]\n"
    "v_pk_fma_f32 v[116:117], v[88:89], v[88:89], v[116:117]\n"
    "v_pk_fma_f32 v[118:119], v[90:91], v[90:91], v[118:119]\n"
    "v_pk_fma_f32 v[116:117], v[92:93], v[92:93], v[116:117]\n"
    "v_pk_fma_f32 v[118:119], v[94:95], v[94:95], v[118:119]\n"
    "v_pk_fma_f32 v[116:117], v[96:97], v[96:97], v[116:117]\n"
    "v_pk_fma_f32 v[118:119], v[98:99], v[98:99], v[118:119]\n"
    "v_pk_add_f32 v[116:117], v[116:117], v[118:119]\n"
    "v_add_f32 v113, v116, v117\n"
    "s_nop 1\n"
    "v_add_f32_dpp v14, v113, v113 row_ror:8 row_mask:0xf bank_mask:0xf\n"
    "v_add_f32_dpp v15, v113, v113 row_ror:8 row_mask:0xf bank_mask:0xf\n"
    "s_nop 1\n"
    "v_permlane16_swap_b32 v14, v15\n"
    "v_add_f32 v113, v14, v15\n"
    "v_add_f32 v14, v14, v15\n"
    "s_nop 1\n"
    "v_permlane32_swap_b32 v113, v14\n"
    "v_add_f32 v113, v113, v14\n"
    "ds_write_b64 v10, v[112:113]\n"
    "s_waitcnt vmcnt(17)\n"
    "s_add_u32 s75, s66, 1\n"
    "s_min_i32 s75, s75, s68\n"
    "s_sub_u32 s76, s68, 2\n"
    "s_max_i32 s75, s75, s76\n"
    "s_sub_u32 s75, s75, 1\n"
    "v_mov_b32 v14, s75\n"
    "s_mov_b64 exec, s[64:65]\n"
    "buffer_store_dword v14, v13, s[36:39], 0 offen sc1\n"
    "s_mov_b64 exec, -1\n"
    "s_waitcnt lgkmcnt(0)\n"
    "s_barrier\n"
    "ds_read_b128 v[32:35], v11\n"
    "ds_read_b128 v[36:39], v11 offset:16\n"
    "v_xor_b32 v10, 256, v10\n"
    "v_xor_b32 v11, 256, v11\n"
    "s_waitcnt lgkmcnt(0)\n"
    "v_pk_add_f32 v[114:115], v[32:33], v[34:35]\n"
    "v_pk_add_f32 v[116:117], v[36:37], v[38:39]\n"
    "v_pk_add_f32 v[114:115], v[114:115], v[116:117]\n"
    "v_fmac_f32 v32, 0xbe800000, v114\n"
    "v_fmac_f32 v34, 0xbe800000, v114\n"
    "v_fmac_f32 v36, 0xbe800000, v114\n"
    "v_fmac_f32 v38, 0xbe800000, v114\n"
    "v_fmac_f32 v112, 0xbe800000, v114\n"
    "v_mul_f32 v116, v32, v32\n"
    "v_fmac_f32 v116, v34, v34\n"
    "v_fmac_f32 v116, v36, v36\n"
    "v_fmac_f32 v116, v38, v38\n"
    "v_fmac_f32 v115, 0x43100000, v116\n"
    "v_mov_b32 v116, 0x3727c5ac\n"
    "v_fmac_f32 v116, 0x3ae38e39, v115\n"
    "v_rsq_f32 v116, v116\n"
    "s_nop 0\n"
    "v_mul_f32 v114, v112, v116\n"
    "v_pk_fma_f32 v[80:81], v[80:81], v[116:117], v[114:115] op_sel_hi:[1,0,0]\n"
    "v_pk_fma_f32 v[82:83], v[82:83], v[116:117], v[114:115] op_sel_hi:[1,0,0]\n"
    "v_pk_fma_f32 v[84:85], v[84:85], v[116:117], v[114:115] op_sel_hi:[1,0,0]\n"
    "v_pk_fma_f32 v[86:87], v[86:87], v[116:117], v[114:115] op_sel_hi:[1,0,0]\n"
    "v_pk_fma_f32 v[88:89], v[88:89], v[116:117], v[114:115] op_sel_hi:[1,0,0]\n"
    "v_pk_fma_f32 v[90:91], v[90:91], v[116:117], v[114:115] op_sel_hi:[1,0,0]\n"
    "v_pk_fma_f32 v[92:93], v[92:93], v[116:117], v[114:115] op_sel_hi:[1,0,0]\n"
    "v_pk_fma_f32 v[94:95], v[94:95], v[116:117], v[114:115] op_sel_hi:[1,0,0]\n"
    "v_pk_fma_f32 v[96:97], v[96:97], v[116:117], v[114:115] op_sel_hi:[1,0,0]\n"
    "v_pk_fma_f32 v[98:99], v[98:99], v[116:117], v[114:115] op_sel_hi:[1,0,0]\n"
    "v_cvt_pk_bf16_f32 v100, v80, v81\n"
    "v_cvt_pk_bf16_f32 v101, v82, v83\n"
    "v_cvt_pk_bf16_f32 v102, v84, v85\n"
    "v_cvt_pk_bf16_f32 v103, v86, v87\n"
    "v_cvt_pk_bf16_f32 v104, v88, v89\n"
    "v_cvt_pk_bf16_f32 v105, v90, v91\n"
    "v_cvt_pk_bf16_f32 v106, v92, v93\n"
    "v_cvt_pk_bf16_f32 v107, v94, v95\n"
    "v_cvt_pk_bf16_f32 v108, v96, v97\n"
    "v_cvt_pk_bf16_f32 v109, v98, v99\n"
    "v_permlane16_swap_b32 v100, v102\n"
    "v_permlane16_swap_b32 v101, v103\n"
    "buffer_store_dwordx4 v[100:103], v4, s[44:47], s77 offen sc1\n"
    "v_permlane16_swap_b32 v104, v106\n"
    "v_permlane16_swap_b32 v105, v107\n"
    "buffer_store_dwordx4 v[104:107], v4, s[44:47], s78 offen sc1\n"
    "v_permlane16_swap_b32 v108, v110\n"
    "v_permlane16_swap_b32 v109, v111\n"
    "buffer_store_dwordx4 v[108:111], v5, s[44:47], s77 offen sc1\n"
    "s_add_u32 s68, s68, 1\n"
    "s_add_u32 s69, s68, 4\n"
    "s_and_b32 s75, s68, s58\n"
    "s_mul_i32 s77, s75, 81920\n"
    "s_add_u32 s77, s77, s59\n"
    "s_add_u32 s78, s77, s60\n"
    "s_and_b32 s73, s69, s55\n"
    "s_mul_i32 s73, s73, s54\n"
    "s_add_u32 s73, s73, s53\n"
    "s_add_u32 s74, s73, s56\n"
    "s_waitcnt vmcnt(27)\n"
    "v_mfma_f32_16x16x32_bf16 v[80:83], v[24:27], v[68:71], 0\n"
    "v_mfma_f32_16x16x32_bf16 v[84:87], v[28:31], v[68:71], 0\n"
    "s_waitcnt vmcnt(26)\n"
    "v_mfma_f32_16x16x32_bf16 v[88:91], v[24:27], v[72:75], 0\n"
    "v_mfma_f32_16x16x32_bf16 v[92:95], v[28:31], v[72:75], 0\n"
    "s_waitcnt vmcnt(25)\n"
    "v_mfma_f32_16x16x32_bf16 v[96:99], v[24:27], v[76:79], 0\n"
    "buffer_load_dwordx4 v[68:71], v6, s[40:43], s73 offen sc1\n"
    "buffer_load_dwordx4 v[72:75], v6, s[40:43], s74 offen sc1\n"
    "buffer_load_dwordx4 v[76:79], v7, s[40:43], s73 offen sc1\n"
    "v_pk_add_f32 v[112:113], v[80:81], v[82:83]\n"
    "v_pk_add_f32 v[114:115], v[84:85], v[86:87]\n"
    "v_pk_add_f32 v[112:113], v[112:113], v[88:89]\n"
    "v_pk_add_f32 v[114:115], v[114:115], v[90:91]\n"
    "v_pk_add_f32 v[112:113], v[112:113], v[92:93]\n"
    "v_pk_add_f32 v[114:115], v[114:115], v[94:95]\n"
    "v_pk_add_f32 v[112:113], v[112:113], v[96:97]\n"
    "v_pk_add_f32 v[114:115], v[114:115], v[98:99]\n"
    "v_pk_add_f32 v[112:113], v[112:113], v[114:115]\n"
    "v_add_f32 v112, v112, v113\n"
    "s_nop 1\n"
    "v_add_f32_dpp v14, v112, v112 row_ror:8 row_mask:0xf bank_mask:0xf\n"
    "v_add_f32_dpp v15, v112, v112 row_ror:8 row_mask:0xf bank_mask:0xf\n"
    "s_nop 1\n"
    "v_permlane16_swap_b32 v14, v15\n"
    "v_add_f32 v112, v14, v15\n"
    "v_add_f32 v14, v14, v15\n"
    "s_nop 1\n"
    "v_permlane32_swap_b32 v112, v14\n"
    "v_add_f32 v112, v112, v14\n"
    "v_mul_f32 v112, 0x3be38e39, v112\n"
    "v_mul_f32 v114, v112, v12\n"
    "v_pk_add_f32 v[80:81], v[80:81], v[112:113] op_sel_hi:[1,0] neg_lo:[0,1] neg_hi:[0,1]\n"
    "v_pk_add_f32 v[82:83], v[82:83], v[112:113] op_sel_hi:[1,0] neg_lo:[0,1] neg_hi:[0,1]\n"
    "v_pk_add_f32 v[84:85], v[84:85], v[112:113] op_sel_hi:[1,0] neg_lo:[0,1] neg_hi:[0,1]\n"
    "v_pk_add_f32 v[86:87], v[86:87], v[112:113] op_sel_hi:[1,0] neg_lo:[0,1] neg_hi:[0,1]\n"
    "v_pk_add_f32 v[88:89], v[88:89], v[112:113] op_sel_hi:[1,0] neg_lo:[0,1] neg_hi:[0,1]\n"
    "v_pk_add_f32 v[90:91], v[90:91], v[112:113] op_sel_hi:[1,0] neg_lo:[0,1] neg_hi:[0,1]\n"
    "v_pk_add_f32 v[92:93], v[92:93], v[112:113] op_sel_hi:[1,0] neg_lo:[0,1] neg_hi:[0,1]\n"
    "v_pk_add_f32 v[94:95], v[94:95], v[112:113] op_sel_hi:[1,0] neg_lo:[0,1] neg_hi:[0,1]\n"
    "v_pk_add_f32 v[96:97], v[96:97], v[114:115] op_sel_hi:[1,0] neg_lo:[0,1] neg_hi:[0,1]\n"
    "v_pk_add_f32 v[98:99], v[98:99], v[114:115] op_sel_hi:[1,0] neg_lo:[0,1] neg_hi:[0,1]\n"
    "v_pk_mul_f32 v[116:117], v[80:81], v[80:81]\n"
    "v_pk_mul_f32 v[118:119], v[82:83], v[82:83]\n"
    "v_pk_fma_f32 v[116:117], v[84:85], v[84:85], v[116:117]\n"
    "v_pk_fma_f32 v[118:119], v[86:87], v[86:87], v[118:119]\n"
    "v_pk_fma_f32 v[116:117], v[88:89], v[88:89], v[116:117]\n"
    "v_pk_fma_f32 v[118:119], v[90:91], v[90:91], v[118:119]\n"
    "v_pk_fma_f32 v[116:117], v[92:93], v[92:93], v[116:117]\n"
    "v_pk_fma_f32 v[118:119], v[94:95], v[94:95], v[118:119]\n"
    "v_pk_fma_f32 v[116:117], v[96:97], v[96:97], v[116:117]\n"
    "v_pk_fma_f32 v[118:119], v[98:99], v[98:99], v[118:119]\n"
    "v_pk_add_f32 v[116:117], v[116:117], v[118:119]\n"
    "v_add_f32 v113, v116, v117\n"
    "s_nop 1\n"
    "v_add_f32_dpp v14, v113, v113 row_ror:8 row_mask:0xf bank_mask:0xf\n"
    "v_add_f32_dpp v15, v113, v113 row_ror:8 row_mask:0xf bank_mask:0xf\n"
    "s_nop 1\n"
    "v_permlane16_swap_b32 v14, v15\n"
    "v_add_f32 v113, v14, v15\n"
    "v_add_f32 v14, v14, v15\n"
    "s_nop 1\n"
    "v_permlane32_swap_b32 v113, v14\n"
    "v_add_f32 v113, v113, v14\n"
    "ds_write_b64 v10, v[112:113]\n"
    "s_waitcnt vmcnt(17)\n"
    "s_add_u32 s75, s66, 1\n"
    "s_min_i32 s75, s75, s68\n"
    "s_sub_u32 s76, s68, 2\n"
    "s_max_i32 s75, s75, s76\n"
    "s_sub_u32 s75, s75, 1\n"
    "v_mov_b32 v14, s75\n"
    "s_mov_b64 exec, s[64:65]\n"
    "buffer_store_dword v14, v13, s[36:39], 0 offen sc1\n"
    "s_mov_b64 exec, -1\n"
    "s_waitcnt lgkmcnt(0)\n"
    "s_barrier\n"
    "ds_read_b128 v[32:35], v11\n"
    "ds_read_b128 v[36:39], v11 offset:16\n"
    "v_xor_b32 v10, 256, v10\n"
    "v_xor_b32 v11, 256, v11\n"
    "s_waitcnt lgkmcnt(0)\n"
    "v_pk_add_f32 v[114:115], v[32:33], v[34:35]\n"
    "v_pk_add_f32 v[116:117], v[36:37], v[38:39]\n"
    "v_pk_add_f32 v[114:115], v[114:115], v[116:117]\n"
    "v_fmac_f32 v32, 0xbe800000, v114\n"
    "v_fmac_f32 v34, 0xbe800000, v114\n"
    "v_fmac_f32 v36, 0xbe800000, v114\n"
    "v_fmac_f32 v38, 0xbe800000, v114\n"
    "v_fmac_f32 v112, 0xbe800000, v114\n"
    "v_mul_f32 v116, v32, v32\n"
    "v_fmac_f32 v116, v34, v34\n"
    "v_fmac_f32 v116, v36, v36\n"
    "v_fmac_f32 v116, v38, v38\n"
    "v_fmac_f32 v115, 0x43100000, v116\n"
    "v_mov_b32 v116, 0x3727c5ac\n"
    "v_fmac_f32 v116, 0x3ae38e39, v115\n"
    "v_rsq_f32 v116, v116\n"
    "s_nop 0\n"
    "v_mul_f32 v114, v112, v116\n"
    "v_pk_fma_f32 v[80:81], v[80:81], v[116:117], v[114:115] op_sel_hi:[1,0,0]\n"
    "v_pk_fma_f32 v[82:83], v[82:83], v[116:117], v[114:115] op_sel_hi:[1,0,0]\n"
    "v_pk_fma_f32 v[84:85], v[84:85], v[116:117], v[114:115] op_sel_hi:[1,0,0]\n"
    "v_pk_fma_f32 v[86:87], v[86:87], v[116:117], v[114:115] op_sel_hi:[1,0,0]\n"
    "v_pk_fma_f32 v[88:89], v[88:89], v[116:117], v[114:115] op_sel_hi:[1,0,0]\n"
    "v_pk_fma_f32 v[90:91], v[90:91], v[116:117], v[114:115] op_sel_hi:[1,0,0]\n"
    "v_pk_fma_f32 v[92:93], v[92:93], v[116:117], v[114:115] op_sel_hi:[1,0,0]\n"
    "v_pk_fma_f32 v[94:95], v[94:95], v[116:117], v[114:115] op_sel_hi:[1,0,0]\n"
    "v_pk_fma_f32 v[96:97], v[96:97], v[116:117], v[114:115] op_sel_hi:[1,0,0]\n"
    "v_pk_fma_f32 v[98:99], v[98:99], v[116:117], v[114:115] op_sel_hi:[1,0,0]\n"
    "v_cvt_pk_bf16_f32 v100, v80, v81\n"
    "v_cvt_pk_bf16_f32 v101, v82, v83\n"
    "v_cvt_pk_bf16_f32 v102, v84, v85\n"
    "v_cvt_pk_bf16_f32 v103, v86, v87\n"
    "v_cvt_pk_bf16_f32 v104, v88, v89\n"
    "v_cvt_pk_bf16_f32 v105, v90, v91\n"
    "v_cvt_pk_bf16_f32 v106, v92, v93\n"
    "v_cvt_pk_bf16_f32 v107, v94, v95\n"
    "v_cvt_pk_bf16_f32 v108, v96, v97\n"
    "v_cvt_pk_bf16_f32 v109, v98, v99\n"
    "v_permlane16_swap_b32 v100, v102\n"
    "v_permlane16_swap_b32 v101, v103\n"
    "buffer_store_dwordx4 v[100:103], v4, s[44:47], s77 offen sc1\n"
    "v_permlane16_swap_b32 v104, v106\n"
    "v_permlane16_swap_b32 v105, v107\n"
    "buffer_store_dwordx4 v[104:107], v4, s[44:47], s78 offen sc1\n"
    "v_permlane16_swap_b32 v108, v110\n"
    "v_permlane16_swap_b32 v109, v111\n"
    "buffer_store_dwordx4 v[108:111], v5, s[44:47], s77 offen sc1\n"
    "s_add_u32 s68, s68, 1\n"
    "s_add_u32 s69, s68, 4\n"
    "s_and_b32 s75, s68, s58\n"
    "s_mul_i32 s77, s75, 81920\n"
    "s_add_u32 s77, s77, s59\n"
    "s_add_u32 s78, s77, s60\n"
    "s_and_b32 s73, s69, s55\n"
    "s_mul_i32 s73, s73, s54\n"
    "s_add_u32 s73, s73, s53\n"
    "s_add_u32 s74, s73, s56\n"
    "s_waitcnt vmcnt(27)\n"
    "v_mfma_f32_16x16x32_bf16 v[80:83], v[24:27], v[124:127], 0\n"
    "v_mfma_f32_16x16x32_bf16 v[84:87], v[28:31], v[124:127], 0\n"
    "s_waitcnt vmcnt(26)\n"
    "v_mfma_f32_16x16x32_bf16 v[88:91], v[24:27], v[128:131], 0\n"
    "v_mfma_f32_16x16x32_bf16 v[92:95], v[28:31], v[128:131], 0\n"
    "s_waitcnt vmcnt(25)\n"
    "v_mfma_f32_16x16x32_bf16 v[96:99], v[24:27], v[132:135], 0\n"
    "buffer_load_dwordx4 v[124:127], v6, s[40:43], s73 offen sc1\n"
    "buffer_load_dwordx4 v[128:131], v6, s[40:43], s74 offen sc1\n"
    "buffer_load_dwordx4 v[132:135], v7, s[40:43], s73 offen sc1\n"
    "v_pk_add_f32 v[112:113], v[80:81], v[82:83]\n"
    "v_pk_add_f32 v[114:115], v[84:85], v[86:87]\n"
    "v_pk_add_f32 v[112:113], v[112:113], v[88:89]\n"
    "v_pk_add_f32 v[114:115], v[114:115], v[90:91]\n"
    "v_pk_add_f32 v[112:113], v[112:113], v[92:93]\n"
    "v_pk_add_f32 v[114:115], v[114:115], v[94:95]\n"
    "v_pk_add_f32 v[112:113], v[112:113], v[96:97]\n"
    "v_pk_add_f32 v[114:115], v[114:115], v[98:99]\n"
    "v_pk_add_f32 v[112:113], v[112:113], v[114:115]\n"
    "v_add_f32 v112, v112, v113\n"
    "s_nop 1\n"
    "v_add_f32_dpp v14, v112, v112 row_ror:8 row_mask:0xf bank_mask:0xf\n"
    "v_add_f32_dpp v15, v112, v112 row_ror:8 row_mask:0xf bank_mask:0xf\n"
    "s_nop 1\n"
    "v_permlane16_swap_b32 v14, v15\n"
    "v_add_f32 v112, v14, v15\n"
    "v_add_f32 v14, v14, v15\n"
    "s_nop 1\n"
    "v_permlane32_swap_b32 v112, v14\n"
    "v_add_f32 v112, v112, v14\n"
    "v_mul_f32 v112, 0x3be38e39, v112\n"
    "v_mul_f32 v114, v112, v12\n"
    "v_pk_add_f32 v[80:81], v[80:81], v[112:113] op_sel_hi:[1,0] neg_lo:[0,1] neg_hi:[0,1]\n"
    "v_pk_add_f32 v[82:83], v[82:83], v[112:113] op_sel_hi:[1,0] neg_lo:[0,1] neg_hi:[0,1]\n"
    "v_pk_add_f32 v[84:85], v[84:85], v[112:113] op_sel_hi:[1,0] neg_lo:[0,1] neg_hi:[0,1]\n"
    "v_pk_add_f32 v[86:87], v[86:87], v[112:113] op_sel_hi:[1,0] neg_lo:[0,1] neg_hi:[0,1]\n"
    "v_pk_add_f32 v[88:89], v[88:89], v[112:113] op_sel_hi:[1,0] neg_lo:[0,1] neg_hi:[0,1]\n"
    "v_pk_add_f32 v[90:91], v[90:91], v[112:113] op_sel_hi:[1,0] neg_lo:[0,1] neg_hi:[0,1]\n"
    "v_pk_add_f32 v[92:93], v[92:93], v[112:113] op_sel_hi:[1,0] neg_lo:[0,1] neg_hi:[0,1]\n"
    "v_pk_add_f32 v[94:95], v[94:95], v[112:113] op_sel_hi:[1,0] neg_lo:[0,1] neg_hi:[0,1]\n"
    "v_pk_add_f32 v[96:97], v[96:97], v[114:115] op_sel_hi:[1,0] neg_lo:[0,1] neg_hi:[0,1]\n"
    "v_pk_add_f32 v[98:99], v[98:99], v[114:115] op_sel_hi:[1,0] neg_lo:[0,1] neg_hi:[0,1]\n"
    "v_pk_mul_f32 v[116:117], v[80:81], v[80:81]\n"
    "v_pk_mul_f32 v[118:119], v[82:83], v[82:83]\n"
    "v_pk_fma_f32 v[116:117], v[84:85], v[84:85], v[116:117]\n"
    "v_pk_fma_f32 v[118:119], v[86:87], v[86:87], v[118:119]\n"
    "v_pk_fma_f32 v[116:117], v[88:89], v[88:89], v[116:117]\n"
    "v_pk_fma_f32 v[118:119], v[90:91], v[90:91], v[118:119]\n"
    "v_pk_fma_f32 v[116:117], v[92:93], v[92:93], v[116:117]\n"
    "v_pk_fma_f32 v[118:119], v[94:95], v[94:95], v[118:119]\n"
    "v_pk_fma_f32 v[116:117], v[96:97], v[96:97], v[116:117]\n"
    "v_pk_fma_f32 v[118:119], v[98:99], v[98:99], v[118:119]\n"
    "v_pk_add_f32 v[116:117], v[116:117], v[118:119]\n"
    "v_add_f32 v113, v116, v117\n"
    "s_nop 1\n"
    "v_add_f32_dpp v14, v113, v113 row_ror:8 row_mask:0xf bank_mask:0xf\n"
    "v_add_f32_dpp v15, v113, v113 row_ror:8 row_mask:0xf bank_mask:0xf\n"
    "s_nop 1\n"
    "v_permlane16_swap_b32 v14, v15\n"
    "v_add_f32 v113, v14, v15\n"
    "v_add_f32 v14, v14, v15\n"
    "s_nop 1\n"
    "v_permlane32_swap_b32 v113, v14\n"
    "v_add_f32 v113, v113, v14\n"
    "ds_write_b64 v10, v[112:113]\n"
    "s_waitcnt vmcnt(17)\n"
    "s_add_u32 s75, s66, 1\n"
    "s_min_i32 s75, s75, s68\n"
    "s_sub_u32 s76, s68, 2\n"
    "s_max_i32 s75, s75, s76\n"
    "s_sub_u32 s75, s75, 1\n"
    "v_mov_b32 v14, s75\n"
    "s_mov_b64 exec, s[64:65]\n"
    "buffer_store_dword v14, v13, s[36:39], 0 offen sc1\n"
    "s_mov_b64 exec, -1\n"
    "s_waitcnt lgkmcnt(0)\n"
    "s_barrier\n"
    "ds_read_b128 v[32:35], v11\n"
    "ds_read_b128 v[36:39], v11 offset:16\n"
    "v_xor_b32 v10, 256, v10\n"
    "v_xor_b32 v11, 256, v11\n"
    "s_waitcnt lgkmcnt(0)\n"
    "v_pk_add_f32 v[114:115], v[32:33], v[34:35]\n"
    "v_pk_add_f32 v[116:117], v[36:37], v[38:39]\n"
    "v_pk_add_f32 v[114:115], v[114:115], v[116:117]\n"
    "v_fmac_f32 v32, 0xbe800000, v114\n"
    "v_fmac_f32 v34, 0xbe800000, v114\n"
    "v_fmac_f32 v36, 0xbe800000, v114\n"
    "v_fmac_f32 v38, 0xbe800000, v114\n"
    "v_fmac_f32 v112, 0xbe800000, v114\n"
    "v_mul_f32 v116, v32, v32\n"
    "v_fmac_f32 v116, v34, v34\n"
    "v_fmac_f32 v116, v36, v36\n"
    "v_fmac_f32 v116, v38, v38\n"
    "v_fmac_f32 v115, 0x43100000, v116\n"
    "v_mov_b32 v116, 0x3727c5ac\n"
    "v_fmac_f32 v116, 0x3ae38e39, v115\n"
    "v_rsq_f32 v116, v116\n"
    "s_nop 0\n"
    "v_mul_f32 v114, v112, v116\n"
    "v_pk_fma_f32 v[80:81], v[80:81], v[116:117], v[114:115] op_sel_hi:[1,0,0]\n"
    "v_pk_fma_f32 v[82:83], v[82:83], v[116:117], v[114:115] op_sel_hi:[1,0,0]\n"
    "v_pk_fma_f32 v[84:85], v[84:85], v[116:117], v[114:115] op_sel_hi:[1,0,0]\n"
    "v_pk_fma_f32 v[86:87], v[86:87], v[116:117], v[114:115] op_sel_hi:[1,0,0]\n"
    "v_pk_fma_f32 v[88:89], v[88:89], v[116:117], v[114:115] op_sel_hi:[1,0,0]\n"
    "v_pk_fma_f32 v[90:91], v[90:91], v[116:117], v[114:115] op_sel_hi:[1,0,0]\n"
    "v_pk_fma_f32 v[92:93], v[92:93], v[116:117], v[114:115] op_sel_hi:[1,0,0]\n"
    "v_pk_fma_f32 v[94:95], v[94:95], v[116:117], v[114:115] op_sel_hi:[1,0,0]\n"
    "v_pk_fma_f32 v[96:97], v[96:97], v[116:117], v[114:115] op_sel_hi:[1,0,0]\n"
    "v_pk_fma_f32 v[98:99], v[98:99], v[116:117], v[114:115] op_sel_hi:[1,0,0]\n"
    "v_cvt_pk_bf16_f32 v100, v80, v81\n"
    "v_cvt_pk_bf16_f32 v101, v82, v83\n"
    "v_cvt_pk_bf16_f32 v102, v84, v85\n"
    "v_cvt_pk_bf16_f32 v103, v86, v87\n"
    "v_cvt_pk_bf16_f32 v104, v88, v89\n"
    "v_cvt_pk_bf16_f32 v105, v90, v91\n"
    "v_cvt_pk_bf16_f32 v106, v92, v93\n"
    "v_cvt_pk_bf16_f32 v107, v94, v95\n"
    "v_cvt_pk_bf16_f32 v108, v96, v97\n"
    "v_cvt_pk_bf16_f32 v109, v98, v99\n"
    "v_permlane16_swap_b32 v100, v102\n"
    "v_permlane16_swap_b32 v101, v103\n"
    "buffer_store_dwordx4 v[100:103], v4, s[44:47], s77 offen sc1\n"
    "v_permlane16_swap_b32 v104, v106\n"
    "v_permlane16_swap_b32 v105, v107\n"
    "buffer_store_dwordx4 v[104:107], v4, s[44:47], s78 offen sc1\n"
    "v_permlane16_swap_b32 v108, v110\n"
    "v_permlane16_swap_b32 v109, v111\n"
    "buffer_store_dwordx4 v[108:111], v5, s[44:47], s77 offen sc1\n"
    "s_cmp_eq_u32 s68, s67\n"
    "s_cbranch_scc1 .LrLN0_fin_%=\n"
    "s_branch .LrLN0_top_%=\n"
    ".LrLN0_LOOPE_%=:\n"
    ".LrLN0_fin_%=:\n"
    "s_waitcnt vmcnt(0)\n"
    "s_mov_b32 s68, 63\n"
    "v_mov_b32 v14, s68\n"
    "s_mov_b64 exec, s[64:65]\n"
    "buffer_store_dword v14, v13, s[36:39], 0 offen sc1\n"
    "s_mov_b64 exec, -1\n"
    "s_waitcnt vmcnt(0)\n"
    "s_barrier\n"
    "s_branch .LrLN0_FIN_%=\n"
    ".LrLN0_first_%=:\n"
    "s_waitcnt vmcnt(0)\n"
    "v_mov_b32 v14, s68\n"
    "s_mov_b64 exec, s[64:65]\n"
    "buffer_store_dword v14, v13, s[36:39], 0 offen sc1\n"
    "s_mov_b64 exec, -1\n"
    "s_branch .LrLN0_first1_%=\n"
    ".LrLN0_FIN_%=:\n"
  );
}
DEVI void role_mid(v16u sa, v16u sb, v16u va, v16u vb) {
  RUN_RED(
    "s_nop 4\n"
    "s_mov_b32 s69, 0\n"
    "v_mov_b32 v122, -1\n"
    "s_mov_b32 s70, 262144\n"
    ".LrMID_par_%=:\n"
    "buffer_load_dword v14, off, s[36:39], 0 sc1\n"
    "s_waitcnt vmcnt(0)\n"
    "v_readfirstlane_b32 s75, v14\n"
    "s_nop 0\n"
    "s_cmp_ge_i32 s75, 0\n"
    "s_cbranch_scc1 .LrMID_par1_%=\n"
    "s_sleep 4\n"
    "s_sub_u32 s70, s70, 1\n"
    "s_cbranch_scc0 .LrMID_par_%=\n"
    "s_mov_b32 s70, 0\n"
    ".LrMID_par1_%=:\n"
    "s_and_b32 s75, s75, 1\n"
    "s_lshl_b32 s75, s75, 13\n"
    "s_add_u32 s36, s36, s75\n"
    "s_addc_u32 s37, s37, 0\n"
    "s_and_b32 s76, s75, s61\n"
    "s_add_u32 s40, s40, s76\n"
    "s_addc_u32 s41, s41, 0\n"
    "s_and_b32 s76, s75, s63\n"
    "s_add_u32 s44, s44, s76\n"
    "s_addc_u32 s45, s45, 0\n"
    "s_branch .LrMID_block_%=\n"
    ".LrMID_LOOPB_%=:\n"
    ".LrMID_top_%=:\n"
    "s_mov_b32 s68, s69\n"
    "s_add_u32 s69, s68, 1\n"
    "s_and_b32 s75, s68, s58\n"
    "s_mul_i32 s77, s75, 4194304\n"
    "s_add_u32 s77, s77, s59\n"
    "s_add_u32 s78, s77, s60\n"
    "s_and_b32 s71, s69, 7\n"
    "s_mul_i32 s71, s71, 4194304\n"
    "s_add_u32 s71, s71, s52\n"
    "s_add_u32 s72, s71, 4096\n"
    "s_and_b32 s73, s69, s55\n"
    "s_mul_i32 s73, s73, s54\n"
    "s_add_u32 s73, s73, s53\n"
    "s_add_u32 s74, s73, s56\n"
    "s_mul_i32 s75, s75, 4194304\n"
    "s_add_u32 s75, s75, s57\n"
    "s_add_u32 s76, s75, 1024\n"
    "s_waitcnt vmcnt(12)\n"
    "v_mfma_f32_16x16x32_bf16 v[80:83], v[16:19], v[32:35], 0\n"
    "s_waitcnt vmcnt(11)\n"
    "v_mfma_f32_16x16x32_bf16 v[80:83], v[20:23], v[36:39], v[80:83]\n"
    "s_waitcnt vmcnt(10)\n"
    "v_mfma_f32_16x16x32_bf16 v[84:87], v[16:19], v[40:43], 0\n"
    "s_waitcnt vmcnt(9)\n"
    "v_mfma_f32_16x16x32_bf16 v[84:87], v[20:23], v[44:47], v[84:87]\n"
    "s_waitcnt vmcnt(8)\n"
    "v_mfma_f32_16x16x32_bf16 v[88:91], v[16:19], v[48:51], 0\n"
    "s_waitcnt vmcnt(7)\n"
    "v_mfma_f32_16x16x32_bf16 v[88:91], v[20:23], v[52:55], v[88:91]\n"
    "s_waitcnt vmcnt(6)\n"
    "v_mfma_f32_16x16x32_bf16 v[92:95], v[16:19], v[56:59], 0\n"
    "s_waitcnt vmcnt(5)\n"
    "v_mfma_f32_16x16x32_bf16 v[92:95], v[20:23], v[60:63], v[92:95]\n"
    "s_waitcnt vmcnt(4)\n"
    "v_mfma_f32_16x16x32_bf16 v[96:99], v[16:19], v[64:67], 0\n"
    "s_waitcnt vmcnt(3)\n"
    "v_mfma_f32_16x16x32_bf16 v[80:83], v[24:27], v[68:71], v[80:83]\n"
    "v_mfma_f32_16x16x32_bf16 v[84:87], v[28:31], v[68:71], v[84:87]\n"
    "s_waitcnt vmcnt(2)\n"
    "v_mfma_f32_16x16x32_bf16 v[88:91], v[24:27], v[72:75], v[88:91]\n"
    "v_mfma_f32_16x16x32_bf16 v[92:95], v[28:31], v[72:75], v[92:95]\n"
    "s_waitcnt vmcnt(1)\n"
    "v_mfma_f32_16x16x32_bf16 v[96:99], v[24:27], v[76:79], v[96:99]\n"
    "v_pk_add_f32 v[112:113], v[80:81], v[82:83]\n"
    "v_pk_add_f32 v[114:115], v[84:85], v[86:87]\n"
    "v_pk_add_f32 v[112:113], v[112:113], v[88:89]\n"
    "v_pk_add_f32 v[114:115], v[114:115], v[90:91]\n"
    "v_pk_add_f32 v[112:113], v[112:113], v[92:93]\n"
    "v_pk_add_f32 v[114:115], v[114:115], v[94:95]\n"
    "s_nop 1\n"
    "v_pk_add_f32 v[112:113], v[112:113], v[96:97]\n"
    "v_pk_add_f32 v[114:115], v[114:115], v[98:99]\n"
    "v_pk_add_f32 v[112:113], v[112:113], v[114:115]\n"
    "v_add_f32 v112, v112, v113\n"
    "v_cvt_pk_bf16_f32 v100, v80, v81\n"
    "v_cvt_pk_bf16_f32 v101, v82, v83\n"
    "v_cvt_pk_bf16_f32 v102, v84, v85\n"
    "v_cvt_pk_bf16_f32 v103, v86, v87\n"
    "v_cvt_pk_bf16_f32 v104, v88, v89\n"
    "v_cvt_pk_bf16_f32 v105, v90, v91\n"
    "v_cvt_pk_bf16_f32 v106, v92, v93\n"
    "v_cvt_pk_bf16_f32 v107, v94, v95\n"
    "v_cvt_pk_bf16_f32 v108, v96, v97\n"
    "v_cvt_pk_bf16_f32 v109, v98, v99\n"
    "v_add_f32_dpp v14, v112, v112 row_ror:8 row_mask:0xf bank_mask:0xf\n"
    "v_add_f32_dpp v15, v112, v112 row_ror:8 row_mask:0xf bank_mask:0xf\n"
    "s_nop 1\n"
    "v_permlane16_swap_b32 v14, v15\n"
    "v_add_f32 v112, v14, v15\n"
    "v_add_f32 v14, v14, v15\n"
    "s_nop 1\n"
    "v_permlane32_swap_b32 v112, v14\n"
    "v_add_f32 v112, v112, v14\n"
    "v_mul_f32 v112, 0x3be38e39, v112\n"
    "v_mul_f32 v114, v112, v12\n"
    "v_pk_add_f32 v[80:81], v[80:81], v[112:113] op_sel_hi:[1,0] neg_lo:[0,1] neg_hi:[0,1]\n"
    "v_pk_add_f32 v[82:83], v[82:83], v[112:113] op_sel_hi:[1,0] neg_lo:[0,1] neg_hi:[0,1]\n"
    "v_pk_add_f32 v[84:85], v[84:85], v[112:113] op_sel_hi:[1,0] neg_lo:[0,1] neg_hi:[0,1]\n"
    "v_pk_add_f32 v[86:87], v[86:87], v[112:113] op_sel_hi:[1,0] neg_lo:[0,1] neg_hi:[0,1]\n"
    "v_pk_add_f32 v[88:89], v[88:89], v[112:113] op_sel_hi:[1,0] neg_lo:[0,1] neg_hi:[0,1]\n"
    "v_pk_add_f32 v[90:91], v[90:91], v[112:113] op_sel_hi:[1,0] neg_lo:[0,1] neg_hi:[0,1]\n"
    "v_pk_add_f32 v[92:93], v[92:93], v[112:113] op_sel_hi:[1,0] neg_lo:[0,1] neg_hi:[0,1]\n"
    "v_pk_add_f32 v[94:95], v[94:95], v[112:113] op_sel_hi:[1,0] neg_lo:[0,1] neg_hi:[0,1]\n"
    "v_pk_add_f32 v[96:97], v[96:97], v[114:115] op_sel_hi:[1,0] neg_lo:[0,1] neg_hi:[0,1]\n"
    "v_pk_add_f32 v[98:99], v[98:99], v[114:115] op_sel_hi:[1,0] neg_lo:[0,1] neg_hi:[0,1]\n"
    "v_pk_mul_f32 v[116:117], v[80:81], v[80:81]\n"
    "v_pk_mul_f32 v[118:119], v[82:83], v[82:83]\n"
    "v_pk_fma_f32 v[116:117], v[84:85], v[84:85], v[116:117]\n"
    "v_pk_fma_f32 v[118:119], v[86:87], v[86:87], v[118:119]\n"
    "v_pk_fma_f32 v[116:117], v[88:89], v[88:89], v[116:117]\n"
    "v_pk_fma_f32 v[118:119], v[90:91], v[90:91], v[118:119]\n"
    "v_pk_fma_f32 v[116:117], v[92:93], v[92:93], v[116:117]\n"
    "v_pk_fma_f32 v[118:119], v[94:95], v[94:95], v[118:119]\n"
    "v_pk_fma_f32 v[116:117], v[96:97], v[96:97], v[116:117]\n"
    "v_pk_fma_f32 v[118:119], v[98:99], v[98:99], v[118:119]\n"
    "v_pk_add_f32 v[116:117], v[116:117], v[118:119]\n"
    "v_add_f32 v113, v116, v117\n"
    "s_nop 1\n"
    "v_add_f32_dpp v14, v113, v113 row_ror:8 row_mask:0xf bank_mask:0xf\n"
    "v_add_f32_dpp v15, v113, v113 row_ror:8 row_mask:0xf bank_mask:0xf\n"
    "s_nop 1\n"
    "v_permlane16_swap_b32 v14, v15\n"
    "v_add_f32 v113, v14, v15\n"
    "v_add_f32 v14, v14, v15\n"
    "s_nop 1\n"
    "v_permlane32_swap_b32 v113, v14\n"
    "v_add_f32 v113, v113, v14\n"
    "ds_write_b64 v10, v[112:113]\n"
    "v_add_u32 v120, v122, v1\n"
    "v_cmp_gt_i32 vcc, s69, v120\n"
    "s_nop 0\n"
    "s_or_b64 exec, vcc, 1\n"
    "buffer_atomic_smax v122, v0, s[36:39], 0 offen sc0 sc1\n"
    "s_mov_b64 exec, -1\n"
    "s_nop 0\n"
    "v_permlane16_swap_b32 v100, v102\n"
    "v_permlane16_swap_b32 v101, v103\n"
    "v_permlane16_swap_b32 v104, v106\n"
    "v_permlane16_swap_b32 v105, v107\n"
    "v_permlane16_swap_b32 v108, v110\n"
    "v_permlane16_swap_b32 v109, v111\n"
    "s_waitcnt lgkmcnt(0)\n"
    "s_barrier\n"
    "ds_read_b128 v[32:35], v11\n"
    "ds_read_b128 v[36:39], v11 offset:16\n"
    "buffer_store_dwordx4 v[100:103], v8, s[36:39], s75 offen sc1\n"
    "buffer_store_dwordx4 v[104:107], v8, s[36:39], s76 offen sc1\n"
    "buffer_store_dwordx4 v[108:111], v9, s[36:39], s75 offen sc1\n"
    "v_xor_b32 v10, 256, v10\n"
    "v_xor_b32 v11, 256, v11\n"
    "s_waitcnt lgkmcnt(0)\n"
    "v_pk_add_f32 v[114:115], v[32:33], v[34:35]\n"
    "v_pk_add_f32 v[116:117], v[36:37], v[38:39]\n"
    "v_pk_add_f32 v[114:115], v[114:115], v[116:117]\n"
    "v_fmac_f32 v32, 0xbe800000, v114\n"
    "v_fmac_f32 v34, 0xbe800000, v114\n"
    "v_fmac_f32 v36, 0xbe800000, v114\n"
    "v_fmac_f32 v38, 0xbe800000, v114\n"
    "v_fmac_f32 v112, 0xbe800000, v114\n"
    "v_mul_f32 v116, v32, v32\n"
    "v_fmac_f32 v116, v34, v34\n"
    "v_fmac_f32 v116, v36, v36\n"
    "v_fmac_f32 v116, v38, v38\n"
    "v_fmac_f32 v115, 0x43100000, v116\n"
    "v_mov_b32 v116, 0x3727c5ac\n"
    "v_fmac_f32 v116, 0x3ae38e39, v115\n"
    "v_rsq_f32 v116, v116\n"
    "s_nop 0\n"
    "v_mul_f32 v114, v112, v116\n"
    "v_pk_fma_f32 v[80:81], v[80:81], v[116:117], v[114:115] op_sel_hi:[1,0,0]\n"
    "v_pk_fma_f32 v[82:83], v[82:83], v[116:117], v[114:115] op_sel_hi:[1,0,0]\n"
    "v_pk_fma_f32 v[84:85], v[84:85], v[116:117], v[114:115] op_sel_hi:[1,0,0]\n"
    "v_pk_fma_f32 v[86:87], v[86:87], v[116:117], v[114:115] op_sel_hi:[1,0,0]\n"
    "v_pk_fma_f32 v[88:89], v[88:89], v[116:117], v[114:115] op_sel_hi:[1,0,0]\n"
    "v_pk_fma_f32 v[90:91], v[90:91], v[116:117], v[114:115] op_sel_hi:[1,0,0]\n"
    "v_pk_fma_f32 v[92:93], v[92:93], v[116:117], v[114:115] op_sel_hi:[1,0,0]\n"
    "v_pk_fma_f32 v[94:95], v[94:95], v[116:117], v[114:115] op_sel_hi:[1,0,0]\n"
    "v_pk_fma_f32 v[96:97], v[96:97], v[116:117], v[114:115] op_sel_hi:[1,0,0]\n"
    "v_pk_fma_f32 v[98:99], v[98:99], v[116:117], v[114:115] op_sel_hi:[1,0,0]\n"
    "v_cvt_pk_bf16_f32 v100, v80, v81\n"
    "v_cvt_pk_bf16_f32 v101, v82, v83\n"
    "v_cvt_pk_bf16_f32 v102, v84, v85\n"
    "v_cvt_pk_bf16_f32 v103, v86, v87\n"
    "v_cvt_pk_bf16_f32 v104, v88, v89\n"
    "v_cvt_pk_bf16_f32 v105, v90, v91\n"
    "v_cvt_pk_bf16_f32 v106, v92, v93\n"
    "v_cvt_pk_bf16_f32 v107, v94, v95\n"
    "v_cvt_pk_bf16_f32 v108, v96, v97\n"
    "v_cvt_pk_bf16_f32 v109, v98, v99\n"
    "v_permlane16_swap_b32 v100, v102\n"
    "v_permlane16_swap_b32 v101, v103\n"
    "buffer_store_dwordx4 v[100:103], v4, s[44:47], s77 offen sc1\n"
    "v_permlane16_swap_b32 v104, v106\n"
    "v_permlane16_swap_b32 v105, v107\n"
    "buffer_store_dwordx4 v[104:107], v4, s[44:47], s78 offen sc1\n"
    "v_permlane16_swap_b32 v108, v110\n"
    "v_permlane16_swap_b32 v109, v111\n"
    "buffer_store_dwordx4 v[108:111], v5, s[44:47], s77 offen sc1\n"
    "s_cmp_eq_u32 s69, 64\n"
    "s_cbranch_scc1 .LrMID_fin_%=\n"
    "s_waitcnt vmcnt(6)\n"
    "v_add_u32 v120, v122, v1\n"
    "v_cmp_gt_i32 vcc, s69, v120\n"
    "s_cbranch_vccnz .LrMID_idle_%=\n"
    "buffer_load_dwordx4 v[32:35], v2, s[36:39], s71 offen sc1\n"
    "buffer_load_dwordx4 v[36:39], v2, s[36:39], s71 offen offset:512 sc1\n"
    "buffer_load_dwordx4 v[40:43], v2, s[36:39], s71 offen offset:2048 sc1\n"
    "buffer_load_dwordx4 v[44:47], v2, s[36:39], s71 offen offset:2560 sc1\n"
    "buffer_load_dwordx4 v[48:51], v2, s[36:39], s72 offen sc1\n"
    "buffer_load_dwordx4 v[52:55], v2, s[36:39], s72 offen offset:512 sc1\n"
    "buffer_load_dwordx4 v[56:59], v2, s[36:39], s72 offen offset:2048 sc1\n"
    "buffer_load_dwordx4 v[60:63], v2, s[36:39], s72 offen offset:2560 sc1\n"
    "buffer_load_dwordx4 v[64:67], v3, s[36:39], s71 offen sc1\n"
    "buffer_load_dwordx4 v[68:71], v6, s[40:43], s73 offen sc1\n"
    "buffer_load_dwordx4 v[72:75], v6, s[40:43], s74 offen sc1\n"
    "buffer_load_dwordx4 v[76:79], v7, s[40:43], s73 offen sc1\n"
    "s_waitcnt vmcnt(12)\n"
    "v_mov_b32 v14, s68\n"
    "s_mov_b64 exec, s[64:65]\n"
    "buffer_store_dword v14, v13, s[36:39], 0 offen sc1\n"
    "s_mov_b64 exec, -1\n"
    "s_branch .LrMID_top_%=\n"
    ".LrMID_LOOPE_%=:\n"
    ".LrMID_fin_%=:\n"
    "s_waitcnt vmcnt(0)\n"
    "s_mov_b32 s68, 63\n"
    "v_mov_b32 v14, s68\n"
    "s_mov_b64 exec, s[64:65]\n"
    "buffer_store_dword v14, v13, s[36:39], 0 offen sc1\n"
    "s_mov_b64 exec, -1\n"
    "s_waitcnt vmcnt(0)\n"
    "s_barrier\n"
    "s_branch .LrMID_FIN_%=\n"
    ".LrMID_idle_%=:\n"
    "v_add_u32 v120, v122, v1\n"
    "v_cmp_gt_i32 vcc, s69, v120\n"
    "s_nop 0\n"
    "s_or_b64 exec, vcc, 1\n"
    "buffer_atomic_smax v122, v0, s[36:39], 0 offen sc0 sc1\n"
    "s_mov_b64 exec, -1\n"
    "s_waitcnt vmcnt(1)\n"
    "v_mov_b32 v14, s68\n"
    "s_mov_b64 exec, s[64:65]\n"
    "buffer_store_dword v14, v13, s[36:39], 0 offen sc1\n"
    "s_mov_b64 exec, -1\n"
    "s_waitcnt vmcnt(1)\n"
    "s_branch .LrMID_block1_%=\n"
    ".LrMID_block_%=:\n"
    "v_add_u32 v120, v122, v1\n"
    "v_cmp_gt_i32 vcc, s69, v120\n"
    "s_nop 0\n"
    "s_or_b64 exec, vcc, 1\n"
    "buffer_atomic_smax v122, v0, s[36:39], 0 offen sc0 sc1\n"
    "s_mov_b64 exec, -1\n"
    "s_waitcnt vmcnt(0)\n"
    ".LrMID_block1_%=:\n"
    "v_add_u32 v120, v122, v1\n"
    "v_cmp_gt_i32 vcc, s69, v120\n"
    "s_cbranch_vccz .LrMID_go_%=\n"
    "s_sleep 2\n"
    "s_sub_u32 s70, s70, 1\n"
    "s_cbranch_scc0 .LrMID_block_%=\n"
    "s_mov_b32 s70, 0\n"
    ".LrMID_go_%=:\n"
    "s_and_b32 s71, s69, 7\n"
    "s_mul_i32 s71, s71, 4194304\n"
    "s_add_u32 s71, s71, s52\n"
    "s_add_u32 s72, s71, 4096\n"
    "s_and_b32 s73, s69, s55\n"
    "s_mul_i32 s73, s73, s54\n"
    "s_add_u32 s73, s73, s53\n"
    "s_add_u32 s74, s73, s56\n"
    "buffer_load_dwordx4 v[32:35], v2, s[36:39], s71 offen sc1\n"
    "buffer_load_dwordx4 v[36:39], v2, s[36:39], s71 offen offset:512 sc1\n"
    "buffer_load_dwordx4 v[40:43], v2, s[36:39], s71 offen offset:2048 sc1\n"
    "buffer_load_dwordx4 v[44:47], v2, s[36:39], s71 offen offset:2560 sc1\n"
    "buffer_load_dwordx4 v[48:51], v2, s[36:39], s72 offen sc1\n"
    "buffer_load_dwordx4 v[52:55], v2, s[36:39], s72 offen offset:512 sc1\n"
    "buffer_load_dwordx4 v[56:59], v2, s[36:39], s72 offen offset:2048 sc1\n"
    "buffer_load_dwordx4 v[60:63], v2, s[36:39], s72 offen offset:2560 sc1\n"
    "buffer_load_dwordx4 v[64:67], v3, s[36:39], s71 offen sc1\n"
    "buffer_load_dwordx4 v[68:71], v6, s[40:43], s73 offen sc1\n"
    "buffer_load_dwordx4 v[72:75], v6, s[40:43], s74 offen sc1\n"
    "buffer_load_dwordx4 v[76:79], v7, s[40:43], s73 offen sc1\n"
    "buffer_load_dword v121, v7, s[40:43], s73 offen\n"
    "s_branch .LrMID_top_%=\n"
    ".LrMID_FIN_%=:\n"
  );
}
DEVI void role_fin(const Params& p, int q, u32 dbg) {
  v16u sa, sb, va, vb;
  red_args<2>(p, 1, q, dbg, sa, sb, va, vb);
  RUN_RED(
    "s_nop 4\n"
    "s_mov_b32 s69, 0\n"
    "v_mov_b32 v122, -1\n"
    "s_mov_b32 s70, 262144\n"
    ".LrFIN_par_%=:\n"
    "buffer_load_dword v14, off, s[36:39], 0 sc1\n"
    "s_waitcnt vmcnt(0)\n"
    "v_readfirstlane_b32 s75, v14\n"
    "s_nop 0\n"
    "s_cmp_ge_i32 s75, 0\n"
    "s_cbranch_scc1 .LrFIN_par1_%=\n"
    "s_sleep 4\n"
    "s_sub_u32 s70, s70, 1\n"
    "s_cbranch_scc0 .LrFIN_par_%=\n"
    "s_mov_b32 s70, 0\n"
    ".LrFIN_par1_%=:\n"
    "s_and_b32 s75, s75, 1\n"
    "s_lshl_b32 s75, s75, 13\n"
    "s_add_u32 s36, s36, s75\n"
    "s_addc_u32 s37, s37, 0\n"
    "s_and_b32 s76, s75, s61\n"
    "s_add_u32 s40, s40, s76\n"
    "s_addc_u32 s41, s41, 0\n"
    "s_and_b32 s76, s75, s63\n"
    "s_add_u32 s44, s44, s76\n"
    "s_addc_u32 s45, s45, 0\n"
    "s_branch .LrFIN_block_%=\n"
    ".LrFIN_LOOPB_%=:\n"
    ".LrFIN_top_%=:\n"
    "s_mov_b32 s68, s69\n"
    "s_add_u32 s69, s68, 1\n"
    "s_and_b32 s75, s68, s58\n"
    "s_mul_i32 s77, s75, 36864\n"
    "s_add_u32 s77, s77, s59\n"
    "s_add_u32 s78, s77, s60\n"
    "s_and_b32 s71, s69, 7\n"
    "s_mul_i32 s71, s71, 4194304\n"
    "s_add_u32 s71, s71, s52\n"
    "s_add_u32 s72, s71, 4096\n"
    "s_and_b32 s73, s69, s55\n"
    "s_mul_i32 s73, s73, s54\n"
    "s_add_u32 s73, s73, s53\n"
    "s_add_u32 s74, s73, s56\n"
    "s_waitcnt vmcnt(11)\n"
    "v_mfma_f32_16x16x32_bf16 v[80:83], v[16:19], v[32:35], 0\n"
    "s_waitcnt vmcnt(10)\n"
    "v_mfma_f32_16x16x32_bf16 v[80:83], v[20:23], v[36:39], v[80:83]\n"
    "s_waitcnt vmcnt(9)\n"
    "v_mfma_f32_16x16x32_bf16 v[84:87], v[16:19], v[40:43], 0\n"
    "s_waitcnt vmcnt(8)\n"
    "v_mfma_f32_16x16x32_bf16 v[84:87], v[20:23], v[44:47], v[84:87]\n"
    "s_waitcnt vmcnt(7)\n"
    "v_mfma_f32_16x16x32_bf16 v[88:91], v[16:19], v[48:51], 0\n"
    "s_waitcnt vmcnt(6)\n"
    "v_mfma_f32_16x16x32_bf16 v[88:91], v[20:23], v[52:55], v[88:91]\n"
    "s_waitcnt vmcnt(5)\n"
    "v_mfma_f32_16x16x32_bf16 v[92:95], v[16:19], v[56:59], 0\n"
    "s_waitcnt vmcnt(4)\n"
    "v_mfma_f32_16x16x32_bf16 v[92:95], v[20:23], v[60:63], v[92:95]\n"
    "s_waitcnt vmcnt(3)\n"
    "v_mfma_f32_16x16x32_bf16 v[96:99], v[16:19], v[64:67], 0\n"
    "s_waitcnt vmcnt(2)\n"
    "v_mfma_f32_16x16x32_bf16 v[80:83], v[24:27], v[68:71], v[80:83]\n"
    "v_mfma_f32_16x16x32_bf16 v[84:87], v[28:31], v[68:71], v[84:87]\n"
    "s_waitcnt vmcnt(1)\n"
    "v_mfma_f32_16x16x32_bf16 v[88:91], v[24:27], v[72:75], v[88:91]\n"
    "v_mfma_f32_16x16x32_bf16 v[92:95], v[28:31], v[72:75], v[92:95]\n"
    "s_waitcnt vmcnt(0)\n"
    "v_mfma_f32_16x16x32_bf16 v[96:99], v[24:27], v[76:79], v[96:99]\n"
    "v_add_u32 v120, v122, v1\n"
    "v_cmp_gt_i32 vcc, s69, v120\n"
    "s_nop 0\n"
    "s_or_b64 exec, vcc, 1\n"
    "buffer_atomic_smax v122, v0, s[36:39], 0 offen sc0 sc1\n"
    "s_mov_b64 exec, -1\n"
    "v_mov_b32 v14, s68\n"
    "s_mov_b64 exec, s[64:65]\n"
    "buffer_store_dword v14, v13, s[36:39], 0 offen sc1\n"
    "s_mov_b64 exec, -1\n"
    "v_cvt_pk_bf16_f32 v100, v80, v81\n"
    "v_cvt_pk_bf16_f32 v101, v82, v83\n"
    "v_cvt_pk_bf16_f32 v102, v84, v85\n"
    "v_cvt_pk_bf16_f32 v103, v86, v87\n"
    "v_cvt_pk_bf16_f32 v104, v88, v89\n"
    "v_cvt_pk_bf16_f32 v105, v90, v91\n"
    "v_cvt_pk_bf16_f32 v106, v92, v93\n"
    "v_cvt_pk_bf16_f32 v107, v94, v95\n"
    "v_cvt_pk_bf16_f32 v108, v96, v97\n"
    "v_cvt_pk_bf16_f32 v109, v98, v99\n"
    "v_permlane16_swap_b32 v100, v102\n"
    "v_permlane16_swap_b32 v101, v103\n"
    "buffer_store_dwordx4 v[100:103], v4, s[44:47], s77 offen\n"
    "v_permlane16_swap_b32 v104, v106\n"
    "v_permlane16_swap_b32 v105, v107\n"
    "buffer_store_dwordx4 v[104:107], v4, s[44:47], s78 offen\n"
    "v_permlane16_swap_b32 v108, v110\n"
    "v_permlane16_swap_b32 v109, v111\n"
    "buffer_store_dwordx4 v[108:111], v5, s[44:47], s77 offen\n"
    "s_cmp_eq_u32 s69, 64\n"
    "s_cbranch_scc1 .LrFIN_fin_%=\n"
    "s_waitcnt vmcnt(4)\n"
    "v_add_u32 v120, v122, v1\n"
    "v_cmp_gt_i32 vcc, s69, v120\n"
    "s_cbranch_vccnz .LrFIN_idle_%=\n"
    "buffer_load_dwordx4 v[32:35], v2, s[36:39], s71 offen sc1\n"
    "buffer_load_dwordx4 v[36:39], v2, s[36:39], s71 offen offset:512 sc1\n"
    "buffer_load_dwordx4 v[40:43], v2, s[36:39], s71 offen offset:2048 sc1\n"
    "buffer_load_dwordx4 v[44:47], v2, s[36:39], s71 offen offset:2560 sc1\n"
    "buffer_load_dwordx4 v[48:51], v2, s[36:39], s72 offen sc1\n"
    "buffer_load_dwordx4 v[52:55], v2, s[36:39], s72 offen offset:512 sc1\n"
    "buffer_load_dwordx4 v[56:59], v2, s[36:39], s72 offen offset:2048 sc1\n"
    "buffer_load_dwordx4 v[60:63], v2, s[36:39], s72 offen offset:2560 sc1\n"
    "buffer_load_dwordx4 v[64:67], v3, s[36:39], s71 offen sc1\n"
    "buffer_load_dwordx4 v[68:71], v6, s[40:43], s73 offen sc1\n"
    "buffer_load_dwordx4 v[72:75], v6, s[40:43], s74 offen sc1\n"
    "buffer_load_dwordx4 v[76:79], v7, s[40:43], s73 offen sc1\n"
    "s_branch .LrFIN_top_%=\n"
    ".LrFIN_LOOPE_%=:\n"
    ".LrFIN_fin_%=:\n"
    "s_waitcnt vmcnt(0)\n"
    "s_branch .LrFIN_FIN_%=\n"
    ".LrFIN_idle_%=:\n"
    ".LrFIN_block_%=:\n"
    "v_add_u32 v120, v122, v1\n"
    "v_cmp_gt_i32 vcc, s69, v120\n"
    "s_nop 0\n"
    "s_or_b64 exec, vcc, 1\n"
    "buffer_atomic_smax v122, v0, s[36:39], 0 offen sc0 sc1\n"
    "s_mov_b64 exec, -1\n"
    "s_waitcnt vmcnt(0)\n"
    ".LrFIN_block1_%=:\n"
    "v_add_u32 v120, v122, v1\n"
    "v_cmp_gt_i32 vcc, s69, v120\n"
    "s_cbranch_vccz .LrFIN_go_%=\n"
    "s_sleep 2\n"
    "s_sub_u32 s70, s70, 1\n"
    "s_cbranch_scc0 .LrFIN_block_%=\n"
    "s_mov_b32 s70, 0\n"
    ".LrFIN_go_%=:\n"
    "s_and_b32 s71, s69, 7\n"
    "s_mul_i32 s71, s71, 4194304\n"
    "s_add_u32 s71, s71, s52\n"
    "s_add_u32 s72, s71, 4096\n"
    "s_and_b32 s73, s69, s55\n"
    "s_mul_i32 s73, s73, s54\n"
    "s_add_u32 s73, s73, s53\n"
    "s_add_u32 s74, s73, s56\n"
    "buffer_load_dwordx4 v[32:35], v2, s[36:39], s71 offen sc1\n"
    "buffer_load_dwordx4 v[36:39], v2, s[36:39], s71 offen offset:512 sc1\n"
    "buffer_load_dwordx4 v[40:43], v2, s[36:39], s71 offen offset:2048 sc1\n"
    "buffer_load_dwordx4 v[44:47], v2, s[36:39], s71 offen offset:2560 sc1\n"
    "buffer_load_dwordx4 v[48:51], v2, s[36:39], s72 offen sc1\n"
    "buffer_load_dwordx4 v[52:55], v2, s[36:39], s72 offen offset:512 sc1\n"
    "buffer_load_dwordx4 v[56:59], v2, s[36:39], s72 offen offset:2048 sc1\n"
    "buffer_load_dwordx4 v[60:63], v2, s[36:39], s72 offen offset:2560 sc1\n"
    "buffer_load_dwordx4 v[64:67], v3, s[36:39], s71 offen sc1\n"
    "buffer_load_dwordx4 v[68:71], v6, s[40:43], s73 offen sc1\n"
    "buffer_load_dwordx4 v[72:75], v6, s[40:43], s74 offen sc1\n"
    "buffer_load_dwordx4 v[76:79], v7, s[40:43], s73 offen sc1\n"
    "s_branch .LrFIN_top_%=\n"
    ".LrFIN_FIN_%=:\n"
  );
}

__global__ void __launch_bounds__(256, 1) mega(Params p) {
  extern __shared__ __attribute__((aligned(16))) unsigned char smem[];
  const rsrc_t rw = __builtin_amdgcn_make_buffer_rsrc(p.ws, 0, WS_END + 8192, 0x00020000);
  if (threadIdx.x == 0) {
    const int x0 = (int)(__builtin_amdgcn_s_getreg(20 | (3 << 11)) & 7u);
    int rid = 0;
    for (int k = 0; k < 8; ++k) {
      const int x = (x0 + k) & 7;
      const u32 t = __hip_atomic_fetch_add((u32*)(p.ws + TICK + x * 128), 1u, __ATOMIC_RELAXED, __HIP_MEMORY_SCOPE_AGENT) + 1u;
      if (t < 32u) {
        rid = x * 32 + (int)t;
        if (PLACE == 1) {
          const int u = (int)t >> 1, jj = 2 * (x & 1) + ((int)t & 1);
          const int sl = (x & 2) == 0 ? (u < 8 ? u : (u < 14 ? SL_UP + u - 8 : SL_REDA + u - 14)) : (u < 13 ? SL_P + u : SL_DOWN + u - 13);
          rid = ((x & 4) + jj) * 32 + sl;
        }
        if (PLACE == 2) {
          const int u = ((x & 3) - 1) * 32 + (int)t;
          rid = (x & 3) == 0 ? ((x & 4) + ((int)t >> 3)) * 32 + ((int)t & 7) : ((x & 4) + u / 24) * 32 + 8 + u % 24;
        }
        if (PLACE == 3) {
          const int u = (int)t >> 1, jj = 2 * (x & 1) + ((int)t & 1);
          const int sl = (x & 2) == 0 ? (u < 8 ? u : (u < 13 ? SL_ATT + u - 8 : (u < 15 ? SL_REDA + u - 13 : SL_P + 7))) : (u < 7 ? SL_P + u : SL_UP + u - 7);
          rid = ((x & 4) + jj) * 32 + sl;
        }
        break;
      }
    }
    *(volatile int*)smem = rid;
  }
  __syncthreads();
  const int rid = (int)rfl(*(volatile u32*)smem);
  __syncthreads();
  const int L = rid >> 7, j = (rid >> 5) & 3, sl = rid & 31;
  const u32 dbg = OFF_DBG + (u32)rid * 66 * 256;
  auto on = [&](int r) { return ((p.role_mask >> (2 + 8 * L + r)) & 1) != 0; };
  STAMP(65, 8);
  STAMP(65 + 32, 9);
  if (sl < SL_P) { delay_start(L ? DL_R1 : 0); if (on(1)) role_rec(p, L, j, sl, dbg, smem); }
  else if (sl < SL_ATT) { delay_start(L ? DL_R1 : 0); if (on(0)) role_p(p, L, j, sl - SL_P, dbg); }
  else if (sl < SL_WO) {
    const u32 shift = find_shift(rw, rid == SL_ATT && threadIdx.x < 64 && (p.role_mask & 1));
    if (p.role_mask & 2) hot_init(p, rw, L, 2 * j + sl - SL_ATT, shift);
    if (on(2)) role_att(p, L, j, sl - SL_ATT, dbg, shift);
  }
  else if (sl < SL_UP) { delay_start(L ? DL_W1 : DL_W0); if (on(3)) role_wo(p, L, j, sl - SL_WO, dbg); }
  else if (sl < SL_DOWN) { delay_start(L ? DL_U1 : DL_U0); if (on(5)) role_up(p, L, j, sl - SL_UP, dbg); }
  else if (sl < SL_REDA) { delay_start(L ? DL_D1 : DL_D0); if (on(6)) role_down(p, L, j, sl - SL_DOWN, dbg); }
  else {
    if (sl == SL_REDA && (p.role_mask & 2)) hot_init(p, rw, L, 8 + j, find_shift(rw, false));
    if ((p.role_mask & 1) && (L == 1 || sl == SL_REDF)) role_ln0(p, L == 0 ? 0 : (sl == SL_REDA ? 1 : 2), j, dbg);
    if (sl == SL_REDF && L == 1) {
      if (on(7)) role_fin(p, j, dbg);
    } else {
      v16u sa, sb, va, vb;
      if (sl == SL_REDA) red_args<1>(p, L, j, dbg, sa, sb, va, vb);
      else red_args<2>(p, 0, j, dbg, sa, sb, va, vb);
      if (on(sl == SL_REDA ? 4 : 7)) role_mid(sa, sb, va, vb);
    }
  }
}

static void fill_params(Params& p, void* const* d_in, void* d_out, void* d_ws) {
  p.x = (const u16*)d_in[0]; p.mem0 = (const u16*)d_in[1];
  p.Wq = (const u16*)d_in[2]; p.Wk = (const u16*)d_in[3]; p.Wv = (const u16*)d_in[4]; p.Wo = (const u16*)d_in[5];
  p.w1 = (const u16*)d_in[8]; p.w3 = (const u16*)d_in[9]; p.w2 = (const u16*)d_in[10];
  p.out = (u16*)d_out; p.ws = (unsigned char*)d_ws;
}

extern "C" void kernel_launch(void* const* d_in, const int* in_sizes, int n_in, void* d_out, int out_size, void* d_ws,
                              size_t ws_size, hipStream_t stream) {
  Params p;
  fill_params(p, d_in, d_out, d_ws);
  (void)hipFuncSetAttribute((const void*)mega, hipFuncAttributeMaxDynamicSharedMemorySize, LDS_BYTES);
  (void)hipMemsetAsync(d_ws, 0xFF, CTRL_BYTES, stream);
  p.role_mask = 0x3FFFF; p.unused = 0;
  mega<<<256, 256, LDS_BYTES, stream>>>(p);
}
```

```cpp
#include <hip/hip_runtime.h>
#include <stdint.h>
#include <type_traits>
#include <utility>

typedef unsigned short u16;
typedef unsigned int u32;
typedef short bf16x8 __attribute__((ext_vector_type(8)));
typedef short bf16x4 __attribute__((ext_vector_type(4)));
typedef float f32x4 __attribute__((ext_vector_type(4)));
typedef float f32x2 __attribute__((ext_vector_type(2)));
typedef __bf16 bf2_t __attribute__((ext_vector_type(2)));
typedef unsigned v16u __attribute__((ext_vector_type(16)));
typedef unsigned v8u __attribute__((ext_vector_type(8)));
typedef unsigned v4u __attribute__((ext_vector_type(4)));
typedef unsigned v2u __attribute__((ext_vector_type(2)));
typedef __amdgpu_buffer_rsrc_t rsrc_t;
#define DEVI __device__ __forceinline__

static constexpr int NSEG = 64;
static constexpr int PULL = 1, RQ = 8, RO = 8, RP = 8, RH = 8, RX = 8, RX2 = 16, LN_B = 28, LN_C = 52, PS_ATT = 2, PS_RED = 2, DL_R1 = 6, DL_W0 = 7, DL_W1 = 12, DL_U0 = 8, DL_U1 = 14, DL_D0 = 11, DL_D1 = 16;

static constexpr u32 PARW = 0, EPOCH = 128, READY = 2048, CLAIM = 4096;
static constexpr u32 CHUNK = 16384;
static constexpr u32 MBOX = CHUNK;
static constexpr u32 INITF = 5 * CHUNK;
static constexpr u32 CTRL_BYTES = 6 * CHUNK + 8192;
static constexpr u32 HOT = 6 * CHUNK, HOT2 = 2u << 20;
static constexpr u32 SLOT = 4u << 20;
static constexpr u32 PGA = SLOT, PGB = SLOT + (2u << 20);
static constexpr int cmax(int a, int b) { return a > b ? a : b; }
static constexpr int NSLOT = cmax(cmax(cmax(RQ, RO), cmax(RP, RH)), cmax(RX, RX2));
DEVI u32 pga(int L, int blk) { return PGA + (u32)blk * CHUNK + (u32)L * 8192; }
DEVI u32 pgb(int L, int blk) { return PGB + (u32)blk * CHUNK + (u32)(1 - L) * 8192; }
static constexpr int B_QKV = 0, B_KVM = 96;
static constexpr int B_O = 0, B_H = 32, B_PA = 68, B_PF = 88, B_XN2 = 108, B_X1 = 113, B_XN = 118, B_X = 123;
static constexpr u32 OFF_XN0 = SLOT * (1 + NSLOT);
static constexpr u32 OFF_DBG = OFF_XN0 + 320 * CHUNK;
static constexpr u32 WS_END = OFF_DBG + 256u * 66 * 256;
DEVI u32 kb_off(int kb) { return (u32)((kb >> 3) * CHUNK + (kb & 7) * 1024); }
static constexpr u32 OOB = 0x80000000u;
static constexpr int LDS_BYTES = 163840;
static constexpr unsigned SPIN_LIMIT = 1u << 18;
static constexpr int SL_REC = 0, SL_P = 8, SL_ATT = 16, SL_WO = 18, SL_UP = 21, SL_DOWN = 27, SL_REDA = 30, SL_REDF = 31;
DEVI u32 mbox(int L, int j, int role, int slot) { return MBOX + (u32)(j * CHUNK + L * 8192 + role * 256 + 4 * slot); }
DEVI u32 mbox_att(int L, int j, int hl, int slot) { return mbox(L, j, SL_ATT + (hl >> 2), 16 * (hl & 3) + slot); }

struct Params {
  const u16* x; const u16* mem0; const u16* Wq; const u16* Wk; const u16* Wv; const u16* Wo;
  const u16* w1; const u16* w3; const u16* w2; u16* out; unsigned char* ws;
  int role_mask; int unused;
};

template <class F, int... I> DEVI void static_for_impl(F&& f, std::integer_sequence<int, I...>) { (f(std::integral_constant<int, I>{}), ...); }
template <int N, class F> DEVI void static_for(F&& f) { static_for_impl(f, std::make_integer_sequence<int, N>{}); }
DEVI float bflo(u32 d) { return __uint_as_float(d << 16); }
DEVI float bfhi(u32 d) { return __uint_as_float(d & 0xffff0000u); }
DEVI u32 pk2(float a, float b) {
  f32x2 f = {a, b};
  bf2_t r = __builtin_convertvector(f, bf2_t);
  return __builtin_bit_cast(u32, r);
}
DEVI bf16x8 as_frag(v4u v) { return __builtin_bit_cast(bf16x8, v); }
DEVI bf16x4 as_frag4(v2u v) { return __builtin_bit_cast(bf16x4, v); }
DEVI f32x4 mfma32(v4u a, v4u b, f32x4 c) { return __builtin_amdgcn_mfma_f32_16x16x32_bf16(as_frag(a), as_frag(b), c, 0, 0, 0); }
DEVI f32x4 mfma16(v2u a, v2u b, f32x4 c) { return __builtin_amdgcn_mfma_f32_16x16x16bf16_1k(as_frag4(a), as_frag4(b), c, 0, 0, 0); }
DEVI v4u ld16(rsrc_t r, u32 voff, u32 soff) { return __builtin_amdgcn_raw_buffer_load_b128(r, voff, soff, 16); }
DEVI v2u ld8(rsrc_t r, u32 voff, u32 soff) { return __builtin_amdgcn_raw_buffer_load_b64(r, voff, soff, 16); }
DEVI u32 ld4(rsrc_t r, u32 voff, u32 soff) { return __builtin_amdgcn_raw_buffer_load_b32(r, voff, soff, 16); }
DEVI void st16(rsrc_t r, v4u v, u32 voff, u32 soff) { __builtin_amdgcn_raw_buffer_store_b128(v, r, voff, soff, 16); }
DEVI void st8(rsrc_t r, v2u v, u32 voff, u32 soff) { __builtin_amdgcn_raw_buffer_store_b64(v, r, voff, soff, 16); }
DEVI void st4(rsrc_t r, u32 v, u32 voff, u32 soff) { __builtin_amdgcn_raw_buffer_store_b32(v, r, voff, soff, 16); }
DEVI void drain() { asm volatile("s_waitcnt vmcnt(0)" ::: "memory"); }
DEVI void cbar() { asm volatile("" ::: "memory"); }
DEVI void sbar() { __builtin_amdgcn_sched_barrier(0); }
DEVI u32 rfl(u32 v) { return (u32)__builtin_amdgcn_readfirstlane((int)v); }
DEVI float xmax16_32(float x) {
  u32 a = __float_as_uint(x);
  auto r = __builtin_amdgcn_permlane16_swap(a, a, false, false);
  x = fmaxf(__uint_as_float(r[0]), __uint_as_float(r[1]));
  a = __float_as_uint(x);
  auto q = __builtin_amdgcn_permlane32_swap(a, a, false, false);
  return fmaxf(__uint_as_float(q[0]), __uint_as_float(q[1]));
}
DEVI float xsum16_32(float x) {
  u32 a = __float_as_uint(x);
  auto r = __builtin_amdgcn_permlane16_swap(a, a, false, false);
  x = __uint_as_float(r[0]) + __uint_as_float(r[1]);
  a = __float_as_uint(x);
  auto q = __builtin_amdgcn_permlane32_swap(a, a, false, false);
  return __uint_as_float(q[0]) + __uint_as_float(q[1]);
}
DEVI float ror8(float x) { return __int_as_float(__builtin_amdgcn_update_dpp(0, __float_as_int(x), 0x128, 0xf, 0xf, false)); }
DEVI v4u swap16(v2u a, v2u b) {
  auto s0 = __builtin_amdgcn_permlane16_swap(a.x, b.x, false, false);
  auto s1 = __builtin_amdgcn_permlane16_swap(a.y, b.y, false, false);
  return (v4u){s0[0], s1[0], s0[1], s1[1]};
}

#define STAMP(row, slot) do { } while (0)

DEVI bool flags_ok(int v, int bias, int s) {
  asm volatile("" : "+v"(v));
  return __all(v + bias >= s);
}
template <int SLP> DEVI void wait_flags(rsrc_t r, u32 addr, int bias, int s, unsigned& budget) {
  for (;;) {
    cbar();
    const int v = (int)ld4(r, addr, 0);
    if (flags_ok(v, bias, s) || budget == 0) break;
    --budget;
    __builtin_amdgcn_s_sleep(SLP);
  }
  cbar();
}
DEVI void publish(rsrc_t r, u32 flag, int s, int lane) {
  drain();
  if (lane == 0) st4(r, (u32)s, flag, 0);
  cbar();
}
DEVI void delay_start(int us, unsigned long long t0) {
  if (us == 0) return;
  while (__builtin_amdgcn_s_memrealtime() - t0 < (unsigned long long)us * 100u) __builtin_amdgcn_s_sleep(16);
}
DEVI void pull_weights(const Params& p, int j, int n) {
  const int tid = threadIdx.x;
  u32 acc = 0;
  if (n < 3) {
    const rsrc_t r = __builtin_amdgcn_make_buffer_rsrc((void*)(p.Wo + (size_t)j * 1024 * 576), 0, 1024 * 1152, 0x00020000);
#pragma unroll
    for (int k = 0; k < 12; ++k) {
      const int l = (k * 3 + n) * 256 + tid, hf = l / 4608, m = l % 4608;
      acc ^= __builtin_amdgcn_raw_buffer_load_b32(r, (u32)((m / 576) * 1152 + hf * 576 + m % 576) * 128, 0, 0);
    }
  } else {
    const u16* w = n % 3 == 0 ? p.Wq : (n % 3 == 1 ? p.Wk : p.Wv);
    const rsrc_t r = __builtin_amdgcn_make_buffer_rsrc((void*)(w + j * 1024), 0, 576 * 8192 - j * 2048, 0x00020000);
#pragma unroll
    for (int k = 0; k < 12; ++k) {
      const int row = (k * 3 + n / 3 - 1) * 16 + (tid >> 4);
      acc ^= __builtin_amdgcn_raw_buffer_load_b32(r, (u32)(row * 8192 + (tid & 15) * 128), 0, 0);
    }
  }
  asm volatile("" :: "v"(acc));
}
DEVI v4u make_srd(const void* p, u32 bytes) {
  const unsigned long long a = (unsigned long long)p;
  return (v4u){rfl((u32)a), rfl((u32)(a >> 32) & 0xffffu), bytes, 0x00020000u};
}
DEVI v16u cat4(v4u a, v4u b, v4u c, v4u d) {
  return (v16u){a.x, a.y, a.z, a.w, b.x, b.y, b.z, b.w, c.x, c.y, c.z, c.w, d.x, d.y, d.z, d.w};
}
DEVI u32 wswz(int j, int g) { return (u32)(4 * ((j >> 1) & 1) ^ 2 * (g & 1)); }
DEVI u32 tr_base(int lane, int chunk) {
  const int g = lane >> 4, q = (lane >> 2) & 3;
  return (u32)(g * 1024 + q * 128) + (((u32)chunk ^ wswz(q, g)) << 4);
}

#define RUN_ASM(TEXT) do { for (int i_ = 0; i_ < 16; ++i_) { sa[i_] = rfl(sa[i_]); sb[i_] = rfl(sb[i_]); } \
    asm volatile(TEXT "s_endpgm\n" :: "{s[36:51]}"(sa), "{s[52:67]}"(sb), "{v[0:15]}"(va) : "v255", "a255", "s99", "vcc", "memory"); \
    __builtin_unreachable(); } while (0)

#define GEMM_DECL v4u g_w0, g_w1, g_w2; u32 g_ldw, g_in_base, g_in_stride, g_in_mask, g_out_base, g_out_kb, g_out_mask, g_flag, g_paddr; int g_pbias, g_pinit = -1
#define GEMM_ARGS g_w0, g_w1, g_w2, g_ldw, g_in_base, g_in_stride, g_in_mask, g_out_base, g_out_kb, g_out_mask, g_flag, g_paddr, g_pbias, g_pinit
DEVI void gemm_args(v4u g_w0, v4u g_w1, v4u g_w2, u32 g_ldw, u32 g_in_base, u32 g_in_stride, u32 g_in_mask, u32 g_out_base, u32 g_out_kb,
                    u32 g_out_mask, u32 g_flag, u32 g_paddr, int g_pbias, int g_pinit, const Params& p, u32 dbg, unsigned long long tag, v16u& sa, v16u& sb, v16u& va) {
  const int tid = threadIdx.x, lane = tid & 63, w = (int)rfl(tid >> 6);
  const int c = lane & 15, gg = lane >> 4, pp = lane & 3;
  const unsigned long long fm = __ballot(g_flag != OOB);
  sa = cat4(make_srd(p.ws, WS_END), g_w0, g_w1, g_w2);
  sb = (v16u){g_in_base, g_in_stride, g_in_mask, g_out_base, SLOT, g_out_mask, 0, (u32)w * 1024, dbg + w * 64, 0, (u32)fm, (u32)(fm >> 32), 0, 0, (u32)(2 * tag), (u32)((2 * tag) >> 32)};
  va[0] = lane * 16;
  va[1] = lane * 16 + w * 2048;
  va[2] = g_paddr;
  va[3] = (u32)g_pbias;
  va[4] = kb_off((int)g_out_kb + 8 + 2 * (w >> 1)) + (u32)((gg & 1) * 1024 + ((2 * (w & 1) + (gg >> 1)) * 16 + c) * 16);
  va[5] = (u32)(8 * w + (lane >> 3)) * g_ldw + ((((u32)lane & 7) ^ wswz(lane >> 3, w)) << 4);
  va[6] = (u32)(w >> 1) * 4096 + tr_base(lane, 4 * (w & 1) + pp);
  va[7] = 2 * 4096 + tr_base(lane, 2 * w + (pp >> 1)) + 8 * (pp & 1);
  for (int i = 8; i < 16; ++i) va[i] = 0;
  va[11] = (u32)g_pinit;
  va[14] = g_flag;
  va[15] = kb_off((int)g_out_kb + 2 * w) + lane * 16;
}

DEVI void role_p(const Params& p, int L, int j, int i, u32 dbg, unsigned long long tag) {
  const int lane = threadIdx.x & 63, w = threadIdx.x >> 6, h = 8 * j + i;
  const u32 wb = 576u * 4096 * 2 - h * 256;
  GEMM_DECL;
  g_w0 = make_srd(p.Wq + h * 128, wb); g_w1 = make_srd(p.Wk + h * 128, wb); g_w2 = make_srd(p.Wv + h * 128, wb);
  g_ldw = 8192;
  g_in_base = L ? pgb(1, B_XN) : OFF_XN0; g_in_stride = L ? SLOT : 5 * CHUNK; g_in_mask = L ? RX2 - 1 : 63;
  g_out_base = pga(L, B_QKV + 3 * h); g_out_kb = 0; g_out_mask = RQ - 1;
  g_flag = lane == 0 ? mbox(L, j, SL_REC + i, w) : (lane == 1 ? mbox_att(L, j, i, 8 + w) : OOB);
  g_paddr = mbox(L, j, SL_P + i, lane);
  g_pbias = lane < (L ? 16 : 48) ? 0 : (lane == 48 ? RQ : 1000);
  g_pinit = lane < 16 || lane >= 48 ? -1 : (lane < 32 ? LN_B - 1 : LN_C - 1);
  v16u sa, sb, va;
  gemm_args(GEMM_ARGS, p, dbg, tag, sa, sb, va);
  RUN_ASM(
    "s_nop 4\n"
    "v_add_u32 v8, s59, v0\n"
    "s_mov_b32 s68, 0\n"
    "s_mov_b32 s71, 0\n"
    "s_mov_b32 s78, 65536\n"
    "v_add_u32 v9, s78, v0\n"
    "s_mov_b32 s64, 0xbfb8aa3b\n"
    "s_mov_b32 s72, 262144\n"
    "s_add_u32 m0, s59, 0\n"
    "s_mov_b32 s74, 0\n"
    "buffer_load_dwordx4 v5, s[40:43], s74 offen lds\n"
    "s_add_u32 m0, s59, 4096\n"
    "s_mov_b32 s74, 128\n"
    "buffer_load_dwordx4 v5, s[40:43], s74 offen lds\n"
    "s_add_u32 m0, s59, 8192\n"
    "s_mov_b32 s74, 0\n"
    "buffer_load_dwordx4 v5, s[44:47], s74 offen lds\n"
    "s_add_u32 m0, s59, 12288\n"
    "s_mov_b32 s74, 128\n"
    "buffer_load_dwordx4 v5, s[44:47], s74 offen lds\n"
    "s_add_u32 m0, s59, 16384\n"
    "s_mov_b32 s74, 0\n"
    "buffer_load_dwordx4 v5, s[48:51], s74 offen lds\n"
    "s_add_u32 m0, s59, 20480\n"
    "s_mov_b32 s74, 128\n"
    "buffer_load_dwordx4 v5, s[48:51], s74 offen lds\n"
    "s_add_u32 m0, s59, 24576\n"
    "s_mov_b32 s74, 262144\n"
    "buffer_load_dwordx4 v5, s[40:43], s74 offen lds\n"
    "s_add_u32 m0, s59, 28672\n"
    "s_mov_b32 s74, 262272\n"
    "buffer_load_dwordx4 v5, s[40:43], s74 offen lds\n"
    "s_add_u32 m0, s59, 32768\n"
    "s_mov_b32 s74, 262144\n"
    "buffer_load_dwordx4 v5, s[44:47], s74 offen lds\n"
    "s_add_u32 m0, s59, 36864\n"
    "s_mov_b32 s74, 262272\n"
    "buffer_load_dwordx4 v5, s[44:47], s74 offen lds\n"
    "s_add_u32 m0, s59, 40960\n"
    "s_mov_b32 s74, 262144\n"
    "buffer_load_dwordx4 v5, s[48:51], s74 offen lds\n"
    "s_add_u32 m0, s59, 45056\n"
    "s_mov_b32 s74, 262272\n"
    "buffer_load_dwordx4 v5, s[48:51], s74 offen lds\n"
    "s_add_u32 m0, s59, 49152\n"
    "s_mov_b32 s74, 524288\n"
    "buffer_load_dwordx4 v5, s[40:43], s74 offen lds\n"
    "s_add_u32 m0, s59, 53248\n"
    "s_mov_b32 s74, 524416\n"
    "buffer_load_dwordx4 v5, s[40:43], s74 offen lds\n"
    "s_add_u32 m0, s59, 57344\n"
    "s_mov_b32 s74, 524288\n"
    "buffer_load_dwordx4 v5, s[44:47], s74 offen lds\n"
    "s_add_u32 m0, s59, 61440\n"
    "s_mov_b32 s74, 524416\n"
    "buffer_load_dwordx4 v5, s[44:47], s74 offen lds\n"
    "s_add_u32 m0, s59, 65536\n"
    "s_mov_b32 s74, 524288\n"
    "buffer_load_dwordx4 v5, s[48:51], s74 offen lds\n"
    "s_add_u32 m0, s59, 69632\n"
    "s_mov_b32 s74, 524416\n"
    "buffer_load_dwordx4 v5, s[48:51], s74 offen lds\n"
    "s_add_u32 m0, s59, 73728\n"
    "s_mov_b32 s74, 786432\n"
    "buffer_load_dwordx4 v5, s[40:43], s74 offen lds\n"
    "s_add_u32 m0, s59, 77824\n"
    "s_mov_b32 s74, 786560\n"
    "buffer_load_dwordx4 v5, s[40:43], s74 offen lds\n"
    "s_add_u32 m0, s59, 81920\n"
    "s_mov_b32 s74, 786432\n"
    "buffer_load_dwordx4 v5, s[44:47], s74 offen lds\n"
    "s_add_u32 m0, s59, 86016\n"
    "s_mov_b32 s74, 786560\n"
    "buffer_load_dwordx4 v5, s[44:47], s74 offen lds\n"
    "s_add_u32 m0, s59, 90112\n"
    "s_mov_b32 s74, 786432\n"
    "buffer_load_dwordx4 v5, s[48:51], s74 offen lds\n"
    "s_add_u32 m0, s59, 94208\n"
    "s_mov_b32 s74, 786560\n"
    "buffer_load_dwordx4 v5, s[48:51], s74 offen lds\n"
    "s_add_u32 m0, s59, 98304\n"
    "s_mov_b32 s74, 1048576\n"
    "buffer_load_dwordx4 v5, s[40:43], s74 offen lds\n"
    "s_add_u32 m0, s59, 102400\n"
    "s_mov_b32 s74, 1048704\n"
    "buffer_load_dwordx4 v5, s[40:43], s74 offen lds\n"
    "s_add_u32 m0, s59, 106496\n"
    "s_mov_b32 s74, 1048576\n"
    "buffer_load_dwordx4 v5, s[44:47], s74 offen lds\n"
    "s_add_u32 m0, s59, 110592\n"
    "s_mov_b32 s74, 1048704\n"
    "buffer_load_dwordx4 v5, s[44:47], s74 offen lds\n"
    "s_add_u32 m0, s59, 114688\n"
    "s_mov_b32 s74, 1048576\n"
    "buffer_load_dwordx4 v5, s[48:51], s74 offen lds\n"
    "s_add_u32 m0, s59, 118784\n"
    "s_mov_b32 s74, 1048704\n"
    "buffer_load_dwordx4 v5, s[48:51], s74 offen lds\n"
    "v_add_u32 v16, 49152, v6\n"
    "v_add_u32 v17, 98304, v6\n"
    "v_add_u32 v18, 49152, v7\n"
    "v_add_u32 v19, 98304, v7\n"
    "s_waitcnt vmcnt(24) lgkmcnt(0)\n"
    "s_barrier\n"
    "s_add_u32 m0, s59, 122880\n"
    "s_mov_b32 s74, 1310720\n"
    "buffer_load_dwordx4 v5, s[40:43], s74 offen lds\n"
    "s_add_u32 m0, s59, 126976\n"
    "s_mov_b32 s74, 1310848\n"
    "buffer_load_dwordx4 v5, s[40:43], s74 offen lds\n"
    "s_add_u32 m0, s59, 131072\n"
    "s_mov_b32 s74, 1310720\n"
    "buffer_load_dwordx4 v5, s[44:47], s74 offen lds\n"
    "s_add_u32 m0, s59, 135168\n"
    "s_mov_b32 s74, 1310848\n"
    "buffer_load_dwordx4 v5, s[44:47], s74 offen lds\n"
    "s_add_u32 m0, s59, 139264\n"
    "s_mov_b32 s74, 1310720\n"
    "buffer_load_dwordx4 v5, s[48:51], s74 offen lds\n"
    "s_add_u32 m0, s59, 143360\n"
    "s_mov_b32 s74, 1310848\n"
    "buffer_load_dwordx4 v5, s[48:51], s74 offen lds\n"
    "ds_read_b64_tr_b16 a[0:1], v6 offset:8192\n"
    "ds_read_b64_tr_b16 a[2:3], v6 offset:8704\n"
    "ds_read_b64_tr_b16 a[4:5], v6 offset:8200\n"
    "ds_read_b64_tr_b16 a[6:7], v6 offset:8712\n"
    "ds_read_b64_tr_b16 a[8:9], v6 offset:16384\n"
    "ds_read_b64_tr_b16 a[10:11], v6 offset:16896\n"
    "ds_read_b64_tr_b16 a[12:13], v6 offset:16392\n"
    "ds_read_b64_tr_b16 a[14:15], v6 offset:16904\n"
    "ds_read_b64_tr_b16 a[16:17], v6 offset:0\n"
    "ds_read_b64_tr_b16 a[18:19], v6 offset:512\n"
    "ds_read_b64_tr_b16 a[20:21], v6 offset:8\n"
    "ds_read_b64_tr_b16 a[22:23], v6 offset:520\n"
    "s_waitcnt vmcnt(24) lgkmcnt(0)\n"
    "s_barrier\n"
    "s_add_u32 m0, s59, 0\n"
    "s_mov_b32 s74, 1572864\n"
    "buffer_load_dwordx4 v5, s[40:43], s74 offen lds\n"
    "s_add_u32 m0, s59, 4096\n"
    "s_mov_b32 s74, 1572992\n"
    "buffer_load_dwordx4 v5, s[40:43], s74 offen lds\n"
    "s_add_u32 m0, s59, 8192\n"
    "s_mov_b32 s74, 1572864\n"
    "buffer_load_dwordx4 v5, s[44:47], s74 offen lds\n"
    "s_add_u32 m0, s59, 12288\n"
    "s_mov_b32 s74, 1572992\n"
    "buffer_load_dwordx4 v5, s[44:47], s74 offen lds\n"
    "s_add_u32 m0, s59, 16384\n"
    "s_mov_b32 s74, 1572864\n"
    "buffer_load_dwordx4 v5, s[48:51], s74 offen lds\n"
    "s_add_u32 m0, s59, 20480\n"
    "s_mov_b32 s74, 1572992\n"
    "buffer_load_dwordx4 v5, s[48:51], s74 offen lds\n"
    "ds_read_b64_tr_b16 a[24:25], v6 offset:32768\n"
    "ds_read_b64_tr_b16 a[26:27], v6 offset:33280\n"
    "ds_read_b64_tr_b16 a[28:29], v6 offset:32776\n"
    "ds_read_b64_tr_b16 a[30:31], v6 offset:33288\n"
    "ds_read_b64_tr_b16 a[32:33], v6 offset:40960\n"
    "ds_read_b64_tr_b16 a[34:35], v6 offset:41472\n"
    "ds_read_b64_tr_b16 a[36:37], v6 offset:40968\n"
    "ds_read_b64_tr_b16 a[38:39], v6 offset:41480\n"
    "ds_read_b64_tr_b16 a[40:41], v6 offset:24576\n"
    "ds_read_b64_tr_b16 a[42:43], v6 offset:25088\n"
    "ds_read_b64_tr_b16 a[44:45], v6 offset:24584\n"
    "ds_read_b64_tr_b16 a[46:47], v6 offset:25096\n"
    "s_waitcnt vmcnt(24) lgkmcnt(0)\n"
    "s_barrier\n"
    "s_add_u32 m0, s59, 24576\n"
    "s_mov_b32 s74, 1835008\n"
    "buffer_load_dwordx4 v5, s[40:43], s74 offen lds\n"
    "s_add_u32 m0, s59, 28672\n"
    "s_mov_b32 s74, 1835136\n"
    "buffer_load_dwordx4 v5, s[40:43], s74 offen lds\n"
    "s_add_u32 m0, s59, 32768\n"
    "s_mov_b32 s74, 1835008\n"
    "buffer_load_dwordx4 v5, s[44:47], s74 offen lds\n"
    "s_add_u32 m0, s59, 36864\n"
    "s_mov_b32 s74, 1835136\n"
    "buffer_load_dwordx4 v5, s[44:47], s74 offen lds\n"
    "s_add_u32 m0, s59, 40960\n"
    "s_mov_b32 s74, 1835008\n"
    "buffer_load_dwordx4 v5, s[48:51], s74 offen lds\n"
    "s_add_u32 m0, s59, 45056\n"
    "s_mov_b32 s74, 1835136\n"
    "buffer_load_dwordx4 v5, s[48:51], s74 offen lds\n"
    "ds_read_b64_tr_b16 a[48:49], v16 offset:8192\n"
    "ds_read_b64_tr_b16 a[50:51], v16 offset:8704\n"
    "ds_read_b64_tr_b16 a[52:53], v16 offset:8200\n"
    "ds_read_b64_tr_b16 a[54:55], v16 offset:8712\n"
    "ds_read_b64_tr_b16 a[56:57], v16 offset:16384\n"
    "ds_read_b64_tr_b16 a[58:59], v16 offset:16896\n"
    "ds_read_b64_tr_b16 a[60:61], v16 offset:16392\n"
    "ds_read_b64_tr_b16 a[62:63], v16 offset:16904\n"
    "ds_read_b64_tr_b16 a[64:65], v16 offset:0\n"
    "ds_read_b64_tr_b16 a[66:67], v16 offset:512\n"
    "ds_read_b64_tr_b16 a[68:69], v16 offset:8\n"
    "ds_read_b64_tr_b16 a[70:71], v16 offset:520\n"
    "s_waitcnt vmcnt(24) lgkmcnt(0)\n"
    "s_barrier\n"
    "s_add_u32 m0, s59, 49152\n"
    "s_mov_b32 s74, 2097152\n"
    "buffer_load_dwordx4 v5, s[40:43], s74 offen lds\n"
    "s_add_u32 m0, s59, 53248\n"
    "s_mov_b32 s74, 2097280\n"
    "buffer_load_dwordx4 v5, s[40:43], s74 offen lds\n"
    "s_add_u32 m0, s59, 57344\n"
    "s_mov_b32 s74, 2097152\n"
    "buffer_load_dwordx4 v5, s[44:47], s74 offen lds\n"
    "s_add_u32 m0, s59, 61440\n"
    "s_mov_b32 s74, 2097280\n"
    "buffer_load_dwordx4 v5, s[44:47], s74 offen lds\n"
    "s_add_u32 m0, s59, 65536\n"
    "s_mov_b32 s74, 2097152\n"
    "buffer_load_dwordx4 v5, s[48:51], s74 offen lds\n"
    "s_add_u32 m0, s59, 69632\n"
    "s_mov_b32 s74, 2097280\n"
    "buffer_load_dwordx4 v5, s[48:51], s74 offen lds\n"
    "ds_read_b64_tr_b16 a[72:73], v16 offset:32768\n"
    "ds_read_b64_tr_b16 a[74:75], v16 offset:33280\n"
    "ds_read_b64_tr_b16 a[76:77], v16 offset:32776\n"
    "ds_read_b64_tr_b16 a[78:79], v16 offset:33288\n"
    "ds_read_b64_tr_b16 a[80:81], v16 offset:40960\n"
    "ds_read_b64_tr_b16 a[82:83], v16 offset:41472\n"
    "ds_read_b64_tr_b16 a[84:85], v16 offset:40968\n"
    "ds_read_b64_tr_b16 a[86:87], v16 offset:41480\n"
    "ds_read_b64_tr_b16 a[88:89], v16 offset:24576\n"
    "ds_read_b64_tr_b16 a[90:91], v16 offset:25088\n"
    "ds_read_b64_tr_b16 a[92:93], v16 offset:24584\n"
    "ds_read_b64_tr_b16 a[94:95], v16 offset:25096\n"
    "s_waitcnt vmcnt(24) lgkmcnt(0)\n"
    "s_barrier\n"
    "s_add_u32 m0, s59, 73728\n"
    "s_mov_b32 s74, 2359296\n"
    "buffer_load_dwordx4 v5, s[40:43], s74 offen lds\n"
    "s_add_u32 m0, s59, 77824\n"
    "s_mov_b32 s74, 2359424\n"
    "buffer_load_dwordx4 v5, s[40:43], s74 offen lds\n"
    "s_add_u32 m0, s59, 81920\n"
    "s_mov_b32 s74, 2359296\n"
    "buffer_load_dwordx4 v5, s[44:47], s74 offen lds\n"
    "s_add_u32 m0, s59, 86016\n"
    "s_mov_b32 s74, 2359424\n"
    "buffer_load_dwordx4 v5, s[44:47], s74 offen lds\n"
    "s_add_u32 m0, s59, 90112\n"
    "s_mov_b32 s74, 2359296\n"
    "buffer_load_dwordx4 v5, s[48:51], s74 offen lds\n"
    "s_add_u32 m0, s59, 94208\n"
    "s_mov_b32 s74, 2359424\n"
    "buffer_load_dwordx4 v5, s[48:51], s74 offen lds\n"
    "ds_read_b64_tr_b16 a[96:97], v17 offset:8192\n"
    "ds_read_b64_tr_b16 a[98:99], v17 offset:8704\n"
    "ds_read_b64_tr_b16 a[100:101], v17 offset:8200\n"
    "ds_read_b64_tr_b16 a[102:103], v17 offset:8712\n"
    "ds_read_b64_tr_b16 a[104:105], v17 offset:16384\n"
    "ds_read_b64_tr_b16 a[106:107], v17 offset:16896\n"
    "ds_read_b64_tr_b16 a[108:109], v17 offset:16392\n"
    "ds_read_b64_tr_b16 a[110:111], v17 offset:16904\n"
    "ds_read_b64_tr_b16 a[112:113], v17 offset:0\n"
    "ds_read_b64_tr_b16 a[114:115], v17 offset:512\n"
    "ds_read_b64_tr_b16 a[116:117], v17 offset:8\n"
    "ds_read_b64_tr_b16 a[118:119], v17 offset:520\n"
    "s_waitcnt vmcnt(24) lgkmcnt(0)\n"
    "s_barrier\n"
    "s_add_u32 m0, s59, 98304\n"
    "s_mov_b32 s74, 2621440\n"
    "buffer_load_dwordx4 v5, s[40:43], s74 offen lds\n"
    "s_add_u32 m0, s59, 102400\n"
    "s_mov_b32 s74, 2621568\n"
    "buffer_load_dwordx4 v5, s[40:43], s74 offen lds\n"
    "s_add_u32 m0, s59, 106496\n"
    "s_mov_b32 s74, 2621440\n"
    "buffer_load_dwordx4 v5, s[44:47], s74 offen lds\n"
    "s_add_u32 m0, s59, 110592\n"
    "s_mov_b32 s74, 2621568\n"
    "buffer_load_dwordx4 v5, s[44:47], s74 offen lds\n"
    "s_add_u32 m0, s59, 114688\n"
    "s_mov_b32 s74, 2621440\n"
    "buffer_load_dwordx4 v5, s[48:51], s74 offen lds\n"
    "s_add_u32 m0, s59, 118784\n"
    "s_mov_b32 s74, 2621568\n"
    "buffer_load_dwordx4 v5, s[48:51], s74 offen lds\n"
    "ds_read_b64_tr_b16 a[120:121], v17 offset:32768\n"
    "ds_read_b64_tr_b16 a[122:123], v17 offset:33280\n"
    "ds_read_b64_tr_b16 a[124:125], v17 offset:32776\n"
    "ds_read_b64_tr_b16 a[126:127], v17 offset:33288\n"
    "ds_read_b64_tr_b16 a[128:129], v17 offset:40960\n"
    "ds_read_b64_tr_b16 a[130:131], v17 offset:41472\n"
    "ds_read_b64_tr_b16 a[132:133], v17 offset:40968\n"
    "ds_read_b64_tr_b16 a[134:135], v17 offset:41480\n"
    "ds_read_b64_tr_b16 a[136:137], v17 offset:24576\n"
    "ds_read_b64_tr_b16 a[138:139], v17 offset:25088\n"
    "ds_read_b64_tr_b16 a[140:141], v17 offset:24584\n"
    "ds_read_b64_tr_b16 a[142:143], v17 offset:25096\n"
    "s_waitcnt vmcnt(24) lgkmcnt(0)\n"
    "s_barrier\n"
    "s_add_u32 m0, s59, 122880\n"
    "s_mov_b32 s74, 2883584\n"
    "buffer_load_dwordx4 v5, s[40:43], s74 offen lds\n"
    "s_add_u32 m0, s59, 126976\n"
    "s_mov_b32 s74, 2883712\n"
    "buffer_load_dwordx4 v5, s[40:43], s74 offen lds\n"
    "s_add_u32 m0, s59, 131072\n"
    "s_mov_b32 s74, 2883584\n"
    "buffer_load_dwordx4 v5, s[44:47], s74 offen lds\n"
    "s_add_u32 m0, s59, 135168\n"
    "s_mov_b32 s74, 2883712\n"
    "buffer_load_dwordx4 v5, s[44:47], s74 offen lds\n"
    "s_add_u32 m0, s59, 139264\n"
    "s_mov_b32 s74, 2883584\n"
    "buffer_load_dwordx4 v5, s[48:51], s74 offen lds\n"
    "s_add_u32 m0, s59, 143360\n"
    "s_mov_b32 s74, 2883712\n"
    "buffer_load_dwordx4 v5, s[48:51], s74 offen lds\n"
    "ds_read_b64_tr_b16 a[144:145], v6 offset:8192\n"
    "ds_read_b64_tr_b16 a[146:147], v6 offset:8704\n"
    "ds_read_b64_tr_b16 a[148:149], v6 offset:8200\n"
    "ds_read_b64_tr_b16 a[150:151], v6 offset:8712\n"
    "ds_read_b64_tr_b16 a[152:153], v6 offset:16384\n"
    "ds_read_b64_tr_b16 a[154:155], v6 offset:16896\n"
    "ds_read_b64_tr_b16 a[156:157], v6 offset:16392\n"
    "ds_read_b64_tr_b16 a[158:159], v6 offset:16904\n"
    "ds_read_b64_tr_b16 a[160:161], v6 offset:0\n"
    "ds_read_b64_tr_b16 a[162:163], v6 offset:512\n"
    "ds_read_b64_tr_b16 a[164:165], v6 offset:8\n"
    "ds_read_b64_tr_b16 a[166:167], v6 offset:520\n"
    "s_waitcnt vmcnt(24) lgkmcnt(0)\n"
    "s_barrier\n"
    "s_add_u32 m0, s59, 0\n"
    "s_mov_b32 s74, 3145728\n"
    "buffer_load_dwordx4 v5, s[40:43], s74 offen lds\n"
    "s_add_u32 m0, s59, 4096\n"
    "s_mov_b32 s74, 3145856\n"
    "buffer_load_dwordx4 v5, s[40:43], s74 offen lds\n"
    "s_add_u32 m0, s59, 8192\n"
    "s_mov_b32 s74, 3145728\n"
    "buffer_load_dwordx4 v5, s[44:47], s74 offen lds\n"
    "s_add_u32 m0, s59, 12288\n"
    "s_mov_b32 s74, 3145856\n"
    "buffer_load_dwordx4 v5, s[44:47], s74 offen lds\n"
    "s_add_u32 m0, s59, 16384\n"
    "s_mov_b32 s74, 3145728\n"
    "buffer_load_dwordx4 v5, s[48:51], s74 offen lds\n"
    "s_add_u32 m0, s59, 20480\n"
    "s_mov_b32 s74, 3145856\n"
    "buffer_load_dwordx4 v5, s[48:51], s74 offen lds\n"
    "ds_read_b64_tr_b16 a[168:169], v6 offset:32768\n"
    "ds_read_b64_tr_b16 a[170:171], v6 offset:33280\n"
    "ds_read_b64_tr_b16 a[172:173], v6 offset:32776\n"
    "ds_read_b64_tr_b16 a[174:175], v6 offset:33288\n"
    "ds_read_b64_tr_b16 a[176:177], v6 offset:40960\n"
    "ds_read_b64_tr_b16 a[178:179], v6 offset:41472\n"
    "ds_read_b64_tr_b16 a[180:181], v6 offset:40968\n"
    "ds_read_b64_tr_b16 a[182:183], v6 offset:41480\n"
    "ds_read_b64_tr_b16 a[184:185], v6 offset:24576\n"
    "ds_read_b64_tr_b16 a[186:187], v6 offset:25088\n"
    "ds_read_b64_tr_b16 a[188:189], v6 offset:24584\n"
    "ds_read_b64_tr_b16 a[190:191], v6 offset:25096\n"
    "s_waitcnt vmcnt(24) lgkmcnt(0)\n"
    "s_barrier\n"
    "s_add_u32 m0, s59, 24576\n"
    "s_mov_b32 s74, 3407872\n"
    "buffer_load_dwordx4 v5, s[40:43], s74 offen lds\n"
    "s_add_u32 m0, s59, 28672\n"
    "s_mov_b32 s74, 3408000\n"
    "buffer_load_dwordx4 v5, s[40:43], s74 offen lds\n"
    "s_add_u32 m0, s59, 32768\n"
    "s_mov_b32 s74, 3407872\n"
    "buffer_load_dwordx4 v5, s[44:47], s74 offen lds\n"
    "s_add_u32 m0, s59, 36864\n"
    "s_mov_b32 s74, 3408000\n"
    "buffer_load_dwordx4 v5, s[44:47], s74 offen lds\n"
    "s_add_u32 m0, s59, 40960\n"
    "s_mov_b32 s74, 3407872\n"
    "buffer_load_dwordx4 v5, s[48:51], s74 offen lds\n"
    "s_add_u32 m0, s59, 45056\n"
    "s_mov_b32 s74, 3408000\n"
    "buffer_load_dwordx4 v5, s[48:51], s74 offen lds\n"
    "ds_read_b64_tr_b16 a[192:193], v16 offset:8192\n"
    "ds_read_b64_tr_b16 a[194:195], v16 offset:8704\n"
    "ds_read_b64_tr_b16 a[196:197], v16 offset:8200\n"
    "ds_read_b64_tr_b16 a[198:199], v16 offset:8712\n"
    "ds_read_b64_tr_b16 a[200:201], v16 offset:16384\n"
    "ds_read_b64_tr_b16 a[202:203], v16 offset:16896\n"
    "ds_read_b64_tr_b16 a[204:205], v16 offset:16392\n"
    "ds_read_b64_tr_b16 a[206:207], v16 offset:16904\n"
    "ds_read_b64_tr_b16 a[208:209], v16 offset:0\n"
    "ds_read_b64_tr_b16 a[210:211], v16 offset:512\n"
    "ds_read_b64_tr_b16 a[212:213], v16 offset:8\n"
    "ds_read_b64_tr_b16 a[214:215], v16 offset:520\n"
    "s_waitcnt vmcnt(24) lgkmcnt(0)\n"
    "s_barrier\n"
    "s_add_u32 m0, s59, 49152\n"
    "s_mov_b32 s74, 3670016\n"
    "buffer_load_dwordx4 v5, s[40:43], s74 offen lds\n"
    "s_add_u32 m0, s59, 53248\n"
    "s_mov_b32 s74, 3670144\n"
    "buffer_load_dwordx4 v5, s[40:43], s74 offen lds\n"
    "s_add_u32 m0, s59, 57344\n"
    "s_mov_b32 s74, 3670016\n"
    "buffer_load_dwordx4 v5, s[44:47], s74 offen lds\n"
    "s_add_u32 m0, s59, 61440\n"
    "s_mov_b32 s74, 3670144\n"
    "buffer_load_dwordx4 v5, s[44:47], s74 offen lds\n"
    "s_add_u32 m0, s59, 65536\n"
    "s_mov_b32 s74, 3670016\n"
    "buffer_load_dwordx4 v5, s[48:51], s74 offen lds\n"
    "s_add_u32 m0, s59, 69632\n"
    "s_mov_b32 s74, 3670144\n"
    "buffer_load_dwordx4 v5, s[48:51], s74 offen lds\n"
    "ds_read_b64_tr_b16 a[216:217], v16 offset:32768\n"
    "ds_read_b64_tr_b16 a[218:219], v16 offset:33280\n"
    "ds_read_b64_tr_b16 a[220:221], v16 offset:32776\n"
    "ds_read_b64_tr_b16 a[222:223], v16 offset:33288\n"
    "ds_read_b64_tr_b16 a[224:225], v16 offset:40960\n"
    "ds_read_b64_tr_b16 a[226:227], v16 offset:41472\n"
    "ds_read_b64_tr_b16 a[228:229], v16 offset:40968\n"
    "ds_read_b64_tr_b16 a[230:231], v16 offset:41480\n"
    "ds_read_b64_tr_b16 a[232:233], v16 offset:24576\n"
    "ds_read_b64_tr_b16 a[234:235], v16 offset:25088\n"
    "ds_read_b64_tr_b16 a[236:237], v16 offset:24584\n"
    "ds_read_b64_tr_b16 a[238:239], v16 offset:25096\n"
    "s_waitcnt vmcnt(24) lgkmcnt(0)\n"
    "s_barrier\n"
    "s_add_u32 m0, s59, 73728\n"
    "s_mov_b32 s74, 3932160\n"
    "buffer_load_dwordx4 v5, s[40:43], s74 offen lds\n"
    "s_add_u32 m0, s59, 77824\n"
    "s_mov_b32 s74, 3932288\n"
    "buffer_load_dwordx4 v5, s[40:43], s74 offen lds\n"
    "s_add_u32 m0, s59, 81920\n"
    "s_mov_b32 s74, 3932160\n"
    "buffer_load_dwordx4 v5, s[44:47], s74 offen lds\n"
    "s_add_u32 m0, s59, 86016\n"
    "s_mov_b32 s74, 3932288\n"
    "buffer_load_dwordx4 v5, s[44:47], s74 offen lds\n"
    "s_add_u32 m0, s59, 90112\n"
    "s_mov_b32 s74, 3932160\n"
    "buffer_load_dwordx4 v5, s[48:51], s74 offen lds\n"
    "s_add_u32 m0, s59, 94208\n"
    "s_mov_b32 s74, 3932288\n"
    "buffer_load_dwordx4 v5, s[48:51], s74 offen lds\n"
    "ds_read_b64_tr_b16 a[240:241], v17 offset:8192\n"
    "ds_read_b64_tr_b16 a[242:243], v17 offset:8704\n"
    "ds_read_b64_tr_b16 a[244:245], v17 offset:8200\n"
    "ds_read_b64_tr_b16 a[246:247], v17 offset:8712\n"
    "ds_read_b64_tr_b16 a[248:249], v17 offset:16384\n"
    "ds_read_b64_tr_b16 a[250:251], v17 offset:16896\n"
    "ds_read_b64_tr_b16 a[252:253], v17 offset:16392\n"
    "ds_read_b64_tr_b16 a[254:255], v17 offset:16904\n"
    "ds_read_b64_tr_b16 v[80:81], v17 offset:0\n"
    "ds_read_b64_tr_b16 v[82:83], v17 offset:512\n"
    "ds_read_b64_tr_b16 v[84:85], v17 offset:8\n"
    "ds_read_b64_tr_b16 v[86:87], v17 offset:520\n"
    "s_waitcnt vmcnt(24) lgkmcnt(0)\n"
    "s_barrier\n"
    "s_add_u32 m0, s59, 98304\n"
    "s_mov_b32 s74, 4194304\n"
    "buffer_load_dwordx4 v5, s[40:43], s74 offen lds\n"
    "s_add_u32 m0, s59, 102400\n"
    "s_mov_b32 s74, 4194432\n"
    "buffer_load_dwordx4 v5, s[40:43], s74 offen lds\n"
    "s_add_u32 m0, s59, 106496\n"
    "s_mov_b32 s74, 4194304\n"
    "buffer_load_dwordx4 v5, s[44:47], s74 offen lds\n"
    "s_add_u32 m0, s59, 110592\n"
    "s_mov_b32 s74, 4194432\n"
    "buffer_load_dwordx4 v5, s[44:47], s74 offen lds\n"
    "s_add_u32 m0, s59, 114688\n"
    "s_mov_b32 s74, 4194304\n"
    "buffer_load_dwordx4 v5, s[48:51], s74 offen lds\n"
    "s_add_u32 m0, s59, 118784\n"
    "s_mov_b32 s74, 4194432\n"
    "buffer_load_dwordx4 v5, s[48:51], s74 offen lds\n"
    "ds_read_b64_tr_b16 v[88:89], v17 offset:32768\n"
    "ds_read_b64_tr_b16 v[90:91], v17 offset:33280\n"
    "ds_read_b64_tr_b16 v[92:93], v17 offset:32776\n"
    "ds_read_b64_tr_b16 v[94:95], v17 offset:33288\n"
    "ds_read_b64_tr_b16 v[96:97], v17 offset:40960\n"
    "ds_read_b64_tr_b16 v[98:99], v17 offset:41472\n"
    "ds_read_b64_tr_b16 v[100:101], v17 offset:40968\n"
    "ds_read_b64_tr_b16 v[102:103], v17 offset:41480\n"
    "ds_read_b64_tr_b16 v[104:105], v17 offset:24576\n"
    "ds_read_b64_tr_b16 v[106:107], v17 offset:25088\n"
    "ds_read_b64_tr_b16 v[108:109], v17 offset:24584\n"
    "ds_read_b64_tr_b16 v[110:111], v17 offset:25096\n"
    "s_waitcnt vmcnt(24) lgkmcnt(0)\n"
    "s_barrier\n"
    "s_add_u32 m0, s59, 122880\n"
    "s_mov_b32 s74, 4456448\n"
    "buffer_load_dwordx4 v5, s[40:43], s74 offen lds\n"
    "s_add_u32 m0, s59, 126976\n"
    "s_mov_b32 s74, 4456576\n"
    "buffer_load_dwordx4 v5, s[40:43], s74 offen lds\n"
    "s_add_u32 m0, s59, 131072\n"
    "s_mov_b32 s74, 4456448\n"
    "buffer_load_dwordx4 v5, s[44:47], s74 offen lds\n"
    "s_add_u32 m0, s59, 135168\n"
    "s_mov_b32 s74, 4456576\n"
    "buffer_load_dwordx4 v5, s[44:47], s74 offen lds\n"
    "s_add_u32 m0, s59, 139264\n"
    "s_mov_b32 s74, 4456448\n"
    "buffer_load_dwordx4 v5, s[48:51], s74 offen lds\n"
    "s_add_u32 m0, s59, 143360\n"
    "s_mov_b32 s74, 4456576\n"
    "buffer_load_dwordx4 v5, s[48:51], s74 offen lds\n"
    "buffer_load_dwordx2 v[12:13], off, s[36:39], 0 sc1\n"
    "ds_read_b64_tr_b16 v[112:113], v6 offset:8192\n"
    "ds_read_b64_tr_b16 v[114:115], v6 offset:8704\n"
    "ds_read_b64_tr_b16 v[116:117], v6 offset:8200\n"
    "ds_read_b64_tr_b16 v[118:119], v6 offset:8712\n"
    "ds_read_b64_tr_b16 v[120:121], v6 offset:16384\n"
    "ds_read_b64_tr_b16 v[122:123], v6 offset:16896\n"
    "ds_read_b64_tr_b16 v[124:125], v6 offset:16392\n"
    "ds_read_b64_tr_b16 v[126:127], v6 offset:16904\n"
    "ds_read_b64_tr_b16 v[128:129], v6 offset:0\n"
    "ds_read_b64_tr_b16 v[130:131], v6 offset:512\n"
    "ds_read_b64_tr_b16 v[132:133], v6 offset:8\n"
    "ds_read_b64_tr_b16 v[134:135], v6 offset:520\n"
    "s_waitcnt vmcnt(25) lgkmcnt(0)\n"
    "s_barrier\n"
    "ds_read_b64_tr_b16 v[136:137], v6 offset:32768\n"
    "ds_read_b64_tr_b16 v[138:139], v6 offset:33280\n"
    "ds_read_b64_tr_b16 v[140:141], v6 offset:32776\n"
    "ds_read_b64_tr_b16 v[142:143], v6 offset:33288\n"
    "ds_read_b64_tr_b16 v[144:145], v6 offset:40960\n"
    "ds_read_b64_tr_b16 v[146:147], v6 offset:41472\n"
    "ds_read_b64_tr_b16 v[148:149], v6 offset:40968\n"
    "ds_read_b64_tr_b16 v[150:151], v6 offset:41480\n"
    "ds_read_b64_tr_b16 v[152:153], v6 offset:24576\n"
    "ds_read_b64_tr_b16 v[154:155], v6 offset:25088\n"
    "ds_read_b64_tr_b16 v[156:157], v6 offset:24584\n"
    "ds_read_b64_tr_b16 v[158:159], v6 offset:25096\n"
    "s_waitcnt vmcnt(19) lgkmcnt(0)\n"
    "s_barrier\n"
    "ds_read_b64_tr_b16 v[160:161], v16 offset:8192\n"
    "ds_read_b64_tr_b16 v[162:163], v16 offset:8704\n"
    "ds_read_b64_tr_b16 v[164:165], v16 offset:8200\n"
    "ds_read_b64_tr_b16 v[166:167], v16 offset:8712\n"
    "ds_read_b64_tr_b16 v[168:169], v16 offset:16384\n"
    "ds_read_b64_tr_b16 v[170:171], v16 offset:16896\n"
    "ds_read_b64_tr_b16 v[172:173], v16 offset:16392\n"
    "ds_read_b64_tr_b16 v[174:175], v16 offset:16904\n"
    "ds_read_b64_tr_b16 v[176:177], v16 offset:0\n"
    "ds_read_b64_tr_b16 v[178:179], v16 offset:512\n"
    "ds_read_b64_tr_b16 v[180:181], v16 offset:8\n"
    "ds_read_b64_tr_b16 v[182:183], v16 offset:520\n"
    "s_waitcnt vmcnt(13) lgkmcnt(0)\n"
    "s_barrier\n"
    "ds_read_b64_tr_b16 v[184:185], v16 offset:32768\n"
    "ds_read_b64_tr_b16 v[186:187], v16 offset:33280\n"
    "ds_read_b64_tr_b16 v[188:189], v16 offset:32776\n"
    "ds_read_b64_tr_b16 v[190:191], v16 offset:33288\n"
    "ds_read_b64_tr_b16 v[192:193], v16 offset:40960\n"
    "ds_read_b64_tr_b16 v[194:195], v16 offset:41472\n"
    "ds_read_b64_tr_b16 v[196:197], v16 offset:40968\n"
    "ds_read_b64_tr_b16 v[198:199], v16 offset:41480\n"
    "ds_read_b64_tr_b16 v[200:201], v16 offset:24576\n"
    "ds_read_b64_tr_b16 v[202:203], v16 offset:25088\n"
    "ds_read_b64_tr_b16 v[204:205], v16 offset:24584\n"
    "ds_read_b64_tr_b16 v[206:207], v16 offset:25096\n"
    "s_waitcnt vmcnt(7) lgkmcnt(0)\n"
    "s_barrier\n"
    "ds_read_b64_tr_b16 v[208:209], v17 offset:8192\n"
    "ds_read_b64_tr_b16 v[210:211], v17 offset:8704\n"
    "ds_read_b64_tr_b16 v[212:213], v17 offset:8200\n"
    "ds_read_b64_tr_b16 v[214:215], v17 offset:8712\n"
    "ds_read_b64_tr_b16 v[216:217], v17 offset:16384\n"
    "ds_read_b64_tr_b16 v[218:219], v17 offset:16896\n"
    "ds_read_b64_tr_b16 v[220:221], v17 offset:16392\n"
    "ds_read_b64_tr_b16 v[222:223], v17 offset:16904\n"
    "ds_read_b64_tr_b16 v[224:225], v17 offset:0\n"
    "ds_read_b64_tr_b16 v[226:227], v17 offset:512\n"
    "ds_read_b64_tr_b16 v[228:229], v17 offset:8\n"
    "ds_read_b64_tr_b16 v[230:231], v17 offset:520\n"
    "s_waitcnt vmcnt(0) lgkmcnt(0)\n"
    "s_barrier\n"
    ".LgP_par_%=:\n"
    "v_readfirstlane_b32 s74, v12\n"
    "v_readfirstlane_b32 s75, v13\n"
    "s_xor_b32 s74, s74, s66\n"
    "s_xor_b32 s75, s75, s67\n"
    "s_cselect_b32 s75, 2, 0\n"
    "s_or_b32 s75, s75, s74\n"
    "s_cmp_lt_u32 s75, 2\n"
    "s_cbranch_scc1 .LgP_par1_%=\n"
    "s_sleep 4\n"
    "buffer_load_dwordx2 v[12:13], off, s[36:39], 0 sc1\n"
    "s_waitcnt vmcnt(0)\n"
    "s_sub_u32 s72, s72, 1\n"
    "s_cbranch_scc0 .LgP_par_%=\n"
    "s_mov_b32 s72, 0\n"
    ".LgP_par1_%=:\n"
    "s_and_b32 s74, s74, 1\n"
    "s_lshl_b32 s74, s74, 13\n"
    "s_add_u32 s36, s36, s74\n"
    "s_addc_u32 s37, s37, 0\n"
    "v_add_u32 v13, v11, v3\n"
    "v_cmp_gt_i32 vcc, s71, v13\n"
    "s_nop 0\n"
    "s_or_b64 exec, vcc, 1\n"
    "buffer_atomic_smax v11, v2, s[36:39], 0 offen sc0 sc1\n"
    "s_mov_b64 exec, -1\n"
    "ds_read_b64_tr_b16 v[232:233], v17 offset:32768\n"
    "ds_read_b64_tr_b16 v[234:235], v17 offset:33280\n"
    "ds_read_b64_tr_b16 v[236:237], v17 offset:32776\n"
    "ds_read_b64_tr_b16 v[238:239], v17 offset:33288\n"
    "ds_read_b64_tr_b16 v[240:241], v17 offset:40960\n"
    "ds_read_b64_tr_b16 v[242:243], v17 offset:41472\n"
    "ds_read_b64_tr_b16 v[244:245], v17 offset:40968\n"
    "ds_read_b64_tr_b16 v[246:247], v17 offset:41480\n"
    "ds_read_b64_tr_b16 v[248:249], v17 offset:24576\n"
    "ds_read_b64_tr_b16 v[250:251], v17 offset:25088\n"
    "ds_read_b64_tr_b16 v[252:253], v17 offset:24584\n"
    "ds_read_b64_tr_b16 v[254:255], v17 offset:25096\n"
    "s_waitcnt lgkmcnt(0)\n"
    "s_barrier\n"
    "s_waitcnt vmcnt(0)\n"
    "s_branch .LgP_block1_%=\n"
    ".LgP_LOOPB_%=:\n"
    ".LgP_head_%=:\n"
    "v_mfma_f32_16x16x32_bf16 v[48:51], v[16:19], a[8:11], 0\n"
    "s_waitcnt lgkmcnt(2)\n"
    "s_sub_u32 s74, s68, 1\n"
    "v_mfma_f32_16x16x32_bf16 v[56:59], v[16:19], a[12:15], 0\n"
    "ds_read_b128 v[16:19], v9 offset:0\n"
    "v_cvt_pk_bf16_f32 v40, v40, v41\n"
    "v_mfma_f32_16x16x32_bf16 v[52:55], v[20:23], a[8:11], 0\n"
    "s_waitcnt lgkmcnt(2)\n"
    "v_cvt_pk_bf16_f32 v41, v42, v43\n"
    "v_cvt_pk_bf16_f32 v42, v44, v45\n"
    "v_mfma_f32_16x16x32_bf16 v[60:63], v[20:23], a[12:15], 0\n"
    "ds_read_b128 v[20:23], v9 offset:2048\n"
    "v_cvt_pk_bf16_f32 v43, v46, v47\n"
    "v_mfma_f32_16x16x32_bf16 v[48:51], v[24:27], a[32:35], v[48:51]\n"
    "s_waitcnt lgkmcnt(2)\n"
    "v_cvt_pk_bf16_f32 v72, v72, v73\n"
    "v_cvt_pk_bf16_f32 v73, v74, v75\n"
    "v_mfma_f32_16x16x32_bf16 v[56:59], v[24:27], a[36:39], v[56:59]\n"
    "ds_read_b128 v[24:27], v9 offset:1024\n"
    "v_cvt_pk_bf16_f32 v74, v76, v77\n"
    "v_mfma_f32_16x16x32_bf16 v[52:55], v[28:31], a[32:35], v[52:55]\n"
    "s_waitcnt lgkmcnt(2)\n"
    "v_cvt_pk_bf16_f32 v75, v78, v79\n"
    "v_mfma_f32_16x16x32_bf16 v[60:63], v[28:31], a[36:39], v[60:63]\n"
    "ds_read_b128 v[28:31], v9 offset:3072\n"
    "v_mfma_f32_16x16x32_bf16 v[32:35], a[0:3], v[16:19], 0\n"
    "s_and_b32 s76, s71, s54\n"
    "buffer_store_dwordx4 v[40:43], v1, s[36:39], s79 offen offset:1024 sc1\n"
    "v_mfma_f32_16x16x32_bf16 v[36:39], a[4:7], v[16:19], 0\n"
    "s_mul_i32 s76, s76, s53\n"
    "buffer_store_dwordx4 v[72:75], v1, s[36:39], s73 offen offset:1024 sc1\n"
    "v_mfma_f32_16x16x32_bf16 v[64:67], a[16:19], v[16:19], 0\n"
    "s_waitcnt lgkmcnt(2)\n"
    "s_add_u32 s76, s76, s52\n"
    "v_mfma_f32_16x16x32_bf16 v[68:71], a[20:23], v[16:19], 0\n"
    "ds_read_b128 v[16:19], v9 offset:4096\n"
    "v_mfma_f32_16x16x32_bf16 v[32:35], a[24:27], v[20:23], v[32:35]\n"
    "s_and_b32 s77, s71, 1\n"
    "v_mfma_f32_16x16x32_bf16 v[36:39], a[28:31], v[20:23], v[36:39]\n"
    "s_mul_i32 s77, s77, 65536\n"
    "v_mfma_f32_16x16x32_bf16 v[64:67], a[40:43], v[20:23], v[64:67]\n"
    "s_waitcnt lgkmcnt(2)\n"
    "s_add_u32 s77, s77, s59\n"
    "v_mfma_f32_16x16x32_bf16 v[68:71], a[44:47], v[20:23], v[68:71]\n"
    "ds_read_b128 v[20:23], v9 offset:5120\n"
    "v_mfma_f32_16x16x32_bf16 v[40:43], a[0:3], v[24:27], 0\n"
    "v_mfma_f32_16x16x32_bf16 v[44:47], a[4:7], v[24:27], 0\n"
    "v_mfma_f32_16x16x32_bf16 v[72:75], a[16:19], v[24:27], 0\n"
    "s_waitcnt lgkmcnt(2)\n"
    "v_mfma_f32_16x16x32_bf16 v[76:79], a[20:23], v[24:27], 0\n"
    "ds_read_b128 v[24:27], v9 offset:6144\n"
    "v_mfma_f32_16x16x32_bf16 v[40:43], a[24:27], v[28:31], v[40:43]\n"
    "v_mfma_f32_16x16x32_bf16 v[44:47], a[28:31], v[28:31], v[44:47]\n"
    "v_mfma_f32_16x16x32_bf16 v[72:75], a[40:43], v[28:31], v[72:75]\n"
    "s_waitcnt lgkmcnt(2)\n"
    "v_mfma_f32_16x16x32_bf16 v[76:79], a[44:47], v[28:31], v[76:79]\n"
    "ds_read_b128 v[28:31], v9 offset:7168\n"
    "v_mfma_f32_16x16x32_bf16 v[32:35], a[48:51], v[16:19], v[32:35]\n"
    "v_mfma_f32_16x16x32_bf16 v[36:39], a[52:55], v[16:19], v[36:39]\n"
    "v_mfma_f32_16x16x32_bf16 v[48:51], v[16:19], a[56:59], v[48:51]\n"
    "v_mfma_f32_16x16x32_bf16 v[56:59], v[16:19], a[60:63], v[56:59]\n"
    "v_mfma_f32_16x16x32_bf16 v[64:67], a[64:67], v[16:19], v[64:67]\n"
    "s_waitcnt lgkmcnt(2)\n"
    "v_mfma_f32_16x16x32_bf16 v[68:71], a[68:71], v[16:19], v[68:71]\n"
    "ds_read_b128 v[16:19], v9 offset:8192\n"
    "v_mfma_f32_16x16x32_bf16 v[40:43], a[48:51], v[20:23], v[40:43]\n"
    "v_mfma_f32_16x16x32_bf16 v[44:47], a[52:55], v[20:23], v[44:47]\n"
    "v_mfma_f32_16x16x32_bf16 v[52:55], v[20:23], a[56:59], v[52:55]\n"
    "v_mfma_f32_16x16x32_bf16 v[60:63], v[20:23], a[60:63], v[60:63]\n"
    "v_mfma_f32_16x16x32_bf16 v[72:75], a[64:67], v[20:23], v[72:75]\n"
    "s_waitcnt lgkmcnt(2)\n"
    "v_mfma_f32_16x16x32_bf16 v[76:79], a[68:71], v[20:23], v[76:79]\n"
    "ds_read_b128 v[20:23], v9 offset:9216\n"
    "v_mfma_f32_16x16x32_bf16 v[32:35], a[72:75], v[24:27], v[32:35]\n"
    "v_mfma_f32_16x16x32_bf16 v[36:39], a[76:79], v[24:27], v[36:39]\n"
    "v_mfma_f32_16x16x32_bf16 v[48:51], v[24:27], a[80:83], v[48:51]\n"
    "v_mfma_f32_16x16x32_bf16 v[56:59], v[24:27], a[84:87], v[56:59]\n"
    "v_mfma_f32_16x16x32_bf16 v[64:67], a[88:91], v[24:27], v[64:67]\n"
    "s_waitcnt lgkmcnt(2)\n"
    "v_mfma_f32_16x16x32_bf16 v[68:71], a[92:95], v[24:27], v[68:71]\n"
    "ds_read_b128 v[24:27], v9 offset:10240\n"
    "v_mfma_f32_16x16x32_bf16 v[40:43], a[72:75], v[28:31], v[40:43]\n"
    "v_mfma_f32_16x16x32_bf16 v[44:47], a[76:79], v[28:31], v[44:47]\n"
    "v_mfma_f32_16x16x32_bf16 v[52:55], v[28:31], a[80:83], v[52:55]\n"
    "v_mfma_f32_16x16x32_bf16 v[60:63], v[28:31], a[84:87], v[60:63]\n"
    "v_mfma_f32_16x16x32_bf16 v[72:75], a[88:91], v[28:31], v[72:75]\n"
    "s_waitcnt lgkmcnt(2)\n"
    "v_mfma_f32_16x16x32_bf16 v[76:79], a[92:95], v[28:31], v[76:79]\n"
    "ds_read_b128 v[28:31], v9 offset:11264\n"
    "v_mfma_f32_16x16x32_bf16 v[32:35], a[96:99], v[16:19], v[32:35]\n"
    "v_mov_b32 v12, s74\n"
    "v_mfma_f32_16x16x32_bf16 v[36:39], a[100:103], v[16:19], v[36:39]\n"
    "s_waitcnt vmcnt(0)\n"
    "v_add_u32 v13, v11, v3\n"
    "v_mfma_f32_16x16x32_bf16 v[48:51], v[16:19], a[104:107], v[48:51]\n"
    "buffer_atomic_smax v12, v14, s[36:39], 0 offen sc1\n"
    "v_cmp_gt_i32 vcc, s71, v13\n"
    "v_mfma_f32_16x16x32_bf16 v[56:59], v[16:19], a[108:111], v[56:59]\n"
    "v_mfma_f32_16x16x32_bf16 v[64:67], a[112:115], v[16:19], v[64:67]\n"
    "s_waitcnt lgkmcnt(2)\n"
    "v_mfma_f32_16x16x32_bf16 v[68:71], a[116:119], v[16:19], v[68:71]\n"
    "ds_read_b128 v[16:19], v9 offset:12288\n"
    ".LgP_mid_%=:\n"
    "s_cbranch_vccnz .LgP_chk0_%=\n"
    "v_mfma_f32_16x16x32_bf16 v[40:43], a[96:99], v[20:23], v[40:43]\n"
    "s_add_u32 m0, s77, s59\n"
    "s_add_u32 s77, s77, 8192\n"
    "buffer_load_dwordx4 v1, s[36:39], s76 offen sc1 lds\n"
    "v_mfma_f32_16x16x32_bf16 v[44:47], a[100:103], v[20:23], v[44:47]\n"
    "s_mov_b32 s70, 1\n"
    "v_mfma_f32_16x16x32_bf16 v[52:55], v[20:23], a[104:107], v[52:55]\n"
    "s_and_b32 s73, s68, s57\n"
    "v_mfma_f32_16x16x32_bf16 v[60:63], v[20:23], a[108:111], v[60:63]\n"
    "buffer_load_dwordx4 v1, s[36:39], s76 offen offset:1024 sc1 lds\n"
    "s_add_u32 s76, s76, 16384\n"
    "v_mfma_f32_16x16x32_bf16 v[72:75], a[112:115], v[20:23], v[72:75]\n"
    "s_waitcnt lgkmcnt(2)\n"
    "s_mul_i32 s73, s73, s56\n"
    "v_mfma_f32_16x16x32_bf16 v[76:79], a[116:119], v[20:23], v[76:79]\n"
    "ds_read_b128 v[20:23], v9 offset:13312\n"
    "v_mfma_f32_16x16x32_bf16 v[32:35], a[120:123], v[24:27], v[32:35]\n"
    "s_add_u32 s73, s73, s55\n"
    "v_mfma_f32_16x16x32_bf16 v[36:39], a[124:127], v[24:27], v[36:39]\n"
    "s_add_u32 s79, s73, 16384\n"
    "v_mfma_f32_16x16x32_bf16 v[48:51], v[24:27], a[128:131], v[48:51]\n"
    "s_add_u32 m0, s77, s59\n"
    "s_add_u32 s77, s77, 8192\n"
    "buffer_load_dwordx4 v1, s[36:39], s76 offen sc1 lds\n"
    "v_mfma_f32_16x16x32_bf16 v[56:59], v[24:27], a[132:135], v[56:59]\n"
    "s_add_u32 s80, s73, 32768\n"
    "v_mfma_f32_16x16x32_bf16 v[64:67], a[136:139], v[24:27], v[64:67]\n"
    "s_waitcnt lgkmcnt(2)\n"
    "v_mfma_f32_16x16x32_bf16 v[68:71], a[140:143], v[24:27], v[68:71]\n"
    "ds_read_b128 v[24:27], v9 offset:14336\n"
    "v_mfma_f32_16x16x32_bf16 v[40:43], a[120:123], v[28:31], v[40:43]\n"
    "v_mfma_f32_16x16x32_bf16 v[44:47], a[124:127], v[28:31], v[44:47]\n"
    "buffer_load_dwordx4 v1, s[36:39], s76 offen offset:1024 sc1 lds\n"
    "s_add_u32 s76, s76, 16384\n"
    "v_mfma_f32_16x16x32_bf16 v[52:55], v[28:31], a[128:131], v[52:55]\n"
    "v_mfma_f32_16x16x32_bf16 v[60:63], v[28:31], a[132:135], v[60:63]\n"
    "v_mfma_f32_16x16x32_bf16 v[72:75], a[136:139], v[28:31], v[72:75]\n"
    "s_waitcnt lgkmcnt(2)\n"
    "v_mfma_f32_16x16x32_bf16 v[76:79], a[140:143], v[28:31], v[76:79]\n"
    "ds_read_b128 v[28:31], v9 offset:15360\n"
    "v_mfma_f32_16x16x32_bf16 v[32:35], a[144:147], v[16:19], v[32:35]\n"
    "s_add_u32 m0, s77, s59\n"
    "s_add_u32 s77, s77, 8192\n"
    "buffer_load_dwordx4 v1, s[36:39], s76 offen sc1 lds\n"
    "v_mfma_f32_16x16x32_bf16 v[36:39], a[148:151], v[16:19], v[36:39]\n"
    "v_mfma_f32_16x16x32_bf16 v[48:51], v[16:19], a[152:155], v[48:51]\n"
    "v_mfma_f32_16x16x32_bf16 v[56:59], v[16:19], a[156:159], v[56:59]\n"
    "v_mfma_f32_16x16x32_bf16 v[64:67], a[160:163], v[16:19], v[64:67]\n"
    "s_waitcnt lgkmcnt(2)\n"
    "buffer_load_dwordx4 v1, s[36:39], s76 offen offset:1024 sc1 lds\n"
    "s_add_u32 s76, s76, 16384\n"
    "v_mfma_f32_16x16x32_bf16 v[68:71], a[164:167], v[16:19], v[68:71]\n"
    "ds_read_b128 v[16:19], v9 offset:16384\n"
    "v_mfma_f32_16x16x32_bf16 v[40:43], a[144:147], v[20:23], v[40:43]\n"
    "v_mfma_f32_16x16x32_bf16 v[44:47], a[148:151], v[20:23], v[44:47]\n"
    "v_mfma_f32_16x16x32_bf16 v[52:55], v[20:23], a[152:155], v[52:55]\n"
    "v_mfma_f32_16x16x32_bf16 v[60:63], v[20:23], a[156:159], v[60:63]\n"
    "s_add_u32 m0, s77, s59\n"
    "s_add_u32 s77, s77, 8192\n"
    "buffer_load_dwordx4 v1, s[36:39], s76 offen sc1 lds\n"
    "v_mfma_f32_16x16x32_bf16 v[72:75], a[160:163], v[20:23], v[72:75]\n"
    "s_waitcnt lgkmcnt(2)\n"
    "v_mfma_f32_16x16x32_bf16 v[76:79], a[164:167], v[20:23], v[76:79]\n"
    "ds_read_b128 v[20:23], v9 offset:17408\n"
    "v_mfma_f32_16x16x32_bf16 v[32:35], a[168:171], v[24:27], v[32:35]\n"
    "v_mfma_f32_16x16x32_bf16 v[36:39], a[172:175], v[24:27], v[36:39]\n"
    "v_mfma_f32_16x16x32_bf16 v[48:51], v[24:27], a[176:179], v[48:51]\n"
    "buffer_load_dwordx4 v1, s[36:39], s76 offen offset:1024 sc1 lds\n"
    "s_add_u32 s76, s76, 16384\n"
    "v_mfma_f32_16x16x32_bf16 v[56:59], v[24:27], a[180:183], v[56:59]\n"
    "v_mfma_f32_16x16x32_bf16 v[64:67], a[184:187], v[24:27], v[64:67]\n"
    "s_waitcnt lgkmcnt(2)\n"
    "v_mfma_f32_16x16x32_bf16 v[68:71], a[188:191], v[24:27], v[68:71]\n"
    "ds_read_b128 v[24:27], v9 offset:18432\n"
    "v_mfma_f32_16x16x32_bf16 v[40:43], a[168:171], v[28:31], v[40:43]\n"
    "v_mfma_f32_16x16x32_bf16 v[44:47], a[172:175], v[28:31], v[44:47]\n"
    "s_mov_b32 m0, s77\n"
    "s_nop 0\n"
    "buffer_load_dwordx4 v8, s[36:39], s76 offen sc1 lds\n"
    "v_mfma_f32_16x16x32_bf16 v[52:55], v[28:31], a[176:179], v[52:55]\n"
    "v_mfma_f32_16x16x32_bf16 v[60:63], v[28:31], a[180:183], v[60:63]\n"
    "v_mfma_f32_16x16x32_bf16 v[72:75], a[184:187], v[28:31], v[72:75]\n"
    "s_waitcnt lgkmcnt(2)\n"
    "v_mfma_f32_16x16x32_bf16 v[76:79], a[188:191], v[28:31], v[76:79]\n"
    "ds_read_b128 v[28:31], v9 offset:19456\n"
    "v_mfma_f32_16x16x32_bf16 v[32:35], a[192:195], v[16:19], v[32:35]\n"
    ".LgP_seg0_%=:\n"
    "s_cmp_lg_u32 s70, 0\n"
    "s_cbranch_scc0 .LgP_chk1_%=\n"
    ".LgP_plain1_%=:\n"
    "v_mfma_f32_16x16x32_bf16 v[36:39], a[196:199], v[16:19], v[36:39]\n"
    "v_mfma_f32_16x16x32_bf16 v[48:51], v[16:19], a[200:203], v[48:51]\n"
    "v_mfma_f32_16x16x32_bf16 v[56:59], v[16:19], a[204:207], v[56:59]\n"
    "v_mfma_f32_16x16x32_bf16 v[64:67], a[208:211], v[16:19], v[64:67]\n"
    "s_waitcnt lgkmcnt(2)\n"
    "v_mfma_f32_16x16x32_bf16 v[68:71], a[212:215], v[16:19], v[68:71]\n"
    "ds_read_b128 v[16:19], v9 offset:20480\n"
    "v_mfma_f32_16x16x32_bf16 v[40:43], a[192:195], v[20:23], v[40:43]\n"
    "v_mfma_f32_16x16x32_bf16 v[44:47], a[196:199], v[20:23], v[44:47]\n"
    "v_mfma_f32_16x16x32_bf16 v[52:55], v[20:23], a[200:203], v[52:55]\n"
    "v_mfma_f32_16x16x32_bf16 v[60:63], v[20:23], a[204:207], v[60:63]\n"
    "v_mfma_f32_16x16x32_bf16 v[72:75], a[208:211], v[20:23], v[72:75]\n"
    "s_waitcnt lgkmcnt(2)\n"
    "v_mfma_f32_16x16x32_bf16 v[76:79], a[212:215], v[20:23], v[76:79]\n"
    "ds_read_b128 v[20:23], v9 offset:21504\n"
    "v_mfma_f32_16x16x32_bf16 v[32:35], a[216:219], v[24:27], v[32:35]\n"
    "v_mfma_f32_16x16x32_bf16 v[36:39], a[220:223], v[24:27], v[36:39]\n"
    "v_mfma_f32_16x16x32_bf16 v[48:51], v[24:27], a[224:227], v[48:51]\n"
    "v_mfma_f32_16x16x32_bf16 v[56:59], v[24:27], a[228:231], v[56:59]\n"
    "v_mfma_f32_16x16x32_bf16 v[64:67], a[232:235], v[24:27], v[64:67]\n"
    "s_waitcnt lgkmcnt(2)\n"
    "v_mfma_f32_16x16x32_bf16 v[68:71], a[236:239], v[24:27], v[68:71]\n"
    "ds_read_b128 v[24:27], v9 offset:22528\n"
    "v_mfma_f32_16x16x32_bf16 v[40:43], a[216:219], v[28:31], v[40:43]\n"
    "v_mfma_f32_16x16x32_bf16 v[44:47], a[220:223], v[28:31], v[44:47]\n"
    "v_mfma_f32_16x16x32_bf16 v[52:55], v[28:31], a[224:227], v[52:55]\n"
    "v_mfma_f32_16x16x32_bf16 v[60:63], v[28:31], a[228:231], v[60:63]\n"
    "v_mfma_f32_16x16x32_bf16 v[72:75], a[232:235], v[28:31], v[72:75]\n"
    "s_waitcnt lgkmcnt(2)\n"
    "v_mfma_f32_16x16x32_bf16 v[76:79], a[236:239], v[28:31], v[76:79]\n"
    "ds_read_b128 v[28:31], v9 offset:23552\n"
    "v_mfma_f32_16x16x32_bf16 v[32:35], a[240:243], v[16:19], v[32:35]\n"
    "v_mfma_f32_16x16x32_bf16 v[36:39], a[244:247], v[16:19], v[36:39]\n"
    "v_mfma_f32_16x16x32_bf16 v[48:51], v[16:19], a[248:251], v[48:51]\n"
    "v_mfma_f32_16x16x32_bf16 v[56:59], v[16:19], a[252:255], v[56:59]\n"
    "v_mfma_f32_16x16x32_bf16 v[64:67], v[80:83], v[16:19], v[64:67]\n"
    "s_waitcnt lgkmcnt(2)\n"
    "v_mfma_f32_16x16x32_bf16 v[68:71], v[84:87], v[16:19], v[68:71]\n"
    "ds_read_b128 v[16:19], v9 offset:24576\n"
    "v_mfma_f32_16x16x32_bf16 v[40:43], a[240:243], v[20:23], v[40:43]\n"
    "v_mfma_f32_16x16x32_bf16 v[44:47], a[244:247], v[20:23], v[44:47]\n"
    "v_mfma_f32_16x16x32_bf16 v[52:55], v[20:23], a[248:251], v[52:55]\n"
    "v_mfma_f32_16x16x32_bf16 v[60:63], v[20:23], a[252:255], v[60:63]\n"
    "v_mfma_f32_16x16x32_bf16 v[72:75], v[80:83], v[20:23], v[72:75]\n"
    "s_waitcnt lgkmcnt(2)\n"
    "v_mfma_f32_16x16x32_bf16 v[76:79], v[84:87], v[20:23], v[76:79]\n"
    "ds_read_b128 v[20:23], v9 offset:25600\n"
    "v_mfma_f32_16x16x32_bf16 v[32:35], v[88:91], v[24:27], v[32:35]\n"
    "v_mfma_f32_16x16x32_bf16 v[36:39], v[92:95], v[24:27], v[36:39]\n"
    "v_mfma_f32_16x16x32_bf16 v[48:51], v[24:27], v[96:99], v[48:51]\n"
    "v_mfma_f32_16x16x32_bf16 v[56:59], v[24:27], v[100:103], v[56:59]\n"
    "v_mfma_f32_16x16x32_bf16 v[64:67], v[104:107], v[24:27], v[64:67]\n"
    "s_waitcnt lgkmcnt(2)\n"
    "v_mfma_f32_16x16x32_bf16 v[68:71], v[108:111], v[24:27], v[68:71]\n"
    "ds_read_b128 v[24:27], v9 offset:26624\n"
    "v_mfma_f32_16x16x32_bf16 v[40:43], v[88:91], v[28:31], v[40:43]\n"
    "v_mfma_f32_16x16x32_bf16 v[44:47], v[92:95], v[28:31], v[44:47]\n"
    ".LgP_seg1_%=:\n"
    "s_cmp_lg_u32 s70, 0\n"
    "s_cbranch_scc0 .LgP_chk2_%=\n"
    ".LgP_plain2_%=:\n"
    "v_mfma_f32_16x16x32_bf16 v[52:55], v[28:31], v[96:99], v[52:55]\n"
    "v_mfma_f32_16x16x32_bf16 v[60:63], v[28:31], v[100:103], v[60:63]\n"
    "v_mfma_f32_16x16x32_bf16 v[72:75], v[104:107], v[28:31], v[72:75]\n"
    "s_waitcnt lgkmcnt(2)\n"
    "v_mfma_f32_16x16x32_bf16 v[76:79], v[108:111], v[28:31], v[76:79]\n"
    "ds_read_b128 v[28:31], v9 offset:27648\n"
    "v_mfma_f32_16x16x32_bf16 v[32:35], v[112:115], v[16:19], v[32:35]\n"
    "v_mfma_f32_16x16x32_bf16 v[36:39], v[116:119], v[16:19], v[36:39]\n"
    "v_mfma_f32_16x16x32_bf16 v[48:51], v[16:19], v[120:123], v[48:51]\n"
    "v_mfma_f32_16x16x32_bf16 v[56:59], v[16:19], v[124:127], v[56:59]\n"
    "v_mfma_f32_16x16x32_bf16 v[64:67], v[128:131], v[16:19], v[64:67]\n"
    "s_waitcnt lgkmcnt(2)\n"
    "v_mfma_f32_16x16x32_bf16 v[68:71], v[132:135], v[16:19], v[68:71]\n"
    "ds_read_b128 v[16:19], v9 offset:28672\n"
    "v_mfma_f32_16x16x32_bf16 v[40:43], v[112:115], v[20:23], v[40:43]\n"
    "v_mfma_f32_16x16x32_bf16 v[44:47], v[116:119], v[20:23], v[44:47]\n"
    "v_mfma_f32_16x16x32_bf16 v[52:55], v[20:23], v[120:123], v[52:55]\n"
    "v_mfma_f32_16x16x32_bf16 v[60:63], v[20:23], v[124:127], v[60:63]\n"
    "v_mfma_f32_16x16x32_bf16 v[72:75], v[128:131], v[20:23], v[72:75]\n"
    "s_waitcnt lgkmcnt(2)\n"
    "v_mfma_f32_16x16x32_bf16 v[76:79], v[132:135], v[20:23], v[76:79]\n"
    "ds_read_b128 v[20:23], v9 offset:29696\n"
    "v_mfma_f32_16x16x32_bf16 v[32:35], v[136:139], v[24:27], v[32:35]\n"
    "v_mfma_f32_16x16x32_bf16 v[36:39], v[140:143], v[24:27], v[36:39]\n"
    "v_mfma_f32_16x16x32_bf16 v[48:51], v[24:27], v[144:147], v[48:51]\n"
    "v_mfma_f32_16x16x32_bf16 v[56:59], v[24:27], v[148:151], v[56:59]\n"
    "v_mfma_f32_16x16x32_bf16 v[64:67], v[152:155], v[24:27], v[64:67]\n"
    "s_waitcnt lgkmcnt(2)\n"
    "v_mfma_f32_16x16x32_bf16 v[68:71], v[156:159], v[24:27], v[68:71]\n"
    "ds_read_b128 v[24:27], v9 offset:30720\n"
    "v_mfma_f32_16x16x32_bf16 v[40:43], v[136:139], v[28:31], v[40:43]\n"
    "v_mfma_f32_16x16x32_bf16 v[44:47], v[140:143], v[28:31], v[44:47]\n"
    "v_mfma_f32_16x16x32_bf16 v[52:55], v[28:31], v[144:147], v[52:55]\n"
    "v_mfma_f32_16x16x32_bf16 v[60:63], v[28:31], v[148:151], v[60:63]\n"
    "v_mfma_f32_16x16x32_bf16 v[72:75], v[152:155], v[28:31], v[72:75]\n"
    "s_waitcnt lgkmcnt(2)\n"
    "v_mfma_f32_16x16x32_bf16 v[76:79], v[156:159], v[28:31], v[76:79]\n"
    "ds_read_b128 v[28:31], v9 offset:31744\n"
    "v_mfma_f32_16x16x32_bf16 v[32:35], v[160:163], v[16:19], v[32:35]\n"
    "v_mfma_f32_16x16x32_bf16 v[36:39], v[164:167], v[16:19], v[36:39]\n"
    "v_mfma_f32_16x16x32_bf16 v[48:51], v[16:19], v[168:171], v[48:51]\n"
    "v_mfma_f32_16x16x32_bf16 v[56:59], v[16:19], v[172:175], v[56:59]\n"
    "v_mfma_f32_16x16x32_bf16 v[64:67], v[176:179], v[16:19], v[64:67]\n"
    "s_waitcnt lgkmcnt(2)\n"
    "v_mfma_f32_16x16x32_bf16 v[68:71], v[180:183], v[16:19], v[68:71]\n"
    "ds_read_b128 v[16:19], v9 offset:32768\n"
    "v_mfma_f32_16x16x32_bf16 v[40:43], v[160:163], v[20:23], v[40:43]\n"
    "v_mfma_f32_16x16x32_bf16 v[44:47], v[164:167], v[20:23], v[44:47]\n"
    "v_mfma_f32_16x16x32_bf16 v[52:55], v[20:23], v[168:171], v[52:55]\n"
    "v_mfma_f32_16x16x32_bf16 v[60:63], v[20:23], v[172:175], v[60:63]\n"
    "v_mfma_f32_16x16x32_bf16 v[72:75], v[176:179], v[20:23], v[72:75]\n"
    "s_waitcnt lgkmcnt(2)\n"
    "v_mfma_f32_16x16x32_bf16 v[76:79], v[180:183], v[20:23], v[76:79]\n"
    "ds_read_b128 v[20:23], v9 offset:33792\n"
    "v_mfma_f32_16x16x32_bf16 v[32:35], v[184:187], v[24:27], v[32:35]\n"
    "v_mfma_f32_16x16x32_bf16 v[36:39], v[188:191], v[24:27], v[36:39]\n"
    "v_mfma_f32_16x16x32_bf16 v[48:51], v[24:27], v[192:195], v[48:51]\n"
    "v_mfma_f32_16x16x32_bf16 v[56:59], v[24:27], v[196:199], v[56:59]\n"
    ".LgP_seg2_%=:\n"
    "s_cmp_lg_u32 s70, 1\n"
    "s_cbranch_scc1 .LgP_slow_%=\n"
    "v_mfma_f32_16x16x32_bf16 v[64:67], v[200:203], v[24:27], v[64:67]\n"
    "s_waitcnt lgkmcnt(2)\n"
    "s_mov_b32 s68, s71\n"
    "v_mfma_f32_16x16x32_bf16 v[68:71], v[204:207], v[24:27], v[68:71]\n"
    "ds_read_b128 v[24:27], v9 offset:34816\n"
    "v_mfma_f32_16x16x32_bf16 v[40:43], v[184:187], v[28:31], v[40:43]\n"
    "s_add_u32 s71, s68, 1\n"
    "v_mfma_f32_16x16x32_bf16 v[44:47], v[188:191], v[28:31], v[44:47]\n"
    "s_cmp_eq_u32 s71, 64\n"
    "v_mfma_f32_16x16x32_bf16 v[52:55], v[28:31], v[192:195], v[52:55]\n"
    "s_cselect_b32 s70, 2, 0\n"
    "v_mfma_f32_16x16x32_bf16 v[60:63], v[28:31], v[196:199], v[60:63]\n"
    "v_mfma_f32_16x16x32_bf16 v[72:75], v[200:203], v[28:31], v[72:75]\n"
    "s_waitcnt lgkmcnt(2)\n"
    "v_mfma_f32_16x16x32_bf16 v[76:79], v[204:207], v[28:31], v[76:79]\n"
    "ds_read_b128 v[28:31], v9 offset:35840\n"
    "v_mfma_f32_16x16x32_bf16 v[48:51], v[16:19], v[216:219], v[48:51]\n"
    "s_waitcnt lgkmcnt(2)\n"
    "v_mfma_f32_16x16x32_bf16 v[56:59], v[16:19], v[220:223], v[56:59]\n"
    "ds_read_b128 v[16:19], v9 offset:32768\n"
    "v_mfma_f32_16x16x32_bf16 v[52:55], v[20:23], v[216:219], v[52:55]\n"
    "s_waitcnt lgkmcnt(2)\n"
    "v_mfma_f32_16x16x32_bf16 v[60:63], v[20:23], v[220:223], v[60:63]\n"
    "ds_read_b128 v[20:23], v9 offset:34816\n"
    "v_mfma_f32_16x16x32_bf16 v[48:51], v[24:27], v[240:243], v[48:51]\n"
    "s_waitcnt lgkmcnt(2)\n"
    "v_mfma_f32_16x16x32_bf16 v[56:59], v[24:27], v[244:247], v[56:59]\n"
    "ds_read_b128 v[24:27], v9 offset:33792\n"
    "v_mfma_f32_16x16x32_bf16 v[52:55], v[28:31], v[240:243], v[52:55]\n"
    "s_waitcnt lgkmcnt(2)\n"
    "v_mfma_f32_16x16x32_bf16 v[60:63], v[28:31], v[244:247], v[60:63]\n"
    "ds_read_b128 v[28:31], v9 offset:35840\n"
    "v_add_u32 v13, v11, v3\n"
    "v_mfma_f32_16x16x32_bf16 v[32:35], v[208:211], v[16:19], v[32:35]\n"
    "v_cmp_gt_i32 vcc, s71, v13\n"
    "v_mfma_f32_16x16x32_bf16 v[36:39], v[212:215], v[16:19], v[36:39]\n"
    "v_cvt_pk_bf16_f32 v48, v48, v49\n"
    "v_cvt_pk_bf16_f32 v49, v50, v51\n"
    "v_mfma_f32_16x16x32_bf16 v[64:67], v[224:227], v[16:19], v[64:67]\n"
    "s_waitcnt lgkmcnt(2)\n"
    "v_cvt_pk_bf16_f32 v50, v52, v53\n"
    "v_cvt_pk_bf16_f32 v51, v54, v55\n"
    "v_xor_b32 v9, s78, v9\n"
    "v_mfma_f32_16x16x32_bf16 v[68:71], v[228:231], v[16:19], v[68:71]\n"
    "s_waitcnt vmcnt(0) lgkmcnt(0)\n"
    "s_barrier\n"
    "ds_read_b128 v[16:19], v9 offset:0\n"
    "v_cvt_pk_bf16_f32 v56, v56, v57\n"
    "v_mfma_f32_16x16x32_bf16 v[32:35], v[232:235], v[20:23], v[32:35]\n"
    "s_or_b64 exec, vcc, 1\n"
    "buffer_atomic_smax v11, v2, s[36:39], 0 offen sc0 sc1\n"
    "s_mov_b64 exec, -1\n"
    "v_cvt_pk_bf16_f32 v57, v58, v59\n"
    "v_cvt_pk_bf16_f32 v58, v60, v61\n"
    "v_mfma_f32_16x16x32_bf16 v[36:39], v[236:239], v[20:23], v[36:39]\n"
    "v_cvt_pk_bf16_f32 v59, v62, v63\n"
    "v_mfma_f32_16x16x32_bf16 v[64:67], v[248:251], v[20:23], v[64:67]\n"
    "buffer_store_dwordx4 v[48:51], v1, s[36:39], s80 offen sc1\n"
    "v_mfma_f32_16x16x32_bf16 v[68:71], v[252:255], v[20:23], v[68:71]\n"
    "ds_read_b128 v[20:23], v9 offset:1024\n"
    "v_mfma_f32_16x16x32_bf16 v[40:43], v[208:211], v[24:27], v[40:43]\n"
    "buffer_store_dwordx4 v[56:59], v1, s[36:39], s80 offen offset:1024 sc1\n"
    "v_mfma_f32_16x16x32_bf16 v[44:47], v[212:215], v[24:27], v[44:47]\n"
    "v_cvt_pk_bf16_f32 v32, v32, v33\n"
    "v_cvt_pk_bf16_f32 v33, v34, v35\n"
    "v_mfma_f32_16x16x32_bf16 v[72:75], v[224:227], v[24:27], v[72:75]\n"
    "v_cvt_pk_bf16_f32 v34, v36, v37\n"
    "v_cvt_pk_bf16_f32 v35, v38, v39\n"
    "v_mfma_f32_16x16x32_bf16 v[76:79], v[228:231], v[24:27], v[76:79]\n"
    "ds_read_b128 v[24:27], v9 offset:2048\n"
    "v_cvt_pk_bf16_f32 v64, v64, v65\n"
    "v_mfma_f32_16x16x32_bf16 v[40:43], v[232:235], v[28:31], v[40:43]\n"
    "v_cvt_pk_bf16_f32 v65, v66, v67\n"
    "v_cvt_pk_bf16_f32 v66, v68, v69\n"
    "v_mfma_f32_16x16x32_bf16 v[44:47], v[236:239], v[28:31], v[44:47]\n"
    "v_cvt_pk_bf16_f32 v67, v70, v71\n"
    "v_mfma_f32_16x16x32_bf16 v[72:75], v[248:251], v[28:31], v[72:75]\n"
    "s_waitcnt lgkmcnt(2)\n"
    "buffer_store_dwordx4 v[32:35], v1, s[36:39], s79 offen sc1\n"
    "v_mfma_f32_16x16x32_bf16 v[76:79], v[252:255], v[28:31], v[76:79]\n"
    "ds_read_b128 v[28:31], v9 offset:3072\n"
    "buffer_store_dwordx4 v[64:67], v1, s[36:39], s73 offen sc1\n"
    "s_branch .LgP_head_%=\n"
    ".LgP_LOOPE_%=:\n"
    ".LgP_done_%=:\n"
    "s_waitcnt vmcnt(0)\n"
    "s_mov_b32 s74, 63\n"
    "v_mov_b32 v12, s74\n"
    "s_mov_b64 exec, s[62:63]\n"
    "buffer_atomic_smax v12, v14, s[36:39], 0 offen sc1\n"
    "s_mov_b64 exec, -1\n"
    "s_waitcnt vmcnt(0)\n"
    "s_branch .LgP_FIN_%=\n"
    ".LgP_chk0_%=:\n"
    "s_or_b64 exec, vcc, 1\n"
    "buffer_atomic_smax v11, v2, s[36:39], 0 offen sc0 sc1\n"
    "s_mov_b64 exec, -1\n"
    "v_mfma_f32_16x16x32_bf16 v[40:43], a[96:99], v[20:23], v[40:43]\n"
    "s_and_b32 s73, s68, s57\n"
    "v_mfma_f32_16x16x32_bf16 v[44:47], a[100:103], v[20:23], v[44:47]\n"
    "s_mul_i32 s73, s73, s56\n"
    "v_mfma_f32_16x16x32_bf16 v[52:55], v[20:23], a[104:107], v[52:55]\n"
    "s_add_u32 s73, s73, s55\n"
    "v_mfma_f32_16x16x32_bf16 v[60:63], v[20:23], a[108:111], v[60:63]\n"
    "s_add_u32 s79, s73, 16384\n"
    "v_mfma_f32_16x16x32_bf16 v[72:75], a[112:115], v[20:23], v[72:75]\n"
    "s_waitcnt lgkmcnt(2)\n"
    "s_add_u32 s80, s73, 32768\n"
    "v_mfma_f32_16x16x32_bf16 v[76:79], a[116:119], v[20:23], v[76:79]\n"
    "ds_read_b128 v[20:23], v9 offset:13312\n"
    "v_mfma_f32_16x16x32_bf16 v[32:35], a[120:123], v[24:27], v[32:35]\n"
    "v_mfma_f32_16x16x32_bf16 v[36:39], a[124:127], v[24:27], v[36:39]\n"
    "v_mfma_f32_16x16x32_bf16 v[48:51], v[24:27], a[128:131], v[48:51]\n"
    "v_mfma_f32_16x16x32_bf16 v[56:59], v[24:27], a[132:135], v[56:59]\n"
    "v_mfma_f32_16x16x32_bf16 v[64:67], a[136:139], v[24:27], v[64:67]\n"
    "s_waitcnt lgkmcnt(2)\n"
    "v_mfma_f32_16x16x32_bf16 v[68:71], a[140:143], v[24:27], v[68:71]\n"
    "ds_read_b128 v[24:27], v9 offset:14336\n"
    "v_mfma_f32_16x16x32_bf16 v[40:43], a[120:123], v[28:31], v[40:43]\n"
    "v_mfma_f32_16x16x32_bf16 v[44:47], a[124:127], v[28:31], v[44:47]\n"
    "v_mfma_f32_16x16x32_bf16 v[52:55], v[28:31], a[128:131], v[52:55]\n"
    "v_mfma_f32_16x16x32_bf16 v[60:63], v[28:31], a[132:135], v[60:63]\n"
    "v_mfma_f32_16x16x32_bf16 v[72:75], a[136:139], v[28:31], v[72:75]\n"
    "s_waitcnt lgkmcnt(2)\n"
    "v_mfma_f32_16x16x32_bf16 v[76:79], a[140:143], v[28:31], v[76:79]\n"
    "ds_read_b128 v[28:31], v9 offset:15360\n"
    "v_mfma_f32_16x16x32_bf16 v[32:35], a[144:147], v[16:19], v[32:35]\n"
    "v_mfma_f32_16x16x32_bf16 v[36:39], a[148:151], v[16:19], v[36:39]\n"
    "v_mfma_f32_16x16x32_bf16 v[48:51], v[16:19], a[152:155], v[48:51]\n"
    "v_mfma_f32_16x16x32_bf16 v[56:59], v[16:19], a[156:159], v[56:59]\n"
    "v_mfma_f32_16x16x32_bf16 v[64:67], a[160:163], v[16:19], v[64:67]\n"
    "s_waitcnt lgkmcnt(2)\n"
    "v_mfma_f32_16x16x32_bf16 v[68:71], a[164:167], v[16:19], v[68:71]\n"
    "ds_read_b128 v[16:19], v9 offset:16384\n"
    "v_mfma_f32_16x16x32_bf16 v[40:43], a[144:147], v[20:23], v[40:43]\n"
    "v_mfma_f32_16x16x32_bf16 v[44:47], a[148:151], v[20:23], v[44:47]\n"
    "v_mfma_f32_16x16x32_bf16 v[52:55], v[20:23], a[152:155], v[52:55]\n"
    "v_mfma_f32_16x16x32_bf16 v[60:63], v[20:23], a[156:159], v[60:63]\n"
    "v_mfma_f32_16x16x32_bf16 v[72:75], a[160:163], v[20:23], v[72:75]\n"
    "s_waitcnt lgkmcnt(2)\n"
    "v_mfma_f32_16x16x32_bf16 v[76:79], a[164:167], v[20:23], v[76:79]\n"
    "ds_read_b128 v[20:23], v9 offset:17408\n"
    "v_mfma_f32_16x16x32_bf16 v[32:35], a[168:171], v[24:27], v[32:35]\n"
    "v_mfma_f32_16x16x32_bf16 v[36:39], a[172:175], v[24:27], v[36:39]\n"
    "v_mfma_f32_16x16x32_bf16 v[48:51], v[24:27], a[176:179], v[48:51]\n"
    "v_mfma_f32_16x16x32_bf16 v[56:59], v[24:27], a[180:183], v[56:59]\n"
    "v_mfma_f32_16x16x32_bf16 v[64:67], a[184:187], v[24:27], v[64:67]\n"
    "s_waitcnt lgkmcnt(2)\n"
    "v_mfma_f32_16x16x32_bf16 v[68:71], a[188:191], v[24:27], v[68:71]\n"
    "ds_read_b128 v[24:27], v9 offset:18432\n"
    "v_mfma_f32_16x16x32_bf16 v[40:43], a[168:171], v[28:31], v[40:43]\n"
    "v_mfma_f32_16x16x32_bf16 v[44:47], a[172:175], v[28:31], v[44:47]\n"
    "v_mfma_f32_16x16x32_bf16 v[52:55], v[28:31], a[176:179], v[52:55]\n"
    "v_mfma_f32_16x16x32_bf16 v[60:63], v[28:31], a[180:183], v[60:63]\n"
    "v_mfma_f32_16x16x32_bf16 v[72:75], a[184:187], v[28:31], v[72:75]\n"
    "s_waitcnt lgkmcnt(2)\n"
    "v_mfma_f32_16x16x32_bf16 v[76:79], a[188:191], v[28:31], v[76:79]\n"
    "ds_read_b128 v[28:31], v9 offset:19456\n"
    "v_mfma_f32_16x16x32_bf16 v[32:35], a[192:195], v[16:19], v[32:35]\n"
    "s_branch .LgP_seg0_%=\n"
    ".LgP_chk1_%=:\n"
    "s_waitcnt vmcnt(0)\n"
    "v_add_u32 v13, v11, v3\n"
    "v_cmp_gt_i32 vcc, s71, v13\n"
    "s_cbranch_vccz .LgP_dma1_%=\n"
    "s_or_b64 exec, vcc, 1\n"
    "buffer_atomic_smax v11, v2, s[36:39], 0 offen sc0 sc1\n"
    "s_mov_b64 exec, -1\n"
    "s_branch .LgP_plain1_%=\n"
    ".LgP_dma1_%=:\n"
    "s_mov_b32 s70, 1\n"
    "v_mfma_f32_16x16x32_bf16 v[36:39], a[196:199], v[16:19], v[36:39]\n"
    "s_add_u32 m0, s77, s59\n"
    "s_add_u32 s77, s77, 8192\n"
    "buffer_load_dwordx4 v1, s[36:39], s76 offen sc1 lds\n"
    "v_mfma_f32_16x16x32_bf16 v[48:51], v[16:19], a[200:203], v[48:51]\n"
    "v_mfma_f32_16x16x32_bf16 v[56:59], v[16:19], a[204:207], v[56:59]\n"
    "v_mfma_f32_16x16x32_bf16 v[64:67], a[208:211], v[16:19], v[64:67]\n"
    "s_waitcnt lgkmcnt(2)\n"
    "buffer_load_dwordx4 v1, s[36:39], s76 offen offset:1024 sc1 lds\n"
    "s_add_u32 s76, s76, 16384\n"
    "v_mfma_f32_16x16x32_bf16 v[68:71], a[212:215], v[16:19], v[68:71]\n"
    "ds_read_b128 v[16:19], v9 offset:20480\n"
    "v_mfma_f32_16x16x32_bf16 v[40:43], a[192:195], v[20:23], v[40:43]\n"
    "v_mfma_f32_16x16x32_bf16 v[44:47], a[196:199], v[20:23], v[44:47]\n"
    "v_mfma_f32_16x16x32_bf16 v[52:55], v[20:23], a[200:203], v[52:55]\n"
    "v_mfma_f32_16x16x32_bf16 v[60:63], v[20:23], a[204:207], v[60:63]\n"
    "s_add_u32 m0, s77, s59\n"
    "s_add_u32 s77, s77, 8192\n"
    "buffer_load_dwordx4 v1, s[36:39], s76 offen sc1 lds\n"
    "v_mfma_f32_16x16x32_bf16 v[72:75], a[208:211], v[20:23], v[72:75]\n"
    "s_waitcnt lgkmcnt(2)\n"
    "v_mfma_f32_16x16x32_bf16 v[76:79], a[212:215], v[20:23], v[76:79]\n"
    "ds_read_b128 v[20:23], v9 offset:21504\n"
    "v_mfma_f32_16x16x32_bf16 v[32:35], a[216:219], v[24:27], v[32:35]\n"
    "v_mfma_f32_16x16x32_bf16 v[36:39], a[220:223], v[24:27], v[36:39]\n"
    "v_mfma_f32_16x16x32_bf16 v[48:51], v[24:27], a[224:227], v[48:51]\n"
    "buffer_load_dwordx4 v1, s[36:39], s76 offen offset:1024 sc1 lds\n"
    "s_add_u32 s76, s76, 16384\n"
    "v_mfma_f32_16x16x32_bf16 v[56:59], v[24:27], a[228:231], v[56:59]\n"
    "v_mfma_f32_16x16x32_bf16 v[64:67], a[232:235], v[24:27], v[64:67]\n"
    "s_waitcnt lgkmcnt(2)\n"
    "v_mfma_f32_16x16x32_bf16 v[68:71], a[236:239], v[24:27], v[68:71]\n"
    "ds_read_b128 v[24:27], v9 offset:22528\n"
    "v_mfma_f32_16x16x32_bf16 v[40:43], a[216:219], v[28:31], v[40:43]\n"
    "v_mfma_f32_16x16x32_bf16 v[44:47], a[220:223], v[28:31], v[44:47]\n"
    "s_add_u32 m0, s77, s59\n"
    "s_add_u32 s77, s77, 8192\n"
    "buffer_load_dwordx4 v1, s[36:39], s76 offen sc1 lds\n"
    "v_mfma_f32_16x16x32_bf16 v[52:55], v[28:31], a[224:227], v[52:55]\n"
    "v_mfma_f32_16x16x32_bf16 v[60:63], v[28:31], a[228:231], v[60:63]\n"
    "v_mfma_f32_16x16x32_bf16 v[72:75], a[232:235], v[28:31], v[72:75]\n"
    "s_waitcnt lgkmcnt(2)\n"
    "v_mfma_f32_16x16x32_bf16 v[76:79], a[236:239], v[28:31], v[76:79]\n"
    "ds_read_b128 v[28:31], v9 offset:23552\n"
    "v_mfma_f32_16x16x32_bf16 v[32:35], a[240:243], v[16:19], v[32:35]\n"
    "buffer_load_dwordx4 v1, s[36:39], s76 offen offset:1024 sc1 lds\n"
    "s_add_u32 s76, s76, 16384\n"
    "v_mfma_f32_16x16x32_bf16 v[36:39], a[244:247], v[16:19], v[36:39]\n"
    "v_mfma_f32_16x16x32_bf16 v[48:51], v[16:19], a[248:251], v[48:51]\n"
    "v_mfma_f32_16x16x32_bf16 v[56:59], v[16:19], a[252:255], v[56:59]\n"
    "v_mfma_f32_16x16x32_bf16 v[64:67], v[80:83], v[16:19], v[64:67]\n"
    "s_waitcnt lgkmcnt(2)\n"
    "s_add_u32 m0, s77, s59\n"
    "s_add_u32 s77, s77, 8192\n"
    "buffer_load_dwordx4 v1, s[36:39], s76 offen sc1 lds\n"
    "v_mfma_f32_16x16x32_bf16 v[68:71], v[84:87], v[16:19], v[68:71]\n"
    "ds_read_b128 v[16:19], v9 offset:24576\n"
    "v_mfma_f32_16x16x32_bf16 v[40:43], a[240:243], v[20:23], v[40:43]\n"
    "v_mfma_f32_16x16x32_bf16 v[44:47], a[244:247], v[20:23], v[44:47]\n"
    "v_mfma_f32_16x16x32_bf16 v[52:55], v[20:23], a[248:251], v[52:55]\n"
    "v_mfma_f32_16x16x32_bf16 v[60:63], v[20:23], a[252:255], v[60:63]\n"
    "buffer_load_dwordx4 v1, s[36:39], s76 offen offset:1024 sc1 lds\n"
    "s_add_u32 s76, s76, 16384\n"
    "v_mfma_f32_16x16x32_bf16 v[72:75], v[80:83], v[20:23], v[72:75]\n"
    "s_waitcnt lgkmcnt(2)\n"
    "v_mfma_f32_16x16x32_bf16 v[76:79], v[84:87], v[20:23], v[76:79]\n"
    "ds_read_b128 v[20:23], v9 offset:25600\n"
    "v_mfma_f32_16x16x32_bf16 v[32:35], v[88:91], v[24:27], v[32:35]\n"
    "v_mfma_f32_16x16x32_bf16 v[36:39], v[92:95], v[24:27], v[36:39]\n"
    "v_mfma_f32_16x16x32_bf16 v[48:51], v[24:27], v[96:99], v[48:51]\n"
    "s_mov_b32 m0, s77\n"
    "s_nop 0\n"
    "buffer_load_dwordx4 v8, s[36:39], s76 offen sc1 lds\n"
    "v_mfma_f32_16x16x32_bf16 v[56:59], v[24:27], v[100:103], v[56:59]\n"
    "v_mfma_f32_16x16x32_bf16 v[64:67], v[104:107], v[24:27], v[64:67]\n"
    "s_waitcnt lgkmcnt(2)\n"
    "v_mfma_f32_16x16x32_bf16 v[68:71], v[108:111], v[24:27], v[68:71]\n"
    "ds_read_b128 v[24:27], v9 offset:26624\n"
    "v_mfma_f32_16x16x32_bf16 v[40:43], v[88:91], v[28:31], v[40:43]\n"
    "v_mfma_f32_16x16x32_bf16 v[44:47], v[92:95], v[28:31], v[44:47]\n"
    "s_branch .LgP_seg1_%=\n"
    ".LgP_chk2_%=:\n"
    "s_waitcnt vmcnt(0)\n"
    "v_add_u32 v13, v11, v3\n"
    "v_cmp_gt_i32 vcc, s71, v13\n"
    "s_cbranch_vccz .LgP_dma2_%=\n"
    "s_or_b64 exec, vcc, 1\n"
    "buffer_atomic_smax v11, v2, s[36:39], 0 offen sc0 sc1\n"
    "s_mov_b64 exec, -1\n"
    "s_branch .LgP_plain2_%=\n"
    ".LgP_dma2_%=:\n"
    "s_mov_b32 s70, 2\n"
    "v_mfma_f32_16x16x32_bf16 v[52:55], v[28:31], v[96:99], v[52:55]\n"
    "s_add_u32 m0, s77, s59\n"
    "s_add_u32 s77, s77, 8192\n"
    "buffer_load_dwordx4 v1, s[36:39], s76 offen sc1 lds\n"
    "v_mfma_f32_16x16x32_bf16 v[60:63], v[28:31], v[100:103], v[60:63]\n"
    "v_mfma_f32_16x16x32_bf16 v[72:75], v[104:107], v[28:31], v[72:75]\n"
    "s_waitcnt lgkmcnt(2)\n"
    "v_mfma_f32_16x16x32_bf16 v[76:79], v[108:111], v[28:31], v[76:79]\n"
    "ds_read_b128 v[28:31], v9 offset:27648\n"
    "v_mfma_f32_16x16x32_bf16 v[32:35], v[112:115], v[16:19], v[32:35]\n"
    "v_mfma_f32_16x16x32_bf16 v[36:39], v[116:119], v[16:19], v[36:39]\n"
    "buffer_load_dwordx4 v1, s[36:39], s76 offen offset:1024 sc1 lds\n"
    "s_add_u32 s76, s76, 16384\n"
    "v_mfma_f32_16x16x32_bf16 v[48:51], v[16:19], v[120:123], v[48:51]\n"
    "v_mfma_f32_16x16x32_bf16 v[56:59], v[16:19], v[124:127], v[56:59]\n"
    "v_mfma_f32_16x16x32_bf16 v[64:67], v[128:131], v[16:19], v[64:67]\n"
    "s_waitcnt lgkmcnt(2)\n"
    "v_mfma_f32_16x16x32_bf16 v[68:71], v[132:135], v[16:19], v[68:71]\n"
    "ds_read_b128 v[16:19], v9 offset:28672\n"
    "v_mfma_f32_16x16x32_bf16 v[40:43], v[112:115], v[20:23], v[40:43]\n"
    "s_add_u32 m0, s77, s59\n"
    "s_add_u32 s77, s77, 8192\n"
    "buffer_load_dwordx4 v1, s[36:39], s76 offen sc1 lds\n"
    "v_mfma_f32_16x16x32_bf16 v[44:47], v[116:119], v[20:23], v[44:47]\n"
    "v_mfma_f32_16x16x32_bf16 v[52:55], v[20:23], v[120:123], v[52:55]\n"
    "v_mfma_f32_16x16x32_bf16 v[60:63], v[20:23], v[124:127], v[60:63]\n"
    "v_mfma_f32_16x16x32_bf16 v[72:75], v[128:131], v[20:23], v[72:75]\n"
    "s_waitcnt lgkmcnt(2)\n"
    "buffer_load_dwordx4 v1, s[36:39], s76 offen offset:1024 sc1 lds\n"
    "s_add_u32 s76, s76, 16384\n"
    "v_mfma_f32_16x16x32_bf16 v[76:79], v[132:135], v[20:23], v[76:79]\n"
    "ds_read_b128 v[20:23], v9 offset:29696\n"
    "v_mfma_f32_16x16x32_bf16 v[32:35], v[136:139], v[24:27], v[32:35]\n"
    "v_mfma_f32_16x16x32_bf16 v[36:39], v[140:143], v[24:27], v[36:39]\n"
    "v_mfma_f32_16x16x32_bf16 v[48:51], v[24:27], v[144:147], v[48:51]\n"
    "v_mfma_f32_16x16x32_bf16 v[56:59], v[24:27], v[148:151], v[56:59]\n"
    "s_add_u32 m0, s77, s59\n"
    "s_add_u32 s77, s77, 8192\n"
    "buffer_load_dwordx4 v1, s[36:39], s76 offen sc1 lds\n"
    "v_mfma_f32_16x16x32_bf16 v[64:67], v[152:155], v[24:27], v[64:67]\n"
    "s_waitcnt lgkmcnt(2)\n"
    "v_mfma_f32_16x16x32_bf16 v[68:71], v[156:159], v[24:27], v[68:71]\n"
    "ds_read_b128 v[24:27], v9 offset:30720\n"
    "v_mfma_f32_16x16x32_bf16 v[40:43], v[136:139], v[28:31], v[40:43]\n"
    "v_mfma_f32_16x16x32_bf16 v[44:47], v[140:143], v[28:31], v[44:47]\n"
    "v_mfma_f32_16x16x32_bf16 v[52:55], v[28:31], v[144:147], v[52:55]\n"
    "buffer_load_dwordx4 v1, s[36:39], s76 offen offset:1024 sc1 lds\n"
    "s_add_u32 s76, s76, 16384\n"
    "v_mfma_f32_16x16x32_bf16 v[60:63], v[28:31], v[148:151], v[60:63]\n"
    "v_mfma_f32_16x16x32_bf16 v[72:75], v[152:155], v[28:31], v[72:75]\n"
    "s_waitcnt lgkmcnt(2)\n"
    "v_mfma_f32_16x16x32_bf16 v[76:79], v[156:159], v[28:31], v[76:79]\n"
    "ds_read_b128 v[28:31], v9 offset:31744\n"
    "v_mfma_f32_16x16x32_bf16 v[32:35], v[160:163], v[16:19], v[32:35]\n"
    "v_mfma_f32_16x16x32_bf16 v[36:39], v[164:167], v[16:19], v[36:39]\n"
    "s_add_u32 m0, s77, s59\n"
    "s_add_u32 s77, s77, 8192\n"
    "buffer_load_dwordx4 v1, s[36:39], s76 offen sc1 lds\n"
    "v_mfma_f32_16x16x32_bf16 v[48:51], v[16:19], v[168:171], v[48:51]\n"
    "v_mfma_f32_16x16x32_bf16 v[56:59], v[16:19], v[172:175], v[56:59]\n"
    "v_mfma_f32_16x16x32_bf16 v[64:67], v[176:179], v[16:19], v[64:67]\n"
    "s_waitcnt lgkmcnt(2)\n"
    "v_mfma_f32_16x16x32_bf16 v[68:71], v[180:183], v[16:19], v[68:71]\n"
    "ds_read_b128 v[16:19], v9 offset:32768\n"
    "v_mfma_f32_16x16x32_bf16 v[40:43], v[160:163], v[20:23], v[40:43]\n"
    "buffer_load_dwordx4 v1, s[36:39], s76 offen offset:1024 sc1 lds\n"
    "s_add_u32 s76, s76, 16384\n"
    "v_mfma_f32_16x16x32_bf16 v[44:47], v[164:167], v[20:23], v[44:47]\n"
    "v_mfma_f32_16x16x32_bf16 v[52:55], v[20:23], v[168:171], v[52:55]\n"
    "v_mfma_f32_16x16x32_bf16 v[60:63], v[20:23], v[172:175], v[60:63]\n"
    "v_mfma_f32_16x16x32_bf16 v[72:75], v[176:179], v[20:23], v[72:75]\n"
    "s_waitcnt lgkmcnt(2)\n"
    "s_mov_b32 m0, s77\n"
    "s_nop 0\n"
    "buffer_load_dwordx4 v8, s[36:39], s76 offen sc1 lds\n"
    "v_mfma_f32_16x16x32_bf16 v[76:79], v[180:183], v[20:23], v[76:79]\n"
    "ds_read_b128 v[20:23], v9 offset:33792\n"
    "v_mfma_f32_16x16x32_bf16 v[32:35], v[184:187], v[24:27], v[32:35]\n"
    "v_mfma_f32_16x16x32_bf16 v[36:39], v[188:191], v[24:27], v[36:39]\n"
    "v_mfma_f32_16x16x32_bf16 v[48:51], v[24:27], v[192:195], v[48:51]\n"
    "v_mfma_f32_16x16x32_bf16 v[56:59], v[24:27], v[196:199], v[56:59]\n"
    "s_branch .LgP_seg2_%=\n"
    ".LgP_slow_%=:\n"
    "s_cmp_lg_u32 s70, 0\n"
    "s_cbranch_scc1 .LgP_nolast_%=\n"
    "s_waitcnt vmcnt(0)\n"
    "v_add_u32 v13, v11, v3\n"
    "v_cmp_gt_i32 vcc, s71, v13\n"
    "s_nop 0\n"
    "s_or_b64 exec, vcc, 1\n"
    "buffer_atomic_smax v11, v2, s[36:39], 0 offen sc0 sc1\n"
    "s_mov_b64 exec, -1\n"
    ".LgP_nolast_%=:\n"
    "v_mfma_f32_16x16x32_bf16 v[64:67], v[200:203], v[24:27], v[64:67]\n"
    "s_waitcnt lgkmcnt(2)\n"
    "v_mfma_f32_16x16x32_bf16 v[68:71], v[204:207], v[24:27], v[68:71]\n"
    "ds_read_b128 v[24:27], v9 offset:34816\n"
    "v_mfma_f32_16x16x32_bf16 v[40:43], v[184:187], v[28:31], v[40:43]\n"
    "v_mfma_f32_16x16x32_bf16 v[44:47], v[188:191], v[28:31], v[44:47]\n"
    "v_mfma_f32_16x16x32_bf16 v[52:55], v[28:31], v[192:195], v[52:55]\n"
    "v_mfma_f32_16x16x32_bf16 v[60:63], v[28:31], v[196:199], v[60:63]\n"
    "v_mfma_f32_16x16x32_bf16 v[72:75], v[200:203], v[28:31], v[72:75]\n"
    "s_waitcnt lgkmcnt(2)\n"
    "v_mfma_f32_16x16x32_bf16 v[76:79], v[204:207], v[28:31], v[76:79]\n"
    "ds_read_b128 v[28:31], v9 offset:35840\n"
    "v_mfma_f32_16x16x32_bf16 v[48:51], v[16:19], v[216:219], v[48:51]\n"
    "s_waitcnt lgkmcnt(2)\n"
    "v_mfma_f32_16x16x32_bf16 v[56:59], v[16:19], v[220:223], v[56:59]\n"
    "ds_read_b128 v[16:19], v9 offset:32768\n"
    "v_mfma_f32_16x16x32_bf16 v[52:55], v[20:23], v[216:219], v[52:55]\n"
    "s_waitcnt lgkmcnt(2)\n"
    "v_mfma_f32_16x16x32_bf16 v[60:63], v[20:23], v[220:223], v[60:63]\n"
    "ds_read_b128 v[20:23], v9 offset:34816\n"
    "v_mfma_f32_16x16x32_bf16 v[48:51], v[24:27], v[240:243], v[48:51]\n"
    "s_waitcnt lgkmcnt(2)\n"
    "v_mfma_f32_16x16x32_bf16 v[56:59], v[24:27], v[244:247], v[56:59]\n"
    "ds_read_b128 v[24:27], v9 offset:33792\n"
    "v_mfma_f32_16x16x32_bf16 v[52:55], v[28:31], v[240:243], v[52:55]\n"
    "s_waitcnt lgkmcnt(2)\n"
    "v_mfma_f32_16x16x32_bf16 v[60:63], v[28:31], v[244:247], v[60:63]\n"
    "ds_read_b128 v[28:31], v9 offset:35840\n"
    "v_mfma_f32_16x16x32_bf16 v[32:35], v[208:211], v[16:19], v[32:35]\n"
    "v_mfma_f32_16x16x32_bf16 v[36:39], v[212:215], v[16:19], v[36:39]\n"
    "v_cvt_pk_bf16_f32 v48, v48, v49\n"
    "v_cvt_pk_bf16_f32 v49, v50, v51\n"
    "v_mfma_f32_16x16x32_bf16 v[64:67], v[224:227], v[16:19], v[64:67]\n"
    "s_waitcnt lgkmcnt(2)\n"
    "v_cvt_pk_bf16_f32 v50, v52, v53\n"
    "v_cvt_pk_bf16_f32 v51, v54, v55\n"
    "v_mfma_f32_16x16x32_bf16 v[68:71], v[228:231], v[16:19], v[68:71]\n"
    "v_cvt_pk_bf16_f32 v56, v56, v57\n"
    "v_mfma_f32_16x16x32_bf16 v[32:35], v[232:235], v[20:23], v[32:35]\n"
    "v_cvt_pk_bf16_f32 v57, v58, v59\n"
    "v_cvt_pk_bf16_f32 v58, v60, v61\n"
    "v_mfma_f32_16x16x32_bf16 v[36:39], v[236:239], v[20:23], v[36:39]\n"
    "v_cvt_pk_bf16_f32 v59, v62, v63\n"
    "v_mfma_f32_16x16x32_bf16 v[64:67], v[248:251], v[20:23], v[64:67]\n"
    "s_waitcnt lgkmcnt(1)\n"
    "buffer_store_dwordx4 v[48:51], v1, s[36:39], s80 offen sc1\n"
    "v_mfma_f32_16x16x32_bf16 v[68:71], v[252:255], v[20:23], v[68:71]\n"
    "v_mfma_f32_16x16x32_bf16 v[40:43], v[208:211], v[24:27], v[40:43]\n"
    "buffer_store_dwordx4 v[56:59], v1, s[36:39], s80 offen offset:1024 sc1\n"
    "v_mfma_f32_16x16x32_bf16 v[44:47], v[212:215], v[24:27], v[44:47]\n"
    "v_cvt_pk_bf16_f32 v32, v32, v33\n"
    "v_cvt_pk_bf16_f32 v33, v34, v35\n"
    "v_mfma_f32_16x16x32_bf16 v[72:75], v[224:227], v[24:27], v[72:75]\n"
    "s_waitcnt lgkmcnt(0)\n"
    "v_cvt_pk_bf16_f32 v34, v36, v37\n"
    "v_cvt_pk_bf16_f32 v35, v38, v39\n"
    "v_mfma_f32_16x16x32_bf16 v[76:79], v[228:231], v[24:27], v[76:79]\n"
    "v_cvt_pk_bf16_f32 v64, v64, v65\n"
    "v_mfma_f32_16x16x32_bf16 v[40:43], v[232:235], v[28:31], v[40:43]\n"
    "v_cvt_pk_bf16_f32 v65, v66, v67\n"
    "v_cvt_pk_bf16_f32 v66, v68, v69\n"
    "v_mfma_f32_16x16x32_bf16 v[44:47], v[236:239], v[28:31], v[44:47]\n"
    "v_cvt_pk_bf16_f32 v67, v70, v71\n"
    "v_mfma_f32_16x16x32_bf16 v[72:75], v[248:251], v[28:31], v[72:75]\n"
    "buffer_store_dwordx4 v[32:35], v1, s[36:39], s79 offen sc1\n"
    "v_mfma_f32_16x16x32_bf16 v[76:79], v[252:255], v[28:31], v[76:79]\n"
    "buffer_store_dwordx4 v[64:67], v1, s[36:39], s73 offen sc1\n"
    "v_cvt_pk_bf16_f32 v40, v40, v41\n"
    "v_cvt_pk_bf16_f32 v41, v42, v43\n"
    "v_cvt_pk_bf16_f32 v42, v44, v45\n"
    "v_cvt_pk_bf16_f32 v43, v46, v47\n"
    "buffer_store_dwordx4 v[40:43], v1, s[36:39], s79 offen offset:1024 sc1\n"
    "v_cvt_pk_bf16_f32 v72, v72, v73\n"
    "v_cvt_pk_bf16_f32 v73, v74, v75\n"
    "v_cvt_pk_bf16_f32 v74, v76, v77\n"
    "v_cvt_pk_bf16_f32 v75, v78, v79\n"
    "buffer_store_dwordx4 v[72:75], v1, s[36:39], s73 offen offset:1024 sc1\n"
    "s_mov_b32 s68, s71\n"
    "s_cmp_eq_u32 s70, 0\n"
    "s_cbranch_scc1 .LgP_idle_%=\n"
    "s_cmp_eq_u32 s68, 64\n"
    "s_cbranch_scc1 .LgP_done_%=\n"
    "s_waitcnt vmcnt(6)\n"
    ".LgP_bar_%=:\n"
    "v_xor_b32 v9, s78, v9\n"
    "s_add_u32 s71, s68, 1\n"
    "s_cmp_eq_u32 s71, 64\n"
    "s_cselect_b32 s70, 2, 0\n"
    "s_barrier\n"
    "ds_read_b128 v[16:19], v9 offset:0\n"
    "ds_read_b128 v[20:23], v9 offset:1024\n"
    "ds_read_b128 v[24:27], v9 offset:2048\n"
    "ds_read_b128 v[28:31], v9 offset:3072\n"
    "v_add_u32 v13, v11, v3\n"
    "v_cmp_gt_i32 vcc, s71, v13\n"
    "s_nop 0\n"
    "s_or_b64 exec, vcc, 1\n"
    "buffer_atomic_smax v11, v2, s[36:39], 0 offen sc0 sc1\n"
    "s_mov_b64 exec, -1\n"
    "s_waitcnt lgkmcnt(3)\n"
    "v_mfma_f32_16x16x32_bf16 v[48:51], v[16:19], a[8:11], 0\n"
    "s_waitcnt lgkmcnt(2)\n"
    "s_sub_u32 s74, s68, 1\n"
    "v_mfma_f32_16x16x32_bf16 v[56:59], v[16:19], a[12:15], 0\n"
    "ds_read_b128 v[16:19], v9 offset:0\n"
    "v_mfma_f32_16x16x32_bf16 v[52:55], v[20:23], a[8:11], 0\n"
    "s_waitcnt lgkmcnt(2)\n"
    "s_and_b32 s76, s71, s54\n"
    "v_mfma_f32_16x16x32_bf16 v[60:63], v[20:23], a[12:15], 0\n"
    "ds_read_b128 v[20:23], v9 offset:2048\n"
    "v_mfma_f32_16x16x32_bf16 v[48:51], v[24:27], a[32:35], v[48:51]\n"
    "s_waitcnt lgkmcnt(2)\n"
    "s_mul_i32 s76, s76, s53\n"
    "v_mfma_f32_16x16x32_bf16 v[56:59], v[24:27], a[36:39], v[56:59]\n"
    "ds_read_b128 v[24:27], v9 offset:1024\n"
    "v_mfma_f32_16x16x32_bf16 v[52:55], v[28:31], a[32:35], v[52:55]\n"
    "s_waitcnt lgkmcnt(2)\n"
    "s_add_u32 s76, s76, s52\n"
    "v_mfma_f32_16x16x32_bf16 v[60:63], v[28:31], a[36:39], v[60:63]\n"
    "ds_read_b128 v[28:31], v9 offset:3072\n"
    "v_mfma_f32_16x16x32_bf16 v[32:35], a[0:3], v[16:19], 0\n"
    "s_and_b32 s77, s71, 1\n"
    "v_mfma_f32_16x16x32_bf16 v[36:39], a[4:7], v[16:19], 0\n"
    "s_mul_i32 s77, s77, 65536\n"
    "v_mfma_f32_16x16x32_bf16 v[64:67], a[16:19], v[16:19], 0\n"
    "s_waitcnt lgkmcnt(2)\n"
    "s_add_u32 s77, s77, s59\n"
    "v_mfma_f32_16x16x32_bf16 v[68:71], a[20:23], v[16:19], 0\n"
    "ds_read_b128 v[16:19], v9 offset:4096\n"
    "v_mfma_f32_16x16x32_bf16 v[32:35], a[24:27], v[20:23], v[32:35]\n"
    "v_mfma_f32_16x16x32_bf16 v[36:39], a[28:31], v[20:23], v[36:39]\n"
    "v_mfma_f32_16x16x32_bf16 v[64:67], a[40:43], v[20:23], v[64:67]\n"
    "s_waitcnt lgkmcnt(2)\n"
    "v_mfma_f32_16x16x32_bf16 v[68:71], a[44:47], v[20:23], v[68:71]\n"
    "ds_read_b128 v[20:23], v9 offset:5120\n"
    "v_mfma_f32_16x16x32_bf16 v[40:43], a[0:3], v[24:27], 0\n"
    "v_mfma_f32_16x16x32_bf16 v[44:47], a[4:7], v[24:27], 0\n"
    "v_mfma_f32_16x16x32_bf16 v[72:75], a[16:19], v[24:27], 0\n"
    "s_waitcnt lgkmcnt(2)\n"
    "v_mfma_f32_16x16x32_bf16 v[76:79], a[20:23], v[24:27], 0\n"
    "ds_read_b128 v[24:27], v9 offset:6144\n"
    "v_mfma_f32_16x16x32_bf16 v[40:43], a[24:27], v[28:31], v[40:43]\n"
    "v_mfma_f32_16x16x32_bf16 v[44:47], a[28:31], v[28:31], v[44:47]\n"
    "v_mfma_f32_16x16x32_bf16 v[72:75], a[40:43], v[28:31], v[72:75]\n"
    "s_waitcnt lgkmcnt(2)\n"
    "v_mfma_f32_16x16x32_bf16 v[76:79], a[44:47], v[28:31], v[76:79]\n"
    "ds_read_b128 v[28:31], v9 offset:7168\n"
    "v_mfma_f32_16x16x32_bf16 v[32:35], a[48:51], v[16:19], v[32:35]\n"
    "v_mfma_f32_16x16x32_bf16 v[36:39], a[52:55], v[16:19], v[36:39]\n"
    "v_mfma_f32_16x16x32_bf16 v[48:51], v[16:19], a[56:59], v[48:51]\n"
    "v_mfma_f32_16x16x32_bf16 v[56:59], v[16:19], a[60:63], v[56:59]\n"
    "v_mfma_f32_16x16x32_bf16 v[64:67], a[64:67], v[16:19], v[64:67]\n"
    "s_waitcnt lgkmcnt(2)\n"
    "v_mfma_f32_16x16x32_bf16 v[68:71], a[68:71], v[16:19], v[68:71]\n"
    "ds_read_b128 v[16:19], v9 offset:8192\n"
    "v_mfma_f32_16x16x32_bf16 v[40:43], a[48:51], v[20:23], v[40:43]\n"
    "v_mfma_f32_16x16x32_bf16 v[44:47], a[52:55], v[20:23], v[44:47]\n"
    "v_mfma_f32_16x16x32_bf16 v[52:55], v[20:23], a[56:59], v[52:55]\n"
    "v_mfma_f32_16x16x32_bf16 v[60:63], v[20:23], a[60:63], v[60:63]\n"
    "v_mfma_f32_16x16x32_bf16 v[72:75], a[64:67], v[20:23], v[72:75]\n"
    "s_waitcnt lgkmcnt(2)\n"
    "v_mfma_f32_16x16x32_bf16 v[76:79], a[68:71], v[20:23], v[76:79]\n"
    "ds_read_b128 v[20:23], v9 offset:9216\n"
    "v_mfma_f32_16x16x32_bf16 v[32:35], a[72:75], v[24:27], v[32:35]\n"
    "v_mfma_f32_16x16x32_bf16 v[36:39], a[76:79], v[24:27], v[36:39]\n"
    "v_mfma_f32_16x16x32_bf16 v[48:51], v[24:27], a[80:83], v[48:51]\n"
    "v_mfma_f32_16x16x32_bf16 v[56:59], v[24:27], a[84:87], v[56:59]\n"
    "v_mfma_f32_16x16x32_bf16 v[64:67], a[88:91], v[24:27], v[64:67]\n"
    "s_waitcnt lgkmcnt(2)\n"
    "v_mfma_f32_16x16x32_bf16 v[68:71], a[92:95], v[24:27], v[68:71]\n"
    "ds_read_b128 v[24:27], v9 offset:10240\n"
    "v_mfma_f32_16x16x32_bf16 v[40:43], a[72:75], v[28:31], v[40:43]\n"
    "v_mfma_f32_16x16x32_bf16 v[44:47], a[76:79], v[28:31], v[44:47]\n"
    "v_mfma_f32_16x16x32_bf16 v[52:55], v[28:31], a[80:83], v[52:55]\n"
    "v_mfma_f32_16x16x32_bf16 v[60:63], v[28:31], a[84:87], v[60:63]\n"
    "v_mfma_f32_16x16x32_bf16 v[72:75], a[88:91], v[28:31], v[72:75]\n"
    "s_waitcnt lgkmcnt(2)\n"
    "v_mfma_f32_16x16x32_bf16 v[76:79], a[92:95], v[28:31], v[76:79]\n"
    "ds_read_b128 v[28:31], v9 offset:11264\n"
    "v_mfma_f32_16x16x32_bf16 v[32:35], a[96:99], v[16:19], v[32:35]\n"
    "v_mov_b32 v12, s74\n"
    "v_mfma_f32_16x16x32_bf16 v[36:39], a[100:103], v[16:19], v[36:39]\n"
    "s_waitcnt vmcnt(0)\n"
    "v_add_u32 v13, v11, v3\n"
    "v_mfma_f32_16x16x32_bf16 v[48:51], v[16:19], a[104:107], v[48:51]\n"
    "buffer_atomic_smax v12, v14, s[36:39], 0 offen sc1\n"
    "v_cmp_gt_i32 vcc, s71, v13\n"
    "v_mfma_f32_16x16x32_bf16 v[56:59], v[16:19], a[108:111], v[56:59]\n"
    "v_mfma_f32_16x16x32_bf16 v[64:67], a[112:115], v[16:19], v[64:67]\n"
    "s_waitcnt lgkmcnt(2)\n"
    "v_mfma_f32_16x16x32_bf16 v[68:71], a[116:119], v[16:19], v[68:71]\n"
    "ds_read_b128 v[16:19], v9 offset:12288\n"
    "s_branch .LgP_mid_%=\n"
    ".LgP_idle_%=:\n"
    "s_waitcnt vmcnt(6)\n"
    "v_add_u32 v13, v11, v3\n"
    "v_cmp_gt_i32 vcc, s71, v13\n"
    "s_cbranch_vccnz .LgP_idle2_%=\n"
    "s_and_b32 s76, s71, s54\n"
    "s_mul_i32 s76, s76, s53\n"
    "s_add_u32 s76, s76, s52\n"
    "s_and_b32 s77, s71, 1\n"
    "s_mul_i32 s77, s77, 65536\n"
    "s_add_u32 s77, s77, s59\n"
    "s_add_u32 m0, s77, s59\n"
    "s_add_u32 s77, s77, 8192\n"
    "buffer_load_dwordx4 v1, s[36:39], s76 offen sc1 lds\n"
    "buffer_load_dwordx4 v1, s[36:39], s76 offen offset:1024 sc1 lds\n"
    "s_add_u32 s76, s76, 16384\n"
    "s_add_u32 m0, s77, s59\n"
    "s_add_u32 s77, s77, 8192\n"
    "buffer_load_dwordx4 v1, s[36:39], s76 offen sc1 lds\n"
    "buffer_load_dwordx4 v1, s[36:39], s76 offen offset:1024 sc1 lds\n"
    "s_add_u32 s76, s76, 16384\n"
    "s_add_u32 m0, s77, s59\n"
    "s_add_u32 s77, s77, 8192\n"
    "buffer_load_dwordx4 v1, s[36:39], s76 offen sc1 lds\n"
    "buffer_load_dwordx4 v1, s[36:39], s76 offen offset:1024 sc1 lds\n"
    "s_add_u32 s76, s76, 16384\n"
    "s_add_u32 m0, s77, s59\n"
    "s_add_u32 s77, s77, 8192\n"
    "buffer_load_dwordx4 v1, s[36:39], s76 offen sc1 lds\n"
    "buffer_load_dwordx4 v1, s[36:39], s76 offen offset:1024 sc1 lds\n"
    "s_add_u32 s76, s76, 16384\n"
    "s_mov_b32 m0, s77\n"
    "s_nop 0\n"
    "buffer_load_dwordx4 v8, s[36:39], s76 offen sc1 lds\n"
    "s_waitcnt vmcnt(9)\n"
    "s_sub_u32 s74, s68, 1\n"
    "v_mov_b32 v12, s74\n"
    "s_mov_b64 exec, s[62:63]\n"
    "buffer_atomic_smax v12, v14, s[36:39], 0 offen sc1\n"
    "s_mov_b64 exec, -1\n"
    "s_waitcnt vmcnt(1)\n"
    "s_branch .LgP_bar_%=\n"
    ".LgP_idle2_%=:\n"
    "s_or_b64 exec, vcc, 1\n"
    "buffer_atomic_smax v11, v2, s[36:39], 0 offen sc0 sc1\n"
    "s_mov_b64 exec, -1\n"
    "s_waitcnt vmcnt(1)\n"
    "s_sub_u32 s74, s68, 1\n"
    "v_mov_b32 v12, s74\n"
    "s_mov_b64 exec, s[62:63]\n"
    "buffer_atomic_smax v12, v14, s[36:39], 0 offen sc1\n"
    "s_mov_b64 exec, -1\n"
    "s_waitcnt vmcnt(1)\n"
    "s_branch .LgP_block1_%=\n"
    ".LgP_block_%=:\n"
    "v_add_u32 v13, v11, v3\n"
    "v_cmp_gt_i32 vcc, s71, v13\n"
    "s_nop 0\n"
    "s_or_b64 exec, vcc, 1\n"
    "buffer_atomic_smax v11, v2, s[36:39], 0 offen sc0 sc1\n"
    "s_mov_b64 exec, -1\n"
    "s_waitcnt vmcnt(0)\n"
    ".LgP_block1_%=:\n"
    "v_add_u32 v13, v11, v3\n"
    "v_cmp_gt_i32 vcc, s71, v13\n"
    "s_cbranch_vccz .LgP_go_%=\n"
    "s_sleep 4\n"
    "s_sub_u32 s72, s72, 1\n"
    "s_cbranch_scc0 .LgP_block_%=\n"
    "s_mov_b32 s72, 0\n"
    ".LgP_go_%=:\n"
    "s_and_b32 s76, s71, s54\n"
    "s_mul_i32 s76, s76, s53\n"
    "s_add_u32 s76, s76, s52\n"
    "s_and_b32 s77, s71, 1\n"
    "s_mul_i32 s77, s77, 65536\n"
    "s_add_u32 s77, s77, s59\n"
    "s_add_u32 m0, s77, s59\n"
    "s_add_u32 s77, s77, 8192\n"
    "buffer_load_dwordx4 v1, s[36:39], s76 offen sc1 lds\n"
    "buffer_load_dwordx4 v1, s[36:39], s76 offen offset:1024 sc1 lds\n"
    "s_add_u32 s76, s76, 16384\n"
    "s_add_u32 m0, s77, s59\n"
    "s_add_u32 s77, s77, 8192\n"
    "buffer_load_dwordx4 v1, s[36:39], s76 offen sc1 lds\n"
    "buffer_load_dwordx4 v1, s[36:39], s76 offen offset:1024 sc1 lds\n"
    "s_add_u32 s76, s76, 16384\n"
    "s_add_u32 m0, s77, s59\n"
    "s_add_u32 s77, s77, 8192\n"
    "buffer_load_dwordx4 v1, s[36:39], s76 offen sc1 lds\n"
    "buffer_load_dwordx4 v1, s[36:39], s76 offen offset:1024 sc1 lds\n"
    "s_add_u32 s76, s76, 16384\n"
    "s_add_u32 m0, s77, s59\n"
    "s_add_u32 s77, s77, 8192\n"
    "buffer_load_dwordx4 v1, s[36:39], s76 offen sc1 lds\n"
    "buffer_load_dwordx4 v1, s[36:39], s76 offen offset:1024 sc1 lds\n"
    "s_add_u32 s76, s76, 16384\n"
    "s_mov_b32 m0, s77\n"
    "s_nop 0\n"
    "buffer_load_dwordx4 v8, s[36:39], s76 offen sc1 lds\n"
    "s_waitcnt vmcnt(0)\n"
    "s_branch .LgP_bar_%=\n"
    ".LgP_FIN_%=:\n"
  );
}
DEVI void role_up(const Params& p, int L, int j, int i, u32 dbg, unsigned long long tag) {
  const int lane = threadIdx.x & 63, w = threadIdx.x >> 6, col = (6 * j + i) * 192;
  const u32 wb = 576u * 4608 * 2 - col * 2;
  GEMM_DECL;
  g_w0 = make_srd(p.w1 + col, wb); g_w1 = make_srd(p.w3 + col, wb); g_w2 = g_w1;
  g_ldw = 9216;
  g_in_base = pgb(L, B_XN2); g_in_stride = SLOT; g_in_mask = RX - 1;
  g_out_base = pgb(L, B_H + 9 * j); g_out_kb = 12 * i; g_out_mask = RH - 1;
  g_flag = lane < 3 ? mbox(L, j, SL_DOWN + lane, 4 * i + w) : OOB;
  g_paddr = mbox(L, j, SL_UP + i, lane);
  g_pbias = lane < 16 ? 0 : (lane < 19 ? RH : 1000);
  v16u sa, sb, va;
  gemm_args(GEMM_ARGS, p, dbg, tag, sa, sb, va);
  RUN_ASM(
    "s_nop 4\n"
    "v_add_u32 v8, s59, v0\n"
    "s_mov_b32 s68, 0\n"
    "s_mov_b32 s71, 0\n"
    "s_mov_b32 s78, 65536\n"
    "v_add_u32 v9, s78, v0\n"
    "s_mov_b32 s64, 0xbfb8aa3b\n"
    "s_mov_b32 s72, 262144\n"
    "s_add_u32 m0, s59, 0\n"
    "s_mov_b32 s74, 0\n"
    "buffer_load_dwordx4 v5, s[40:43], s74 offen lds\n"
    "s_add_u32 m0, s59, 4096\n"
    "s_mov_b32 s74, 128\n"
    "buffer_load_dwordx4 v5, s[40:43], s74 offen lds\n"
    "s_add_u32 m0, s59, 8192\n"
    "s_mov_b32 s74, 256\n"
    "buffer_load_dwordx4 v5, s[40:43], s74 offen lds\n"
    "s_add_u32 m0, s59, 12288\n"
    "s_mov_b32 s74, 0\n"
    "buffer_load_dwordx4 v5, s[44:47], s74 offen lds\n"
    "s_add_u32 m0, s59, 16384\n"
    "s_mov_b32 s74, 128\n"
    "buffer_load_dwordx4 v5, s[44:47], s74 offen lds\n"
    "s_add_u32 m0, s59, 20480\n"
    "s_mov_b32 s74, 256\n"
    "buffer_load_dwordx4 v5, s[44:47], s74 offen lds\n"
    "s_add_u32 m0, s59, 24576\n"
    "s_mov_b32 s74, 294912\n"
    "buffer_load_dwordx4 v5, s[40:43], s74 offen lds\n"
    "s_add_u32 m0, s59, 28672\n"
    "s_mov_b32 s74, 295040\n"
    "buffer_load_dwordx4 v5, s[40:43], s74 offen lds\n"
    "s_add_u32 m0, s59, 32768\n"
    "s_mov_b32 s74, 295168\n"
    "buffer_load_dwordx4 v5, s[40:43], s74 offen lds\n"
    "s_add_u32 m0, s59, 36864\n"
    "s_mov_b32 s74, 294912\n"
    "buffer_load_dwordx4 v5, s[44:47], s74 offen lds\n"
    "s_add_u32 m0, s59, 40960\n"
    "s_mov_b32 s74, 295040\n"
    "buffer_load_dwordx4 v5, s[44:47], s74 offen lds\n"
    "s_add_u32 m0, s59, 45056\n"
    "s_mov_b32 s74, 295168\n"
    "buffer_load_dwordx4 v5, s[44:47], s74 offen lds\n"
    "s_add_u32 m0, s59, 49152\n"
    "s_mov_b32 s74, 589824\n"
    "buffer_load_dwordx4 v5, s[40:43], s74 offen lds\n"
    "s_add_u32 m0, s59, 53248\n"
    "s_mov_b32 s74, 589952\n"
    "buffer_load_dwordx4 v5, s[40:43], s74 offen lds\n"
    "s_add_u32 m0, s59, 57344\n"
    "s_mov_b32 s74, 590080\n"
    "buffer_load_dwordx4 v5, s[40:43], s74 offen lds\n"
    "s_add_u32 m0, s59, 61440\n"
    "s_mov_b32 s74, 589824\n"
    "buffer_load_dwordx4 v5, s[44:47], s74 offen lds\n"
    "s_add_u32 m0, s59, 65536\n"
    "s_mov_b32 s74, 589952\n"
    "buffer_load_dwordx4 v5, s[44:47], s74 offen lds\n"
    "s_add_u32 m0, s59, 69632\n"
    "s_mov_b32 s74, 590080\n"
    "buffer_load_dwordx4 v5, s[44:47], s74 offen lds\n"
    "s_add_u32 m0, s59, 73728\n"
    "s_mov_b32 s74, 884736\n"
    "buffer_load_dwordx4 v5, s[40:43], s74 offen lds\n"
    "s_add_u32 m0, s59, 77824\n"
    "s_mov_b32 s74, 884864\n"
    "buffer_load_dwordx4 v5, s[40:43], s74 offen lds\n"
    "s_add_u32 m0, s59, 81920\n"
    "s_mov_b32 s74, 884992\n"
    "buffer_load_dwordx4 v5, s[40:43], s74 offen lds\n"
    "s_add_u32 m0, s59, 86016\n"
    "s_mov_b32 s74, 884736\n"
    "buffer_load_dwordx4 v5, s[44:47], s74 offen lds\n"
    "s_add_u32 m0, s59, 90112\n"
    "s_mov_b32 s74, 884864\n"
    "buffer_load_dwordx4 v5, s[44:47], s74 offen lds\n"
    "s_add_u32 m0, s59, 94208\n"
    "s_mov_b32 s74, 884992\n"
    "buffer_load_dwordx4 v5, s[44:47], s74 offen lds\n"
    "s_add_u32 m0, s59, 98304\n"
    "s_mov_b32 s74, 1179648\n"
    "buffer_load_dwordx4 v5, s[40:43], s74 offen lds\n"
    "s_add_u32 m0, s59, 102400\n"
    "s_mov_b32 s74, 1179776\n"
    "buffer_load_dwordx4 v5, s[40:43], s74 offen lds\n"
    "s_add_u32 m0, s59, 106496\n"
    "s_mov_b32 s74, 1179904\n"
    "buffer_load_dwordx4 v5, s[40:43], s74 offen lds\n"
    "s_add_u32 m0, s59, 110592\n"
    "s_mov_b32 s74, 1179648\n"
    "buffer_load_dwordx4 v5, s[44:47], s74 offen lds\n"
    "s_add_u32 m0, s59, 114688\n"
    "s_mov_b32 s74, 1179776\n"
    "buffer_load_dwordx4 v5, s[44:47], s74 offen lds\n"
    "s_add_u32 m0, s59, 118784\n"
    "s_mov_b32 s74, 1179904\n"
    "buffer_load_dwordx4 v5, s[44:47], s74 offen lds\n"
    "v_add_u32 v16, 49152, v6\n"
    "v_add_u32 v17, 98304, v6\n"
    "v_add_u32 v18, 49152, v7\n"
    "v_add_u32 v19, 98304, v7\n"
    "s_waitcnt vmcnt(24) lgkmcnt(0)\n"
    "s_barrier\n"
    "s_add_u32 m0, s59, 122880\n"
    "s_mov_b32 s74, 1474560\n"
    "buffer_load_dwordx4 v5, s[40:43], s74 offen lds\n"
    "s_add_u32 m0, s59, 126976\n"
    "s_mov_b32 s74, 1474688\n"
    "buffer_load_dwordx4 v5, s[40:43], s74 offen lds\n"
    "s_add_u32 m0, s59, 131072\n"
    "s_mov_b32 s74, 1474816\n"
    "buffer_load_dwordx4 v5, s[40:43], s74 offen lds\n"
    "s_add_u32 m0, s59, 135168\n"
    "s_mov_b32 s74, 1474560\n"
    "buffer_load_dwordx4 v5, s[44:47], s74 offen lds\n"
    "s_add_u32 m0, s59, 139264\n"
    "s_mov_b32 s74, 1474688\n"
    "buffer_load_dwordx4 v5, s[44:47], s74 offen lds\n"
    "s_add_u32 m0, s59, 143360\n"
    "s_mov_b32 s74, 1474816\n"
    "buffer_load_dwordx4 v5, s[44:47], s74 offen lds\n"
    "ds_read_b64_tr_b16 a[0:1], v6 offset:0\n"
    "ds_read_b64_tr_b16 a[2:3], v6 offset:512\n"
    "ds_read_b64_tr_b16 a[4:5], v6 offset:8\n"
    "ds_read_b64_tr_b16 a[6:7], v6 offset:520\n"
    "ds_read_b64_tr_b16 a[8:9], v7 offset:0\n"
    "ds_read_b64_tr_b16 a[10:11], v7 offset:512\n"
    "ds_read_b64_tr_b16 a[12:13], v6 offset:12288\n"
    "ds_read_b64_tr_b16 a[14:15], v6 offset:12800\n"
    "ds_read_b64_tr_b16 a[16:17], v6 offset:12296\n"
    "ds_read_b64_tr_b16 a[18:19], v6 offset:12808\n"
    "ds_read_b64_tr_b16 a[20:21], v7 offset:12288\n"
    "ds_read_b64_tr_b16 a[22:23], v7 offset:12800\n"
    "s_waitcnt vmcnt(24) lgkmcnt(0)\n"
    "s_barrier\n"
    "s_add_u32 m0, s59, 0\n"
    "s_mov_b32 s74, 1769472\n"
    "buffer_load_dwordx4 v5, s[40:43], s74 offen lds\n"
    "s_add_u32 m0, s59, 4096\n"
    "s_mov_b32 s74, 1769600\n"
    "buffer_load_dwordx4 v5, s[40:43], s74 offen lds\n"
    "s_add_u32 m0, s59, 8192\n"
    "s_mov_b32 s74, 1769728\n"
    "buffer_load_dwordx4 v5, s[40:43], s74 offen lds\n"
    "s_add_u32 m0, s59, 12288\n"
    "s_mov_b32 s74, 1769472\n"
    "buffer_load_dwordx4 v5, s[44:47], s74 offen lds\n"
    "s_add_u32 m0, s59, 16384\n"
    "s_mov_b32 s74, 1769600\n"
    "buffer_load_dwordx4 v5, s[44:47], s74 offen lds\n"
    "s_add_u32 m0, s59, 20480\n"
    "s_mov_b32 s74, 1769728\n"
    "buffer_load_dwordx4 v5, s[44:47], s74 offen lds\n"
    "ds_read_b64_tr_b16 a[24:25], v6 offset:24576\n"
    "ds_read_b64_tr_b16 a[26:27], v6 offset:25088\n"
    "ds_read_b64_tr_b16 a[28:29], v6 offset:24584\n"
    "ds_read_b64_tr_b16 a[30:31], v6 offset:25096\n"
    "ds_read_b64_tr_b16 a[32:33], v7 offset:24576\n"
    "ds_read_b64_tr_b16 a[34:35], v7 offset:25088\n"
    "ds_read_b64_tr_b16 a[36:37], v6 offset:36864\n"
    "ds_read_b64_tr_b16 a[38:39], v6 offset:37376\n"
    "ds_read_b64_tr_b16 a[40:41], v6 offset:36872\n"
    "ds_read_b64_tr_b16 a[42:43], v6 offset:37384\n"
    "ds_read_b64_tr_b16 a[44:45], v7 offset:36864\n"
    "ds_read_b64_tr_b16 a[46:47], v7 offset:37376\n"
    "s_waitcnt vmcnt(24) lgkmcnt(0)\n"
    "s_barrier\n"
    "s_add_u32 m0, s59, 24576\n"
    "s_mov_b32 s74, 2064384\n"
    "buffer_load_dwordx4 v5, s[40:43], s74 offen lds\n"
    "s_add_u32 m0, s59, 28672\n"
    "s_mov_b32 s74, 2064512\n"
    "buffer_load_dwordx4 v5, s[40:43], s74 offen lds\n"
    "s_add_u32 m0, s59, 32768\n"
    "s_mov_b32 s74, 2064640\n"
    "buffer_load_dwordx4 v5, s[40:43], s74 offen lds\n"
    "s_add_u32 m0, s59, 36864\n"
    "s_mov_b32 s74, 2064384\n"
    "buffer_load_dwordx4 v5, s[44:47], s74 offen lds\n"
    "s_add_u32 m0, s59, 40960\n"
    "s_mov_b32 s74, 2064512\n"
    "buffer_load_dwordx4 v5, s[44:47], s74 offen lds\n"
    "s_add_u32 m0, s59, 45056\n"
    "s_mov_b32 s74, 2064640\n"
    "buffer_load_dwordx4 v5, s[44:47], s74 offen lds\n"
    "ds_read_b64_tr_b16 a[48:49], v16 offset:0\n"
    "ds_read_b64_tr_b16 a[50:51], v16 offset:512\n"
    "ds_read_b64_tr_b16 a[52:53], v16 offset:8\n"
    "ds_read_b64_tr_b16 a[54:55], v16 offset:520\n"
    "ds_read_b64_tr_b16 a[56:57], v18 offset:0\n"
    "ds_read_b64_tr_b16 a[58:59], v18 offset:512\n"
    "ds_read_b64_tr_b16 a[60:61], v16 offset:12288\n"
    "ds_read_b64_tr_b16 a[62:63], v16 offset:12800\n"
    "ds_read_b64_tr_b16 a[64:65], v16 offset:12296\n"
    "ds_read_b64_tr_b16 a[66:67], v16 offset:12808\n"
    "ds_read_b64_tr_b16 a[68:69], v18 offset:12288\n"
    "ds_read_b64_tr_b16 a[70:71], v18 offset:12800\n"
    "s_waitcnt vmcnt(24) lgkmcnt(0)\n"
    "s_barrier\n"
    "s_add_u32 m0, s59, 49152\n"
    "s_mov_b32 s74, 2359296\n"
    "buffer_load_dwordx4 v5, s[40:43], s74 offen lds\n"
    "s_add_u32 m0, s59, 53248\n"
    "s_mov_b32 s74, 2359424\n"
    "buffer_load_dwordx4 v5, s[40:43], s74 offen lds\n"
    "s_add_u32 m0, s59, 57344\n"
    "s_mov_b32 s74, 2359552\n"
    "buffer_load_dwordx4 v5, s[40:43], s74 offen lds\n"
    "s_add_u32 m0, s59, 61440\n"
    "s_mov_b32 s74, 2359296\n"
    "buffer_load_dwordx4 v5, s[44:47], s74 offen lds\n"
    "s_add_u32 m0, s59, 65536\n"
    "s_mov_b32 s74, 2359424\n"
    "buffer_load_dwordx4 v5, s[44:47], s74 offen lds\n"
    "s_add_u32 m0, s59, 69632\n"
    "s_mov_b32 s74, 2359552\n"
    "buffer_load_dwordx4 v5, s[44:47], s74 offen lds\n"
    "ds_read_b64_tr_b16 a[72:73], v16 offset:24576\n"
    "ds_read_b64_tr_b16 a[74:75], v16 offset:25088\n"
    "ds_read_b64_tr_b16 a[76:77], v16 offset:24584\n"
    "ds_read_b64_tr_b16 a[78:79], v16 offset:25096\n"
    "ds_read_b64_tr_b16 a[80:81], v18 offset:24576\n"
    "ds_read_b64_tr_b16 a[82:83], v18 offset:25088\n"
    "ds_read_b64_tr_b16 a[84:85], v16 offset:36864\n"
    "ds_read_b64_tr_b16 a[86:87], v16 offset:37376\n"
    "ds_read_b64_tr_b16 a[88:89], v16 offset:36872\n"
    "ds_read_b64_tr_b16 a[90:91], v16 offset:37384\n"
    "ds_read_b64_tr_b16 a[92:93], v18 offset:36864\n"
    "ds_read_b64_tr_b16 a[94:95], v18 offset:37376\n"
    "s_waitcnt vmcnt(24) lgkmcnt(0)\n"
    "s_barrier\n"
    "s_add_u32 m0, s59, 73728\n"
    "s_mov_b32 s74, 2654208\n"
    "buffer_load_dwordx4 v5, s[40:43], s74 offen lds\n"
    "s_add_u32 m0, s59, 77824\n"
    "s_mov_b32 s74, 2654336\n"
    "buffer_load_dwordx4 v5, s[40:43], s74 offen lds\n"
    "s_add_u32 m0, s59, 81920\n"
    "s_mov_b32 s74, 2654464\n"
    "buffer_load_dwordx4 v5, s[40:43], s74 offen lds\n"
    "s_add_u32 m0, s59, 86016\n"
    "s_mov_b32 s74, 2654208\n"
    "buffer_load_dwordx4 v5, s[44:47], s74 offen lds\n"
    "s_add_u32 m0, s59, 90112\n"
    "s_mov_b32 s74, 2654336\n"
    "buffer_load_dwordx4 v5, s[44:47], s74 offen lds\n"
    "s_add_u32 m0, s59, 94208\n"
    "s_mov_b32 s74, 2654464\n"
    "buffer_load_dwordx4 v5, s[44:47], s74 offen lds\n"
    "ds_read_b64_tr_b16 a[96:97], v17 offset:0\n"
    "ds_read_b64_tr_b16 a[98:99], v17 offset:512\n"
    "ds_read_b64_tr_b16 a[100:101], v17 offset:8\n"
    "ds_read_b64_tr_b16 a[102:103], v17 offset:520\n"
    "ds_read_b64_tr_b16 a[104:105], v19 offset:0\n"
    "ds_read_b64_tr_b16 a[106:107], v19 offset:512\n"
    "ds_read_b64_tr_b16 a[108:109], v17 offset:12288\n"
    "ds_read_b64_tr_b16 a[110:111], v17 offset:12800\n"
    "ds_read_b64_tr_b16 a[112:113], v17 offset:12296\n"
    "ds_read_b64_tr_b16 a[114:115], v17 offset:12808\n"
    "ds_read_b64_tr_b16 a[116:117], v19 offset:12288\n"
    "ds_read_b64_tr_b16 a[118:119], v19 offset:12800\n"
    "s_waitcnt vmcnt(24) lgkmcnt(0)\n"
    "s_barrier\n"
    "s_add_u32 m0, s59, 98304\n"
    "s_mov_b32 s74, 2949120\n"
    "buffer_load_dwordx4 v5, s[40:43], s74 offen lds\n"
    "s_add_u32 m0, s59, 102400\n"
    "s_mov_b32 s74, 2949248\n"
    "buffer_load_dwordx4 v5, s[40:43], s74 offen lds\n"
    "s_add_u32 m0, s59, 106496\n"
    "s_mov_b32 s74, 2949376\n"
    "buffer_load_dwordx4 v5, s[40:43], s74 offen lds\n"
    "s_add_u32 m0, s59, 110592\n"
    "s_mov_b32 s74, 2949120\n"
    "buffer_load_dwordx4 v5, s[44:47], s74 offen lds\n"
    "s_add_u32 m0, s59, 114688\n"
    "s_mov_b32 s74, 2949248\n"
    "buffer_load_dwordx4 v5, s[44:47], s74 offen lds\n"
    "s_add_u32 m0, s59, 118784\n"
    "s_mov_b32 s74, 2949376\n"
    "buffer_load_dwordx4 v5, s[44:47], s74 offen lds\n"
    "ds_read_b64_tr_b16 a[120:121], v17 offset:24576\n"
    "ds_read_b64_tr_b16 a[122:123], v17 offset:25088\n"
    "ds_read_b64_tr_b16 a[124:125], v17 offset:24584\n"
    "ds_read_b64_tr_b16 a[126:127], v17 offset:25096\n"
    "ds_read_b64_tr_b16 a[128:129], v19 offset:24576\n"
    "ds_read_b64_tr_b16 a[130:131], v19 offset:25088\n"
    "ds_read_b64_tr_b16 a[132:133], v17 offset:36864\n"
    "ds_read_b64_tr_b16 a[134:135], v17 offset:37376\n"
    "ds_read_b64_tr_b16 a[136:137], v17 offset:36872\n"
    "ds_read_b64_tr_b16 a[138:139], v17 offset:37384\n"
    "ds_read_b64_tr_b16 a[140:141], v19 offset:36864\n"
    "ds_read_b64_tr_b16 a[142:143], v19 offset:37376\n"
    "s_waitcnt vmcnt(24) lgkmcnt(0)\n"
    "s_barrier\n"
    "s_add_u32 m0, s59, 122880\n"
    "s_mov_b32 s74, 3244032\n"
    "buffer_load_dwordx4 v5, s[40:43], s74 offen lds\n"
    "s_add_u32 m0, s59, 126976\n"
    "s_mov_b32 s74, 3244160\n"
    "buffer_load_dwordx4 v5, s[40:43], s74 offen lds\n"
    "s_add_u32 m0, s59, 131072\n"
    "s_mov_b32 s74, 3244288\n"
    "buffer_load_dwordx4 v5, s[40:43], s74 offen lds\n"
    "s_add_u32 m0, s59, 135168\n"
    "s_mov_b32 s74, 3244032\n"
    "buffer_load_dwordx4 v5, s[44:47], s74 offen lds\n"
    "s_add_u32 m0, s59, 139264\n"
    "s_mov_b32 s74, 3244160\n"
    "buffer_load_dwordx4 v5, s[44:47], s74 offen lds\n"
    "s_add_u32 m0, s59, 143360\n"
    "s_mov_b32 s74, 3244288\n"
    "buffer_load_dwordx4 v5, s[44:47], s74 offen lds\n"
    "ds_read_b64_tr_b16 a[144:145], v6 offset:0\n"
    "ds_read_b64_tr_b16 a[146:147], v6 offset:512\n"
    "ds_read_b64_tr_b16 a[148:149], v6 offset:8\n"
    "ds_read_b64_tr_b16 a[150:151], v6 offset:520\n"
    "ds_read_b64_tr_b16 a[152:153], v7 offset:0\n"
    "ds_read_b64_tr_b16 a[154:155], v7 offset:512\n"
    "ds_read_b64_tr_b16 a[156:157], v6 offset:12288\n"
    "ds_read_b64_tr_b16 a[158:159], v6 offset:12800\n"
    "ds_read_b64_tr_b16 a[160:161], v6 offset:12296\n"
    "ds_read_b64_tr_b16 a[162:163], v6 offset:12808\n"
    "ds_read_b64_tr_b16 a[164:165], v7 offset:12288\n"
    "ds_read_b64_tr_b16 a[166:167], v7 offset:12800\n"
    "s_waitcnt vmcnt(24) lgkmcnt(0)\n"
    "s_barrier\n"
    "s_add_u32 m0, s59, 0\n"
    "s_mov_b32 s74, 3538944\n"
    "buffer_load_dwordx4 v5, s[40:43], s74 offen lds\n"
    "s_add_u32 m0, s59, 4096\n"
    "s_mov_b32 s74, 3539072\n"
    "buffer_load_dwordx4 v5, s[40:43], s74 offen lds\n"
    "s_add_u32 m0, s59, 8192\n"
    "s_mov_b32 s74, 3539200\n"
    "buffer_load_dwordx4 v5, s[40:43], s74 offen lds\n"
    "s_add_u32 m0, s59, 12288\n"
    "s_mov_b32 s74, 3538944\n"
    "buffer_load_dwordx4 v5, s[44:47], s74 offen lds\n"
    "s_add_u32 m0, s59, 16384\n"
    "s_mov_b32 s74, 3539072\n"
    "buffer_load_dwordx4 v5, s[44:47], s74 offen lds\n"
    "s_add_u32 m0, s59, 20480\n"
    "s_mov_b32 s74, 3539200\n"
    "buffer_load_dwordx4 v5, s[44:47], s74 offen lds\n"
    "ds_read_b64_tr_b16 a[168:169], v6 offset:24576\n"
    "ds_read_b64_tr_b16 a[170:171], v6 offset:25088\n"
    "ds_read_b64_tr_b16 a[172:173], v6 offset:24584\n"
    "ds_read_b64_tr_b16 a[174:175], v6 offset:25096\n"
    "ds_read_b64_tr_b16 a[176:177], v7 offset:24576\n"
    "ds_read_b64_tr_b16 a[178:179], v7 offset:25088\n"
    "ds_read_b64_tr_b16 a[180:181], v6 offset:36864\n"
    "ds_read_b64_tr_b16 a[182:183], v6 offset:37376\n"
    "ds_read_b64_tr_b16 a[184:185], v6 offset:36872\n"
    "ds_read_b64_tr_b16 a[186:187], v6 offset:37384\n"
    "ds_read_b64_tr_b16 a[188:189], v7 offset:36864\n"
    "ds_read_b64_tr_b16 a[190:191], v7 offset:37376\n"
    "s_waitcnt vmcnt(24) lgkmcnt(0)\n"
    "s_barrier\n"
    "s_add_u32 m0, s59, 24576\n"
    "s_mov_b32 s74, 3833856\n"
    "buffer_load_dwordx4 v5, s[40:43], s74 offen lds\n"
    "s_add_u32 m0, s59, 28672\n"
    "s_mov_b32 s74, 3833984\n"
    "buffer_load_dwordx4 v5, s[40:43], s74 offen lds\n"
    "s_add_u32 m0, s59, 32768\n"
    "s_mov_b32 s74, 3834112\n"
    "buffer_load_dwordx4 v5, s[40:43], s74 offen lds\n"
    "s_add_u32 m0, s59, 36864\n"
    "s_mov_b32 s74, 3833856\n"
    "buffer_load_dwordx4 v5, s[44:47], s74 offen lds\n"
    "s_add_u32 m0, s59, 40960\n"
    "s_mov_b32 s74, 3833984\n"
    "buffer_load_dwordx4 v5, s[44:47], s74 offen lds\n"
    "s_add_u32 m0, s59, 45056\n"
    "s_mov_b32 s74, 3834112\n"
    "buffer_load_dwordx4 v5, s[44:47], s74 offen lds\n"
    "ds_read_b64_tr_b16 a[192:193], v16 offset:0\n"
    "ds_read_b64_tr_b16 a[194:195], v16 offset:512\n"
    "ds_read_b64_tr_b16 a[196:197], v16 offset:8\n"
    "ds_read_b64_tr_b16 a[198:199], v16 offset:520\n"
    "ds_read_b64_tr_b16 a[200:201], v18 offset:0\n"
    "ds_read_b64_tr_b16 a[202:203], v18 offset:512\n"
    "ds_read_b64_tr_b16 a[204:205], v16 offset:12288\n"
    "ds_read_b64_tr_b16 a[206:207], v16 offset:12800\n"
    "ds_read_b64_tr_b16 a[208:209], v16 offset:12296\n"
    "ds_read_b64_tr_b16 a[210:211], v16 offset:12808\n"
    "ds_read_b64_tr_b16 a[212:213], v18 offset:12288\n"
    "ds_read_b64_tr_b16 a[214:215], v18 offset:12800\n"
    "s_waitcnt vmcnt(24) lgkmcnt(0)\n"
    "s_barrier\n"
    "s_add_u32 m0, s59, 49152\n"
    "s_mov_b32 s74, 4128768\n"
    "buffer_load_dwordx4 v5, s[40:43], s74 offen lds\n"
    "s_add_u32 m0, s59, 53248\n"
    "s_mov_b32 s74, 4128896\n"
    "buffer_load_dwordx4 v5, s[40:43], s74 offen lds\n"
    "s_add_u32 m0, s59, 57344\n"
    "s_mov_b32 s74, 4129024\n"
    "buffer_load_dwordx4 v5, s[40:43], s74 offen lds\n"
    "s_add_u32 m0, s59, 61440\n"
    "s_mov_b32 s74, 4128768\n"
    "buffer_load_dwordx4 v5, s[44:47], s74 offen lds\n"
    "s_add_u32 m0, s59, 65536\n"
    "s_mov_b32 s74, 4128896\n"
    "buffer_load_dwordx4 v5, s[44:47], s74 offen lds\n"
    "s_add_u32 m0, s59, 69632\n"
    "s_mov_b32 s74, 4129024\n"
    "buffer_load_dwordx4 v5, s[44:47], s74 offen lds\n"
    "ds_read_b64_tr_b16 a[216:217], v16 offset:24576\n"
    "ds_read_b64_tr_b16 a[218:219], v16 offset:25088\n"
    "ds_read_b64_tr_b16 a[220:221], v16 offset:24584\n"
    "ds_read_b64_tr_b16 a[222:223], v16 offset:25096\n"
    "ds_read_b64_tr_b16 a[224:225], v18 offset:24576\n"
    "ds_read_b64_tr_b16 a[226:227], v18 offset:25088\n"
    "ds_read_b64_tr_b16 a[228:229], v16 offset:36864\n"
    "ds_read_b64_tr_b16 a[230:231], v16 offset:37376\n"
    "ds_read_b64_tr_b16 a[232:233], v16 offset:36872\n"
    "ds_read_b64_tr_b16 a[234:235], v16 offset:37384\n"
    "ds_read_b64_tr_b16 a[236:237], v18 offset:36864\n"
    "ds_read_b64_tr_b16 a[238:239], v18 offset:37376\n"
    "s_waitcnt vmcnt(24) lgkmcnt(0)\n"
    "s_barrier\n"
    "s_add_u32 m0, s59, 73728\n"
    "s_mov_b32 s74, 4423680\n"
    "buffer_load_dwordx4 v5, s[40:43], s74 offen lds\n"
    "s_add_u32 m0, s59, 77824\n"
    "s_mov_b32 s74, 4423808\n"
    "buffer_load_dwordx4 v5, s[40:43], s74 offen lds\n"
    "s_add_u32 m0, s59, 81920\n"
    "s_mov_b32 s74, 4423936\n"
    "buffer_load_dwordx4 v5, s[40:43], s74 offen lds\n"
    "s_add_u32 m0, s59, 86016\n"
    "s_mov_b32 s74, 4423680\n"
    "buffer_load_dwordx4 v5, s[44:47], s74 offen lds\n"
    "s_add_u32 m0, s59, 90112\n"
    "s_mov_b32 s74, 4423808\n"
    "buffer_load_dwordx4 v5, s[44:47], s74 offen lds\n"
    "s_add_u32 m0, s59, 94208\n"
    "s_mov_b32 s74, 4423936\n"
    "buffer_load_dwordx4 v5, s[44:47], s74 offen lds\n"
    "ds_read_b64_tr_b16 a[240:241], v17 offset:0\n"
    "ds_read_b64_tr_b16 a[242:243], v17 offset:512\n"
    "ds_read_b64_tr_b16 a[244:245], v17 offset:8\n"
    "ds_read_b64_tr_b16 a[246:247], v17 offset:520\n"
    "ds_read_b64_tr_b16 a[248:249], v19 offset:0\n"
    "ds_read_b64_tr_b16 a[250:251], v19 offset:512\n"
    "ds_read_b64_tr_b16 a[252:253], v17 offset:12288\n"
    "ds_read_b64_tr_b16 a[254:255], v17 offset:12800\n"
    "ds_read_b64_tr_b16 v[80:81], v17 offset:12296\n"
    "ds_read_b64_tr_b16 v[82:83], v17 offset:12808\n"
    "ds_read_b64_tr_b16 v[84:85], v19 offset:12288\n"
    "ds_read_b64_tr_b16 v[86:87], v19 offset:12800\n"
    "s_waitcnt vmcnt(24) lgkmcnt(0)\n"
    "s_barrier\n"
    "s_add_u32 m0, s59, 98304\n"
    "s_mov_b32 s74, 4718592\n"
    "buffer_load_dwordx4 v5, s[40:43], s74 offen lds\n"
    "s_add_u32 m0, s59, 102400\n"
    "s_mov_b32 s74, 4718720\n"
    "buffer_load_dwordx4 v5, s[40:43], s74 offen lds\n"
    "s_add_u32 m0, s59, 106496\n"
    "s_mov_b32 s74, 4718848\n"
    "buffer_load_dwordx4 v5, s[40:43], s74 offen lds\n"
    "s_add_u32 m0, s59, 110592\n"
    "s_mov_b32 s74, 4718592\n"
    "buffer_load_dwordx4 v5, s[44:47], s74 offen lds\n"
    "s_add_u32 m0, s59, 114688\n"
    "s_mov_b32 s74, 4718720\n"
    "buffer_load_dwordx4 v5, s[44:47], s74 offen lds\n"
    "s_add_u32 m0, s59, 118784\n"
    "s_mov_b32 s74, 4718848\n"
    "buffer_load_dwordx4 v5, s[44:47], s74 offen lds\n"
    "ds_read_b64_tr_b16 v[88:89], v17 offset:24576\n"
    "ds_read_b64_tr_b16 v[90:91], v17 offset:25088\n"
    "ds_read_b64_tr_b16 v[92:93], v17 offset:24584\n"
    "ds_read_b64_tr_b16 v[94:95], v17 offset:25096\n"
    "ds_read_b64_tr_b16 v[96:97], v19 offset:24576\n"
    "ds_read_b64_tr_b16 v[98:99], v19 offset:25088\n"
    "ds_read_b64_tr_b16 v[100:101], v17 offset:36864\n"
    "ds_read_b64_tr_b16 v[102:103], v17 offset:37376\n"
    "ds_read_b64_tr_b16 v[104:105], v17 offset:36872\n"
    "ds_read_b64_tr_b16 v[106:107], v17 offset:37384\n"
    "ds_read_b64_tr_b16 v[108:109], v19 offset:36864\n"
    "ds_read_b64_tr_b16 v[110:111], v19 offset:37376\n"
    "s_waitcnt vmcnt(24) lgkmcnt(0)\n"
    "s_barrier\n"
    "s_add_u32 m0, s59, 122880\n"
    "s_mov_b32 s74, 5013504\n"
    "buffer_load_dwordx4 v5, s[40:43], s74 offen lds\n"
    "s_add_u32 m0, s59, 126976\n"
    "s_mov_b32 s74, 5013632\n"
    "buffer_load_dwordx4 v5, s[40:43], s74 offen lds\n"
    "s_add_u32 m0, s59, 131072\n"
    "s_mov_b32 s74, 5013760\n"
    "buffer_load_dwordx4 v5, s[40:43], s74 offen lds\n"
    "s_add_u32 m0, s59, 135168\n"
    "s_mov_b32 s74, 5013504\n"
    "buffer_load_dwordx4 v5, s[44:47], s74 offen lds\n"
    "s_add_u32 m0, s59, 139264\n"
    "s_mov_b32 s74, 5013632\n"
    "buffer_load_dwordx4 v5, s[44:47], s74 offen lds\n"
    "s_add_u32 m0, s59, 143360\n"
    "s_mov_b32 s74, 5013760\n"
    "buffer_load_dwordx4 v5, s[44:47], s74 offen lds\n"
    "buffer_load_dwordx2 v[12:13], off, s[36:39], 0 sc1\n"
    "ds_read_b64_tr_b16 v[112:113], v6 offset:0\n"
    "ds_read_b64_tr_b16 v[114:115], v6 offset:512\n"
    "ds_read_b64_tr_b16 v[116:117], v6 offset:8\n"
    "ds_read_b64_tr_b16 v[118:119], v6 offset:520\n"
    "ds_read_b64_tr_b16 v[120:121], v7 offset:0\n"
    "ds_read_b64_tr_b16 v[122:123], v7 offset:512\n"
    "ds_read_b64_tr_b16 v[124:125], v6 offset:12288\n"
    "ds_read_b64_tr_b16 v[126:127], v6 offset:12800\n"
    "ds_read_b64_tr_b16 v[128:129], v6 offset:12296\n"
    "ds_read_b64_tr_b16 v[130:131], v6 offset:12808\n"
    "ds_read_b64_tr_b16 v[132:133], v7 offset:12288\n"
    "ds_read_b64_tr_b16 v[134:135], v7 offset:12800\n"
    "s_waitcnt vmcnt(25) lgkmcnt(0)\n"
    "s_barrier\n"
    "ds_read_b64_tr_b16 v[136:137], v6 offset:24576\n"
    "ds_read_b64_tr_b16 v[138:139], v6 offset:25088\n"
    "ds_read_b64_tr_b16 v[140:141], v6 offset:24584\n"
    "ds_read_b64_tr_b16 v[142:143], v6 offset:25096\n"
    "ds_read_b64_tr_b16 v[144:145], v7 offset:24576\n"
    "ds_read_b64_tr_b16 v[146:147], v7 offset:25088\n"
    "ds_read_b64_tr_b16 v[148:149], v6 offset:36864\n"
    "ds_read_b64_tr_b16 v[150:151], v6 offset:37376\n"
    "ds_read_b64_tr_b16 v[152:153], v6 offset:36872\n"
    "ds_read_b64_tr_b16 v[154:155], v6 offset:37384\n"
    "ds_read_b64_tr_b16 v[156:157], v7 offset:36864\n"
    "ds_read_b64_tr_b16 v[158:159], v7 offset:37376\n"
    "s_waitcnt vmcnt(19) lgkmcnt(0)\n"
    "s_barrier\n"
    "ds_read_b64_tr_b16 v[160:161], v16 offset:0\n"
    "ds_read_b64_tr_b16 v[162:163], v16 offset:512\n"
    "ds_read_b64_tr_b16 v[164:165], v16 offset:8\n"
    "ds_read_b64_tr_b16 v[166:167], v16 offset:520\n"
    "ds_read_b64_tr_b16 v[168:169], v18 offset:0\n"
    "ds_read_b64_tr_b16 v[170:171], v18 offset:512\n"
    "ds_read_b64_tr_b16 v[172:173], v16 offset:12288\n"
    "ds_read_b64_tr_b16 v[174:175], v16 offset:12800\n"
    "ds_read_b64_tr_b16 v[176:177], v16 offset:12296\n"
    "ds_read_b64_tr_b16 v[178:179], v16 offset:12808\n"
    "ds_read_b64_tr_b16 v[180:181], v18 offset:12288\n"
    "ds_read_b64_tr_b16 v[182:183], v18 offset:12800\n"
    "s_waitcnt vmcnt(13) lgkmcnt(0)\n"
    "s_barrier\n"
    "ds_read_b64_tr_b16 v[184:185], v16 offset:24576\n"
    "ds_read_b64_tr_b16 v[186:187], v16 offset:25088\n"
    "ds_read_b64_tr_b16 v[188:189], v16 offset:24584\n"
    "ds_read_b64_tr_b16 v[190:191], v16 offset:25096\n"
    "ds_read_b64_tr_b16 v[192:193], v18 offset:24576\n"
    "ds_read_b64_tr_b16 v[194:195], v18 offset:25088\n"
    "ds_read_b64_tr_b16 v[196:197], v16 offset:36864\n"
    "ds_read_b64_tr_b16 v[198:199], v16 offset:37376\n"
    "ds_read_b64_tr_b16 v[200:201], v16 offset:36872\n"
    "ds_read_b64_tr_b16 v[202:203], v16 offset:37384\n"
    "ds_read_b64_tr_b16 v[204:205], v18 offset:36864\n"
    "ds_read_b64_tr_b16 v[206:207], v18 offset:37376\n"
    "s_waitcnt vmcnt(7) lgkmcnt(0)\n"
    "s_barrier\n"
    "ds_read_b64_tr_b16 v[208:209], v17 offset:0\n"
    "ds_read_b64_tr_b16 v[210:211], v17 offset:512\n"
    "ds_read_b64_tr_b16 v[212:213], v17 offset:8\n"
    "ds_read_b64_tr_b16 v[214:215], v17 offset:520\n"
    "ds_read_b64_tr_b16 v[216:217], v19 offset:0\n"
    "ds_read_b64_tr_b16 v[218:219], v19 offset:512\n"
    "ds_read_b64_tr_b16 v[220:221], v17 offset:12288\n"
    "ds_read_b64_tr_b16 v[222:223], v17 offset:12800\n"
    "ds_read_b64_tr_b16 v[224:225], v17 offset:12296\n"
    "ds_read_b64_tr_b16 v[226:227], v17 offset:12808\n"
    "ds_read_b64_tr_b16 v[228:229], v19 offset:12288\n"
    "ds_read_b64_tr_b16 v[230:231], v19 offset:12800\n"
    "s_waitcnt vmcnt(0) lgkmcnt(0)\n"
    "s_barrier\n"
    ".LgUP_par_%=:\n"
    "v_readfirstlane_b32 s74, v12\n"
    "v_readfirstlane_b32 s75, v13\n"
    "s_xor_b32 s74, s74, s66\n"
    "s_xor_b32 s75, s75, s67\n"
    "s_cselect_b32 s75, 2, 0\n"
    "s_or_b32 s75, s75, s74\n"
    "s_cmp_lt_u32 s75, 2\n"
    "s_cbranch_scc1 .LgUP_par1_%=\n"
    "s_sleep 4\n"
    "buffer_load_dwordx2 v[12:13], off, s[36:39], 0 sc1\n"
    "s_waitcnt vmcnt(0)\n"
    "s_sub_u32 s72, s72, 1\n"
    "s_cbranch_scc0 .LgUP_par_%=\n"
    "s_mov_b32 s72, 0\n"
    ".LgUP_par1_%=:\n"
    "s_and_b32 s74, s74, 1\n"
    "s_lshl_b32 s74, s74, 13\n"
    "s_add_u32 s36, s36, s74\n"
    "s_addc_u32 s37, s37, 0\n"
    "v_add_u32 v13, v11, v3\n"
    "v_cmp_gt_i32 vcc, s71, v13\n"
    "s_nop 0\n"
    "s_or_b64 exec, vcc, 1\n"
    "buffer_atomic_smax v11, v2, s[36:39], 0 offen sc0 sc1\n"
    "s_mov_b64 exec, -1\n"
    "ds_read_b64_tr_b16 v[232:233], v17 offset:24576\n"
    "ds_read_b64_tr_b16 v[234:235], v17 offset:25088\n"
    "ds_read_b64_tr_b16 v[236:237], v17 offset:24584\n"
    "ds_read_b64_tr_b16 v[238:239], v17 offset:25096\n"
    "ds_read_b64_tr_b16 v[240:241], v19 offset:24576\n"
    "ds_read_b64_tr_b16 v[242:243], v19 offset:25088\n"
    "ds_read_b64_tr_b16 v[244:245], v17 offset:36864\n"
    "ds_read_b64_tr_b16 v[246:247], v17 offset:37376\n"
    "ds_read_b64_tr_b16 v[248:249], v17 offset:36872\n"
    "ds_read_b64_tr_b16 v[250:251], v17 offset:37384\n"
    "ds_read_b64_tr_b16 v[252:253], v19 offset:36864\n"
    "ds_read_b64_tr_b16 v[254:255], v19 offset:37376\n"
    "s_waitcnt lgkmcnt(0)\n"
    "s_barrier\n"
    "s_waitcnt vmcnt(0)\n"
    "s_branch .LgUP_block1_%=\n"
    ".LgUP_LOOPB_%=:\n"
    ".LgUP_head_%=:\n"
    "v_mfma_f32_16x16x32_bf16 v[48:51], a[8:11], v[16:19], 0\n"
    "s_waitcnt lgkmcnt(2)\n"
    "s_sub_u32 s74, s68, 1\n"
    "v_mfma_f32_16x16x32_bf16 v[72:75], a[20:23], v[16:19], 0\n"
    "ds_read_b128 v[16:19], v9 offset:4096\n"
    "v_mul_f32 v64, v40, v64\n"
    "v_mfma_f32_16x16x32_bf16 v[52:55], a[8:11], v[20:23], 0\n"
    "s_waitcnt lgkmcnt(2)\n"
    "v_mul_f32 v65, v41, v65\n"
    "v_mul_f32 v66, v42, v66\n"
    "v_mfma_f32_16x16x32_bf16 v[76:79], a[20:23], v[20:23], 0\n"
    "ds_read_b128 v[20:23], v9 offset:5120\n"
    "v_mul_f32 v67, v43, v67\n"
    "v_mfma_f32_16x16x32_bf16 v[48:51], a[32:35], v[24:27], v[48:51]\n"
    "s_waitcnt lgkmcnt(2)\n"
    "v_mul_f32 v68, v44, v68\n"
    "v_mul_f32 v69, v45, v69\n"
    "v_mfma_f32_16x16x32_bf16 v[72:75], a[44:47], v[24:27], v[72:75]\n"
    "ds_read_b128 v[24:27], v9 offset:6144\n"
    "v_mul_f32 v70, v46, v70\n"
    "v_mfma_f32_16x16x32_bf16 v[52:55], a[32:35], v[28:31], v[52:55]\n"
    "s_waitcnt lgkmcnt(2)\n"
    "v_mul_f32 v71, v47, v71\n"
    "v_mul_f32 v40, 0xbfb8aa3b, v40\n"
    "v_mfma_f32_16x16x32_bf16 v[76:79], a[44:47], v[28:31], v[76:79]\n"
    "ds_read_b128 v[28:31], v9 offset:7168\n"
    "v_mul_f32 v41, 0xbfb8aa3b, v41\n"
    "v_mfma_f32_16x16x32_bf16 v[48:51], a[56:59], v[16:19], v[48:51]\n"
    "s_waitcnt lgkmcnt(2)\n"
    "v_exp_f32 v40, v40\n"
    "v_mfma_f32_16x16x32_bf16 v[72:75], a[68:71], v[16:19], v[72:75]\n"
    "ds_read_b128 v[16:19], v9 offset:8192\n"
    "v_mul_f32 v42, 0xbfb8aa3b, v42\n"
    "v_mfma_f32_16x16x32_bf16 v[52:55], a[56:59], v[20:23], v[52:55]\n"
    "s_waitcnt lgkmcnt(2)\n"
    "v_exp_f32 v41, v41\n"
    "v_mfma_f32_16x16x32_bf16 v[76:79], a[68:71], v[20:23], v[76:79]\n"
    "ds_read_b128 v[20:23], v9 offset:9216\n"
    "v_mul_f32 v43, 0xbfb8aa3b, v43\n"
    "v_mfma_f32_16x16x32_bf16 v[48:51], a[80:83], v[24:27], v[48:51]\n"
    "s_waitcnt lgkmcnt(2)\n"
    "v_exp_f32 v42, v42\n"
    "v_mfma_f32_16x16x32_bf16 v[72:75], a[92:95], v[24:27], v[72:75]\n"
    "ds_read_b128 v[24:27], v9 offset:10240\n"
    "v_mul_f32 v44, 0xbfb8aa3b, v44\n"
    "v_mfma_f32_16x16x32_bf16 v[52:55], a[80:83], v[28:31], v[52:55]\n"
    "s_waitcnt lgkmcnt(2)\n"
    "v_exp_f32 v43, v43\n"
    "v_mfma_f32_16x16x32_bf16 v[76:79], a[92:95], v[28:31], v[76:79]\n"
    "ds_read_b128 v[28:31], v9 offset:11264\n"
    "v_mul_f32 v45, 0xbfb8aa3b, v45\n"
    "v_mfma_f32_16x16x32_bf16 v[48:51], a[104:107], v[16:19], v[48:51]\n"
    "s_waitcnt lgkmcnt(2)\n"
    "v_exp_f32 v44, v44\n"
    "v_mfma_f32_16x16x32_bf16 v[72:75], a[116:119], v[16:19], v[72:75]\n"
    "ds_read_b128 v[16:19], v9 offset:12288\n"
    "v_mul_f32 v46, 0xbfb8aa3b, v46\n"
    "v_mfma_f32_16x16x32_bf16 v[52:55], a[104:107], v[20:23], v[52:55]\n"
    "s_waitcnt lgkmcnt(2)\n"
    "v_exp_f32 v45, v45\n"
    "v_mfma_f32_16x16x32_bf16 v[76:79], a[116:119], v[20:23], v[76:79]\n"
    "ds_read_b128 v[20:23], v9 offset:13312\n"
    "v_mul_f32 v47, 0xbfb8aa3b, v47\n"
    "v_mfma_f32_16x16x32_bf16 v[48:51], a[128:131], v[24:27], v[48:51]\n"
    "s_waitcnt lgkmcnt(2)\n"
    "v_exp_f32 v46, v46\n"
    "v_mfma_f32_16x16x32_bf16 v[72:75], a[140:143], v[24:27], v[72:75]\n"
    "ds_read_b128 v[24:27], v9 offset:14336\n"
    "v_add_f32 v40, 1.0, v40\n"
    "v_mfma_f32_16x16x32_bf16 v[52:55], a[128:131], v[28:31], v[52:55]\n"
    "s_waitcnt lgkmcnt(2)\n"
    "v_exp_f32 v47, v47\n"
    "v_mfma_f32_16x16x32_bf16 v[76:79], a[140:143], v[28:31], v[76:79]\n"
    "ds_read_b128 v[28:31], v9 offset:15360\n"
    "v_add_f32 v41, 1.0, v41\n"
    "v_mfma_f32_16x16x32_bf16 v[48:51], a[152:155], v[16:19], v[48:51]\n"
    "s_waitcnt lgkmcnt(2)\n"
    "v_rcp_f32 v40, v40\n"
    "v_mfma_f32_16x16x32_bf16 v[72:75], a[164:167], v[16:19], v[72:75]\n"
    "ds_read_b128 v[16:19], v9 offset:16384\n"
    "v_add_f32 v42, 1.0, v42\n"
    "v_mfma_f32_16x16x32_bf16 v[52:55], a[152:155], v[20:23], v[52:55]\n"
    "s_waitcnt lgkmcnt(2)\n"
    "v_rcp_f32 v41, v41\n"
    "v_mfma_f32_16x16x32_bf16 v[76:79], a[164:167], v[20:23], v[76:79]\n"
    "ds_read_b128 v[20:23], v9 offset:17408\n"
    "v_add_f32 v43, 1.0, v43\n"
    "v_mfma_f32_16x16x32_bf16 v[48:51], a[176:179], v[24:27], v[48:51]\n"
    "s_waitcnt lgkmcnt(2)\n"
    "v_rcp_f32 v42, v42\n"
    "v_mfma_f32_16x16x32_bf16 v[72:75], a[188:191], v[24:27], v[72:75]\n"
    "ds_read_b128 v[24:27], v9 offset:18432\n"
    "v_add_f32 v44, 1.0, v44\n"
    "v_mfma_f32_16x16x32_bf16 v[52:55], a[176:179], v[28:31], v[52:55]\n"
    "s_waitcnt lgkmcnt(2)\n"
    "v_rcp_f32 v43, v43\n"
    "v_mfma_f32_16x16x32_bf16 v[76:79], a[188:191], v[28:31], v[76:79]\n"
    "ds_read_b128 v[28:31], v9 offset:19456\n"
    "v_add_f32 v45, 1.0, v45\n"
    "v_mfma_f32_16x16x32_bf16 v[48:51], a[200:203], v[16:19], v[48:51]\n"
    "s_waitcnt lgkmcnt(2)\n"
    "v_rcp_f32 v44, v44\n"
    "v_mfma_f32_16x16x32_bf16 v[72:75], a[212:215], v[16:19], v[72:75]\n"
    "ds_read_b128 v[16:19], v9 offset:20480\n"
    "v_add_f32 v46, 1.0, v46\n"
    "v_mfma_f32_16x16x32_bf16 v[52:55], a[200:203], v[20:23], v[52:55]\n"
    "s_waitcnt lgkmcnt(2)\n"
    "v_rcp_f32 v45, v45\n"
    "v_mfma_f32_16x16x32_bf16 v[76:79], a[212:215], v[20:23], v[76:79]\n"
    "ds_read_b128 v[20:23], v9 offset:21504\n"
    "v_add_f32 v47, 1.0, v47\n"
    "v_mfma_f32_16x16x32_bf16 v[48:51], a[224:227], v[24:27], v[48:51]\n"
    "s_waitcnt lgkmcnt(2)\n"
    "v_rcp_f32 v46, v46\n"
    "v_mfma_f32_16x16x32_bf16 v[72:75], a[236:239], v[24:27], v[72:75]\n"
    "ds_read_b128 v[24:27], v9 offset:22528\n"
    "v_mul_f32 v40, v40, v64\n"
    "v_mfma_f32_16x16x32_bf16 v[52:55], a[224:227], v[28:31], v[52:55]\n"
    "s_waitcnt lgkmcnt(2)\n"
    "v_rcp_f32 v47, v47\n"
    "v_mfma_f32_16x16x32_bf16 v[76:79], a[236:239], v[28:31], v[76:79]\n"
    "ds_read_b128 v[28:31], v9 offset:23552\n"
    "v_mul_f32 v41, v41, v65\n"
    "v_mfma_f32_16x16x32_bf16 v[48:51], a[248:251], v[16:19], v[48:51]\n"
    "s_waitcnt lgkmcnt(2)\n"
    "v_mul_f32 v42, v42, v66\n"
    "v_mul_f32 v43, v43, v67\n"
    "v_mfma_f32_16x16x32_bf16 v[72:75], v[84:87], v[16:19], v[72:75]\n"
    "ds_read_b128 v[16:19], v9 offset:24576\n"
    "v_mul_f32 v44, v44, v68\n"
    "v_mfma_f32_16x16x32_bf16 v[52:55], a[248:251], v[20:23], v[52:55]\n"
    "s_waitcnt lgkmcnt(2)\n"
    "v_mul_f32 v45, v45, v69\n"
    "v_mul_f32 v46, v46, v70\n"
    "v_mfma_f32_16x16x32_bf16 v[76:79], v[84:87], v[20:23], v[76:79]\n"
    "ds_read_b128 v[20:23], v9 offset:25600\n"
    "v_mul_f32 v47, v47, v71\n"
    "v_mfma_f32_16x16x32_bf16 v[48:51], v[96:99], v[24:27], v[48:51]\n"
    "s_waitcnt lgkmcnt(2)\n"
    "v_cvt_pk_bf16_f32 v40, v40, v41\n"
    "v_cvt_pk_bf16_f32 v41, v42, v43\n"
    "v_mfma_f32_16x16x32_bf16 v[72:75], v[108:111], v[24:27], v[72:75]\n"
    "ds_read_b128 v[24:27], v9 offset:26624\n"
    "v_cvt_pk_bf16_f32 v42, v44, v45\n"
    "v_mfma_f32_16x16x32_bf16 v[52:55], v[96:99], v[28:31], v[52:55]\n"
    "s_waitcnt lgkmcnt(2)\n"
    "v_cvt_pk_bf16_f32 v43, v46, v47\n"
    "v_mfma_f32_16x16x32_bf16 v[76:79], v[108:111], v[28:31], v[76:79]\n"
    "ds_read_b128 v[28:31], v9 offset:27648\n"
    "v_mfma_f32_16x16x32_bf16 v[48:51], v[120:123], v[16:19], v[48:51]\n"
    "s_waitcnt lgkmcnt(2)\n"
    "s_and_b32 s76, s71, s54\n"
    "buffer_store_dwordx4 v[40:43], v15, s[36:39], s73 offen offset:1024 sc1\n"
    "v_mfma_f32_16x16x32_bf16 v[72:75], v[132:135], v[16:19], v[72:75]\n"
    "ds_read_b128 v[16:19], v9 offset:28672\n"
    "v_mfma_f32_16x16x32_bf16 v[52:55], v[120:123], v[20:23], v[52:55]\n"
    "s_waitcnt lgkmcnt(2)\n"
    "s_mul_i32 s76, s76, s53\n"
    "v_mfma_f32_16x16x32_bf16 v[76:79], v[132:135], v[20:23], v[76:79]\n"
    "ds_read_b128 v[20:23], v9 offset:29696\n"
    "v_mfma_f32_16x16x32_bf16 v[48:51], v[144:147], v[24:27], v[48:51]\n"
    "s_waitcnt lgkmcnt(2)\n"
    "s_add_u32 s76, s76, s52\n"
    "v_mfma_f32_16x16x32_bf16 v[72:75], v[156:159], v[24:27], v[72:75]\n"
    "ds_read_b128 v[24:27], v9 offset:30720\n"
    "v_mfma_f32_16x16x32_bf16 v[52:55], v[144:147], v[28:31], v[52:55]\n"
    "s_waitcnt lgkmcnt(2)\n"
    "s_and_b32 s77, s71, 1\n"
    "v_mfma_f32_16x16x32_bf16 v[76:79], v[156:159], v[28:31], v[76:79]\n"
    "ds_read_b128 v[28:31], v9 offset:31744\n"
    "v_mfma_f32_16x16x32_bf16 v[48:51], v[168:171], v[16:19], v[48:51]\n"
    "s_waitcnt lgkmcnt(2)\n"
    "s_mul_i32 s77, s77, 65536\n"
    "v_mfma_f32_16x16x32_bf16 v[72:75], v[180:183], v[16:19], v[72:75]\n"
    "ds_read_b128 v[16:19], v9 offset:32768\n"
    "v_mfma_f32_16x16x32_bf16 v[52:55], v[168:171], v[20:23], v[52:55]\n"
    "s_waitcnt lgkmcnt(2)\n"
    "s_add_u32 s77, s77, s59\n"
    "v_mfma_f32_16x16x32_bf16 v[76:79], v[180:183], v[20:23], v[76:79]\n"
    "ds_read_b128 v[20:23], v9 offset:33792\n"
    "v_mfma_f32_16x16x32_bf16 v[48:51], v[192:195], v[24:27], v[48:51]\n"
    "s_waitcnt lgkmcnt(2)\n"
    "v_mfma_f32_16x16x32_bf16 v[72:75], v[204:207], v[24:27], v[72:75]\n"
    "ds_read_b128 v[24:27], v9 offset:34816\n"
    "v_mfma_f32_16x16x32_bf16 v[52:55], v[192:195], v[28:31], v[52:55]\n"
    "s_waitcnt lgkmcnt(2)\n"
    "v_mfma_f32_16x16x32_bf16 v[76:79], v[204:207], v[28:31], v[76:79]\n"
    "ds_read_b128 v[28:31], v9 offset:35840\n"
    "v_mfma_f32_16x16x32_bf16 v[48:51], v[216:219], v[16:19], v[48:51]\n"
    "s_waitcnt lgkmcnt(2)\n"
    "v_mfma_f32_16x16x32_bf16 v[72:75], v[228:231], v[16:19], v[72:75]\n"
    "ds_read_b128 v[16:19], v9 offset:0\n"
    "v_mfma_f32_16x16x32_bf16 v[52:55], v[216:219], v[20:23], v[52:55]\n"
    "s_waitcnt lgkmcnt(2)\n"
    "v_mfma_f32_16x16x32_bf16 v[76:79], v[228:231], v[20:23], v[76:79]\n"
    "ds_read_b128 v[20:23], v9 offset:2048\n"
    "v_mfma_f32_16x16x32_bf16 v[48:51], v[240:243], v[24:27], v[48:51]\n"
    "s_waitcnt lgkmcnt(2)\n"
    "v_mfma_f32_16x16x32_bf16 v[72:75], v[252:255], v[24:27], v[72:75]\n"
    "ds_read_b128 v[24:27], v9 offset:4096\n"
    "v_mfma_f32_16x16x32_bf16 v[52:55], v[240:243], v[28:31], v[52:55]\n"
    "s_waitcnt lgkmcnt(2)\n"
    "v_mfma_f32_16x16x32_bf16 v[76:79], v[252:255], v[28:31], v[76:79]\n"
    "ds_read_b128 v[28:31], v9 offset:6144\n"
    "v_mfma_f32_16x16x32_bf16 v[32:35], a[0:3], v[16:19], 0\n"
    "v_mfma_f32_16x16x32_bf16 v[36:39], a[4:7], v[16:19], 0\n"
    "v_mul_f32 v72, v48, v72\n"
    "v_mul_f32 v73, v49, v73\n"
    "v_mfma_f32_16x16x32_bf16 v[56:59], a[12:15], v[16:19], 0\n"
    "s_waitcnt lgkmcnt(2)\n"
    "v_mul_f32 v74, v50, v74\n"
    "v_mul_f32 v75, v51, v75\n"
    "v_mfma_f32_16x16x32_bf16 v[60:63], a[16:19], v[16:19], 0\n"
    "ds_read_b128 v[16:19], v9 offset:8192\n"
    "v_mul_f32 v76, v52, v76\n"
    "v_mfma_f32_16x16x32_bf16 v[32:35], a[24:27], v[20:23], v[32:35]\n"
    "v_mul_f32 v77, v53, v77\n"
    "v_mul_f32 v78, v54, v78\n"
    "v_mfma_f32_16x16x32_bf16 v[36:39], a[28:31], v[20:23], v[36:39]\n"
    "v_mul_f32 v79, v55, v79\n"
    "v_mul_f32 v48, 0xbfb8aa3b, v48\n"
    "v_mfma_f32_16x16x32_bf16 v[56:59], a[36:39], v[20:23], v[56:59]\n"
    "s_waitcnt lgkmcnt(2)\n"
    "v_exp_f32 v48, v48\n"
    "v_mfma_f32_16x16x32_bf16 v[60:63], a[40:43], v[20:23], v[60:63]\n"
    "ds_read_b128 v[20:23], v9 offset:10240\n"
    "v_mul_f32 v49, 0xbfb8aa3b, v49\n"
    "v_mfma_f32_16x16x32_bf16 v[32:35], a[48:51], v[24:27], v[32:35]\n"
    "v_exp_f32 v49, v49\n"
    "v_mfma_f32_16x16x32_bf16 v[36:39], a[52:55], v[24:27], v[36:39]\n"
    "v_mul_f32 v50, 0xbfb8aa3b, v50\n"
    "v_mul_f32 v51, 0xbfb8aa3b, v51\n"
    "v_mfma_f32_16x16x32_bf16 v[56:59], a[60:63], v[24:27], v[56:59]\n"
    "s_waitcnt lgkmcnt(2)\n"
    "v_exp_f32 v50, v50\n"
    "v_mfma_f32_16x16x32_bf16 v[60:63], a[64:67], v[24:27], v[60:63]\n"
    "ds_read_b128 v[24:27], v9 offset:12288\n"
    "v_mul_f32 v52, 0xbfb8aa3b, v52\n"
    "v_mfma_f32_16x16x32_bf16 v[32:35], a[72:75], v[28:31], v[32:35]\n"
    "v_exp_f32 v51, v51\n"
    "v_mfma_f32_16x16x32_bf16 v[36:39], a[76:79], v[28:31], v[36:39]\n"
    "v_exp_f32 v52, v52\n"
    "v_mov_b32 v12, s74\n"
    "v_mfma_f32_16x16x32_bf16 v[56:59], a[84:87], v[28:31], v[56:59]\n"
    "s_waitcnt lgkmcnt(2)\n"
    "s_waitcnt vmcnt(0)\n"
    "v_mul_f32 v53, 0xbfb8aa3b, v53\n"
    "v_mul_f32 v54, 0xbfb8aa3b, v54\n"
    "v_add_u32 v13, v11, v3\n"
    "v_mfma_f32_16x16x32_bf16 v[60:63], a[88:91], v[28:31], v[60:63]\n"
    "ds_read_b128 v[28:31], v9 offset:14336\n"
    "buffer_atomic_smax v12, v14, s[36:39], 0 offen sc1\n"
    "v_mul_f32 v55, 0xbfb8aa3b, v55\n"
    "v_cmp_gt_i32 vcc, s71, v13\n"
    "v_mfma_f32_16x16x32_bf16 v[32:35], a[96:99], v[16:19], v[32:35]\n"
    "v_exp_f32 v53, v53\n"
    "v_mfma_f32_16x16x32_bf16 v[36:39], a[100:103], v[16:19], v[36:39]\n"
    "v_exp_f32 v54, v54\n"
    "v_mfma_f32_16x16x32_bf16 v[56:59], a[108:111], v[16:19], v[56:59]\n"
    "s_waitcnt lgkmcnt(2)\n"
    "v_exp_f32 v55, v55\n"
    ".LgUP_mid_%=:\n"
    "s_cbranch_vccnz .LgUP_chk0_%=\n"
    "v_mfma_f32_16x16x32_bf16 v[60:63], a[112:115], v[16:19], v[60:63]\n"
    "ds_read_b128 v[16:19], v9 offset:16384\n"
    "v_add_f32 v48, 1.0, v48\n"
    "v_mfma_f32_16x16x32_bf16 v[32:35], a[120:123], v[20:23], v[32:35]\n"
    "s_add_u32 m0, s77, s59\n"
    "s_add_u32 s77, s77, 8192\n"
    "buffer_load_dwordx4 v1, s[36:39], s76 offen sc1 lds\n"
    "v_rcp_f32 v48, v48\n"
    "v_mfma_f32_16x16x32_bf16 v[36:39], a[124:127], v[20:23], v[36:39]\n"
    "s_mov_b32 s70, 1\n"
    "v_add_f32 v49, 1.0, v49\n"
    "v_add_f32 v50, 1.0, v50\n"
    "v_mfma_f32_16x16x32_bf16 v[56:59], a[132:135], v[20:23], v[56:59]\n"
    "s_waitcnt lgkmcnt(2)\n"
    "buffer_load_dwordx4 v1, s[36:39], s76 offen offset:1024 sc1 lds\n"
    "s_add_u32 s76, s76, 16384\n"
    "v_rcp_f32 v49, v49\n"
    "v_mfma_f32_16x16x32_bf16 v[60:63], a[136:139], v[20:23], v[60:63]\n"
    "ds_read_b128 v[20:23], v9 offset:18432\n"
    "v_add_f32 v51, 1.0, v51\n"
    "v_mfma_f32_16x16x32_bf16 v[32:35], a[144:147], v[24:27], v[32:35]\n"
    "s_and_b32 s73, s68, s57\n"
    "v_rcp_f32 v50, v50\n"
    "v_mfma_f32_16x16x32_bf16 v[36:39], a[148:151], v[24:27], v[36:39]\n"
    "s_mul_i32 s73, s73, s56\n"
    "v_rcp_f32 v51, v51\n"
    "v_mfma_f32_16x16x32_bf16 v[56:59], a[156:159], v[24:27], v[56:59]\n"
    "s_waitcnt lgkmcnt(2)\n"
    "s_add_u32 m0, s77, s59\n"
    "s_add_u32 s77, s77, 8192\n"
    "buffer_load_dwordx4 v1, s[36:39], s76 offen sc1 lds\n"
    "v_add_f32 v52, 1.0, v52\n"
    "v_add_f32 v53, 1.0, v53\n"
    "v_mfma_f32_16x16x32_bf16 v[60:63], a[160:163], v[24:27], v[60:63]\n"
    "ds_read_b128 v[24:27], v9 offset:20480\n"
    "v_add_f32 v54, 1.0, v54\n"
    "v_mfma_f32_16x16x32_bf16 v[32:35], a[168:171], v[28:31], v[32:35]\n"
    "s_add_u32 s73, s73, s55\n"
    "v_rcp_f32 v52, v52\n"
    "v_mfma_f32_16x16x32_bf16 v[36:39], a[172:175], v[28:31], v[36:39]\n"
    "buffer_load_dwordx4 v1, s[36:39], s76 offen offset:1024 sc1 lds\n"
    "s_add_u32 s76, s76, 16384\n"
    "v_rcp_f32 v53, v53\n"
    "v_mfma_f32_16x16x32_bf16 v[56:59], a[180:183], v[28:31], v[56:59]\n"
    "s_waitcnt lgkmcnt(2)\n"
    "v_rcp_f32 v54, v54\n"
    "v_mfma_f32_16x16x32_bf16 v[60:63], a[184:187], v[28:31], v[60:63]\n"
    "ds_read_b128 v[28:31], v9 offset:22528\n"
    "v_add_f32 v55, 1.0, v55\n"
    "v_mfma_f32_16x16x32_bf16 v[32:35], a[192:195], v[16:19], v[32:35]\n"
    "v_rcp_f32 v55, v55\n"
    "v_mfma_f32_16x16x32_bf16 v[36:39], a[196:199], v[16:19], v[36:39]\n"
    "s_add_u32 m0, s77, s59\n"
    "s_add_u32 s77, s77, 8192\n"
    "buffer_load_dwordx4 v1, s[36:39], s76 offen sc1 lds\n"
    "v_mul_f32 v48, v48, v72\n"
    "v_mul_f32 v49, v49, v73\n"
    "v_mfma_f32_16x16x32_bf16 v[56:59], a[204:207], v[16:19], v[56:59]\n"
    "s_waitcnt lgkmcnt(2)\n"
    "v_mul_f32 v50, v50, v74\n"
    "v_mul_f32 v51, v51, v75\n"
    "v_mfma_f32_16x16x32_bf16 v[60:63], a[208:211], v[16:19], v[60:63]\n"
    "ds_read_b128 v[16:19], v9 offset:24576\n"
    "v_mul_f32 v52, v52, v76\n"
    "v_mfma_f32_16x16x32_bf16 v[32:35], a[216:219], v[20:23], v[32:35]\n"
    "buffer_load_dwordx4 v1, s[36:39], s76 offen offset:1024 sc1 lds\n"
    "s_add_u32 s76, s76, 16384\n"
    "v_mul_f32 v53, v53, v77\n"
    "v_mul_f32 v54, v54, v78\n"
    "v_mfma_f32_16x16x32_bf16 v[36:39], a[220:223], v[20:23], v[36:39]\n"
    "v_mul_f32 v55, v55, v79\n"
    "v_cvt_pk_bf16_f32 v48, v48, v49\n"
    "v_mfma_f32_16x16x32_bf16 v[56:59], a[228:231], v[20:23], v[56:59]\n"
    "s_waitcnt lgkmcnt(2)\n"
    "v_cvt_pk_bf16_f32 v49, v50, v51\n"
    "v_cvt_pk_bf16_f32 v50, v52, v53\n"
    "v_mfma_f32_16x16x32_bf16 v[60:63], a[232:235], v[20:23], v[60:63]\n"
    "ds_read_b128 v[20:23], v9 offset:26624\n"
    "v_cvt_pk_bf16_f32 v51, v54, v55\n"
    "v_mfma_f32_16x16x32_bf16 v[32:35], a[240:243], v[24:27], v[32:35]\n"
    "s_add_u32 m0, s77, s59\n"
    "s_add_u32 s77, s77, 8192\n"
    "buffer_load_dwordx4 v1, s[36:39], s76 offen sc1 lds\n"
    "s_nop 0\n"
    "v_mfma_f32_16x16x32_bf16 v[36:39], a[244:247], v[24:27], v[36:39]\n"
    "v_permlane16_swap_b32 v48, v50\n"
    "v_mfma_f32_16x16x32_bf16 v[56:59], a[252:255], v[24:27], v[56:59]\n"
    "s_waitcnt lgkmcnt(2)\n"
    "buffer_load_dwordx4 v1, s[36:39], s76 offen offset:1024 sc1 lds\n"
    "s_add_u32 s76, s76, 16384\n"
    "v_permlane16_swap_b32 v49, v51\n"
    "v_mfma_f32_16x16x32_bf16 v[60:63], v[80:83], v[24:27], v[60:63]\n"
    "ds_read_b128 v[24:27], v9 offset:28672\n"
    "v_mfma_f32_16x16x32_bf16 v[32:35], v[88:91], v[28:31], v[32:35]\n"
    "v_mfma_f32_16x16x32_bf16 v[36:39], v[92:95], v[28:31], v[36:39]\n"
    "v_mfma_f32_16x16x32_bf16 v[56:59], v[100:103], v[28:31], v[56:59]\n"
    "s_waitcnt lgkmcnt(2)\n"
    "s_mov_b32 m0, s77\n"
    "s_nop 0\n"
    "buffer_load_dwordx4 v8, s[36:39], s76 offen sc1 lds\n"
    "v_mfma_f32_16x16x32_bf16 v[60:63], v[104:107], v[28:31], v[60:63]\n"
    "ds_read_b128 v[28:31], v9 offset:30720\n"
    "v_mfma_f32_16x16x32_bf16 v[32:35], v[112:115], v[16:19], v[32:35]\n"
    "v_mfma_f32_16x16x32_bf16 v[36:39], v[116:119], v[16:19], v[36:39]\n"
    "v_mfma_f32_16x16x32_bf16 v[56:59], v[124:127], v[16:19], v[56:59]\n"
    "s_waitcnt lgkmcnt(2)\n"
    ".LgUP_seg0_%=:\n"
    "s_cmp_lg_u32 s70, 0\n"
    "s_cbranch_scc0 .LgUP_chk1_%=\n"
    ".LgUP_plain1_%=:\n"
    "v_mfma_f32_16x16x32_bf16 v[60:63], v[128:131], v[16:19], v[60:63]\n"
    "ds_read_b128 v[16:19], v9 offset:32768\n"
    "v_mfma_f32_16x16x32_bf16 v[32:35], v[136:139], v[20:23], v[32:35]\n"
    "v_mfma_f32_16x16x32_bf16 v[36:39], v[140:143], v[20:23], v[36:39]\n"
    "v_mfma_f32_16x16x32_bf16 v[56:59], v[148:151], v[20:23], v[56:59]\n"
    "s_waitcnt lgkmcnt(2)\n"
    "v_mfma_f32_16x16x32_bf16 v[60:63], v[152:155], v[20:23], v[60:63]\n"
    "ds_read_b128 v[20:23], v9 offset:34816\n"
    "v_mfma_f32_16x16x32_bf16 v[32:35], v[160:163], v[24:27], v[32:35]\n"
    "v_mfma_f32_16x16x32_bf16 v[36:39], v[164:167], v[24:27], v[36:39]\n"
    "v_mfma_f32_16x16x32_bf16 v[56:59], v[172:175], v[24:27], v[56:59]\n"
    "s_waitcnt lgkmcnt(2)\n"
    "v_mfma_f32_16x16x32_bf16 v[60:63], v[176:179], v[24:27], v[60:63]\n"
    "ds_read_b128 v[24:27], v9 offset:1024\n"
    "v_mfma_f32_16x16x32_bf16 v[32:35], v[184:187], v[28:31], v[32:35]\n"
    "v_mfma_f32_16x16x32_bf16 v[36:39], v[188:191], v[28:31], v[36:39]\n"
    "v_mfma_f32_16x16x32_bf16 v[56:59], v[196:199], v[28:31], v[56:59]\n"
    "s_waitcnt lgkmcnt(2)\n"
    "v_mfma_f32_16x16x32_bf16 v[60:63], v[200:203], v[28:31], v[60:63]\n"
    "ds_read_b128 v[28:31], v9 offset:3072\n"
    "v_mfma_f32_16x16x32_bf16 v[32:35], v[208:211], v[16:19], v[32:35]\n"
    "v_mfma_f32_16x16x32_bf16 v[36:39], v[212:215], v[16:19], v[36:39]\n"
    "v_mfma_f32_16x16x32_bf16 v[56:59], v[220:223], v[16:19], v[56:59]\n"
    "s_waitcnt lgkmcnt(2)\n"
    "v_mfma_f32_16x16x32_bf16 v[60:63], v[224:227], v[16:19], v[60:63]\n"
    "ds_read_b128 v[16:19], v9 offset:5120\n"
    "v_mfma_f32_16x16x32_bf16 v[32:35], v[232:235], v[20:23], v[32:35]\n"
    "v_mfma_f32_16x16x32_bf16 v[36:39], v[236:239], v[20:23], v[36:39]\n"
    "v_mfma_f32_16x16x32_bf16 v[56:59], v[244:247], v[20:23], v[56:59]\n"
    "s_waitcnt lgkmcnt(2)\n"
    "v_mfma_f32_16x16x32_bf16 v[60:63], v[248:251], v[20:23], v[60:63]\n"
    "ds_read_b128 v[20:23], v9 offset:7168\n"
    "v_mfma_f32_16x16x32_bf16 v[40:43], a[0:3], v[24:27], 0\n"
    "v_mfma_f32_16x16x32_bf16 v[44:47], a[4:7], v[24:27], 0\n"
    "v_mul_f32 v56, v32, v56\n"
    "v_mul_f32 v57, v33, v57\n"
    "v_mfma_f32_16x16x32_bf16 v[64:67], a[12:15], v[24:27], 0\n"
    "s_waitcnt lgkmcnt(2)\n"
    "v_mul_f32 v58, v34, v58\n"
    "v_mul_f32 v59, v35, v59\n"
    "v_mfma_f32_16x16x32_bf16 v[68:71], a[16:19], v[24:27], 0\n"
    "ds_read_b128 v[24:27], v9 offset:9216\n"
    "v_mul_f32 v60, v36, v60\n"
    "v_mfma_f32_16x16x32_bf16 v[40:43], a[24:27], v[28:31], v[40:43]\n"
    "v_mul_f32 v61, v37, v61\n"
    "v_mul_f32 v62, v38, v62\n"
    "v_mfma_f32_16x16x32_bf16 v[44:47], a[28:31], v[28:31], v[44:47]\n"
    "v_mul_f32 v63, v39, v63\n"
    "v_mul_f32 v32, 0xbfb8aa3b, v32\n"
    "v_mfma_f32_16x16x32_bf16 v[64:67], a[36:39], v[28:31], v[64:67]\n"
    "s_waitcnt lgkmcnt(2)\n"
    "v_exp_f32 v32, v32\n"
    "v_mfma_f32_16x16x32_bf16 v[68:71], a[40:43], v[28:31], v[68:71]\n"
    "ds_read_b128 v[28:31], v9 offset:11264\n"
    "v_mul_f32 v33, 0xbfb8aa3b, v33\n"
    "v_mfma_f32_16x16x32_bf16 v[40:43], a[48:51], v[16:19], v[40:43]\n"
    "v_exp_f32 v33, v33\n"
    ".LgUP_seg1_%=:\n"
    "s_cmp_lg_u32 s70, 0\n"
    "s_cbranch_scc0 .LgUP_chk2_%=\n"
    ".LgUP_plain2_%=:\n"
    "v_mfma_f32_16x16x32_bf16 v[44:47], a[52:55], v[16:19], v[44:47]\n"
    "v_mul_f32 v34, 0xbfb8aa3b, v34\n"
    "v_mul_f32 v35, 0xbfb8aa3b, v35\n"
    "v_mfma_f32_16x16x32_bf16 v[64:67], a[60:63], v[16:19], v[64:67]\n"
    "s_waitcnt lgkmcnt(2)\n"
    "v_exp_f32 v34, v34\n"
    "v_mfma_f32_16x16x32_bf16 v[68:71], a[64:67], v[16:19], v[68:71]\n"
    "ds_read_b128 v[16:19], v9 offset:13312\n"
    "v_mul_f32 v36, 0xbfb8aa3b, v36\n"
    "v_mfma_f32_16x16x32_bf16 v[40:43], a[72:75], v[20:23], v[40:43]\n"
    "v_exp_f32 v35, v35\n"
    "v_mfma_f32_16x16x32_bf16 v[44:47], a[76:79], v[20:23], v[44:47]\n"
    "v_exp_f32 v36, v36\n"
    "v_mfma_f32_16x16x32_bf16 v[64:67], a[84:87], v[20:23], v[64:67]\n"
    "s_waitcnt lgkmcnt(2)\n"
    "v_mul_f32 v37, 0xbfb8aa3b, v37\n"
    "v_mul_f32 v38, 0xbfb8aa3b, v38\n"
    "v_mfma_f32_16x16x32_bf16 v[68:71], a[88:91], v[20:23], v[68:71]\n"
    "ds_read_b128 v[20:23], v9 offset:15360\n"
    "v_mul_f32 v39, 0xbfb8aa3b, v39\n"
    "v_mfma_f32_16x16x32_bf16 v[40:43], a[96:99], v[24:27], v[40:43]\n"
    "v_exp_f32 v37, v37\n"
    "v_mfma_f32_16x16x32_bf16 v[44:47], a[100:103], v[24:27], v[44:47]\n"
    "v_exp_f32 v38, v38\n"
    "v_mfma_f32_16x16x32_bf16 v[64:67], a[108:111], v[24:27], v[64:67]\n"
    "s_waitcnt lgkmcnt(2)\n"
    "v_exp_f32 v39, v39\n"
    "v_mfma_f32_16x16x32_bf16 v[68:71], a[112:115], v[24:27], v[68:71]\n"
    "ds_read_b128 v[24:27], v9 offset:17408\n"
    "v_add_f32 v32, 1.0, v32\n"
    "v_mfma_f32_16x16x32_bf16 v[40:43], a[120:123], v[28:31], v[40:43]\n"
    "v_rcp_f32 v32, v32\n"
    "v_mfma_f32_16x16x32_bf16 v[44:47], a[124:127], v[28:31], v[44:47]\n"
    "v_add_f32 v33, 1.0, v33\n"
    "v_add_f32 v34, 1.0, v34\n"
    "v_mfma_f32_16x16x32_bf16 v[64:67], a[132:135], v[28:31], v[64:67]\n"
    "s_waitcnt lgkmcnt(2)\n"
    "v_rcp_f32 v33, v33\n"
    "v_mfma_f32_16x16x32_bf16 v[68:71], a[136:139], v[28:31], v[68:71]\n"
    "ds_read_b128 v[28:31], v9 offset:19456\n"
    "v_add_f32 v35, 1.0, v35\n"
    "v_mfma_f32_16x16x32_bf16 v[40:43], a[144:147], v[16:19], v[40:43]\n"
    "v_rcp_f32 v34, v34\n"
    "v_mfma_f32_16x16x32_bf16 v[44:47], a[148:151], v[16:19], v[44:47]\n"
    "v_rcp_f32 v35, v35\n"
    "v_mfma_f32_16x16x32_bf16 v[64:67], a[156:159], v[16:19], v[64:67]\n"
    "s_waitcnt lgkmcnt(2)\n"
    "v_add_f32 v36, 1.0, v36\n"
    "v_add_f32 v37, 1.0, v37\n"
    "v_mfma_f32_16x16x32_bf16 v[68:71], a[160:163], v[16:19], v[68:71]\n"
    "ds_read_b128 v[16:19], v9 offset:21504\n"
    "v_add_f32 v38, 1.0, v38\n"
    "v_mfma_f32_16x16x32_bf16 v[40:43], a[168:171], v[20:23], v[40:43]\n"
    "v_rcp_f32 v36, v36\n"
    "v_mfma_f32_16x16x32_bf16 v[44:47], a[172:175], v[20:23], v[44:47]\n"
    "v_rcp_f32 v37, v37\n"
    "v_mfma_f32_16x16x32_bf16 v[64:67], a[180:183], v[20:23], v[64:67]\n"
    "s_waitcnt lgkmcnt(2)\n"
    "v_rcp_f32 v38, v38\n"
    "v_mfma_f32_16x16x32_bf16 v[68:71], a[184:187], v[20:23], v[68:71]\n"
    "ds_read_b128 v[20:23], v9 offset:23552\n"
    "v_add_f32 v39, 1.0, v39\n"
    "v_mfma_f32_16x16x32_bf16 v[40:43], a[192:195], v[24:27], v[40:43]\n"
    "v_rcp_f32 v39, v39\n"
    "v_mfma_f32_16x16x32_bf16 v[44:47], a[196:199], v[24:27], v[44:47]\n"
    "v_mul_f32 v32, v32, v56\n"
    "v_mul_f32 v33, v33, v57\n"
    "v_mfma_f32_16x16x32_bf16 v[64:67], a[204:207], v[24:27], v[64:67]\n"
    "s_waitcnt lgkmcnt(2)\n"
    "v_mul_f32 v34, v34, v58\n"
    "v_mul_f32 v35, v35, v59\n"
    "v_mfma_f32_16x16x32_bf16 v[68:71], a[208:211], v[24:27], v[68:71]\n"
    "ds_read_b128 v[24:27], v9 offset:25600\n"
    "v_mul_f32 v36, v36, v60\n"
    "v_mfma_f32_16x16x32_bf16 v[40:43], a[216:219], v[28:31], v[40:43]\n"
    "v_mul_f32 v37, v37, v61\n"
    "v_mul_f32 v38, v38, v62\n"
    "v_mfma_f32_16x16x32_bf16 v[44:47], a[220:223], v[28:31], v[44:47]\n"
    "v_mul_f32 v39, v39, v63\n"
    "v_cvt_pk_bf16_f32 v32, v32, v33\n"
    "v_mfma_f32_16x16x32_bf16 v[64:67], a[228:231], v[28:31], v[64:67]\n"
    "s_waitcnt lgkmcnt(2)\n"
    "v_cvt_pk_bf16_f32 v33, v34, v35\n"
    "v_cvt_pk_bf16_f32 v34, v36, v37\n"
    "v_mfma_f32_16x16x32_bf16 v[68:71], a[232:235], v[28:31], v[68:71]\n"
    "ds_read_b128 v[28:31], v9 offset:27648\n"
    "v_cvt_pk_bf16_f32 v35, v38, v39\n"
    ".LgUP_seg2_%=:\n"
    "s_cmp_lg_u32 s70, 1\n"
    "s_cbranch_scc1 .LgUP_slow_%=\n"
    "v_mfma_f32_16x16x32_bf16 v[40:43], a[240:243], v[16:19], v[40:43]\n"
    "s_mov_b32 s68, s71\n"
    "buffer_store_dwordx4 v[48:51], v4, s[36:39], s73 offen sc1\n"
    "v_mfma_f32_16x16x32_bf16 v[44:47], a[244:247], v[16:19], v[44:47]\n"
    "s_add_u32 s71, s68, 1\n"
    "v_mfma_f32_16x16x32_bf16 v[64:67], a[252:255], v[16:19], v[64:67]\n"
    "s_waitcnt lgkmcnt(2)\n"
    "s_cmp_eq_u32 s71, 64\n"
    "buffer_store_dwordx4 v[32:35], v15, s[36:39], s73 offen sc1\n"
    "v_mfma_f32_16x16x32_bf16 v[68:71], v[80:83], v[16:19], v[68:71]\n"
    "ds_read_b128 v[16:19], v9 offset:29696\n"
    "v_mfma_f32_16x16x32_bf16 v[40:43], v[88:91], v[20:23], v[40:43]\n"
    "s_cselect_b32 s70, 2, 0\n"
    "v_mfma_f32_16x16x32_bf16 v[44:47], v[92:95], v[20:23], v[44:47]\n"
    "v_mfma_f32_16x16x32_bf16 v[64:67], v[100:103], v[20:23], v[64:67]\n"
    "s_waitcnt lgkmcnt(2)\n"
    "v_mfma_f32_16x16x32_bf16 v[68:71], v[104:107], v[20:23], v[68:71]\n"
    "ds_read_b128 v[20:23], v9 offset:31744\n"
    "v_mfma_f32_16x16x32_bf16 v[40:43], v[112:115], v[24:27], v[40:43]\n"
    "v_mfma_f32_16x16x32_bf16 v[44:47], v[116:119], v[24:27], v[44:47]\n"
    "v_mfma_f32_16x16x32_bf16 v[64:67], v[124:127], v[24:27], v[64:67]\n"
    "s_waitcnt lgkmcnt(2)\n"
    "v_mfma_f32_16x16x32_bf16 v[68:71], v[128:131], v[24:27], v[68:71]\n"
    "ds_read_b128 v[24:27], v9 offset:33792\n"
    "v_mfma_f32_16x16x32_bf16 v[40:43], v[136:139], v[28:31], v[40:43]\n"
    "v_mfma_f32_16x16x32_bf16 v[44:47], v[140:143], v[28:31], v[44:47]\n"
    "v_mfma_f32_16x16x32_bf16 v[64:67], v[148:151], v[28:31], v[64:67]\n"
    "s_waitcnt lgkmcnt(2)\n"
    "v_mfma_f32_16x16x32_bf16 v[68:71], v[152:155], v[28:31], v[68:71]\n"
    "ds_read_b128 v[28:31], v9 offset:35840\n"
    "v_add_u32 v13, v11, v3\n"
    "v_mfma_f32_16x16x32_bf16 v[40:43], v[160:163], v[16:19], v[40:43]\n"
    "v_cmp_gt_i32 vcc, s71, v13\n"
    "v_mfma_f32_16x16x32_bf16 v[44:47], v[164:167], v[16:19], v[44:47]\n"
    "v_mfma_f32_16x16x32_bf16 v[64:67], v[172:175], v[16:19], v[64:67]\n"
    "s_waitcnt lgkmcnt(2)\n"
    "v_xor_b32 v9, s78, v9\n"
    "v_mfma_f32_16x16x32_bf16 v[68:71], v[176:179], v[16:19], v[68:71]\n"
    "s_waitcnt vmcnt(2) lgkmcnt(0)\n"
    "s_barrier\n"
    "ds_read_b128 v[16:19], v9 offset:0\n"
    "v_mfma_f32_16x16x32_bf16 v[40:43], v[184:187], v[20:23], v[40:43]\n"
    "s_or_b64 exec, vcc, 1\n"
    "buffer_atomic_smax v11, v2, s[36:39], 0 offen sc0 sc1\n"
    "s_mov_b64 exec, -1\n"
    "v_mfma_f32_16x16x32_bf16 v[44:47], v[188:191], v[20:23], v[44:47]\n"
    "v_mfma_f32_16x16x32_bf16 v[64:67], v[196:199], v[20:23], v[64:67]\n"
    "v_mfma_f32_16x16x32_bf16 v[68:71], v[200:203], v[20:23], v[68:71]\n"
    "ds_read_b128 v[20:23], v9 offset:1024\n"
    "v_mfma_f32_16x16x32_bf16 v[40:43], v[208:211], v[24:27], v[40:43]\n"
    "v_mfma_f32_16x16x32_bf16 v[44:47], v[212:215], v[24:27], v[44:47]\n"
    "v_mfma_f32_16x16x32_bf16 v[64:67], v[220:223], v[24:27], v[64:67]\n"
    "v_mfma_f32_16x16x32_bf16 v[68:71], v[224:227], v[24:27], v[68:71]\n"
    "ds_read_b128 v[24:27], v9 offset:2048\n"
    "v_mfma_f32_16x16x32_bf16 v[40:43], v[232:235], v[28:31], v[40:43]\n"
    "v_mfma_f32_16x16x32_bf16 v[44:47], v[236:239], v[28:31], v[44:47]\n"
    "v_mfma_f32_16x16x32_bf16 v[64:67], v[244:247], v[28:31], v[64:67]\n"
    "s_waitcnt lgkmcnt(2)\n"
    "v_mfma_f32_16x16x32_bf16 v[68:71], v[248:251], v[28:31], v[68:71]\n"
    "ds_read_b128 v[28:31], v9 offset:3072\n"
    "s_branch .LgUP_head_%=\n"
    ".LgUP_LOOPE_%=:\n"
    ".LgUP_done_%=:\n"
    "s_waitcnt vmcnt(0)\n"
    "s_mov_b32 s74, 63\n"
    "v_mov_b32 v12, s74\n"
    "s_mov_b64 exec, s[62:63]\n"
    "buffer_atomic_smax v12, v14, s[36:39], 0 offen sc1\n"
    "s_mov_b64 exec, -1\n"
    "s_waitcnt vmcnt(0)\n"
    "s_branch .LgUP_FIN_%=\n"
    ".LgUP_chk0_%=:\n"
    "s_or_b64 exec, vcc, 1\n"
    "buffer_atomic_smax v11, v2, s[36:39], 0 offen sc0 sc1\n"
    "s_mov_b64 exec, -1\n"
    "v_mfma_f32_16x16x32_bf16 v[60:63], a[112:115], v[16:19], v[60:63]\n"
    "ds_read_b128 v[16:19], v9 offset:16384\n"
    "v_add_f32 v48, 1.0, v48\n"
    "v_mfma_f32_16x16x32_bf16 v[32:35], a[120:123], v[20:23], v[32:35]\n"
    "s_and_b32 s73, s68, s57\n"
    "v_rcp_f32 v48, v48\n"
    "v_mfma_f32_16x16x32_bf16 v[36:39], a[124:127], v[20:23], v[36:39]\n"
    "s_mul_i32 s73, s73, s56\n"
    "v_add_f32 v49, 1.0, v49\n"
    "v_add_f32 v50, 1.0, v50\n"
    "v_mfma_f32_16x16x32_bf16 v[56:59], a[132:135], v[20:23], v[56:59]\n"
    "s_waitcnt lgkmcnt(2)\n"
    "v_rcp_f32 v49, v49\n"
    "v_mfma_f32_16x16x32_bf16 v[60:63], a[136:139], v[20:23], v[60:63]\n"
    "ds_read_b128 v[20:23], v9 offset:18432\n"
    "v_add_f32 v51, 1.0, v51\n"
    "v_mfma_f32_16x16x32_bf16 v[32:35], a[144:147], v[24:27], v[32:35]\n"
    "s_add_u32 s73, s73, s55\n"
    "v_rcp_f32 v50, v50\n"
    "v_mfma_f32_16x16x32_bf16 v[36:39], a[148:151], v[24:27], v[36:39]\n"
    "v_rcp_f32 v51, v51\n"
    "v_mfma_f32_16x16x32_bf16 v[56:59], a[156:159], v[24:27], v[56:59]\n"
    "s_waitcnt lgkmcnt(2)\n"
    "v_add_f32 v52, 1.0, v52\n"
    "v_add_f32 v53, 1.0, v53\n"
    "v_mfma_f32_16x16x32_bf16 v[60:63], a[160:163], v[24:27], v[60:63]\n"
    "ds_read_b128 v[24:27], v9 offset:20480\n"
    "v_add_f32 v54, 1.0, v54\n"
    "v_mfma_f32_16x16x32_bf16 v[32:35], a[168:171], v[28:31], v[32:35]\n"
    "v_rcp_f32 v52, v52\n"
    "v_mfma_f32_16x16x32_bf16 v[36:39], a[172:175], v[28:31], v[36:39]\n"
    "v_rcp_f32 v53, v53\n"
    "v_mfma_f32_16x16x32_bf16 v[56:59], a[180:183], v[28:31], v[56:59]\n"
    "s_waitcnt lgkmcnt(2)\n"
    "v_rcp_f32 v54, v54\n"
    "v_mfma_f32_16x16x32_bf16 v[60:63], a[184:187], v[28:31], v[60:63]\n"
    "ds_read_b128 v[28:31], v9 offset:22528\n"
    "v_add_f32 v55, 1.0, v55\n"
    "v_mfma_f32_16x16x32_bf16 v[32:35], a[192:195], v[16:19], v[32:35]\n"
    "v_rcp_f32 v55, v55\n"
    "v_mfma_f32_16x16x32_bf16 v[36:39], a[196:199], v[16:19], v[36:39]\n"
    "v_mul_f32 v48, v48, v72\n"
    "v_mul_f32 v49, v49, v73\n"
    "v_mfma_f32_16x16x32_bf16 v[56:59], a[204:207], v[16:19], v[56:59]\n"
    "s_waitcnt lgkmcnt(2)\n"
    "v_mul_f32 v50, v50, v74\n"
    "v_mul_f32 v51, v51, v75\n"
    "v_mfma_f32_16x16x32_bf16 v[60:63], a[208:211], v[16:19], v[60:63]\n"
    "ds_read_b128 v[16:19], v9 offset:24576\n"
    "v_mul_f32 v52, v52, v76\n"
    "v_mfma_f32_16x16x32_bf16 v[32:35], a[216:219], v[20:23], v[32:35]\n"
    "v_mul_f32 v53, v53, v77\n"
    "v_mul_f32 v54, v54, v78\n"
    "v_mfma_f32_16x16x32_bf16 v[36:39], a[220:223], v[20:23], v[36:39]\n"
    "v_mul_f32 v55, v55, v79\n"
    "v_cvt_pk_bf16_f32 v48, v48, v49\n"
    "v_mfma_f32_16x16x32_bf16 v[56:59], a[228:231], v[20:23], v[56:59]\n"
    "s_waitcnt lgkmcnt(2)\n"
    "v_cvt_pk_bf16_f32 v49, v50, v51\n"
    "v_cvt_pk_bf16_f32 v50, v52, v53\n"
    "v_mfma_f32_16x16x32_bf16 v[60:63], a[232:235], v[20:23], v[60:63]\n"
    "ds_read_b128 v[20:23], v9 offset:26624\n"
    "v_cvt_pk_bf16_f32 v51, v54, v55\n"
    "v_mfma_f32_16x16x32_bf16 v[32:35], a[240:243], v[24:27], v[32:35]\n"
    "s_nop 0\n"
    "v_mfma_f32_16x16x32_bf16 v[36:39], a[244:247], v[24:27], v[36:39]\n"
    "v_permlane16_swap_b32 v48, v50\n"
    "v_mfma_f32_16x16x32_bf16 v[56:59], a[252:255], v[24:27], v[56:59]\n"
    "s_waitcnt lgkmcnt(2)\n"
    "v_permlane16_swap_b32 v49, v51\n"
    "v_mfma_f32_16x16x32_bf16 v[60:63], v[80:83], v[24:27], v[60:63]\n"
    "ds_read_b128 v[24:27], v9 offset:28672\n"
    "v_mfma_f32_16x16x32_bf16 v[32:35], v[88:91], v[28:31], v[32:35]\n"
    "v_mfma_f32_16x16x32_bf16 v[36:39], v[92:95], v[28:31], v[36:39]\n"
    "v_mfma_f32_16x16x32_bf16 v[56:59], v[100:103], v[28:31], v[56:59]\n"
    "s_waitcnt lgkmcnt(2)\n"
    "v_mfma_f32_16x16x32_bf16 v[60:63], v[104:107], v[28:31], v[60:63]\n"
    "ds_read_b128 v[28:31], v9 offset:30720\n"
    "v_mfma_f32_16x16x32_bf16 v[32:35], v[112:115], v[16:19], v[32:35]\n"
    "v_mfma_f32_16x16x32_bf16 v[36:39], v[116:119], v[16:19], v[36:39]\n"
    "v_mfma_f32_16x16x32_bf16 v[56:59], v[124:127], v[16:19], v[56:59]\n"
    "s_waitcnt lgkmcnt(2)\n"
    "s_branch .LgUP_seg0_%=\n"
    ".LgUP_chk1_%=:\n"
    "s_waitcnt vmcnt(0)\n"
    "v_add_u32 v13, v11, v3\n"
    "v_cmp_gt_i32 vcc, s71, v13\n"
    "s_cbranch_vccz .LgUP_dma1_%=\n"
    "s_or_b64 exec, vcc, 1\n"
    "buffer_atomic_smax v11, v2, s[36:39], 0 offen sc0 sc1\n"
    "s_mov_b64 exec, -1\n"
    "s_branch .LgUP_plain1_%=\n"
    ".LgUP_dma1_%=:\n"
    "s_mov_b32 s70, 1\n"
    "v_mfma_f32_16x16x32_bf16 v[60:63], v[128:131], v[16:19], v[60:63]\n"
    "ds_read_b128 v[16:19], v9 offset:32768\n"
    "v_mfma_f32_16x16x32_bf16 v[32:35], v[136:139], v[20:23], v[32:35]\n"
    "s_add_u32 m0, s77, s59\n"
    "s_add_u32 s77, s77, 8192\n"
    "buffer_load_dwordx4 v1, s[36:39], s76 offen sc1 lds\n"
    "v_mfma_f32_16x16x32_bf16 v[36:39], v[140:143], v[20:23], v[36:39]\n"
    "v_mfma_f32_16x16x32_bf16 v[56:59], v[148:151], v[20:23], v[56:59]\n"
    "s_waitcnt lgkmcnt(2)\n"
    "buffer_load_dwordx4 v1, s[36:39], s76 offen offset:1024 sc1 lds\n"
    "s_add_u32 s76, s76, 16384\n"
    "v_mfma_f32_16x16x32_bf16 v[60:63], v[152:155], v[20:23], v[60:63]\n"
    "ds_read_b128 v[20:23], v9 offset:34816\n"
    "v_mfma_f32_16x16x32_bf16 v[32:35], v[160:163], v[24:27], v[32:35]\n"
    "v_mfma_f32_16x16x32_bf16 v[36:39], v[164:167], v[24:27], v[36:39]\n"
    "s_add_u32 m0, s77, s59\n"
    "s_add_u32 s77, s77, 8192\n"
    "buffer_load_dwordx4 v1, s[36:39], s76 offen sc1 lds\n"
    "v_mfma_f32_16x16x32_bf16 v[56:59], v[172:175], v[24:27], v[56:59]\n"
    "s_waitcnt lgkmcnt(2)\n"
    "v_mfma_f32_16x16x32_bf16 v[60:63], v[176:179], v[24:27], v[60:63]\n"
    "ds_read_b128 v[24:27], v9 offset:1024\n"
    "v_mfma_f32_16x16x32_bf16 v[32:35], v[184:187], v[28:31], v[32:35]\n"
    "v_mfma_f32_16x16x32_bf16 v[36:39], v[188:191], v[28:31], v[36:39]\n"
    "buffer_load_dwordx4 v1, s[36:39], s76 offen offset:1024 sc1 lds\n"
    "s_add_u32 s76, s76, 16384\n"
    "v_mfma_f32_16x16x32_bf16 v[56:59], v[196:199], v[28:31], v[56:59]\n"
    "s_waitcnt lgkmcnt(2)\n"
    "v_mfma_f32_16x16x32_bf16 v[60:63], v[200:203], v[28:31], v[60:63]\n"
    "ds_read_b128 v[28:31], v9 offset:3072\n"
    "v_mfma_f32_16x16x32_bf16 v[32:35], v[208:211], v[16:19], v[32:35]\n"
    "s_add_u32 m0, s77, s59\n"
    "s_add_u32 s77, s77, 8192\n"
    "buffer_load_dwordx4 v1, s[36:39], s76 offen sc1 lds\n"
    "v_mfma_f32_16x16x32_bf16 v[36:39], v[212:215], v[16:19], v[36:39]\n"
    "v_mfma_f32_16x16x32_bf16 v[56:59], v[220:223], v[16:19], v[56:59]\n"
    "s_waitcnt lgkmcnt(2)\n"
    "buffer_load_dwordx4 v1, s[36:39], s76 offen offset:1024 sc1 lds\n"
    "s_add_u32 s76, s76, 16384\n"
    "v_mfma_f32_16x16x32_bf16 v[60:63], v[224:227], v[16:19], v[60:63]\n"
    "ds_read_b128 v[16:19], v9 offset:5120\n"
    "v_mfma_f32_16x16x32_bf16 v[32:35], v[232:235], v[20:23], v[32:35]\n"
    "v_mfma_f32_16x16x32_bf16 v[36:39], v[236:239], v[20:23], v[36:39]\n"
    "v_mfma_f32_16x16x32_bf16 v[56:59], v[244:247], v[20:23], v[56:59]\n"
    "s_waitcnt lgkmcnt(2)\n"
    "s_add_u32 m0, s77, s59\n"
    "s_add_u32 s77, s77, 8192\n"
    "buffer_load_dwordx4 v1, s[36:39], s76 offen sc1 lds\n"
    "v_mfma_f32_16x16x32_bf16 v[60:63], v[248:251], v[20:23], v[60:63]\n"
    "ds_read_b128 v[20:23], v9 offset:7168\n"
    "v_mfma_f32_16x16x32_bf16 v[40:43], a[0:3], v[24:27], 0\n"
    "v_mfma_f32_16x16x32_bf16 v[44:47], a[4:7], v[24:27], 0\n"
    "buffer_load_dwordx4 v1, s[36:39], s76 offen offset:1024 sc1 lds\n"
    "s_add_u32 s76, s76, 16384\n"
    "v_mul_f32 v56, v32, v56\n"
    "v_mul_f32 v57, v33, v57\n"
    "v_mfma_f32_16x16x32_bf16 v[64:67], a[12:15], v[24:27], 0\n"
    "s_waitcnt lgkmcnt(2)\n"
    "v_mul_f32 v58, v34, v58\n"
    "v_mul_f32 v59, v35, v59\n"
    "v_mfma_f32_16x16x32_bf16 v[68:71], a[16:19], v[24:27], 0\n"
    "ds_read_b128 v[24:27], v9 offset:9216\n"
    "v_mul_f32 v60, v36, v60\n"
    "v_mfma_f32_16x16x32_bf16 v[40:43], a[24:27], v[28:31], v[40:43]\n"
    "s_mov_b32 m0, s77\n"
    "s_nop 0\n"
    "buffer_load_dwordx4 v8, s[36:39], s76 offen sc1 lds\n"
    "v_mul_f32 v61, v37, v61\n"
    "v_mul_f32 v62, v38, v62\n"
    "v_mfma_f32_16x16x32_bf16 v[44:47], a[28:31], v[28:31], v[44:47]\n"
    "v_mul_f32 v63, v39, v63\n"
    "v_mul_f32 v32, 0xbfb8aa3b, v32\n"
    "v_mfma_f32_16x16x32_bf16 v[64:67], a[36:39], v[28:31], v[64:67]\n"
    "s_waitcnt lgkmcnt(2)\n"
    "v_exp_f32 v32, v32\n"
    "v_mfma_f32_16x16x32_bf16 v[68:71], a[40:43], v[28:31], v[68:71]\n"
    "ds_read_b128 v[28:31], v9 offset:11264\n"
    "v_mul_f32 v33, 0xbfb8aa3b, v33\n"
    "v_mfma_f32_16x16x32_bf16 v[40:43], a[48:51], v[16:19], v[40:43]\n"
    "v_exp_f32 v33, v33\n"
    "s_branch .LgUP_seg1_%=\n"
    ".LgUP_chk2_%=:\n"
    "s_waitcnt vmcnt(0)\n"
    "v_add_u32 v13, v11, v3\n"
    "v_cmp_gt_i32 vcc, s71, v13\n"
    "s_cbranch_vccz .LgUP_dma2_%=\n"
    "s_or_b64 exec, vcc, 1\n"
    "buffer_atomic_smax v11, v2, s[36:39], 0 offen sc0 sc1\n"
    "s_mov_b64 exec, -1\n"
    "s_branch .LgUP_plain2_%=\n"
    ".LgUP_dma2_%=:\n"
    "s_mov_b32 s70, 2\n"
    "v_mfma_f32_16x16x32_bf16 v[44:47], a[52:55], v[16:19], v[44:47]\n"
    "s_add_u32 m0, s77, s59\n"
    "s_add_u32 s77, s77, 8192\n"
    "buffer_load_dwordx4 v1, s[36:39], s76 offen sc1 lds\n"
    "v_mul_f32 v34, 0xbfb8aa3b, v34\n"
    "v_mul_f32 v35, 0xbfb8aa3b, v35\n"
    "v_mfma_f32_16x16x32_bf16 v[64:67], a[60:63], v[16:19], v[64:67]\n"
    "s_waitcnt lgkmcnt(2)\n"
    "v_exp_f32 v34, v34\n"
    "v_mfma_f32_16x16x32_bf16 v[68:71], a[64:67], v[16:19], v[68:71]\n"
    "ds_read_b128 v[16:19], v9 offset:13312\n"
    "v_mul_f32 v36, 0xbfb8aa3b, v36\n"
    "v_mfma_f32_16x16x32_bf16 v[40:43], a[72:75], v[20:23], v[40:43]\n"
    "buffer_load_dwordx4 v1, s[36:39], s76 offen offset:1024 sc1 lds\n"
    "s_add_u32 s76, s76, 16384\n"
    "v_exp_f32 v35, v35\n"
    "v_mfma_f32_16x16x32_bf16 v[44:47], a[76:79], v[20:23], v[44:47]\n"
    "v_exp_f32 v36, v36\n"
    "v_mfma_f32_16x16x32_bf16 v[64:67], a[84:87], v[20:23], v[64:67]\n"
    "s_waitcnt lgkmcnt(2)\n"
    "v_mul_f32 v37, 0xbfb8aa3b, v37\n"
    "v_mul_f32 v38, 0xbfb8aa3b, v38\n"
    "v_mfma_f32_16x16x32_bf16 v[68:71], a[88:91], v[20:23], v[68:71]\n"
    "ds_read_b128 v[20:23], v9 offset:15360\n"
    "v_mul_f32 v39, 0xbfb8aa3b, v39\n"
    "v_mfma_f32_16x16x32_bf16 v[40:43], a[96:99], v[24:27], v[40:43]\n"
    "s_add_u32 m0, s77, s59\n"
    "s_add_u32 s77, s77, 8192\n"
    "buffer_load_dwordx4 v1, s[36:39], s76 offen sc1 lds\n"
    "v_exp_f32 v37, v37\n"
    "v_mfma_f32_16x16x32_bf16 v[44:47], a[100:103], v[24:27], v[44:47]\n"
    "v_exp_f32 v38, v38\n"
    "v_mfma_f32_16x16x32_bf16 v[64:67], a[108:111], v[24:27], v[64:67]\n"
    "s_waitcnt lgkmcnt(2)\n"
    "buffer_load_dwordx4 v1, s[36:39], s76 offen offset:1024 sc1 lds\n"
    "s_add_u32 s76, s76, 16384\n"
    "v_exp_f32 v39, v39\n"
    "v_mfma_f32_16x16x32_bf16 v[68:71], a[112:115], v[24:27], v[68:71]\n"
    "ds_read_b128 v[24:27], v9 offset:17408\n"
    "v_add_f32 v32, 1.0, v32\n"
    "v_mfma_f32_16x16x32_bf16 v[40:43], a[120:123], v[28:31], v[40:43]\n"
    "v_rcp_f32 v32, v32\n"
    "v_mfma_f32_16x16x32_bf16 v[44:47], a[124:127], v[28:31], v[44:47]\n"
    "v_add_f32 v33, 1.0, v33\n"
    "v_add_f32 v34, 1.0, v34\n"
    "v_mfma_f32_16x16x32_bf16 v[64:67], a[132:135], v[28:31], v[64:67]\n"
    "s_waitcnt lgkmcnt(2)\n"
    "s_add_u32 m0, s77, s59\n"
    "s_add_u32 s77, s77, 8192\n"
    "buffer_load_dwordx4 v1, s[36:39], s76 offen sc1 lds\n"
    "v_rcp_f32 v33, v33\n"
    "v_mfma_f32_16x16x32_bf16 v[68:71], a[136:139], v[28:31], v[68:71]\n"
    "ds_read_b128 v[28:31], v9 offset:19456\n"
    "v_add_f32 v35, 1.0, v35\n"
    "v_mfma_f32_16x16x32_bf16 v[40:43], a[144:147], v[16:19], v[40:43]\n"
    "v_rcp_f32 v34, v34\n"
    "v_mfma_f32_16x16x32_bf16 v[44:47], a[148:151], v[16:19], v[44:47]\n"
    "buffer_load_dwordx4 v1, s[36:39], s76 offen offset:1024 sc1 lds\n"
    "s_add_u32 s76, s76, 16384\n"
    "v_rcp_f32 v35, v35\n"
    "v_mfma_f32_16x16x32_bf16 v[64:67], a[156:159], v[16:19], v[64:67]\n"
    "s_waitcnt lgkmcnt(2)\n"
    "v_add_f32 v36, 1.0, v36\n"
    "v_add_f32 v37, 1.0, v37\n"
    "v_mfma_f32_16x16x32_bf16 v[68:71], a[160:163], v[16:19], v[68:71]\n"
    "ds_read_b128 v[16:19], v9 offset:21504\n"
    "v_add_f32 v38, 1.0, v38\n"
    "v_mfma_f32_16x16x32_bf16 v[40:43], a[168:171], v[20:23], v[40:43]\n"
    "v_rcp_f32 v36, v36\n"
    "v_mfma_f32_16x16x32_bf16 v[44:47], a[172:175], v[20:23], v[44:47]\n"
    "s_add_u32 m0, s77, s59\n"
    "s_add_u32 s77, s77, 8192\n"
    "buffer_load_dwordx4 v1, s[36:39], s76 offen sc1 lds\n"
    "v_rcp_f32 v37, v37\n"
    "v_mfma_f32_16x16x32_bf16 v[64:67], a[180:183], v[20:23], v[64:67]\n"
    "s_waitcnt lgkmcnt(2)\n"
    "v_rcp_f32 v38, v38\n"
    "v_mfma_f32_16x16x32_bf16 v[68:71], a[184:187], v[20:23], v[68:71]\n"
    "ds_read_b128 v[20:23], v9 offset:23552\n"
    "v_add_f32 v39, 1.0, v39\n"
    "v_mfma_f32_16x16x32_bf16 v[40:43], a[192:195], v[24:27], v[40:43]\n"
    "buffer_load_dwordx4 v1, s[36:39], s76 offen offset:1024 sc1 lds\n"
    "s_add_u32 s76, s76, 16384\n"
    "v_rcp_f32 v39, v39\n"
    "v_mfma_f32_16x16x32_bf16 v[44:47], a[196:199], v[24:27], v[44:47]\n"
    "v_mul_f32 v32, v32, v56\n"
    "v_mul_f32 v33, v33, v57\n"
    "v_mfma_f32_16x16x32_bf16 v[64:67], a[204:207], v[24:27], v[64:67]\n"
    "s_waitcnt lgkmcnt(2)\n"
    "v_mul_f32 v34, v34, v58\n"
    "v_mul_f32 v35, v35, v59\n"
    "v_mfma_f32_16x16x32_bf16 v[68:71], a[208:211], v[24:27], v[68:71]\n"
    "ds_read_b128 v[24:27], v9 offset:25600\n"
    "v_mul_f32 v36, v36, v60\n"
    "v_mfma_f32_16x16x32_bf16 v[40:43], a[216:219], v[28:31], v[40:43]\n"
    "s_mov_b32 m0, s77\n"
    "s_nop 0\n"
    "buffer_load_dwordx4 v8, s[36:39], s76 offen sc1 lds\n"
    "v_mul_f32 v37, v37, v61\n"
    "v_mul_f32 v38, v38, v62\n"
    "v_mfma_f32_16x16x32_bf16 v[44:47], a[220:223], v[28:31], v[44:47]\n"
    "v_mul_f32 v39, v39, v63\n"
    "v_cvt_pk_bf16_f32 v32, v32, v33\n"
    "v_mfma_f32_16x16x32_bf16 v[64:67], a[228:231], v[28:31], v[64:67]\n"
    "s_waitcnt lgkmcnt(2)\n"
    "v_cvt_pk_bf16_f32 v33, v34, v35\n"
    "v_cvt_pk_bf16_f32 v34, v36, v37\n"
    "v_mfma_f32_16x16x32_bf16 v[68:71], a[232:235], v[28:31], v[68:71]\n"
    "ds_read_b128 v[28:31], v9 offset:27648\n"
    "v_cvt_pk_bf16_f32 v35, v38, v39\n"
    "s_branch .LgUP_seg2_%=\n"
    ".LgUP_slow_%=:\n"
    "s_cmp_lg_u32 s70, 0\n"
    "s_cbranch_scc1 .LgUP_nolast_%=\n"
    "s_waitcnt vmcnt(0)\n"
    "v_add_u32 v13, v11, v3\n"
    "v_cmp_gt_i32 vcc, s71, v13\n"
    "s_nop 0\n"
    "s_or_b64 exec, vcc, 1\n"
    "buffer_atomic_smax v11, v2, s[36:39], 0 offen sc0 sc1\n"
    "s_mov_b64 exec, -1\n"
    ".LgUP_nolast_%=:\n"
    "v_mfma_f32_16x16x32_bf16 v[40:43], a[240:243], v[16:19], v[40:43]\n"
    "buffer_store_dwordx4 v[48:51], v4, s[36:39], s73 offen sc1\n"
    "v_mfma_f32_16x16x32_bf16 v[44:47], a[244:247], v[16:19], v[44:47]\n"
    "v_mfma_f32_16x16x32_bf16 v[64:67], a[252:255], v[16:19], v[64:67]\n"
    "s_waitcnt lgkmcnt(2)\n"
    "buffer_store_dwordx4 v[32:35], v15, s[36:39], s73 offen sc1\n"
    "v_mfma_f32_16x16x32_bf16 v[68:71], v[80:83], v[16:19], v[68:71]\n"
    "ds_read_b128 v[16:19], v9 offset:29696\n"
    "v_mfma_f32_16x16x32_bf16 v[40:43], v[88:91], v[20:23], v[40:43]\n"
    "v_mfma_f32_16x16x32_bf16 v[44:47], v[92:95], v[20:23], v[44:47]\n"
    "v_mfma_f32_16x16x32_bf16 v[64:67], v[100:103], v[20:23], v[64:67]\n"
    "s_waitcnt lgkmcnt(2)\n"
    "v_mfma_f32_16x16x32_bf16 v[68:71], v[104:107], v[20:23], v[68:71]\n"
    "ds_read_b128 v[20:23], v9 offset:31744\n"
    "v_mfma_f32_16x16x32_bf16 v[40:43], v[112:115], v[24:27], v[40:43]\n"
    "v_mfma_f32_16x16x32_bf16 v[44:47], v[116:119], v[24:27], v[44:47]\n"
    "v_mfma_f32_16x16x32_bf16 v[64:67], v[124:127], v[24:27], v[64:67]\n"
    "s_waitcnt lgkmcnt(2)\n"
    "v_mfma_f32_16x16x32_bf16 v[68:71], v[128:131], v[24:27], v[68:71]\n"
    "ds_read_b128 v[24:27], v9 offset:33792\n"
    "v_mfma_f32_16x16x32_bf16 v[40:43], v[136:139], v[28:31], v[40:43]\n"
    "v_mfma_f32_16x16x32_bf16 v[44:47], v[140:143], v[28:31], v[44:47]\n"
    "v_mfma_f32_16x16x32_bf16 v[64:67], v[148:151], v[28:31], v[64:67]\n"
    "s_waitcnt lgkmcnt(2)\n"
    "v_mfma_f32_16x16x32_bf16 v[68:71], v[152:155], v[28:31], v[68:71]\n"
    "ds_read_b128 v[28:31], v9 offset:35840\n"
    "v_mfma_f32_16x16x32_bf16 v[40:43], v[160:163], v[16:19], v[40:43]\n"
    "v_mfma_f32_16x16x32_bf16 v[44:47], v[164:167], v[16:19], v[44:47]\n"
    "v_mfma_f32_16x16x32_bf16 v[64:67], v[172:175], v[16:19], v[64:67]\n"
    "s_waitcnt lgkmcnt(2)\n"
    "v_mfma_f32_16x16x32_bf16 v[68:71], v[176:179], v[16:19], v[68:71]\n"
    "v_mfma_f32_16x16x32_bf16 v[40:43], v[184:187], v[20:23], v[40:43]\n"
    "v_mfma_f32_16x16x32_bf16 v[44:47], v[188:191], v[20:23], v[44:47]\n"
    "v_mfma_f32_16x16x32_bf16 v[64:67], v[196:199], v[20:23], v[64:67]\n"
    "s_waitcnt lgkmcnt(1)\n"
    "v_mfma_f32_16x16x32_bf16 v[68:71], v[200:203], v[20:23], v[68:71]\n"
    "v_mfma_f32_16x16x32_bf16 v[40:43], v[208:211], v[24:27], v[40:43]\n"
    "v_mfma_f32_16x16x32_bf16 v[44:47], v[212:215], v[24:27], v[44:47]\n"
    "v_mfma_f32_16x16x32_bf16 v[64:67], v[220:223], v[24:27], v[64:67]\n"
    "s_waitcnt lgkmcnt(0)\n"
    "v_mfma_f32_16x16x32_bf16 v[68:71], v[224:227], v[24:27], v[68:71]\n"
    "v_mfma_f32_16x16x32_bf16 v[40:43], v[232:235], v[28:31], v[40:43]\n"
    "v_mfma_f32_16x16x32_bf16 v[44:47], v[236:239], v[28:31], v[44:47]\n"
    "v_mfma_f32_16x16x32_bf16 v[64:67], v[244:247], v[28:31], v[64:67]\n"
    "v_mfma_f32_16x16x32_bf16 v[68:71], v[248:251], v[28:31], v[68:71]\n"
    "s_nop 3\n"
    "v_pk_mul_f32 v[64:65], v[40:41], v[64:65]\n"
    "v_pk_mul_f32 v[66:67], v[42:43], v[66:67]\n"
    "s_nop 1\n"
    "v_pk_mul_f32 v[68:69], v[44:45], v[68:69]\n"
    "v_pk_mul_f32 v[70:71], v[46:47], v[70:71]\n"
    "v_pk_mul_f32 v[40:41], v[40:41], s[64:65] op_sel_hi:[1,0]\n"
    "v_pk_mul_f32 v[42:43], v[42:43], s[64:65] op_sel_hi:[1,0]\n"
    "v_pk_mul_f32 v[44:45], v[44:45], s[64:65] op_sel_hi:[1,0]\n"
    "v_pk_mul_f32 v[46:47], v[46:47], s[64:65] op_sel_hi:[1,0]\n"
    "v_exp_f32 v40, v40\n"
    "v_exp_f32 v41, v41\n"
    "v_exp_f32 v42, v42\n"
    "v_exp_f32 v43, v43\n"
    "v_exp_f32 v44, v44\n"
    "v_exp_f32 v45, v45\n"
    "v_exp_f32 v46, v46\n"
    "v_exp_f32 v47, v47\n"
    "v_pk_add_f32 v[40:41], v[40:41], 1.0 op_sel_hi:[1,0]\n"
    "v_pk_add_f32 v[42:43], v[42:43], 1.0 op_sel_hi:[1,0]\n"
    "v_pk_add_f32 v[44:45], v[44:45], 1.0 op_sel_hi:[1,0]\n"
    "v_pk_add_f32 v[46:47], v[46:47], 1.0 op_sel_hi:[1,0]\n"
    "v_rcp_f32 v40, v40\n"
    "v_rcp_f32 v41, v41\n"
    "v_rcp_f32 v42, v42\n"
    "v_rcp_f32 v43, v43\n"
    "v_rcp_f32 v44, v44\n"
    "v_rcp_f32 v45, v45\n"
    "v_rcp_f32 v46, v46\n"
    "v_rcp_f32 v47, v47\n"
    "v_pk_mul_f32 v[40:41], v[40:41], v[64:65]\n"
    "v_pk_mul_f32 v[42:43], v[42:43], v[66:67]\n"
    "v_pk_mul_f32 v[44:45], v[44:45], v[68:69]\n"
    "v_pk_mul_f32 v[46:47], v[46:47], v[70:71]\n"
    "v_cvt_pk_bf16_f32 v40, v40, v41\n"
    "v_cvt_pk_bf16_f32 v41, v42, v43\n"
    "v_cvt_pk_bf16_f32 v42, v44, v45\n"
    "v_cvt_pk_bf16_f32 v43, v46, v47\n"
    "buffer_store_dwordx4 v[40:43], v15, s[36:39], s73 offen offset:1024 sc1\n"
    "s_mov_b32 s68, s71\n"
    "s_cmp_eq_u32 s70, 0\n"
    "s_cbranch_scc1 .LgUP_idle_%=\n"
    "s_cmp_eq_u32 s68, 64\n"
    "s_cbranch_scc1 .LgUP_done_%=\n"
    "s_waitcnt vmcnt(3)\n"
    ".LgUP_bar_%=:\n"
    "v_xor_b32 v9, s78, v9\n"
    "s_add_u32 s71, s68, 1\n"
    "s_cmp_eq_u32 s71, 64\n"
    "s_cselect_b32 s70, 2, 0\n"
    "s_barrier\n"
    "ds_read_b128 v[16:19], v9 offset:0\n"
    "ds_read_b128 v[20:23], v9 offset:1024\n"
    "ds_read_b128 v[24:27], v9 offset:2048\n"
    "ds_read_b128 v[28:31], v9 offset:3072\n"
    "v_add_u32 v13, v11, v3\n"
    "v_cmp_gt_i32 vcc, s71, v13\n"
    "s_nop 0\n"
    "s_or_b64 exec, vcc, 1\n"
    "buffer_atomic_smax v11, v2, s[36:39], 0 offen sc0 sc1\n"
    "s_mov_b64 exec, -1\n"
    "s_waitcnt lgkmcnt(3)\n"
    "v_mfma_f32_16x16x32_bf16 v[48:51], a[8:11], v[16:19], 0\n"
    "s_waitcnt lgkmcnt(2)\n"
    "s_sub_u32 s74, s68, 1\n"
    "v_mfma_f32_16x16x32_bf16 v[72:75], a[20:23], v[16:19], 0\n"
    "ds_read_b128 v[16:19], v9 offset:4096\n"
    "v_mfma_f32_16x16x32_bf16 v[52:55], a[8:11], v[20:23], 0\n"
    "s_waitcnt lgkmcnt(2)\n"
    "s_and_b32 s76, s71, s54\n"
    "v_mfma_f32_16x16x32_bf16 v[76:79], a[20:23], v[20:23], 0\n"
    "ds_read_b128 v[20:23], v9 offset:5120\n"
    "v_mfma_f32_16x16x32_bf16 v[48:51], a[32:35], v[24:27], v[48:51]\n"
    "s_waitcnt lgkmcnt(2)\n"
    "s_mul_i32 s76, s76, s53\n"
    "v_mfma_f32_16x16x32_bf16 v[72:75], a[44:47], v[24:27], v[72:75]\n"
    "ds_read_b128 v[24:27], v9 offset:6144\n"
    "v_mfma_f32_16x16x32_bf16 v[52:55], a[32:35], v[28:31], v[52:55]\n"
    "s_waitcnt lgkmcnt(2)\n"
    "s_add_u32 s76, s76, s52\n"
    "v_mfma_f32_16x16x32_bf16 v[76:79], a[44:47], v[28:31], v[76:79]\n"
    "ds_read_b128 v[28:31], v9 offset:7168\n"
    "v_mfma_f32_16x16x32_bf16 v[48:51], a[56:59], v[16:19], v[48:51]\n"
    "s_waitcnt lgkmcnt(2)\n"
    "s_and_b32 s77, s71, 1\n"
    "v_mfma_f32_16x16x32_bf16 v[72:75], a[68:71], v[16:19], v[72:75]\n"
    "ds_read_b128 v[16:19], v9 offset:8192\n"
    "v_mfma_f32_16x16x32_bf16 v[52:55], a[56:59], v[20:23], v[52:55]\n"
    "s_waitcnt lgkmcnt(2)\n"
    "s_mul_i32 s77, s77, 65536\n"
    "v_mfma_f32_16x16x32_bf16 v[76:79], a[68:71], v[20:23], v[76:79]\n"
    "ds_read_b128 v[20:23], v9 offset:9216\n"
    "v_mfma_f32_16x16x32_bf16 v[48:51], a[80:83], v[24:27], v[48:51]\n"
    "s_waitcnt lgkmcnt(2)\n"
    "s_add_u32 s77, s77, s59\n"
    "v_mfma_f32_16x16x32_bf16 v[72:75], a[92:95], v[24:27], v[72:75]\n"
    "ds_read_b128 v[24:27], v9 offset:10240\n"
    "v_mfma_f32_16x16x32_bf16 v[52:55], a[80:83], v[28:31], v[52:55]\n"
    "s_waitcnt lgkmcnt(2)\n"
    "v_mfma_f32_16x16x32_bf16 v[76:79], a[92:95], v[28:31], v[76:79]\n"
    "ds_read_b128 v[28:31], v9 offset:11264\n"
    "v_mfma_f32_16x16x32_bf16 v[48:51], a[104:107], v[16:19], v[48:51]\n"
    "s_waitcnt lgkmcnt(2)\n"
    "v_mfma_f32_16x16x32_bf16 v[72:75], a[116:119], v[16:19], v[72:75]\n"
    "ds_read_b128 v[16:19], v9 offset:12288\n"
    "v_mfma_f32_16x16x32_bf16 v[52:55], a[104:107], v[20:23], v[52:55]\n"
    "s_waitcnt lgkmcnt(2)\n"
    "v_mfma_f32_16x16x32_bf16 v[76:79], a[116:119], v[20:23], v[76:79]\n"
    "ds_read_b128 v[20:23], v9 offset:13312\n"
    "v_mfma_f32_16x16x32_bf16 v[48:51], a[128:131], v[24:27], v[48:51]\n"
    "s_waitcnt lgkmcnt(2)\n"
    "v_mfma_f32_16x16x32_bf16 v[72:75], a[140:143], v[24:27], v[72:75]\n"
    "ds_read_b128 v[24:27], v9 offset:14336\n"
    "v_mfma_f32_16x16x32_bf16 v[52:55], a[128:131], v[28:31], v[52:55]\n"
    "s_waitcnt lgkmcnt(2)\n"
    "v_mfma_f32_16x16x32_bf16 v[76:79], a[140:143], v[28:31], v[76:79]\n"
    "ds_read_b128 v[28:31], v9 offset:15360\n"
    "v_mfma_f32_16x16x32_bf16 v[48:51], a[152:155], v[16:19], v[48:51]\n"
    "s_waitcnt lgkmcnt(2)\n"
    "v_mfma_f32_16x16x32_bf16 v[72:75], a[164:167], v[16:19], v[72:75]\n"
    "ds_read_b128 v[16:19], v9 offset:16384\n"
    "v_mfma_f32_16x16x32_bf16 v[52:55], a[152:155], v[20:23], v[52:55]\n"
    "s_waitcnt lgkmcnt(2)\n"
    "v_mfma_f32_16x16x32_bf16 v[76:79], a[164:167], v[20:23], v[76:79]\n"
    "ds_read_b128 v[20:23], v9 offset:17408\n"
    "v_mfma_f32_16x16x32_bf16 v[48:51], a[176:179], v[24:27], v[48:51]\n"
    "s_waitcnt lgkmcnt(2)\n"
    "v_mfma_f32_16x16x32_bf16 v[72:75], a[188:191], v[24:27], v[72:75]\n"
    "ds_read_b128 v[24:27], v9 offset:18432\n"
    "v_mfma_f32_16x16x32_bf16 v[52:55], a[176:179], v[28:31], v[52:55]\n"
    "s_waitcnt lgkmcnt(2)\n"
    "v_mfma_f32_16x16x32_bf16 v[76:79], a[188:191], v[28:31], v[76:79]\n"
    "ds_read_b128 v[28:31], v9 offset:19456\n"
    "v_mfma_f32_16x16x32_bf16 v[48:51], a[200:203], v[16:19], v[48:51]\n"
    "s_waitcnt lgkmcnt(2)\n"
    "v_mfma_f32_16x16x32_bf16 v[72:75], a[212:215], v[16:19], v[72:75]\n"
    "ds_read_b128 v[16:19], v9 offset:20480\n"
    "v_mfma_f32_16x16x32_bf16 v[52:55], a[200:203], v[20:23], v[52:55]\n"
    "s_waitcnt lgkmcnt(2)\n"
    "v_mfma_f32_16x16x32_bf16 v[76:79], a[212:215], v[20:23], v[76:79]\n"
    "ds_read_b128 v[20:23], v9 offset:21504\n"
    "v_mfma_f32_16x16x32_bf16 v[48:51], a[224:227], v[24:27], v[48:51]\n"
    "s_waitcnt lgkmcnt(2)\n"
    "v_mfma_f32_16x16x32_bf16 v[72:75], a[236:239], v[24:27], v[72:75]\n"
    "ds_read_b128 v[24:27], v9 offset:22528\n"
    "v_mfma_f32_16x16x32_bf16 v[52:55], a[224:227], v[28:31], v[52:55]\n"
    "s_waitcnt lgkmcnt(2)\n"
    "v_mfma_f32_16x16x32_bf16 v[76:79], a[236:239], v[28:31], v[76:79]\n"
    "ds_read_b128 v[28:31], v9 offset:23552\n"
    "v_mfma_f32_16x16x32_bf16 v[48:51], a[248:251], v[16:19], v[48:51]\n"
    "s_waitcnt lgkmcnt(2)\n"
    "v_mfma_f32_16x16x32_bf16 v[72:75], v[84:87], v[16:19], v[72:75]\n"
    "ds_read_b128 v[16:19], v9 offset:24576\n"
    "v_mfma_f32_16x16x32_bf16 v[52:55], a[248:251], v[20:23], v[52:55]\n"
    "s_waitcnt lgkmcnt(2)\n"
    "v_mfma_f32_16x16x32_bf16 v[76:79], v[84:87], v[20:23], v[76:79]\n"
    "ds_read_b128 v[20:23], v9 offset:25600\n"
    "v_mfma_f32_16x16x32_bf16 v[48:51], v[96:99], v[24:27], v[48:51]\n"
    "s_waitcnt lgkmcnt(2)\n"
    "v_mfma_f32_16x16x32_bf16 v[72:75], v[108:111], v[24:27], v[72:75]\n"
    "ds_read_b128 v[24:27], v9 offset:26624\n"
    "v_mfma_f32_16x16x32_bf16 v[52:55], v[96:99], v[28:31], v[52:55]\n"
    "s_waitcnt lgkmcnt(2)\n"
    "v_mfma_f32_16x16x32_bf16 v[76:79], v[108:111], v[28:31], v[76:79]\n"
    "ds_read_b128 v[28:31], v9 offset:27648\n"
    "v_mfma_f32_16x16x32_bf16 v[48:51], v[120:123], v[16:19], v[48:51]\n"
    "s_waitcnt lgkmcnt(2)\n"
    "v_mfma_f32_16x16x32_bf16 v[72:75], v[132:135], v[16:19], v[72:75]\n"
    "ds_read_b128 v[16:19], v9 offset:28672\n"
    "v_mfma_f32_16x16x32_bf16 v[52:55], v[120:123], v[20:23], v[52:55]\n"
    "s_waitcnt lgkmcnt(2)\n"
    "v_mfma_f32_16x16x32_bf16 v[76:79], v[132:135], v[20:23], v[76:79]\n"
    "ds_read_b128 v[20:23], v9 offset:29696\n"
    "v_mfma_f32_16x16x32_bf16 v[48:51], v[144:147], v[24:27], v[48:51]\n"
    "s_waitcnt lgkmcnt(2)\n"
    "v_mfma_f32_16x16x32_bf16 v[72:75], v[156:159], v[24:27], v[72:75]\n"
    "ds_read_b128 v[24:27], v9 offset:30720\n"
    "v_mfma_f32_16x16x32_bf16 v[52:55], v[144:147], v[28:31], v[52:55]\n"
    "s_waitcnt lgkmcnt(2)\n"
    "v_mfma_f32_16x16x32_bf16 v[76:79], v[156:159], v[28:31], v[76:79]\n"
    "ds_read_b128 v[28:31], v9 offset:31744\n"
    "v_mfma_f32_16x16x32_bf16 v[48:51], v[168:171], v[16:19], v[48:51]\n"
    "s_waitcnt lgkmcnt(2)\n"
    "v_mfma_f32_16x16x32_bf16 v[72:75], v[180:183], v[16:19], v[72:75]\n"
    "ds_read_b128 v[16:19], v9 offset:32768\n"
    "v_mfma_f32_16x16x32_bf16 v[52:55], v[168:171], v[20:23], v[52:55]\n"
    "s_waitcnt lgkmcnt(2)\n"
    "v_mfma_f32_16x16x32_bf16 v[76:79], v[180:183], v[20:23], v[76:79]\n"
    "ds_read_b128 v[20:23], v9 offset:33792\n"
    "v_mfma_f32_16x16x32_bf16 v[48:51], v[192:195], v[24:27], v[48:51]\n"
    "s_waitcnt lgkmcnt(2)\n"
    "v_mfma_f32_16x16x32_bf16 v[72:75], v[204:207], v[24:27], v[72:75]\n"
    "ds_read_b128 v[24:27], v9 offset:34816\n"
    "v_mfma_f32_16x16x32_bf16 v[52:55], v[192:195], v[28:31], v[52:55]\n"
    "s_waitcnt lgkmcnt(2)\n"
    "v_mfma_f32_16x16x32_bf16 v[76:79], v[204:207], v[28:31], v[76:79]\n"
    "ds_read_b128 v[28:31], v9 offset:35840\n"
    "v_mfma_f32_16x16x32_bf16 v[48:51], v[216:219], v[16:19], v[48:51]\n"
    "s_waitcnt lgkmcnt(2)\n"
    "v_mfma_f32_16x16x32_bf16 v[72:75], v[228:231], v[16:19], v[72:75]\n"
    "ds_read_b128 v[16:19], v9 offset:0\n"
    "v_mfma_f32_16x16x32_bf16 v[52:55], v[216:219], v[20:23], v[52:55]\n"
    "s_waitcnt lgkmcnt(2)\n"
    "v_mfma_f32_16x16x32_bf16 v[76:79], v[228:231], v[20:23], v[76:79]\n"
    "ds_read_b128 v[20:23], v9 offset:2048\n"
    "v_mfma_f32_16x16x32_bf16 v[48:51], v[240:243], v[24:27], v[48:51]\n"
    "s_waitcnt lgkmcnt(2)\n"
    "v_mfma_f32_16x16x32_bf16 v[72:75], v[252:255], v[24:27], v[72:75]\n"
    "ds_read_b128 v[24:27], v9 offset:4096\n"
    "v_mfma_f32_16x16x32_bf16 v[52:55], v[240:243], v[28:31], v[52:55]\n"
    "s_waitcnt lgkmcnt(2)\n"
    "v_mfma_f32_16x16x32_bf16 v[76:79], v[252:255], v[28:31], v[76:79]\n"
    "ds_read_b128 v[28:31], v9 offset:6144\n"
    "v_mfma_f32_16x16x32_bf16 v[32:35], a[0:3], v[16:19], 0\n"
    "v_mfma_f32_16x16x32_bf16 v[36:39], a[4:7], v[16:19], 0\n"
    "v_mul_f32 v72, v48, v72\n"
    "v_mul_f32 v73, v49, v73\n"
    "v_mfma_f32_16x16x32_bf16 v[56:59], a[12:15], v[16:19], 0\n"
    "s_waitcnt lgkmcnt(2)\n"
    "v_mul_f32 v74, v50, v74\n"
    "v_mul_f32 v75, v51, v75\n"
    "v_mfma_f32_16x16x32_bf16 v[60:63], a[16:19], v[16:19], 0\n"
    "ds_read_b128 v[16:19], v9 offset:8192\n"
    "v_mul_f32 v76, v52, v76\n"
    "v_mfma_f32_16x16x32_bf16 v[32:35], a[24:27], v[20:23], v[32:35]\n"
    "v_mul_f32 v77, v53, v77\n"
    "v_mul_f32 v78, v54, v78\n"
    "v_mfma_f32_16x16x32_bf16 v[36:39], a[28:31], v[20:23], v[36:39]\n"
    "v_mul_f32 v79, v55, v79\n"
    "v_mul_f32 v48, 0xbfb8aa3b, v48\n"
    "v_mfma_f32_16x16x32_bf16 v[56:59], a[36:39], v[20:23], v[56:59]\n"
    "s_waitcnt lgkmcnt(2)\n"
    "v_exp_f32 v48, v48\n"
    "v_mfma_f32_16x16x32_bf16 v[60:63], a[40:43], v[20:23], v[60:63]\n"
    "ds_read_b128 v[20:23], v9 offset:10240\n"
    "v_mul_f32 v49, 0xbfb8aa3b, v49\n"
    "v_mfma_f32_16x16x32_bf16 v[32:35], a[48:51], v[24:27], v[32:35]\n"
    "v_exp_f32 v49, v49\n"
    "v_mfma_f32_16x16x32_bf16 v[36:39], a[52:55], v[24:27], v[36:39]\n"
    "v_mul_f32 v50, 0xbfb8aa3b, v50\n"
    "v_mul_f32 v51, 0xbfb8aa3b, v51\n"
    "v_mfma_f32_16x16x32_bf16 v[56:59], a[60:63], v[24:27], v[56:59]\n"
    "s_waitcnt lgkmcnt(2)\n"
    "v_exp_f32 v50, v50\n"
    "v_mfma_f32_16x16x32_bf16 v[60:63], a[64:67], v[24:27], v[60:63]\n"
    "ds_read_b128 v[24:27], v9 offset:12288\n"
    "v_mul_f32 v52, 0xbfb8aa3b, v52\n"
    "v_mfma_f32_16x16x32_bf16 v[32:35], a[72:75], v[28:31], v[32:35]\n"
    "v_exp_f32 v51, v51\n"
    "v_mfma_f32_16x16x32_bf16 v[36:39], a[76:79], v[28:31], v[36:39]\n"
    "v_exp_f32 v52, v52\n"
    "v_mov_b32 v12, s74\n"
    "v_mfma_f32_16x16x32_bf16 v[56:59], a[84:87], v[28:31], v[56:59]\n"
    "s_waitcnt lgkmcnt(2)\n"
    "s_waitcnt vmcnt(0)\n"
    "v_mul_f32 v53, 0xbfb8aa3b, v53\n"
    "v_mul_f32 v54, 0xbfb8aa3b, v54\n"
    "v_add_u32 v13, v11, v3\n"
    "v_mfma_f32_16x16x32_bf16 v[60:63], a[88:91], v[28:31], v[60:63]\n"
    "ds_read_b128 v[28:31], v9 offset:14336\n"
    "buffer_atomic_smax v12, v14, s[36:39], 0 offen sc1\n"
    "v_mul_f32 v55, 0xbfb8aa3b, v55\n"
    "v_cmp_gt_i32 vcc, s71, v13\n"
    "v_mfma_f32_16x16x32_bf16 v[32:35], a[96:99], v[16:19], v[32:35]\n"
    "v_exp_f32 v53, v53\n"
    "v_mfma_f32_16x16x32_bf16 v[36:39], a[100:103], v[16:19], v[36:39]\n"
    "v_exp_f32 v54, v54\n"
    "v_mfma_f32_16x16x32_bf16 v[56:59], a[108:111], v[16:19], v[56:59]\n"
    "s_waitcnt lgkmcnt(2)\n"
    "v_exp_f32 v55, v55\n"
    "s_branch .LgUP_mid_%=\n"
    ".LgUP_idle_%=:\n"
    "s_waitcnt vmcnt(3)\n"
    "v_add_u32 v13, v11, v3\n"
    "v_cmp_gt_i32 vcc, s71, v13\n"
    "s_cbranch_vccnz .LgUP_idle2_%=\n"
    "s_and_b32 s76, s71, s54\n"
    "s_mul_i32 s76, s76, s53\n"
    "s_add_u32 s76, s76, s52\n"
    "s_and_b32 s77, s71, 1\n"
    "s_mul_i32 s77, s77, 65536\n"
    "s_add_u32 s77, s77, s59\n"
    "s_add_u32 m0, s77, s59\n"
    "s_add_u32 s77, s77, 8192\n"
    "buffer_load_dwordx4 v1, s[36:39], s76 offen sc1 lds\n"
    "buffer_load_dwordx4 v1, s[36:39], s76 offen offset:1024 sc1 lds\n"
    "s_add_u32 s76, s76, 16384\n"
    "s_add_u32 m0, s77, s59\n"
    "s_add_u32 s77, s77, 8192\n"
    "buffer_load_dwordx4 v1, s[36:39], s76 offen sc1 lds\n"
    "buffer_load_dwordx4 v1, s[36:39], s76 offen offset:1024 sc1 lds\n"
    "s_add_u32 s76, s76, 16384\n"
    "s_add_u32 m0, s77, s59\n"
    "s_add_u32 s77, s77, 8192\n"
    "buffer_load_dwordx4 v1, s[36:39], s76 offen sc1 lds\n"
    "buffer_load_dwordx4 v1, s[36:39], s76 offen offset:1024 sc1 lds\n"
    "s_add_u32 s76, s76, 16384\n"
    "s_add_u32 m0, s77, s59\n"
    "s_add_u32 s77, s77, 8192\n"
    "buffer_load_dwordx4 v1, s[36:39], s76 offen sc1 lds\n"
    "buffer_load_dwordx4 v1, s[36:39], s76 offen offset:1024 sc1 lds\n"
    "s_add_u32 s76, s76, 16384\n"
    "s_mov_b32 m0, s77\n"
    "s_nop 0\n"
    "buffer_load_dwordx4 v8, s[36:39], s76 offen sc1 lds\n"
    "s_waitcnt vmcnt(9)\n"
    "s_sub_u32 s74, s68, 1\n"
    "v_mov_b32 v12, s74\n"
    "s_mov_b64 exec, s[62:63]\n"
    "buffer_atomic_smax v12, v14, s[36:39], 0 offen sc1\n"
    "s_mov_b64 exec, -1\n"
    "s_waitcnt vmcnt(1)\n"
    "s_branch .LgUP_bar_%=\n"
    ".LgUP_idle2_%=:\n"
    "s_or_b64 exec, vcc, 1\n"
    "buffer_atomic_smax v11, v2, s[36:39], 0 offen sc0 sc1\n"
    "s_mov_b64 exec, -1\n"
    "s_waitcnt vmcnt(1)\n"
    "s_sub_u32 s74, s68, 1\n"
    "v_mov_b32 v12, s74\n"
    "s_mov_b64 exec, s[62:63]\n"
    "buffer_atomic_smax v12, v14, s[36:39], 0 offen sc1\n"
    "s_mov_b64 exec, -1\n"
    "s_waitcnt vmcnt(1)\n"
    "s_branch .LgUP_block1_%=\n"
    ".LgUP_block_%=:\n"
    "v_add_u32 v13, v11, v3\n"
    "v_cmp_gt_i32 vcc, s71, v13\n"
    "s_nop 0\n"
    "s_or_b64 exec, vcc, 1\n"
    "buffer_atomic_smax v11, v2, s[36:39], 0 offen sc0 sc1\n"
    "s_mov_b64 exec, -1\n"
    "s_waitcnt vmcnt(0)\n"
    ".LgUP_block1_%=:\n"
    "v_add_u32 v13, v11, v3\n"
    "v_cmp_gt_i32 vcc, s71, v13\n"
    "s_cbranch_vccz .LgUP_go_%=\n"
    "s_sleep 4\n"
    "s_sub_u32 s72, s72, 1\n"
    "s_cbranch_scc0 .LgUP_block_%=\n"
    "s_mov_b32 s72, 0\n"
    ".LgUP_go_%=:\n"
    "s_and_b32 s76, s71, s54\n"
    "s_mul_i32 s76, s76, s53\n"
    "s_add_u32 s76, s76, s52\n"
    "s_and_b32 s77, s71, 1\n"
    "s_mul_i32 s77, s77, 65536\n"
    "s_add_u32 s77, s77, s59\n"
    "s_add_u32 m0, s77, s59\n"
    "s_add_u32 s77, s77, 8192\n"
    "buffer_load_dwordx4 v1, s[36:39], s76 offen sc1 lds\n"
    "buffer_load_dwordx4 v1, s[36:39], s76 offen offset:1024 sc1 lds\n"
    "s_add_u32 s76, s76, 16384\n"
    "s_add_u32 m0, s77, s59\n"
    "s_add_u32 s77, s77, 8192\n"
    "buffer_load_dwordx4 v1, s[36:39], s76 offen sc1 lds\n"
    "buffer_load_dwordx4 v1, s[36:39], s76 offen offset:1024 sc1 lds\n"
    "s_add_u32 s76, s76, 16384\n"
    "s_add_u32 m0, s77, s59\n"
    "s_add_u32 s77, s77, 8192\n"
    "buffer_load_dwordx4 v1, s[36:39], s76 offen sc1 lds\n"
    "buffer_load_dwordx4 v1, s[36:39], s76 offen offset:1024 sc1 lds\n"
    "s_add_u32 s76, s76, 16384\n"
    "s_add_u32 m0, s77, s59\n"
    "s_add_u32 s77, s77, 8192\n"
    "buffer_load_dwordx4 v1, s[36:39], s76 offen sc1 lds\n"
    "buffer_load_dwordx4 v1, s[36:39], s76 offen offset:1024 sc1 lds\n"
    "s_add_u32 s76, s76, 16384\n"
    "s_mov_b32 m0, s77\n"
    "s_nop 0\n"
    "buffer_load_dwordx4 v8, s[36:39], s76 offen sc1 lds\n"
    "s_waitcnt vmcnt(0)\n"
    "s_branch .LgUP_bar_%=\n"
    ".LgUP_FIN_%=:\n"
  );
}
DEVI void role_wo(const Params& p, int L, int j, int i, u32 dbg, unsigned long long tag) {
  const int lane = threadIdx.x & 63, w = threadIdx.x >> 6, e0 = j * 1024 * 576 + i * 192;
  GEMM_DECL;
  g_w0 = make_srd(p.Wo + e0, 4096u * 576 * 2 - e0 * 2); g_w1 = g_w0; g_w2 = g_w0;
  g_ldw = 1152;
  g_in_base = pgb(L, B_O + 8 * j); g_in_stride = SLOT; g_in_mask = RO - 1;
  g_out_base = pgb(L, B_PA + 5 * j); g_out_kb = 12 * i; g_out_mask = RP - 1;
  g_flag = lane < 4 ? mbox(L, lane, SL_REDA, 12 * j + 4 * i + w) : ((lane < 12 && w == 0) ? mbox_att(L, j, lane - 4, 4 + i) : OOB);
  g_paddr = mbox(L, j, SL_WO + i, lane);
  g_pbias = lane < 8 ? 0 : (lane < 24 ? RP : 1000);
  v16u sa, sb, va;
  gemm_args(GEMM_ARGS, p, dbg, tag, sa, sb, va);
  RUN_ASM(
    "s_nop 4\n"
    "v_add_u32 v8, s59, v0\n"
    "s_mov_b32 s68, 0\n"
    "s_mov_b32 s71, 0\n"
    "s_mov_b32 s78, 65536\n"
    "v_add_u32 v9, s78, v0\n"
    "s_mov_b32 s64, 0xbfb8aa3b\n"
    "s_mov_b32 s72, 262144\n"
    "s_add_u32 m0, s59, 0\n"
    "s_mov_b32 s74, 0\n"
    "buffer_load_dwordx4 v5, s[40:43], s74 offen lds\n"
    "s_add_u32 m0, s59, 4096\n"
    "s_mov_b32 s74, 128\n"
    "buffer_load_dwordx4 v5, s[40:43], s74 offen lds\n"
    "s_add_u32 m0, s59, 8192\n"
    "s_mov_b32 s74, 256\n"
    "buffer_load_dwordx4 v5, s[40:43], s74 offen lds\n"
    "s_add_u32 m0, s59, 12288\n"
    "s_mov_b32 s74, 36864\n"
    "buffer_load_dwordx4 v5, s[40:43], s74 offen lds\n"
    "s_add_u32 m0, s59, 16384\n"
    "s_mov_b32 s74, 36992\n"
    "buffer_load_dwordx4 v5, s[40:43], s74 offen lds\n"
    "s_add_u32 m0, s59, 20480\n"
    "s_mov_b32 s74, 37120\n"
    "buffer_load_dwordx4 v5, s[40:43], s74 offen lds\n"
    "s_add_u32 m0, s59, 24576\n"
    "s_mov_b32 s74, 73728\n"
    "buffer_load_dwordx4 v5, s[40:43], s74 offen lds\n"
    "s_add_u32 m0, s59, 28672\n"
    "s_mov_b32 s74, 73856\n"
    "buffer_load_dwordx4 v5, s[40:43], s74 offen lds\n"
    "s_add_u32 m0, s59, 32768\n"
    "s_mov_b32 s74, 73984\n"
    "buffer_load_dwordx4 v5, s[40:43], s74 offen lds\n"
    "s_add_u32 m0, s59, 36864\n"
    "s_mov_b32 s74, 110592\n"
    "buffer_load_dwordx4 v5, s[40:43], s74 offen lds\n"
    "s_add_u32 m0, s59, 40960\n"
    "s_mov_b32 s74, 110720\n"
    "buffer_load_dwordx4 v5, s[40:43], s74 offen lds\n"
    "s_add_u32 m0, s59, 45056\n"
    "s_mov_b32 s74, 110848\n"
    "buffer_load_dwordx4 v5, s[40:43], s74 offen lds\n"
    "s_add_u32 m0, s59, 49152\n"
    "s_mov_b32 s74, 147456\n"
    "buffer_load_dwordx4 v5, s[40:43], s74 offen lds\n"
    "s_add_u32 m0, s59, 53248\n"
    "s_mov_b32 s74, 147584\n"
    "buffer_load_dwordx4 v5, s[40:43], s74 offen lds\n"
    "s_add_u32 m0, s59, 57344\n"
    "s_mov_b32 s74, 147712\n"
    "buffer_load_dwordx4 v5, s[40:43], s74 offen lds\n"
    "s_add_u32 m0, s59, 61440\n"
    "s_mov_b32 s74, 184320\n"
    "buffer_load_dwordx4 v5, s[40:43], s74 offen lds\n"
    "s_add_u32 m0, s59, 65536\n"
    "s_mov_b32 s74, 184448\n"
    "buffer_load_dwordx4 v5, s[40:43], s74 offen lds\n"
    "s_add_u32 m0, s59, 69632\n"
    "s_mov_b32 s74, 184576\n"
    "buffer_load_dwordx4 v5, s[40:43], s74 offen lds\n"
    "s_add_u32 m0, s59, 73728\n"
    "s_mov_b32 s74, 221184\n"
    "buffer_load_dwordx4 v5, s[40:43], s74 offen lds\n"
    "s_add_u32 m0, s59, 77824\n"
    "s_mov_b32 s74, 221312\n"
    "buffer_load_dwordx4 v5, s[40:43], s74 offen lds\n"
    "s_add_u32 m0, s59, 81920\n"
    "s_mov_b32 s74, 221440\n"
    "buffer_load_dwordx4 v5, s[40:43], s74 offen lds\n"
    "s_add_u32 m0, s59, 86016\n"
    "s_mov_b32 s74, 258048\n"
    "buffer_load_dwordx4 v5, s[40:43], s74 offen lds\n"
    "s_add_u32 m0, s59, 90112\n"
    "s_mov_b32 s74, 258176\n"
    "buffer_load_dwordx4 v5, s[40:43], s74 offen lds\n"
    "s_add_u32 m0, s59, 94208\n"
    "s_mov_b32 s74, 258304\n"
    "buffer_load_dwordx4 v5, s[40:43], s74 offen lds\n"
    "s_add_u32 m0, s59, 98304\n"
    "s_mov_b32 s74, 294912\n"
    "buffer_load_dwordx4 v5, s[40:43], s74 offen lds\n"
    "s_add_u32 m0, s59, 102400\n"
    "s_mov_b32 s74, 295040\n"
    "buffer_load_dwordx4 v5, s[40:43], s74 offen lds\n"
    "s_add_u32 m0, s59, 106496\n"
    "s_mov_b32 s74, 295168\n"
    "buffer_load_dwordx4 v5, s[40:43], s74 offen lds\n"
    "s_add_u32 m0, s59, 110592\n"
    "s_mov_b32 s74, 331776\n"
    "buffer_load_dwordx4 v5, s[40:43], s74 offen lds\n"
    "s_add_u32 m0, s59, 114688\n"
    "s_mov_b32 s74, 331904\n"
    "buffer_load_dwordx4 v5, s[40:43], s74 offen lds\n"
    "s_add_u32 m0, s59, 118784\n"
    "s_mov_b32 s74, 332032\n"
    "buffer_load_dwordx4 v5, s[40:43], s74 offen lds\n"
    "v_add_u32 v16, 49152, v6\n"
    "v_add_u32 v17, 98304, v6\n"
    "v_add_u32 v18, 49152, v7\n"
    "v_add_u32 v19, 98304, v7\n"
    "s_waitcnt vmcnt(24) lgkmcnt(0)\n"
    "s_barrier\n"
    "s_add_u32 m0, s59, 122880\n"
    "s_mov_b32 s74, 368640\n"
    "buffer_load_dwordx4 v5, s[40:43], s74 offen lds\n"
    "s_add_u32 m0, s59, 126976\n"
    "s_mov_b32 s74, 368768\n"
    "buffer_load_dwordx4 v5, s[40:43], s74 offen lds\n"
    "s_add_u32 m0, s59, 131072\n"
    "s_mov_b32 s74, 368896\n"
    "buffer_load_dwordx4 v5, s[40:43], s74 offen lds\n"
    "s_add_u32 m0, s59, 135168\n"
    "s_mov_b32 s74, 405504\n"
    "buffer_load_dwordx4 v5, s[40:43], s74 offen lds\n"
    "s_add_u32 m0, s59, 139264\n"
    "s_mov_b32 s74, 405632\n"
    "buffer_load_dwordx4 v5, s[40:43], s74 offen lds\n"
    "s_add_u32 m0, s59, 143360\n"
    "s_mov_b32 s74, 405760\n"
    "buffer_load_dwordx4 v5, s[40:43], s74 offen lds\n"
    "ds_read_b64_tr_b16 a[0:1], v6 offset:0\n"
    "ds_read_b64_tr_b16 a[2:3], v6 offset:512\n"
    "ds_read_b64_tr_b16 a[4:5], v6 offset:8\n"
    "ds_read_b64_tr_b16 a[6:7], v6 offset:520\n"
    "ds_read_b64_tr_b16 a[8:9], v7 offset:0\n"
    "ds_read_b64_tr_b16 a[10:11], v7 offset:512\n"
    "ds_read_b64_tr_b16 a[12:13], v6 offset:12288\n"
    "ds_read_b64_tr_b16 a[14:15], v6 offset:12800\n"
    "ds_read_b64_tr_b16 a[16:17], v6 offset:12296\n"
    "ds_read_b64_tr_b16 a[18:19], v6 offset:12808\n"
    "ds_read_b64_tr_b16 a[20:21], v7 offset:12288\n"
    "ds_read_b64_tr_b16 a[22:23], v7 offset:12800\n"
    "s_waitcnt vmcnt(24) lgkmcnt(0)\n"
    "s_barrier\n"
    "s_add_u32 m0, s59, 0\n"
    "s_mov_b32 s74, 442368\n"
    "buffer_load_dwordx4 v5, s[40:43], s74 offen lds\n"
    "s_add_u32 m0, s59, 4096\n"
    "s_mov_b32 s74, 442496\n"
    "buffer_load_dwordx4 v5, s[40:43], s74 offen lds\n"
    "s_add_u32 m0, s59, 8192\n"
    "s_mov_b32 s74, 442624\n"
    "buffer_load_dwordx4 v5, s[40:43], s74 offen lds\n"
    "s_add_u32 m0, s59, 12288\n"
    "s_mov_b32 s74, 479232\n"
    "buffer_load_dwordx4 v5, s[40:43], s74 offen lds\n"
    "s_add_u32 m0, s59, 16384\n"
    "s_mov_b32 s74, 479360\n"
    "buffer_load_dwordx4 v5, s[40:43], s74 offen lds\n"
    "s_add_u32 m0, s59, 20480\n"
    "s_mov_b32 s74, 479488\n"
    "buffer_load_dwordx4 v5, s[40:43], s74 offen lds\n"
    "ds_read_b64_tr_b16 a[24:25], v6 offset:24576\n"
    "ds_read_b64_tr_b16 a[26:27], v6 offset:25088\n"
    "ds_read_b64_tr_b16 a[28:29], v6 offset:24584\n"
    "ds_read_b64_tr_b16 a[30:31], v6 offset:25096\n"
    "ds_read_b64_tr_b16 a[32:33], v7 offset:24576\n"
    "ds_read_b64_tr_b16 a[34:35], v7 offset:25088\n"
    "ds_read_b64_tr_b16 a[36:37], v6 offset:36864\n"
    "ds_read_b64_tr_b16 a[38:39], v6 offset:37376\n"
    "ds_read_b64_tr_b16 a[40:41], v6 offset:36872\n"
    "ds_read_b64_tr_b16 a[42:43], v6 offset:37384\n"
    "ds_read_b64_tr_b16 a[44:45], v7 offset:36864\n"
    "ds_read_b64_tr_b16 a[46:47], v7 offset:37376\n"
    "s_waitcnt vmcnt(24) lgkmcnt(0)\n"
    "s_barrier\n"
    "s_add_u32 m0, s59, 24576\n"
    "s_mov_b32 s74, 516096\n"
    "buffer_load_dwordx4 v5, s[40:43], s74 offen lds\n"
    "s_add_u32 m0, s59, 28672\n"
    "s_mov_b32 s74, 516224\n"
    "buffer_load_dwordx4 v5, s[40:43], s74 offen lds\n"
    "s_add_u32 m0, s59, 32768\n"
    "s_mov_b32 s74, 516352\n"
    "buffer_load_dwordx4 v5, s[40:43], s74 offen lds\n"
    "s_add_u32 m0, s59, 36864\n"
    "s_mov_b32 s74, 552960\n"
    "buffer_load_dwordx4 v5, s[40:43], s74 offen lds\n"
    "s_add_u32 m0, s59, 40960\n"
    "s_mov_b32 s74, 553088\n"
    "buffer_load_dwordx4 v5, s[40:43], s74 offen lds\n"
    "s_add_u32 m0, s59, 45056\n"
    "s_mov_b32 s74, 553216\n"
    "buffer_load_dwordx4 v5, s[40:43], s74 offen lds\n"
    "ds_read_b64_tr_b16 a[48:49], v16 offset:0\n"
    "ds_read_b64_tr_b16 a[50:51], v16 offset:512\n"
    "ds_read_b64_tr_b16 a[52:53], v16 offset:8\n"
    "ds_read_b64_tr_b16 a[54:55], v16 offset:520\n"
    "ds_read_b64_tr_b16 a[56:57], v18 offset:0\n"
    "ds_read_b64_tr_b16 a[58:59], v18 offset:512\n"
    "ds_read_b64_tr_b16 a[60:61], v16 offset:12288\n"
    "ds_read_b64_tr_b16 a[62:63], v16 offset:12800\n"
    "ds_read_b64_tr_b16 a[64:65], v16 offset:12296\n"
    "ds_read_b64_tr_b16 a[66:67], v16 offset:12808\n"
    "ds_read_b64_tr_b16 a[68:69], v18 offset:12288\n"
    "ds_read_b64_tr_b16 a[70:71], v18 offset:12800\n"
    "s_waitcnt vmcnt(24) lgkmcnt(0)\n"
    "s_barrier\n"
    "s_add_u32 m0, s59, 49152\n"
    "s_mov_b32 s74, 589824\n"
    "buffer_load_dwordx4 v5, s[40:43], s74 offen lds\n"
    "s_add_u32 m0, s59, 53248\n"
    "s_mov_b32 s74, 589952\n"
    "buffer_load_dwordx4 v5, s[40:43], s74 offen lds\n"
    "s_add_u32 m0, s59, 57344\n"
    "s_mov_b32 s74, 590080\n"
    "buffer_load_dwordx4 v5, s[40:43], s74 offen lds\n"
    "s_add_u32 m0, s59, 61440\n"
    "s_mov_b32 s74, 626688\n"
    "buffer_load_dwordx4 v5, s[40:43], s74 offen lds\n"
    "s_add_u32 m0, s59, 65536\n"
    "s_mov_b32 s74, 626816\n"
    "buffer_load_dwordx4 v5, s[40:43], s74 offen lds\n"
    "s_add_u32 m0, s59, 69632\n"
    "s_mov_b32 s74, 626944\n"
    "buffer_load_dwordx4 v5, s[40:43], s74 offen lds\n"
    "ds_read_b64_tr_b16 a[72:73], v16 offset:24576\n"
    "ds_read_b64_tr_b16 a[74:75], v16 offset:25088\n"
    "ds_read_b64_tr_b16 a[76:77], v16 offset:24584\n"
    "ds_read_b64_tr_b16 a[78:79], v16 offset:25096\n"
    "ds_read_b64_tr_b16 a[80:81], v18 offset:24576\n"
    "ds_read_b64_tr_b16 a[82:83], v18 offset:25088\n"
    "ds_read_b64_tr_b16 a[84:85], v16 offset:36864\n"
    "ds_read_b64_tr_b16 a[86:87], v16 offset:37376\n"
    "ds_read_b64_tr_b16 a[88:89], v16 offset:36872\n"
    "ds_read_b64_tr_b16 a[90:91], v16 offset:37384\n"
    "ds_read_b64_tr_b16 a[92:93], v18 offset:36864\n"
    "ds_read_b64_tr_b16 a[94:95], v18 offset:37376\n"
    "s_waitcnt vmcnt(24) lgkmcnt(0)\n"
    "s_barrier\n"
    "s_add_u32 m0, s59, 73728\n"
    "s_mov_b32 s74, 663552\n"
    "buffer_load_dwordx4 v5, s[40:43], s74 offen lds\n"
    "s_add_u32 m0, s59, 77824\n"
    "s_mov_b32 s74, 663680\n"
    "buffer_load_dwordx4 v5, s[40:43], s74 offen lds\n"
    "s_add_u32 m0, s59, 81920\n"
    "s_mov_b32 s74, 663808\n"
    "buffer_load_dwordx4 v5, s[40:43], s74 offen lds\n"
    "s_add_u32 m0, s59, 86016\n"
    "s_mov_b32 s74, 700416\n"
    "buffer_load_dwordx4 v5, s[40:43], s74 offen lds\n"
    "s_add_u32 m0, s59, 90112\n"
    "s_mov_b32 s74, 700544\n"
    "buffer_load_dwordx4 v5, s[40:43], s74 offen lds\n"
    "s_add_u32 m0, s59, 94208\n"
    "s_mov_b32 s74, 700672\n"
    "buffer_load_dwordx4 v5, s[40:43], s74 offen lds\n"
    "ds_read_b64_tr_b16 a[96:97], v17 offset:0\n"
    "ds_read_b64_tr_b16 a[98:99], v17 offset:512\n"
    "ds_read_b64_tr_b16 a[100:101], v17 offset:8\n"
    "ds_read_b64_tr_b16 a[102:103], v17 offset:520\n"
    "ds_read_b64_tr_b16 a[104:105], v19 offset:0\n"
    "ds_read_b64_tr_b16 a[106:107], v19 offset:512\n"
    "ds_read_b64_tr_b16 a[108:109], v17 offset:12288\n"
    "ds_read_b64_tr_b16 a[110:111], v17 offset:12800\n"
    "ds_read_b64_tr_b16 a[112:113], v17 offset:12296\n"
    "ds_read_b64_tr_b16 a[114:115], v17 offset:12808\n"
    "ds_read_b64_tr_b16 a[116:117], v19 offset:12288\n"
    "ds_read_b64_tr_b16 a[118:119], v19 offset:12800\n"
    "s_waitcnt vmcnt(24) lgkmcnt(0)\n"
    "s_barrier\n"
    "s_add_u32 m0, s59, 98304\n"
    "s_mov_b32 s74, 737280\n"
    "buffer_load_dwordx4 v5, s[40:43], s74 offen lds\n"
    "s_add_u32 m0, s59, 102400\n"
    "s_mov_b32 s74, 737408\n"
    "buffer_load_dwordx4 v5, s[40:43], s74 offen lds\n"
    "s_add_u32 m0, s59, 106496\n"
    "s_mov_b32 s74, 737536\n"
    "buffer_load_dwordx4 v5, s[40:43], s74 offen lds\n"
    "s_add_u32 m0, s59, 110592\n"
    "s_mov_b32 s74, 774144\n"
    "buffer_load_dwordx4 v5, s[40:43], s74 offen lds\n"
    "s_add_u32 m0, s59, 114688\n"
    "s_mov_b32 s74, 774272\n"
    "buffer_load_dwordx4 v5, s[40:43], s74 offen lds\n"
    "s_add_u32 m0, s59, 118784\n"
    "s_mov_b32 s74, 774400\n"
    "buffer_load_dwordx4 v5, s[40:43], s74 offen lds\n"
    "ds_read_b64_tr_b16 a[120:121], v17 offset:24576\n"
    "ds_read_b64_tr_b16 a[122:123], v17 offset:25088\n"
    "ds_read_b64_tr_b16 a[124:125], v17 offset:24584\n"
    "ds_read_b64_tr_b16 a[126:127], v17 offset:25096\n"
    "ds_read_b64_tr_b16 a[128:129], v19 offset:24576\n"
    "ds_read_b64_tr_b16 a[130:131], v19 offset:25088\n"
    "ds_read_b64_tr_b16 a[132:133], v17 offset:36864\n"
    "ds_read_b64_tr_b16 a[134:135], v17 offset:37376\n"
    "ds_read_b64_tr_b16 a[136:137], v17 offset:36872\n"
    "ds_read_b64_tr_b16 a[138:139], v17 offset:37384\n"
    "ds_read_b64_tr_b16 a[140:141], v19 offset:36864\n"
    "ds_read_b64_tr_b16 a[142:143], v19 offset:37376\n"
    "s_waitcnt vmcnt(24) lgkmcnt(0)\n"
    "s_barrier\n"
    "s_add_u32 m0, s59, 122880\n"
    "s_mov_b32 s74, 811008\n"
    "buffer_load_dwordx4 v5, s[40:43], s74 offen lds\n"
    "s_add_u32 m0, s59, 126976\n"
    "s_mov_b32 s74, 811136\n"
    "buffer_load_dwordx4 v5, s[40:43], s74 offen lds\n"
    "s_add_u32 m0, s59, 131072\n"
    "s_mov_b32 s74, 811264\n"
    "buffer_load_dwordx4 v5, s[40:43], s74 offen lds\n"
    "s_add_u32 m0, s59, 135168\n"
    "s_mov_b32 s74, 847872\n"
    "buffer_load_dwordx4 v5, s[40:43], s74 offen lds\n"
    "s_add_u32 m0, s59, 139264\n"
    "s_mov_b32 s74, 848000\n"
    "buffer_load_dwordx4 v5, s[40:43], s74 offen lds\n"
    "s_add_u32 m0, s59, 143360\n"
    "s_mov_b32 s74, 848128\n"
    "buffer_load_dwordx4 v5, s[40:43], s74 offen lds\n"
    "ds_read_b64_tr_b16 a[144:145], v6 offset:0\n"
    "ds_read_b64_tr_b16 a[146:147], v6 offset:512\n"
    "ds_read_b64_tr_b16 a[148:149], v6 offset:8\n"
    "ds_read_b64_tr_b16 a[150:151], v6 offset:520\n"
    "ds_read_b64_tr_b16 a[152:153], v7 offset:0\n"
    "ds_read_b64_tr_b16 a[154:155], v7 offset:512\n"
    "ds_read_b64_tr_b16 a[156:157], v6 offset:12288\n"
    "ds_read_b64_tr_b16 a[158:159], v6 offset:12800\n"
    "ds_read_b64_tr_b16 a[160:161], v6 offset:12296\n"
    "ds_read_b64_tr_b16 a[162:163], v6 offset:12808\n"
    "ds_read_b64_tr_b16 a[164:165], v7 offset:12288\n"
    "ds_read_b64_tr_b16 a[166:167], v7 offset:12800\n"
    "s_waitcnt vmcnt(24) lgkmcnt(0)\n"
    "s_barrier\n"
    "s_add_u32 m0, s59, 0\n"
    "s_mov_b32 s74, 884736\n"
    "buffer_load_dwordx4 v5, s[40:43], s74 offen lds\n"
    "s_add_u32 m0, s59, 4096\n"
    "s_mov_b32 s74, 884864\n"
    "buffer_load_dwordx4 v5, s[40:43], s74 offen lds\n"
    "s_add_u32 m0, s59, 8192\n"
    "s_mov_b32 s74, 884992\n"
    "buffer_load_dwordx4 v5, s[40:43], s74 offen lds\n"
    "s_add_u32 m0, s59, 12288\n"
    "s_mov_b32 s74, 921600\n"
    "buffer_load_dwordx4 v5, s[40:43], s74 offen lds\n"
    "s_add_u32 m0, s59, 16384\n"
    "s_mov_b32 s74, 921728\n"
    "buffer_load_dwordx4 v5, s[40:43], s74 offen lds\n"
    "s_add_u32 m0, s59, 20480\n"
    "s_mov_b32 s74, 921856\n"
    "buffer_load_dwordx4 v5, s[40:43], s74 offen lds\n"
    "ds_read_b64_tr_b16 a[168:169], v6 offset:24576\n"
    "ds_read_b64_tr_b16 a[170:171], v6 offset:25088\n"
    "ds_read_b64_tr_b16 a[172:173], v6 offset:24584\n"
    "ds_read_b64_tr_b16 a[174:175], v6 offset:25096\n"
    "ds_read_b64_tr_b16 a[176:177], v7 offset:24576\n"
    "ds_read_b64_tr_b16 a[178:179], v7 offset:25088\n"
    "ds_read_b64_tr_b16 a[180:181], v6 offset:36864\n"
    "ds_read_b64_tr_b16 a[182:183], v6 offset:37376\n"
    "ds_read_b64_tr_b16 a[184:185], v6 offset:36872\n"
    "ds_read_b64_tr_b16 a[186:187], v6 offset:37384\n"
    "ds_read_b64_tr_b16 a[188:189], v7 offset:36864\n"
    "ds_read_b64_tr_b16 a[190:191], v7 offset:37376\n"
    "s_waitcnt vmcnt(24) lgkmcnt(0)\n"
    "s_barrier\n"
    "s_add_u32 m0, s59, 24576\n"
    "s_mov_b32 s74, 958464\n"
    "buffer_load_dwordx4 v5, s[40:43], s74 offen lds\n"
    "s_add_u32 m0, s59, 28672\n"
    "s_mov_b32 s74, 958592\n"
    "buffer_load_dwordx4 v5, s[40:43], s74 offen lds\n"
    "s_add_u32 m0, s59, 32768\n"
    "s_mov_b32 s74, 958720\n"
    "buffer_load_dwordx4 v5, s[40:43], s74 offen lds\n"
    "s_add_u32 m0, s59, 36864\n"
    "s_mov_b32 s74, 995328\n"
    "buffer_load_dwordx4 v5, s[40:43], s74 offen lds\n"
    "s_add_u32 m0, s59, 40960\n"
    "s_mov_b32 s74, 995456\n"
    "buffer_load_dwordx4 v5, s[40:43], s74 offen lds\n"
    "s_add_u32 m0, s59, 45056\n"
    "s_mov_b32 s74, 995584\n"
    "buffer_load_dwordx4 v5, s[40:43], s74 offen lds\n"
    "ds_read_b64_tr_b16 a[192:193], v16 offset:0\n"
    "ds_read_b64_tr_b16 a[194:195], v16 offset:512\n"
    "ds_read_b64_tr_b16 a[196:197], v16 offset:8\n"
    "ds_read_b64_tr_b16 a[198:199], v16 offset:520\n"
    "ds_read_b64_tr_b16 a[200:201], v18 offset:0\n"
    "ds_read_b64_tr_b16 a[202:203], v18 offset:512\n"
    "ds_read_b64_tr_b16 a[204:205], v16 offset:12288\n"
    "ds_read_b64_tr_b16 a[206:207], v16 offset:12800\n"
    "ds_read_b64_tr_b16 a[208:209], v16 offset:12296\n"
    "ds_read_b64_tr_b16 a[210:211], v16 offset:12808\n"
    "ds_read_b64_tr_b16 a[212:213], v18 offset:12288\n"
    "ds_read_b64_tr_b16 a[214:215], v18 offset:12800\n"
    "s_waitcnt vmcnt(24) lgkmcnt(0)\n"
    "s_barrier\n"
    "s_add_u32 m0, s59, 49152\n"
    "s_mov_b32 s74, 1032192\n"
    "buffer_load_dwordx4 v5, s[40:43], s74 offen lds\n"
    "s_add_u32 m0, s59, 53248\n"
    "s_mov_b32 s74, 1032320\n"
    "buffer_load_dwordx4 v5, s[40:43], s74 offen lds\n"
    "s_add_u32 m0, s59, 57344\n"
    "s_mov_b32 s74, 1032448\n"
    "buffer_load_dwordx4 v5, s[40:43], s74 offen lds\n"
    "s_add_u32 m0, s59, 61440\n"
    "s_mov_b32 s74, 1069056\n"
    "buffer_load_dwordx4 v5, s[40:43], s74 offen lds\n"
    "s_add_u32 m0, s59, 65536\n"
    "s_mov_b32 s74, 1069184\n"
    "buffer_load_dwordx4 v5, s[40:43], s74 offen lds\n"
    "s_add_u32 m0, s59, 69632\n"
    "s_mov_b32 s74, 1069312\n"
    "buffer_load_dwordx4 v5, s[40:43], s74 offen lds\n"
    "ds_read_b64_tr_b16 a[216:217], v16 offset:24576\n"
    "ds_read_b64_tr_b16 a[218:219], v16 offset:25088\n"
    "ds_read_b64_tr_b16 a[220:221], v16 offset:24584\n"
    "ds_read_b64_tr_b16 a[222:223], v16 offset:25096\n"
    "ds_read_b64_tr_b16 a[224:225], v18 offset:24576\n"
    "ds_read_b64_tr_b16 a[226:227], v18 offset:25088\n"
    "ds_read_b64_tr_b16 a[228:229], v16 offset:36864\n"
    "ds_read_b64_tr_b16 a[230:231], v16 offset:37376\n"
    "ds_read_b64_tr_b16 a[232:233], v16 offset:36872\n"
    "ds_read_b64_tr_b16 a[234:235], v16 offset:37384\n"
    "ds_read_b64_tr_b16 a[236:237], v18 offset:36864\n"
    "ds_read_b64_tr_b16 a[238:239], v18 offset:37376\n"
    "s_waitcnt vmcnt(24) lgkmcnt(0)\n"
    "s_barrier\n"
    "s_add_u32 m0, s59, 73728\n"
    "s_mov_b32 s74, 1105920\n"
    "buffer_load_dwordx4 v5, s[40:43], s74 offen lds\n"
    "s_add_u32 m0, s59, 77824\n"
    "s_mov_b32 s74, 1106048\n"
    "buffer_load_dwordx4 v5, s[40:43], s74 offen lds\n"
    "s_add_u32 m0, s59, 81920\n"
    "s_mov_b32 s74, 1106176\n"
    "buffer_load_dwordx4 v5, s[40:43], s74 offen lds\n"
    "s_add_u32 m0, s59, 86016\n"
    "s_mov_b32 s74, 1142784\n"
    "buffer_load_dwordx4 v5, s[40:43], s74 offen lds\n"
    "s_add_u32 m0, s59, 90112\n"
    "s_mov_b32 s74, 1142912\n"
    "buffer_load_dwordx4 v5, s[40:43], s74 offen lds\n"
    "s_add_u32 m0, s59, 94208\n"
    "s_mov_b32 s74, 1143040\n"
    "buffer_load_dwordx4 v5, s[40:43], s74 offen lds\n"
    "buffer_load_dwordx2 v[12:13], off, s[36:39], 0 sc1\n"
    "ds_read_b64_tr_b16 a[240:241], v17 offset:0\n"
    "ds_read_b64_tr_b16 a[242:243], v17 offset:512\n"
    "ds_read_b64_tr_b16 a[244:245], v17 offset:8\n"
    "ds_read_b64_tr_b16 a[246:247], v17 offset:520\n"
    "ds_read_b64_tr_b16 a[248:249], v19 offset:0\n"
    "ds_read_b64_tr_b16 a[250:251], v19 offset:512\n"
    "ds_read_b64_tr_b16 a[252:253], v17 offset:12288\n"
    "ds_read_b64_tr_b16 a[254:255], v17 offset:12800\n"
    "ds_read_b64_tr_b16 v[128:129], v17 offset:12296\n"
    "ds_read_b64_tr_b16 v[130:131], v17 offset:12808\n"
    "ds_read_b64_tr_b16 v[132:133], v19 offset:12288\n"
    "ds_read_b64_tr_b16 v[134:135], v19 offset:12800\n"
    "s_waitcnt vmcnt(25) lgkmcnt(0)\n"
    "s_barrier\n"
    "ds_read_b64_tr_b16 v[136:137], v17 offset:24576\n"
    "ds_read_b64_tr_b16 v[138:139], v17 offset:25088\n"
    "ds_read_b64_tr_b16 v[140:141], v17 offset:24584\n"
    "ds_read_b64_tr_b16 v[142:143], v17 offset:25096\n"
    "ds_read_b64_tr_b16 v[144:145], v19 offset:24576\n"
    "ds_read_b64_tr_b16 v[146:147], v19 offset:25088\n"
    "ds_read_b64_tr_b16 v[148:149], v17 offset:36864\n"
    "ds_read_b64_tr_b16 v[150:151], v17 offset:37376\n"
    "ds_read_b64_tr_b16 v[152:153], v17 offset:36872\n"
    "ds_read_b64_tr_b16 v[154:155], v17 offset:37384\n"
    "ds_read_b64_tr_b16 v[156:157], v19 offset:36864\n"
    "ds_read_b64_tr_b16 v[158:159], v19 offset:37376\n"
    "s_waitcnt vmcnt(19) lgkmcnt(0)\n"
    "s_barrier\n"
    "ds_read_b64_tr_b16 v[160:161], v6 offset:0\n"
    "ds_read_b64_tr_b16 v[162:163], v6 offset:512\n"
    "ds_read_b64_tr_b16 v[164:165], v6 offset:8\n"
    "ds_read_b64_tr_b16 v[166:167], v6 offset:520\n"
    "ds_read_b64_tr_b16 v[168:169], v7 offset:0\n"
    "ds_read_b64_tr_b16 v[170:171], v7 offset:512\n"
    "ds_read_b64_tr_b16 v[172:173], v6 offset:12288\n"
    "ds_read_b64_tr_b16 v[174:175], v6 offset:12800\n"
    "ds_read_b64_tr_b16 v[176:177], v6 offset:12296\n"
    "ds_read_b64_tr_b16 v[178:179], v6 offset:12808\n"
    "ds_read_b64_tr_b16 v[180:181], v7 offset:12288\n"
    "ds_read_b64_tr_b16 v[182:183], v7 offset:12800\n"
    "s_waitcnt vmcnt(13) lgkmcnt(0)\n"
    "s_barrier\n"
    "ds_read_b64_tr_b16 v[184:185], v6 offset:24576\n"
    "ds_read_b64_tr_b16 v[186:187], v6 offset:25088\n"
    "ds_read_b64_tr_b16 v[188:189], v6 offset:24584\n"
    "ds_read_b64_tr_b16 v[190:191], v6 offset:25096\n"
    "ds_read_b64_tr_b16 v[192:193], v7 offset:24576\n"
    "ds_read_b64_tr_b16 v[194:195], v7 offset:25088\n"
    "ds_read_b64_tr_b16 v[196:197], v6 offset:36864\n"
    "ds_read_b64_tr_b16 v[198:199], v6 offset:37376\n"
    "ds_read_b64_tr_b16 v[200:201], v6 offset:36872\n"
    "ds_read_b64_tr_b16 v[202:203], v6 offset:37384\n"
    "ds_read_b64_tr_b16 v[204:205], v7 offset:36864\n"
    "ds_read_b64_tr_b16 v[206:207], v7 offset:37376\n"
    "s_waitcnt vmcnt(7) lgkmcnt(0)\n"
    "s_barrier\n"
    "ds_read_b64_tr_b16 v[208:209], v16 offset:0\n"
    "ds_read_b64_tr_b16 v[210:211], v16 offset:512\n"
    "ds_read_b64_tr_b16 v[212:213], v16 offset:8\n"
    "ds_read_b64_tr_b16 v[214:215], v16 offset:520\n"
    "ds_read_b64_tr_b16 v[216:217], v18 offset:0\n"
    "ds_read_b64_tr_b16 v[218:219], v18 offset:512\n"
    "ds_read_b64_tr_b16 v[220:221], v16 offset:12288\n"
    "ds_read_b64_tr_b16 v[222:223], v16 offset:12800\n"
    "ds_read_b64_tr_b16 v[224:225], v16 offset:12296\n"
    "ds_read_b64_tr_b16 v[226:227], v16 offset:12808\n"
    "ds_read_b64_tr_b16 v[228:229], v18 offset:12288\n"
    "ds_read_b64_tr_b16 v[230:231], v18 offset:12800\n"
    "s_waitcnt vmcnt(0) lgkmcnt(0)\n"
    "s_barrier\n"
    ".LgWO_par_%=:\n"
    "v_readfirstlane_b32 s74, v12\n"
    "v_readfirstlane_b32 s75, v13\n"
    "s_xor_b32 s74, s74, s66\n"
    "s_xor_b32 s75, s75, s67\n"
    "s_cselect_b32 s75, 2, 0\n"
    "s_or_b32 s75, s75, s74\n"
    "s_cmp_lt_u32 s75, 2\n"
    "s_cbranch_scc1 .LgWO_par1_%=\n"
    "s_sleep 4\n"
    "buffer_load_dwordx2 v[12:13], off, s[36:39], 0 sc1\n"
    "s_waitcnt vmcnt(0)\n"
    "s_sub_u32 s72, s72, 1\n"
    "s_cbranch_scc0 .LgWO_par_%=\n"
    "s_mov_b32 s72, 0\n"
    ".LgWO_par1_%=:\n"
    "s_and_b32 s74, s74, 1\n"
    "s_lshl_b32 s74, s74, 13\n"
    "s_add_u32 s36, s36, s74\n"
    "s_addc_u32 s37, s37, 0\n"
    "v_add_u32 v13, v11, v3\n"
    "v_cmp_gt_i32 vcc, s71, v13\n"
    "s_nop 0\n"
    "s_or_b64 exec, vcc, 1\n"
    "buffer_atomic_smax v11, v2, s[36:39], 0 offen sc0 sc1\n"
    "s_mov_b64 exec, -1\n"
    "ds_read_b64_tr_b16 v[232:233], v16 offset:24576\n"
    "ds_read_b64_tr_b16 v[234:235], v16 offset:25088\n"
    "ds_read_b64_tr_b16 v[236:237], v16 offset:24584\n"
    "ds_read_b64_tr_b16 v[238:239], v16 offset:25096\n"
    "ds_read_b64_tr_b16 v[240:241], v18 offset:24576\n"
    "ds_read_b64_tr_b16 v[242:243], v18 offset:25088\n"
    "ds_read_b64_tr_b16 v[244:245], v16 offset:36864\n"
    "ds_read_b64_tr_b16 v[246:247], v16 offset:37376\n"
    "ds_read_b64_tr_b16 v[248:249], v16 offset:36872\n"
    "ds_read_b64_tr_b16 v[250:251], v16 offset:37384\n"
    "ds_read_b64_tr_b16 v[252:253], v18 offset:36864\n"
    "ds_read_b64_tr_b16 v[254:255], v18 offset:37376\n"
    "s_waitcnt lgkmcnt(0)\n"
    "s_barrier\n"
    "s_waitcnt vmcnt(0)\n"
    "s_branch .LgWO_block1_%=\n"
    ".LgWO_LOOPB_%=:\n"
    ".LgWO_head_%=:\n"
    "v_mfma_f32_16x16x32_bf16 v[64:67], a[8:11], v[16:19], 0\n"
    "s_waitcnt lgkmcnt(6)\n"
    "ds_read_b128 v[16:19], v9 offset:0\n"
    "v_mfma_f32_16x16x32_bf16 v[68:71], a[8:11], v[20:23], 0\n"
    "s_waitcnt lgkmcnt(6)\n"
    "ds_read_b128 v[20:23], v9 offset:2048\n"
    "v_mfma_f32_16x16x32_bf16 v[64:67], a[20:23], v[24:27], v[64:67]\n"
    "s_waitcnt lgkmcnt(6)\n"
    "ds_read_b128 v[24:27], v9 offset:4096\n"
    "v_mfma_f32_16x16x32_bf16 v[68:71], a[20:23], v[28:31], v[68:71]\n"
    "s_waitcnt lgkmcnt(6)\n"
    "ds_read_b128 v[28:31], v9 offset:6144\n"
    "v_mfma_f32_16x16x32_bf16 v[64:67], a[32:35], v[32:35], v[64:67]\n"
    "s_waitcnt lgkmcnt(6)\n"
    "ds_read_b128 v[32:35], v9 offset:1024\n"
    "v_mfma_f32_16x16x32_bf16 v[68:71], a[32:35], v[36:39], v[68:71]\n"
    "s_waitcnt lgkmcnt(6)\n"
    "ds_read_b128 v[36:39], v9 offset:3072\n"
    "v_mfma_f32_16x16x32_bf16 v[64:67], a[44:47], v[40:43], v[64:67]\n"
    "s_waitcnt lgkmcnt(6)\n"
    "ds_read_b128 v[40:43], v9 offset:5120\n"
    "v_mfma_f32_16x16x32_bf16 v[68:71], a[44:47], v[44:47], v[68:71]\n"
    "s_waitcnt lgkmcnt(6)\n"
    "ds_read_b128 v[44:47], v9 offset:7168\n"
    "v_mfma_f32_16x16x32_bf16 v[48:51], a[0:3], v[16:19], 0\n"
    "s_waitcnt lgkmcnt(6)\n"
    "v_cvt_pk_bf16_f32 v56, v56, v57\n"
    "v_cvt_pk_bf16_f32 v57, v58, v59\n"
    "v_mfma_f32_16x16x32_bf16 v[52:55], a[4:7], v[16:19], 0\n"
    "ds_read_b128 v[16:19], v9 offset:8192\n"
    "v_cvt_pk_bf16_f32 v58, v60, v61\n"
    "v_mfma_f32_16x16x32_bf16 v[48:51], a[12:15], v[20:23], v[48:51]\n"
    "s_waitcnt lgkmcnt(6)\n"
    "v_cvt_pk_bf16_f32 v59, v62, v63\n"
    "v_mfma_f32_16x16x32_bf16 v[52:55], a[16:19], v[20:23], v[52:55]\n"
    "ds_read_b128 v[20:23], v9 offset:9216\n"
    "v_mfma_f32_16x16x32_bf16 v[48:51], a[24:27], v[24:27], v[48:51]\n"
    "s_waitcnt lgkmcnt(6)\n"
    "s_sub_u32 s74, s68, 1\n"
    "buffer_store_dwordx4 v[56:59], v15, s[36:39], s73 offen offset:1024 sc1\n"
    "v_mfma_f32_16x16x32_bf16 v[52:55], a[28:31], v[24:27], v[52:55]\n"
    "ds_read_b128 v[24:27], v9 offset:10240\n"
    "v_mfma_f32_16x16x32_bf16 v[48:51], a[36:39], v[28:31], v[48:51]\n"
    "s_waitcnt lgkmcnt(6)\n"
    "s_and_b32 s76, s71, s54\n"
    "v_mfma_f32_16x16x32_bf16 v[52:55], a[40:43], v[28:31], v[52:55]\n"
    "ds_read_b128 v[28:31], v9 offset:11264\n"
    "v_mfma_f32_16x16x32_bf16 v[56:59], a[0:3], v[32:35], 0\n"
    "s_waitcnt lgkmcnt(6)\n"
    "s_mul_i32 s76, s76, s53\n"
    "v_mfma_f32_16x16x32_bf16 v[60:63], a[4:7], v[32:35], 0\n"
    "ds_read_b128 v[32:35], v9 offset:12288\n"
    "v_mfma_f32_16x16x32_bf16 v[56:59], a[12:15], v[36:39], v[56:59]\n"
    "s_waitcnt lgkmcnt(6)\n"
    "s_add_u32 s76, s76, s52\n"
    "v_mfma_f32_16x16x32_bf16 v[60:63], a[16:19], v[36:39], v[60:63]\n"
    "ds_read_b128 v[36:39], v9 offset:13312\n"
    "v_mfma_f32_16x16x32_bf16 v[56:59], a[24:27], v[40:43], v[56:59]\n"
    "s_waitcnt lgkmcnt(6)\n"
    "s_and_b32 s77, s71, 1\n"
    "v_mfma_f32_16x16x32_bf16 v[60:63], a[28:31], v[40:43], v[60:63]\n"
    "ds_read_b128 v[40:43], v9 offset:14336\n"
    "v_mfma_f32_16x16x32_bf16 v[56:59], a[36:39], v[44:47], v[56:59]\n"
    "s_waitcnt lgkmcnt(6)\n"
    "s_mul_i32 s77, s77, 65536\n"
    "v_mfma_f32_16x16x32_bf16 v[60:63], a[40:43], v[44:47], v[60:63]\n"
    "ds_read_b128 v[44:47], v9 offset:15360\n"
    "v_mfma_f32_16x16x32_bf16 v[48:51], a[48:51], v[16:19], v[48:51]\n"
    "s_add_u32 s77, s77, s59\n"
    "v_mfma_f32_16x16x32_bf16 v[52:55], a[52:55], v[16:19], v[52:55]\n"
    "s_waitcnt lgkmcnt(6)\n"
    "v_mfma_f32_16x16x32_bf16 v[64:67], a[56:59], v[16:19], v[64:67]\n"
    "ds_read_b128 v[16:19], v9 offset:16384\n"
    "v_mfma_f32_16x16x32_bf16 v[56:59], a[48:51], v[20:23], v[56:59]\n"
    "v_mfma_f32_16x16x32_bf16 v[60:63], a[52:55], v[20:23], v[60:63]\n"
    "s_waitcnt lgkmcnt(6)\n"
    "v_mfma_f32_16x16x32_bf16 v[68:71], a[56:59], v[20:23], v[68:71]\n"
    "ds_read_b128 v[20:23], v9 offset:17408\n"
    "v_mfma_f32_16x16x32_bf16 v[48:51], a[60:63], v[24:27], v[48:51]\n"
    "v_mfma_f32_16x16x32_bf16 v[52:55], a[64:67], v[24:27], v[52:55]\n"
    "s_waitcnt lgkmcnt(6)\n"
    "v_mfma_f32_16x16x32_bf16 v[64:67], a[68:71], v[24:27], v[64:67]\n"
    "ds_read_b128 v[24:27], v9 offset:18432\n"
    "v_mfma_f32_16x16x32_bf16 v[56:59], a[60:63], v[28:31], v[56:59]\n"
    "v_mfma_f32_16x16x32_bf16 v[60:63], a[64:67], v[28:31], v[60:63]\n"
    "s_waitcnt lgkmcnt(6)\n"
    "v_mfma_f32_16x16x32_bf16 v[68:71], a[68:71], v[28:31], v[68:71]\n"
    "ds_read_b128 v[28:31], v9 offset:19456\n"
    "v_mfma_f32_16x16x32_bf16 v[48:51], a[72:75], v[32:35], v[48:51]\n"
    "v_mfma_f32_16x16x32_bf16 v[52:55], a[76:79], v[32:35], v[52:55]\n"
    "s_waitcnt lgkmcnt(6)\n"
    "v_mfma_f32_16x16x32_bf16 v[64:67], a[80:83], v[32:35], v[64:67]\n"
    "ds_read_b128 v[32:35], v9 offset:20480\n"
    "v_mfma_f32_16x16x32_bf16 v[56:59], a[72:75], v[36:39], v[56:59]\n"
    "v_mfma_f32_16x16x32_bf16 v[60:63], a[76:79], v[36:39], v[60:63]\n"
    "s_waitcnt lgkmcnt(6)\n"
    "v_mfma_f32_16x16x32_bf16 v[68:71], a[80:83], v[36:39], v[68:71]\n"
    "ds_read_b128 v[36:39], v9 offset:21504\n"
    "v_mfma_f32_16x16x32_bf16 v[48:51], a[84:87], v[40:43], v[48:51]\n"
    "v_mov_b32 v12, s74\n"
    "v_mfma_f32_16x16x32_bf16 v[52:55], a[88:91], v[40:43], v[52:55]\n"
    "s_waitcnt lgkmcnt(6)\n"
    "s_waitcnt vmcnt(0)\n"
    "v_add_u32 v13, v11, v3\n"
    "v_mfma_f32_16x16x32_bf16 v[64:67], a[92:95], v[40:43], v[64:67]\n"
    "ds_read_b128 v[40:43], v9 offset:22528\n"
    "buffer_atomic_smax v12, v14, s[36:39], 0 offen sc1\n"
    "v_cmp_gt_i32 vcc, s71, v13\n"
    "v_mfma_f32_16x16x32_bf16 v[56:59], a[84:87], v[44:47], v[56:59]\n"
    "v_add_u32 v10, 49152, v9\n"
    "v_mfma_f32_16x16x32_bf16 v[60:63], a[88:91], v[44:47], v[60:63]\n"
    "s_waitcnt lgkmcnt(6)\n"
    "v_mfma_f32_16x16x32_bf16 v[68:71], a[92:95], v[44:47], v[68:71]\n"
    "ds_read_b128 v[44:47], v9 offset:23552\n"
    ".LgWO_mid_%=:\n"
    "s_cbranch_vccnz .LgWO_chk0_%=\n"
    "v_mfma_f32_16x16x32_bf16 v[48:51], a[96:99], v[16:19], v[48:51]\n"
    "s_add_u32 m0, s77, s59\n"
    "s_add_u32 s77, s77, 8192\n"
    "buffer_load_dwordx4 v1, s[36:39], s76 offen sc1 lds\n"
    "v_mfma_f32_16x16x32_bf16 v[52:55], a[100:103], v[16:19], v[52:55]\n"
    "s_waitcnt lgkmcnt(6)\n"
    "buffer_load_dwordx4 v1, s[36:39], s76 offen offset:1024 sc1 lds\n"
    "s_add_u32 s76, s76, 16384\n"
    "v_mfma_f32_16x16x32_bf16 v[64:67], a[104:107], v[16:19], v[64:67]\n"
    "ds_read_b128 v[16:19], v9 offset:24576\n"
    "v_mfma_f32_16x16x32_bf16 v[56:59], a[96:99], v[20:23], v[56:59]\n"
    "s_mov_b32 s70, 1\n"
    "v_mfma_f32_16x16x32_bf16 v[60:63], a[100:103], v[20:23], v[60:63]\n"
    "s_waitcnt lgkmcnt(6)\n"
    "s_add_u32 m0, s77, s59\n"
    "s_add_u32 s77, s77, 8192\n"
    "buffer_load_dwordx4 v1, s[36:39], s76 offen sc1 lds\n"
    "v_mfma_f32_16x16x32_bf16 v[68:71], a[104:107], v[20:23], v[68:71]\n"
    "ds_read_b128 v[20:23], v9 offset:25600\n"
    "v_mfma_f32_16x16x32_bf16 v[48:51], a[108:111], v[24:27], v[48:51]\n"
    "buffer_load_dwordx4 v1, s[36:39], s76 offen offset:1024 sc1 lds\n"
    "s_add_u32 s76, s76, 16384\n"
    "v_mfma_f32_16x16x32_bf16 v[52:55], a[112:115], v[24:27], v[52:55]\n"
    "s_waitcnt lgkmcnt(6)\n"
    "s_and_b32 s73, s68, s57\n"
    "v_mfma_f32_16x16x32_bf16 v[64:67], a[116:119], v[24:27], v[64:67]\n"
    "ds_read_b128 v[24:27], v9 offset:26624\n"
    "v_mfma_f32_16x16x32_bf16 v[56:59], a[108:111], v[28:31], v[56:59]\n"
    "s_add_u32 m0, s77, s59\n"
    "s_add_u32 s77, s77, 8192\n"
    "buffer_load_dwordx4 v1, s[36:39], s76 offen sc1 lds\n"
    "v_mfma_f32_16x16x32_bf16 v[60:63], a[112:115], v[28:31], v[60:63]\n"
    "s_waitcnt lgkmcnt(6)\n"
    "buffer_load_dwordx4 v1, s[36:39], s76 offen offset:1024 sc1 lds\n"
    "s_add_u32 s76, s76, 16384\n"
    "v_mfma_f32_16x16x32_bf16 v[68:71], a[116:119], v[28:31], v[68:71]\n"
    "ds_read_b128 v[28:31], v9 offset:27648\n"
    "v_mfma_f32_16x16x32_bf16 v[48:51], a[120:123], v[32:35], v[48:51]\n"
    "s_mul_i32 s73, s73, s56\n"
    "v_mfma_f32_16x16x32_bf16 v[52:55], a[124:127], v[32:35], v[52:55]\n"
    "s_waitcnt lgkmcnt(6)\n"
    "s_add_u32 m0, s77, s59\n"
    "s_add_u32 s77, s77, 8192\n"
    "buffer_load_dwordx4 v1, s[36:39], s76 offen sc1 lds\n"
    "v_mfma_f32_16x16x32_bf16 v[64:67], a[128:131], v[32:35], v[64:67]\n"
    "ds_read_b128 v[32:35], v9 offset:28672\n"
    "v_mfma_f32_16x16x32_bf16 v[56:59], a[120:123], v[36:39], v[56:59]\n"
    "buffer_load_dwordx4 v1, s[36:39], s76 offen offset:1024 sc1 lds\n"
    "s_add_u32 s76, s76, 16384\n"
    "v_mfma_f32_16x16x32_bf16 v[60:63], a[124:127], v[36:39], v[60:63]\n"
    "s_waitcnt lgkmcnt(6)\n"
    "s_add_u32 s73, s73, s55\n"
    "v_mfma_f32_16x16x32_bf16 v[68:71], a[128:131], v[36:39], v[68:71]\n"
    "ds_read_b128 v[36:39], v9 offset:29696\n"
    "v_mfma_f32_16x16x32_bf16 v[48:51], a[132:135], v[40:43], v[48:51]\n"
    "s_add_u32 m0, s77, s59\n"
    "s_add_u32 s77, s77, 8192\n"
    "buffer_load_dwordx4 v1, s[36:39], s76 offen sc1 lds\n"
    "v_mfma_f32_16x16x32_bf16 v[52:55], a[136:139], v[40:43], v[52:55]\n"
    "s_waitcnt lgkmcnt(6)\n"
    "v_mfma_f32_16x16x32_bf16 v[64:67], a[140:143], v[40:43], v[64:67]\n"
    "ds_read_b128 v[40:43], v9 offset:30720\n"
    "v_mfma_f32_16x16x32_bf16 v[56:59], a[132:135], v[44:47], v[56:59]\n"
    "buffer_load_dwordx4 v1, s[36:39], s76 offen offset:1024 sc1 lds\n"
    "s_add_u32 s76, s76, 16384\n"
    "v_mfma_f32_16x16x32_bf16 v[60:63], a[136:139], v[44:47], v[60:63]\n"
    "s_waitcnt lgkmcnt(6)\n"
    "s_add_u32 m0, s77, s59\n"
    "s_add_u32 s77, s77, 8192\n"
    "buffer_load_dwordx4 v1, s[36:39], s76 offen sc1 lds\n"
    "v_mfma_f32_16x16x32_bf16 v[68:71], a[140:143], v[44:47], v[68:71]\n"
    "ds_read_b128 v[44:47], v9 offset:31744\n"
    "v_mfma_f32_16x16x32_bf16 v[48:51], a[144:147], v[16:19], v[48:51]\n"
    "v_mfma_f32_16x16x32_bf16 v[52:55], a[148:151], v[16:19], v[52:55]\n"
    "s_waitcnt lgkmcnt(6)\n"
    "buffer_load_dwordx4 v1, s[36:39], s76 offen offset:1024 sc1 lds\n"
    "s_add_u32 s76, s76, 16384\n"
    "v_mfma_f32_16x16x32_bf16 v[64:67], a[152:155], v[16:19], v[64:67]\n"
    "ds_read_b128 v[16:19], v9 offset:32768\n"
    "v_mfma_f32_16x16x32_bf16 v[56:59], a[144:147], v[20:23], v[56:59]\n"
    "s_add_u32 m0, s77, s59\n"
    "s_add_u32 s77, s77, 8192\n"
    "buffer_load_dwordx4 v1, s[36:39], s76 offen sc1 lds\n"
    "v_mfma_f32_16x16x32_bf16 v[60:63], a[148:151], v[20:23], v[60:63]\n"
    "s_waitcnt lgkmcnt(6)\n"
    "v_mfma_f32_16x16x32_bf16 v[68:71], a[152:155], v[20:23], v[68:71]\n"
    "ds_read_b128 v[20:23], v9 offset:33792\n"
    "v_mfma_f32_16x16x32_bf16 v[48:51], a[156:159], v[24:27], v[48:51]\n"
    "buffer_load_dwordx4 v1, s[36:39], s76 offen offset:1024 sc1 lds\n"
    "s_add_u32 s76, s76, 16384\n"
    "v_mfma_f32_16x16x32_bf16 v[52:55], a[160:163], v[24:27], v[52:55]\n"
    "s_waitcnt lgkmcnt(6)\n"
    "s_add_u32 m0, s77, s59\n"
    "s_add_u32 s77, s77, 8192\n"
    "buffer_load_dwordx4 v1, s[36:39], s76 offen sc1 lds\n"
    "v_mfma_f32_16x16x32_bf16 v[64:67], a[164:167], v[24:27], v[64:67]\n"
    "ds_read_b128 v[24:27], v9 offset:34816\n"
    "v_mfma_f32_16x16x32_bf16 v[56:59], a[156:159], v[28:31], v[56:59]\n"
    "v_mfma_f32_16x16x32_bf16 v[60:63], a[160:163], v[28:31], v[60:63]\n"
    "s_waitcnt lgkmcnt(6)\n"
    "buffer_load_dwordx4 v1, s[36:39], s76 offen offset:1024 sc1 lds\n"
    "s_add_u32 s76, s76, 16384\n"
    "v_mfma_f32_16x16x32_bf16 v[68:71], a[164:167], v[28:31], v[68:71]\n"
    "ds_read_b128 v[28:31], v9 offset:35840\n"
    "v_mfma_f32_16x16x32_bf16 v[48:51], a[168:171], v[32:35], v[48:51]\n"
    "v_mfma_f32_16x16x32_bf16 v[52:55], a[172:175], v[32:35], v[52:55]\n"
    "s_waitcnt lgkmcnt(6)\n"
    ".LgWO_seg0_%=:\n"
    "s_cmp_lg_u32 s70, 0\n"
    "s_cbranch_scc0 .LgWO_chk1_%=\n"
    ".LgWO_plain1_%=:\n"
    "v_mfma_f32_16x16x32_bf16 v[64:67], a[176:179], v[32:35], v[64:67]\n"
    "ds_read_b128 v[32:35], v9 offset:36864\n"
    "v_mfma_f32_16x16x32_bf16 v[56:59], a[168:171], v[36:39], v[56:59]\n"
    "v_mfma_f32_16x16x32_bf16 v[60:63], a[172:175], v[36:39], v[60:63]\n"
    "s_waitcnt lgkmcnt(6)\n"
    "v_mfma_f32_16x16x32_bf16 v[68:71], a[176:179], v[36:39], v[68:71]\n"
    "ds_read_b128 v[36:39], v9 offset:37888\n"
    "v_mfma_f32_16x16x32_bf16 v[48:51], a[180:183], v[40:43], v[48:51]\n"
    "v_mfma_f32_16x16x32_bf16 v[52:55], a[184:187], v[40:43], v[52:55]\n"
    "s_waitcnt lgkmcnt(6)\n"
    "v_mfma_f32_16x16x32_bf16 v[64:67], a[188:191], v[40:43], v[64:67]\n"
    "ds_read_b128 v[40:43], v9 offset:38912\n"
    "v_mfma_f32_16x16x32_bf16 v[56:59], a[180:183], v[44:47], v[56:59]\n"
    "v_mfma_f32_16x16x32_bf16 v[60:63], a[184:187], v[44:47], v[60:63]\n"
    "s_waitcnt lgkmcnt(6)\n"
    "v_mfma_f32_16x16x32_bf16 v[68:71], a[188:191], v[44:47], v[68:71]\n"
    "ds_read_b128 v[44:47], v9 offset:39936\n"
    "v_mfma_f32_16x16x32_bf16 v[48:51], a[192:195], v[16:19], v[48:51]\n"
    "v_mfma_f32_16x16x32_bf16 v[52:55], a[196:199], v[16:19], v[52:55]\n"
    "s_waitcnt lgkmcnt(6)\n"
    "v_mfma_f32_16x16x32_bf16 v[64:67], a[200:203], v[16:19], v[64:67]\n"
    "ds_read_b128 v[16:19], v9 offset:40960\n"
    "v_mfma_f32_16x16x32_bf16 v[56:59], a[192:195], v[20:23], v[56:59]\n"
    "v_mfma_f32_16x16x32_bf16 v[60:63], a[196:199], v[20:23], v[60:63]\n"
    "s_waitcnt lgkmcnt(6)\n"
    "v_mfma_f32_16x16x32_bf16 v[68:71], a[200:203], v[20:23], v[68:71]\n"
    "ds_read_b128 v[20:23], v9 offset:41984\n"
    "v_mfma_f32_16x16x32_bf16 v[48:51], a[204:207], v[24:27], v[48:51]\n"
    "v_mfma_f32_16x16x32_bf16 v[52:55], a[208:211], v[24:27], v[52:55]\n"
    "s_waitcnt lgkmcnt(6)\n"
    "v_mfma_f32_16x16x32_bf16 v[64:67], a[212:215], v[24:27], v[64:67]\n"
    "ds_read_b128 v[24:27], v9 offset:43008\n"
    "v_mfma_f32_16x16x32_bf16 v[56:59], a[204:207], v[28:31], v[56:59]\n"
    "v_mfma_f32_16x16x32_bf16 v[60:63], a[208:211], v[28:31], v[60:63]\n"
    "s_waitcnt lgkmcnt(6)\n"
    "v_mfma_f32_16x16x32_bf16 v[68:71], a[212:215], v[28:31], v[68:71]\n"
    "ds_read_b128 v[28:31], v9 offset:44032\n"
    "v_mfma_f32_16x16x32_bf16 v[48:51], a[216:219], v[32:35], v[48:51]\n"
    "v_mfma_f32_16x16x32_bf16 v[52:55], a[220:223], v[32:35], v[52:55]\n"
    "s_waitcnt lgkmcnt(6)\n"
    "v_mfma_f32_16x16x32_bf16 v[64:67], a[224:227], v[32:35], v[64:67]\n"
    "ds_read_b128 v[32:35], v9 offset:45056\n"
    "v_mfma_f32_16x16x32_bf16 v[56:59], a[216:219], v[36:39], v[56:59]\n"
    "v_mfma_f32_16x16x32_bf16 v[60:63], a[220:223], v[36:39], v[60:63]\n"
    "s_waitcnt lgkmcnt(6)\n"
    "v_mfma_f32_16x16x32_bf16 v[68:71], a[224:227], v[36:39], v[68:71]\n"
    "ds_read_b128 v[36:39], v9 offset:46080\n"
    "v_mfma_f32_16x16x32_bf16 v[48:51], a[228:231], v[40:43], v[48:51]\n"
    "v_mfma_f32_16x16x32_bf16 v[52:55], a[232:235], v[40:43], v[52:55]\n"
    "s_waitcnt lgkmcnt(6)\n"
    "v_mfma_f32_16x16x32_bf16 v[64:67], a[236:239], v[40:43], v[64:67]\n"
    "ds_read_b128 v[40:43], v9 offset:47104\n"
    "v_mfma_f32_16x16x32_bf16 v[56:59], a[228:231], v[44:47], v[56:59]\n"
    "v_mfma_f32_16x16x32_bf16 v[60:63], a[232:235], v[44:47], v[60:63]\n"
    "s_waitcnt lgkmcnt(6)\n"
    "v_mfma_f32_16x16x32_bf16 v[68:71], a[236:239], v[44:47], v[68:71]\n"
    "ds_read_b128 v[44:47], v9 offset:48128\n"
    "v_mfma_f32_16x16x32_bf16 v[48:51], a[240:243], v[16:19], v[48:51]\n"
    "v_mfma_f32_16x16x32_bf16 v[52:55], a[244:247], v[16:19], v[52:55]\n"
    "s_waitcnt lgkmcnt(6)\n"
    "v_mfma_f32_16x16x32_bf16 v[64:67], a[248:251], v[16:19], v[64:67]\n"
    "ds_read_b128 v[16:19], v10 offset:0\n"
    "v_mfma_f32_16x16x32_bf16 v[56:59], a[240:243], v[20:23], v[56:59]\n"
    "v_mfma_f32_16x16x32_bf16 v[60:63], a[244:247], v[20:23], v[60:63]\n"
    "s_waitcnt lgkmcnt(6)\n"
    ".LgWO_seg1_%=:\n"
    "s_cmp_lg_u32 s70, 0\n"
    "s_cbranch_scc0 .LgWO_chk2_%=\n"
    ".LgWO_plain2_%=:\n"
    "v_mfma_f32_16x16x32_bf16 v[68:71], a[248:251], v[20:23], v[68:71]\n"
    "ds_read_b128 v[20:23], v10 offset:1024\n"
    "v_mfma_f32_16x16x32_bf16 v[48:51], a[252:255], v[24:27], v[48:51]\n"
    "v_mfma_f32_16x16x32_bf16 v[52:55], v[128:131], v[24:27], v[52:55]\n"
    "s_waitcnt lgkmcnt(6)\n"
    "v_mfma_f32_16x16x32_bf16 v[64:67], v[132:135], v[24:27], v[64:67]\n"
    "ds_read_b128 v[24:27], v10 offset:2048\n"
    "v_mfma_f32_16x16x32_bf16 v[56:59], a[252:255], v[28:31], v[56:59]\n"
    "v_mfma_f32_16x16x32_bf16 v[60:63], v[128:131], v[28:31], v[60:63]\n"
    "s_waitcnt lgkmcnt(6)\n"
    "v_mfma_f32_16x16x32_bf16 v[68:71], v[132:135], v[28:31], v[68:71]\n"
    "ds_read_b128 v[28:31], v10 offset:3072\n"
    "v_mfma_f32_16x16x32_bf16 v[48:51], v[136:139], v[32:35], v[48:51]\n"
    "v_mfma_f32_16x16x32_bf16 v[52:55], v[140:143], v[32:35], v[52:55]\n"
    "s_waitcnt lgkmcnt(6)\n"
    "v_mfma_f32_16x16x32_bf16 v[64:67], v[144:147], v[32:35], v[64:67]\n"
    "ds_read_b128 v[32:35], v10 offset:4096\n"
    "v_mfma_f32_16x16x32_bf16 v[56:59], v[136:139], v[36:39], v[56:59]\n"
    "v_mfma_f32_16x16x32_bf16 v[60:63], v[140:143], v[36:39], v[60:63]\n"
    "s_waitcnt lgkmcnt(6)\n"
    "v_mfma_f32_16x16x32_bf16 v[68:71], v[144:147], v[36:39], v[68:71]\n"
    "ds_read_b128 v[36:39], v10 offset:5120\n"
    "v_mfma_f32_16x16x32_bf16 v[48:51], v[148:151], v[40:43], v[48:51]\n"
    "v_mfma_f32_16x16x32_bf16 v[52:55], v[152:155], v[40:43], v[52:55]\n"
    "s_waitcnt lgkmcnt(6)\n"
    "v_mfma_f32_16x16x32_bf16 v[64:67], v[156:159], v[40:43], v[64:67]\n"
    "ds_read_b128 v[40:43], v10 offset:6144\n"
    "v_mfma_f32_16x16x32_bf16 v[56:59], v[148:151], v[44:47], v[56:59]\n"
    "v_mfma_f32_16x16x32_bf16 v[60:63], v[152:155], v[44:47], v[60:63]\n"
    "s_waitcnt lgkmcnt(6)\n"
    "v_mfma_f32_16x16x32_bf16 v[68:71], v[156:159], v[44:47], v[68:71]\n"
    "ds_read_b128 v[44:47], v10 offset:7168\n"
    "v_mfma_f32_16x16x32_bf16 v[48:51], v[160:163], v[16:19], v[48:51]\n"
    "v_mfma_f32_16x16x32_bf16 v[52:55], v[164:167], v[16:19], v[52:55]\n"
    "s_waitcnt lgkmcnt(6)\n"
    "v_mfma_f32_16x16x32_bf16 v[64:67], v[168:171], v[16:19], v[64:67]\n"
    "ds_read_b128 v[16:19], v10 offset:8192\n"
    "v_mfma_f32_16x16x32_bf16 v[56:59], v[160:163], v[20:23], v[56:59]\n"
    "v_mfma_f32_16x16x32_bf16 v[60:63], v[164:167], v[20:23], v[60:63]\n"
    "s_waitcnt lgkmcnt(6)\n"
    "v_mfma_f32_16x16x32_bf16 v[68:71], v[168:171], v[20:23], v[68:71]\n"
    "ds_read_b128 v[20:23], v10 offset:9216\n"
    "v_mfma_f32_16x16x32_bf16 v[48:51], v[172:175], v[24:27], v[48:51]\n"
    "v_mfma_f32_16x16x32_bf16 v[52:55], v[176:179], v[24:27], v[52:55]\n"
    "s_waitcnt lgkmcnt(6)\n"
    "v_mfma_f32_16x16x32_bf16 v[64:67], v[180:183], v[24:27], v[64:67]\n"
    "ds_read_b128 v[24:27], v10 offset:10240\n"
    "v_mfma_f32_16x16x32_bf16 v[56:59], v[172:175], v[28:31], v[56:59]\n"
    "v_mfma_f32_16x16x32_bf16 v[60:63], v[176:179], v[28:31], v[60:63]\n"
    "s_waitcnt lgkmcnt(6)\n"
    "v_mfma_f32_16x16x32_bf16 v[68:71], v[180:183], v[28:31], v[68:71]\n"
    "ds_read_b128 v[28:31], v10 offset:11264\n"
    "v_mfma_f32_16x16x32_bf16 v[48:51], v[184:187], v[32:35], v[48:51]\n"
    "v_mfma_f32_16x16x32_bf16 v[52:55], v[188:191], v[32:35], v[52:55]\n"
    "s_waitcnt lgkmcnt(6)\n"
    "v_mfma_f32_16x16x32_bf16 v[64:67], v[192:195], v[32:35], v[64:67]\n"
    "ds_read_b128 v[32:35], v10 offset:12288\n"
    "v_mfma_f32_16x16x32_bf16 v[56:59], v[184:187], v[36:39], v[56:59]\n"
    "v_mfma_f32_16x16x32_bf16 v[60:63], v[188:191], v[36:39], v[60:63]\n"
    "s_waitcnt lgkmcnt(6)\n"
    "v_mfma_f32_16x16x32_bf16 v[68:71], v[192:195], v[36:39], v[68:71]\n"
    "ds_read_b128 v[36:39], v10 offset:13312\n"
    "v_mfma_f32_16x16x32_bf16 v[48:51], v[196:199], v[40:43], v[48:51]\n"
    ".LgWO_seg2_%=:\n"
    "s_cmp_lg_u32 s70, 1\n"
    "s_cbranch_scc1 .LgWO_slow_%=\n"
    "v_mfma_f32_16x16x32_bf16 v[52:55], v[200:203], v[40:43], v[52:55]\n"
    "s_waitcnt lgkmcnt(6)\n"
    "s_mov_b32 s68, s71\n"
    "v_mfma_f32_16x16x32_bf16 v[64:67], v[204:207], v[40:43], v[64:67]\n"
    "ds_read_b128 v[40:43], v10 offset:14336\n"
    "v_mfma_f32_16x16x32_bf16 v[56:59], v[196:199], v[44:47], v[56:59]\n"
    "s_add_u32 s71, s68, 1\n"
    "v_mfma_f32_16x16x32_bf16 v[60:63], v[200:203], v[44:47], v[60:63]\n"
    "s_waitcnt lgkmcnt(6)\n"
    "s_cmp_eq_u32 s71, 64\n"
    "v_mfma_f32_16x16x32_bf16 v[68:71], v[204:207], v[44:47], v[68:71]\n"
    "ds_read_b128 v[44:47], v10 offset:15360\n"
    "v_mfma_f32_16x16x32_bf16 v[64:67], v[216:219], v[16:19], v[64:67]\n"
    "s_waitcnt lgkmcnt(6)\n"
    "ds_read_b128 v[16:19], v10 offset:8192\n"
    "v_mfma_f32_16x16x32_bf16 v[68:71], v[216:219], v[20:23], v[68:71]\n"
    "s_waitcnt lgkmcnt(6)\n"
    "ds_read_b128 v[20:23], v10 offset:10240\n"
    "v_mfma_f32_16x16x32_bf16 v[64:67], v[228:231], v[24:27], v[64:67]\n"
    "s_waitcnt lgkmcnt(6)\n"
    "ds_read_b128 v[24:27], v10 offset:12288\n"
    "v_mfma_f32_16x16x32_bf16 v[68:71], v[228:231], v[28:31], v[68:71]\n"
    "s_waitcnt lgkmcnt(6)\n"
    "ds_read_b128 v[28:31], v10 offset:14336\n"
    "v_mfma_f32_16x16x32_bf16 v[64:67], v[240:243], v[32:35], v[64:67]\n"
    "s_waitcnt lgkmcnt(6)\n"
    "ds_read_b128 v[32:35], v10 offset:9216\n"
    "v_mfma_f32_16x16x32_bf16 v[68:71], v[240:243], v[36:39], v[68:71]\n"
    "s_waitcnt lgkmcnt(6)\n"
    "ds_read_b128 v[36:39], v10 offset:11264\n"
    "v_mfma_f32_16x16x32_bf16 v[64:67], v[252:255], v[40:43], v[64:67]\n"
    "s_waitcnt lgkmcnt(6)\n"
    "ds_read_b128 v[40:43], v10 offset:13312\n"
    "s_cselect_b32 s70, 2, 0\n"
    "v_mfma_f32_16x16x32_bf16 v[68:71], v[252:255], v[44:47], v[68:71]\n"
    "s_waitcnt lgkmcnt(6)\n"
    "ds_read_b128 v[44:47], v10 offset:15360\n"
    "v_add_u32 v13, v11, v3\n"
    "v_mfma_f32_16x16x32_bf16 v[48:51], v[208:211], v[16:19], v[48:51]\n"
    "v_cmp_gt_i32 vcc, s71, v13\n"
    "v_mfma_f32_16x16x32_bf16 v[52:55], v[212:215], v[16:19], v[52:55]\n"
    "s_waitcnt lgkmcnt(6)\n"
    "v_cvt_pk_bf16_f32 v64, v64, v65\n"
    "v_cvt_pk_bf16_f32 v65, v66, v67\n"
    "v_mfma_f32_16x16x32_bf16 v[48:51], v[220:223], v[20:23], v[48:51]\n"
    "s_waitcnt lgkmcnt(5)\n"
    "v_cvt_pk_bf16_f32 v66, v68, v69\n"
    "v_cvt_pk_bf16_f32 v67, v70, v71\n"
    "v_xor_b32 v9, s78, v9\n"
    "v_mfma_f32_16x16x32_bf16 v[52:55], v[224:227], v[20:23], v[52:55]\n"
    "s_waitcnt vmcnt(0) lgkmcnt(0)\n"
    "s_barrier\n"
    "ds_read_b128 v[16:19], v9 offset:0\n"
    "s_nop 0\n"
    "v_mfma_f32_16x16x32_bf16 v[48:51], v[232:235], v[24:27], v[48:51]\n"
    "ds_read_b128 v[20:23], v9 offset:1024\n"
    "s_or_b64 exec, vcc, 1\n"
    "buffer_atomic_smax v11, v2, s[36:39], 0 offen sc0 sc1\n"
    "s_mov_b64 exec, -1\n"
    "v_mfma_f32_16x16x32_bf16 v[52:55], v[236:239], v[24:27], v[52:55]\n"
    "ds_read_b128 v[24:27], v9 offset:2048\n"
    "v_mfma_f32_16x16x32_bf16 v[48:51], v[244:247], v[28:31], v[48:51]\n"
    "v_permlane16_swap_b32 v64, v66\n"
    "v_mfma_f32_16x16x32_bf16 v[52:55], v[248:251], v[28:31], v[52:55]\n"
    "ds_read_b128 v[28:31], v9 offset:3072\n"
    "v_mfma_f32_16x16x32_bf16 v[56:59], v[208:211], v[32:35], v[56:59]\n"
    "v_permlane16_swap_b32 v65, v67\n"
    "v_mfma_f32_16x16x32_bf16 v[60:63], v[212:215], v[32:35], v[60:63]\n"
    "ds_read_b128 v[32:35], v9 offset:4096\n"
    "v_cvt_pk_bf16_f32 v48, v48, v49\n"
    "v_mfma_f32_16x16x32_bf16 v[56:59], v[220:223], v[36:39], v[56:59]\n"
    "buffer_store_dwordx4 v[64:67], v4, s[36:39], s73 offen sc1\n"
    "v_cvt_pk_bf16_f32 v49, v50, v51\n"
    "v_cvt_pk_bf16_f32 v50, v52, v53\n"
    "v_mfma_f32_16x16x32_bf16 v[60:63], v[224:227], v[36:39], v[60:63]\n"
    "ds_read_b128 v[36:39], v9 offset:5120\n"
    "v_cvt_pk_bf16_f32 v51, v54, v55\n"
    "v_mfma_f32_16x16x32_bf16 v[56:59], v[232:235], v[40:43], v[56:59]\n"
    "buffer_store_dwordx4 v[48:51], v15, s[36:39], s73 offen sc1\n"
    "v_mfma_f32_16x16x32_bf16 v[60:63], v[236:239], v[40:43], v[60:63]\n"
    "ds_read_b128 v[40:43], v9 offset:6144\n"
    "v_mfma_f32_16x16x32_bf16 v[56:59], v[244:247], v[44:47], v[56:59]\n"
    "s_waitcnt lgkmcnt(6)\n"
    "v_mfma_f32_16x16x32_bf16 v[60:63], v[248:251], v[44:47], v[60:63]\n"
    "ds_read_b128 v[44:47], v9 offset:7168\n"
    "s_branch .LgWO_head_%=\n"
    ".LgWO_LOOPE_%=:\n"
    ".LgWO_done_%=:\n"
    "s_waitcnt vmcnt(0)\n"
    "s_mov_b32 s74, 63\n"
    "v_mov_b32 v12, s74\n"
    "s_mov_b64 exec, s[62:63]\n"
    "buffer_atomic_smax v12, v14, s[36:39], 0 offen sc1\n"
    "s_mov_b64 exec, -1\n"
    "s_waitcnt vmcnt(0)\n"
    "s_branch .LgWO_FIN_%=\n"
    ".LgWO_chk0_%=:\n"
    "s_or_b64 exec, vcc, 1\n"
    "buffer_atomic_smax v11, v2, s[36:39], 0 offen sc0 sc1\n"
    "s_mov_b64 exec, -1\n"
    "v_mfma_f32_16x16x32_bf16 v[48:51], a[96:99], v[16:19], v[48:51]\n"
    "s_and_b32 s73, s68, s57\n"
    "v_mfma_f32_16x16x32_bf16 v[52:55], a[100:103], v[16:19], v[52:55]\n"
    "s_waitcnt lgkmcnt(6)\n"
    "s_mul_i32 s73, s73, s56\n"
    "v_mfma_f32_16x16x32_bf16 v[64:67], a[104:107], v[16:19], v[64:67]\n"
    "ds_read_b128 v[16:19], v9 offset:24576\n"
    "v_mfma_f32_16x16x32_bf16 v[56:59], a[96:99], v[20:23], v[56:59]\n"
    "s_add_u32 s73, s73, s55\n"
    "v_mfma_f32_16x16x32_bf16 v[60:63], a[100:103], v[20:23], v[60:63]\n"
    "s_waitcnt lgkmcnt(6)\n"
    "v_mfma_f32_16x16x32_bf16 v[68:71], a[104:107], v[20:23], v[68:71]\n"
    "ds_read_b128 v[20:23], v9 offset:25600\n"
    "v_mfma_f32_16x16x32_bf16 v[48:51], a[108:111], v[24:27], v[48:51]\n"
    "v_mfma_f32_16x16x32_bf16 v[52:55], a[112:115], v[24:27], v[52:55]\n"
    "s_waitcnt lgkmcnt(6)\n"
    "v_mfma_f32_16x16x32_bf16 v[64:67], a[116:119], v[24:27], v[64:67]\n"
    "ds_read_b128 v[24:27], v9 offset:26624\n"
    "v_mfma_f32_16x16x32_bf16 v[56:59], a[108:111], v[28:31], v[56:59]\n"
    "v_mfma_f32_16x16x32_bf16 v[60:63], a[112:115], v[28:31], v[60:63]\n"
    "s_waitcnt lgkmcnt(6)\n"
    "v_mfma_f32_16x16x32_bf16 v[68:71], a[116:119], v[28:31], v[68:71]\n"
    "ds_read_b128 v[28:31], v9 offset:27648\n"
    "v_mfma_f32_16x16x32_bf16 v[48:51], a[120:123], v[32:35], v[48:51]\n"
    "v_mfma_f32_16x16x32_bf16 v[52:55], a[124:127], v[32:35], v[52:55]\n"
    "s_waitcnt lgkmcnt(6)\n"
    "v_mfma_f32_16x16x32_bf16 v[64:67], a[128:131], v[32:35], v[64:67]\n"
    "ds_read_b128 v[32:35], v9 offset:28672\n"
    "v_mfma_f32_16x16x32_bf16 v[56:59], a[120:123], v[36:39], v[56:59]\n"
    "v_mfma_f32_16x16x32_bf16 v[60:63], a[124:127], v[36:39], v[60:63]\n"
    "s_waitcnt lgkmcnt(6)\n"
    "v_mfma_f32_16x16x32_bf16 v[68:71], a[128:131], v[36:39], v[68:71]\n"
    "ds_read_b128 v[36:39], v9 offset:29696\n"
    "v_mfma_f32_16x16x32_bf16 v[48:51], a[132:135], v[40:43], v[48:51]\n"
    "v_mfma_f32_16x16x32_bf16 v[52:55], a[136:139], v[40:43], v[52:55]\n"
    "s_waitcnt lgkmcnt(6)\n"
    "v_mfma_f32_16x16x32_bf16 v[64:67], a[140:143], v[40:43], v[64:67]\n"
    "ds_read_b128 v[40:43], v9 offset:30720\n"
    "v_mfma_f32_16x16x32_bf16 v[56:59], a[132:135], v[44:47], v[56:59]\n"
    "v_mfma_f32_16x16x32_bf16 v[60:63], a[136:139], v[44:47], v[60:63]\n"
    "s_waitcnt lgkmcnt(6)\n"
    "v_mfma_f32_16x16x32_bf16 v[68:71], a[140:143], v[44:47], v[68:71]\n"
    "ds_read_b128 v[44:47], v9 offset:31744\n"
    "v_mfma_f32_16x16x32_bf16 v[48:51], a[144:147], v[16:19], v[48:51]\n"
    "v_mfma_f32_16x16x32_bf16 v[52:55], a[148:151], v[16:19], v[52:55]\n"
    "s_waitcnt lgkmcnt(6)\n"
    "v_mfma_f32_16x16x32_bf16 v[64:67], a[152:155], v[16:19], v[64:67]\n"
    "ds_read_b128 v[16:19], v9 offset:32768\n"
    "v_mfma_f32_16x16x32_bf16 v[56:59], a[144:147], v[20:23], v[56:59]\n"
    "v_mfma_f32_16x16x32_bf16 v[60:63], a[148:151], v[20:23], v[60:63]\n"
    "s_waitcnt lgkmcnt(6)\n"
    "v_mfma_f32_16x16x32_bf16 v[68:71], a[152:155], v[20:23], v[68:71]\n"
    "ds_read_b128 v[20:23], v9 offset:33792\n"
    "v_mfma_f32_16x16x32_bf16 v[48:51], a[156:159], v[24:27], v[48:51]\n"
    "v_mfma_f32_16x16x32_bf16 v[52:55], a[160:163], v[24:27], v[52:55]\n"
    "s_waitcnt lgkmcnt(6)\n"
    "v_mfma_f32_16x16x32_bf16 v[64:67], a[164:167], v[24:27], v[64:67]\n"
    "ds_read_b128 v[24:27], v9 offset:34816\n"
    "v_mfma_f32_16x16x32_bf16 v[56:59], a[156:159], v[28:31], v[56:59]\n"
    "v_mfma_f32_16x16x32_bf16 v[60:63], a[160:163], v[28:31], v[60:63]\n"
    "s_waitcnt lgkmcnt(6)\n"
    "v_mfma_f32_16x16x32_bf16 v[68:71], a[164:167], v[28:31], v[68:71]\n"
    "ds_read_b128 v[28:31], v9 offset:35840\n"
    "v_mfma_f32_16x16x32_bf16 v[48:51], a[168:171], v[32:35], v[48:51]\n"
    "v_mfma_f32_16x16x32_bf16 v[52:55], a[172:175], v[32:35], v[52:55]\n"
    "s_waitcnt lgkmcnt(6)\n"
    "s_branch .LgWO_seg0_%=\n"
    ".LgWO_chk1_%=:\n"
    "s_waitcnt vmcnt(0)\n"
    "v_add_u32 v13, v11, v3\n"
    "v_cmp_gt_i32 vcc, s71, v13\n"
    "s_cbranch_vccz .LgWO_dma1_%=\n"
    "s_or_b64 exec, vcc, 1\n"
    "buffer_atomic_smax v11, v2, s[36:39], 0 offen sc0 sc1\n"
    "s_mov_b64 exec, -1\n"
    "s_branch .LgWO_plain1_%=\n"
    ".LgWO_dma1_%=:\n"
    "s_mov_b32 s70, 1\n"
    "v_mfma_f32_16x16x32_bf16 v[64:67], a[176:179], v[32:35], v[64:67]\n"
    "ds_read_b128 v[32:35], v9 offset:36864\n"
    "v_mfma_f32_16x16x32_bf16 v[56:59], a[168:171], v[36:39], v[56:59]\n"
    "s_add_u32 m0, s77, s59\n"
    "s_add_u32 s77, s77, 8192\n"
    "buffer_load_dwordx4 v1, s[36:39], s76 offen sc1 lds\n"
    "v_mfma_f32_16x16x32_bf16 v[60:63], a[172:175], v[36:39], v[60:63]\n"
    "s_waitcnt lgkmcnt(6)\n"
    "buffer_load_dwordx4 v1, s[36:39], s76 offen offset:1024 sc1 lds\n"
    "s_add_u32 s76, s76, 16384\n"
    "v_mfma_f32_16x16x32_bf16 v[68:71], a[176:179], v[36:39], v[68:71]\n"
    "ds_read_b128 v[36:39], v9 offset:37888\n"
    "v_mfma_f32_16x16x32_bf16 v[48:51], a[180:183], v[40:43], v[48:51]\n"
    "v_mfma_f32_16x16x32_bf16 v[52:55], a[184:187], v[40:43], v[52:55]\n"
    "s_waitcnt lgkmcnt(6)\n"
    "s_add_u32 m0, s77, s59\n"
    "s_add_u32 s77, s77, 8192\n"
    "buffer_load_dwordx4 v1, s[36:39], s76 offen sc1 lds\n"
    "v_mfma_f32_16x16x32_bf16 v[64:67], a[188:191], v[40:43], v[64:67]\n"
    "ds_read_b128 v[40:43], v9 offset:38912\n"
    "v_mfma_f32_16x16x32_bf16 v[56:59], a[180:183], v[44:47], v[56:59]\n"
    "buffer_load_dwordx4 v1, s[36:39], s76 offen offset:1024 sc1 lds\n"
    "s_add_u32 s76, s76, 16384\n"
    "v_mfma_f32_16x16x32_bf16 v[60:63], a[184:187], v[44:47], v[60:63]\n"
    "s_waitcnt lgkmcnt(6)\n"
    "v_mfma_f32_16x16x32_bf16 v[68:71], a[188:191], v[44:47], v[68:71]\n"
    "ds_read_b128 v[44:47], v9 offset:39936\n"
    "v_mfma_f32_16x16x32_bf16 v[48:51], a[192:195], v[16:19], v[48:51]\n"
    "s_add_u32 m0, s77, s59\n"
    "s_add_u32 s77, s77, 8192\n"
    "buffer_load_dwordx4 v1, s[36:39], s76 offen sc1 lds\n"
    "v_mfma_f32_16x16x32_bf16 v[52:55], a[196:199], v[16:19], v[52:55]\n"
    "s_waitcnt lgkmcnt(6)\n"
    "buffer_load_dwordx4 v1, s[36:39], s76 offen offset:1024 sc1 lds\n"
    "s_add_u32 s76, s76, 16384\n"
    "v_mfma_f32_16x16x32_bf16 v[64:67], a[200:203], v[16:19], v[64:67]\n"
    "ds_read_b128 v[16:19], v9 offset:40960\n"
    "v_mfma_f32_16x16x32_bf16 v[56:59], a[192:195], v[20:23], v[56:59]\n"
    "v_mfma_f32_16x16x32_bf16 v[60:63], a[196:199], v[20:23], v[60:63]\n"
    "s_waitcnt lgkmcnt(6)\n"
    "s_add_u32 m0, s77, s59\n"
    "s_add_u32 s77, s77, 8192\n"
    "buffer_load_dwordx4 v1, s[36:39], s76 offen sc1 lds\n"
    "v_mfma_f32_16x16x32_bf16 v[68:71], a[200:203], v[20:23], v[68:71]\n"
    "ds_read_b128 v[20:23], v9 offset:41984\n"
    "v_mfma_f32_16x16x32_bf16 v[48:51], a[204:207], v[24:27], v[48:51]\n"
    "buffer_load_dwordx4 v1, s[36:39], s76 offen offset:1024 sc1 lds\n"
    "s_add_u32 s76, s76, 16384\n"
    "v_mfma_f32_16x16x32_bf16 v[52:55], a[208:211], v[24:27], v[52:55]\n"
    "s_waitcnt lgkmcnt(6)\n"
    "v_mfma_f32_16x16x32_bf16 v[64:67], a[212:215], v[24:27], v[64:67]\n"
    "ds_read_b128 v[24:27], v9 offset:43008\n"
    "v_mfma_f32_16x16x32_bf16 v[56:59], a[204:207], v[28:31], v[56:59]\n"
    "s_add_u32 m0, s77, s59\n"
    "s_add_u32 s77, s77, 8192\n"
    "buffer_load_dwordx4 v1, s[36:39], s76 offen sc1 lds\n"
    "v_mfma_f32_16x16x32_bf16 v[60:63], a[208:211], v[28:31], v[60:63]\n"
    "s_waitcnt lgkmcnt(6)\n"
    "v_mfma_f32_16x16x32_bf16 v[68:71], a[212:215], v[28:31], v[68:71]\n"
    "ds_read_b128 v[28:31], v9 offset:44032\n"
    "v_mfma_f32_16x16x32_bf16 v[48:51], a[216:219], v[32:35], v[48:51]\n"
    "buffer_load_dwordx4 v1, s[36:39], s76 offen offset:1024 sc1 lds\n"
    "s_add_u32 s76, s76, 16384\n"
    "v_mfma_f32_16x16x32_bf16 v[52:55], a[220:223], v[32:35], v[52:55]\n"
    "s_waitcnt lgkmcnt(6)\n"
    "s_add_u32 m0, s77, s59\n"
    "s_add_u32 s77, s77, 8192\n"
    "buffer_load_dwordx4 v1, s[36:39], s76 offen sc1 lds\n"
    "v_mfma_f32_16x16x32_bf16 v[64:67], a[224:227], v[32:35], v[64:67]\n"
    "ds_read_b128 v[32:35], v9 offset:45056\n"
    "v_mfma_f32_16x16x32_bf16 v[56:59], a[216:219], v[36:39], v[56:59]\n"
    "v_mfma_f32_16x16x32_bf16 v[60:63], a[220:223], v[36:39], v[60:63]\n"
    "s_waitcnt lgkmcnt(6)\n"
    "buffer_load_dwordx4 v1, s[36:39], s76 offen offset:1024 sc1 lds\n"
    "s_add_u32 s76, s76, 16384\n"
    "v_mfma_f32_16x16x32_bf16 v[68:71], a[224:227], v[36:39], v[68:71]\n"
    "ds_read_b128 v[36:39], v9 offset:46080\n"
    "v_mfma_f32_16x16x32_bf16 v[48:51], a[228:231], v[40:43], v[48:51]\n"
    "s_add_u32 m0, s77, s59\n"
    "s_add_u32 s77, s77, 8192\n"
    "buffer_load_dwordx4 v1, s[36:39], s76 offen sc1 lds\n"
    "v_mfma_f32_16x16x32_bf16 v[52:55], a[232:235], v[40:43], v[52:55]\n"
    "s_waitcnt lgkmcnt(6)\n"
    "v_mfma_f32_16x16x32_bf16 v[64:67], a[236:239], v[40:43], v[64:67]\n"
    "ds_read_b128 v[40:43], v9 offset:47104\n"
    "v_mfma_f32_16x16x32_bf16 v[56:59], a[228:231], v[44:47], v[56:59]\n"
    "buffer_load_dwordx4 v1, s[36:39], s76 offen offset:1024 sc1 lds\n"
    "s_add_u32 s76, s76, 16384\n"
    "v_mfma_f32_16x16x32_bf16 v[60:63], a[232:235], v[44:47], v[60:63]\n"
    "s_waitcnt lgkmcnt(6)\n"
    "s_add_u32 m0, s77, s59\n"
    "s_add_u32 s77, s77, 8192\n"
    "buffer_load_dwordx4 v1, s[36:39], s76 offen sc1 lds\n"
    "v_mfma_f32_16x16x32_bf16 v[68:71], a[236:239], v[44:47], v[68:71]\n"
    "ds_read_b128 v[44:47], v9 offset:48128\n"
    "v_mfma_f32_16x16x32_bf16 v[48:51], a[240:243], v[16:19], v[48:51]\n"
    "v_mfma_f32_16x16x32_bf16 v[52:55], a[244:247], v[16:19], v[52:55]\n"
    "s_waitcnt lgkmcnt(6)\n"
    "buffer_load_dwordx4 v1, s[36:39], s76 offen offset:1024 sc1 lds\n"
    "s_add_u32 s76, s76, 16384\n"
    "v_mfma_f32_16x16x32_bf16 v[64:67], a[248:251], v[16:19], v[64:67]\n"
    "ds_read_b128 v[16:19], v10 offset:0\n"
    "v_mfma_f32_16x16x32_bf16 v[56:59], a[240:243], v[20:23], v[56:59]\n"
    "v_mfma_f32_16x16x32_bf16 v[60:63], a[244:247], v[20:23], v[60:63]\n"
    "s_waitcnt lgkmcnt(6)\n"
    "s_branch .LgWO_seg1_%=\n"
    ".LgWO_chk2_%=:\n"
    "s_waitcnt vmcnt(0)\n"
    "v_add_u32 v13, v11, v3\n"
    "v_cmp_gt_i32 vcc, s71, v13\n"
    "s_cbranch_vccz .LgWO_dma2_%=\n"
    "s_or_b64 exec, vcc, 1\n"
    "buffer_atomic_smax v11, v2, s[36:39], 0 offen sc0 sc1\n"
    "s_mov_b64 exec, -1\n"
    "s_branch .LgWO_plain2_%=\n"
    ".LgWO_dma2_%=:\n"
    "s_mov_b32 s70, 2\n"
    "v_mfma_f32_16x16x32_bf16 v[68:71], a[248:251], v[20:23], v[68:71]\n"
    "ds_read_b128 v[20:23], v10 offset:1024\n"
    "v_mfma_f32_16x16x32_bf16 v[48:51], a[252:255], v[24:27], v[48:51]\n"
    "s_add_u32 m0, s77, s59\n"
    "s_add_u32 s77, s77, 8192\n"
    "buffer_load_dwordx4 v1, s[36:39], s76 offen sc1 lds\n"
    "v_mfma_f32_16x16x32_bf16 v[52:55], v[128:131], v[24:27], v[52:55]\n"
    "s_waitcnt lgkmcnt(6)\n"
    "buffer_load_dwordx4 v1, s[36:39], s76 offen offset:1024 sc1 lds\n"
    "s_add_u32 s76, s76, 16384\n"
    "v_mfma_f32_16x16x32_bf16 v[64:67], v[132:135], v[24:27], v[64:67]\n"
    "ds_read_b128 v[24:27], v10 offset:2048\n"
    "v_mfma_f32_16x16x32_bf16 v[56:59], a[252:255], v[28:31], v[56:59]\n"
    "v_mfma_f32_16x16x32_bf16 v[60:63], v[128:131], v[28:31], v[60:63]\n"
    "s_waitcnt lgkmcnt(6)\n"
    "s_add_u32 m0, s77, s59\n"
    "s_add_u32 s77, s77, 8192\n"
    "buffer_load_dwordx4 v1, s[36:39], s76 offen sc1 lds\n"
    "v_mfma_f32_16x16x32_bf16 v[68:71], v[132:135], v[28:31], v[68:71]\n"
    "ds_read_b128 v[28:31], v10 offset:3072\n"
    "v_mfma_f32_16x16x32_bf16 v[48:51], v[136:139], v[32:35], v[48:51]\n"
    "buffer_load_dwordx4 v1, s[36:39], s76 offen offset:1024 sc1 lds\n"
    "s_add_u32 s76, s76, 16384\n"
    "v_mfma_f32_16x16x32_bf16 v[52:55], v[140:143], v[32:35], v[52:55]\n"
    "s_waitcnt lgkmcnt(6)\n"
    "v_mfma_f32_16x16x32_bf16 v[64:67], v[144:147], v[32:35], v[64:67]\n"
    "ds_read_b128 v[32:35], v10 offset:4096\n"
    "v_mfma_f32_16x16x32_bf16 v[56:59], v[136:139], v[36:39], v[56:59]\n"
    "s_add_u32 m0, s77, s59\n"
    "s_add_u32 s77, s77, 8192\n"
    "buffer_load_dwordx4 v1, s[36:39], s76 offen sc1 lds\n"
    "v_mfma_f32_16x16x32_bf16 v[60:63], v[140:143], v[36:39], v[60:63]\n"
    "s_waitcnt lgkmcnt(6)\n"
    "buffer_load_dwordx4 v1, s[36:39], s76 offen offset:1024 sc1 lds\n"
    "s_add_u32 s76, s76, 16384\n"
    "v_mfma_f32_16x16x32_bf16 v[68:71], v[144:147], v[36:39], v[68:71]\n"
    "ds_read_b128 v[36:39], v10 offset:5120\n"
    "v_mfma_f32_16x16x32_bf16 v[48:51], v[148:151], v[40:43], v[48:51]\n"
    "v_mfma_f32_16x16x32_bf16 v[52:55], v[152:155], v[40:43], v[52:55]\n"
    "s_waitcnt lgkmcnt(6)\n"
    "s_add_u32 m0, s77, s59\n"
    "s_add_u32 s77, s77, 8192\n"
    "buffer_load_dwordx4 v1, s[36:39], s76 offen sc1 lds\n"
    "v_mfma_f32_16x16x32_bf16 v[64:67], v[156:159], v[40:43], v[64:67]\n"
    "ds_read_b128 v[40:43], v10 offset:6144\n"
    "v_mfma_f32_16x16x32_bf16 v[56:59], v[148:151], v[44:47], v[56:59]\n"
    "buffer_load_dwordx4 v1, s[36:39], s76 offen offset:1024 sc1 lds\n"
    "s_add_u32 s76, s76, 16384\n"
    "v_mfma_f32_16x16x32_bf16 v[60:63], v[152:155], v[44:47], v[60:63]\n"
    "s_waitcnt lgkmcnt(6)\n"
    "v_mfma_f32_16x16x32_bf16 v[68:71], v[156:159], v[44:47], v[68:71]\n"
    "ds_read_b128 v[44:47], v10 offset:7168\n"
    "v_mfma_f32_16x16x32_bf16 v[48:51], v[160:163], v[16:19], v[48:51]\n"
    "s_add_u32 m0, s77, s59\n"
    "s_add_u32 s77, s77, 8192\n"
    "buffer_load_dwordx4 v1, s[36:39], s76 offen sc1 lds\n"
    "v_mfma_f32_16x16x32_bf16 v[52:55], v[164:167], v[16:19], v[52:55]\n"
    "s_waitcnt lgkmcnt(6)\n"
    "buffer_load_dwordx4 v1, s[36:39], s76 offen offset:1024 sc1 lds\n"
    "s_add_u32 s76, s76, 16384\n"
    "v_mfma_f32_16x16x32_bf16 v[64:67], v[168:171], v[16:19], v[64:67]\n"
    "ds_read_b128 v[16:19], v10 offset:8192\n"
    "v_mfma_f32_16x16x32_bf16 v[56:59], v[160:163], v[20:23], v[56:59]\n"
    "v_mfma_f32_16x16x32_bf16 v[60:63], v[164:167], v[20:23], v[60:63]\n"
    "s_waitcnt lgkmcnt(6)\n"
    "s_add_u32 m0, s77, s59\n"
    "s_add_u32 s77, s77, 8192\n"
    "buffer_load_dwordx4 v1, s[36:39], s76 offen sc1 lds\n"
    "v_mfma_f32_16x16x32_bf16 v[68:71], v[168:171], v[20:23], v[68:71]\n"
    "ds_read_b128 v[20:23], v10 offset:9216\n"
    "v_mfma_f32_16x16x32_bf16 v[48:51], v[172:175], v[24:27], v[48:51]\n"
    "buffer_load_dwordx4 v1, s[36:39], s76 offen offset:1024 sc1 lds\n"
    "s_add_u32 s76, s76, 16384\n"
    "v_mfma_f32_16x16x32_bf16 v[52:55], v[176:179], v[24:27], v[52:55]\n"
    "s_waitcnt lgkmcnt(6)\n"
    "v_mfma_f32_16x16x32_bf16 v[64:67], v[180:183], v[24:27], v[64:67]\n"
    "ds_read_b128 v[24:27], v10 offset:10240\n"
    "v_mfma_f32_16x16x32_bf16 v[56:59], v[172:175], v[28:31], v[56:59]\n"
    "s_add_u32 m0, s77, s59\n"
    "s_add_u32 s77, s77, 8192\n"
    "buffer_load_dwordx4 v1, s[36:39], s76 offen sc1 lds\n"
    "v_mfma_f32_16x16x32_bf16 v[60:63], v[176:179], v[28:31], v[60:63]\n"
    "s_waitcnt lgkmcnt(6)\n"
    "buffer_load_dwordx4 v1, s[36:39], s76 offen offset:1024 sc1 lds\n"
    "s_add_u32 s76, s76, 16384\n"
    "v_mfma_f32_16x16x32_bf16 v[68:71], v[180:183], v[28:31], v[68:71]\n"
    "ds_read_b128 v[28:31], v10 offset:11264\n"
    "v_mfma_f32_16x16x32_bf16 v[48:51], v[184:187], v[32:35], v[48:51]\n"
    "v_mfma_f32_16x16x32_bf16 v[52:55], v[188:191], v[32:35], v[52:55]\n"
    "s_waitcnt lgkmcnt(6)\n"
    "s_add_u32 m0, s77, s59\n"
    "s_add_u32 s77, s77, 8192\n"
    "buffer_load_dwordx4 v1, s[36:39], s76 offen sc1 lds\n"
    "v_mfma_f32_16x16x32_bf16 v[64:67], v[192:195], v[32:35], v[64:67]\n"
    "ds_read_b128 v[32:35], v10 offset:12288\n"
    "v_mfma_f32_16x16x32_bf16 v[56:59], v[184:187], v[36:39], v[56:59]\n"
    "buffer_load_dwordx4 v1, s[36:39], s76 offen offset:1024 sc1 lds\n"
    "s_add_u32 s76, s76, 16384\n"
    "v_mfma_f32_16x16x32_bf16 v[60:63], v[188:191], v[36:39], v[60:63]\n"
    "s_waitcnt lgkmcnt(6)\n"
    "v_mfma_f32_16x16x32_bf16 v[68:71], v[192:195], v[36:39], v[68:71]\n"
    "ds_read_b128 v[36:39], v10 offset:13312\n"
    "v_mfma_f32_16x16x32_bf16 v[48:51], v[196:199], v[40:43], v[48:51]\n"
    "s_branch .LgWO_seg2_%=\n"
    ".LgWO_slow_%=:\n"
    "s_cmp_lg_u32 s70, 0\n"
    "s_cbranch_scc1 .LgWO_nolast_%=\n"
    "s_waitcnt vmcnt(0)\n"
    "v_add_u32 v13, v11, v3\n"
    "v_cmp_gt_i32 vcc, s71, v13\n"
    "s_nop 0\n"
    "s_or_b64 exec, vcc, 1\n"
    "buffer_atomic_smax v11, v2, s[36:39], 0 offen sc0 sc1\n"
    "s_mov_b64 exec, -1\n"
    ".LgWO_nolast_%=:\n"
    "v_mfma_f32_16x16x32_bf16 v[52:55], v[200:203], v[40:43], v[52:55]\n"
    "s_waitcnt lgkmcnt(6)\n"
    "v_mfma_f32_16x16x32_bf16 v[64:67], v[204:207], v[40:43], v[64:67]\n"
    "ds_read_b128 v[40:43], v10 offset:14336\n"
    "v_mfma_f32_16x16x32_bf16 v[56:59], v[196:199], v[44:47], v[56:59]\n"
    "v_mfma_f32_16x16x32_bf16 v[60:63], v[200:203], v[44:47], v[60:63]\n"
    "s_waitcnt lgkmcnt(6)\n"
    "v_mfma_f32_16x16x32_bf16 v[68:71], v[204:207], v[44:47], v[68:71]\n"
    "ds_read_b128 v[44:47], v10 offset:15360\n"
    "v_mfma_f32_16x16x32_bf16 v[64:67], v[216:219], v[16:19], v[64:67]\n"
    "s_waitcnt lgkmcnt(6)\n"
    "ds_read_b128 v[16:19], v10 offset:8192\n"
    "v_mfma_f32_16x16x32_bf16 v[68:71], v[216:219], v[20:23], v[68:71]\n"
    "s_waitcnt lgkmcnt(6)\n"
    "ds_read_b128 v[20:23], v10 offset:10240\n"
    "v_mfma_f32_16x16x32_bf16 v[64:67], v[228:231], v[24:27], v[64:67]\n"
    "s_waitcnt lgkmcnt(6)\n"
    "ds_read_b128 v[24:27], v10 offset:12288\n"
    "v_mfma_f32_16x16x32_bf16 v[68:71], v[228:231], v[28:31], v[68:71]\n"
    "s_waitcnt lgkmcnt(6)\n"
    "ds_read_b128 v[28:31], v10 offset:14336\n"
    "v_mfma_f32_16x16x32_bf16 v[64:67], v[240:243], v[32:35], v[64:67]\n"
    "s_waitcnt lgkmcnt(6)\n"
    "ds_read_b128 v[32:35], v10 offset:9216\n"
    "v_mfma_f32_16x16x32_bf16 v[68:71], v[240:243], v[36:39], v[68:71]\n"
    "s_waitcnt lgkmcnt(6)\n"
    "ds_read_b128 v[36:39], v10 offset:11264\n"
    "v_mfma_f32_16x16x32_bf16 v[64:67], v[252:255], v[40:43], v[64:67]\n"
    "s_waitcnt lgkmcnt(6)\n"
    "ds_read_b128 v[40:43], v10 offset:13312\n"
    "v_mfma_f32_16x16x32_bf16 v[68:71], v[252:255], v[44:47], v[68:71]\n"
    "s_waitcnt lgkmcnt(6)\n"
    "ds_read_b128 v[44:47], v10 offset:15360\n"
    "v_mfma_f32_16x16x32_bf16 v[48:51], v[208:211], v[16:19], v[48:51]\n"
    "v_mfma_f32_16x16x32_bf16 v[52:55], v[212:215], v[16:19], v[52:55]\n"
    "s_waitcnt lgkmcnt(6)\n"
    "v_cvt_pk_bf16_f32 v64, v64, v65\n"
    "v_cvt_pk_bf16_f32 v65, v66, v67\n"
    "v_mfma_f32_16x16x32_bf16 v[48:51], v[220:223], v[20:23], v[48:51]\n"
    "s_waitcnt lgkmcnt(5)\n"
    "v_cvt_pk_bf16_f32 v66, v68, v69\n"
    "v_cvt_pk_bf16_f32 v67, v70, v71\n"
    "v_mfma_f32_16x16x32_bf16 v[52:55], v[224:227], v[20:23], v[52:55]\n"
    "s_nop 0\n"
    "v_mfma_f32_16x16x32_bf16 v[48:51], v[232:235], v[24:27], v[48:51]\n"
    "v_mfma_f32_16x16x32_bf16 v[52:55], v[236:239], v[24:27], v[52:55]\n"
    "s_waitcnt lgkmcnt(4)\n"
    "v_mfma_f32_16x16x32_bf16 v[48:51], v[244:247], v[28:31], v[48:51]\n"
    "s_waitcnt lgkmcnt(3)\n"
    "v_permlane16_swap_b32 v64, v66\n"
    "v_mfma_f32_16x16x32_bf16 v[52:55], v[248:251], v[28:31], v[52:55]\n"
    "v_mfma_f32_16x16x32_bf16 v[56:59], v[208:211], v[32:35], v[56:59]\n"
    "s_waitcnt lgkmcnt(2)\n"
    "v_permlane16_swap_b32 v65, v67\n"
    "v_mfma_f32_16x16x32_bf16 v[60:63], v[212:215], v[32:35], v[60:63]\n"
    "v_cvt_pk_bf16_f32 v48, v48, v49\n"
    "v_mfma_f32_16x16x32_bf16 v[56:59], v[220:223], v[36:39], v[56:59]\n"
    "s_waitcnt lgkmcnt(1)\n"
    "buffer_store_dwordx4 v[64:67], v4, s[36:39], s73 offen sc1\n"
    "v_cvt_pk_bf16_f32 v49, v50, v51\n"
    "v_cvt_pk_bf16_f32 v50, v52, v53\n"
    "v_mfma_f32_16x16x32_bf16 v[60:63], v[224:227], v[36:39], v[60:63]\n"
    "v_cvt_pk_bf16_f32 v51, v54, v55\n"
    "v_mfma_f32_16x16x32_bf16 v[56:59], v[232:235], v[40:43], v[56:59]\n"
    "s_waitcnt lgkmcnt(0)\n"
    "buffer_store_dwordx4 v[48:51], v15, s[36:39], s73 offen sc1\n"
    "v_mfma_f32_16x16x32_bf16 v[60:63], v[236:239], v[40:43], v[60:63]\n"
    "v_mfma_f32_16x16x32_bf16 v[56:59], v[244:247], v[44:47], v[56:59]\n"
    "v_mfma_f32_16x16x32_bf16 v[60:63], v[248:251], v[44:47], v[60:63]\n"
    "s_nop 3\n"
    "v_cvt_pk_bf16_f32 v56, v56, v57\n"
    "v_cvt_pk_bf16_f32 v57, v58, v59\n"
    "s_nop 1\n"
    "v_cvt_pk_bf16_f32 v58, v60, v61\n"
    "v_cvt_pk_bf16_f32 v59, v62, v63\n"
    "buffer_store_dwordx4 v[56:59], v15, s[36:39], s73 offen offset:1024 sc1\n"
    "s_mov_b32 s68, s71\n"
    "s_cmp_eq_u32 s70, 0\n"
    "s_cbranch_scc1 .LgWO_idle_%=\n"
    "s_cmp_eq_u32 s68, 64\n"
    "s_cbranch_scc1 .LgWO_done_%=\n"
    "s_waitcnt vmcnt(3)\n"
    ".LgWO_bar_%=:\n"
    "v_xor_b32 v9, s78, v9\n"
    "s_add_u32 s71, s68, 1\n"
    "s_cmp_eq_u32 s71, 64\n"
    "s_cselect_b32 s70, 2, 0\n"
    "s_barrier\n"
    "ds_read_b128 v[16:19], v9 offset:0\n"
    "ds_read_b128 v[20:23], v9 offset:1024\n"
    "ds_read_b128 v[24:27], v9 offset:2048\n"
    "ds_read_b128 v[28:31], v9 offset:3072\n"
    "ds_read_b128 v[32:35], v9 offset:4096\n"
    "ds_read_b128 v[36:39], v9 offset:5120\n"
    "ds_read_b128 v[40:43], v9 offset:6144\n"
    "ds_read_b128 v[44:47], v9 offset:7168\n"
    "v_add_u32 v13, v11, v3\n"
    "v_cmp_gt_i32 vcc, s71, v13\n"
    "s_nop 0\n"
    "s_or_b64 exec, vcc, 1\n"
    "buffer_atomic_smax v11, v2, s[36:39], 0 offen sc0 sc1\n"
    "s_mov_b64 exec, -1\n"
    "s_waitcnt lgkmcnt(7)\n"
    "v_mfma_f32_16x16x32_bf16 v[64:67], a[8:11], v[16:19], 0\n"
    "s_waitcnt lgkmcnt(6)\n"
    "ds_read_b128 v[16:19], v9 offset:0\n"
    "v_mfma_f32_16x16x32_bf16 v[68:71], a[8:11], v[20:23], 0\n"
    "s_waitcnt lgkmcnt(6)\n"
    "ds_read_b128 v[20:23], v9 offset:2048\n"
    "v_mfma_f32_16x16x32_bf16 v[64:67], a[20:23], v[24:27], v[64:67]\n"
    "s_waitcnt lgkmcnt(6)\n"
    "ds_read_b128 v[24:27], v9 offset:4096\n"
    "v_mfma_f32_16x16x32_bf16 v[68:71], a[20:23], v[28:31], v[68:71]\n"
    "s_waitcnt lgkmcnt(6)\n"
    "ds_read_b128 v[28:31], v9 offset:6144\n"
    "v_mfma_f32_16x16x32_bf16 v[64:67], a[32:35], v[32:35], v[64:67]\n"
    "s_waitcnt lgkmcnt(6)\n"
    "ds_read_b128 v[32:35], v9 offset:1024\n"
    "v_mfma_f32_16x16x32_bf16 v[68:71], a[32:35], v[36:39], v[68:71]\n"
    "s_waitcnt lgkmcnt(6)\n"
    "ds_read_b128 v[36:39], v9 offset:3072\n"
    "v_mfma_f32_16x16x32_bf16 v[64:67], a[44:47], v[40:43], v[64:67]\n"
    "s_waitcnt lgkmcnt(6)\n"
    "ds_read_b128 v[40:43], v9 offset:5120\n"
    "v_mfma_f32_16x16x32_bf16 v[68:71], a[44:47], v[44:47], v[68:71]\n"
    "s_waitcnt lgkmcnt(6)\n"
    "ds_read_b128 v[44:47], v9 offset:7168\n"
    "v_mfma_f32_16x16x32_bf16 v[48:51], a[0:3], v[16:19], 0\n"
    "s_waitcnt lgkmcnt(6)\n"
    "s_sub_u32 s74, s68, 1\n"
    "v_mfma_f32_16x16x32_bf16 v[52:55], a[4:7], v[16:19], 0\n"
    "ds_read_b128 v[16:19], v9 offset:8192\n"
    "v_mfma_f32_16x16x32_bf16 v[48:51], a[12:15], v[20:23], v[48:51]\n"
    "s_waitcnt lgkmcnt(6)\n"
    "s_and_b32 s76, s71, s54\n"
    "v_mfma_f32_16x16x32_bf16 v[52:55], a[16:19], v[20:23], v[52:55]\n"
    "ds_read_b128 v[20:23], v9 offset:9216\n"
    "v_mfma_f32_16x16x32_bf16 v[48:51], a[24:27], v[24:27], v[48:51]\n"
    "s_waitcnt lgkmcnt(6)\n"
    "s_mul_i32 s76, s76, s53\n"
    "v_mfma_f32_16x16x32_bf16 v[52:55], a[28:31], v[24:27], v[52:55]\n"
    "ds_read_b128 v[24:27], v9 offset:10240\n"
    "v_mfma_f32_16x16x32_bf16 v[48:51], a[36:39], v[28:31], v[48:51]\n"
    "s_waitcnt lgkmcnt(6)\n"
    "s_add_u32 s76, s76, s52\n"
    "v_mfma_f32_16x16x32_bf16 v[52:55], a[40:43], v[28:31], v[52:55]\n"
    "ds_read_b128 v[28:31], v9 offset:11264\n"
    "v_mfma_f32_16x16x32_bf16 v[56:59], a[0:3], v[32:35], 0\n"
    "s_waitcnt lgkmcnt(6)\n"
    "s_and_b32 s77, s71, 1\n"
    "v_mfma_f32_16x16x32_bf16 v[60:63], a[4:7], v[32:35], 0\n"
    "ds_read_b128 v[32:35], v9 offset:12288\n"
    "v_mfma_f32_16x16x32_bf16 v[56:59], a[12:15], v[36:39], v[56:59]\n"
    "s_waitcnt lgkmcnt(6)\n"
    "s_mul_i32 s77, s77, 65536\n"
    "v_mfma_f32_16x16x32_bf16 v[60:63], a[16:19], v[36:39], v[60:63]\n"
    "ds_read_b128 v[36:39], v9 offset:13312\n"
    "v_mfma_f32_16x16x32_bf16 v[56:59], a[24:27], v[40:43], v[56:59]\n"
    "s_waitcnt lgkmcnt(6)\n"
    "s_add_u32 s77, s77, s59\n"
    "v_mfma_f32_16x16x32_bf16 v[60:63], a[28:31], v[40:43], v[60:63]\n"
    "ds_read_b128 v[40:43], v9 offset:14336\n"
    "v_mfma_f32_16x16x32_bf16 v[56:59], a[36:39], v[44:47], v[56:59]\n"
    "s_waitcnt lgkmcnt(6)\n"
    "v_mfma_f32_16x16x32_bf16 v[60:63], a[40:43], v[44:47], v[60:63]\n"
    "ds_read_b128 v[44:47], v9 offset:15360\n"
    "v_mfma_f32_16x16x32_bf16 v[48:51], a[48:51], v[16:19], v[48:51]\n"
    "v_mfma_f32_16x16x32_bf16 v[52:55], a[52:55], v[16:19], v[52:55]\n"
    "s_waitcnt lgkmcnt(6)\n"
    "v_mfma_f32_16x16x32_bf16 v[64:67], a[56:59], v[16:19], v[64:67]\n"
    "ds_read_b128 v[16:19], v9 offset:16384\n"
    "v_mfma_f32_16x16x32_bf16 v[56:59], a[48:51], v[20:23], v[56:59]\n"
    "v_mfma_f32_16x16x32_bf16 v[60:63], a[52:55], v[20:23], v[60:63]\n"
    "s_waitcnt lgkmcnt(6)\n"
    "v_mfma_f32_16x16x32_bf16 v[68:71], a[56:59], v[20:23], v[68:71]\n"
    "ds_read_b128 v[20:23], v9 offset:17408\n"
    "v_mfma_f32_16x16x32_bf16 v[48:51], a[60:63], v[24:27], v[48:51]\n"
    "v_mfma_f32_16x16x32_bf16 v[52:55], a[64:67], v[24:27], v[52:55]\n"
    "s_waitcnt lgkmcnt(6)\n"
    "v_mfma_f32_16x16x32_bf16 v[64:67], a[68:71], v[24:27], v[64:67]\n"
    "ds_read_b128 v[24:27], v9 offset:18432\n"
    "v_mfma_f32_16x16x32_bf16 v[56:59], a[60:63], v[28:31], v[56:59]\n"
    "v_mfma_f32_16x16x32_bf16 v[60:63], a[64:67], v[28:31], v[60:63]\n"
    "s_waitcnt lgkmcnt(6)\n"
    "v_mfma_f32_16x16x32_bf16 v[68:71], a[68:71], v[28:31], v[68:71]\n"
    "ds_read_b128 v[28:31], v9 offset:19456\n"
    "v_mfma_f32_16x16x32_bf16 v[48:51], a[72:75], v[32:35], v[48:51]\n"
    "v_mfma_f32_16x16x32_bf16 v[52:55], a[76:79], v[32:35], v[52:55]\n"
    "s_waitcnt lgkmcnt(6)\n"
    "v_mfma_f32_16x16x32_bf16 v[64:67], a[80:83], v[32:35], v[64:67]\n"
    "ds_read_b128 v[32:35], v9 offset:20480\n"
    "v_mfma_f32_16x16x32_bf16 v[56:59], a[72:75], v[36:39], v[56:59]\n"
    "v_mfma_f32_16x16x32_bf16 v[60:63], a[76:79], v[36:39], v[60:63]\n"
    "s_waitcnt lgkmcnt(6)\n"
    "v_mfma_f32_16x16x32_bf16 v[68:71], a[80:83], v[36:39], v[68:71]\n"
    "ds_read_b128 v[36:39], v9 offset:21504\n"
    "v_mfma_f32_16x16x32_bf16 v[48:51], a[84:87], v[40:43], v[48:51]\n"
    "v_mov_b32 v12, s74\n"
    "v_mfma_f32_16x16x32_bf16 v[52:55], a[88:91], v[40:43], v[52:55]\n"
    "s_waitcnt lgkmcnt(6)\n"
    "s_waitcnt vmcnt(0)\n"
    "v_add_u32 v13, v11, v3\n"
    "v_mfma_f32_16x16x32_bf16 v[64:67], a[92:95], v[40:43], v[64:67]\n"
    "ds_read_b128 v[40:43], v9 offset:22528\n"
    "buffer_atomic_smax v12, v14, s[36:39], 0 offen sc1\n"
    "v_cmp_gt_i32 vcc, s71, v13\n"
    "v_mfma_f32_16x16x32_bf16 v[56:59], a[84:87], v[44:47], v[56:59]\n"
    "v_add_u32 v10, 49152, v9\n"
    "v_mfma_f32_16x16x32_bf16 v[60:63], a[88:91], v[44:47], v[60:63]\n"
    "s_waitcnt lgkmcnt(6)\n"
    "v_mfma_f32_16x16x32_bf16 v[68:71], a[92:95], v[44:47], v[68:71]\n"
    "ds_read_b128 v[44:47], v9 offset:23552\n"
    "s_branch .LgWO_mid_%=\n"
    ".LgWO_idle_%=:\n"
    "s_waitcnt vmcnt(3)\n"
    "v_add_u32 v13, v11, v3\n"
    "v_cmp_gt_i32 vcc, s71, v13\n"
    "s_cbranch_vccnz .LgWO_idle2_%=\n"
    "s_and_b32 s76, s71, s54\n"
    "s_mul_i32 s76, s76, s53\n"
    "s_add_u32 s76, s76, s52\n"
    "s_and_b32 s77, s71, 1\n"
    "s_mul_i32 s77, s77, 65536\n"
    "s_add_u32 s77, s77, s59\n"
    "s_add_u32 m0, s77, s59\n"
    "s_add_u32 s77, s77, 8192\n"
    "buffer_load_dwordx4 v1, s[36:39], s76 offen sc1 lds\n"
    "buffer_load_dwordx4 v1, s[36:39], s76 offen offset:1024 sc1 lds\n"
    "s_add_u32 s76, s76, 16384\n"
    "s_add_u32 m0, s77, s59\n"
    "s_add_u32 s77, s77, 8192\n"
    "buffer_load_dwordx4 v1, s[36:39], s76 offen sc1 lds\n"
    "buffer_load_dwordx4 v1, s[36:39], s76 offen offset:1024 sc1 lds\n"
    "s_add_u32 s76, s76, 16384\n"
    "s_add_u32 m0, s77, s59\n"
    "s_add_u32 s77, s77, 8192\n"
    "buffer_load_dwordx4 v1, s[36:39], s76 offen sc1 lds\n"
    "buffer_load_dwordx4 v1, s[36:39], s76 offen offset:1024 sc1 lds\n"
    "s_add_u32 s76, s76, 16384\n"
    "s_add_u32 m0, s77, s59\n"
    "s_add_u32 s77, s77, 8192\n"
    "buffer_load_dwordx4 v1, s[36:39], s76 offen sc1 lds\n"
    "buffer_load_dwordx4 v1, s[36:39], s76 offen offset:1024 sc1 lds\n"
    "s_add_u32 s76, s76, 16384\n"
    "s_add_u32 m0, s77, s59\n"
    "s_add_u32 s77, s77, 8192\n"
    "buffer_load_dwordx4 v1, s[36:39], s76 offen sc1 lds\n"
    "buffer_load_dwordx4 v1, s[36:39], s76 offen offset:1024 sc1 lds\n"
    "s_add_u32 s76, s76, 16384\n"
    "s_add_u32 m0, s77, s59\n"
    "s_add_u32 s77, s77, 8192\n"
    "buffer_load_dwordx4 v1, s[36:39], s76 offen sc1 lds\n"
    "buffer_load_dwordx4 v1, s[36:39], s76 offen offset:1024 sc1 lds\n"
    "s_add_u32 s76, s76, 16384\n"
    "s_add_u32 m0, s77, s59\n"
    "s_add_u32 s77, s77, 8192\n"
    "buffer_load_dwordx4 v1, s[36:39], s76 offen sc1 lds\n"
    "buffer_load_dwordx4 v1, s[36:39], s76 offen offset:1024 sc1 lds\n"
    "s_add_u32 s76, s76, 16384\n"
    "s_add_u32 m0, s77, s59\n"
    "s_add_u32 s77, s77, 8192\n"
    "buffer_load_dwordx4 v1, s[36:39], s76 offen sc1 lds\n"
    "buffer_load_dwordx4 v1, s[36:39], s76 offen offset:1024 sc1 lds\n"
    "s_add_u32 s76, s76, 16384\n"
    "s_waitcnt vmcnt(16)\n"
    "s_sub_u32 s74, s68, 1\n"
    "v_mov_b32 v12, s74\n"
    "s_mov_b64 exec, s[62:63]\n"
    "buffer_atomic_smax v12, v14, s[36:39], 0 offen sc1\n"
    "s_mov_b64 exec, -1\n"
    "s_waitcnt vmcnt(1)\n"
    "s_branch .LgWO_bar_%=\n"
    ".LgWO_idle2_%=:\n"
    "s_or_b64 exec, vcc, 1\n"
    "buffer_atomic_smax v11, v2, s[36:39], 0 offen sc0 sc1\n"
    "s_mov_b64 exec, -1\n"
    "s_waitcnt vmcnt(1)\n"
    "s_sub_u32 s74, s68, 1\n"
    "v_mov_b32 v12, s74\n"
    "s_mov_b64 exec, s[62:63]\n"
    "buffer_atomic_smax v12, v14, s[36:39], 0 offen sc1\n"
    "s_mov_b64 exec, -1\n"
    "s_waitcnt vmcnt(1)\n"
    "s_branch .LgWO_block1_%=\n"
    ".LgWO_block_%=:\n"
    "v_add_u32 v13, v11, v3\n"
    "v_cmp_gt_i32 vcc, s71, v13\n"
    "s_nop 0\n"
    "s_or_b64 exec, vcc, 1\n"
    "buffer_atomic_smax v11, v2, s[36:39], 0 offen sc0 sc1\n"
    "s_mov_b64 exec, -1\n"
    "s_waitcnt vmcnt(0)\n"
    ".LgWO_block1_%=:\n"
    "v_add_u32 v13, v11, v3\n"
    "v_cmp_gt_i32 vcc, s71, v13\n"
    "s_cbranch_vccz .LgWO_go_%=\n"
    "s_sleep 4\n"
    "s_sub_u32 s72, s72, 1\n"
    "s_cbranch_scc0 .LgWO_block_%=\n"
    "s_mov_b32 s72, 0\n"
    ".LgWO_go_%=:\n"
    "s_and_b32 s76, s71, s54\n"
    "s_mul_i32 s76, s76, s53\n"
    "s_add_u32 s76, s76, s52\n"
    "s_and_b32 s77, s71, 1\n"
    "s_mul_i32 s77, s77, 65536\n"
    "s_add_u32 s77, s77, s59\n"
    "s_add_u32 m0, s77, s59\n"
    "s_add_u32 s77, s77, 8192\n"
    "buffer_load_dwordx4 v1, s[36:39], s76 offen sc1 lds\n"
    "buffer_load_dwordx4 v1, s[36:39], s76 offen offset:1024 sc1 lds\n"
    "s_add_u32 s76, s76, 16384\n"
    "s_add_u32 m0, s77, s59\n"
    "s_add_u32 s77, s77, 8192\n"
    "buffer_load_dwordx4 v1, s[36:39], s76 offen sc1 lds\n"
    "buffer_load_dwordx4 v1, s[36:39], s76 offen offset:1024 sc1 lds\n"
    "s_add_u32 s76, s76, 16384\n"
    "s_add_u32 m0, s77, s59\n"
    "s_add_u32 s77, s77, 8192\n"
    "buffer_load_dwordx4 v1, s[36:39], s76 offen sc1 lds\n"
    "buffer_load_dwordx4 v1, s[36:39], s76 offen offset:1024 sc1 lds\n"
    "s_add_u32 s76, s76, 16384\n"
    "s_add_u32 m0, s77, s59\n"
    "s_add_u32 s77, s77, 8192\n"
    "buffer_load_dwordx4 v1, s[36:39], s76 offen sc1 lds\n"
    "buffer_load_dwordx4 v1, s[36:39], s76 offen offset:1024 sc1 lds\n"
    "s_add_u32 s76, s76, 16384\n"
    "s_add_u32 m0, s77, s59\n"
    "s_add_u32 s77, s77, 8192\n"
    "buffer_load_dwordx4 v1, s[36:39], s76 offen sc1 lds\n"
    "buffer_load_dwordx4 v1, s[36:39], s76 offen offset:1024 sc1 lds\n"
    "s_add_u32 s76, s76, 16384\n"
    "s_add_u32 m0, s77, s59\n"
    "s_add_u32 s77, s77, 8192\n"
    "buffer_load_dwordx4 v1, s[36:39], s76 offen sc1 lds\n"
    "buffer_load_dwordx4 v1, s[36:39], s76 offen offset:1024 sc1 lds\n"
    "s_add_u32 s76, s76, 16384\n"
    "s_add_u32 m0, s77, s59\n"
    "s_add_u32 s77, s77, 8192\n"
    "buffer_load_dwordx4 v1, s[36:39], s76 offen sc1 lds\n"
    "buffer_load_dwordx4 v1, s[36:39], s76 offen offset:1024 sc1 lds\n"
    "s_add_u32 s76, s76, 16384\n"
    "s_add_u32 m0, s77, s59\n"
    "s_add_u32 s77, s77, 8192\n"
    "buffer_load_dwordx4 v1, s[36:39], s76 offen sc1 lds\n"
    "buffer_load_dwordx4 v1, s[36:39], s76 offen offset:1024 sc1 lds\n"
    "s_add_u32 s76, s76, 16384\n"
    "s_waitcnt vmcnt(0)\n"
    "s_branch .LgWO_bar_%=\n"
    ".LgWO_FIN_%=:\n"
  );
}
DEVI void role_down(const Params& p, int L, int j, int i, u32 dbg, unsigned long long tag) {
  const int lane = threadIdx.x & 63, w = threadIdx.x >> 6, e0 = j * 1152 * 576 + i * 192;
  GEMM_DECL;
  g_w0 = make_srd(p.w2 + e0, 4608u * 576 * 2 - e0 * 2); g_w1 = g_w0; g_w2 = g_w0;
  g_ldw = 1152;
  g_in_base = pgb(L, B_H + 9 * j); g_in_stride = SLOT; g_in_mask = RH - 1;
  g_out_base = pgb(L, B_PF + 5 * j); g_out_kb = 12 * i; g_out_mask = RP - 1;
  g_flag = lane < 4 ? mbox(L, lane, SL_REDF, 12 * j + 4 * i + w) : ((lane < 10 && w == 0) ? mbox(L, j, SL_UP + lane - 4, 16 + i) : OOB);
  g_paddr = mbox(L, j, SL_DOWN + i, lane);
  g_pbias = lane < 24 ? 0 : (lane < 40 ? RP : 1000);
  v16u sa, sb, va;
  gemm_args(GEMM_ARGS, p, dbg, tag, sa, sb, va);
  RUN_ASM(
    "s_nop 4\n"
    "v_add_u32 v8, s59, v0\n"
    "s_mov_b32 s68, 0\n"
    "s_mov_b32 s71, 0\n"
    "s_mov_b32 s78, 81920\n"
    "v_add_u32 v9, s78, v0\n"
    "s_mov_b32 s64, 0xbfb8aa3b\n"
    "s_mov_b32 s72, 262144\n"
    "s_add_u32 m0, s59, 0\n"
    "s_mov_b32 s74, 0\n"
    "buffer_load_dwordx4 v5, s[40:43], s74 offen lds\n"
    "s_add_u32 m0, s59, 4096\n"
    "s_mov_b32 s74, 128\n"
    "buffer_load_dwordx4 v5, s[40:43], s74 offen lds\n"
    "s_add_u32 m0, s59, 8192\n"
    "s_mov_b32 s74, 256\n"
    "buffer_load_dwordx4 v5, s[40:43], s74 offen lds\n"
    "s_add_u32 m0, s59, 12288\n"
    "s_mov_b32 s74, 36864\n"
    "buffer_load_dwordx4 v5, s[40:43], s74 offen lds\n"
    "s_add_u32 m0, s59, 16384\n"
    "s_mov_b32 s74, 36992\n"
    "buffer_load_dwordx4 v5, s[40:43], s74 offen lds\n"
    "s_add_u32 m0, s59, 20480\n"
    "s_mov_b32 s74, 37120\n"
    "buffer_load_dwordx4 v5, s[40:43], s74 offen lds\n"
    "s_add_u32 m0, s59, 24576\n"
    "s_mov_b32 s74, 73728\n"
    "buffer_load_dwordx4 v5, s[40:43], s74 offen lds\n"
    "s_add_u32 m0, s59, 28672\n"
    "s_mov_b32 s74, 73856\n"
    "buffer_load_dwordx4 v5, s[40:43], s74 offen lds\n"
    "s_add_u32 m0, s59, 32768\n"
    "s_mov_b32 s74, 73984\n"
    "buffer_load_dwordx4 v5, s[40:43], s74 offen lds\n"
    "s_add_u32 m0, s59, 36864\n"
    "s_mov_b32 s74, 110592\n"
    "buffer_load_dwordx4 v5, s[40:43], s74 offen lds\n"
    "s_add_u32 m0, s59, 40960\n"
    "s_mov_b32 s74, 110720\n"
    "buffer_load_dwordx4 v5, s[40:43], s74 offen lds\n"
    "s_add_u32 m0, s59, 45056\n"
    "s_mov_b32 s74, 110848\n"
    "buffer_load_dwordx4 v5, s[40:43], s74 offen lds\n"
    "s_add_u32 m0, s59, 49152\n"
    "s_mov_b32 s74, 147456\n"
    "buffer_load_dwordx4 v5, s[40:43], s74 offen lds\n"
    "s_add_u32 m0, s59, 53248\n"
    "s_mov_b32 s74, 147584\n"
    "buffer_load_dwordx4 v5, s[40:43], s74 offen lds\n"
    "s_add_u32 m0, s59, 57344\n"
    "s_mov_b32 s74, 147712\n"
    "buffer_load_dwordx4 v5, s[40:43], s74 offen lds\n"
    "s_add_u32 m0, s59, 61440\n"
    "s_mov_b32 s74, 184320\n"
    "buffer_load_dwordx4 v5, s[40:43], s74 offen lds\n"
    "s_add_u32 m0, s59, 65536\n"
    "s_mov_b32 s74, 184448\n"
    "buffer_load_dwordx4 v5, s[40:43], s74 offen lds\n"
    "s_add_u32 m0, s59, 69632\n"
    "s_mov_b32 s74, 184576\n"
    "buffer_load_dwordx4 v5, s[40:43], s74 offen lds\n"
    "s_add_u32 m0, s59, 73728\n"
    "s_mov_b32 s74, 221184\n"
    "buffer_load_dwordx4 v5, s[40:43], s74 offen lds\n"
    "s_add_u32 m0, s59, 77824\n"
    "s_mov_b32 s74, 221312\n"
    "buffer_load_dwordx4 v5, s[40:43], s74 offen lds\n"
    "s_add_u32 m0, s59, 81920\n"
    "s_mov_b32 s74, 221440\n"
    "buffer_load_dwordx4 v5, s[40:43], s74 offen lds\n"
    "s_add_u32 m0, s59, 86016\n"
    "s_mov_b32 s74, 258048\n"
    "buffer_load_dwordx4 v5, s[40:43], s74 offen lds\n"
    "s_add_u32 m0, s59, 90112\n"
    "s_mov_b32 s74, 258176\n"
    "buffer_load_dwordx4 v5, s[40:43], s74 offen lds\n"
    "s_add_u32 m0, s59, 94208\n"
    "s_mov_b32 s74, 258304\n"
    "buffer_load_dwordx4 v5, s[40:43], s74 offen lds\n"
    "s_add_u32 m0, s59, 98304\n"
    "s_mov_b32 s74, 294912\n"
    "buffer_load_dwordx4 v5, s[40:43], s74 offen lds\n"
    "s_add_u32 m0, s59, 102400\n"
    "s_mov_b32 s74, 295040\n"
    "buffer_load_dwordx4 v5, s[40:43], s74 offen lds\n"
    "s_add_u32 m0, s59, 106496\n"
    "s_mov_b32 s74, 295168\n"
    "buffer_load_dwordx4 v5, s[40:43], s74 offen lds\n"
    "s_add_u32 m0, s59, 110592\n"
    "s_mov_b32 s74, 331776\n"
    "buffer_load_dwordx4 v5, s[40:43], s74 offen lds\n"
    "s_add_u32 m0, s59, 114688\n"
    "s_mov_b32 s74, 331904\n"
    "buffer_load_dwordx4 v5, s[40:43], s74 offen lds\n"
    "s_add_u32 m0, s59, 118784\n"
    "s_mov_b32 s74, 332032\n"
    "buffer_load_dwordx4 v5, s[40:43], s74 offen lds\n"
    "v_add_u32 v16, 49152, v6\n"
    "v_add_u32 v17, 98304, v6\n"
    "v_add_u32 v18, 49152, v7\n"
    "v_add_u32 v19, 98304, v7\n"
    "s_waitcnt vmcnt(24) lgkmcnt(0)\n"
    "s_barrier\n"
    "s_add_u32 m0, s59, 122880\n"
    "s_mov_b32 s74, 368640\n"
    "buffer_load_dwordx4 v5, s[40:43], s74 offen lds\n"
    "s_add_u32 m0, s59, 126976\n"
    "s_mov_b32 s74, 368768\n"
    "buffer_load_dwordx4 v5, s[40:43], s74 offen lds\n"
    "s_add_u32 m0, s59, 131072\n"
    "s_mov_b32 s74, 368896\n"
    "buffer_load_dwordx4 v5, s[40:43], s74 offen lds\n"
    "s_add_u32 m0, s59, 135168\n"
    "s_mov_b32 s74, 405504\n"
    "buffer_load_dwordx4 v5, s[40:43], s74 offen lds\n"
    "s_add_u32 m0, s59, 139264\n"
    "s_mov_b32 s74, 405632\n"
    "buffer_load_dwordx4 v5, s[40:43], s74 offen lds\n"
    "s_add_u32 m0, s59, 143360\n"
    "s_mov_b32 s74, 405760\n"
    "buffer_load_dwordx4 v5, s[40:43], s74 offen lds\n"
    "ds_read_b64_tr_b16 a[0:1], v6 offset:0\n"
    "ds_read_b64_tr_b16 a[2:3], v6 offset:512\n"
    "ds_read_b64_tr_b16 a[4:5], v6 offset:8\n"
    "ds_read_b64_tr_b16 a[6:7], v6 offset:520\n"
    "ds_read_b64_tr_b16 a[8:9], v7 offset:0\n"
    "ds_read_b64_tr_b16 a[10:11], v7 offset:512\n"
    "ds_read_b64_tr_b16 a[12:13], v6 offset:12288\n"
    "ds_read_b64_tr_b16 a[14:15], v6 offset:12800\n"
    "ds_read_b64_tr_b16 a[16:17], v6 offset:12296\n"
    "ds_read_b64_tr_b16 a[18:19], v6 offset:12808\n"
    "ds_read_b64_tr_b16 a[20:21], v7 offset:12288\n"
    "ds_read_b64_tr_b16 a[22:23], v7 offset:12800\n"
    "s_waitcnt vmcnt(24) lgkmcnt(0)\n"
    "s_barrier\n"
    "s_add_u32 m0, s59, 0\n"
    "s_mov_b32 s74, 442368\n"
    "buffer_load_dwordx4 v5, s[40:43], s74 offen lds\n"
    "s_add_u32 m0, s59, 4096\n"
    "s_mov_b32 s74, 442496\n"
    "buffer_load_dwordx4 v5, s[40:43], s74 offen lds\n"
    "s_add_u32 m0, s59, 8192\n"
    "s_mov_b32 s74, 442624\n"
    "buffer_load_dwordx4 v5, s[40:43], s74 offen lds\n"
    "s_add_u32 m0, s59, 12288\n"
    "s_mov_b32 s74, 479232\n"
    "buffer_load_dwordx4 v5, s[40:43], s74 offen lds\n"
    "s_add_u32 m0, s59, 16384\n"
    "s_mov_b32 s74, 479360\n"
    "buffer_load_dwordx4 v5, s[40:43], s74 offen lds\n"
    "s_add_u32 m0, s59, 20480\n"
    "s_mov_b32 s74, 479488\n"
    "buffer_load_dwordx4 v5, s[40:43], s74 offen lds\n"
    "ds_read_b64_tr_b16 a[24:25], v6 offset:24576\n"
    "ds_read_b64_tr_b16 a[26:27], v6 offset:25088\n"
    "ds_read_b64_tr_b16 a[28:29], v6 offset:24584\n"
    "ds_read_b64_tr_b16 a[30:31], v6 offset:25096\n"
    "ds_read_b64_tr_b16 a[32:33], v7 offset:24576\n"
    "ds_read_b64_tr_b16 a[34:35], v7 offset:25088\n"
    "ds_read_b64_tr_b16 a[36:37], v6 offset:36864\n"
    "ds_read_b64_tr_b16 a[38:39], v6 offset:37376\n"
    "ds_read_b64_tr_b16 a[40:41], v6 offset:36872\n"
    "ds_read_b64_tr_b16 a[42:43], v6 offset:37384\n"
    "ds_read_b64_tr_b16 a[44:45], v7 offset:36864\n"
    "ds_read_b64_tr_b16 a[46:47], v7 offset:37376\n"
    "s_waitcnt vmcnt(24) lgkmcnt(0)\n"
    "s_barrier\n"
    "s_add_u32 m0, s59, 24576\n"
    "s_mov_b32 s74, 516096\n"
    "buffer_load_dwordx4 v5, s[40:43], s74 offen lds\n"
    "s_add_u32 m0, s59, 28672\n"
    "s_mov_b32 s74, 516224\n"
    "buffer_load_dwordx4 v5, s[40:43], s74 offen lds\n"
    "s_add_u32 m0, s59, 32768\n"
    "s_mov_b32 s74, 516352\n"
    "buffer_load_dwordx4 v5, s[40:43], s74 offen lds\n"
    "s_add_u32 m0, s59, 36864\n"
    "s_mov_b32 s74, 552960\n"
    "buffer_load_dwordx4 v5, s[40:43], s74 offen lds\n"
    "s_add_u32 m0, s59, 40960\n"
    "s_mov_b32 s74, 553088\n"
    "buffer_load_dwordx4 v5, s[40:43], s74 offen lds\n"
    "s_add_u32 m0, s59, 45056\n"
    "s_mov_b32 s74, 553216\n"
    "buffer_load_dwordx4 v5, s[40:43], s74 offen lds\n"
    "ds_read_b64_tr_b16 a[48:49], v16 offset:0\n"
    "ds_read_b64_tr_b16 a[50:51], v16 offset:512\n"
    "ds_read_b64_tr_b16 a[52:53], v16 offset:8\n"
    "ds_read_b64_tr_b16 a[54:55], v16 offset:520\n"
    "ds_read_b64_tr_b16 a[56:57], v18 offset:0\n"
    "ds_read_b64_tr_b16 a[58:59], v18 offset:512\n"
    "ds_read_b64_tr_b16 a[60:61], v16 offset:12288\n"
    "ds_read_b64_tr_b16 a[62:63], v16 offset:12800\n"
    "ds_read_b64_tr_b16 a[64:65], v16 offset:12296\n"
    "ds_read_b64_tr_b16 a[66:67], v16 offset:12808\n"
    "ds_read_b64_tr_b16 a[68:69], v18 offset:12288\n"
    "ds_read_b64_tr_b16 a[70:71], v18 offset:12800\n"
    "s_waitcnt vmcnt(24) lgkmcnt(0)\n"
    "s_barrier\n"
    "s_add_u32 m0, s59, 49152\n"
    "s_mov_b32 s74, 589824\n"
    "buffer_load_dwordx4 v5, s[40:43], s74 offen lds\n"
    "s_add_u32 m0, s59, 53248\n"
    "s_mov_b32 s74, 589952\n"
    "buffer_load_dwordx4 v5, s[40:43], s74 offen lds\n"
    "s_add_u32 m0, s59, 57344\n"
    "s_mov_b32 s74, 590080\n"
    "buffer_load_dwordx4 v5, s[40:43], s74 offen lds\n"
    "s_add_u32 m0, s59, 61440\n"
    "s_mov_b32 s74, 626688\n"
    "buffer_load_dwordx4 v5, s[40:43], s74 offen lds\n"
    "s_add_u32 m0, s59, 65536\n"
    "s_mov_b32 s74, 626816\n"
    "buffer_load_dwordx4 v5, s[40:43], s74 offen lds\n"
    "s_add_u32 m0, s59, 69632\n"
    "s_mov_b32 s74, 626944\n"
    "buffer_load_dwordx4 v5, s[40:43], s74 offen lds\n"
    "ds_read_b64_tr_b16 a[72:73], v16 offset:24576\n"
    "ds_read_b64_tr_b16 a[74:75], v16 offset:25088\n"
    "ds_read_b64_tr_b16 a[76:77], v16 offset:24584\n"
    "ds_read_b64_tr_b16 a[78:79], v16 offset:25096\n"
    "ds_read_b64_tr_b16 a[80:81], v18 offset:24576\n"
    "ds_read_b64_tr_b16 a[82:83], v18 offset:25088\n"
    "ds_read_b64_tr_b16 a[84:85], v16 offset:36864\n"
    "ds_read_b64_tr_b16 a[86:87], v16 offset:37376\n"
    "ds_read_b64_tr_b16 a[88:89], v16 offset:36872\n"
    "ds_read_b64_tr_b16 a[90:91], v16 offset:37384\n"
    "ds_read_b64_tr_b16 a[92:93], v18 offset:36864\n"
    "ds_read_b64_tr_b16 a[94:95], v18 offset:37376\n"
    "s_waitcnt vmcnt(24) lgkmcnt(0)\n"
    "s_barrier\n"
    "s_add_u32 m0, s59, 73728\n"
    "s_mov_b32 s74, 663552\n"
    "buffer_load_dwordx4 v5, s[40:43], s74 offen lds\n"
    "s_add_u32 m0, s59, 77824\n"
    "s_mov_b32 s74, 663680\n"
    "buffer_load_dwordx4 v5, s[40:43], s74 offen lds\n"
    "s_add_u32 m0, s59, 81920\n"
    "s_mov_b32 s74, 663808\n"
    "buffer_load_dwordx4 v5, s[40:43], s74 offen lds\n"
    "s_add_u32 m0, s59, 86016\n"
    "s_mov_b32 s74, 700416\n"
    "buffer_load_dwordx4 v5, s[40:43], s74 offen lds\n"
    "s_add_u32 m0, s59, 90112\n"
    "s_mov_b32 s74, 700544\n"
    "buffer_load_dwordx4 v5, s[40:43], s74 offen lds\n"
    "s_add_u32 m0, s59, 94208\n"
    "s_mov_b32 s74, 700672\n"
    "buffer_load_dwordx4 v5, s[40:43], s74 offen lds\n"
    "ds_read_b64_tr_b16 a[96:97], v17 offset:0\n"
    "ds_read_b64_tr_b16 a[98:99], v17 offset:512\n"
    "ds_read_b64_tr_b16 a[100:101], v17 offset:8\n"
    "ds_read_b64_tr_b16 a[102:103], v17 offset:520\n"
    "ds_read_b64_tr_b16 a[104:105], v19 offset:0\n"
    "ds_read_b64_tr_b16 a[106:107], v19 offset:512\n"
    "ds_read_b64_tr_b16 a[108:109], v17 offset:12288\n"
    "ds_read_b64_tr_b16 a[110:111], v17 offset:12800\n"
    "ds_read_b64_tr_b16 a[112:113], v17 offset:12296\n"
    "ds_read_b64_tr_b16 a[114:115], v17 offset:12808\n"
    "ds_read_b64_tr_b16 a[116:117], v19 offset:12288\n"
    "ds_read_b64_tr_b16 a[118:119], v19 offset:12800\n"
    "s_waitcnt vmcnt(24) lgkmcnt(0)\n"
    "s_barrier\n"
    "s_add_u32 m0, s59, 98304\n"
    "s_mov_b32 s74, 737280\n"
    "buffer_load_dwordx4 v5, s[40:43], s74 offen lds\n"
    "s_add_u32 m0, s59, 102400\n"
    "s_mov_b32 s74, 737408\n"
    "buffer_load_dwordx4 v5, s[40:43], s74 offen lds\n"
    "s_add_u32 m0, s59, 106496\n"
    "s_mov_b32 s74, 737536\n"
    "buffer_load_dwordx4 v5, s[40:43], s74 offen lds\n"
    "s_add_u32 m0, s59, 110592\n"
    "s_mov_b32 s74, 774144\n"
    "buffer_load_dwordx4 v5, s[40:43], s74 offen lds\n"
    "s_add_u32 m0, s59, 114688\n"
    "s_mov_b32 s74, 774272\n"
    "buffer_load_dwordx4 v5, s[40:43], s74 offen lds\n"
    "s_add_u32 m0, s59, 118784\n"
    "s_mov_b32 s74, 774400\n"
    "buffer_load_dwordx4 v5, s[40:43], s74 offen lds\n"
    "ds_read_b64_tr_b16 a[120:121], v17 offset:24576\n"
    "ds_read_b64_tr_b16 a[122:123], v17 offset:25088\n"
    "ds_read_b64_tr_b16 a[124:125], v17 offset:24584\n"
    "ds_read_b64_tr_b16 a[126:127], v17 offset:25096\n"
    "ds_read_b64_tr_b16 a[128:129], v19 offset:24576\n"
    "ds_read_b64_tr_b16 a[130:131], v19 offset:25088\n"
    "ds_read_b64_tr_b16 a[132:133], v17 offset:36864\n"
    "ds_read_b64_tr_b16 a[134:135], v17 offset:37376\n"
    "ds_read_b64_tr_b16 a[136:137], v17 offset:36872\n"
    "ds_read_b64_tr_b16 a[138:139], v17 offset:37384\n"
    "ds_read_b64_tr_b16 a[140:141], v19 offset:36864\n"
    "ds_read_b64_tr_b16 a[142:143], v19 offset:37376\n"
    "s_waitcnt vmcnt(24) lgkmcnt(0)\n"
    "s_barrier\n"
    "s_add_u32 m0, s59, 122880\n"
    "s_mov_b32 s74, 811008\n"
    "buffer_load_dwordx4 v5, s[40:43], s74 offen lds\n"
    "s_add_u32 m0, s59, 126976\n"
    "s_mov_b32 s74, 811136\n"
    "buffer_load_dwordx4 v5, s[40:43], s74 offen lds\n"
    "s_add_u32 m0, s59, 131072\n"
    "s_mov_b32 s74, 811264\n"
    "buffer_load_dwordx4 v5, s[40:43], s74 offen lds\n"
    "s_add_u32 m0, s59, 135168\n"
    "s_mov_b32 s74, 847872\n"
    "buffer_load_dwordx4 v5, s[40:43], s74 offen lds\n"
    "s_add_u32 m0, s59, 139264\n"
    "s_mov_b32 s74, 848000\n"
    "buffer_load_dwordx4 v5, s[40:43], s74 offen lds\n"
    "s_add_u32 m0, s59, 143360\n"
    "s_mov_b32 s74, 848128\n"
    "buffer_load_dwordx4 v5, s[40:43], s74 offen lds\n"
    "ds_read_b64_tr_b16 a[144:145], v6 offset:0\n"
    "ds_read_b64_tr_b16 a[146:147], v6 offset:512\n"
    "ds_read_b64_tr_b16 a[148:149], v6 offset:8\n"
    "ds_read_b64_tr_b16 a[150:151], v6 offset:520\n"
    "ds_read_b64_tr_b16 a[152:153], v7 offset:0\n"
    "ds_read_b64_tr_b16 a[154:155], v7 offset:512\n"
    "ds_read_b64_tr_b16 a[156:157], v6 offset:12288\n"
    "ds_read_b64_tr_b16 a[158:159], v6 offset:12800\n"
    "ds_read_b64_tr_b16 a[160:161], v6 offset:12296\n"
    "ds_read_b64_tr_b16 a[162:163], v6 offset:12808\n"
    "ds_read_b64_tr_b16 a[164:165], v7 offset:12288\n"
    "ds_read_b64_tr_b16 a[166:167], v7 offset:12800\n"
    "s_waitcnt vmcnt(24) lgkmcnt(0)\n"
    "s_barrier\n"
    "s_add_u32 m0, s59, 0\n"
    "s_mov_b32 s74, 884736\n"
    "buffer_load_dwordx4 v5, s[40:43], s74 offen lds\n"
    "s_add_u32 m0, s59, 4096\n"
    "s_mov_b32 s74, 884864\n"
    "buffer_load_dwordx4 v5, s[40:43], s74 offen lds\n"
    "s_add_u32 m0, s59, 8192\n"
    "s_mov_b32 s74, 884992\n"
    "buffer_load_dwordx4 v5, s[40:43], s74 offen lds\n"
    "s_add_u32 m0, s59, 12288\n"
    "s_mov_b32 s74, 921600\n"
    "buffer_load_dwordx4 v5, s[40:43], s74 offen lds\n"
    "s_add_u32 m0, s59, 16384\n"
    "s_mov_b32 s74, 921728\n"
    "buffer_load_dwordx4 v5, s[40:43], s74 offen lds\n"
    "s_add_u32 m0, s59, 20480\n"
    "s_mov_b32 s74, 921856\n"
    "buffer_load_dwordx4 v5, s[40:43], s74 offen lds\n"
    "ds_read_b64_tr_b16 a[168:169], v6 offset:24576\n"
    "ds_read_b64_tr_b16 a[170:171], v6 offset:25088\n"
    "ds_read_b64_tr_b16 a[172:173], v6 offset:24584\n"
    "ds_read_b64_tr_b16 a[174:175], v6 offset:25096\n"
    "ds_read_b64_tr_b16 a[176:177], v7 offset:24576\n"
    "ds_read_b64_tr_b16 a[178:179], v7 offset:25088\n"
    "ds_read_b64_tr_b16 a[180:181], v6 offset:36864\n"
    "ds_read_b64_tr_b16 a[182:183], v6 offset:37376\n"
    "ds_read_b64_tr_b16 a[184:185], v6 offset:36872\n"
    "ds_read_b64_tr_b16 a[186:187], v6 offset:37384\n"
    "ds_read_b64_tr_b16 a[188:189], v7 offset:36864\n"
    "ds_read_b64_tr_b16 a[190:191], v7 offset:37376\n"
    "s_waitcnt vmcnt(24) lgkmcnt(0)\n"
    "s_barrier\n"
    "s_add_u32 m0, s59, 24576\n"
    "s_mov_b32 s74, 958464\n"
    "buffer_load_dwordx4 v5, s[40:43], s74 offen lds\n"
    "s_add_u32 m0, s59, 28672\n"
    "s_mov_b32 s74, 958592\n"
    "buffer_load_dwordx4 v5, s[40:43], s74 offen lds\n"
    "s_add_u32 m0, s59, 32768\n"
    "s_mov_b32 s74, 958720\n"
    "buffer_load_dwordx4 v5, s[40:43], s74 offen lds\n"
    "s_add_u32 m0, s59, 36864\n"
    "s_mov_b32 s74, 995328\n"
    "buffer_load_dwordx4 v5, s[40:43], s74 offen lds\n"
    "s_add_u32 m0, s59, 40960\n"
    "s_mov_b32 s74, 995456\n"
    "buffer_load_dwordx4 v5, s[40:43], s74 offen lds\n"
    "s_add_u32 m0, s59, 45056\n"
    "s_mov_b32 s74, 995584\n"
    "buffer_load_dwordx4 v5, s[40:43], s74 offen lds\n"
    "ds_read_b64_tr_b16 a[192:193], v16 offset:0\n"
    "ds_read_b64_tr_b16 a[194:195], v16 offset:512\n"
    "ds_read_b64_tr_b16 a[196:197], v16 offset:8\n"
    "ds_read_b64_tr_b16 a[198:199], v16 offset:520\n"
    "ds_read_b64_tr_b16 a[200:201], v18 offset:0\n"
    "ds_read_b64_tr_b16 a[202:203], v18 offset:512\n"
    "ds_read_b64_tr_b16 a[204:205], v16 offset:12288\n"
    "ds_read_b64_tr_b16 a[206:207], v16 offset:12800\n"
    "ds_read_b64_tr_b16 a[208:209], v16 offset:12296\n"
    "ds_read_b64_tr_b16 a[210:211], v16 offset:12808\n"
    "ds_read_b64_tr_b16 a[212:213], v18 offset:12288\n"
    "ds_read_b64_tr_b16 a[214:215], v18 offset:12800\n"
    "s_waitcnt vmcnt(24) lgkmcnt(0)\n"
    "s_barrier\n"
    "s_add_u32 m0, s59, 49152\n"
    "s_mov_b32 s74, 1032192\n"
    "buffer_load_dwordx4 v5, s[40:43], s74 offen lds\n"
    "s_add_u32 m0, s59, 53248\n"
    "s_mov_b32 s74, 1032320\n"
    "buffer_load_dwordx4 v5, s[40:43], s74 offen lds\n"
    "s_add_u32 m0, s59, 57344\n"
    "s_mov_b32 s74, 1032448\n"
    "buffer_load_dwordx4 v5, s[40:43], s74 offen lds\n"
    "s_add_u32 m0, s59, 61440\n"
    "s_mov_b32 s74, 1069056\n"
    "buffer_load_dwordx4 v5, s[40:43], s74 offen lds\n"
    "s_add_u32 m0, s59, 65536\n"
    "s_mov_b32 s74, 1069184\n"
    "buffer_load_dwordx4 v5, s[40:43], s74 offen lds\n"
    "s_add_u32 m0, s59, 69632\n"
    "s_mov_b32 s74, 1069312\n"
    "buffer_load_dwordx4 v5, s[40:43], s74 offen lds\n"
    "ds_read_b64_tr_b16 a[216:217], v16 offset:24576\n"
    "ds_read_b64_tr_b16 a[218:219], v16 offset:25088\n"
    "ds_read_b64_tr_b16 a[220:221], v16 offset:24584\n"
    "ds_read_b64_tr_b16 a[222:223], v16 offset:25096\n"
    "ds_read_b64_tr_b16 a[224:225], v18 offset:24576\n"
    "ds_read_b64_tr_b16 a[226:227], v18 offset:25088\n"
    "ds_read_b64_tr_b16 a[228:229], v16 offset:36864\n"
    "ds_read_b64_tr_b16 a[230:231], v16 offset:37376\n"
    "ds_read_b64_tr_b16 a[232:233], v16 offset:36872\n"
    "ds_read_b64_tr_b16 a[234:235], v16 offset:37384\n"
    "ds_read_b64_tr_b16 a[236:237], v18 offset:36864\n"
    "ds_read_b64_tr_b16 a[238:239], v18 offset:37376\n"
    "s_waitcnt vmcnt(24) lgkmcnt(0)\n"
    "s_barrier\n"
    "s_add_u32 m0, s59, 73728\n"
    "s_mov_b32 s74, 1105920\n"
    "buffer_load_dwordx4 v5, s[40:43], s74 offen lds\n"
    "s_add_u32 m0, s59, 77824\n"
    "s_mov_b32 s74, 1106048\n"
    "buffer_load_dwordx4 v5, s[40:43], s74 offen lds\n"
    "s_add_u32 m0, s59, 81920\n"
    "s_mov_b32 s74, 1106176\n"
    "buffer_load_dwordx4 v5, s[40:43], s74 offen lds\n"
    "s_add_u32 m0, s59, 86016\n"
    "s_mov_b32 s74, 1142784\n"
    "buffer_load_dwordx4 v5, s[40:43], s74 offen lds\n"
    "s_add_u32 m0, s59, 90112\n"
    "s_mov_b32 s74, 1142912\n"
    "buffer_load_dwordx4 v5, s[40:43], s74 offen lds\n"
    "s_add_u32 m0, s59, 94208\n"
    "s_mov_b32 s74, 1143040\n"
    "buffer_load_dwordx4 v5, s[40:43], s74 offen lds\n"
    "ds_read_b64_tr_b16 a[240:241], v17 offset:0\n"
    "ds_read_b64_tr_b16 a[242:243], v17 offset:512\n"
    "ds_read_b64_tr_b16 a[244:245], v17 offset:8\n"
    "ds_read_b64_tr_b16 a[246:247], v17 offset:520\n"
    "ds_read_b64_tr_b16 a[248:249], v19 offset:0\n"
    "ds_read_b64_tr_b16 a[250:251], v19 offset:512\n"
    "ds_read_b64_tr_b16 a[252:253], v17 offset:12288\n"
    "ds_read_b64_tr_b16 a[254:255], v17 offset:12800\n"
    "ds_read_b64_tr_b16 v[80:81], v17 offset:12296\n"
    "ds_read_b64_tr_b16 v[82:83], v17 offset:12808\n"
    "ds_read_b64_tr_b16 v[84:85], v19 offset:12288\n"
    "ds_read_b64_tr_b16 v[86:87], v19 offset:12800\n"
    "s_waitcnt vmcnt(24) lgkmcnt(0)\n"
    "s_barrier\n"
    "s_add_u32 m0, s59, 98304\n"
    "s_mov_b32 s74, 1179648\n"
    "buffer_load_dwordx4 v5, s[40:43], s74 offen lds\n"
    "s_add_u32 m0, s59, 102400\n"
    "s_mov_b32 s74, 1179776\n"
    "buffer_load_dwordx4 v5, s[40:43], s74 offen lds\n"
    "s_add_u32 m0, s59, 106496\n"
    "s_mov_b32 s74, 1179904\n"
    "buffer_load_dwordx4 v5, s[40:43], s74 offen lds\n"
    "s_add_u32 m0, s59, 110592\n"
    "s_mov_b32 s74, 1216512\n"
    "buffer_load_dwordx4 v5, s[40:43], s74 offen lds\n"
    "s_add_u32 m0, s59, 114688\n"
    "s_mov_b32 s74, 1216640\n"
    "buffer_load_dwordx4 v5, s[40:43], s74 offen lds\n"
    "s_add_u32 m0, s59, 118784\n"
    "s_mov_b32 s74, 1216768\n"
    "buffer_load_dwordx4 v5, s[40:43], s74 offen lds\n"
    "ds_read_b64_tr_b16 v[88:89], v17 offset:24576\n"
    "ds_read_b64_tr_b16 v[90:91], v17 offset:25088\n"
    "ds_read_b64_tr_b16 v[92:93], v17 offset:24584\n"
    "ds_read_b64_tr_b16 v[94:95], v17 offset:25096\n"
    "ds_read_b64_tr_b16 v[96:97], v19 offset:24576\n"
    "ds_read_b64_tr_b16 v[98:99], v19 offset:25088\n"
    "ds_read_b64_tr_b16 v[100:101], v17 offset:36864\n"
    "ds_read_b64_tr_b16 v[102:103], v17 offset:37376\n"
    "ds_read_b64_tr_b16 v[104:105], v17 offset:36872\n"
    "ds_read_b64_tr_b16 v[106:107], v17 offset:37384\n"
    "ds_read_b64_tr_b16 v[108:109], v19 offset:36864\n"
    "ds_read_b64_tr_b16 v[110:111], v19 offset:37376\n"
    "s_waitcnt vmcnt(24) lgkmcnt(0)\n"
    "s_barrier\n"
    "s_add_u32 m0, s59, 122880\n"
    "s_mov_b32 s74, 1253376\n"
    "buffer_load_dwordx4 v5, s[40:43], s74 offen lds\n"
    "s_add_u32 m0, s59, 126976\n"
    "s_mov_b32 s74, 1253504\n"
    "buffer_load_dwordx4 v5, s[40:43], s74 offen lds\n"
    "s_add_u32 m0, s59, 131072\n"
    "s_mov_b32 s74, 1253632\n"
    "buffer_load_dwordx4 v5, s[40:43], s74 offen lds\n"
    "s_add_u32 m0, s59, 135168\n"
    "s_mov_b32 s74, 1290240\n"
    "buffer_load_dwordx4 v5, s[40:43], s74 offen lds\n"
    "s_add_u32 m0, s59, 139264\n"
    "s_mov_b32 s74, 1290368\n"
    "buffer_load_dwordx4 v5, s[40:43], s74 offen lds\n"
    "s_add_u32 m0, s59, 143360\n"
    "s_mov_b32 s74, 1290496\n"
    "buffer_load_dwordx4 v5, s[40:43], s74 offen lds\n"
    "buffer_load_dwordx2 v[12:13], off, s[36:39], 0 sc1\n"
    "ds_read_b64_tr_b16 v[112:113], v6 offset:0\n"
    "ds_read_b64_tr_b16 v[114:115], v6 offset:512\n"
    "ds_read_b64_tr_b16 v[116:117], v6 offset:8\n"
    "ds_read_b64_tr_b16 v[118:119], v6 offset:520\n"
    "ds_read_b64_tr_b16 v[120:121], v7 offset:0\n"
    "ds_read_b64_tr_b16 v[122:123], v7 offset:512\n"
    "ds_read_b64_tr_b16 v[124:125], v6 offset:12288\n"
    "ds_read_b64_tr_b16 v[126:127], v6 offset:12800\n"
    "ds_read_b64_tr_b16 v[128:129], v6 offset:12296\n"
    "ds_read_b64_tr_b16 v[130:131], v6 offset:12808\n"
    "ds_read_b64_tr_b16 v[132:133], v7 offset:12288\n"
    "ds_read_b64_tr_b16 v[134:135], v7 offset:12800\n"
    "s_waitcnt vmcnt(25) lgkmcnt(0)\n"
    "s_barrier\n"
    "ds_read_b64_tr_b16 v[136:137], v6 offset:24576\n"
    "ds_read_b64_tr_b16 v[138:139], v6 offset:25088\n"
    "ds_read_b64_tr_b16 v[140:141], v6 offset:24584\n"
    "ds_read_b64_tr_b16 v[142:143], v6 offset:25096\n"
    "ds_read_b64_tr_b16 v[144:145], v7 offset:24576\n"
    "ds_read_b64_tr_b16 v[146:147], v7 offset:25088\n"
    "ds_read_b64_tr_b16 v[148:149], v6 offset:36864\n"
    "ds_read_b64_tr_b16 v[150:151], v6 offset:37376\n"
    "ds_read_b64_tr_b16 v[152:153], v6 offset:36872\n"
    "ds_read_b64_tr_b16 v[154:155], v6 offset:37384\n"
    "ds_read_b64_tr_b16 v[156:157], v7 offset:36864\n"
    "ds_read_b64_tr_b16 v[158:159], v7 offset:37376\n"
    "s_waitcnt vmcnt(19) lgkmcnt(0)\n"
    "s_barrier\n"
    "ds_read_b64_tr_b16 v[160:161], v16 offset:0\n"
    "ds_read_b64_tr_b16 v[162:163], v16 offset:512\n"
    "ds_read_b64_tr_b16 v[164:165], v16 offset:8\n"
    "ds_read_b64_tr_b16 v[166:167], v16 offset:520\n"
    "ds_read_b64_tr_b16 v[168:169], v18 offset:0\n"
    "ds_read_b64_tr_b16 v[170:171], v18 offset:512\n"
    "ds_read_b64_tr_b16 v[172:173], v16 offset:12288\n"
    "ds_read_b64_tr_b16 v[174:175], v16 offset:12800\n"
    "ds_read_b64_tr_b16 v[176:177], v16 offset:12296\n"
    "ds_read_b64_tr_b16 v[178:179], v16 offset:12808\n"
    "ds_read_b64_tr_b16 v[180:181], v18 offset:12288\n"
    "ds_read_b64_tr_b16 v[182:183], v18 offset:12800\n"
    "s_waitcnt vmcnt(13) lgkmcnt(0)\n"
    "s_barrier\n"
    "ds_read_b64_tr_b16 v[184:185], v16 offset:24576\n"
    "ds_read_b64_tr_b16 v[186:187], v16 offset:25088\n"
    "ds_read_b64_tr_b16 v[188:189], v16 offset:24584\n"
    "ds_read_b64_tr_b16 v[190:191], v16 offset:25096\n"
    "ds_read_b64_tr_b16 v[192:193], v18 offset:24576\n"
    "ds_read_b64_tr_b16 v[194:195], v18 offset:25088\n"
    "ds_read_b64_tr_b16 v[196:197], v16 offset:36864\n"
    "ds_read_b64_tr_b16 v[198:199], v16 offset:37376\n"
    "ds_read_b64_tr_b16 v[200:201], v16 offset:36872\n"
    "ds_read_b64_tr_b16 v[202:203], v16 offset:37384\n"
    "ds_read_b64_tr_b16 v[204:205], v18 offset:36864\n"
    "ds_read_b64_tr_b16 v[206:207], v18 offset:37376\n"
    "s_waitcnt vmcnt(7) lgkmcnt(0)\n"
    "s_barrier\n"
    "ds_read_b64_tr_b16 v[208:209], v17 offset:0\n"
    "ds_read_b64_tr_b16 v[210:211], v17 offset:512\n"
    "ds_read_b64_tr_b16 v[212:213], v17 offset:8\n"
    "ds_read_b64_tr_b16 v[214:215], v17 offset:520\n"
    "ds_read_b64_tr_b16 v[216:217], v19 offset:0\n"
    "ds_read_b64_tr_b16 v[218:219], v19 offset:512\n"
    "ds_read_b64_tr_b16 v[220:221], v17 offset:12288\n"
    "ds_read_b64_tr_b16 v[222:223], v17 offset:12800\n"
    "ds_read_b64_tr_b16 v[224:225], v17 offset:12296\n"
    "ds_read_b64_tr_b16 v[226:227], v17 offset:12808\n"
    "ds_read_b64_tr_b16 v[228:229], v19 offset:12288\n"
    "ds_read_b64_tr_b16 v[230:231], v19 offset:12800\n"
    "s_waitcnt vmcnt(0) lgkmcnt(0)\n"
    "s_barrier\n"
    ".LgDOWN_par_%=:\n"
    "v_readfirstlane_b32 s74, v12\n"
    "v_readfirstlane_b32 s75, v13\n"
    "s_xor_b32 s74, s74, s66\n"
    "s_xor_b32 s75, s75, s67\n"
    "s_cselect_b32 s75, 2, 0\n"
    "s_or_b32 s75, s75, s74\n"
    "s_cmp_lt_u32 s75, 2\n"
    "s_cbranch_scc1 .LgDOWN_par1_%=\n"
    "s_sleep 4\n"
    "buffer_load_dwordx2 v[12:13], off, s[36:39], 0 sc1\n"
    "s_waitcnt vmcnt(0)\n"
    "s_sub_u32 s72, s72, 1\n"
    "s_cbranch_scc0 .LgDOWN_par_%=\n"
    "s_mov_b32 s72, 0\n"
    ".LgDOWN_par1_%=:\n"
    "s_and_b32 s74, s74, 1\n"
    "s_lshl_b32 s74, s74, 13\n"
    "s_add_u32 s36, s36, s74\n"
    "s_addc_u32 s37, s37, 0\n"
    "v_add_u32 v13, v11, v3\n"
    "v_cmp_gt_i32 vcc, s71, v13\n"
    "s_nop 0\n"
    "s_or_b64 exec, vcc, 1\n"
    "buffer_atomic_smax v11, v2, s[36:39], 0 offen sc0 sc1\n"
    "s_mov_b64 exec, -1\n"
    "ds_read_b64_tr_b16 v[232:233], v17 offset:24576\n"
    "ds_read_b64_tr_b16 v[234:235], v17 offset:25088\n"
    "ds_read_b64_tr_b16 v[236:237], v17 offset:24584\n"
    "ds_read_b64_tr_b16 v[238:239], v17 offset:25096\n"
    "ds_read_b64_tr_b16 v[240:241], v19 offset:24576\n"
    "ds_read_b64_tr_b16 v[242:243], v19 offset:25088\n"
    "ds_read_b64_tr_b16 v[244:245], v17 offset:36864\n"
    "ds_read_b64_tr_b16 v[246:247], v17 offset:37376\n"
    "ds_read_b64_tr_b16 v[248:249], v17 offset:36872\n"
    "ds_read_b64_tr_b16 v[250:251], v17 offset:37384\n"
    "ds_read_b64_tr_b16 v[252:253], v19 offset:36864\n"
    "ds_read_b64_tr_b16 v[254:255], v19 offset:37376\n"
    "s_waitcnt lgkmcnt(0)\n"
    "s_barrier\n"
    "s_waitcnt vmcnt(0)\n"
    "s_branch .LgDOWN_block1_%=\n"
    ".LgDOWN_LOOPB_%=:\n"
    ".LgDOWN_head_%=:\n"
    "v_mfma_f32_16x16x32_bf16 v[64:67], a[8:11], v[16:19], 0\n"
    "s_waitcnt lgkmcnt(6)\n"
    "ds_read_b128 v[16:19], v9 offset:0\n"
    "v_mfma_f32_16x16x32_bf16 v[68:71], a[8:11], v[20:23], 0\n"
    "s_waitcnt lgkmcnt(6)\n"
    "ds_read_b128 v[20:23], v9 offset:2048\n"
    "v_mfma_f32_16x16x32_bf16 v[64:67], a[20:23], v[24:27], v[64:67]\n"
    "s_waitcnt lgkmcnt(6)\n"
    "ds_read_b128 v[24:27], v9 offset:4096\n"
    "v_mfma_f32_16x16x32_bf16 v[68:71], a[20:23], v[28:31], v[68:71]\n"
    "s_waitcnt lgkmcnt(6)\n"
    "ds_read_b128 v[28:31], v9 offset:6144\n"
    "v_mfma_f32_16x16x32_bf16 v[64:67], a[32:35], v[32:35], v[64:67]\n"
    "s_waitcnt lgkmcnt(6)\n"
    "ds_read_b128 v[32:35], v9 offset:1024\n"
    "v_mfma_f32_16x16x32_bf16 v[68:71], a[32:35], v[36:39], v[68:71]\n"
    "s_waitcnt lgkmcnt(6)\n"
    "ds_read_b128 v[36:39], v9 offset:3072\n"
    "v_mfma_f32_16x16x32_bf16 v[64:67], a[44:47], v[40:43], v[64:67]\n"
    "s_waitcnt lgkmcnt(6)\n"
    "ds_read_b128 v[40:43], v9 offset:5120\n"
    "v_mfma_f32_16x16x32_bf16 v[68:71], a[44:47], v[44:47], v[68:71]\n"
    "s_waitcnt lgkmcnt(6)\n"
    "ds_read_b128 v[44:47], v9 offset:7168\n"
    "v_mfma_f32_16x16x32_bf16 v[48:51], a[0:3], v[16:19], 0\n"
    "s_waitcnt lgkmcnt(6)\n"
    "v_cvt_pk_bf16_f32 v56, v56, v57\n"
    "v_cvt_pk_bf16_f32 v57, v58, v59\n"
    "v_mfma_f32_16x16x32_bf16 v[52:55], a[4:7], v[16:19], 0\n"
    "ds_read_b128 v[16:19], v9 offset:8192\n"
    "v_cvt_pk_bf16_f32 v58, v60, v61\n"
    "v_mfma_f32_16x16x32_bf16 v[48:51], a[12:15], v[20:23], v[48:51]\n"
    "s_waitcnt lgkmcnt(6)\n"
    "v_cvt_pk_bf16_f32 v59, v62, v63\n"
    "v_mfma_f32_16x16x32_bf16 v[52:55], a[16:19], v[20:23], v[52:55]\n"
    "ds_read_b128 v[20:23], v9 offset:9216\n"
    "v_mfma_f32_16x16x32_bf16 v[48:51], a[24:27], v[24:27], v[48:51]\n"
    "s_waitcnt lgkmcnt(6)\n"
    "s_sub_u32 s74, s68, 1\n"
    "buffer_store_dwordx4 v[56:59], v15, s[36:39], s73 offen offset:1024 sc1\n"
    "v_mfma_f32_16x16x32_bf16 v[52:55], a[28:31], v[24:27], v[52:55]\n"
    "ds_read_b128 v[24:27], v9 offset:10240\n"
    "v_mfma_f32_16x16x32_bf16 v[48:51], a[36:39], v[28:31], v[48:51]\n"
    "s_waitcnt lgkmcnt(6)\n"
    "s_and_b32 s76, s71, s54\n"
    "v_mfma_f32_16x16x32_bf16 v[52:55], a[40:43], v[28:31], v[52:55]\n"
    "ds_read_b128 v[28:31], v9 offset:11264\n"
    "v_mfma_f32_16x16x32_bf16 v[56:59], a[0:3], v[32:35], 0\n"
    "s_waitcnt lgkmcnt(6)\n"
    "s_mul_i32 s76, s76, s53\n"
    "v_mfma_f32_16x16x32_bf16 v[60:63], a[4:7], v[32:35], 0\n"
    "ds_read_b128 v[32:35], v9 offset:12288\n"
    "v_mfma_f32_16x16x32_bf16 v[56:59], a[12:15], v[36:39], v[56:59]\n"
    "s_waitcnt lgkmcnt(6)\n"
    "s_add_u32 s76, s76, s52\n"
    "v_mfma_f32_16x16x32_bf16 v[60:63], a[16:19], v[36:39], v[60:63]\n"
    "ds_read_b128 v[36:39], v9 offset:13312\n"
    "v_mfma_f32_16x16x32_bf16 v[56:59], a[24:27], v[40:43], v[56:59]\n"
    "s_waitcnt lgkmcnt(6)\n"
    "s_and_b32 s77, s71, 1\n"
    "v_mfma_f32_16x16x32_bf16 v[60:63], a[28:31], v[40:43], v[60:63]\n"
    "ds_read_b128 v[40:43], v9 offset:14336\n"
    "v_mfma_f32_16x16x32_bf16 v[56:59], a[36:39], v[44:47], v[56:59]\n"
    "s_waitcnt lgkmcnt(6)\n"
    "s_mul_i32 s77, s77, 81920\n"
    "v_mfma_f32_16x16x32_bf16 v[60:63], a[40:43], v[44:47], v[60:63]\n"
    "ds_read_b128 v[44:47], v9 offset:15360\n"
    "v_mfma_f32_16x16x32_bf16 v[48:51], a[48:51], v[16:19], v[48:51]\n"
    "s_add_u32 s77, s77, s59\n"
    "v_mfma_f32_16x16x32_bf16 v[52:55], a[52:55], v[16:19], v[52:55]\n"
    "s_waitcnt lgkmcnt(6)\n"
    "v_mfma_f32_16x16x32_bf16 v[64:67], a[56:59], v[16:19], v[64:67]\n"
    "ds_read_b128 v[16:19], v9 offset:16384\n"
    "v_mfma_f32_16x16x32_bf16 v[56:59], a[48:51], v[20:23], v[56:59]\n"
    "v_mfma_f32_16x16x32_bf16 v[60:63], a[52:55], v[20:23], v[60:63]\n"
    "s_waitcnt lgkmcnt(6)\n"
    "v_mfma_f32_16x16x32_bf16 v[68:71], a[56:59], v[20:23], v[68:71]\n"
    "ds_read_b128 v[20:23], v9 offset:17408\n"
    "v_mfma_f32_16x16x32_bf16 v[48:51], a[60:63], v[24:27], v[48:51]\n"
    "v_mfma_f32_16x16x32_bf16 v[52:55], a[64:67], v[24:27], v[52:55]\n"
    "s_waitcnt lgkmcnt(6)\n"
    "v_mfma_f32_16x16x32_bf16 v[64:67], a[68:71], v[24:27], v[64:67]\n"
    "ds_read_b128 v[24:27], v9 offset:18432\n"
    "v_mfma_f32_16x16x32_bf16 v[56:59], a[60:63], v[28:31], v[56:59]\n"
    "v_mfma_f32_16x16x32_bf16 v[60:63], a[64:67], v[28:31], v[60:63]\n"
    "s_waitcnt lgkmcnt(6)\n"
    "v_mfma_f32_16x16x32_bf16 v[68:71], a[68:71], v[28:31], v[68:71]\n"
    "ds_read_b128 v[28:31], v9 offset:19456\n"
    "v_mfma_f32_16x16x32_bf16 v[48:51], a[72:75], v[32:35], v[48:51]\n"
    "v_mfma_f32_16x16x32_bf16 v[52:55], a[76:79], v[32:35], v[52:55]\n"
    "s_waitcnt lgkmcnt(6)\n"
    "v_mfma_f32_16x16x32_bf16 v[64:67], a[80:83], v[32:35], v[64:67]\n"
    "ds_read_b128 v[32:35], v9 offset:20480\n"
    "v_mfma_f32_16x16x32_bf16 v[56:59], a[72:75], v[36:39], v[56:59]\n"
    "v_mfma_f32_16x16x32_bf16 v[60:63], a[76:79], v[36:39], v[60:63]\n"
    "s_waitcnt lgkmcnt(6)\n"
    "v_mfma_f32_16x16x32_bf16 v[68:71], a[80:83], v[36:39], v[68:71]\n"
    "ds_read_b128 v[36:39], v9 offset:21504\n"
    "v_mfma_f32_16x16x32_bf16 v[48:51], a[84:87], v[40:43], v[48:51]\n"
    "v_mfma_f32_16x16x32_bf16 v[52:55], a[88:91], v[40:43], v[52:55]\n"
    "s_waitcnt lgkmcnt(6)\n"
    "v_mfma_f32_16x16x32_bf16 v[64:67], a[92:95], v[40:43], v[64:67]\n"
    "ds_read_b128 v[40:43], v9 offset:22528\n"
    "v_mfma_f32_16x16x32_bf16 v[56:59], a[84:87], v[44:47], v[56:59]\n"
    "v_mfma_f32_16x16x32_bf16 v[60:63], a[88:91], v[44:47], v[60:63]\n"
    "s_waitcnt lgkmcnt(6)\n"
    "v_mfma_f32_16x16x32_bf16 v[68:71], a[92:95], v[44:47], v[68:71]\n"
    "ds_read_b128 v[44:47], v9 offset:23552\n"
    "v_mfma_f32_16x16x32_bf16 v[48:51], a[96:99], v[16:19], v[48:51]\n"
    "v_mov_b32 v12, s74\n"
    "v_mfma_f32_16x16x32_bf16 v[52:55], a[100:103], v[16:19], v[52:55]\n"
    "s_waitcnt lgkmcnt(6)\n"
    "s_waitcnt vmcnt(0)\n"
    "v_add_u32 v13, v11, v3\n"
    "v_mfma_f32_16x16x32_bf16 v[64:67], a[104:107], v[16:19], v[64:67]\n"
    "ds_read_b128 v[16:19], v9 offset:24576\n"
    "buffer_atomic_smax v12, v14, s[36:39], 0 offen sc1\n"
    "v_cmp_gt_i32 vcc, s71, v13\n"
    "v_mfma_f32_16x16x32_bf16 v[56:59], a[96:99], v[20:23], v[56:59]\n"
    "v_add_u32 v10, 49152, v9\n"
    "v_mfma_f32_16x16x32_bf16 v[60:63], a[100:103], v[20:23], v[60:63]\n"
    "s_waitcnt lgkmcnt(6)\n"
    "v_mfma_f32_16x16x32_bf16 v[68:71], a[104:107], v[20:23], v[68:71]\n"
    "ds_read_b128 v[20:23], v9 offset:25600\n"
    ".LgDOWN_mid_%=:\n"
    "s_cbranch_vccnz .LgDOWN_chk0_%=\n"
    "v_mfma_f32_16x16x32_bf16 v[48:51], a[108:111], v[24:27], v[48:51]\n"
    "s_add_u32 m0, s77, s59\n"
    "s_add_u32 s77, s77, 8192\n"
    "buffer_load_dwordx4 v1, s[36:39], s76 offen sc1 lds\n"
    "v_mfma_f32_16x16x32_bf16 v[52:55], a[112:115], v[24:27], v[52:55]\n"
    "s_waitcnt lgkmcnt(6)\n"
    "buffer_load_dwordx4 v1, s[36:39], s76 offen offset:1024 sc1 lds\n"
    "s_add_u32 s76, s76, 16384\n"
    "v_mfma_f32_16x16x32_bf16 v[64:67], a[116:119], v[24:27], v[64:67]\n"
    "ds_read_b128 v[24:27], v9 offset:26624\n"
    "v_mfma_f32_16x16x32_bf16 v[56:59], a[108:111], v[28:31], v[56:59]\n"
    "s_mov_b32 s70, 1\n"
    "v_mfma_f32_16x16x32_bf16 v[60:63], a[112:115], v[28:31], v[60:63]\n"
    "s_waitcnt lgkmcnt(6)\n"
    "s_add_u32 m0, s77, s59\n"
    "s_add_u32 s77, s77, 8192\n"
    "buffer_load_dwordx4 v1, s[36:39], s76 offen sc1 lds\n"
    "v_mfma_f32_16x16x32_bf16 v[68:71], a[116:119], v[28:31], v[68:71]\n"
    "ds_read_b128 v[28:31], v9 offset:27648\n"
    "v_mfma_f32_16x16x32_bf16 v[48:51], a[120:123], v[32:35], v[48:51]\n"
    "buffer_load_dwordx4 v1, s[36:39], s76 offen offset:1024 sc1 lds\n"
    "s_add_u32 s76, s76, 16384\n"
    "v_mfma_f32_16x16x32_bf16 v[52:55], a[124:127], v[32:35], v[52:55]\n"
    "s_waitcnt lgkmcnt(6)\n"
    "s_and_b32 s73, s68, s57\n"
    "v_mfma_f32_16x16x32_bf16 v[64:67], a[128:131], v[32:35], v[64:67]\n"
    "ds_read_b128 v[32:35], v9 offset:28672\n"
    "v_mfma_f32_16x16x32_bf16 v[56:59], a[120:123], v[36:39], v[56:59]\n"
    "s_add_u32 m0, s77, s59\n"
    "s_add_u32 s77, s77, 8192\n"
    "buffer_load_dwordx4 v1, s[36:39], s76 offen sc1 lds\n"
    "v_mfma_f32_16x16x32_bf16 v[60:63], a[124:127], v[36:39], v[60:63]\n"
    "s_waitcnt lgkmcnt(6)\n"
    "buffer_load_dwordx4 v1, s[36:39], s76 offen offset:1024 sc1 lds\n"
    "s_add_u32 s76, s76, 16384\n"
    "v_mfma_f32_16x16x32_bf16 v[68:71], a[128:131], v[36:39], v[68:71]\n"
    "ds_read_b128 v[36:39], v9 offset:29696\n"
    "v_mfma_f32_16x16x32_bf16 v[48:51], a[132:135], v[40:43], v[48:51]\n"
    "s_mul_i32 s73, s73, s56\n"
    "v_mfma_f32_16x16x32_bf16 v[52:55], a[136:139], v[40:43], v[52:55]\n"
    "s_waitcnt lgkmcnt(6)\n"
    "s_add_u32 m0, s77, s59\n"
    "s_add_u32 s77, s77, 8192\n"
    "buffer_load_dwordx4 v1, s[36:39], s76 offen sc1 lds\n"
    "v_mfma_f32_16x16x32_bf16 v[64:67], a[140:143], v[40:43], v[64:67]\n"
    "ds_read_b128 v[40:43], v9 offset:30720\n"
    "v_mfma_f32_16x16x32_bf16 v[56:59], a[132:135], v[44:47], v[56:59]\n"
    "buffer_load_dwordx4 v1, s[36:39], s76 offen offset:1024 sc1 lds\n"
    "s_add_u32 s76, s76, 16384\n"
    "v_mfma_f32_16x16x32_bf16 v[60:63], a[136:139], v[44:47], v[60:63]\n"
    "s_waitcnt lgkmcnt(6)\n"
    "s_add_u32 s73, s73, s55\n"
    "v_mfma_f32_16x16x32_bf16 v[68:71], a[140:143], v[44:47], v[68:71]\n"
    "ds_read_b128 v[44:47], v9 offset:31744\n"
    "v_mfma_f32_16x16x32_bf16 v[48:51], a[144:147], v[16:19], v[48:51]\n"
    "s_add_u32 m0, s77, s59\n"
    "s_add_u32 s77, s77, 8192\n"
    "buffer_load_dwordx4 v1, s[36:39], s76 offen sc1 lds\n"
    "v_mfma_f32_16x16x32_bf16 v[52:55], a[148:151], v[16:19], v[52:55]\n"
    "s_waitcnt lgkmcnt(6)\n"
    "v_mfma_f32_16x16x32_bf16 v[64:67], a[152:155], v[16:19], v[64:67]\n"
    "ds_read_b128 v[16:19], v9 offset:32768\n"
    "v_mfma_f32_16x16x32_bf16 v[56:59], a[144:147], v[20:23], v[56:59]\n"
    "buffer_load_dwordx4 v1, s[36:39], s76 offen offset:1024 sc1 lds\n"
    "s_add_u32 s76, s76, 16384\n"
    "v_mfma_f32_16x16x32_bf16 v[60:63], a[148:151], v[20:23], v[60:63]\n"
    "s_waitcnt lgkmcnt(6)\n"
    "s_add_u32 m0, s77, s59\n"
    "s_add_u32 s77, s77, 8192\n"
    "buffer_load_dwordx4 v1, s[36:39], s76 offen sc1 lds\n"
    "v_mfma_f32_16x16x32_bf16 v[68:71], a[152:155], v[20:23], v[68:71]\n"
    "ds_read_b128 v[20:23], v9 offset:33792\n"
    "v_mfma_f32_16x16x32_bf16 v[48:51], a[156:159], v[24:27], v[48:51]\n"
    "v_mfma_f32_16x16x32_bf16 v[52:55], a[160:163], v[24:27], v[52:55]\n"
    "s_waitcnt lgkmcnt(6)\n"
    "buffer_load_dwordx4 v1, s[36:39], s76 offen offset:1024 sc1 lds\n"
    "s_add_u32 s76, s76, 16384\n"
    "v_mfma_f32_16x16x32_bf16 v[64:67], a[164:167], v[24:27], v[64:67]\n"
    "ds_read_b128 v[24:27], v9 offset:34816\n"
    "v_mfma_f32_16x16x32_bf16 v[56:59], a[156:159], v[28:31], v[56:59]\n"
    "s_add_u32 m0, s77, s59\n"
    "s_add_u32 s77, s77, 8192\n"
    "buffer_load_dwordx4 v1, s[36:39], s76 offen sc1 lds\n"
    "v_mfma_f32_16x16x32_bf16 v[60:63], a[160:163], v[28:31], v[60:63]\n"
    "s_waitcnt lgkmcnt(6)\n"
    "v_mfma_f32_16x16x32_bf16 v[68:71], a[164:167], v[28:31], v[68:71]\n"
    "ds_read_b128 v[28:31], v9 offset:35840\n"
    "v_mfma_f32_16x16x32_bf16 v[48:51], a[168:171], v[32:35], v[48:51]\n"
    "buffer_load_dwordx4 v1, s[36:39], s76 offen offset:1024 sc1 lds\n"
    "s_add_u32 s76, s76, 16384\n"
    "v_mfma_f32_16x16x32_bf16 v[52:55], a[172:175], v[32:35], v[52:55]\n"
    "s_waitcnt lgkmcnt(6)\n"
    "s_add_u32 m0, s77, s59\n"
    "s_add_u32 s77, s77, 8192\n"
    "buffer_load_dwordx4 v1, s[36:39], s76 offen sc1 lds\n"
    "v_mfma_f32_16x16x32_bf16 v[64:67], a[176:179], v[32:35], v[64:67]\n"
    "ds_read_b128 v[32:35], v9 offset:36864\n"
    "v_mfma_f32_16x16x32_bf16 v[56:59], a[168:171], v[36:39], v[56:59]\n"
    "v_mfma_f32_16x16x32_bf16 v[60:63], a[172:175], v[36:39], v[60:63]\n"
    "s_waitcnt lgkmcnt(6)\n"
    "buffer_load_dwordx4 v1, s[36:39], s76 offen offset:1024 sc1 lds\n"
    "s_add_u32 s76, s76, 16384\n"
    "v_mfma_f32_16x16x32_bf16 v[68:71], a[176:179], v[36:39], v[68:71]\n"
    "ds_read_b128 v[36:39], v9 offset:37888\n"
    "v_mfma_f32_16x16x32_bf16 v[48:51], a[180:183], v[40:43], v[48:51]\n"
    "s_add_u32 m0, s77, s59\n"
    "s_add_u32 s77, s77, 8192\n"
    "buffer_load_dwordx4 v1, s[36:39], s76 offen sc1 lds\n"
    "v_mfma_f32_16x16x32_bf16 v[52:55], a[184:187], v[40:43], v[52:55]\n"
    "s_waitcnt lgkmcnt(6)\n"
    "v_mfma_f32_16x16x32_bf16 v[64:67], a[188:191], v[40:43], v[64:67]\n"
    "ds_read_b128 v[40:43], v9 offset:38912\n"
    "v_mfma_f32_16x16x32_bf16 v[56:59], a[180:183], v[44:47], v[56:59]\n"
    "buffer_load_dwordx4 v1, s[36:39], s76 offen offset:1024 sc1 lds\n"
    "s_add_u32 s76, s76, 16384\n"
    "v_mfma_f32_16x16x32_bf16 v[60:63], a[184:187], v[44:47], v[60:63]\n"
    "s_waitcnt lgkmcnt(6)\n"
    "v_mfma_f32_16x16x32_bf16 v[68:71], a[188:191], v[44:47], v[68:71]\n"
    "ds_read_b128 v[44:47], v9 offset:39936\n"
    "v_mfma_f32_16x16x32_bf16 v[48:51], a[192:195], v[16:19], v[48:51]\n"
    ".LgDOWN_seg0_%=:\n"
    "s_cmp_lg_u32 s70, 0\n"
    "s_cbranch_scc0 .LgDOWN_chk1_%=\n"
    ".LgDOWN_plain1_%=:\n"
    "v_mfma_f32_16x16x32_bf16 v[52:55], a[196:199], v[16:19], v[52:55]\n"
    "s_waitcnt lgkmcnt(6)\n"
    "v_mfma_f32_16x16x32_bf16 v[64:67], a[200:203], v[16:19], v[64:67]\n"
    "ds_read_b128 v[16:19], v9 offset:40960\n"
    "v_mfma_f32_16x16x32_bf16 v[56:59], a[192:195], v[20:23], v[56:59]\n"
    "v_mfma_f32_16x16x32_bf16 v[60:63], a[196:199], v[20:23], v[60:63]\n"
    "s_waitcnt lgkmcnt(6)\n"
    "v_mfma_f32_16x16x32_bf16 v[68:71], a[200:203], v[20:23], v[68:71]\n"
    "ds_read_b128 v[20:23], v9 offset:41984\n"
    "v_mfma_f32_16x16x32_bf16 v[48:51], a[204:207], v[24:27], v[48:51]\n"
    "v_mfma_f32_16x16x32_bf16 v[52:55], a[208:211], v[24:27], v[52:55]\n"
    "s_waitcnt lgkmcnt(6)\n"
    "v_mfma_f32_16x16x32_bf16 v[64:67], a[212:215], v[24:27], v[64:67]\n"
    "ds_read_b128 v[24:27], v9 offset:43008\n"
    "v_mfma_f32_16x16x32_bf16 v[56:59], a[204:207], v[28:31], v[56:59]\n"
    "v_mfma_f32_16x16x32_bf16 v[60:63], a[208:211], v[28:31], v[60:63]\n"
    "s_waitcnt lgkmcnt(6)\n"
    "v_mfma_f32_16x16x32_bf16 v[68:71], a[212:215], v[28:31], v[68:71]\n"
    "ds_read_b128 v[28:31], v9 offset:44032\n"
    "v_mfma_f32_16x16x32_bf16 v[48:51], a[216:219], v[32:35], v[48:51]\n"
    "v_mfma_f32_16x16x32_bf16 v[52:55], a[220:223], v[32:35], v[52:55]\n"
    "s_waitcnt lgkmcnt(6)\n"
    "v_mfma_f32_16x16x32_bf16 v[64:67], a[224:227], v[32:35], v[64:67]\n"
    "ds_read_b128 v[32:35], v9 offset:45056\n"
    "v_mfma_f32_16x16x32_bf16 v[56:59], a[216:219], v[36:39], v[56:59]\n"
    "v_mfma_f32_16x16x32_bf16 v[60:63], a[220:223], v[36:39], v[60:63]\n"
    "s_waitcnt lgkmcnt(6)\n"
    "v_mfma_f32_16x16x32_bf16 v[68:71], a[224:227], v[36:39], v[68:71]\n"
    "ds_read_b128 v[36:39], v9 offset:46080\n"
    "v_mfma_f32_16x16x32_bf16 v[48:51], a[228:231], v[40:43], v[48:51]\n"
    "v_mfma_f32_16x16x32_bf16 v[52:55], a[232:235], v[40:43], v[52:55]\n"
    "s_waitcnt lgkmcnt(6)\n"
    "v_mfma_f32_16x16x32_bf16 v[64:67], a[236:239], v[40:43], v[64:67]\n"
    "ds_read_b128 v[40:43], v9 offset:47104\n"
    "v_mfma_f32_16x16x32_bf16 v[56:59], a[228:231], v[44:47], v[56:59]\n"
    "v_mfma_f32_16x16x32_bf16 v[60:63], a[232:235], v[44:47], v[60:63]\n"
    "s_waitcnt lgkmcnt(6)\n"
    "v_mfma_f32_16x16x32_bf16 v[68:71], a[236:239], v[44:47], v[68:71]\n"
    "ds_read_b128 v[44:47], v9 offset:48128\n"
    "v_mfma_f32_16x16x32_bf16 v[48:51], a[240:243], v[16:19], v[48:51]\n"
    "v_mfma_f32_16x16x32_bf16 v[52:55], a[244:247], v[16:19], v[52:55]\n"
    "s_waitcnt lgkmcnt(6)\n"
    "v_mfma_f32_16x16x32_bf16 v[64:67], a[248:251], v[16:19], v[64:67]\n"
    "ds_read_b128 v[16:19], v10 offset:0\n"
    "v_mfma_f32_16x16x32_bf16 v[56:59], a[240:243], v[20:23], v[56:59]\n"
    "v_mfma_f32_16x16x32_bf16 v[60:63], a[244:247], v[20:23], v[60:63]\n"
    "s_waitcnt lgkmcnt(6)\n"
    "v_mfma_f32_16x16x32_bf16 v[68:71], a[248:251], v[20:23], v[68:71]\n"
    "ds_read_b128 v[20:23], v10 offset:1024\n"
    "v_mfma_f32_16x16x32_bf16 v[48:51], a[252:255], v[24:27], v[48:51]\n"
    "v_mfma_f32_16x16x32_bf16 v[52:55], v[80:83], v[24:27], v[52:55]\n"
    "s_waitcnt lgkmcnt(6)\n"
    "v_mfma_f32_16x16x32_bf16 v[64:67], v[84:87], v[24:27], v[64:67]\n"
    "ds_read_b128 v[24:27], v10 offset:2048\n"
    "v_mfma_f32_16x16x32_bf16 v[56:59], a[252:255], v[28:31], v[56:59]\n"
    "v_mfma_f32_16x16x32_bf16 v[60:63], v[80:83], v[28:31], v[60:63]\n"
    "s_waitcnt lgkmcnt(6)\n"
    "v_mfma_f32_16x16x32_bf16 v[68:71], v[84:87], v[28:31], v[68:71]\n"
    "ds_read_b128 v[28:31], v10 offset:3072\n"
    "v_mfma_f32_16x16x32_bf16 v[48:51], v[88:91], v[32:35], v[48:51]\n"
    "v_mfma_f32_16x16x32_bf16 v[52:55], v[92:95], v[32:35], v[52:55]\n"
    "s_waitcnt lgkmcnt(6)\n"
    "v_mfma_f32_16x16x32_bf16 v[64:67], v[96:99], v[32:35], v[64:67]\n"
    "ds_read_b128 v[32:35], v10 offset:4096\n"
    "v_mfma_f32_16x16x32_bf16 v[56:59], v[88:91], v[36:39], v[56:59]\n"
    "v_mfma_f32_16x16x32_bf16 v[60:63], v[92:95], v[36:39], v[60:63]\n"
    "s_waitcnt lgkmcnt(6)\n"
    "v_mfma_f32_16x16x32_bf16 v[68:71], v[96:99], v[36:39], v[68:71]\n"
    "ds_read_b128 v[36:39], v10 offset:5120\n"
    "v_mfma_f32_16x16x32_bf16 v[48:51], v[100:103], v[40:43], v[48:51]\n"
    "v_mfma_f32_16x16x32_bf16 v[52:55], v[104:107], v[40:43], v[52:55]\n"
    "s_waitcnt lgkmcnt(6)\n"
    ".LgDOWN_seg1_%=:\n"
    "s_cmp_lg_u32 s70, 0\n"
    "s_cbranch_scc0 .LgDOWN_chk2_%=\n"
    ".LgDOWN_plain2_%=:\n"
    "v_mfma_f32_16x16x32_bf16 v[64:67], v[108:111], v[40:43], v[64:67]\n"
    "ds_read_b128 v[40:43], v10 offset:6144\n"
    "v_mfma_f32_16x16x32_bf16 v[56:59], v[100:103], v[44:47], v[56:59]\n"
    "v_mfma_f32_16x16x32_bf16 v[60:63], v[104:107], v[44:47], v[60:63]\n"
    "s_waitcnt lgkmcnt(6)\n"
    "v_mfma_f32_16x16x32_bf16 v[68:71], v[108:111], v[44:47], v[68:71]\n"
    "ds_read_b128 v[44:47], v10 offset:7168\n"
    "v_mfma_f32_16x16x32_bf16 v[48:51], v[112:115], v[16:19], v[48:51]\n"
    "v_mfma_f32_16x16x32_bf16 v[52:55], v[116:119], v[16:19], v[52:55]\n"
    "s_waitcnt lgkmcnt(6)\n"
    "v_mfma_f32_16x16x32_bf16 v[64:67], v[120:123], v[16:19], v[64:67]\n"
    "ds_read_b128 v[16:19], v10 offset:8192\n"
    "v_mfma_f32_16x16x32_bf16 v[56:59], v[112:115], v[20:23], v[56:59]\n"
    "v_mfma_f32_16x16x32_bf16 v[60:63], v[116:119], v[20:23], v[60:63]\n"
    "s_waitcnt lgkmcnt(6)\n"
    "v_mfma_f32_16x16x32_bf16 v[68:71], v[120:123], v[20:23], v[68:71]\n"
    "ds_read_b128 v[20:23], v10 offset:9216\n"
    "v_mfma_f32_16x16x32_bf16 v[48:51], v[124:127], v[24:27], v[48:51]\n"
    "v_mfma_f32_16x16x32_bf16 v[52:55], v[128:131], v[24:27], v[52:55]\n"
    "s_waitcnt lgkmcnt(6)\n"
    "v_mfma_f32_16x16x32_bf16 v[64:67], v[132:135], v[24:27], v[64:67]\n"
    "ds_read_b128 v[24:27], v10 offset:10240\n"
    "v_mfma_f32_16x16x32_bf16 v[56:59], v[124:127], v[28:31], v[56:59]\n"
    "v_mfma_f32_16x16x32_bf16 v[60:63], v[128:131], v[28:31], v[60:63]\n"
    "s_waitcnt lgkmcnt(6)\n"
    "v_mfma_f32_16x16x32_bf16 v[68:71], v[132:135], v[28:31], v[68:71]\n"
    "ds_read_b128 v[28:31], v10 offset:11264\n"
    "v_mfma_f32_16x16x32_bf16 v[48:51], v[136:139], v[32:35], v[48:51]\n"
    "v_mfma_f32_16x16x32_bf16 v[52:55], v[140:143], v[32:35], v[52:55]\n"
    "s_waitcnt lgkmcnt(6)\n"
    "v_mfma_f32_16x16x32_bf16 v[64:67], v[144:147], v[32:35], v[64:67]\n"
    "ds_read_b128 v[32:35], v10 offset:12288\n"
    "v_mfma_f32_16x16x32_bf16 v[56:59], v[136:139], v[36:39], v[56:59]\n"
    "v_mfma_f32_16x16x32_bf16 v[60:63], v[140:143], v[36:39], v[60:63]\n"
    "s_waitcnt lgkmcnt(6)\n"
    "v_mfma_f32_16x16x32_bf16 v[68:71], v[144:147], v[36:39], v[68:71]\n"
    "ds_read_b128 v[36:39], v10 offset:13312\n"
    "v_mfma_f32_16x16x32_bf16 v[48:51], v[148:151], v[40:43], v[48:51]\n"
    "v_mfma_f32_16x16x32_bf16 v[52:55], v[152:155], v[40:43], v[52:55]\n"
    "s_waitcnt lgkmcnt(6)\n"
    "v_mfma_f32_16x16x32_bf16 v[64:67], v[156:159], v[40:43], v[64:67]\n"
    "ds_read_b128 v[40:43], v10 offset:14336\n"
    "v_mfma_f32_16x16x32_bf16 v[56:59], v[148:151], v[44:47], v[56:59]\n"
    "v_mfma_f32_16x16x32_bf16 v[60:63], v[152:155], v[44:47], v[60:63]\n"
    "s_waitcnt lgkmcnt(6)\n"
    "v_mfma_f32_16x16x32_bf16 v[68:71], v[156:159], v[44:47], v[68:71]\n"
    "ds_read_b128 v[44:47], v10 offset:15360\n"
    "v_mfma_f32_16x16x32_bf16 v[48:51], v[160:163], v[16:19], v[48:51]\n"
    "v_mfma_f32_16x16x32_bf16 v[52:55], v[164:167], v[16:19], v[52:55]\n"
    "s_waitcnt lgkmcnt(6)\n"
    "v_mfma_f32_16x16x32_bf16 v[64:67], v[168:171], v[16:19], v[64:67]\n"
    "ds_read_b128 v[16:19], v10 offset:16384\n"
    "v_mfma_f32_16x16x32_bf16 v[56:59], v[160:163], v[20:23], v[56:59]\n"
    "v_mfma_f32_16x16x32_bf16 v[60:63], v[164:167], v[20:23], v[60:63]\n"
    "s_waitcnt lgkmcnt(6)\n"
    "v_mfma_f32_16x16x32_bf16 v[68:71], v[168:171], v[20:23], v[68:71]\n"
    "ds_read_b128 v[20:23], v10 offset:17408\n"
    "v_mfma_f32_16x16x32_bf16 v[48:51], v[172:175], v[24:27], v[48:51]\n"
    "v_mfma_f32_16x16x32_bf16 v[52:55], v[176:179], v[24:27], v[52:55]\n"
    "s_waitcnt lgkmcnt(6)\n"
    "v_mfma_f32_16x16x32_bf16 v[64:67], v[180:183], v[24:27], v[64:67]\n"
    "ds_read_b128 v[24:27], v10 offset:18432\n"
    "v_mfma_f32_16x16x32_bf16 v[56:59], v[172:175], v[28:31], v[56:59]\n"
    "v_mfma_f32_16x16x32_bf16 v[60:63], v[176:179], v[28:31], v[60:63]\n"
    "s_waitcnt lgkmcnt(6)\n"
    "v_mfma_f32_16x16x32_bf16 v[68:71], v[180:183], v[28:31], v[68:71]\n"
    "ds_read_b128 v[28:31], v10 offset:19456\n"
    "v_mfma_f32_16x16x32_bf16 v[48:51], v[184:187], v[32:35], v[48:51]\n"
    "v_mfma_f32_16x16x32_bf16 v[52:55], v[188:191], v[32:35], v[52:55]\n"
    "s_waitcnt lgkmcnt(6)\n"
    "v_mfma_f32_16x16x32_bf16 v[64:67], v[192:195], v[32:35], v[64:67]\n"
    "ds_read_b128 v[32:35], v10 offset:20480\n"
    "v_mfma_f32_16x16x32_bf16 v[56:59], v[184:187], v[36:39], v[56:59]\n"
    ".LgDOWN_seg2_%=:\n"
    "s_cmp_lg_u32 s70, 1\n"
    "s_cbranch_scc1 .LgDOWN_slow_%=\n"
    "v_mfma_f32_16x16x32_bf16 v[60:63], v[188:191], v[36:39], v[60:63]\n"
    "s_waitcnt lgkmcnt(6)\n"
    "s_mov_b32 s68, s71\n"
    "v_mfma_f32_16x16x32_bf16 v[68:71], v[192:195], v[36:39], v[68:71]\n"
    "ds_read_b128 v[36:39], v10 offset:21504\n"
    "v_mfma_f32_16x16x32_bf16 v[48:51], v[196:199], v[40:43], v[48:51]\n"
    "s_add_u32 s71, s68, 1\n"
    "v_mfma_f32_16x16x32_bf16 v[52:55], v[200:203], v[40:43], v[52:55]\n"
    "s_waitcnt lgkmcnt(6)\n"
    "s_cmp_eq_u32 s71, 64\n"
    "v_mfma_f32_16x16x32_bf16 v[64:67], v[204:207], v[40:43], v[64:67]\n"
    "ds_read_b128 v[40:43], v10 offset:22528\n"
    "v_mfma_f32_16x16x32_bf16 v[56:59], v[196:199], v[44:47], v[56:59]\n"
    "s_cselect_b32 s70, 2, 0\n"
    "v_mfma_f32_16x16x32_bf16 v[60:63], v[200:203], v[44:47], v[60:63]\n"
    "s_waitcnt lgkmcnt(6)\n"
    "v_mfma_f32_16x16x32_bf16 v[68:71], v[204:207], v[44:47], v[68:71]\n"
    "ds_read_b128 v[44:47], v10 offset:23552\n"
    "v_mfma_f32_16x16x32_bf16 v[64:67], v[216:219], v[16:19], v[64:67]\n"
    "s_waitcnt lgkmcnt(6)\n"
    "ds_read_b128 v[16:19], v10 offset:16384\n"
    "v_mfma_f32_16x16x32_bf16 v[68:71], v[216:219], v[20:23], v[68:71]\n"
    "s_waitcnt lgkmcnt(6)\n"
    "ds_read_b128 v[20:23], v10 offset:18432\n"
    "v_mfma_f32_16x16x32_bf16 v[64:67], v[228:231], v[24:27], v[64:67]\n"
    "s_waitcnt lgkmcnt(6)\n"
    "ds_read_b128 v[24:27], v10 offset:20480\n"
    "v_mfma_f32_16x16x32_bf16 v[68:71], v[228:231], v[28:31], v[68:71]\n"
    "s_waitcnt lgkmcnt(6)\n"
    "ds_read_b128 v[28:31], v10 offset:22528\n"
    "v_mfma_f32_16x16x32_bf16 v[64:67], v[240:243], v[32:35], v[64:67]\n"
    "s_waitcnt lgkmcnt(6)\n"
    "ds_read_b128 v[32:35], v10 offset:17408\n"
    "v_mfma_f32_16x16x32_bf16 v[68:71], v[240:243], v[36:39], v[68:71]\n"
    "s_waitcnt lgkmcnt(6)\n"
    "ds_read_b128 v[36:39], v10 offset:19456\n"
    "v_mfma_f32_16x16x32_bf16 v[64:67], v[252:255], v[40:43], v[64:67]\n"
    "s_waitcnt lgkmcnt(6)\n"
    "ds_read_b128 v[40:43], v10 offset:21504\n"
    "v_mfma_f32_16x16x32_bf16 v[68:71], v[252:255], v[44:47], v[68:71]\n"
    "s_waitcnt lgkmcnt(6)\n"
    "ds_read_b128 v[44:47], v10 offset:23552\n"
    "v_add_u32 v13, v11, v3\n"
    "v_mfma_f32_16x16x32_bf16 v[48:51], v[208:211], v[16:19], v[48:51]\n"
    "v_cmp_gt_i32 vcc, s71, v13\n"
    "v_mfma_f32_16x16x32_bf16 v[52:55], v[212:215], v[16:19], v[52:55]\n"
    "s_waitcnt lgkmcnt(6)\n"
    "v_cvt_pk_bf16_f32 v64, v64, v65\n"
    "v_cvt_pk_bf16_f32 v65, v66, v67\n"
    "v_mfma_f32_16x16x32_bf16 v[48:51], v[220:223], v[20:23], v[48:51]\n"
    "s_waitcnt lgkmcnt(5)\n"
    "v_cvt_pk_bf16_f32 v66, v68, v69\n"
    "v_cvt_pk_bf16_f32 v67, v70, v71\n"
    "v_xor_b32 v9, s78, v9\n"
    "v_mfma_f32_16x16x32_bf16 v[52:55], v[224:227], v[20:23], v[52:55]\n"
    "s_waitcnt vmcnt(0) lgkmcnt(0)\n"
    "s_barrier\n"
    "ds_read_b128 v[16:19], v9 offset:0\n"
    "s_nop 0\n"
    "v_mfma_f32_16x16x32_bf16 v[48:51], v[232:235], v[24:27], v[48:51]\n"
    "ds_read_b128 v[20:23], v9 offset:1024\n"
    "s_or_b64 exec, vcc, 1\n"
    "buffer_atomic_smax v11, v2, s[36:39], 0 offen sc0 sc1\n"
    "s_mov_b64 exec, -1\n"
    "v_mfma_f32_16x16x32_bf16 v[52:55], v[236:239], v[24:27], v[52:55]\n"
    "ds_read_b128 v[24:27], v9 offset:2048\n"
    "v_mfma_f32_16x16x32_bf16 v[48:51], v[244:247], v[28:31], v[48:51]\n"
    "v_permlane16_swap_b32 v64, v66\n"
    "v_mfma_f32_16x16x32_bf16 v[52:55], v[248:251], v[28:31], v[52:55]\n"
    "ds_read_b128 v[28:31], v9 offset:3072\n"
    "v_mfma_f32_16x16x32_bf16 v[56:59], v[208:211], v[32:35], v[56:59]\n"
    "v_permlane16_swap_b32 v65, v67\n"
    "v_mfma_f32_16x16x32_bf16 v[60:63], v[212:215], v[32:35], v[60:63]\n"
    "ds_read_b128 v[32:35], v9 offset:4096\n"
    "v_cvt_pk_bf16_f32 v48, v48, v49\n"
    "v_mfma_f32_16x16x32_bf16 v[56:59], v[220:223], v[36:39], v[56:59]\n"
    "buffer_store_dwordx4 v[64:67], v4, s[36:39], s73 offen sc1\n"
    "v_cvt_pk_bf16_f32 v49, v50, v51\n"
    "v_cvt_pk_bf16_f32 v50, v52, v53\n"
    "v_mfma_f32_16x16x32_bf16 v[60:63], v[224:227], v[36:39], v[60:63]\n"
    "ds_read_b128 v[36:39], v9 offset:5120\n"
    "v_cvt_pk_bf16_f32 v51, v54, v55\n"
    "v_mfma_f32_16x16x32_bf16 v[56:59], v[232:235], v[40:43], v[56:59]\n"
    "buffer_store_dwordx4 v[48:51], v15, s[36:39], s73 offen sc1\n"
    "v_mfma_f32_16x16x32_bf16 v[60:63], v[236:239], v[40:43], v[60:63]\n"
    "ds_read_b128 v[40:43], v9 offset:6144\n"
    "v_mfma_f32_16x16x32_bf16 v[56:59], v[244:247], v[44:47], v[56:59]\n"
    "s_waitcnt lgkmcnt(6)\n"
    "v_mfma_f32_16x16x32_bf16 v[60:63], v[248:251], v[44:47], v[60:63]\n"
    "ds_read_b128 v[44:47], v9 offset:7168\n"
    "s_branch .LgDOWN_head_%=\n"
    ".LgDOWN_LOOPE_%=:\n"
    ".LgDOWN_done_%=:\n"
    "s_waitcnt vmcnt(0)\n"
    "s_mov_b32 s74, 63\n"
    "v_mov_b32 v12, s74\n"
    "s_mov_b64 exec, s[62:63]\n"
    "buffer_atomic_smax v12, v14, s[36:39], 0 offen sc1\n"
    "s_mov_b64 exec, -1\n"
    "s_waitcnt vmcnt(0)\n"
    "s_branch .LgDOWN_FIN_%=\n"
    ".LgDOWN_chk0_%=:\n"
    "s_or_b64 exec, vcc, 1\n"
    "buffer_atomic_smax v11, v2, s[36:39], 0 offen sc0 sc1\n"
    "s_mov_b64 exec, -1\n"
    "v_mfma_f32_16x16x32_bf16 v[48:51], a[108:111], v[24:27], v[48:51]\n"
    "s_and_b32 s73, s68, s57\n"
    "v_mfma_f32_16x16x32_bf16 v[52:55], a[112:115], v[24:27], v[52:55]\n"
    "s_waitcnt lgkmcnt(6)\n"
    "s_mul_i32 s73, s73, s56\n"
    "v_mfma_f32_16x16x32_bf16 v[64:67], a[116:119], v[24:27], v[64:67]\n"
    "ds_read_b128 v[24:27], v9 offset:26624\n"
    "v_mfma_f32_16x16x32_bf16 v[56:59], a[108:111], v[28:31], v[56:59]\n"
    "s_add_u32 s73, s73, s55\n"
    "v_mfma_f32_16x16x32_bf16 v[60:63], a[112:115], v[28:31], v[60:63]\n"
    "s_waitcnt lgkmcnt(6)\n"
    "v_mfma_f32_16x16x32_bf16 v[68:71], a[116:119], v[28:31], v[68:71]\n"
    "ds_read_b128 v[28:31], v9 offset:27648\n"
    "v_mfma_f32_16x16x32_bf16 v[48:51], a[120:123], v[32:35], v[48:51]\n"
    "v_mfma_f32_16x16x32_bf16 v[52:55], a[124:127], v[32:35], v[52:55]\n"
    "s_waitcnt lgkmcnt(6)\n"
    "v_mfma_f32_16x16x32_bf16 v[64:67], a[128:131], v[32:35], v[64:67]\n"
    "ds_read_b128 v[32:35], v9 offset:28672\n"
    "v_mfma_f32_16x16x32_bf16 v[56:59], a[120:123], v[36:39], v[56:59]\n"
    "v_mfma_f32_16x16x32_bf16 v[60:63], a[124:127], v[36:39], v[60:63]\n"
    "s_waitcnt lgkmcnt(6)\n"
    "v_mfma_f32_16x16x32_bf16 v[68:71], a[128:131], v[36:39], v[68:71]\n"
    "ds_read_b128 v[36:39], v9 offset:29696\n"
    "v_mfma_f32_16x16x32_bf16 v[48:51], a[132:135], v[40:43], v[48:51]\n"
    "v_mfma_f32_16x16x32_bf16 v[52:55], a[136:139], v[40:43], v[52:55]\n"
    "s_waitcnt lgkmcnt(6)\n"
    "v_mfma_f32_16x16x32_bf16 v[64:67], a[140:143], v[40:43], v[64:67]\n"
    "ds_read_b128 v[40:43], v9 offset:30720\n"
    "v_mfma_f32_16x16x32_bf16 v[56:59], a[132:135], v[44:47], v[56:59]\n"
    "v_mfma_f32_16x16x32_bf16 v[60:63], a[136:139], v[44:47], v[60:63]\n"
    "s_waitcnt lgkmcnt(6)\n"
    "v_mfma_f32_16x16x32_bf16 v[68:71], a[140:143], v[44:47], v[68:71]\n"
    "ds_read_b128 v[44:47], v9 offset:31744\n"
    "v_mfma_f32_16x16x32_bf16 v[48:51], a[144:147], v[16:19], v[48:51]\n"
    "v_mfma_f32_16x16x32_bf16 v[52:55], a[148:151], v[16:19], v[52:55]\n"
    "s_waitcnt lgkmcnt(6)\n"
    "v_mfma_f32_16x16x32_bf16 v[64:67], a[152:155], v[16:19], v[64:67]\n"
    "ds_read_b128 v[16:19], v9 offset:32768\n"
    "v_mfma_f32_16x16x32_bf16 v[56:59], a[144:147], v[20:23], v[56:59]\n"
    "v_mfma_f32_16x16x32_bf16 v[60:63], a[148:151], v[20:23], v[60:63]\n"
    "s_waitcnt lgkmcnt(6)\n"
    "v_mfma_f32_16x16x32_bf16 v[68:71], a[152:155], v[20:23], v[68:71]\n"
    "ds_read_b128 v[20:23], v9 offset:33792\n"
    "v_mfma_f32_16x16x32_bf16 v[48:51], a[156:159], v[24:27], v[48:51]\n"
    "v_mfma_f32_16x16x32_bf16 v[52:55], a[160:163], v[24:27], v[52:55]\n"
    "s_waitcnt lgkmcnt(6)\n"
    "v_mfma_f32_16x16x32_bf16 v[64:67], a[164:167], v[24:27], v[64:67]\n"
    "ds_read_b128 v[24:27], v9 offset:34816\n"
    "v_mfma_f32_16x16x32_bf16 v[56:59], a[156:159], v[28:31], v[56:59]\n"
    "v_mfma_f32_16x16x32_bf16 v[60:63], a[160:163], v[28:31], v[60:63]\n"
    "s_waitcnt lgkmcnt(6)\n"
    "v_mfma_f32_16x16x32_bf16 v[68:71], a[164:167], v[28:31], v[68:71]\n"
    "ds_read_b128 v[28:31], v9 offset:35840\n"
    "v_mfma_f32_16x16x32_bf16 v[48:51], a[168:171], v[32:35], v[48:51]\n"
    "v_mfma_f32_16x16x32_bf16 v[52:55], a[172:175], v[32:35], v[52:55]\n"
    "s_waitcnt lgkmcnt(6)\n"
    "v_mfma_f32_16x16x32_bf16 v[64:67], a[176:179], v[32:35], v[64:67]\n"
    "ds_read_b128 v[32:35], v9 offset:36864\n"
    "v_mfma_f32_16x16x32_bf16 v[56:59], a[168:171], v[36:39], v[56:59]\n"
    "v_mfma_f32_16x16x32_bf16 v[60:63], a[172:175], v[36:39], v[60:63]\n"
    "s_waitcnt lgkmcnt(6)\n"
    "v_mfma_f32_16x16x32_bf16 v[68:71], a[176:179], v[36:39], v[68:71]\n"
    "ds_read_b128 v[36:39], v9 offset:37888\n"
    "v_mfma_f32_16x16x32_bf16 v[48:51], a[180:183], v[40:43], v[48:51]\n"
    "v_mfma_f32_16x16x32_bf16 v[52:55], a[184:187], v[40:43], v[52:55]\n"
    "s_waitcnt lgkmcnt(6)\n"
    "v_mfma_f32_16x16x32_bf16 v[64:67], a[188:191], v[40:43], v[64:67]\n"
    "ds_read_b128 v[40:43], v9 offset:38912\n"
    "v_mfma_f32_16x16x32_bf16 v[56:59], a[180:183], v[44:47], v[56:59]\n"
    "v_mfma_f32_16x16x32_bf16 v[60:63], a[184:187], v[44:47], v[60:63]\n"
    "s_waitcnt lgkmcnt(6)\n"
    "v_mfma_f32_16x16x32_bf16 v[68:71], a[188:191], v[44:47], v[68:71]\n"
    "ds_read_b128 v[44:47], v9 offset:39936\n"
    "v_mfma_f32_16x16x32_bf16 v[48:51], a[192:195], v[16:19], v[48:51]\n"
    "s_branch .LgDOWN_seg0_%=\n"
    ".LgDOWN_chk1_%=:\n"
    "s_waitcnt vmcnt(0)\n"
    "v_add_u32 v13, v11, v3\n"
    "v_cmp_gt_i32 vcc, s71, v13\n"
    "s_cbranch_vccz .LgDOWN_dma1_%=\n"
    "s_or_b64 exec, vcc, 1\n"
    "buffer_atomic_smax v11, v2, s[36:39], 0 offen sc0 sc1\n"
    "s_mov_b64 exec, -1\n"
    "s_branch .LgDOWN_plain1_%=\n"
    ".LgDOWN_dma1_%=:\n"
    "s_mov_b32 s70, 1\n"
    "v_mfma_f32_16x16x32_bf16 v[52:55], a[196:199], v[16:19], v[52:55]\n"
    "s_waitcnt lgkmcnt(6)\n"
    "s_add_u32 m0, s77, s59\n"
    "s_add_u32 s77, s77, 8192\n"
    "buffer_load_dwordx4 v1, s[36:39], s76 offen sc1 lds\n"
    "v_mfma_f32_16x16x32_bf16 v[64:67], a[200:203], v[16:19], v[64:67]\n"
    "ds_read_b128 v[16:19], v9 offset:40960\n"
    "v_mfma_f32_16x16x32_bf16 v[56:59], a[192:195], v[20:23], v[56:59]\n"
    "buffer_load_dwordx4 v1, s[36:39], s76 offen offset:1024 sc1 lds\n"
    "s_add_u32 s76, s76, 16384\n"
    "v_mfma_f32_16x16x32_bf16 v[60:63], a[196:199], v[20:23], v[60:63]\n"
    "s_waitcnt lgkmcnt(6)\n"
    "v_mfma_f32_16x16x32_bf16 v[68:71], a[200:203], v[20:23], v[68:71]\n"
    "ds_read_b128 v[20:23], v9 offset:41984\n"
    "v_mfma_f32_16x16x32_bf16 v[48:51], a[204:207], v[24:27], v[48:51]\n"
    "s_add_u32 m0, s77, s59\n"
    "s_add_u32 s77, s77, 8192\n"
    "buffer_load_dwordx4 v1, s[36:39], s76 offen sc1 lds\n"
    "v_mfma_f32_16x16x32_bf16 v[52:55], a[208:211], v[24:27], v[52:55]\n"
    "s_waitcnt lgkmcnt(6)\n"
    "buffer_load_dwordx4 v1, s[36:39], s76 offen offset:1024 sc1 lds\n"
    "s_add_u32 s76, s76, 16384\n"
    "v_mfma_f32_16x16x32_bf16 v[64:67], a[212:215], v[24:27], v[64:67]\n"
    "ds_read_b128 v[24:27], v9 offset:43008\n"
    "v_mfma_f32_16x16x32_bf16 v[56:59], a[204:207], v[28:31], v[56:59]\n"
    "v_mfma_f32_16x16x32_bf16 v[60:63], a[208:211], v[28:31], v[60:63]\n"
    "s_waitcnt lgkmcnt(6)\n"
    "s_add_u32 m0, s77, s59\n"
    "s_add_u32 s77, s77, 8192\n"
    "buffer_load_dwordx4 v1, s[36:39], s76 offen sc1 lds\n"
    "v_mfma_f32_16x16x32_bf16 v[68:71], a[212:215], v[28:31], v[68:71]\n"
    "ds_read_b128 v[28:31], v9 offset:44032\n"
    "v_mfma_f32_16x16x32_bf16 v[48:51], a[216:219], v[32:35], v[48:51]\n"
    "buffer_load_dwordx4 v1, s[36:39], s76 offen offset:1024 sc1 lds\n"
    "s_add_u32 s76, s76, 16384\n"
    "v_mfma_f32_16x16x32_bf16 v[52:55], a[220:223], v[32:35], v[52:55]\n"
    "s_waitcnt lgkmcnt(6)\n"
    "v_mfma_f32_16x16x32_bf16 v[64:67], a[224:227], v[32:35], v[64:67]\n"
    "ds_read_b128 v[32:35], v9 offset:45056\n"
    "v_mfma_f32_16x16x32_bf16 v[56:59], a[216:219], v[36:39], v[56:59]\n"
    "s_add_u32 m0, s77, s59\n"
    "s_add_u32 s77, s77, 8192\n"
    "buffer_load_dwordx4 v1, s[36:39], s76 offen sc1 lds\n"
    "v_mfma_f32_16x16x32_bf16 v[60:63], a[220:223], v[36:39], v[60:63]\n"
    "s_waitcnt lgkmcnt(6)\n"
    "buffer_load_dwordx4 v1, s[36:39], s76 offen offset:1024 sc1 lds\n"
    "s_add_u32 s76, s76, 16384\n"
    "v_mfma_f32_16x16x32_bf16 v[68:71], a[224:227], v[36:39], v[68:71]\n"
    "ds_read_b128 v[36:39], v9 offset:46080\n"
    "v_mfma_f32_16x16x32_bf16 v[48:51], a[228:231], v[40:43], v[48:51]\n"
    "v_mfma_f32_16x16x32_bf16 v[52:55], a[232:235], v[40:43], v[52:55]\n"
    "s_waitcnt lgkmcnt(6)\n"
    "s_add_u32 m0, s77, s59\n"
    "s_add_u32 s77, s77, 8192\n"
    "buffer_load_dwordx4 v1, s[36:39], s76 offen sc1 lds\n"
    "v_mfma_f32_16x16x32_bf16 v[64:67], a[236:239], v[40:43], v[64:67]\n"
    "ds_read_b128 v[40:43], v9 offset:47104\n"
    "v_mfma_f32_16x16x32_bf16 v[56:59], a[228:231], v[44:47], v[56:59]\n"
    "v_mfma_f32_16x16x32_bf16 v[60:63], a[232:235], v[44:47], v[60:63]\n"
    "s_waitcnt lgkmcnt(6)\n"
    "buffer_load_dwordx4 v1, s[36:39], s76 offen offset:1024 sc1 lds\n"
    "s_add_u32 s76, s76, 16384\n"
    "v_mfma_f32_16x16x32_bf16 v[68:71], a[236:239], v[44:47], v[68:71]\n"
    "ds_read_b128 v[44:47], v9 offset:48128\n"
    "v_mfma_f32_16x16x32_bf16 v[48:51], a[240:243], v[16:19], v[48:51]\n"
    "s_add_u32 m0, s77, s59\n"
    "s_add_u32 s77, s77, 8192\n"
    "buffer_load_dwordx4 v1, s[36:39], s76 offen sc1 lds\n"
    "v_mfma_f32_16x16x32_bf16 v[52:55], a[244:247], v[16:19], v[52:55]\n"
    "s_waitcnt lgkmcnt(6)\n"
    "v_mfma_f32_16x16x32_bf16 v[64:67], a[248:251], v[16:19], v[64:67]\n"
    "ds_read_b128 v[16:19], v10 offset:0\n"
    "v_mfma_f32_16x16x32_bf16 v[56:59], a[240:243], v[20:23], v[56:59]\n"
    "buffer_load_dwordx4 v1, s[36:39], s76 offen offset:1024 sc1 lds\n"
    "s_add_u32 s76, s76, 16384\n"
    "v_mfma_f32_16x16x32_bf16 v[60:63], a[244:247], v[20:23], v[60:63]\n"
    "s_waitcnt lgkmcnt(6)\n"
    "s_add_u32 m0, s77, s59\n"
    "s_add_u32 s77, s77, 8192\n"
    "buffer_load_dwordx4 v1, s[36:39], s76 offen sc1 lds\n"
    "v_mfma_f32_16x16x32_bf16 v[68:71], a[248:251], v[20:23], v[68:71]\n"
    "ds_read_b128 v[20:23], v10 offset:1024\n"
    "v_mfma_f32_16x16x32_bf16 v[48:51], a[252:255], v[24:27], v[48:51]\n"
    "v_mfma_f32_16x16x32_bf16 v[52:55], v[80:83], v[24:27], v[52:55]\n"
    "s_waitcnt lgkmcnt(6)\n"
    "buffer_load_dwordx4 v1, s[36:39], s76 offen offset:1024 sc1 lds\n"
    "s_add_u32 s76, s76, 16384\n"
    "v_mfma_f32_16x16x32_bf16 v[64:67], v[84:87], v[24:27], v[64:67]\n"
    "ds_read_b128 v[24:27], v10 offset:2048\n"
    "v_mfma_f32_16x16x32_bf16 v[56:59], a[252:255], v[28:31], v[56:59]\n"
    "s_add_u32 m0, s77, s59\n"
    "s_add_u32 s77, s77, 8192\n"
    "buffer_load_dwordx4 v1, s[36:39], s76 offen sc1 lds\n"
    "v_mfma_f32_16x16x32_bf16 v[60:63], v[80:83], v[28:31], v[60:63]\n"
    "s_waitcnt lgkmcnt(6)\n"
    "v_mfma_f32_16x16x32_bf16 v[68:71], v[84:87], v[28:31], v[68:71]\n"
    "ds_read_b128 v[28:31], v10 offset:3072\n"
    "v_mfma_f32_16x16x32_bf16 v[48:51], v[88:91], v[32:35], v[48:51]\n"
    "buffer_load_dwordx4 v1, s[36:39], s76 offen offset:1024 sc1 lds\n"
    "s_add_u32 s76, s76, 16384\n"
    "v_mfma_f32_16x16x32_bf16 v[52:55], v[92:95], v[32:35], v[52:55]\n"
    "s_waitcnt lgkmcnt(6)\n"
    "s_add_u32 m0, s77, s59\n"
    "s_add_u32 s77, s77, 8192\n"
    "buffer_load_dwordx4 v1, s[36:39], s76 offen sc1 lds\n"
    "v_mfma_f32_16x16x32_bf16 v[64:67], v[96:99], v[32:35], v[64:67]\n"
    "ds_read_b128 v[32:35], v10 offset:4096\n"
    "v_mfma_f32_16x16x32_bf16 v[56:59], v[88:91], v[36:39], v[56:59]\n"
    "v_mfma_f32_16x16x32_bf16 v[60:63], v[92:95], v[36:39], v[60:63]\n"
    "s_waitcnt lgkmcnt(6)\n"
    "buffer_load_dwordx4 v1, s[36:39], s76 offen offset:1024 sc1 lds\n"
    "s_add_u32 s76, s76, 16384\n"
    "v_mfma_f32_16x16x32_bf16 v[68:71], v[96:99], v[36:39], v[68:71]\n"
    "ds_read_b128 v[36:39], v10 offset:5120\n"
    "v_mfma_f32_16x16x32_bf16 v[48:51], v[100:103], v[40:43], v[48:51]\n"
    "v_mfma_f32_16x16x32_bf16 v[52:55], v[104:107], v[40:43], v[52:55]\n"
    "s_waitcnt lgkmcnt(6)\n"
    "s_branch .LgDOWN_seg1_%=\n"
    ".LgDOWN_chk2_%=:\n"
    "s_waitcnt vmcnt(0)\n"
    "v_add_u32 v13, v11, v3\n"
    "v_cmp_gt_i32 vcc, s71, v13\n"
    "s_cbranch_vccz .LgDOWN_dma2_%=\n"
    "s_or_b64 exec, vcc, 1\n"
    "buffer_atomic_smax v11, v2, s[36:39], 0 offen sc0 sc1\n"
    "s_mov_b64 exec, -1\n"
    "s_branch .LgDOWN_plain2_%=\n"
    ".LgDOWN_dma2_%=:\n"
    "s_mov_b32 s70, 2\n"
    "v_mfma_f32_16x16x32_bf16 v[64:67], v[108:111], v[40:43], v[64:67]\n"
    "ds_read_b128 v[40:43], v10 offset:6144\n"
    "v_mfma_f32_16x16x32_bf16 v[56:59], v[100:103], v[44:47], v[56:59]\n"
    "s_add_u32 m0, s77, s59\n"
    "s_add_u32 s77, s77, 8192\n"
    "buffer_load_dwordx4 v1, s[36:39], s76 offen sc1 lds\n"
    "v_mfma_f32_16x16x32_bf16 v[60:63], v[104:107], v[44:47], v[60:63]\n"
    "s_waitcnt lgkmcnt(6)\n"
    "buffer_load_dwordx4 v1, s[36:39], s76 offen offset:1024 sc1 lds\n"
    "s_add_u32 s76, s76, 16384\n"
    "v_mfma_f32_16x16x32_bf16 v[68:71], v[108:111], v[44:47], v[68:71]\n"
    "ds_read_b128 v[44:47], v10 offset:7168\n"
    "v_mfma_f32_16x16x32_bf16 v[48:51], v[112:115], v[16:19], v[48:51]\n"
    "v_mfma_f32_16x16x32_bf16 v[52:55], v[116:119], v[16:19], v[52:55]\n"
    "s_waitcnt lgkmcnt(6)\n"
    "s_add_u32 m0, s77, s59\n"
    "s_add_u32 s77, s77, 8192\n"
    "buffer_load_dwordx4 v1, s[36:39], s76 offen sc1 lds\n"
    "v_mfma_f32_16x16x32_bf16 v[64:67], v[120:123], v[16:19], v[64:67]\n"
    "ds_read_b128 v[16:19], v10 offset:8192\n"
    "v_mfma_f32_16x16x32_bf16 v[56:59], v[112:115], v[20:23], v[56:59]\n"
    "buffer_load_dwordx4 v1, s[36:39], s76 offen offset:1024 sc1 lds\n"
    "s_add_u32 s76, s76, 16384\n"
    "v_mfma_f32_16x16x32_bf16 v[60:63], v[116:119], v[20:23], v[60:63]\n"
    "s_waitcnt lgkmcnt(6)\n"
    "v_mfma_f32_16x16x32_bf16 v[68:71], v[120:123], v[20:23], v[68:71]\n"
    "ds_read_b128 v[20:23], v10 offset:9216\n"
    "v_mfma_f32_16x16x32_bf16 v[48:51], v[124:127], v[24:27], v[48:51]\n"
    "s_add_u32 m0, s77, s59\n"
    "s_add_u32 s77, s77, 8192\n"
    "buffer_load_dwordx4 v1, s[36:39], s76 offen sc1 lds\n"
    "v_mfma_f32_16x16x32_bf16 v[52:55], v[128:131], v[24:27], v[52:55]\n"
    "s_waitcnt lgkmcnt(6)\n"
    "buffer_load_dwordx4 v1, s[36:39], s76 offen offset:1024 sc1 lds\n"
    "s_add_u32 s76, s76, 16384\n"
    "v_mfma_f32_16x16x32_bf16 v[64:67], v[132:135], v[24:27], v[64:67]\n"
    "ds_read_b128 v[24:27], v10 offset:10240\n"
    "v_mfma_f32_16x16x32_bf16 v[56:59], v[124:127], v[28:31], v[56:59]\n"
    "v_mfma_f32_16x16x32_bf16 v[60:63], v[128:131], v[28:31], v[60:63]\n"
    "s_waitcnt lgkmcnt(6)\n"
    "s_add_u32 m0, s77, s59\n"
    "s_add_u32 s77, s77, 8192\n"
    "buffer_load_dwordx4 v1, s[36:39], s76 offen sc1 lds\n"
    "v_mfma_f32_16x16x32_bf16 v[68:71], v[132:135], v[28:31], v[68:71]\n"
    "ds_read_b128 v[28:31], v10 offset:11264\n"
    "v_mfma_f32_16x16x32_bf16 v[48:51], v[136:139], v[32:35], v[48:51]\n"
    "buffer_load_dwordx4 v1, s[36:39], s76 offen offset:1024 sc1 lds\n"
    "s_add_u32 s76, s76, 16384\n"
    "v_mfma_f32_16x16x32_bf16 v[52:55], v[140:143], v[32:35], v[52:55]\n"
    "s_waitcnt lgkmcnt(6)\n"
    "v_mfma_f32_16x16x32_bf16 v[64:67], v[144:147], v[32:35], v[64:67]\n"
    "ds_read_b128 v[32:35], v10 offset:12288\n"
    "v_mfma_f32_16x16x32_bf16 v[56:59], v[136:139], v[36:39], v[56:59]\n"
    "s_add_u32 m0, s77, s59\n"
    "s_add_u32 s77, s77, 8192\n"
    "buffer_load_dwordx4 v1, s[36:39], s76 offen sc1 lds\n"
    "v_mfma_f32_16x16x32_bf16 v[60:63], v[140:143], v[36:39], v[60:63]\n"
    "s_waitcnt lgkmcnt(6)\n"
    "v_mfma_f32_16x16x32_bf16 v[68:71], v[144:147], v[36:39], v[68:71]\n"
    "ds_read_b128 v[36:39], v10 offset:13312\n"
    "v_mfma_f32_16x16x32_bf16 v[48:51], v[148:151], v[40:43], v[48:51]\n"
    "buffer_load_dwordx4 v1, s[36:39], s76 offen offset:1024 sc1 lds\n"
    "s_add_u32 s76, s76, 16384\n"
    "v_mfma_f32_16x16x32_bf16 v[52:55], v[152:155], v[40:43], v[52:55]\n"
    "s_waitcnt lgkmcnt(6)\n"
    "s_add_u32 m0, s77, s59\n"
    "s_add_u32 s77, s77, 8192\n"
    "buffer_load_dwordx4 v1, s[36:39], s76 offen sc1 lds\n"
    "v_mfma_f32_16x16x32_bf16 v[64:67], v[156:159], v[40:43], v[64:67]\n"
    "ds_read_b128 v[40:43], v10 offset:14336\n"
    "v_mfma_f32_16x16x32_bf16 v[56:59], v[148:151], v[44:47], v[56:59]\n"
    "v_mfma_f32_16x16x32_bf16 v[60:63], v[152:155], v[44:47], v[60:63]\n"
    "s_waitcnt lgkmcnt(6)\n"
    "buffer_load_dwordx4 v1, s[36:39], s76 offen offset:1024 sc1 lds\n"
    "s_add_u32 s76, s76, 16384\n"
    "v_mfma_f32_16x16x32_bf16 v[68:71], v[156:159], v[44:47], v[68:71]\n"
    "ds_read_b128 v[44:47], v10 offset:15360\n"
    "v_mfma_f32_16x16x32_bf16 v[48:51], v[160:163], v[16:19], v[48:51]\n"
    "s_add_u32 m0, s77, s59\n"
    "s_add_u32 s77, s77, 8192\n"
    "buffer_load_dwordx4 v1, s[36:39], s76 offen sc1 lds\n"
    "v_mfma_f32_16x16x32_bf16 v[52:55], v[164:167], v[16:19], v[52:55]\n"
    "s_waitcnt lgkmcnt(6)\n"
    "v_mfma_f32_16x16x32_bf16 v[64:67], v[168:171], v[16:19], v[64:67]\n"
    "ds_read_b128 v[16:19], v10 offset:16384\n"
    "v_mfma_f32_16x16x32_bf16 v[56:59], v[160:163], v[20:23], v[56:59]\n"
    "buffer_load_dwordx4 v1, s[36:39], s76 offen offset:1024 sc1 lds\n"
    "s_add_u32 s76, s76, 16384\n"
    "v_mfma_f32_16x16x32_bf16 v[60:63], v[164:167], v[20:23], v[60:63]\n"
    "s_waitcnt lgkmcnt(6)\n"
    "s_add_u32 m0, s77, s59\n"
    "s_add_u32 s77, s77, 8192\n"
    "buffer_load_dwordx4 v1, s[36:39], s76 offen sc1 lds\n"
    "v_mfma_f32_16x16x32_bf16 v[68:71], v[168:171], v[20:23], v[68:71]\n"
    "ds_read_b128 v[20:23], v10 offset:17408\n"
    "v_mfma_f32_16x16x32_bf16 v[48:51], v[172:175], v[24:27], v[48:51]\n"
    "v_mfma_f32_16x16x32_bf16 v[52:55], v[176:179], v[24:27], v[52:55]\n"
    "s_waitcnt lgkmcnt(6)\n"
    "buffer_load_dwordx4 v1, s[36:39], s76 offen offset:1024 sc1 lds\n"
    "s_add_u32 s76, s76, 16384\n"
    "v_mfma_f32_16x16x32_bf16 v[64:67], v[180:183], v[24:27], v[64:67]\n"
    "ds_read_b128 v[24:27], v10 offset:18432\n"
    "v_mfma_f32_16x16x32_bf16 v[56:59], v[172:175], v[28:31], v[56:59]\n"
    "s_add_u32 m0, s77, s59\n"
    "s_add_u32 s77, s77, 8192\n"
    "buffer_load_dwordx4 v1, s[36:39], s76 offen sc1 lds\n"
    "v_mfma_f32_16x16x32_bf16 v[60:63], v[176:179], v[28:31], v[60:63]\n"
    "s_waitcnt lgkmcnt(6)\n"
    "v_mfma_f32_16x16x32_bf16 v[68:71], v[180:183], v[28:31], v[68:71]\n"
    "ds_read_b128 v[28:31], v10 offset:19456\n"
    "v_mfma_f32_16x16x32_bf16 v[48:51], v[184:187], v[32:35], v[48:51]\n"
    "buffer_load_dwordx4 v1, s[36:39], s76 offen offset:1024 sc1 lds\n"
    "s_add_u32 s76, s76, 16384\n"
    "v_mfma_f32_16x16x32_bf16 v[52:55], v[188:191], v[32:35], v[52:55]\n"
    "s_waitcnt lgkmcnt(6)\n"
    "v_mfma_f32_16x16x32_bf16 v[64:67], v[192:195], v[32:35], v[64:67]\n"
    "ds_read_b128 v[32:35], v10 offset:20480\n"
    "v_mfma_f32_16x16x32_bf16 v[56:59], v[184:187], v[36:39], v[56:59]\n"
    "s_branch .LgDOWN_seg2_%=\n"
    ".LgDOWN_slow_%=:\n"
    "s_cmp_lg_u32 s70, 0\n"
    "s_cbranch_scc1 .LgDOWN_nolast_%=\n"
    "s_waitcnt vmcnt(0)\n"
    "v_add_u32 v13, v11, v3\n"
    "v_cmp_gt_i32 vcc, s71, v13\n"
    "s_nop 0\n"
    "s_or_b64 exec, vcc, 1\n"
    "buffer_atomic_smax v11, v2, s[36:39], 0 offen sc0 sc1\n"
    "s_mov_b64 exec, -1\n"
    ".LgDOWN_nolast_%=:\n"
    "v_mfma_f32_16x16x32_bf16 v[60:63], v[188:191], v[36:39], v[60:63]\n"
    "s_waitcnt lgkmcnt(6)\n"
    "v_mfma_f32_16x16x32_bf16 v[68:71], v[192:195], v[36:39], v[68:71]\n"
    "ds_read_b128 v[36:39], v10 offset:21504\n"
    "v_mfma_f32_16x16x32_bf16 v[48:51], v[196:199], v[40:43], v[48:51]\n"
    "v_mfma_f32_16x16x32_bf16 v[52:55], v[200:203], v[40:43], v[52:55]\n"
    "s_waitcnt lgkmcnt(6)\n"
    "v_mfma_f32_16x16x32_bf16 v[64:67], v[204:207], v[40:43], v[64:67]\n"
    "ds_read_b128 v[40:43], v10 offset:22528\n"
    "v_mfma_f32_16x16x32_bf16 v[56:59], v[196:199], v[44:47], v[56:59]\n"
    "v_mfma_f32_16x16x32_bf16 v[60:63], v[200:203], v[44:47], v[60:63]\n"
    "s_waitcnt lgkmcnt(6)\n"
    "v_mfma_f32_16x16x32_bf16 v[68:71], v[204:207], v[44:47], v[68:71]\n"
    "ds_read_b128 v[44:47], v10 offset:23552\n"
    "v_mfma_f32_16x16x32_bf16 v[64:67], v[216:219], v[16:19], v[64:67]\n"
    "s_waitcnt lgkmcnt(6)\n"
    "ds_read_b128 v[16:19], v10 offset:16384\n"
    "v_mfma_f32_16x16x32_bf16 v[68:71], v[216:219], v[20:23], v[68:71]\n"
    "s_waitcnt lgkmcnt(6)\n"
    "ds_read_b128 v[20:23], v10 offset:18432\n"
    "v_mfma_f32_16x16x32_bf16 v[64:67], v[228:231], v[24:27], v[64:67]\n"
    "s_waitcnt lgkmcnt(6)\n"
    "ds_read_b128 v[24:27], v10 offset:20480\n"
    "v_mfma_f32_16x16x32_bf16 v[68:71], v[228:231], v[28:31], v[68:71]\n"
    "s_waitcnt lgkmcnt(6)\n"
    "ds_read_b128 v[28:31], v10 offset:22528\n"
    "v_mfma_f32_16x16x32_bf16 v[64:67], v[240:243], v[32:35], v[64:67]\n"
    "s_waitcnt lgkmcnt(6)\n"
    "ds_read_b128 v[32:35], v10 offset:17408\n"
    "v_mfma_f32_16x16x32_bf16 v[68:71], v[240:243], v[36:39], v[68:71]\n"
    "s_waitcnt lgkmcnt(6)\n"
    "ds_read_b128 v[36:39], v10 offset:19456\n"
    "v_mfma_f32_16x16x32_bf16 v[64:67], v[252:255], v[40:43], v[64:67]\n"
    "s_waitcnt lgkmcnt(6)\n"
    "ds_read_b128 v[40:43], v10 offset:21504\n"
    "v_mfma_f32_16x16x32_bf16 v[68:71], v[252:255], v[44:47], v[68:71]\n"
    "s_waitcnt lgkmcnt(6)\n"
    "ds_read_b128 v[44:47], v10 offset:23552\n"
    "v_mfma_f32_16x16x32_bf16 v[48:51], v[208:211], v[16:19], v[48:51]\n"
    "v_mfma_f32_16x16x32_bf16 v[52:55], v[212:215], v[16:19], v[52:55]\n"
    "s_waitcnt lgkmcnt(6)\n"
    "v_cvt_pk_bf16_f32 v64, v64, v65\n"
    "v_cvt_pk_bf16_f32 v65, v66, v67\n"
    "v_mfma_f32_16x16x32_bf16 v[48:51], v[220:223], v[20:23], v[48:51]\n"
    "s_waitcnt lgkmcnt(5)\n"
    "v_cvt_pk_bf16_f32 v66, v68, v69\n"
    "v_cvt_pk_bf16_f32 v67, v70, v71\n"
    "v_mfma_f32_16x16x32_bf16 v[52:55], v[224:227], v[20:23], v[52:55]\n"
    "s_nop 0\n"
    "v_mfma_f32_16x16x32_bf16 v[48:51], v[232:235], v[24:27], v[48:51]\n"
    "v_mfma_f32_16x16x32_bf16 v[52:55], v[236:239], v[24:27], v[52:55]\n"
    "s_waitcnt lgkmcnt(4)\n"
    "v_mfma_f32_16x16x32_bf16 v[48:51], v[244:247], v[28:31], v[48:51]\n"
    "s_waitcnt lgkmcnt(3)\n"
    "v_permlane16_swap_b32 v64, v66\n"
    "v_mfma_f32_16x16x32_bf16 v[52:55], v[248:251], v[28:31], v[52:55]\n"
    "v_mfma_f32_16x16x32_bf16 v[56:59], v[208:211], v[32:35], v[56:59]\n"
    "s_waitcnt lgkmcnt(2)\n"
    "v_permlane16_swap_b32 v65, v67\n"
    "v_mfma_f32_16x16x32_bf16 v[60:63], v[212:215], v[32:35], v[60:63]\n"
    "v_cvt_pk_bf16_f32 v48, v48, v49\n"
    "v_mfma_f32_16x16x32_bf16 v[56:59], v[220:223], v[36:39], v[56:59]\n"
    "s_waitcnt lgkmcnt(1)\n"
    "buffer_store_dwordx4 v[64:67], v4, s[36:39], s73 offen sc1\n"
    "v_cvt_pk_bf16_f32 v49, v50, v51\n"
    "v_cvt_pk_bf16_f32 v50, v52, v53\n"
    "v_mfma_f32_16x16x32_bf16 v[60:63], v[224:227], v[36:39], v[60:63]\n"
    "v_cvt_pk_bf16_f32 v51, v54, v55\n"
    "v_mfma_f32_16x16x32_bf16 v[56:59], v[232:235], v[40:43], v[56:59]\n"
    "s_waitcnt lgkmcnt(0)\n"
    "buffer_store_dwordx4 v[48:51], v15, s[36:39], s73 offen sc1\n"
    "v_mfma_f32_16x16x32_bf16 v[60:63], v[236:239], v[40:43], v[60:63]\n"
    "v_mfma_f32_16x16x32_bf16 v[56:59], v[244:247], v[44:47], v[56:59]\n"
    "v_mfma_f32_16x16x32_bf16 v[60:63], v[248:251], v[44:47], v[60:63]\n"
    "s_nop 3\n"
    "v_cvt_pk_bf16_f32 v56, v56, v57\n"
    "v_cvt_pk_bf16_f32 v57, v58, v59\n"
    "s_nop 1\n"
    "v_cvt_pk_bf16_f32 v58, v60, v61\n"
    "v_cvt_pk_bf16_f32 v59, v62, v63\n"
    "buffer_store_dwordx4 v[56:59], v15, s[36:39], s73 offen offset:1024 sc1\n"
    "s_mov_b32 s68, s71\n"
    "s_cmp_eq_u32 s70, 0\n"
    "s_cbranch_scc1 .LgDOWN_idle_%=\n"
    "s_cmp_eq_u32 s68, 64\n"
    "s_cbranch_scc1 .LgDOWN_done_%=\n"
    "s_waitcnt vmcnt(3)\n"
    ".LgDOWN_bar_%=:\n"
    "v_xor_b32 v9, s78, v9\n"
    "s_add_u32 s71, s68, 1\n"
    "s_cmp_eq_u32 s71, 64\n"
    "s_cselect_b32 s70, 2, 0\n"
    "s_barrier\n"
    "ds_read_b128 v[16:19], v9 offset:0\n"
    "ds_read_b128 v[20:23], v9 offset:1024\n"
    "ds_read_b128 v[24:27], v9 offset:2048\n"
    "ds_read_b128 v[28:31], v9 offset:3072\n"
    "ds_read_b128 v[32:35], v9 offset:4096\n"
    "ds_read_b128 v[36:39], v9 offset:5120\n"
    "ds_read_b128 v[40:43], v9 offset:6144\n"
    "ds_read_b128 v[44:47], v9 offset:7168\n"
    "v_add_u32 v13, v11, v3\n"
    "v_cmp_gt_i32 vcc, s71, v13\n"
    "s_nop 0\n"
    "s_or_b64 exec, vcc, 1\n"
    "buffer_atomic_smax v11, v2, s[36:39], 0 offen sc0 sc1\n"
    "s_mov_b64 exec, -1\n"
    "s_waitcnt lgkmcnt(7)\n"
    "v_mfma_f32_16x16x32_bf16 v[64:67], a[8:11], v[16:19], 0\n"
    "s_waitcnt lgkmcnt(6)\n"
    "ds_read_b128 v[16:19], v9 offset:0\n"
    "v_mfma_f32_16x16x32_bf16 v[68:71], a[8:11], v[20:23], 0\n"
    "s_waitcnt lgkmcnt(6)\n"
    "ds_read_b128 v[20:23], v9 offset:2048\n"
    "v_mfma_f32_16x16x32_bf16 v[64:67], a[20:23], v[24:27], v[64:67]\n"
    "s_waitcnt lgkmcnt(6)\n"
    "ds_read_b128 v[24:27], v9 offset:4096\n"
    "v_mfma_f32_16x16x32_bf16 v[68:71], a[20:23], v[28:31], v[68:71]\n"
    "s_waitcnt lgkmcnt(6)\n"
    "ds_read_b128 v[28:31], v9 offset:6144\n"
    "v_mfma_f32_16x16x32_bf16 v[64:67], a[32:35], v[32:35], v[64:67]\n"
    "s_waitcnt lgkmcnt(6)\n"
    "ds_read_b128 v[32:35], v9 offset:1024\n"
    "v_mfma_f32_16x16x32_bf16 v[68:71], a[32:35], v[36:39], v[68:71]\n"
    "s_waitcnt lgkmcnt(6)\n"
    "ds_read_b128 v[36:39], v9 offset:3072\n"
    "v_mfma_f32_16x16x32_bf16 v[64:67], a[44:47], v[40:43], v[64:67]\n"
    "s_waitcnt lgkmcnt(6)\n"
    "ds_read_b128 v[40:43], v9 offset:5120\n"
    "v_mfma_f32_16x16x32_bf16 v[68:71], a[44:47], v[44:47], v[68:71]\n"
    "s_waitcnt lgkmcnt(6)\n"
    "ds_read_b128 v[44:47], v9 offset:7168\n"
    "v_mfma_f32_16x16x32_bf16 v[48:51], a[0:3], v[16:19], 0\n"
    "s_waitcnt lgkmcnt(6)\n"
    "s_sub_u32 s74, s68, 1\n"
    "v_mfma_f32_16x16x32_bf16 v[52:55], a[4:7], v[16:19], 0\n"
    "ds_read_b128 v[16:19], v9 offset:8192\n"
    "v_mfma_f32_16x16x32_bf16 v[48:51], a[12:15], v[20:23], v[48:51]\n"
    "s_waitcnt lgkmcnt(6)\n"
    "s_and_b32 s76, s71, s54\n"
    "v_mfma_f32_16x16x32_bf16 v[52:55], a[16:19], v[20:23], v[52:55]\n"
    "ds_read_b128 v[20:23], v9 offset:9216\n"
    "v_mfma_f32_16x16x32_bf16 v[48:51], a[24:27], v[24:27], v[48:51]\n"
    "s_waitcnt lgkmcnt(6)\n"
    "s_mul_i32 s76, s76, s53\n"
    "v_mfma_f32_16x16x32_bf16 v[52:55], a[28:31], v[24:27], v[52:55]\n"
    "ds_read_b128 v[24:27], v9 offset:10240\n"
    "v_mfma_f32_16x16x32_bf16 v[48:51], a[36:39], v[28:31], v[48:51]\n"
    "s_waitcnt lgkmcnt(6)\n"
    "s_add_u32 s76, s76, s52\n"
    "v_mfma_f32_16x16x32_bf16 v[52:55], a[40:43], v[28:31], v[52:55]\n"
    "ds_read_b128 v[28:31], v9 offset:11264\n"
    "v_mfma_f32_16x16x32_bf16 v[56:59], a[0:3], v[32:35], 0\n"
    "s_waitcnt lgkmcnt(6)\n"
    "s_and_b32 s77, s71, 1\n"
    "v_mfma_f32_16x16x32_bf16 v[60:63], a[4:7], v[32:35], 0\n"
    "ds_read_b128 v[32:35], v9 offset:12288\n"
    "v_mfma_f32_16x16x32_bf16 v[56:59], a[12:15], v[36:39], v[56:59]\n"
    "s_waitcnt lgkmcnt(6)\n"
    "s_mul_i32 s77, s77, 81920\n"
    "v_mfma_f32_16x16x32_bf16 v[60:63], a[16:19], v[36:39], v[60:63]\n"
    "ds_read_b128 v[36:39], v9 offset:13312\n"
    "v_mfma_f32_16x16x32_bf16 v[56:59], a[24:27], v[40:43], v[56:59]\n"
    "s_waitcnt lgkmcnt(6)\n"
    "s_add_u32 s77, s77, s59\n"
    "v_mfma_f32_16x16x32_bf16 v[60:63], a[28:31], v[40:43], v[60:63]\n"
    "ds_read_b128 v[40:43], v9 offset:14336\n"
    "v_mfma_f32_16x16x32_bf16 v[56:59], a[36:39], v[44:47], v[56:59]\n"
    "s_waitcnt lgkmcnt(6)\n"
    "v_mfma_f32_16x16x32_bf16 v[60:63], a[40:43], v[44:47], v[60:63]\n"
    "ds_read_b128 v[44:47], v9 offset:15360\n"
    "v_mfma_f32_16x16x32_bf16 v[48:51], a[48:51], v[16:19], v[48:51]\n"
    "v_mfma_f32_16x16x32_bf16 v[52:55], a[52:55], v[16:19], v[52:55]\n"
    "s_waitcnt lgkmcnt(6)\n"
    "v_mfma_f32_16x16x32_bf16 v[64:67], a[56:59], v[16:19], v[64:67]\n"
    "ds_read_b128 v[16:19], v9 offset:16384\n"
    "v_mfma_f32_16x16x32_bf16 v[56:59], a[48:51], v[20:23], v[56:59]\n"
    "v_mfma_f32_16x16x32_bf16 v[60:63], a[52:55], v[20:23], v[60:63]\n"
    "s_waitcnt lgkmcnt(6)\n"
    "v_mfma_f32_16x16x32_bf16 v[68:71], a[56:59], v[20:23], v[68:71]\n"
    "ds_read_b128 v[20:23], v9 offset:17408\n"
    "v_mfma_f32_16x16x32_bf16 v[48:51], a[60:63], v[24:27], v[48:51]\n"
    "v_mfma_f32_16x16x32_bf16 v[52:55], a[64:67], v[24:27], v[52:55]\n"
    "s_waitcnt lgkmcnt(6)\n"
    "v_mfma_f32_16x16x32_bf16 v[64:67], a[68:71], v[24:27], v[64:67]\n"
    "ds_read_b128 v[24:27], v9 offset:18432\n"
    "v_mfma_f32_16x16x32_bf16 v[56:59], a[60:63], v[28:31], v[56:59]\n"
    "v_mfma_f32_16x16x32_bf16 v[60:63], a[64:67], v[28:31], v[60:63]\n"
    "s_waitcnt lgkmcnt(6)\n"
    "v_mfma_f32_16x16x32_bf16 v[68:71], a[68:71], v[28:31], v[68:71]\n"
    "ds_read_b128 v[28:31], v9 offset:19456\n"
    "v_mfma_f32_16x16x32_bf16 v[48:51], a[72:75], v[32:35], v[48:51]\n"
    "v_mfma_f32_16x16x32_bf16 v[52:55], a[76:79], v[32:35], v[52:55]\n"
    "s_waitcnt lgkmcnt(6)\n"
    "v_mfma_f32_16x16x32_bf16 v[64:67], a[80:83], v[32:35], v[64:67]\n"
    "ds_read_b128 v[32:35], v9 offset:20480\n"
    "v_mfma_f32_16x16x32_bf16 v[56:59], a[72:75], v[36:39], v[56:59]\n"
    "v_mfma_f32_16x16x32_bf16 v[60:63], a[76:79], v[36:39], v[60:63]\n"
    "s_waitcnt lgkmcnt(6)\n"
    "v_mfma_f32_16x16x32_bf16 v[68:71], a[80:83], v[36:39], v[68:71]\n"
    "ds_read_b128 v[36:39], v9 offset:21504\n"
    "v_mfma_f32_16x16x32_bf16 v[48:51], a[84:87], v[40:43], v[48:51]\n"
    "v_mfma_f32_16x16x32_bf16 v[52:55], a[88:91], v[40:43], v[52:55]\n"
    "s_waitcnt lgkmcnt(6)\n"
    "v_mfma_f32_16x16x32_bf16 v[64:67], a[92:95], v[40:43], v[64:67]\n"
    "ds_read_b128 v[40:43], v9 offset:22528\n"
    "v_mfma_f32_16x16x32_bf16 v[56:59], a[84:87], v[44:47], v[56:59]\n"
    "v_mfma_f32_16x16x32_bf16 v[60:63], a[88:91], v[44:47], v[60:63]\n"
    "s_waitcnt lgkmcnt(6)\n"
    "v_mfma_f32_16x16x32_bf16 v[68:71], a[92:95], v[44:47], v[68:71]\n"
    "ds_read_b128 v[44:47], v9 offset:23552\n"
    "v_mfma_f32_16x16x32_bf16 v[48:51], a[96:99], v[16:19], v[48:51]\n"
    "v_mov_b32 v12, s74\n"
    "v_mfma_f32_16x16x32_bf16 v[52:55], a[100:103], v[16:19], v[52:55]\n"
    "s_waitcnt lgkmcnt(6)\n"
    "s_waitcnt vmcnt(0)\n"
    "v_add_u32 v13, v11, v3\n"
    "v_mfma_f32_16x16x32_bf16 v[64:67], a[104:107], v[16:19], v[64:67]\n"
    "ds_read_b128 v[16:19], v9 offset:24576\n"
    "buffer_atomic_smax v12, v14, s[36:39], 0 offen sc1\n"
    "v_cmp_gt_i32 vcc, s71, v13\n"
    "v_mfma_f32_16x16x32_bf16 v[56:59], a[96:99], v[20:23], v[56:59]\n"
    "v_add_u32 v10, 49152, v9\n"
    "v_mfma_f32_16x16x32_bf16 v[60:63], a[100:103], v[20:23], v[60:63]\n"
    "s_waitcnt lgkmcnt(6)\n"
    "v_mfma_f32_16x16x32_bf16 v[68:71], a[104:107], v[20:23], v[68:71]\n"
    "ds_read_b128 v[20:23], v9 offset:25600\n"
    "s_branch .LgDOWN_mid_%=\n"
    ".LgDOWN_idle_%=:\n"
    "s_waitcnt vmcnt(3)\n"
    "v_add_u32 v13, v11, v3\n"
    "v_cmp_gt_i32 vcc, s71, v13\n"
    "s_cbranch_vccnz .LgDOWN_idle2_%=\n"
    "s_and_b32 s76, s71, s54\n"
    "s_mul_i32 s76, s76, s53\n"
    "s_add_u32 s76, s76, s52\n"
    "s_and_b32 s77, s71, 1\n"
    "s_mul_i32 s77, s77, 81920\n"
    "s_add_u32 s77, s77, s59\n"
    "s_add_u32 m0, s77, s59\n"
    "s_add_u32 s77, s77, 8192\n"
    "buffer_load_dwordx4 v1, s[36:39], s76 offen sc1 lds\n"
    "buffer_load_dwordx4 v1, s[36:39], s76 offen offset:1024 sc1 lds\n"
    "s_add_u32 s76, s76, 16384\n"
    "s_add_u32 m0, s77, s59\n"
    "s_add_u32 s77, s77, 8192\n"
    "buffer_load_dwordx4 v1, s[36:39], s76 offen sc1 lds\n"
    "buffer_load_dwordx4 v1, s[36:39], s76 offen offset:1024 sc1 lds\n"
    "s_add_u32 s76, s76, 16384\n"
    "s_add_u32 m0, s77, s59\n"
    "s_add_u32 s77, s77, 8192\n"
    "buffer_load_dwordx4 v1, s[36:39], s76 offen sc1 lds\n"
    "buffer_load_dwordx4 v1, s[36:39], s76 offen offset:1024 sc1 lds\n"
    "s_add_u32 s76, s76, 16384\n"
    "s_add_u32 m0, s77, s59\n"
    "s_add_u32 s77, s77, 8192\n"
    "buffer_load_dwordx4 v1, s[36:39], s76 offen sc1 lds\n"
    "buffer_load_dwordx4 v1, s[36:39], s76 offen offset:1024 sc1 lds\n"
    "s_add_u32 s76, s76, 16384\n"
    "s_add_u32 m0, s77, s59\n"
    "s_add_u32 s77, s77, 8192\n"
    "buffer_load_dwordx4 v1, s[36:39], s76 offen sc1 lds\n"
    "buffer_load_dwordx4 v1, s[36:39], s76 offen offset:1024 sc1 lds\n"
    "s_add_u32 s76, s76, 16384\n"
    "s_add_u32 m0, s77, s59\n"
    "s_add_u32 s77, s77, 8192\n"
    "buffer_load_dwordx4 v1, s[36:39], s76 offen sc1 lds\n"
    "buffer_load_dwordx4 v1, s[36:39], s76 offen offset:1024 sc1 lds\n"
    "s_add_u32 s76, s76, 16384\n"
    "s_add_u32 m0, s77, s59\n"
    "s_add_u32 s77, s77, 8192\n"
    "buffer_load_dwordx4 v1, s[36:39], s76 offen sc1 lds\n"
    "buffer_load_dwordx4 v1, s[36:39], s76 offen offset:1024 sc1 lds\n"
    "s_add_u32 s76, s76, 16384\n"
    "s_add_u32 m0, s77, s59\n"
    "s_add_u32 s77, s77, 8192\n"
    "buffer_load_dwordx4 v1, s[36:39], s76 offen sc1 lds\n"
    "buffer_load_dwordx4 v1, s[36:39], s76 offen offset:1024 sc1 lds\n"
    "s_add_u32 s76, s76, 16384\n"
    "s_add_u32 m0, s77, s59\n"
    "s_add_u32 s77, s77, 8192\n"
    "buffer_load_dwordx4 v1, s[36:39], s76 offen sc1 lds\n"
    "buffer_load_dwordx4 v1, s[36:39], s76 offen offset:1024 sc1 lds\n"
    "s_add_u32 s76, s76, 16384\n"
    "s_waitcnt vmcnt(18)\n"
    "s_sub_u32 s74, s68, 1\n"
    "v_mov_b32 v12, s74\n"
    "s_mov_b64 exec, s[62:63]\n"
    "buffer_atomic_smax v12, v14, s[36:39], 0 offen sc1\n"
    "s_mov_b64 exec, -1\n"
    "s_waitcnt vmcnt(1)\n"
    "s_branch .LgDOWN_bar_%=\n"
    ".LgDOWN_idle2_%=:\n"
    "s_or_b64 exec, vcc, 1\n"
    "buffer_atomic_smax v11, v2, s[36:39], 0 offen sc0 sc1\n"
    "s_mov_b64 exec, -1\n"
    "s_waitcnt vmcnt(1)\n"
    "s_sub_u32 s74, s68, 1\n"
    "v_mov_b32 v12, s74\n"
    "s_mov_b64 exec, s[62:63]\n"
    "buffer_atomic_smax v12, v14, s[36:39], 0 offen sc1\n"
    "s_mov_b64 exec, -1\n"
    "s_waitcnt vmcnt(1)\n"
    "s_branch .LgDOWN_block1_%=\n"
    ".LgDOWN_block_%=:\n"
    "v_add_u32 v13, v11, v3\n"
    "v_cmp_gt_i32 vcc, s71, v13\n"
    "s_nop 0\n"
    "s_or_b64 exec, vcc, 1\n"
    "buffer_atomic_smax v11, v2, s[36:39], 0 offen sc0 sc1\n"
    "s_mov_b64 exec, -1\n"
    "s_waitcnt vmcnt(0)\n"
    ".LgDOWN_block1_%=:\n"
    "v_add_u32 v13, v11, v3\n"
    "v_cmp_gt_i32 vcc, s71, v13\n"
    "s_cbranch_vccz .LgDOWN_go_%=\n"
    "s_sleep 4\n"
    "s_sub_u32 s72, s72, 1\n"
    "s_cbranch_scc0 .LgDOWN_block_%=\n"
    "s_mov_b32 s72, 0\n"
    ".LgDOWN_go_%=:\n"
    "s_and_b32 s76, s71, s54\n"
    "s_mul_i32 s76, s76, s53\n"
    "s_add_u32 s76, s76, s52\n"
    "s_and_b32 s77, s71, 1\n"
    "s_mul_i32 s77, s77, 81920\n"
    "s_add_u32 s77, s77, s59\n"
    "s_add_u32 m0, s77, s59\n"
    "s_add_u32 s77, s77, 8192\n"
    "buffer_load_dwordx4 v1, s[36:39], s76 offen sc1 lds\n"
    "buffer_load_dwordx4 v1, s[36:39], s76 offen offset:1024 sc1 lds\n"
    "s_add_u32 s76, s76, 16384\n"
    "s_add_u32 m0, s77, s59\n"
    "s_add_u32 s77, s77, 8192\n"
    "buffer_load_dwordx4 v1, s[36:39], s76 offen sc1 lds\n"
    "buffer_load_dwordx4 v1, s[36:39], s76 offen offset:1024 sc1 lds\n"
    "s_add_u32 s76, s76, 16384\n"
    "s_add_u32 m0, s77, s59\n"
    "s_add_u32 s77, s77, 8192\n"
    "buffer_load_dwordx4 v1, s[36:39], s76 offen sc1 lds\n"
    "buffer_load_dwordx4 v1, s[36:39], s76 offen offset:1024 sc1 lds\n"
    "s_add_u32 s76, s76, 16384\n"
    "s_add_u32 m0, s77, s59\n"
    "s_add_u32 s77, s77, 8192\n"
    "buffer_load_dwordx4 v1, s[36:39], s76 offen sc1 lds\n"
    "buffer_load_dwordx4 v1, s[36:39], s76 offen offset:1024 sc1 lds\n"
    "s_add_u32 s76, s76, 16384\n"
    "s_add_u32 m0, s77, s59\n"
    "s_add_u32 s77, s77, 8192\n"
    "buffer_load_dwordx4 v1, s[36:39], s76 offen sc1 lds\n"
    "buffer_load_dwordx4 v1, s[36:39], s76 offen offset:1024 sc1 lds\n"
    "s_add_u32 s76, s76, 16384\n"
    "s_add_u32 m0, s77, s59\n"
    "s_add_u32 s77, s77, 8192\n"
    "buffer_load_dwordx4 v1, s[36:39], s76 offen sc1 lds\n"
    "buffer_load_dwordx4 v1, s[36:39], s76 offen offset:1024 sc1 lds\n"
    "s_add_u32 s76, s76, 16384\n"
    "s_add_u32 m0, s77, s59\n"
    "s_add_u32 s77, s77, 8192\n"
    "buffer_load_dwordx4 v1, s[36:39], s76 offen sc1 lds\n"
    "buffer_load_dwordx4 v1, s[36:39], s76 offen offset:1024 sc1 lds\n"
    "s_add_u32 s76, s76, 16384\n"
    "s_add_u32 m0, s77, s59\n"
    "s_add_u32 s77, s77, 8192\n"
    "buffer_load_dwordx4 v1, s[36:39], s76 offen sc1 lds\n"
    "buffer_load_dwordx4 v1, s[36:39], s76 offen offset:1024 sc1 lds\n"
    "s_add_u32 s76, s76, 16384\n"
    "s_add_u32 m0, s77, s59\n"
    "s_add_u32 s77, s77, 8192\n"
    "buffer_load_dwordx4 v1, s[36:39], s76 offen sc1 lds\n"
    "buffer_load_dwordx4 v1, s[36:39], s76 offen offset:1024 sc1 lds\n"
    "s_add_u32 s76, s76, 16384\n"
    "s_waitcnt vmcnt(0)\n"
    "s_branch .LgDOWN_bar_%=\n"
    ".LgDOWN_FIN_%=:\n"
  );
}

DEVI void role_rec(const Params& p, int L, int j, int i, u32 dbg, unsigned long long tag, unsigned char* smem) {
  const int tid = threadIdx.x, lane = tid & 63, w = (int)rfl(tid >> 6), h = 8 * j + i;
  const int c = lane & 15, g = lane >> 4, pp = lane & 3;
  {
    v4u t;
    if (w == 0 || w == 3) {
#pragma unroll
      for (int d = 0; d < 4; ++d) {
        u32 v = 0;
#pragma unroll
        for (int e = 0; e < 2; ++e) {
          const int q = 2 * d + e;
          const bool on = w == 0 ? ((q >> 2) == (c >> 3) && ((4 * g + (q & 3)) & 7) == (c & 7)) : (q == (c & 7));
          v |= on ? 0x3F80u << (16 * e) : 0u;
        }
        t[d] = v;
      }
    } else if (w == 1) {
      t.x = (u32)(((g & 1) * 16 + c) * 8);
      t.y = (u32)(g * 128 + (c & 7) * 16 + (c >> 3) * 8);
      t.z = c < 8 ? (u32)(g * 128 + c * 16) : OOB;
      t.w = (u32)(g * 32768 + c * 16);
    } else {
      t.x = (u32)(g * 16384 + c * 16);
      t.y = g == 0 ? (u32)(c * 16) : OOB;
      t.z = (u32)((g >> 1) * 128 + (c & 7) * 16 + (2 * (g & 1) + (c >> 3)) * 32768);
      t.w = lane < 32 ? (u32)lane * 64 : OOB;
    }
    *(v4u*)(smem + 12288 + tid * 16) = t;
    asm volatile("s_waitcnt lgkmcnt(0)" ::: "memory");
  }
  const u32 wb = 576u * 4096 * 2 - h * 256;
  v16u sa = cat4(make_srd(p.ws, WS_END), make_srd(p.Wq + h * 128, wb), make_srd(p.Wk + h * 128, wb), make_srd(p.Wv + h * 128, wb));
  const v4u so = make_srd(p.Wo + h * 128 * 576, 128u * 1152);
  const int unit = w == 0 ? h : ((w == 1 && i == 0) ? 32 + j : -1);
  const u32 hot = HOT + L * 8192, hot2 = HOT2 + (1 - L) * 8192, img = hot2 + j * 2 * CHUNK;
  v16u sb = {so.x, so.y, so.z, so.w,
             pga(L, B_QKV + 3 * h + 1) + w * 2048, pga(L, B_KVM + h) + w * 1536, mbox_att(L, j, i, w), mbox(L, j, SL_REC + i, w),
             (u32)w * 1024, dbg + w * 64, INITF + L * 8192, hot + 8 * w * CHUNK + h * 256,
             unit < 0 ? OOB : hot + unit * CHUNK, unit < 0 ? OOB : hot2 + (unit >> 5) * CHUNK + (unit & 31) * 256, img, hot + (32 + w) * CHUNK + h * 256};
  const int jr = lane >> 3;
  const u32 row = (u32)(16 * (w >> 1) + 8 * ((jr >> 1) & 1) + 4 * (w & 1) + 2 * (jr >> 2) + (jr & 1));
  const u32 ch = (((u32)lane & 7) ^ wswz(jr, w)) << 4;
  v16u va = {0, 0, 0, 0, 0, 0, 0, 0, 0, 0, 0, 0, 0, 0, 0, 0};
  va[0] = lane * 16;
  va[1] = 16384 + w * 2048 + lane * 16;
  va[2] = (u32)((c >> 3) * 0xAAAA) | 0x4444u;
  const int qq = (lane >> 2) & 3;
  va[3] = (u32)((g >> 1) * 36864 + 32768 + (g & 1) * 2048 + (4 * (qq >> 1) + (qq & 1)) * 128 + ((2 * w + (pp >> 1)) << 4) + 8 * (pp & 1));
  va[4] = row * 8192 + ch;
  va[5] = row * 1152 + ch;
  va[6] = (u32)(w >> 1) * 4096 + tr_base(lane, 4 * (w & 1) + pp);
  va[7] = (u32)w * 8192 + tr_base(lane, 4 * (qq >> 1) + (pp >> 1)) + 8 * (pp & 1);
  va[8] = hot2 + 32 * CHUNK;
  va[9] = (u32)(h * 64 + 4 * w);
  va[10] = w >= 2 ? hot + h * CHUNK + (w - 2) * 4096 : OOB;
  va[11] = w == 3 ? hot + (32 + j) * CHUNK + i * 1024 : (w == 2 ? img + i * 1024 : (i == 1 ? img + CHUNK : OOB));
  va[12] = w == 3 ? ~0u : 0u;
  va[13] = w == 3 ? 0u : ~0u;
  va[14] = (u32)(2 * tag); va[15] = (u32)((2 * tag) >> 32);
  RUN_ASM(
    "s_nop 4\n"
    "v_mov_b32 v48, v8\n"
    "v_mov_b32 v49, v9\n"
    "v_mov_b32 v50, v10\n"
    "v_mov_b32 v51, v11\n"
    "v_mov_b32 v52, v12\n"
    "v_mov_b32 v53, v13\n"
    "v_readfirstlane_b32 s30, v14\n"
    "v_readfirstlane_b32 s31, v15\n"
    "s_memtime s[34:35]\n"
    "s_memrealtime s[92:93]\n"
    "s_mov_b32 s69, 262144\n"
    "s_add_u32 m0, s60, 16384\n"
    "s_mov_b32 s73, 0\n"
    "buffer_load_dwordx4 v5, s[52:55], s73 offen lds\n"
    "s_add_u32 m0, s60, 20480\n"
    "s_mov_b32 s73, 128\n"
    "buffer_load_dwordx4 v5, s[52:55], s73 offen lds\n"
    "s_add_u32 m0, s60, 24576\n"
    "s_mov_b32 s73, 256\n"
    "buffer_load_dwordx4 v5, s[52:55], s73 offen lds\n"
    "s_add_u32 m0, s60, 28672\n"
    "s_mov_b32 s73, 384\n"
    "buffer_load_dwordx4 v5, s[52:55], s73 offen lds\n"
    "s_add_u32 m0, s60, 32768\n"
    "s_mov_b32 s73, 512\n"
    "buffer_load_dwordx4 v5, s[52:55], s73 offen lds\n"
    "s_add_u32 m0, s60, 36864\n"
    "s_mov_b32 s73, 640\n"
    "buffer_load_dwordx4 v5, s[52:55], s73 offen lds\n"
    "s_add_u32 m0, s60, 40960\n"
    "s_mov_b32 s73, 768\n"
    "buffer_load_dwordx4 v5, s[52:55], s73 offen lds\n"
    "s_add_u32 m0, s60, 45056\n"
    "s_mov_b32 s73, 896\n"
    "buffer_load_dwordx4 v5, s[52:55], s73 offen lds\n"
    "s_add_u32 m0, s60, 49152\n"
    "s_mov_b32 s73, 1024\n"
    "buffer_load_dwordx4 v5, s[52:55], s73 offen lds\n"
    "s_add_u32 m0, s60, 53248\n"
    "s_mov_b32 s73, 36864\n"
    "buffer_load_dwordx4 v5, s[52:55], s73 offen lds\n"
    "s_add_u32 m0, s60, 57344\n"
    "s_mov_b32 s73, 36992\n"
    "buffer_load_dwordx4 v5, s[52:55], s73 offen lds\n"
    "s_add_u32 m0, s60, 61440\n"
    "s_mov_b32 s73, 37120\n"
    "buffer_load_dwordx4 v5, s[52:55], s73 offen lds\n"
    "s_add_u32 m0, s60, 65536\n"
    "s_mov_b32 s73, 37248\n"
    "buffer_load_dwordx4 v5, s[52:55], s73 offen lds\n"
    "s_add_u32 m0, s60, 69632\n"
    "s_mov_b32 s73, 37376\n"
    "buffer_load_dwordx4 v5, s[52:55], s73 offen lds\n"
    "s_add_u32 m0, s60, 73728\n"
    "s_mov_b32 s73, 37504\n"
    "buffer_load_dwordx4 v5, s[52:55], s73 offen lds\n"
    "s_add_u32 m0, s60, 77824\n"
    "s_mov_b32 s73, 37632\n"
    "buffer_load_dwordx4 v5, s[52:55], s73 offen lds\n"
    "s_add_u32 m0, s60, 81920\n"
    "s_mov_b32 s73, 37760\n"
    "buffer_load_dwordx4 v5, s[52:55], s73 offen lds\n"
    "s_add_u32 m0, s60, 86016\n"
    "s_mov_b32 s73, 37888\n"
    "buffer_load_dwordx4 v5, s[52:55], s73 offen lds\n"
    "s_add_u32 m0, s60, 90112\n"
    "s_mov_b32 s73, 73728\n"
    "buffer_load_dwordx4 v5, s[52:55], s73 offen lds\n"
    "s_add_u32 m0, s60, 94208\n"
    "s_mov_b32 s73, 73856\n"
    "buffer_load_dwordx4 v5, s[52:55], s73 offen lds\n"
    "s_add_u32 m0, s60, 98304\n"
    "s_mov_b32 s73, 73984\n"
    "buffer_load_dwordx4 v5, s[52:55], s73 offen lds\n"
    "s_add_u32 m0, s60, 102400\n"
    "s_mov_b32 s73, 74112\n"
    "buffer_load_dwordx4 v5, s[52:55], s73 offen lds\n"
    "s_add_u32 m0, s60, 106496\n"
    "s_mov_b32 s73, 74240\n"
    "buffer_load_dwordx4 v5, s[52:55], s73 offen lds\n"
    "s_add_u32 m0, s60, 110592\n"
    "s_mov_b32 s73, 74368\n"
    "buffer_load_dwordx4 v5, s[52:55], s73 offen lds\n"
    "s_add_u32 m0, s60, 114688\n"
    "s_mov_b32 s73, 74496\n"
    "buffer_load_dwordx4 v5, s[52:55], s73 offen lds\n"
    "s_add_u32 m0, s60, 118784\n"
    "s_mov_b32 s73, 74624\n"
    "buffer_load_dwordx4 v5, s[52:55], s73 offen lds\n"
    "s_add_u32 m0, s60, 122880\n"
    "s_mov_b32 s73, 74752\n"
    "buffer_load_dwordx4 v5, s[52:55], s73 offen lds\n"
    "s_add_u32 m0, s60, 126976\n"
    "s_mov_b32 s73, 110592\n"
    "buffer_load_dwordx4 v5, s[52:55], s73 offen lds\n"
    "s_add_u32 m0, s60, 131072\n"
    "s_mov_b32 s73, 110720\n"
    "buffer_load_dwordx4 v5, s[52:55], s73 offen lds\n"
    "s_add_u32 m0, s60, 135168\n"
    "s_mov_b32 s73, 110848\n"
    "buffer_load_dwordx4 v5, s[52:55], s73 offen lds\n"
    "s_add_u32 m0, s60, 139264\n"
    "s_mov_b32 s73, 110976\n"
    "buffer_load_dwordx4 v5, s[52:55], s73 offen lds\n"
    "s_add_u32 m0, s60, 143360\n"
    "s_mov_b32 s73, 111104\n"
    "buffer_load_dwordx4 v5, s[52:55], s73 offen lds\n"
    "s_add_u32 m0, s60, 147456\n"
    "s_mov_b32 s73, 111232\n"
    "buffer_load_dwordx4 v5, s[52:55], s73 offen lds\n"
    "s_add_u32 m0, s60, 151552\n"
    "s_mov_b32 s73, 111360\n"
    "buffer_load_dwordx4 v5, s[52:55], s73 offen lds\n"
    "s_add_u32 m0, s60, 155648\n"
    "s_mov_b32 s73, 111488\n"
    "buffer_load_dwordx4 v5, s[52:55], s73 offen lds\n"
    "s_add_u32 m0, s60, 159744\n"
    "s_mov_b32 s73, 111616\n"
    "buffer_load_dwordx4 v5, s[52:55], s73 offen lds\n"
    "s_waitcnt vmcnt(18) lgkmcnt(0)\n"
    "s_barrier\n"
    "v_add_u32 v8, 16384, v7\n"
    "v_xor_b32 v9, 32, v8\n"
    "v_xor_b32 v10, 320, v8\n"
    "v_xor_b32 v11, 32, v10\n"
    "v_add_u32 v12, 16384, v3\n"
    "v_xor_b32 v13, 320, v12\n"
    "ds_read_b64_tr_b16 v[112:113], v8 offset:0\n"
    "ds_read_b64_tr_b16 v[114:115], v8 offset:512\n"
    "ds_read_b64_tr_b16 v[116:117], v8 offset:36864\n"
    "ds_read_b64_tr_b16 v[118:119], v8 offset:37376\n"
    "ds_read_b64_tr_b16 v[144:145], v10 offset:0\n"
    "ds_read_b64_tr_b16 v[146:147], v10 offset:512\n"
    "ds_read_b64_tr_b16 v[148:149], v10 offset:36864\n"
    "ds_read_b64_tr_b16 v[150:151], v10 offset:37376\n"
    "s_waitcnt lgkmcnt(6)\n"
    "ds_read_b64_tr_b16 v[128:129], v9 offset:0\n"
    "ds_read_b64_tr_b16 v[130:131], v9 offset:512\n"
    "ds_read_b64_tr_b16 v[132:133], v9 offset:36864\n"
    "ds_read_b64_tr_b16 v[134:135], v9 offset:37376\n"
    "ds_read_b64_tr_b16 v[152:153], v11 offset:0\n"
    "ds_read_b64_tr_b16 v[154:155], v11 offset:512\n"
    "ds_read_b64_tr_b16 v[156:157], v11 offset:36864\n"
    "ds_read_b64_tr_b16 v[158:159], v11 offset:37376\n"
    "s_waitcnt lgkmcnt(6)\n"
    "ds_read_b64_tr_b16 v[160:161], v8 offset:4096\n"
    "ds_read_b64_tr_b16 v[162:163], v8 offset:4608\n"
    "ds_read_b64_tr_b16 v[164:165], v8 offset:40960\n"
    "ds_read_b64_tr_b16 v[166:167], v8 offset:41472\n"
    "ds_read_b64_tr_b16 v[176:177], v10 offset:4096\n"
    "ds_read_b64_tr_b16 v[178:179], v10 offset:4608\n"
    "ds_read_b64_tr_b16 v[180:181], v10 offset:40960\n"
    "ds_read_b64_tr_b16 v[182:183], v10 offset:41472\n"
    "s_waitcnt lgkmcnt(6)\n"
    "ds_read_b64_tr_b16 v[168:169], v9 offset:4096\n"
    "ds_read_b64_tr_b16 v[170:171], v9 offset:4608\n"
    "ds_read_b64_tr_b16 v[172:173], v9 offset:40960\n"
    "ds_read_b64_tr_b16 v[174:175], v9 offset:41472\n"
    "ds_read_b64_tr_b16 v[184:185], v11 offset:4096\n"
    "ds_read_b64_tr_b16 v[186:187], v11 offset:4608\n"
    "ds_read_b64_tr_b16 v[188:189], v11 offset:40960\n"
    "ds_read_b64_tr_b16 v[190:191], v11 offset:41472\n"
    "s_waitcnt lgkmcnt(6)\n"
    "v_xor_b32 v8, 32, v12\n"
    "v_xor_b32 v9, 32, v13\n"
    "ds_read_b64_tr_b16 v[192:193], v12 offset:0\n"
    "ds_read_b64_tr_b16 v[194:195], v8 offset:1024\n"
    "ds_read_b64_tr_b16 v[196:197], v13 offset:0\n"
    "ds_read_b64_tr_b16 v[198:199], v9 offset:1024\n"
    "s_waitcnt vmcnt(0) lgkmcnt(0)\n"
    "s_barrier\n"
    "s_add_u32 m0, s60, 16384\n"
    "s_mov_b32 s73, 0\n"
    "buffer_load_dwordx4 v4, s[40:43], s73 offen lds\n"
    "s_add_u32 m0, s60, 20480\n"
    "s_mov_b32 s73, 128\n"
    "buffer_load_dwordx4 v4, s[40:43], s73 offen lds\n"
    "s_add_u32 m0, s60, 24576\n"
    "s_mov_b32 s73, 0\n"
    "buffer_load_dwordx4 v4, s[44:47], s73 offen lds\n"
    "s_add_u32 m0, s60, 28672\n"
    "s_mov_b32 s73, 128\n"
    "buffer_load_dwordx4 v4, s[44:47], s73 offen lds\n"
    "s_add_u32 m0, s60, 32768\n"
    "s_mov_b32 s73, 262144\n"
    "buffer_load_dwordx4 v4, s[40:43], s73 offen lds\n"
    "s_add_u32 m0, s60, 36864\n"
    "s_mov_b32 s73, 262272\n"
    "buffer_load_dwordx4 v4, s[40:43], s73 offen lds\n"
    "s_add_u32 m0, s60, 40960\n"
    "s_mov_b32 s73, 262144\n"
    "buffer_load_dwordx4 v4, s[44:47], s73 offen lds\n"
    "s_add_u32 m0, s60, 45056\n"
    "s_mov_b32 s73, 262272\n"
    "buffer_load_dwordx4 v4, s[44:47], s73 offen lds\n"
    "s_add_u32 m0, s60, 49152\n"
    "s_mov_b32 s73, 524288\n"
    "buffer_load_dwordx4 v4, s[40:43], s73 offen lds\n"
    "s_add_u32 m0, s60, 53248\n"
    "s_mov_b32 s73, 524416\n"
    "buffer_load_dwordx4 v4, s[40:43], s73 offen lds\n"
    "s_add_u32 m0, s60, 57344\n"
    "s_mov_b32 s73, 524288\n"
    "buffer_load_dwordx4 v4, s[44:47], s73 offen lds\n"
    "s_add_u32 m0, s60, 61440\n"
    "s_mov_b32 s73, 524416\n"
    "buffer_load_dwordx4 v4, s[44:47], s73 offen lds\n"
    "s_add_u32 m0, s60, 65536\n"
    "s_mov_b32 s73, 786432\n"
    "buffer_load_dwordx4 v4, s[40:43], s73 offen lds\n"
    "s_add_u32 m0, s60, 69632\n"
    "s_mov_b32 s73, 786560\n"
    "buffer_load_dwordx4 v4, s[40:43], s73 offen lds\n"
    "s_add_u32 m0, s60, 73728\n"
    "s_mov_b32 s73, 786432\n"
    "buffer_load_dwordx4 v4, s[44:47], s73 offen lds\n"
    "s_add_u32 m0, s60, 77824\n"
    "s_mov_b32 s73, 786560\n"
    "buffer_load_dwordx4 v4, s[44:47], s73 offen lds\n"
    "v_add_u32 v8, 90112, v7\n"
    "v_xor_b32 v9, 32, v8\n"
    "v_xor_b32 v10, 320, v8\n"
    "v_xor_b32 v11, 32, v10\n"
    "v_add_u32 v12, 90112, v3\n"
    "v_xor_b32 v13, 320, v12\n"
    "ds_read_b64_tr_b16 v[120:121], v8 offset:0\n"
    "ds_read_b64_tr_b16 v[122:123], v8 offset:512\n"
    "ds_read_b64_tr_b16 v[124:125], v8 offset:36864\n"
    "ds_read_b64_tr_b16 v[126:127], v8 offset:37376\n"
    "ds_read_b64_tr_b16 v[208:209], v10 offset:0\n"
    "ds_read_b64_tr_b16 v[210:211], v10 offset:512\n"
    "ds_read_b64_tr_b16 v[212:213], v10 offset:36864\n"
    "ds_read_b64_tr_b16 v[214:215], v10 offset:37376\n"
    "s_waitcnt lgkmcnt(6)\n"
    "ds_read_b64_tr_b16 v[136:137], v9 offset:0\n"
    "ds_read_b64_tr_b16 v[138:139], v9 offset:512\n"
    "ds_read_b64_tr_b16 v[140:141], v9 offset:36864\n"
    "ds_read_b64_tr_b16 v[142:143], v9 offset:37376\n"
    "ds_read_b64_tr_b16 v[216:217], v11 offset:0\n"
    "ds_read_b64_tr_b16 v[218:219], v11 offset:512\n"
    "ds_read_b64_tr_b16 v[220:221], v11 offset:36864\n"
    "ds_read_b64_tr_b16 v[222:223], v11 offset:37376\n"
    "s_waitcnt lgkmcnt(6)\n"
    "ds_read_b64_tr_b16 v[224:225], v8 offset:4096\n"
    "ds_read_b64_tr_b16 v[226:227], v8 offset:4608\n"
    "ds_read_b64_tr_b16 v[228:229], v8 offset:40960\n"
    "ds_read_b64_tr_b16 v[230:231], v8 offset:41472\n"
    "ds_read_b64_tr_b16 v[240:241], v10 offset:4096\n"
    "ds_read_b64_tr_b16 v[242:243], v10 offset:4608\n"
    "ds_read_b64_tr_b16 v[244:245], v10 offset:40960\n"
    "ds_read_b64_tr_b16 v[246:247], v10 offset:41472\n"
    "s_waitcnt lgkmcnt(6)\n"
    "ds_read_b64_tr_b16 v[232:233], v9 offset:4096\n"
    "ds_read_b64_tr_b16 v[234:235], v9 offset:4608\n"
    "ds_read_b64_tr_b16 v[236:237], v9 offset:40960\n"
    "ds_read_b64_tr_b16 v[238:239], v9 offset:41472\n"
    "ds_read_b64_tr_b16 v[248:249], v11 offset:4096\n"
    "ds_read_b64_tr_b16 v[250:251], v11 offset:4608\n"
    "ds_read_b64_tr_b16 v[252:253], v11 offset:40960\n"
    "ds_read_b64_tr_b16 v[254:255], v11 offset:41472\n"
    "s_waitcnt lgkmcnt(6)\n"
    "v_xor_b32 v8, 32, v12\n"
    "v_xor_b32 v9, 32, v13\n"
    "ds_read_b64_tr_b16 v[200:201], v12 offset:0\n"
    "ds_read_b64_tr_b16 v[202:203], v8 offset:1024\n"
    "ds_read_b64_tr_b16 v[204:205], v13 offset:0\n"
    "ds_read_b64_tr_b16 v[206:207], v9 offset:1024\n"
    "s_waitcnt vmcnt(12) lgkmcnt(0)\n"
    "s_barrier\n"
    "s_add_u32 m0, s60, 81920\n"
    "s_mov_b32 s73, 1048576\n"
    "buffer_load_dwordx4 v4, s[40:43], s73 offen lds\n"
    "s_add_u32 m0, s60, 86016\n"
    "s_mov_b32 s73, 1048704\n"
    "buffer_load_dwordx4 v4, s[40:43], s73 offen lds\n"
    "s_add_u32 m0, s60, 90112\n"
    "s_mov_b32 s73, 1048576\n"
    "buffer_load_dwordx4 v4, s[44:47], s73 offen lds\n"
    "s_add_u32 m0, s60, 94208\n"
    "s_mov_b32 s73, 1048704\n"
    "buffer_load_dwordx4 v4, s[44:47], s73 offen lds\n"
    "s_add_u32 m0, s60, 98304\n"
    "s_mov_b32 s73, 1310720\n"
    "buffer_load_dwordx4 v4, s[40:43], s73 offen lds\n"
    "s_add_u32 m0, s60, 102400\n"
    "s_mov_b32 s73, 1310848\n"
    "buffer_load_dwordx4 v4, s[40:43], s73 offen lds\n"
    "s_add_u32 m0, s60, 106496\n"
    "s_mov_b32 s73, 1310720\n"
    "buffer_load_dwordx4 v4, s[44:47], s73 offen lds\n"
    "s_add_u32 m0, s60, 110592\n"
    "s_mov_b32 s73, 1310848\n"
    "buffer_load_dwordx4 v4, s[44:47], s73 offen lds\n"
    "s_add_u32 m0, s60, 114688\n"
    "s_mov_b32 s73, 1572864\n"
    "buffer_load_dwordx4 v4, s[40:43], s73 offen lds\n"
    "s_add_u32 m0, s60, 118784\n"
    "s_mov_b32 s73, 1572992\n"
    "buffer_load_dwordx4 v4, s[40:43], s73 offen lds\n"
    "s_add_u32 m0, s60, 122880\n"
    "s_mov_b32 s73, 1572864\n"
    "buffer_load_dwordx4 v4, s[44:47], s73 offen lds\n"
    "s_add_u32 m0, s60, 126976\n"
    "s_mov_b32 s73, 1572992\n"
    "buffer_load_dwordx4 v4, s[44:47], s73 offen lds\n"
    "s_add_u32 m0, s60, 131072\n"
    "s_mov_b32 s73, 1835008\n"
    "buffer_load_dwordx4 v4, s[40:43], s73 offen lds\n"
    "s_add_u32 m0, s60, 135168\n"
    "s_mov_b32 s73, 1835136\n"
    "buffer_load_dwordx4 v4, s[40:43], s73 offen lds\n"
    "s_add_u32 m0, s60, 139264\n"
    "s_mov_b32 s73, 1835008\n"
    "buffer_load_dwordx4 v4, s[44:47], s73 offen lds\n"
    "s_add_u32 m0, s60, 143360\n"
    "s_mov_b32 s73, 1835136\n"
    "buffer_load_dwordx4 v4, s[44:47], s73 offen lds\n"
    "s_add_u32 m0, s60, 147456\n"
    "s_mov_b32 s73, 2097152\n"
    "buffer_load_dwordx4 v4, s[40:43], s73 offen lds\n"
    "s_add_u32 m0, s60, 151552\n"
    "s_mov_b32 s73, 2097280\n"
    "buffer_load_dwordx4 v4, s[40:43], s73 offen lds\n"
    "s_add_u32 m0, s60, 155648\n"
    "s_mov_b32 s73, 2097152\n"
    "buffer_load_dwordx4 v4, s[44:47], s73 offen lds\n"
    "s_add_u32 m0, s60, 159744\n"
    "s_mov_b32 s73, 2097280\n"
    "buffer_load_dwordx4 v4, s[44:47], s73 offen lds\n"
    "v_add_u32 v8, 16384, v6\n"
    "ds_read_b64_tr_b16 a[0:1], v8 offset:0\n"
    "ds_read_b64_tr_b16 a[2:3], v8 offset:512\n"
    "ds_read_b64_tr_b16 a[8:9], v8 offset:8\n"
    "ds_read_b64_tr_b16 a[10:11], v8 offset:520\n"
    "ds_read_b64_tr_b16 a[144:145], v8 offset:8192\n"
    "ds_read_b64_tr_b16 a[146:147], v8 offset:8704\n"
    "ds_read_b64_tr_b16 a[152:153], v8 offset:8200\n"
    "ds_read_b64_tr_b16 a[154:155], v8 offset:8712\n"
    "s_waitcnt vmcnt(28) lgkmcnt(0)\n"
    "s_barrier\n"
    "s_add_u32 m0, s60, 16384\n"
    "s_mov_b32 s73, 2359296\n"
    "buffer_load_dwordx4 v4, s[40:43], s73 offen lds\n"
    "s_add_u32 m0, s60, 20480\n"
    "s_mov_b32 s73, 2359424\n"
    "buffer_load_dwordx4 v4, s[40:43], s73 offen lds\n"
    "s_add_u32 m0, s60, 24576\n"
    "s_mov_b32 s73, 2359296\n"
    "buffer_load_dwordx4 v4, s[44:47], s73 offen lds\n"
    "s_add_u32 m0, s60, 28672\n"
    "s_mov_b32 s73, 2359424\n"
    "buffer_load_dwordx4 v4, s[44:47], s73 offen lds\n"
    "v_add_u32 v8, 32768, v6\n"
    "ds_read_b64_tr_b16 a[4:5], v8 offset:0\n"
    "ds_read_b64_tr_b16 a[6:7], v8 offset:512\n"
    "ds_read_b64_tr_b16 a[12:13], v8 offset:8\n"
    "ds_read_b64_tr_b16 a[14:15], v8 offset:520\n"
    "ds_read_b64_tr_b16 a[148:149], v8 offset:8192\n"
    "ds_read_b64_tr_b16 a[150:151], v8 offset:8704\n"
    "ds_read_b64_tr_b16 a[156:157], v8 offset:8200\n"
    "ds_read_b64_tr_b16 a[158:159], v8 offset:8712\n"
    "s_waitcnt vmcnt(28) lgkmcnt(0)\n"
    "s_barrier\n"
    "s_add_u32 m0, s60, 32768\n"
    "s_mov_b32 s73, 2621440\n"
    "buffer_load_dwordx4 v4, s[40:43], s73 offen lds\n"
    "s_add_u32 m0, s60, 36864\n"
    "s_mov_b32 s73, 2621568\n"
    "buffer_load_dwordx4 v4, s[40:43], s73 offen lds\n"
    "s_add_u32 m0, s60, 40960\n"
    "s_mov_b32 s73, 2621440\n"
    "buffer_load_dwordx4 v4, s[44:47], s73 offen lds\n"
    "s_add_u32 m0, s60, 45056\n"
    "s_mov_b32 s73, 2621568\n"
    "buffer_load_dwordx4 v4, s[44:47], s73 offen lds\n"
    "v_add_u32 v8, 49152, v6\n"
    "ds_read_b64_tr_b16 a[16:17], v8 offset:0\n"
    "ds_read_b64_tr_b16 a[18:19], v8 offset:512\n"
    "ds_read_b64_tr_b16 a[24:25], v8 offset:8\n"
    "ds_read_b64_tr_b16 a[26:27], v8 offset:520\n"
    "ds_read_b64_tr_b16 a[160:161], v8 offset:8192\n"
    "ds_read_b64_tr_b16 a[162:163], v8 offset:8704\n"
    "ds_read_b64_tr_b16 a[168:169], v8 offset:8200\n"
    "ds_read_b64_tr_b16 a[170:171], v8 offset:8712\n"
    "s_waitcnt vmcnt(28) lgkmcnt(0)\n"
    "s_barrier\n"
    "s_add_u32 m0, s60, 49152\n"
    "s_mov_b32 s73, 2883584\n"
    "buffer_load_dwordx4 v4, s[40:43], s73 offen lds\n"
    "s_add_u32 m0, s60, 53248\n"
    "s_mov_b32 s73, 2883712\n"
    "buffer_load_dwordx4 v4, s[40:43], s73 offen lds\n"
    "s_add_u32 m0, s60, 57344\n"
    "s_mov_b32 s73, 2883584\n"
    "buffer_load_dwordx4 v4, s[44:47], s73 offen lds\n"
    "s_add_u32 m0, s60, 61440\n"
    "s_mov_b32 s73, 2883712\n"
    "buffer_load_dwordx4 v4, s[44:47], s73 offen lds\n"
    "v_add_u32 v8, 65536, v6\n"
    "ds_read_b64_tr_b16 a[20:21], v8 offset:0\n"
    "ds_read_b64_tr_b16 a[22:23], v8 offset:512\n"
    "ds_read_b64_tr_b16 a[28:29], v8 offset:8\n"
    "ds_read_b64_tr_b16 a[30:31], v8 offset:520\n"
    "ds_read_b64_tr_b16 a[164:165], v8 offset:8192\n"
    "ds_read_b64_tr_b16 a[166:167], v8 offset:8704\n"
    "ds_read_b64_tr_b16 a[172:173], v8 offset:8200\n"
    "ds_read_b64_tr_b16 a[174:175], v8 offset:8712\n"
    "s_waitcnt vmcnt(28) lgkmcnt(0)\n"
    "s_barrier\n"
    "s_add_u32 m0, s60, 65536\n"
    "s_mov_b32 s73, 3145728\n"
    "buffer_load_dwordx4 v4, s[40:43], s73 offen lds\n"
    "s_add_u32 m0, s60, 69632\n"
    "s_mov_b32 s73, 3145856\n"
    "buffer_load_dwordx4 v4, s[40:43], s73 offen lds\n"
    "s_add_u32 m0, s60, 73728\n"
    "s_mov_b32 s73, 3145728\n"
    "buffer_load_dwordx4 v4, s[44:47], s73 offen lds\n"
    "s_add_u32 m0, s60, 77824\n"
    "s_mov_b32 s73, 3145856\n"
    "buffer_load_dwordx4 v4, s[44:47], s73 offen lds\n"
    "v_add_u32 v8, 81920, v6\n"
    "ds_read_b64_tr_b16 a[32:33], v8 offset:0\n"
    "ds_read_b64_tr_b16 a[34:35], v8 offset:512\n"
    "ds_read_b64_tr_b16 a[40:41], v8 offset:8\n"
    "ds_read_b64_tr_b16 a[42:43], v8 offset:520\n"
    "ds_read_b64_tr_b16 a[176:177], v8 offset:8192\n"
    "ds_read_b64_tr_b16 a[178:179], v8 offset:8704\n"
    "ds_read_b64_tr_b16 a[184:185], v8 offset:8200\n"
    "ds_read_b64_tr_b16 a[186:187], v8 offset:8712\n"
    "s_waitcnt vmcnt(28) lgkmcnt(0)\n"
    "s_barrier\n"
    "s_add_u32 m0, s60, 81920\n"
    "s_mov_b32 s73, 3407872\n"
    "buffer_load_dwordx4 v4, s[40:43], s73 offen lds\n"
    "s_add_u32 m0, s60, 86016\n"
    "s_mov_b32 s73, 3408000\n"
    "buffer_load_dwordx4 v4, s[40:43], s73 offen lds\n"
    "s_add_u32 m0, s60, 90112\n"
    "s_mov_b32 s73, 3407872\n"
    "buffer_load_dwordx4 v4, s[44:47], s73 offen lds\n"
    "s_add_u32 m0, s60, 94208\n"
    "s_mov_b32 s73, 3408000\n"
    "buffer_load_dwordx4 v4, s[44:47], s73 offen lds\n"
    "v_add_u32 v8, 98304, v6\n"
    "ds_read_b64_tr_b16 a[36:37], v8 offset:0\n"
    "ds_read_b64_tr_b16 a[38:39], v8 offset:512\n"
    "ds_read_b64_tr_b16 a[44:45], v8 offset:8\n"
    "ds_read_b64_tr_b16 a[46:47], v8 offset:520\n"
    "ds_read_b64_tr_b16 a[180:181], v8 offset:8192\n"
    "ds_read_b64_tr_b16 a[182:183], v8 offset:8704\n"
    "ds_read_b64_tr_b16 a[188:189], v8 offset:8200\n"
    "ds_read_b64_tr_b16 a[190:191], v8 offset:8712\n"
    "s_waitcnt vmcnt(28) lgkmcnt(0)\n"
    "s_barrier\n"
    "s_add_u32 m0, s60, 98304\n"
    "s_mov_b32 s73, 3670016\n"
    "buffer_load_dwordx4 v4, s[40:43], s73 offen lds\n"
    "s_add_u32 m0, s60, 102400\n"
    "s_mov_b32 s73, 3670144\n"
    "buffer_load_dwordx4 v4, s[40:43], s73 offen lds\n"
    "s_add_u32 m0, s60, 106496\n"
    "s_mov_b32 s73, 3670016\n"
    "buffer_load_dwordx4 v4, s[44:47], s73 offen lds\n"
    "s_add_u32 m0, s60, 110592\n"
    "s_mov_b32 s73, 3670144\n"
    "buffer_load_dwordx4 v4, s[44:47], s73 offen lds\n"
    "v_add_u32 v8, 114688, v6\n"
    "ds_read_b64_tr_b16 a[48:49], v8 offset:0\n"
    "ds_read_b64_tr_b16 a[50:51], v8 offset:512\n"
    "ds_read_b64_tr_b16 a[56:57], v8 offset:8\n"
    "ds_read_b64_tr_b16 a[58:59], v8 offset:520\n"
    "ds_read_b64_tr_b16 a[192:193], v8 offset:8192\n"
    "ds_read_b64_tr_b16 a[194:195], v8 offset:8704\n"
    "ds_read_b64_tr_b16 a[200:201], v8 offset:8200\n"
    "ds_read_b64_tr_b16 a[202:203], v8 offset:8712\n"
    "s_waitcnt vmcnt(28) lgkmcnt(0)\n"
    "s_barrier\n"
    "s_add_u32 m0, s60, 114688\n"
    "s_mov_b32 s73, 3932160\n"
    "buffer_load_dwordx4 v4, s[40:43], s73 offen lds\n"
    "s_add_u32 m0, s60, 118784\n"
    "s_mov_b32 s73, 3932288\n"
    "buffer_load_dwordx4 v4, s[40:43], s73 offen lds\n"
    "s_add_u32 m0, s60, 122880\n"
    "s_mov_b32 s73, 3932160\n"
    "buffer_load_dwordx4 v4, s[44:47], s73 offen lds\n"
    "s_add_u32 m0, s60, 126976\n"
    "s_mov_b32 s73, 3932288\n"
    "buffer_load_dwordx4 v4, s[44:47], s73 offen lds\n"
    "v_add_u32 v8, 131072, v6\n"
    "ds_read_b64_tr_b16 a[52:53], v8 offset:0\n"
    "ds_read_b64_tr_b16 a[54:55], v8 offset:512\n"
    "ds_read_b64_tr_b16 a[60:61], v8 offset:8\n"
    "ds_read_b64_tr_b16 a[62:63], v8 offset:520\n"
    "ds_read_b64_tr_b16 a[196:197], v8 offset:8192\n"
    "ds_read_b64_tr_b16 a[198:199], v8 offset:8704\n"
    "ds_read_b64_tr_b16 a[204:205], v8 offset:8200\n"
    "ds_read_b64_tr_b16 a[206:207], v8 offset:8712\n"
    "s_waitcnt vmcnt(28) lgkmcnt(0)\n"
    "s_barrier\n"
    "s_add_u32 m0, s60, 131072\n"
    "s_mov_b32 s73, 4194304\n"
    "buffer_load_dwordx4 v4, s[40:43], s73 offen lds\n"
    "s_add_u32 m0, s60, 135168\n"
    "s_mov_b32 s73, 4194432\n"
    "buffer_load_dwordx4 v4, s[40:43], s73 offen lds\n"
    "s_add_u32 m0, s60, 139264\n"
    "s_mov_b32 s73, 4194304\n"
    "buffer_load_dwordx4 v4, s[44:47], s73 offen lds\n"
    "s_add_u32 m0, s60, 143360\n"
    "s_mov_b32 s73, 4194432\n"
    "buffer_load_dwordx4 v4, s[44:47], s73 offen lds\n"
    "v_add_u32 v8, 147456, v6\n"
    "ds_read_b64_tr_b16 a[64:65], v8 offset:0\n"
    "ds_read_b64_tr_b16 a[66:67], v8 offset:512\n"
    "ds_read_b64_tr_b16 a[72:73], v8 offset:8\n"
    "ds_read_b64_tr_b16 a[74:75], v8 offset:520\n"
    "ds_read_b64_tr_b16 a[208:209], v8 offset:8192\n"
    "ds_read_b64_tr_b16 a[210:211], v8 offset:8704\n"
    "ds_read_b64_tr_b16 a[216:217], v8 offset:8200\n"
    "ds_read_b64_tr_b16 a[218:219], v8 offset:8712\n"
    "s_waitcnt vmcnt(28) lgkmcnt(0)\n"
    "s_barrier\n"
    "s_add_u32 m0, s60, 147456\n"
    "s_mov_b32 s73, 4456448\n"
    "buffer_load_dwordx4 v4, s[40:43], s73 offen lds\n"
    "s_add_u32 m0, s60, 151552\n"
    "s_mov_b32 s73, 4456576\n"
    "buffer_load_dwordx4 v4, s[40:43], s73 offen lds\n"
    "s_add_u32 m0, s60, 155648\n"
    "s_mov_b32 s73, 4456448\n"
    "buffer_load_dwordx4 v4, s[44:47], s73 offen lds\n"
    "s_add_u32 m0, s60, 159744\n"
    "s_mov_b32 s73, 4456576\n"
    "buffer_load_dwordx4 v4, s[44:47], s73 offen lds\n"
    "v_add_u32 v8, 16384, v6\n"
    "ds_read_b64_tr_b16 a[68:69], v8 offset:0\n"
    "ds_read_b64_tr_b16 a[70:71], v8 offset:512\n"
    "ds_read_b64_tr_b16 a[76:77], v8 offset:8\n"
    "ds_read_b64_tr_b16 a[78:79], v8 offset:520\n"
    "ds_read_b64_tr_b16 a[212:213], v8 offset:8192\n"
    "ds_read_b64_tr_b16 a[214:215], v8 offset:8704\n"
    "ds_read_b64_tr_b16 a[220:221], v8 offset:8200\n"
    "ds_read_b64_tr_b16 a[222:223], v8 offset:8712\n"
    "s_waitcnt vmcnt(28) lgkmcnt(0)\n"
    "s_barrier\n"
    "s_add_u32 m0, s60, 16384\n"
    "s_mov_b32 s73, 0\n"
    "buffer_load_dwordx4 v4, s[48:51], s73 offen lds\n"
    "s_add_u32 m0, s60, 20480\n"
    "s_mov_b32 s73, 128\n"
    "buffer_load_dwordx4 v4, s[48:51], s73 offen lds\n"
    "s_add_u32 m0, s60, 24576\n"
    "s_mov_b32 s73, 262144\n"
    "buffer_load_dwordx4 v4, s[48:51], s73 offen lds\n"
    "s_add_u32 m0, s60, 28672\n"
    "s_mov_b32 s73, 262272\n"
    "buffer_load_dwordx4 v4, s[48:51], s73 offen lds\n"
    "v_add_u32 v8, 32768, v6\n"
    "ds_read_b64_tr_b16 a[80:81], v8 offset:0\n"
    "ds_read_b64_tr_b16 a[82:83], v8 offset:512\n"
    "ds_read_b64_tr_b16 a[88:89], v8 offset:8\n"
    "ds_read_b64_tr_b16 a[90:91], v8 offset:520\n"
    "ds_read_b64_tr_b16 a[224:225], v8 offset:8192\n"
    "ds_read_b64_tr_b16 a[226:227], v8 offset:8704\n"
    "ds_read_b64_tr_b16 a[232:233], v8 offset:8200\n"
    "ds_read_b64_tr_b16 a[234:235], v8 offset:8712\n"
    "s_waitcnt vmcnt(28) lgkmcnt(0)\n"
    "s_barrier\n"
    "s_add_u32 m0, s60, 32768\n"
    "s_mov_b32 s73, 524288\n"
    "buffer_load_dwordx4 v4, s[48:51], s73 offen lds\n"
    "s_add_u32 m0, s60, 36864\n"
    "s_mov_b32 s73, 524416\n"
    "buffer_load_dwordx4 v4, s[48:51], s73 offen lds\n"
    "s_add_u32 m0, s60, 40960\n"
    "s_mov_b32 s73, 786432\n"
    "buffer_load_dwordx4 v4, s[48:51], s73 offen lds\n"
    "s_add_u32 m0, s60, 45056\n"
    "s_mov_b32 s73, 786560\n"
    "buffer_load_dwordx4 v4, s[48:51], s73 offen lds\n"
    "v_add_u32 v8, 49152, v6\n"
    "ds_read_b64_tr_b16 a[84:85], v8 offset:0\n"
    "ds_read_b64_tr_b16 a[86:87], v8 offset:512\n"
    "ds_read_b64_tr_b16 a[92:93], v8 offset:8\n"
    "ds_read_b64_tr_b16 a[94:95], v8 offset:520\n"
    "ds_read_b64_tr_b16 a[228:229], v8 offset:8192\n"
    "ds_read_b64_tr_b16 a[230:231], v8 offset:8704\n"
    "ds_read_b64_tr_b16 a[236:237], v8 offset:8200\n"
    "ds_read_b64_tr_b16 a[238:239], v8 offset:8712\n"
    "s_waitcnt vmcnt(28) lgkmcnt(0)\n"
    "s_barrier\n"
    "s_add_u32 m0, s60, 49152\n"
    "s_mov_b32 s73, 1048576\n"
    "buffer_load_dwordx4 v4, s[48:51], s73 offen lds\n"
    "s_add_u32 m0, s60, 53248\n"
    "s_mov_b32 s73, 1048704\n"
    "buffer_load_dwordx4 v4, s[48:51], s73 offen lds\n"
    "s_add_u32 m0, s60, 57344\n"
    "s_mov_b32 s73, 1310720\n"
    "buffer_load_dwordx4 v4, s[48:51], s73 offen lds\n"
    "s_add_u32 m0, s60, 61440\n"
    "s_mov_b32 s73, 1310848\n"
    "buffer_load_dwordx4 v4, s[48:51], s73 offen lds\n"
    "v_add_u32 v8, 65536, v6\n"
    "ds_read_b64_tr_b16 a[96:97], v8 offset:0\n"
    "ds_read_b64_tr_b16 a[98:99], v8 offset:512\n"
    "ds_read_b64_tr_b16 a[104:105], v8 offset:8\n"
    "ds_read_b64_tr_b16 a[106:107], v8 offset:520\n"
    "ds_read_b64_tr_b16 a[240:241], v8 offset:8192\n"
    "ds_read_b64_tr_b16 a[242:243], v8 offset:8704\n"
    "ds_read_b64_tr_b16 a[248:249], v8 offset:8200\n"
    "ds_read_b64_tr_b16 a[250:251], v8 offset:8712\n"
    "s_waitcnt vmcnt(28) lgkmcnt(0)\n"
    "s_barrier\n"
    "s_add_u32 m0, s60, 65536\n"
    "s_mov_b32 s73, 1572864\n"
    "buffer_load_dwordx4 v4, s[48:51], s73 offen lds\n"
    "s_add_u32 m0, s60, 69632\n"
    "s_mov_b32 s73, 1572992\n"
    "buffer_load_dwordx4 v4, s[48:51], s73 offen lds\n"
    "s_add_u32 m0, s60, 73728\n"
    "s_mov_b32 s73, 1835008\n"
    "buffer_load_dwordx4 v4, s[48:51], s73 offen lds\n"
    "s_add_u32 m0, s60, 77824\n"
    "s_mov_b32 s73, 1835136\n"
    "buffer_load_dwordx4 v4, s[48:51], s73 offen lds\n"
    "v_add_u32 v8, 81920, v6\n"
    "ds_read_b64_tr_b16 a[100:101], v8 offset:0\n"
    "ds_read_b64_tr_b16 a[102:103], v8 offset:512\n"
    "ds_read_b64_tr_b16 a[108:109], v8 offset:8\n"
    "ds_read_b64_tr_b16 a[110:111], v8 offset:520\n"
    "ds_read_b64_tr_b16 a[244:245], v8 offset:8192\n"
    "ds_read_b64_tr_b16 a[246:247], v8 offset:8704\n"
    "ds_read_b64_tr_b16 a[252:253], v8 offset:8200\n"
    "ds_read_b64_tr_b16 a[254:255], v8 offset:8712\n"
    "s_waitcnt vmcnt(28) lgkmcnt(0)\n"
    "s_barrier\n"
    "s_add_u32 m0, s60, 81920\n"
    "s_mov_b32 s73, 2097152\n"
    "buffer_load_dwordx4 v4, s[48:51], s73 offen lds\n"
    "s_add_u32 m0, s60, 86016\n"
    "s_mov_b32 s73, 2097280\n"
    "buffer_load_dwordx4 v4, s[48:51], s73 offen lds\n"
    "s_add_u32 m0, s60, 90112\n"
    "s_mov_b32 s73, 2359296\n"
    "buffer_load_dwordx4 v4, s[48:51], s73 offen lds\n"
    "s_add_u32 m0, s60, 94208\n"
    "s_mov_b32 s73, 2359424\n"
    "buffer_load_dwordx4 v4, s[48:51], s73 offen lds\n"
    "v_add_u32 v8, 98304, v6\n"
    "ds_read_b64_tr_b16 a[112:113], v8 offset:0\n"
    "ds_read_b64_tr_b16 a[114:115], v8 offset:512\n"
    "ds_read_b64_tr_b16 a[120:121], v8 offset:8\n"
    "ds_read_b64_tr_b16 a[122:123], v8 offset:520\n"
    "ds_read_b64_tr_b16 v[80:81], v8 offset:8192\n"
    "ds_read_b64_tr_b16 v[82:83], v8 offset:8704\n"
    "ds_read_b64_tr_b16 v[88:89], v8 offset:8200\n"
    "ds_read_b64_tr_b16 v[90:91], v8 offset:8712\n"
    "s_waitcnt vmcnt(28) lgkmcnt(0)\n"
    "s_barrier\n"
    "s_add_u32 m0, s60, 98304\n"
    "s_mov_b32 s73, 2621440\n"
    "buffer_load_dwordx4 v4, s[48:51], s73 offen lds\n"
    "s_add_u32 m0, s60, 102400\n"
    "s_mov_b32 s73, 2621568\n"
    "buffer_load_dwordx4 v4, s[48:51], s73 offen lds\n"
    "s_add_u32 m0, s60, 106496\n"
    "s_mov_b32 s73, 2883584\n"
    "buffer_load_dwordx4 v4, s[48:51], s73 offen lds\n"
    "s_add_u32 m0, s60, 110592\n"
    "s_mov_b32 s73, 2883712\n"
    "buffer_load_dwordx4 v4, s[48:51], s73 offen lds\n"
    "v_add_u32 v8, 114688, v6\n"
    "ds_read_b64_tr_b16 a[116:117], v8 offset:0\n"
    "ds_read_b64_tr_b16 a[118:119], v8 offset:512\n"
    "ds_read_b64_tr_b16 a[124:125], v8 offset:8\n"
    "ds_read_b64_tr_b16 a[126:127], v8 offset:520\n"
    "ds_read_b64_tr_b16 v[84:85], v8 offset:8192\n"
    "ds_read_b64_tr_b16 v[86:87], v8 offset:8704\n"
    "ds_read_b64_tr_b16 v[92:93], v8 offset:8200\n"
    "ds_read_b64_tr_b16 v[94:95], v8 offset:8712\n"
    "s_waitcnt vmcnt(28) lgkmcnt(0)\n"
    "s_barrier\n"
    "s_add_u32 m0, s60, 114688\n"
    "s_mov_b32 s73, 3145728\n"
    "buffer_load_dwordx4 v4, s[48:51], s73 offen lds\n"
    "s_add_u32 m0, s60, 118784\n"
    "s_mov_b32 s73, 3145856\n"
    "buffer_load_dwordx4 v4, s[48:51], s73 offen lds\n"
    "s_add_u32 m0, s60, 122880\n"
    "s_mov_b32 s73, 3407872\n"
    "buffer_load_dwordx4 v4, s[48:51], s73 offen lds\n"
    "s_add_u32 m0, s60, 126976\n"
    "s_mov_b32 s73, 3408000\n"
    "buffer_load_dwordx4 v4, s[48:51], s73 offen lds\n"
    "v_add_u32 v8, 131072, v6\n"
    "ds_read_b64_tr_b16 a[128:129], v8 offset:0\n"
    "ds_read_b64_tr_b16 a[130:131], v8 offset:512\n"
    "ds_read_b64_tr_b16 a[136:137], v8 offset:8\n"
    "ds_read_b64_tr_b16 a[138:139], v8 offset:520\n"
    "ds_read_b64_tr_b16 v[96:97], v8 offset:8192\n"
    "ds_read_b64_tr_b16 v[98:99], v8 offset:8704\n"
    "ds_read_b64_tr_b16 v[104:105], v8 offset:8200\n"
    "ds_read_b64_tr_b16 v[106:107], v8 offset:8712\n"
    "s_waitcnt vmcnt(28) lgkmcnt(0)\n"
    "s_barrier\n"
    "s_add_u32 m0, s60, 131072\n"
    "s_mov_b32 s73, 3670016\n"
    "buffer_load_dwordx4 v4, s[48:51], s73 offen lds\n"
    "s_add_u32 m0, s60, 135168\n"
    "s_mov_b32 s73, 3670144\n"
    "buffer_load_dwordx4 v4, s[48:51], s73 offen lds\n"
    "s_add_u32 m0, s60, 139264\n"
    "s_mov_b32 s73, 3932160\n"
    "buffer_load_dwordx4 v4, s[48:51], s73 offen lds\n"
    "s_add_u32 m0, s60, 143360\n"
    "s_mov_b32 s73, 3932288\n"
    "buffer_load_dwordx4 v4, s[48:51], s73 offen lds\n"
    "v_add_u32 v8, 147456, v6\n"
    "ds_read_b64_tr_b16 a[132:133], v8 offset:0\n"
    "ds_read_b64_tr_b16 a[134:135], v8 offset:512\n"
    "ds_read_b64_tr_b16 a[140:141], v8 offset:8\n"
    "ds_read_b64_tr_b16 a[142:143], v8 offset:520\n"
    "ds_read_b64_tr_b16 v[100:101], v8 offset:8192\n"
    "ds_read_b64_tr_b16 v[102:103], v8 offset:8704\n"
    "ds_read_b64_tr_b16 v[108:109], v8 offset:8200\n"
    "ds_read_b64_tr_b16 v[110:111], v8 offset:8712\n"
    "s_waitcnt vmcnt(28) lgkmcnt(0)\n"
    "s_barrier\n"
    "s_add_u32 m0, s60, 147456\n"
    "s_mov_b32 s73, 4194304\n"
    "buffer_load_dwordx4 v4, s[48:51], s73 offen lds\n"
    "s_add_u32 m0, s60, 151552\n"
    "s_mov_b32 s73, 4194432\n"
    "buffer_load_dwordx4 v4, s[48:51], s73 offen lds\n"
    "s_add_u32 m0, s60, 155648\n"
    "s_mov_b32 s73, 4456448\n"
    "buffer_load_dwordx4 v4, s[48:51], s73 offen lds\n"
    "s_add_u32 m0, s60, 159744\n"
    "s_mov_b32 s73, 4456576\n"
    "buffer_load_dwordx4 v4, s[48:51], s73 offen lds\n"
    "buffer_load_dwordx2 v[54:55], off, s[36:39], 0 sc1\n"
    "v_add_u32 v8, 16384, v6\n"
    "ds_read_b64_tr_b16 v[16:17], v8 offset:0\n"
    "ds_read_b64_tr_b16 v[18:19], v8 offset:512\n"
    "ds_read_b64_tr_b16 v[24:25], v8 offset:8\n"
    "ds_read_b64_tr_b16 v[26:27], v8 offset:520\n"
    "ds_read_b64_tr_b16 v[20:21], v8 offset:8192\n"
    "ds_read_b64_tr_b16 v[22:23], v8 offset:8704\n"
    "ds_read_b64_tr_b16 v[28:29], v8 offset:8200\n"
    "ds_read_b64_tr_b16 v[30:31], v8 offset:8712\n"
    "s_waitcnt vmcnt(29) lgkmcnt(0)\n"
    "s_barrier\n"
    "v_add_u32 v14, 0, v1\n"
    "ds_write_b128 v14, v[112:115] offset:0\n"
    "ds_write_b128 v14, v[116:119] offset:1024\n"
    "ds_write_b128 v14, v[24:27] offset:8192\n"
    "ds_write_b128 v14, v[28:31] offset:9216\n"
    "v_add_u32 v8, 32768, v6\n"
    "ds_read_b64_tr_b16 v[32:33], v8 offset:0\n"
    "ds_read_b64_tr_b16 v[34:35], v8 offset:512\n"
    "ds_read_b64_tr_b16 v[40:41], v8 offset:8\n"
    "ds_read_b64_tr_b16 v[42:43], v8 offset:520\n"
    "ds_read_b64_tr_b16 v[36:37], v8 offset:8192\n"
    "ds_read_b64_tr_b16 v[38:39], v8 offset:8704\n"
    "ds_read_b64_tr_b16 v[44:45], v8 offset:8200\n"
    "ds_read_b64_tr_b16 v[46:47], v8 offset:8712\n"
    "s_waitcnt vmcnt(25) lgkmcnt(0)\n"
    "s_barrier\n"
    "v_mov_b64 v[112:113], v[16:17]\n"
    "v_mov_b64 v[114:115], v[18:19]\n"
    "v_mov_b64 v[116:117], v[20:21]\n"
    "v_mov_b64 v[118:119], v[22:23]\n"
    "v_add_u32 v14, 16384, v1\n"
    "ds_write_b128 v14, v[120:123] offset:0\n"
    "ds_write_b128 v14, v[124:127] offset:1024\n"
    "ds_write_b128 v14, v[40:43] offset:8192\n"
    "ds_write_b128 v14, v[44:47] offset:9216\n"
    "v_add_u32 v8, 49152, v6\n"
    "ds_read_b64_tr_b16 v[16:17], v8 offset:0\n"
    "ds_read_b64_tr_b16 v[18:19], v8 offset:512\n"
    "ds_read_b64_tr_b16 v[24:25], v8 offset:8\n"
    "ds_read_b64_tr_b16 v[26:27], v8 offset:520\n"
    "ds_read_b64_tr_b16 v[20:21], v8 offset:8192\n"
    "ds_read_b64_tr_b16 v[22:23], v8 offset:8704\n"
    "ds_read_b64_tr_b16 v[28:29], v8 offset:8200\n"
    "ds_read_b64_tr_b16 v[30:31], v8 offset:8712\n"
    "s_waitcnt vmcnt(21) lgkmcnt(0)\n"
    "s_barrier\n"
    "v_mov_b64 v[120:121], v[32:33]\n"
    "v_mov_b64 v[122:123], v[34:35]\n"
    "v_mov_b64 v[124:125], v[36:37]\n"
    "v_mov_b64 v[126:127], v[38:39]\n"
    "v_add_u32 v14, 32768, v1\n"
    "ds_write_b128 v14, v[128:131] offset:0\n"
    "ds_write_b128 v14, v[132:135] offset:1024\n"
    "ds_write_b128 v14, v[24:27] offset:8192\n"
    "ds_write_b128 v14, v[28:31] offset:9216\n"
    "v_add_u32 v8, 65536, v6\n"
    "ds_read_b64_tr_b16 v[32:33], v8 offset:0\n"
    "ds_read_b64_tr_b16 v[34:35], v8 offset:512\n"
    "ds_read_b64_tr_b16 v[40:41], v8 offset:8\n"
    "ds_read_b64_tr_b16 v[42:43], v8 offset:520\n"
    "ds_read_b64_tr_b16 v[36:37], v8 offset:8192\n"
    "ds_read_b64_tr_b16 v[38:39], v8 offset:8704\n"
    "ds_read_b64_tr_b16 v[44:45], v8 offset:8200\n"
    "ds_read_b64_tr_b16 v[46:47], v8 offset:8712\n"
    "s_waitcnt vmcnt(17) lgkmcnt(0)\n"
    "s_barrier\n"
    "v_mov_b64 v[128:129], v[16:17]\n"
    "v_mov_b64 v[130:131], v[18:19]\n"
    "v_mov_b64 v[132:133], v[20:21]\n"
    "v_mov_b64 v[134:135], v[22:23]\n"
    "v_add_u32 v14, 49152, v1\n"
    "ds_write_b128 v14, v[136:139] offset:0\n"
    "ds_write_b128 v14, v[140:143] offset:1024\n"
    "ds_write_b128 v14, v[40:43] offset:8192\n"
    "ds_write_b128 v14, v[44:47] offset:9216\n"
    "v_add_u32 v8, 81920, v6\n"
    "ds_read_b64_tr_b16 v[16:17], v8 offset:0\n"
    "ds_read_b64_tr_b16 v[18:19], v8 offset:512\n"
    "ds_read_b64_tr_b16 v[24:25], v8 offset:8\n"
    "ds_read_b64_tr_b16 v[26:27], v8 offset:520\n"
    "ds_read_b64_tr_b16 v[20:21], v8 offset:8192\n"
    "ds_read_b64_tr_b16 v[22:23], v8 offset:8704\n"
    "ds_read_b64_tr_b16 v[28:29], v8 offset:8200\n"
    "ds_read_b64_tr_b16 v[30:31], v8 offset:8712\n"
    "s_waitcnt vmcnt(13) lgkmcnt(0)\n"
    "s_barrier\n"
    "v_mov_b64 v[136:137], v[32:33]\n"
    "v_mov_b64 v[138:139], v[34:35]\n"
    "v_mov_b64 v[140:141], v[36:37]\n"
    "v_mov_b64 v[142:143], v[38:39]\n"
    "v_add_u32 v14, 65536, v1\n"
    "ds_write_b128 v14, v[144:147] offset:0\n"
    "ds_write_b128 v14, v[148:151] offset:1024\n"
    "ds_write_b128 v14, v[24:27] offset:8192\n"
    "ds_write_b128 v14, v[28:31] offset:9216\n"
    "v_add_u32 v8, 98304, v6\n"
    "ds_read_b64_tr_b16 v[32:33], v8 offset:0\n"
    "ds_read_b64_tr_b16 v[34:35], v8 offset:512\n"
    "ds_read_b64_tr_b16 v[40:41], v8 offset:8\n"
    "ds_read_b64_tr_b16 v[42:43], v8 offset:520\n"
    "ds_read_b64_tr_b16 v[36:37], v8 offset:8192\n"
    "ds_read_b64_tr_b16 v[38:39], v8 offset:8704\n"
    "ds_read_b64_tr_b16 v[44:45], v8 offset:8200\n"
    "ds_read_b64_tr_b16 v[46:47], v8 offset:8712\n"
    "s_waitcnt vmcnt(9) lgkmcnt(0)\n"
    "s_barrier\n"
    "v_mov_b64 v[144:145], v[16:17]\n"
    "v_mov_b64 v[146:147], v[18:19]\n"
    "v_mov_b64 v[148:149], v[20:21]\n"
    "v_mov_b64 v[150:151], v[22:23]\n"
    "v_add_u32 v14, 81920, v1\n"
    "ds_write_b128 v14, v[152:155] offset:0\n"
    "ds_write_b128 v14, v[156:159] offset:1024\n"
    "ds_write_b128 v14, v[40:43] offset:8192\n"
    "ds_write_b128 v14, v[44:47] offset:9216\n"
    "v_add_u32 v8, 114688, v6\n"
    "ds_read_b64_tr_b16 v[16:17], v8 offset:0\n"
    "ds_read_b64_tr_b16 v[18:19], v8 offset:512\n"
    "ds_read_b64_tr_b16 v[24:25], v8 offset:8\n"
    "ds_read_b64_tr_b16 v[26:27], v8 offset:520\n"
    "ds_read_b64_tr_b16 v[20:21], v8 offset:8192\n"
    "ds_read_b64_tr_b16 v[22:23], v8 offset:8704\n"
    "ds_read_b64_tr_b16 v[28:29], v8 offset:8200\n"
    "ds_read_b64_tr_b16 v[30:31], v8 offset:8712\n"
    "s_waitcnt vmcnt(5) lgkmcnt(0)\n"
    "s_barrier\n"
    "v_mov_b64 v[152:153], v[32:33]\n"
    "v_mov_b64 v[154:155], v[34:35]\n"
    "v_mov_b64 v[156:157], v[36:37]\n"
    "v_mov_b64 v[158:159], v[38:39]\n"
    "v_add_u32 v14, 98304, v1\n"
    "ds_write_b128 v14, v[160:163] offset:0\n"
    "ds_write_b128 v14, v[164:167] offset:1024\n"
    "ds_write_b128 v14, v[168:171] offset:8192\n"
    "ds_write_b128 v14, v[172:175] offset:9216\n"
    "v_add_u32 v8, 131072, v6\n"
    "ds_read_b64_tr_b16 v[32:33], v8 offset:0\n"
    "ds_read_b64_tr_b16 v[34:35], v8 offset:512\n"
    "ds_read_b64_tr_b16 v[40:41], v8 offset:8\n"
    "ds_read_b64_tr_b16 v[42:43], v8 offset:520\n"
    "ds_read_b64_tr_b16 v[36:37], v8 offset:8192\n"
    "ds_read_b64_tr_b16 v[38:39], v8 offset:8704\n"
    "ds_read_b64_tr_b16 v[44:45], v8 offset:8200\n"
    "ds_read_b64_tr_b16 v[46:47], v8 offset:8712\n"
    "s_waitcnt vmcnt(0) lgkmcnt(0)\n"
    "s_barrier\n"
    ".Lrec_par_%=:\n"
    "v_readfirstlane_b32 s73, v54\n"
    "v_readfirstlane_b32 s74, v55\n"
    "s_xor_b32 s73, s73, s30\n"
    "s_xor_b32 s74, s74, s31\n"
    "s_cselect_b32 s74, 2, 0\n"
    "s_or_b32 s74, s74, s73\n"
    "s_cmp_lt_u32 s74, 2\n"
    "s_cbranch_scc1 .Lrec_par1_%=\n"
    "s_sleep 4\n"
    "buffer_load_dwordx2 v[54:55], off, s[36:39], 0 sc1\n"
    "s_waitcnt vmcnt(0)\n"
    "s_sub_u32 s69, s69, 1\n"
    "s_cbranch_scc0 .Lrec_par_%=\n"
    "s_mov_b32 s69, 0\n"
    ".Lrec_par1_%=:\n"
    "s_and_b32 s73, s73, 1\n"
    "s_lshl_b32 s73, s73, 13\n"
    "s_add_u32 s36, s36, s73\n"
    "s_addc_u32 s37, s37, 0\n"
    "v_lshlrev_b32 v56, 3, v0\n"
    "v_mov_b32 v57, 0x80000000\n"
    "v_cmp_gt_u32 vcc, 192, v0\n"
    "s_nop 1\n"
    "v_cndmask_b32 v56, v57, v56, vcc\n"
    "v_mov_b32 v58, -1\n"
    "buffer_load_dword v57, v56, s[36:39], s62 offen sc1\n"
    "s_mov_b64 exec, 1\n"
    "buffer_atomic_smax v58, off, s[36:39], s59 sc0 sc1\n"
    "s_mov_b64 exec, -1\n"
    "v_mov_b64 v[160:161], v[16:17]\n"
    "v_mov_b64 v[162:163], v[18:19]\n"
    "v_mov_b64 v[164:165], v[20:21]\n"
    "v_mov_b64 v[166:167], v[22:23]\n"
    "v_mov_b64 v[168:169], v[24:25]\n"
    "v_mov_b64 v[170:171], v[26:27]\n"
    "v_mov_b64 v[172:173], v[28:29]\n"
    "v_mov_b64 v[174:175], v[30:31]\n"
    "v_add_u32 v14, 114688, v1\n"
    "ds_write_b128 v14, v[176:179] offset:0\n"
    "ds_write_b128 v14, v[180:183] offset:1024\n"
    "ds_write_b128 v14, v[184:187] offset:8192\n"
    "ds_write_b128 v14, v[188:191] offset:9216\n"
    "v_add_u32 v8, 147456, v6\n"
    "ds_read_b64_tr_b16 v[16:17], v8 offset:0\n"
    "ds_read_b64_tr_b16 v[18:19], v8 offset:512\n"
    "ds_read_b64_tr_b16 v[24:25], v8 offset:8\n"
    "ds_read_b64_tr_b16 v[26:27], v8 offset:520\n"
    "ds_read_b64_tr_b16 v[20:21], v8 offset:8192\n"
    "ds_read_b64_tr_b16 v[22:23], v8 offset:8704\n"
    "ds_read_b64_tr_b16 v[28:29], v8 offset:8200\n"
    "ds_read_b64_tr_b16 v[30:31], v8 offset:8712\n"
    "s_waitcnt lgkmcnt(0)\n"
    "s_barrier\n"
    "v_mov_b64 v[176:177], v[32:33]\n"
    "v_mov_b64 v[178:179], v[34:35]\n"
    "v_mov_b64 v[180:181], v[36:37]\n"
    "v_mov_b64 v[182:183], v[38:39]\n"
    "v_mov_b64 v[184:185], v[40:41]\n"
    "v_mov_b64 v[186:187], v[42:43]\n"
    "v_mov_b64 v[188:189], v[44:45]\n"
    "v_mov_b64 v[190:191], v[46:47]\n"
    "v_add_u32 v14, 131072, v1\n"
    "ds_write_b128 v14, v[192:195] offset:0\n"
    "ds_write_b128 v14, v[196:199] offset:1024\n"
    "ds_write_b128 v14, v[200:203] offset:8192\n"
    "ds_write_b128 v14, v[204:207] offset:9216\n"
    "s_waitcnt lgkmcnt(0)\n"
    "v_mov_b64 v[192:193], v[16:17]\n"
    "v_mov_b64 v[194:195], v[18:19]\n"
    "v_mov_b64 v[196:197], v[20:21]\n"
    "v_mov_b64 v[198:199], v[22:23]\n"
    "v_mov_b64 v[200:201], v[24:25]\n"
    "v_mov_b64 v[202:203], v[26:27]\n"
    "v_mov_b64 v[204:205], v[28:29]\n"
    "v_mov_b64 v[206:207], v[30:31]\n"
    "s_memtime s[32:33]\n"
    "s_memrealtime s[28:29]\n"
    "s_mov_b32 s84, 0x3e0293ee\n"
    "s_waitcnt vmcnt(0)\n"
    ".Lrec_init_%=:\n"
    "v_cmp_gt_i32 vcc, 0, v57\n"
    "s_cbranch_vccz .Lrec_init1_%=\n"
    "s_sleep 2\n"
    "buffer_load_dword v57, v56, s[36:39], s62 offen sc1\n"
    "s_waitcnt vmcnt(0)\n"
    "s_sub_u32 s69, s69, 1\n"
    "s_cbranch_scc0 .Lrec_init_%=\n"
    "s_mov_b32 s69, 0\n"
    ".Lrec_init1_%=:\n"
    "v_readfirstlane_b32 s81, v58\n"
    "s_mov_b32 s75, s56\n"
    "buffer_load_dwordx4 v[64:67], v0, s[36:39], s75 offen sc1\n"
    "buffer_load_dwordx4 v[68:71], v0, s[36:39], s75 offen offset:1024 sc1\n"
    "s_cmp_ge_i32 s81, 0\n"
    "s_cselect_b32 s81, 1, 0\n"
    "v_readfirstlane_b32 s40, v48\n"
    "v_readfirstlane_b32 s41, v49\n"
    "v_readfirstlane_b32 s42, v50\n"
    "v_readfirstlane_b32 s43, v51\n"
    "v_readfirstlane_b32 s44, v52\n"
    "v_readfirstlane_b32 s45, v53\n"
    "s_mov_b32 s82, 1179648\n"
    "s_waitcnt lgkmcnt(0)\n"
    "s_mov_b32 s46, s34\n"
    "s_lshl_b32 s47, s92, 2\n"
    "s_mov_b32 s33, s32\n"
    "s_lshl_b32 s28, s28, 2\n"
    "s_sub_u32 s46, s33, s46\n"
    "s_sub_u32 s47, s28, s47\n"
    "v_cvt_f32_u32 v78, s46\n"
    "v_cvt_f32_u32 v79, s47\n"
    "v_rcp_f32 v79, v79\n"
    "s_mov_b32 s46, s33\n"
    "s_mov_b32 s47, s28\n"
    "v_mul_f32 v78, v78, v79\n"
    "v_mov_b32 v77, 0x40c80000\n"
    "v_med3_f32 v78, v78, 4.0, v77\n"
    "v_rcp_f32 v79, v78\n"
    "v_mul_f32 v78, 0x43800000, v78\n"
    "v_mul_f32 v79, 0x44b54000, v79\n"
    "v_cvt_u32_f32 v78, v78\n"
    "v_cvt_u32_f32 v79, v79\n"
    "v_readfirstlane_b32 s48, v78\n"
    "v_readfirstlane_b32 s49, v79\n"
    "s_mov_b32 s52, 1120\n"
    "s_mov_b32 s53, 0\n"
    "s_mov_b32 s54, 0x7fffffff\n"
    "s_mov_b32 s55, 0\n"
    "s_mov_b32 s68, -1\n"
    "s_mov_b32 s50, s82\n"
    "s_add_u32 s82, s82, 589824\n"
    "s_cmp_eq_u32 s82, 1769472\n"
    "s_cselect_b32 s82, 0, s82\n"
    "s_and_b32 s80, s68, 3\n"
    "s_lshl_b32 s80, s80, 17\n"
    "s_and_b32 s80, s80, s45\n"
    "s_and_b32 s91, s50, s44\n"
    "s_add_u32 s80, s80, s91\n"
    "s_add_u32 s80, s80, s43\n"
    "s_add_u32 s89, s42, s50\n"
    "s_add_u32 s68, s68, 1\n"
    "s_and_b32 s70, s68, 3\n"
    "s_lshl_b32 s70, s70, 17\n"
    "s_add_u32 s70, s70, s66\n"
    "s_add_u32 s71, s70, 4096\n"
    "s_add_u32 s72, s70, 16384\n"
    "s_lshr_b32 s90, s68, 2\n"
    "s_lshl_b32 s90, s90, 14\n"
    "s_and_b32 s91, s68, 3\n"
    "s_lshl_b32 s91, s91, 11\n"
    "s_add_u32 s90, s90, s91\n"
    "s_add_u32 s90, s90, s40\n"
    "s_add_u32 s51, s90, s41\n"
    "s_and_b32 s76, s68, 7\n"
    "s_mul_i32 s76, s76, 4194304\n"
    "s_add_u32 s83, s76, s56\n"
    "s_add_u32 s83, s83, 16384\n"
    "s_add_u32 s76, s76, s57\n"
    "s_add_u32 s77, s63, s82\n"
    "s_add_u32 s86, s77, 65536\n"
    "s_add_u32 s87, s67, s82\n"
    "v_mov_b64 v[40:41], 0\n"
    "v_mov_b64 v[42:43], 0\n"
    "v_mov_b64 v[44:45], 0\n"
    "v_mov_b64 v[46:47], 0\n"
    "v_mov_b64 v[48:49], 0\n"
    "v_mov_b64 v[50:51], 0\n"
    "v_mov_b64 v[52:53], 0\n"
    "v_mov_b64 v[54:55], 0\n"
    "v_mov_b64 v[56:57], 0\n"
    "v_mov_b64 v[58:59], 0\n"
    "v_mov_b64 v[60:61], 0\n"
    "v_mov_b64 v[62:63], 0\n"
    "s_waitcnt lgkmcnt(0)\n"
    "s_barrier\n"
    "s_branch .Lrec_entry_%=\n"
    ".Lrec_LOOPB_%=:\n"
    ".Lrec_top_%=:\n"
    "buffer_load_dwordx4 v[4:7], v0, s[36:39], s70 offen sc1\n"
    "s_nop 3\n"
    "buffer_load_dwordx4 v[8:11], v0, s[36:39], s70 offen offset:1024 sc1\n"
    "s_nop 3\n"
    "buffer_load_dwordx4 v[12:15], v0, s[36:39], s70 offen offset:2048 sc1\n"
    "s_nop 3\n"
    "buffer_load_dwordx4 v[16:19], v0, s[36:39], s70 offen offset:3072 sc1\n"
    "s_nop 3\n"
    "buffer_load_dwordx4 v[20:23], v0, s[36:39], s71 offen sc1\n"
    "s_nop 3\n"
    "buffer_load_dwordx4 v[24:27], v0, s[36:39], s71 offen offset:1024 sc1\n"
    "s_nop 3\n"
    "buffer_load_dwordx4 v[28:31], v0, s[36:39], s71 offen offset:2048 sc1\n"
    "s_nop 3\n"
    "buffer_load_dwordx4 v[32:35], v0, s[36:39], s71 offen offset:3072 sc1\n"
    "s_sleep 1\n"
    "buffer_load_dwordx4 v[36:39], v0, s[36:39], s72 offen sc1\n"
    "s_waitcnt vmcnt(8)\n"
    ".Lrec_start_%=:\n"
    "v_smfmac_f32_16x16x64_bf16 v[40:43], v[4:7], a[0:7], v2\n"
    "v_max3_u32 v3, v4, v5, v6\n"
    "v_smfmac_f32_16x16x64_bf16 v[44:47], v[4:7], a[8:15], v2\n"
    "v_max_u32 v3, v3, v7\n"
    "v_smfmac_f32_16x16x64_bf16 v[48:51], v[4:7], a[144:151], v2\n"
    "v_cmp_eq_u32 vcc, -1, v3\n"
    "v_smfmac_f32_16x16x64_bf16 v[52:55], v[4:7], a[152:159], v2\n"
    "v_smfmac_f32_16x16x64_bf16 v[56:59], v[4:7], v[112:119], v2\n"
    "s_waitcnt vmcnt(7)\n"
    "s_waitcnt lgkmcnt(0)\n"
    "v_smfmac_f32_16x16x64_bf16 v[60:63], v[4:7], v[72:79], v2\n"
    "ds_read_b128 v[72:75], v1 offset:24576\n"
    "v_smfmac_f32_16x16x64_bf16 v[40:43], v[8:11], a[16:23], v2\n"
    "ds_read_b128 v[76:79], v1 offset:25600\n"
    "v_max3_u32 v3, v3, v8, v9\n"
    "v_smfmac_f32_16x16x64_bf16 v[44:47], v[8:11], a[24:31], v2\n"
    "s_cbranch_vccnz .Lrec_retry0_%=\n"
    ".Lrec_back0_%=:\n"
    "v_max3_u32 v3, v3, v10, v11\n"
    "v_smfmac_f32_16x16x64_bf16 v[48:51], v[8:11], a[160:167], v2\n"
    "v_smfmac_f32_16x16x64_bf16 v[52:55], v[8:11], a[168:175], v2\n"
    "v_smfmac_f32_16x16x64_bf16 v[56:59], v[8:11], v[120:127], v2\n"
    "s_waitcnt vmcnt(6)\n"
    "s_waitcnt lgkmcnt(0)\n"
    "v_smfmac_f32_16x16x64_bf16 v[60:63], v[8:11], v[72:79], v2\n"
    "ds_read_b128 v[72:75], v1 offset:40960\n"
    "v_smfmac_f32_16x16x64_bf16 v[40:43], v[12:15], a[32:39], v2\n"
    "ds_read_b128 v[76:79], v1 offset:41984\n"
    "v_max3_u32 v3, v3, v12, v13\n"
    "v_smfmac_f32_16x16x64_bf16 v[44:47], v[12:15], a[40:47], v2\n"
    "buffer_load_dwordx4 v[4:7], v0, s[36:39], s83 offen sc1\n"
    "v_max3_u32 v3, v3, v14, v15\n"
    "v_smfmac_f32_16x16x64_bf16 v[48:51], v[12:15], a[176:183], v2\n"
    "v_smfmac_f32_16x16x64_bf16 v[52:55], v[12:15], a[184:191], v2\n"
    "v_smfmac_f32_16x16x64_bf16 v[56:59], v[12:15], v[128:135], v2\n"
    "s_waitcnt vmcnt(6)\n"
    "s_waitcnt lgkmcnt(0)\n"
    "v_max3_u32 v3, v3, v16, v17\n"
    "v_smfmac_f32_16x16x64_bf16 v[60:63], v[12:15], v[72:79], v2\n"
    "ds_read_b128 v[72:75], v1 offset:57344\n"
    "v_max3_u32 v3, v3, v18, v19\n"
    "v_smfmac_f32_16x16x64_bf16 v[40:43], v[16:19], a[48:55], v2\n"
    "ds_read_b128 v[76:79], v1 offset:58368\n"
    "v_cmp_eq_u32 vcc, -1, v3\n"
    "v_smfmac_f32_16x16x64_bf16 v[44:47], v[16:19], a[56:63], v2\n"
    "v_add_u32 v1, 65536, v1\n"
    "v_smfmac_f32_16x16x64_bf16 v[48:51], v[16:19], a[192:199], v2\n"
    "s_cbranch_vccnz .Lrec_retry1_%=\n"
    ".Lrec_back1_%=:\n"
    "v_smfmac_f32_16x16x64_bf16 v[52:55], v[16:19], a[200:207], v2\n"
    "s_waitcnt vmcnt(5)\n"
    "s_waitcnt lgkmcnt(0)\n"
    "v_max3_u32 v3, v3, v20, v21\n"
    "v_smfmac_f32_16x16x64_bf16 v[60:63], v[16:19], v[72:79], v2\n"
    "ds_read_b128 v[72:75], v1 offset:8192\n"
    "v_max3_u32 v3, v3, v22, v23\n"
    "v_smfmac_f32_16x16x64_bf16 v[40:43], v[20:23], a[64:71], v2\n"
    "ds_read_b128 v[76:79], v1 offset:9216\n"
    "v_smfmac_f32_16x16x64_bf16 v[44:47], v[20:23], a[72:79], v2\n"
    "ds_read_b128 v[12:15], v0 offset:12288\n"
    "v_smfmac_f32_16x16x64_bf16 v[48:51], v[20:23], a[208:215], v2\n"
    "buffer_load_dwordx4 v[8:11], v0, s[36:39], s83 offen offset:1024 sc1\n"
    "v_smfmac_f32_16x16x64_bf16 v[52:55], v[20:23], a[216:223], v2\n"
    "s_waitcnt vmcnt(5)\n"
    "s_waitcnt lgkmcnt(1)\n"
    "v_max3_u32 v3, v3, v24, v25\n"
    "v_smfmac_f32_16x16x64_bf16 v[60:63], v[20:23], v[72:79], v2\n"
    "ds_read_b128 v[72:75], v1 offset:24576\n"
    "v_max3_u32 v3, v3, v26, v27\n"
    "v_smfmac_f32_16x16x64_bf16 v[40:43], v[24:27], a[80:87], v2\n"
    "ds_read_b128 v[76:79], v1 offset:25600\n"
    "v_subrev_u32 v1, 65536, v1\n"
    "v_smfmac_f32_16x16x64_bf16 v[44:47], v[24:27], a[88:95], v2\n"
    "s_waitcnt vmcnt(4)\n"
    "v_max3_u32 v3, v3, v28, v29\n"
    "v_smfmac_f32_16x16x64_bf16 v[48:51], v[24:27], a[224:231], v2\n"
    "s_waitcnt vmcnt(3)\n"
    "v_max3_u32 v3, v3, v30, v31\n"
    "v_smfmac_f32_16x16x64_bf16 v[52:55], v[24:27], a[232:239], v2\n"
    "s_waitcnt lgkmcnt(0)\n"
    "v_max3_u32 v3, v3, v32, v33\n"
    "v_smfmac_f32_16x16x64_bf16 v[60:63], v[24:27], v[72:79], v2\n"
    "ds_read_b128 v[72:75], v0 offset:13312\n"
    "v_max3_u32 v3, v3, v34, v35\n"
    "v_smfmac_f32_16x16x64_bf16 v[40:43], v[28:31], a[96:103], v2\n"
    "s_waitcnt vmcnt(2)\n"
    "v_max3_u32 v3, v3, v36, v37\n"
    "v_smfmac_f32_16x16x64_bf16 v[44:47], v[28:31], a[104:111], v2\n"
    "s_lshr_b32 s73, s60, 1\n"
    "v_max3_u32 v3, v3, v38, v39\n"
    "v_smfmac_f32_16x16x64_bf16 v[40:43], v[32:35], a[112:119], v2\n"
    "v_cmp_eq_u32 vcc, -1, v3\n"
    "v_smfmac_f32_16x16x64_bf16 v[44:47], v[32:35], a[120:127], v2\n"
    "s_waitcnt lgkmcnt(0)\n"
    "s_memrealtime s[28:29]\n"
    "v_add_u32 v79, s60, v0\n"
    "v_smfmac_f32_16x16x64_bf16 v[48:51], v[28:31], a[240:247], v2\n"
    "s_cbranch_vccnz .Lrec_retry2_%=\n"
    ".Lrec_back2_%=:\n"
    "v_xor_b32 v76, 1024, v79\n"
    "v_smfmac_f32_16x16x64_bf16 v[40:43], v[36:39], a[128:135], v2\n"
    "s_memrealtime s[30:31]\n"
    "v_xor_b32 v77, 2048, v79\n"
    "v_smfmac_f32_16x16x64_bf16 v[44:47], v[36:39], a[136:143], v2\n"
    "s_memrealtime s[96:97]\n"
    "v_xor_b32 v78, 3072, v79\n"
    "v_smfmac_f32_16x16x64_bf16 v[52:55], v[28:31], a[248:255], v2\n"
    "s_memtime s[32:33]\n"
    "v_cvt_pk_bf16_f32 v40, v40, v41\n"
    "v_smfmac_f32_16x16x64_bf16 v[48:51], v[32:35], v[80:87], v2\n"
    "s_memrealtime s[98:99]\n"
    "v_cvt_pk_bf16_f32 v41, v42, v43\n"
    "v_smfmac_f32_16x16x64_bf16 v[52:55], v[32:35], v[88:95], v2\n"
    "v_cvt_pk_bf16_f32 v42, v44, v45\n"
    "v_smfmac_f32_16x16x64_bf16 v[48:51], v[36:39], v[96:103], v2\n"
    "v_cvt_pk_bf16_f32 v43, v46, v47\n"
    "v_smfmac_f32_16x16x64_bf16 v[52:55], v[36:39], v[104:111], v2\n"
    "v_lshrrev_b32 v3, 1, v79\n"
    "v_mfma_f32_16x16x32_bf16 v[44:47], v[40:43], v[12:15], 0\n"
    "s_waitcnt lgkmcnt(0)\n"
    "v_cvt_pk_bf16_f32 v48, v48, v49\n"
    "v_cvt_pk_bf16_f32 v49, v50, v51\n"
    "s_add_u32 s28, s28, s30\n"
    "v_cvt_pk_bf16_f32 v50, v52, v53\n"
    "v_cvt_pk_bf16_f32 v51, v54, v55\n"
    "s_add_u32 s96, s96, s98\n"
    "s_add_u32 s28, s28, s96\n"
    "v_mfma_f32_16x16x32_bf16 v[52:55], v[48:51], v[12:15], 0\n"
    "s_add_u32 s30, s28, s49\n"
    "v_cvt_pk_bf16_f32 v40, v44, v45\n"
    "v_cvt_pk_bf16_f32 v41, v46, v47\n"
    "v_mov_b32 v75, s30\n"
    "v_mov_b32_dpp v42, v40 row_ror:8 row_mask:0xf bank_mask:0xf\n"
    "v_mov_b32_dpp v43, v41 row_ror:8 row_mask:0xf bank_mask:0xf\n"
    "buffer_store_dword v75, off, s[36:39], s51 sc1\n"
    "s_nop 0\n"
    "v_mfma_f32_16x16x32_bf16 v[44:47], v[68:71], v[40:43], 0\n"
    "v_cvt_pk_bf16_f32 v48, v52, v53\n"
    "v_cvt_pk_bf16_f32 v49, v54, v55\n"
    "v_mfma_f32_16x16x32_bf16 v[68:71], v[64:67], v[40:43], 0\n"
    "v_mov_b32_dpp v50, v48 row_ror:8 row_mask:0xf bank_mask:0xf\n"
    "v_mov_b32_dpp v51, v49 row_ror:8 row_mask:0xf bank_mask:0xf\n"
    "s_nop 1\n"
    "v_mfma_f32_16x16x32_bf16 v[52:55], v[48:51], v[40:43], 0\n"
    "s_nop 2\n"
    "v_mov_b32_dpp v68, v44 row_ror:8 row_mask:0xf bank_mask:0xc\n"
    "v_mov_b32_dpp v69, v45 row_ror:8 row_mask:0xf bank_mask:0xc\n"
    "v_mov_b32_dpp v70, v46 row_ror:8 row_mask:0xf bank_mask:0xc\n"
    "v_mov_b32_dpp v71, v47 row_ror:8 row_mask:0xf bank_mask:0xc\n"
    "ds_write_b128 v79, v[68:71] offset:0\n"
    "v_mov_b32_dpp v52, v54 row_ror:8 row_mask:0xf bank_mask:0xc\n"
    "v_mov_b32_dpp v53, v55 row_ror:8 row_mask:0xf bank_mask:0xc\n"
    "ds_write_b64 v3, v[52:53] offset:4096\n"
    "v_smfmac_f32_16x16x64_bf16 v[56:59], v[16:19], v[136:143], v2\n"
    "buffer_store_dwordx4 v[48:51], v74, s[36:39], s76 offen sc1\n"
    "v_smfmac_f32_16x16x64_bf16 v[56:59], v[20:23], v[144:151], v2\n"
    "ds_read_b128 v[16:19], v1 offset:0\n"
    "v_smfmac_f32_16x16x64_bf16 v[56:59], v[24:27], v[152:159], v2\n"
    "ds_read_b128 v[20:23], v1 offset:1024\n"
    "v_smfmac_f32_16x16x64_bf16 v[56:59], v[28:31], v[160:167], v2\n"
    "s_waitcnt lgkmcnt(2)\n"
    "s_barrier\n"
    "ds_read_b128 v[64:67], v76 offset:0\n"
    "v_smfmac_f32_16x16x64_bf16 v[60:63], v[28:31], v[168:175], v2\n"
    "ds_read_b128 v[40:43], v77 offset:0\n"
    "v_smfmac_f32_16x16x64_bf16 v[56:59], v[32:35], v[176:183], v2\n"
    "ds_read_b128 v[44:47], v78 offset:0\n"
    "v_smfmac_f32_16x16x64_bf16 v[60:63], v[32:35], v[184:191], v2\n"
    "ds_read_b64 v[12:13], v72 offset:4096\n"
    "ds_read_b64 v[14:15], v72 offset:4608\n"
    "v_smfmac_f32_16x16x64_bf16 v[56:59], v[36:39], v[192:199], v2\n"
    "ds_read_b64 v[52:53], v72 offset:5120\n"
    "ds_read_b64 v[54:55], v72 offset:5632\n"
    "v_smfmac_f32_16x16x64_bf16 v[60:63], v[36:39], v[200:207], v2\n"
    "ds_read_b128 v[24:27], v1 offset:16384\n"
    "s_cmp_eq_u32 s68, 63\n"
    "s_waitcnt lgkmcnt(5)\n"
    "v_add_f32 v68, v68, v64\n"
    "v_add_f32 v69, v69, v65\n"
    "v_add_f32 v70, v70, v66\n"
    "v_add_f32 v71, v71, v67\n"
    "v_pk_add_f32 v[40:41], v[40:41], v[44:45]\n"
    "v_pk_add_f32 v[42:43], v[42:43], v[46:47]\n"
    "s_waitcnt lgkmcnt(1)\n"
    "v_pk_add_f32 v[12:13], v[12:13], v[14:15]\n"
    "v_pk_add_f32 v[52:53], v[52:53], v[54:55]\n"
    "v_pk_add_f32 v[68:69], v[68:69], v[40:41]\n"
    "v_pk_add_f32 v[70:71], v[70:71], v[42:43]\n"
    "v_pk_add_f32 v[12:13], v[12:13], v[52:53]\n"
    "v_max3_f32 v46, v68, v69, v70\n"
    "v_max3_f32 v47, v71, v12, v13\n"
    "v_max_f32 v3, v46, v47\n"
    "v_cvt_pk_bf16_f32 v56, v56, v57\n"
    "v_cvt_pk_bf16_f32 v57, v58, v59\n"
    "v_max_f32_dpp v46, v3, v3 row_ror:8 row_mask:0xf bank_mask:0xf\n"
    "v_max_f32_dpp v47, v3, v3 row_ror:8 row_mask:0xf bank_mask:0xf\n"
    "v_cvt_pk_bf16_f32 v58, v60, v61\n"
    "v_cvt_pk_bf16_f32 v59, v62, v63\n"
    "v_permlane16_swap_b32 v46, v47\n"
    "v_max_f32 v3, v46, v47\n"
    "v_max_f32 v46, v46, v47\n"
    "v_add_u32 v14, s73, v73\n"
    "v_xor_b32 v15, 128, v0\n"
    "v_permlane32_swap_b32 v3, v46\n"
    "v_max_f32 v3, v3, v46\n"
    "v_mul_f32 v46, 0xbe0293ee, v3\n"
    "v_pk_fma_f32 v[68:69], v[68:69], s[84:85], v[46:47] op_sel_hi:[1,0,0]\n"
    "v_pk_fma_f32 v[70:71], v[70:71], s[84:85], v[46:47] op_sel_hi:[1,0,0]\n"
    "v_pk_fma_f32 v[12:13], v[12:13], s[84:85], v[46:47] op_sel_hi:[1,0,0]\n"
    "v_exp_f32 v68, v68\n"
    "v_exp_f32 v69, v69\n"
    "v_exp_f32 v70, v70\n"
    "v_exp_f32 v71, v71\n"
    "v_exp_f32 v12, v12\n"
    "v_cvt_pk_bf16_f32 v40, v68, v69\n"
    "v_cvt_pk_bf16_f32 v41, v70, v71\n"
    "v_exp_f32 v13, v13\n"
    "v_mov_b32_dpp v42, v40 row_ror:8 row_mask:0xf bank_mask:0xf\n"
    "v_mov_b32_dpp v43, v41 row_ror:8 row_mask:0xf bank_mask:0xf\n"
    "v_cvt_pk_bf16_f32 v44, v12, v13\n"
    "v_pk_add_f32 v[68:69], v[68:69], v[70:71]\n"
    "s_waitcnt vmcnt(2)\n"
    "v_mfma_f32_16x16x32_bf16 v[48:51], v[4:7], v[40:43], 0\n"
    "ds_read_b128 v[28:31], v1 offset:17408\n"
    "v_mov_b32_dpp v45, v44 row_ror:8 row_mask:0xf bank_mask:0xf\n"
    "v_add_f32 v3, v12, v13\n"
    "v_mfma_f32_16x16x32_bf16 v[52:55], v[8:11], v[40:43], 0\n"
    "s_cbranch_scc1 .Lrec_fin_%=\n"
    "v_add_f32 v46, v68, v69\n"
    "v_fma_f32 v3, v3, 0.5, v46\n"
    "v_mfma_f32_16x16x16_bf16 v[48:51], v[56:57], v[44:45], v[48:51]\n"
    "ds_read_b128 v[32:35], v1 offset:32768\n"
    "v_add_f32_dpp v46, v3, v3 row_ror:8 row_mask:0xf bank_mask:0xf\n"
    "v_add_f32_dpp v47, v3, v3 row_ror:8 row_mask:0xf bank_mask:0xf\n"
    "v_mfma_f32_16x16x16_bf16 v[52:55], v[58:59], v[44:45], v[52:55]\n"
    "ds_read_b128 v[36:39], v1 offset:33792\n"
    "v_permlane16_swap_b32 v46, v47\n"
    "v_add_f32 v3, v46, v47\n"
    "v_add_f32 v46, v46, v47\n"
    "v_mov_b64 v[64:65], 0\n"
    "v_mov_b64 v[66:67], 0\n"
    "v_permlane32_swap_b32 v3, v46\n"
    "v_add_f32 v3, v3, v46\n"
    "v_rcp_f32 v46, v3\n"
    "v_mov_b32_dpp v48, v52 row_ror:8 row_mask:0xf bank_mask:0xc\n"
    "v_mov_b32_dpp v49, v53 row_ror:8 row_mask:0xf bank_mask:0xc\n"
    "v_mov_b32_dpp v50, v54 row_ror:8 row_mask:0xf bank_mask:0xc\n"
    "v_mov_b32_dpp v51, v55 row_ror:8 row_mask:0xf bank_mask:0xc\n"
    "v_pk_mul_f32 v[48:49], v[48:49], v[46:47] op_sel_hi:[1,0]\n"
    "v_pk_mul_f32 v[50:51], v[50:51], v[46:47] op_sel_hi:[1,0]\n"
    "v_cvt_pk_bf16_f32 v48, v48, v49\n"
    "v_cvt_pk_bf16_f32 v49, v50, v51\n"
    "ds_write_b64 v14, v[48:49] offset:8192\n"
    "buffer_store_dwordx4 v[56:59], v0, s[36:39], s76 offen offset:512 sc1\n"
    "ds_read_b128 v[72:75], v1 offset:49152\n"
    "ds_read_b128 v[76:79], v1 offset:50176\n"
    "v_add_u32 v1, 65536, v1\n"
    "ds_read_b128 v[4:7], v1 offset:0\n"
    "ds_read_b128 v[8:11], v1 offset:1024\n"
    "v_lshlrev_b32 v12, 2, v0\n"
    "s_waitcnt lgkmcnt(4)\n"
    "s_barrier\n"
    "ds_read_b128 v[40:43], v0 offset:8192\n"
    "ds_read_b128 v[44:47], v0 offset:9216\n"
    "ds_read_b128 v[48:51], v15 offset:8192\n"
    "ds_read_b128 v[52:55], v15 offset:9216\n"
    "v_mov_b64 v[56:57], 0\n"
    "v_mov_b64 v[58:59], 0\n"
    "v_mov_b64 v[60:61], 0\n"
    "v_mov_b64 v[62:63], 0\n"
    "s_mov_b32 exec_hi, 0\n"
    "buffer_load_dwordx4 v[12:15], v12, s[36:39], s90 offen sc1\n"
    "s_mov_b32 exec_hi, -1\n"
    "s_waitcnt lgkmcnt(3)\n"
    "v_smfmac_f32_16x16x64_bf16 v[56:59], v[40:43], v[16:23], v2\n"
    "ds_read_b128 v[16:19], v1 offset:16384\n"
    "s_waitcnt lgkmcnt(3)\n"
    "v_smfmac_f32_16x16x64_bf16 v[56:59], v[44:47], v[24:31], v2\n"
    "ds_read_b128 v[20:23], v1 offset:17408\n"
    "v_mov_b64 v[68:69], 0\n"
    "v_smfmac_f32_16x16x64_bf16 v[60:63], v[40:43], v[32:39], v2\n"
    "ds_read_b128 v[24:27], v1 offset:32768\n"
    "v_mov_b64 v[70:71], 0\n"
    "v_smfmac_f32_16x16x64_bf16 v[60:63], v[44:47], v[72:79], v2\n"
    "ds_read_b128 v[28:31], v1 offset:33792\n"
    "s_waitcnt lgkmcnt(5)\n"
    "v_smfmac_f32_16x16x64_bf16 v[56:59], v[48:51], v[4:11], v2\n"
    "ds_read_b128 v[32:35], v1 offset:40960\n"
    "s_waitcnt lgkmcnt(5)\n"
    "v_smfmac_f32_16x16x64_bf16 v[56:59], v[52:55], v[208:215], v2\n"
    "ds_read_b128 v[36:39], v1 offset:41984\n"
    "s_waitcnt lgkmcnt(4)\n"
    "v_smfmac_f32_16x16x64_bf16 v[60:63], v[48:51], v[16:23], v2\n"
    "ds_read_b128 v[72:75], v1 offset:49152\n"
    "v_mov_b32 v3, -1\n"
    "v_smfmac_f32_16x16x64_bf16 v[60:63], v[52:55], v[216:223], v2\n"
    "ds_read_b128 v[76:79], v1 offset:50176\n"
    "ds_read_b128 v[8:11], v0 offset:14336\n"
    "s_waitcnt lgkmcnt(5)\n"
    "v_smfmac_f32_16x16x64_bf16 v[64:67], v[40:43], v[24:31], v2\n"
    "ds_read_b128 v[16:19], v1 offset:57344\n"
    "v_cvt_pk_bf16_f32 v56, v56, v57\n"
    "v_smfmac_f32_16x16x64_bf16 v[64:67], v[44:47], v[224:231], v2\n"
    "ds_read_b128 v[20:23], v1 offset:58368\n"
    "v_cvt_pk_bf16_f32 v57, v58, v59\n"
    "s_waitcnt lgkmcnt(5)\n"
    "v_smfmac_f32_16x16x64_bf16 v[68:71], v[40:43], v[32:39], v2\n"
    "v_add_u32 v1, 65536, v1\n"
    "ds_read_b128 v[24:27], v1 offset:0\n"
    "v_cvt_pk_bf16_f32 v58, v60, v61\n"
    "v_smfmac_f32_16x16x64_bf16 v[68:71], v[44:47], v[232:239], v2\n"
    "ds_read_b128 v[28:31], v1 offset:1024\n"
    "v_cvt_pk_bf16_f32 v59, v62, v63\n"
    "s_waitcnt lgkmcnt(5)\n"
    "v_smfmac_f32_16x16x64_bf16 v[64:67], v[48:51], v[72:79], v2\n"
    "ds_read_b128 v[32:35], v1 offset:8192\n"
    "v_smfmac_f32_16x16x64_bf16 v[64:67], v[52:55], v[240:247], v2\n"
    "ds_read_b128 v[36:39], v1 offset:9216\n"
    "v_permlane16_swap_b32 v56, v58\n"
    "s_waitcnt lgkmcnt(4)\n"
    "v_smfmac_f32_16x16x64_bf16 v[68:71], v[48:51], v[16:23], v2\n"
    "v_permlane16_swap_b32 v57, v59\n"
    "v_smfmac_f32_16x16x64_bf16 v[68:71], v[52:55], v[248:255], v2\n"
    "buffer_store_dwordx4 v[56:59], v8, s[36:39], s77 offen sc1\n"
    "v_cvt_pk_bf16_f32 v64, v64, v65\n"
    "s_waitcnt lgkmcnt(2)\n"
    "v_mfma_f32_16x16x32_bf16 v[4:7], v[40:43], v[24:27], 0\n"
    "v_cvt_pk_bf16_f32 v65, v66, v67\n"
    "v_mfma_f32_16x16x32_bf16 v[4:7], v[48:51], v[28:31], v[4:7]\n"
    "v_cvt_pk_bf16_f32 v66, v68, v69\n"
    "v_cvt_pk_bf16_f32 v67, v70, v71\n"
    "s_waitcnt lgkmcnt(0)\n"
    "v_mfma_f32_16x16x32_bf16 v[4:7], v[44:47], v[32:35], v[4:7]\n"
    "v_permlane16_swap_b32 v64, v66\n"
    "v_mfma_f32_16x16x32_bf16 v[4:7], v[52:55], v[36:39], v[4:7]\n"
    "v_permlane16_swap_b32 v65, v67\n"
    "buffer_store_dwordx4 v[64:67], v8, s[36:39], s86 offen sc1\n"
    "v_subrev_u32 v1, 131072, v1\n"
    "s_nop 4\n"
    "v_cvt_pk_bf16_f32 v4, v4, v5\n"
    "v_cvt_pk_bf16_f32 v5, v6, v7\n"
    "s_nop 0\n"
    "v_permlane16_swap_b32 v4, v6\n"
    "v_permlane16_swap_b32 v5, v7\n"
    "buffer_store_dwordx4 v[4:7], v9, s[36:39], s87 offen sc1\n"
    "s_mov_b64 exec, 1\n"
    "buffer_atomic_smax v3, off, s[36:39], s59 sc0 sc1\n"
    "s_mov_b64 exec, -1\n"
    "ds_read_b128 v[72:75], v0 offset:15360\n"
    "v_mov_b32 v76, v10\n"
    "s_waitcnt vmcnt(4)\n"
    "s_memtime s[96:97]\n"
    "v_mov_b32 v77, s68\n"
    "s_mov_b64 exec, 1\n"
    "buffer_atomic_smax v77, off, s[36:39], s58 sc1\n"
    "s_mov_b64 exec, -1\n"
    "v_max3_u32 v12, v12, v13, v14\n"
    "v_max_u32 v12, v12, v15\n"
    "s_add_u32 s74, s68, 1\n"
    "s_and_b32 s75, s74, 7\n"
    "v_max_u32_dpp v12, v12, v12 row_ror:8 row_mask:0xf bank_mask:0xf\n"
    "s_mul_i32 s75, s75, 4194304\n"
    "s_add_u32 s75, s75, s56\n"
    "v_max_u32_dpp v12, v12, v12 row_ror:4 row_mask:0xf bank_mask:0xf\n"
    "s_and_b32 s79, s74, 3\n"
    "s_lshl_b32 s79, s79, 17\n"
    "v_max_u32_dpp v12, v12, v12 row_ror:2 row_mask:0xf bank_mask:0xf\n"
    "s_add_u32 s79, s79, s65\n"
    "s_add_u32 s78, s64, s82\n"
    "v_max_u32_dpp v12, v12, v12 row_ror:1 row_mask:0xf bank_mask:0xf\n"
    "s_add_u32 s88, s78, 4096\n"
    "s_mov_b32 s33, s32\n"
    "v_readlane_b32 s95, v12, 0\n"
    "v_readlane_b32 s99, v12, 16\n"
    "s_nop 0\n"
    "s_max_u32 s95, s95, s99\n"
    "s_cmp_eq_u32 s95, -1\n"
    "s_cbranch_scc1 .Lrec_tsretry_%=\n"
    ".Lrec_tsgo_%=:\n"
    "s_mov_b32 s31, s95\n"
    "s_sub_u32 s95, s95, s28\n"
    "s_min_u32 s95, s95, 4000\n"
    "s_add_u32 s95, s95, 40\n"
    "s_mul_i32 s95, s95, s48\n"
    "s_lshr_b32 s95, s95, 8\n"
    "s_add_u32 s32, s32, s95\n"
    "s_waitcnt lgkmcnt(0)\n"
    "s_sub_u32 s95, s32, s96\n"
    "s_mul_i32 s95, s95, 43\n"
    "s_ashr_i32 s95, s95, 10\n"
    "s_max_i32 s95, s95, 0\n"
    "s_min_i32 s95, s95, 100\n"
    ".Lrec_tsloop_%=:\n"
    "s_sub_u32 s95, s95, 1\n"
    "s_cbranch_scc0 .Lrec_tsloop_%=\n"
    "s_cmp_lg_u32 s60, 0\n"
    "s_cbranch_scc1 .Lrec_nored_%=\n"
    ".Lrec_red_%=:\n"
    "buffer_load_dwordx4 v[4:7], v0, s[36:39], s78 offen sc1\n"
    "buffer_load_dwordx4 v[8:11], v0, s[36:39], s78 offen offset:1024 sc1\n"
    "buffer_load_dwordx4 v[12:15], v0, s[36:39], s78 offen offset:2048 sc1\n"
    "buffer_load_dwordx4 v[16:19], v0, s[36:39], s78 offen offset:3072 sc1\n"
    "buffer_load_dwordx4 v[20:23], v0, s[36:39], s88 offen sc1\n"
    "buffer_load_dwordx4 v[24:27], v0, s[36:39], s88 offen offset:1024 sc1\n"
    "buffer_load_dwordx4 v[28:31], v0, s[36:39], s88 offen offset:2048 sc1\n"
    "buffer_load_dwordx4 v[32:35], v0, s[36:39], s88 offen offset:3072 sc1\n"
    "s_sub_u32 s95, s31, s53\n"
    "s_mov_b32 s53, s31\n"
    "s_lshr_b32 s99, s52, 2\n"
    "s_sub_u32 s95, s95, s99\n"
    "s_min_u32 s54, s54, s95\n"
    "s_sub_u32 s95, s95, s54\n"
    "s_add_u32 s54, s54, 1\n"
    "s_sub_u32 s52, s52, s55\n"
    "s_cmp_gt_u32 s95, 70\n"
    "s_cselect_b32 s95, 0, 0\n"
    "s_cselect_b32 s55, 0, s55\n"
    "s_add_u32 s52, s52, s95\n"
    "s_min_u32 s52, s52, 1320\n"
    "s_max_u32 s52, s52, 800\n"
    "s_mul_i32 s95, s48, s52\n"
    "s_lshr_b32 s95, s95, 10\n"
    "s_add_u32 s32, s32, s95\n"
    "s_mov_b32 s50, s82\n"
    "s_add_u32 s82, s82, 589824\n"
    "s_cmp_eq_u32 s82, 1769472\n"
    "s_cselect_b32 s82, 0, s82\n"
    "s_and_b32 s80, s68, 3\n"
    "s_lshl_b32 s80, s80, 17\n"
    "s_and_b32 s80, s80, s45\n"
    "s_and_b32 s91, s50, s44\n"
    "s_add_u32 s80, s80, s91\n"
    "s_add_u32 s80, s80, s43\n"
    "s_add_u32 s89, s42, s50\n"
    "s_add_u32 s68, s68, 1\n"
    "s_and_b32 s70, s68, 3\n"
    "s_lshl_b32 s70, s70, 17\n"
    "s_add_u32 s70, s70, s66\n"
    "s_add_u32 s71, s70, 4096\n"
    "s_add_u32 s72, s70, 16384\n"
    "s_lshr_b32 s90, s68, 2\n"
    "s_lshl_b32 s90, s90, 14\n"
    "s_and_b32 s91, s68, 3\n"
    "s_lshl_b32 s91, s91, 11\n"
    "s_add_u32 s90, s90, s91\n"
    "s_add_u32 s90, s90, s40\n"
    "s_add_u32 s51, s90, s41\n"
    "s_and_b32 s76, s68, 7\n"
    "s_mul_i32 s76, s76, 4194304\n"
    "s_add_u32 s83, s76, s56\n"
    "s_add_u32 s83, s83, 16384\n"
    "s_add_u32 s76, s76, s57\n"
    "s_add_u32 s77, s63, s82\n"
    "s_add_u32 s86, s77, 65536\n"
    "s_add_u32 s87, s67, s82\n"
    "v_mov_b64 v[40:41], 0\n"
    "v_mov_b64 v[42:43], 0\n"
    "v_mov_b64 v[44:45], 0\n"
    "v_mov_b64 v[46:47], 0\n"
    "v_mov_b64 v[48:49], 0\n"
    "v_mov_b64 v[50:51], 0\n"
    "v_mov_b64 v[52:53], 0\n"
    "v_mov_b64 v[54:55], 0\n"
    "v_mov_b64 v[56:57], 0\n"
    "v_mov_b64 v[58:59], 0\n"
    "v_mov_b64 v[60:61], 0\n"
    "v_mov_b64 v[62:63], 0\n"
    "s_waitcnt vmcnt(9)\n"
    "buffer_load_dwordx4 v[64:67], v0, s[36:39], s75 offen sc1\n"
    "buffer_load_dwordx4 v[68:71], v0, s[36:39], s75 offen offset:1024 sc1\n"
    "v_readfirstlane_b32 s81, v3\n"
    "s_nop 0\n"
    "s_cmp_ge_i32 s81, s68\n"
    "s_cselect_b32 s81, 1, 0\n"
    "s_sub_u32 s46, s33, s46\n"
    "s_sub_u32 s47, s28, s47\n"
    "v_cvt_f32_u32 v78, s46\n"
    "v_cvt_f32_u32 v79, s47\n"
    "v_rcp_f32 v79, v79\n"
    "s_mov_b32 s46, s33\n"
    "s_mov_b32 s47, s28\n"
    "v_mul_f32 v78, v78, v79\n"
    "v_mov_b32 v77, 0x40c80000\n"
    "v_med3_f32 v78, v78, 4.0, v77\n"
    "v_rcp_f32 v79, v78\n"
    "v_mul_f32 v78, 0x43800000, v78\n"
    "v_mul_f32 v79, 0x44b54000, v79\n"
    "v_cvt_u32_f32 v78, v78\n"
    "v_cvt_u32_f32 v79, v79\n"
    "v_readfirstlane_b32 s48, v78\n"
    "v_readfirstlane_b32 s49, v79\n"
    ".Lrec_rsum_%=:\n"
    "s_waitcnt vmcnt(9)\n"
    "v_mfma_f32_16x16x32_bf16 v[36:39], v[4:7], v[72:75], 0\n"
    "v_max3_u32 v3, v4, v5, v6\n"
    "v_max_u32 v3, v3, v7\n"
    "s_waitcnt vmcnt(8)\n"
    "v_mfma_f32_16x16x32_bf16 v[36:39], v[8:11], v[72:75], v[36:39]\n"
    "v_max3_u32 v3, v3, v8, v9\n"
    "v_max3_u32 v3, v3, v10, v11\n"
    "s_waitcnt vmcnt(7)\n"
    "v_mfma_f32_16x16x32_bf16 v[36:39], v[12:15], v[72:75], v[36:39]\n"
    "v_max3_u32 v3, v3, v12, v13\n"
    "v_max3_u32 v3, v3, v14, v15\n"
    "s_waitcnt vmcnt(6)\n"
    "v_mfma_f32_16x16x32_bf16 v[36:39], v[16:19], v[72:75], v[36:39]\n"
    "v_max3_u32 v3, v3, v16, v17\n"
    "v_max3_u32 v3, v3, v18, v19\n"
    "s_waitcnt vmcnt(5)\n"
    "v_mfma_f32_16x16x32_bf16 v[36:39], v[20:23], v[72:75], v[36:39]\n"
    "v_max3_u32 v3, v3, v20, v21\n"
    "v_max3_u32 v3, v3, v22, v23\n"
    "s_waitcnt vmcnt(4)\n"
    "v_mfma_f32_16x16x32_bf16 v[36:39], v[24:27], v[72:75], v[36:39]\n"
    "v_max3_u32 v3, v3, v24, v25\n"
    "v_max3_u32 v3, v3, v26, v27\n"
    "s_waitcnt vmcnt(3)\n"
    "v_mfma_f32_16x16x32_bf16 v[36:39], v[28:31], v[72:75], v[36:39]\n"
    "v_max3_u32 v3, v3, v28, v29\n"
    "v_max3_u32 v3, v3, v30, v31\n"
    "s_waitcnt vmcnt(2)\n"
    "v_mfma_f32_16x16x32_bf16 v[36:39], v[32:35], v[72:75], v[36:39]\n"
    "v_max3_u32 v3, v3, v32, v33\n"
    "v_max3_u32 v3, v3, v34, v35\n"
    "v_cmp_eq_u32 vcc, -1, v3\n"
    "s_nop 4\n"
    "v_cvt_pk_bf16_f32 v4, v36, v37\n"
    "v_cvt_pk_bf16_f32 v6, v36, v37\n"
    "v_cvt_pk_bf16_f32 v5, v38, v39\n"
    "v_cvt_pk_bf16_f32 v7, v38, v39\n"
    "s_cbranch_vccnz .Lrec_rretry_%=\n"
    ".Lrec_rgo_%=:\n"
    "v_permlane16_swap_b32 v4, v6\n"
    "v_permlane16_swap_b32 v5, v7\n"
    "buffer_store_dwordx4 v[4:7], v76, s[36:39], s79 offen sc1\n"
    ".Lrec_b3_%=:\n"
    ".Lrec_entry_%=:\n"
    "s_memtime s[96:97]\n"
    "ds_read_b128 v[72:75], v1 offset:8192\n"
    "ds_read_b128 v[76:79], v1 offset:9216\n"
    "s_cmp_eq_u32 s81, 0\n"
    "s_cbranch_scc1 .Lrec_waitp_%=\n"
    "s_waitcnt lgkmcnt(0)\n"
    "s_sub_u32 s95, s32, s96\n"
    "s_mul_i32 s95, s95, 43\n"
    "s_ashr_i32 s95, s95, 10\n"
    "s_max_i32 s95, s95, 0\n"
    "s_min_i32 s95, s95, 100\n"
    ".Lrec_gloop_%=:\n"
    "s_sub_u32 s95, s95, 1\n"
    "s_cbranch_scc0 .Lrec_gloop_%=\n"
    "s_branch .Lrec_top_%=\n"
    ".Lrec_LOOPE_%=:\n"
    ".Lrec_fin_%=:\n"
    "buffer_store_dwordx4 v[56:59], v0, s[36:39], s76 offen offset:512 sc1\n"
    "s_waitcnt vmcnt(0) lgkmcnt(0)\n"
    "v_mov_b32 v46, s68\n"
    "s_mov_b64 exec, 1\n"
    "buffer_atomic_smax v46, off, s[36:39], s58 sc1\n"
    "s_mov_b64 exec, -1\n"
    "s_waitcnt vmcnt(0)\n"
    "s_branch .Lrec_FIN_%=\n"
    ".Lrec_retry_%=:\n"
    "s_waitcnt vmcnt(0) lgkmcnt(0)\n"
    "s_sleep 1\n"
    "buffer_load_dwordx4 v[4:7], v0, s[36:39], s70 offen sc1\n"
    "buffer_load_dwordx4 v[8:11], v0, s[36:39], s70 offen offset:1024 sc1\n"
    "buffer_load_dwordx4 v[12:15], v0, s[36:39], s70 offen offset:2048 sc1\n"
    "buffer_load_dwordx4 v[16:19], v0, s[36:39], s70 offen offset:3072 sc1\n"
    "buffer_load_dwordx4 v[20:23], v0, s[36:39], s71 offen sc1\n"
    "buffer_load_dwordx4 v[24:27], v0, s[36:39], s71 offen offset:1024 sc1\n"
    "buffer_load_dwordx4 v[28:31], v0, s[36:39], s71 offen offset:2048 sc1\n"
    "buffer_load_dwordx4 v[32:35], v0, s[36:39], s71 offen offset:3072 sc1\n"
    "buffer_load_dwordx4 v[36:39], v0, s[36:39], s72 offen sc1\n"
    "ds_read_b128 v[72:75], v1 offset:8192\n"
    "ds_read_b128 v[76:79], v1 offset:9216\n"
    "v_mov_b64 v[40:41], 0\n"
    "v_mov_b64 v[42:43], 0\n"
    "v_mov_b64 v[44:45], 0\n"
    "v_mov_b64 v[46:47], 0\n"
    "v_mov_b64 v[48:49], 0\n"
    "v_mov_b64 v[50:51], 0\n"
    "v_mov_b64 v[52:53], 0\n"
    "v_mov_b64 v[54:55], 0\n"
    "v_mov_b64 v[56:57], 0\n"
    "v_mov_b64 v[58:59], 0\n"
    "v_mov_b64 v[60:61], 0\n"
    "v_mov_b64 v[62:63], 0\n"
    "s_waitcnt vmcnt(0)\n"
    "v_max3_u32 v3, v4, v5, v6\n"
    "v_max3_u32 v3, v3, v7, v8\n"
    "v_max3_u32 v3, v3, v9, v10\n"
    "v_max3_u32 v3, v3, v11, v12\n"
    "v_max3_u32 v3, v3, v13, v14\n"
    "v_max3_u32 v3, v3, v15, v16\n"
    "v_max3_u32 v3, v3, v17, v18\n"
    "v_max3_u32 v3, v3, v19, v20\n"
    "v_max3_u32 v3, v3, v21, v22\n"
    "v_max3_u32 v3, v3, v23, v24\n"
    "v_max3_u32 v3, v3, v25, v26\n"
    "v_max3_u32 v3, v3, v27, v28\n"
    "v_max3_u32 v3, v3, v29, v30\n"
    "v_max3_u32 v3, v3, v31, v32\n"
    "v_max3_u32 v3, v3, v33, v34\n"
    "v_max3_u32 v3, v3, v35, v36\n"
    "v_max3_u32 v3, v3, v37, v38\n"
    "v_max_u32 v3, v3, v39\n"
    "v_cmp_eq_u32 vcc, -1, v3\n"
    "s_cbranch_vccz .Lrec_start_%=\n"
    "s_sub_u32 s69, s69, 1\n"
    "s_cbranch_scc0 .Lrec_retry_%=\n"
    "s_mov_b32 s69, 0\n"
    "s_branch .Lrec_start_%=\n"
    ".Lrec_retry0_%=:\n"
    "s_cmp_eq_u32 s69, 0\n"
    "s_cbranch_scc1 .Lrec_back0_%=\n"
    "s_branch .Lrec_retry_%=\n"
    ".Lrec_retry1_%=:\n"
    "s_cmp_eq_u32 s69, 0\n"
    "s_cbranch_scc1 .Lrec_back1_%=\n"
    "v_subrev_u32 v1, 65536, v1\n"
    "s_branch .Lrec_retry_%=\n"
    ".Lrec_retry2_%=:\n"
    "s_cmp_eq_u32 s69, 0\n"
    "s_cbranch_scc1 .Lrec_back2_%=\n"
    "s_branch .Lrec_retry_%=\n"
    ".Lrec_tsretry_%=:\n"
    "s_mov_b32 s98, 24\n"
    ".Lrec_tsretry1_%=:\n"
    "s_mov_b32 s95, s28\n"
    "s_sub_u32 s98, s98, 1\n"
    "s_cbranch_scc1 .Lrec_tsgo_%=\n"
    "s_sleep 2\n"
    "buffer_load_dwordx4 v[12:15], v11, s[36:39], s90 offen sc1\n"
    "s_waitcnt vmcnt(0)\n"
    "s_memtime s[96:97]\n"
    "v_max3_u32 v12, v12, v13, v14\n"
    "v_max_u32 v12, v12, v15\n"
    "s_nop 1\n"
    "v_max_u32_dpp v12, v12, v12 row_ror:8 row_mask:0xf bank_mask:0xf\n"
    "s_nop 1\n"
    "v_max_u32_dpp v12, v12, v12 row_ror:4 row_mask:0xf bank_mask:0xf\n"
    "s_nop 1\n"
    "v_max_u32_dpp v12, v12, v12 row_ror:2 row_mask:0xf bank_mask:0xf\n"
    "s_nop 1\n"
    "v_max_u32_dpp v12, v12, v12 row_ror:1 row_mask:0xf bank_mask:0xf\n"
    "s_nop 0\n"
    "v_readlane_b32 s95, v12, 0\n"
    "v_readlane_b32 s99, v12, 16\n"
    "s_nop 0\n"
    "s_max_u32 s95, s95, s99\n"
    "s_cmp_eq_u32 s95, -1\n"
    "s_cbranch_scc1 .Lrec_tsretry1_%=\n"
    "s_branch .Lrec_tsgo_%=\n"
    ".Lrec_rretry_%=:\n"
    "s_sub_u32 s69, s69, 1\n"
    "s_cbranch_scc1 .Lrec_rdead_%=\n"
    "s_nop 7\n"
    "buffer_load_dwordx4 v[4:7], v0, s[36:39], s78 offen sc1\n"
    "buffer_load_dwordx4 v[8:11], v0, s[36:39], s78 offen offset:1024 sc1\n"
    "buffer_load_dwordx4 v[12:15], v0, s[36:39], s78 offen offset:2048 sc1\n"
    "buffer_load_dwordx4 v[16:19], v0, s[36:39], s78 offen offset:3072 sc1\n"
    "buffer_load_dwordx4 v[20:23], v0, s[36:39], s88 offen sc1\n"
    "buffer_load_dwordx4 v[24:27], v0, s[36:39], s88 offen offset:1024 sc1\n"
    "buffer_load_dwordx4 v[28:31], v0, s[36:39], s88 offen offset:2048 sc1\n"
    "buffer_load_dwordx4 v[32:35], v0, s[36:39], s88 offen offset:3072 sc1\n"
    "s_waitcnt vmcnt(0)\n"
    "s_branch .Lrec_rsum_%=\n"
    ".Lrec_rdead_%=:\n"
    "s_mov_b32 s69, 0\n"
    "s_nop 7\n"
    "s_branch .Lrec_rgo_%=\n"
    ".Lrec_nored_%=:\n"
    "s_nop 8\n"
    "s_cmp_lt_i32 s64, 0\n"
    "s_cbranch_scc0 .Lrec_red_%=\n"
    "s_sub_u32 s95, s31, s53\n"
    "s_mov_b32 s53, s31\n"
    "s_lshr_b32 s99, s52, 2\n"
    "s_sub_u32 s95, s95, s99\n"
    "s_min_u32 s54, s54, s95\n"
    "s_sub_u32 s95, s95, s54\n"
    "s_add_u32 s54, s54, 1\n"
    "s_sub_u32 s52, s52, s55\n"
    "s_cmp_gt_u32 s95, 70\n"
    "s_cselect_b32 s95, 0, 0\n"
    "s_cselect_b32 s55, 0, s55\n"
    "s_add_u32 s52, s52, s95\n"
    "s_min_u32 s52, s52, 1320\n"
    "s_max_u32 s52, s52, 800\n"
    "s_mul_i32 s95, s48, s52\n"
    "s_lshr_b32 s95, s95, 10\n"
    "s_add_u32 s32, s32, s95\n"
    "s_mov_b32 s73, s89\n"
    "s_mov_b32 s74, s80\n"
    "s_mov_b32 s50, s82\n"
    "s_add_u32 s82, s82, 589824\n"
    "s_cmp_eq_u32 s82, 1769472\n"
    "s_cselect_b32 s82, 0, s82\n"
    "s_and_b32 s80, s68, 3\n"
    "s_lshl_b32 s80, s80, 17\n"
    "s_and_b32 s80, s80, s45\n"
    "s_and_b32 s91, s50, s44\n"
    "s_add_u32 s80, s80, s91\n"
    "s_add_u32 s80, s80, s43\n"
    "s_add_u32 s89, s42, s50\n"
    "s_add_u32 s68, s68, 1\n"
    "s_and_b32 s70, s68, 3\n"
    "s_lshl_b32 s70, s70, 17\n"
    "s_add_u32 s70, s70, s66\n"
    "s_add_u32 s71, s70, 4096\n"
    "s_add_u32 s72, s70, 16384\n"
    "s_lshr_b32 s90, s68, 2\n"
    "s_lshl_b32 s90, s90, 14\n"
    "s_and_b32 s91, s68, 3\n"
    "s_lshl_b32 s91, s91, 11\n"
    "s_add_u32 s90, s90, s91\n"
    "s_add_u32 s90, s90, s40\n"
    "s_add_u32 s51, s90, s41\n"
    "s_and_b32 s76, s68, 7\n"
    "s_mul_i32 s76, s76, 4194304\n"
    "s_add_u32 s83, s76, s56\n"
    "s_add_u32 s83, s83, 16384\n"
    "s_add_u32 s76, s76, s57\n"
    "s_add_u32 s77, s63, s82\n"
    "s_add_u32 s86, s77, 65536\n"
    "s_add_u32 s87, s67, s82\n"
    "v_mov_b64 v[40:41], 0\n"
    "v_mov_b64 v[42:43], 0\n"
    "v_mov_b64 v[44:45], 0\n"
    "v_mov_b64 v[46:47], 0\n"
    "v_mov_b64 v[48:49], 0\n"
    "v_mov_b64 v[50:51], 0\n"
    "v_mov_b64 v[52:53], 0\n"
    "v_mov_b64 v[54:55], 0\n"
    "v_mov_b64 v[56:57], 0\n"
    "v_mov_b64 v[58:59], 0\n"
    "v_mov_b64 v[60:61], 0\n"
    "v_mov_b64 v[62:63], 0\n"
    "s_sub_u32 s46, s33, s46\n"
    "s_sub_u32 s47, s28, s47\n"
    "v_cvt_f32_u32 v78, s46\n"
    "v_cvt_f32_u32 v79, s47\n"
    "v_rcp_f32 v79, v79\n"
    "s_mov_b32 s46, s33\n"
    "s_mov_b32 s47, s28\n"
    "v_mul_f32 v78, v78, v79\n"
    "v_mov_b32 v77, 0x40c80000\n"
    "v_med3_f32 v78, v78, 4.0, v77\n"
    "v_rcp_f32 v79, v78\n"
    "v_mul_f32 v78, 0x43800000, v78\n"
    "v_mul_f32 v79, 0x44b54000, v79\n"
    "v_cvt_u32_f32 v78, v78\n"
    "v_cvt_u32_f32 v79, v79\n"
    "v_readfirstlane_b32 s48, v78\n"
    "v_readfirstlane_b32 s49, v79\n"
    "s_waitcnt vmcnt(1)\n"
    "buffer_load_dwordx4 v[64:67], v0, s[36:39], s75 offen sc1\n"
    "buffer_load_dwordx4 v[68:71], v0, s[36:39], s75 offen offset:1024 sc1\n"
    "v_readfirstlane_b32 s81, v3\n"
    "s_nop 0\n"
    "s_cmp_ge_i32 s81, s68\n"
    "s_cselect_b32 s81, 1, 0\n"
    "v_mov_b64 v[4:5], -1\n"
    "v_mov_b64 v[6:7], -1\n"
    "buffer_store_dwordx4 v[4:7], v0, s[36:39], s73 offen sc1\n"
    "buffer_store_dwordx4 v[4:7], v0, s[36:39], s73 offen offset:1024 sc1\n"
    "buffer_store_dwordx4 v[4:7], v0, s[36:39], s73 offen offset:2048 sc1\n"
    "buffer_store_dwordx4 v[4:7], v0, s[36:39], s73 offen offset:3072 sc1\n"
    "buffer_store_dwordx4 v[4:7], v0, s[36:39], s74 offen sc1\n"
    "s_branch .Lrec_b3_%=\n"
    ".Lrec_waitp_%=:\n"
    "v_mov_b32 v3, -1\n"
    "s_mov_b64 exec, 1\n"
    "buffer_atomic_smax v3, off, s[36:39], s59 sc0 sc1\n"
    "s_mov_b64 exec, -1\n"
    "s_waitcnt vmcnt(0)\n"
    "v_readfirstlane_b32 s81, v3\n"
    "s_nop 0\n"
    "s_cmp_ge_i32 s81, s68\n"
    "s_cbranch_scc1 .Lrec_waitp1_%=\n"
    "s_sleep 1\n"
    "s_sub_u32 s69, s69, 1\n"
    "s_cbranch_scc0 .Lrec_waitp_%=\n"
    "s_mov_b32 s69, 0\n"
    ".Lrec_waitp1_%=:\n"
    "s_and_b32 s75, s68, 7\n"
    "s_mul_i32 s75, s75, 4194304\n"
    "s_add_u32 s75, s75, s56\n"
    "buffer_load_dwordx4 v[64:67], v0, s[36:39], s75 offen sc1\n"
    "buffer_load_dwordx4 v[68:71], v0, s[36:39], s75 offen offset:1024 sc1\n"
    "s_branch .Lrec_top_%=\n"
    ".Lrec_FIN_%=:\n"
  );
}

typedef unsigned long long u64;
DEVI u64 ld8u(rsrc_t r, u32 voff) { v2u v = ld8(r, voff, 0); asm volatile("" : "+v"(v)); return (u64)v.x | ((u64)v.y << 32); }
DEVI u32 find_shift(rsrc_t rw, bool decide, u64 tag) {
  const int lane = threadIdx.x & 63;
  unsigned budget = SPIN_LIMIT;
  if (decide) {
    u32 best[2] = {~0u, ~0u};
    const u32 probe = 126 * CHUNK + 8192;
#pragma unroll 1
    for (int r = 0; r < 5; ++r) {
      const u32 a = lane == 0 ? probe + (u32)(r & 1) * 8192 : OOB;
      u32 v = 0;
      const unsigned long long t0 = __builtin_amdgcn_s_memtime();
      asm volatile("s_waitcnt lgkmcnt(0)\n\tbuffer_atomic_or %0, %1, %2, 0 offen sc0 sc1\n\ts_waitcnt vmcnt(0)" : "+v"(v) : "v"(a), "s"(rw) : "memory");
      const u32 dt = (u32)(__builtin_amdgcn_s_memtime() - t0);
      if (r & 1) best[1] = min(best[1], dt); else best[0] = min(best[0], dt);
    }
    for (;;) {
      v2u v[4];
      const u32 a = READY + (u32)lane * 8;
#pragma unroll
      for (int k = 0; k < 4; ++k) v[k] = (v2u){0u, 0u};
      asm volatile("buffer_atomic_or_x2 %0, %4, %5, 0 offen sc0 sc1\n\tbuffer_atomic_or_x2 %1, %4, %5, 0 offen offset:512 sc0 sc1\n\t"
                   "buffer_atomic_or_x2 %2, %4, %5, 0 offen offset:1024 sc0 sc1\n\tbuffer_atomic_or_x2 %3, %4, %5, 0 offen offset:1536 sc0 sc1\n\ts_waitcnt vmcnt(0)"
                   : "+v"(v[0]), "+v"(v[1]), "+v"(v[2]), "+v"(v[3]) : "v"(a), "s"(rw) : "memory");
      bool ok = true;
#pragma unroll
      for (int k = 0; k < 4; ++k) ok = ok & (v[k].x == (u32)tag) & (v[k].y == (u32)(tag >> 32));
      if (__all(ok) || budget == 0) break;
      --budget;
    }
    const u64 v = lane == 0 ? 2 * tag + (best[0] < best[1] ? 0u : 1u) : tag;
    st8(rw, (v2u){(u32)v, (u32)(v >> 32)}, lane == 0 ? PARW : (lane == 1 ? EPOCH : OOB), 0);
  }
  u64 v;
  for (;;) {
    cbar();
    v = ld8u(rw, PARW) ^ (2 * tag);
    if (__all(v < 2) || budget == 0) break;
    --budget;
    __builtin_amdgcn_s_sleep(4);
  }
  cbar();
  return (rfl((u32)v) & 1u) * 8192;
}
DEVI void hot_init(const Params& p, rsrc_t rw, int L, int n, u32 shift) {
  const int tid = threadIdx.x, w = (int)rfl(tid >> 6);
  const u32 hot = HOT + L * 8192 + shift, hot2 = HOT2 + (1 - L) * 8192 + shift;
  const v4u ones = {~0u, ~0u, ~0u, ~0u};
#pragma unroll
  for (int k = 0; k < 9; ++k) {
    st16(rw, ones, tid * 16, hot + (u32)(9 * n + k) * CHUNK);
    st16(rw, ones, tid * 16 + 4096, hot + (u32)(9 * n + k) * CHUNK);
  }
  {
    const u32 b = hot2 + (u32)(4 + n) * 2 * CHUNK;
    st16(rw, ones, tid * 16, b); st16(rw, ones, tid * 16 + 4096, b);
    st16(rw, ones, w == 0 ? (u32)tid * 16 : OOB, b + CHUNK);
  }
  if (n < 8) {
#pragma unroll
    for (int k = 0; k < 2; ++k) {
      st16(rw, ones, tid * 16, hot2 + (u32)(32 + 2 * n + k) * CHUNK);
      st16(rw, ones, tid * 16 + 4096, hot2 + (u32)(32 + 2 * n + k) * CHUNK);
    }
  }
  if (n >= 8) {
    const u32 b = hot2 + (u32)(n - 8) * 2 * CHUNK;
#pragma unroll
    for (int k = 0; k < 3; ++k) {
      const int pc = tid + 256 * k;
      if (pc < 576) {
        v4u v = *(const v4u*)(p.mem0 + (pc & 7) * 576 + (pc >> 3) * 8);
#pragma unroll
        for (int d = 0; d < 4; ++d) v[d] = min(v[d], 0xFFFFFFFEu);
        st16(rw, v, pc < 512 ? (u32)pc * 16 : CHUNK + (u32)(pc - 512) * 16, b);
      }
    }
  }
  drain();
  __syncthreads();
  if (tid == 0) st4(rw, 0u, INITF + L * 8192 + n * 128 + shift, 0);
}

DEVI void role_att(const Params& p, int L, int j, int a, u32 dbg, u32 shift) {
  const int tid = threadIdx.x, lane = tid & 63, w = (int)rfl(tid >> 6);
  const int c = lane & 15, g = lane >> 4;
  const int hl = 4 * a + w, h = 8 * j + hl;
  const v4u ws = make_srd(p.ws + shift, WS_END);
  v16u sa = cat4(ws, ws, ws, ws);
  v16u sb = {pga(L, B_QKV + 3 * h), pga(L, B_KVM + h), pgb(L, B_O + h), 0, dbg + w * 64,
             0, 0, 0, 0, 0, 0, 0, 0, 0, 0, 0};
  v16u va = {0, 0, 0, 0, 0, 0, 0, 0, 0, 0, 0, 0, 0, 0, 0, 0};
  va[0] = lane * 16;
  va[1] = (u32)(g * 128 + (c & 7) * 16);
  va[2] = lane < 3 ? mbox(L, j, SL_WO + lane, hl) : mbox(L, j, SL_P + hl, 48);
  va[4] = mbox_att(L, j, hl, lane & 15);
  va[5] = (u32)(lane < 4 ? 0 : (lane < 7 ? RO : ((lane >> 2) == 2 ? -1 : 1000)));
  va[6] = (u32)(c - 4 * g);
  va[7] = 0;
  RUN_ASM(
    "s_nop 4\n"
    "v_cmp_gt_i32 s[10:11], 0, v6\n"
    "v_cmp_gt_i32 s[12:13], 1, v6\n"
    "v_cmp_gt_i32 s[14:15], 2, v6\n"
    "v_cmp_gt_i32 s[16:17], 3, v6\n"
    "v_mov_b32 v10, 0xff61b1e6\n"
    "s_mov_b32 s18, 0x3e0293ee\n"
    "s_mov_b32 s68, 0\n"
    "v_mov_b32 v8, -1\n"
    "s_mov_b32 s69, 262144\n"
    "s_branch .Latt_waitp_%=\n"
    ".Latt_LOOPB_%=:\n"
    ".Latt_top_%=:\n"
    "s_waitcnt vmcnt(23)\n"
    "v_mfma_f32_16x16x32_bf16 v[160:163], v[16:19], v[48:51], 0\n"
    "v_mfma_f32_16x16x32_bf16 v[164:167], v[16:19], v[52:55], 0\n"
    "v_mfma_f32_16x16x32_bf16 v[168:171], v[20:23], v[52:55], 0\n"
    "s_waitcnt vmcnt(19)\n"
    "v_mfma_f32_16x16x32_bf16 v[160:163], v[24:27], v[56:59], v[160:163]\n"
    "v_mfma_f32_16x16x32_bf16 v[164:167], v[24:27], v[60:63], v[164:167]\n"
    "v_mfma_f32_16x16x32_bf16 v[168:171], v[28:31], v[60:63], v[168:171]\n"
    "s_waitcnt vmcnt(15)\n"
    "v_mfma_f32_16x16x32_bf16 v[160:163], v[32:35], v[64:67], v[160:163]\n"
    "v_mfma_f32_16x16x32_bf16 v[164:167], v[32:35], v[68:71], v[164:167]\n"
    "v_mfma_f32_16x16x32_bf16 v[168:171], v[36:39], v[68:71], v[168:171]\n"
    "s_waitcnt vmcnt(9)\n"
    "v_mfma_f32_16x16x32_bf16 v[160:163], v[40:43], v[72:75], v[160:163]\n"
    "v_mfma_f32_16x16x32_bf16 v[164:167], v[40:43], v[76:79], v[164:167]\n"
    "v_mfma_f32_16x16x32_bf16 v[168:171], v[44:47], v[76:79], v[168:171]\n"
    "v_add_u32 v11, v8, v5\n"
    "v_cmp_gt_i32 vcc, s68, v11\n"
    "s_nop 0\n"
    "s_or_b64 exec, vcc, 1\n"
    "buffer_atomic_smax v8, v4, s[36:39], 0 offen sc0 sc1\n"
    "s_mov_b64 exec, -1\n"
    "s_add_u32 s72, s68, 1\n"
    "s_and_b32 s4, s72, 7\n"
    "s_mul_i32 s4, s4, 4194304\n"
    "s_add_u32 s4, s4, s52\n"
    "s_add_u32 s5, s4, 4096\n"
    "s_add_u32 s6, s4, 16384\n"
    "s_add_u32 s7, s4, 20480\n"
    "s_add_u32 s8, s4, 32768\n"
    "s_add_u32 s9, s4, 36864\n"
    "s_and_b32 s73, s68, 7\n"
    "s_mul_i32 s73, s73, 4194304\n"
    "s_add_u32 s73, s73, s53\n"
    "s_add_u32 s74, s73, 3072\n"
    "s_and_b32 s75, s68, 7\n"
    "s_lshl_b32 s75, s75, 22\n"
    "s_add_u32 s75, s75, s54\n"
    "s_add_u32 s76, s75, 4096\n"
    "v_cndmask_b32 v160, v160, v10, s[10:11]\n"
    "v_cndmask_b32 v161, v161, v10, s[12:13]\n"
    "v_cndmask_b32 v162, v162, v10, s[14:15]\n"
    "v_cndmask_b32 v163, v163, v10, s[16:17]\n"
    "v_cndmask_b32 v168, v168, v10, s[10:11]\n"
    "v_cndmask_b32 v169, v169, v10, s[12:13]\n"
    "v_cndmask_b32 v170, v170, v10, s[14:15]\n"
    "v_cndmask_b32 v171, v171, v10, s[16:17]\n"
    "v_max3_f32 v224, v160, v161, v162\n"
    "v_max3_f32 v226, v164, v165, v166\n"
    "v_max_f32 v224, v224, v163\n"
    "v_max3_f32 v226, v226, v167, v168\n"
    "v_max3_f32 v226, v226, v169, v170\n"
    "v_max_f32 v226, v226, v171\n"
    "v_mov_b32 v238, v224\n"
    "v_mov_b32 v239, v226\n"
    "s_nop 0\n"
    "v_permlane16_swap_b32 v224, v238\n"
    "v_permlane16_swap_b32 v226, v239\n"
    "v_max_f32 v224, v224, v238\n"
    "v_max_f32 v226, v226, v239\n"
    "v_mov_b32 v238, v224\n"
    "v_mov_b32 v239, v226\n"
    "s_nop 0\n"
    "v_permlane32_swap_b32 v224, v238\n"
    "v_permlane32_swap_b32 v226, v239\n"
    "v_max_f32 v224, v224, v238\n"
    "v_max_f32 v226, v226, v239\n"
    "v_mul_f32 v230, 0xbe0293ee, v224\n"
    "v_mul_f32 v232, 0xbe0293ee, v226\n"
    "v_pk_fma_f32 v[160:161], v[160:161], s[18:19], v[230:231] op_sel_hi:[1,0,0]\n"
    "v_pk_fma_f32 v[162:163], v[162:163], s[18:19], v[230:231] op_sel_hi:[1,0,0]\n"
    "v_pk_fma_f32 v[164:165], v[164:165], s[18:19], v[232:233] op_sel_hi:[1,0,0]\n"
    "v_pk_fma_f32 v[166:167], v[166:167], s[18:19], v[232:233] op_sel_hi:[1,0,0]\n"
    "v_pk_fma_f32 v[168:169], v[168:169], s[18:19], v[232:233] op_sel_hi:[1,0,0]\n"
    "v_pk_fma_f32 v[170:171], v[170:171], s[18:19], v[232:233] op_sel_hi:[1,0,0]\n"
    "v_exp_f32 v160, v160\n"
    "v_exp_f32 v161, v161\n"
    "v_exp_f32 v162, v162\n"
    "v_exp_f32 v163, v163\n"
    "v_exp_f32 v164, v164\n"
    "v_exp_f32 v165, v165\n"
    "v_exp_f32 v166, v166\n"
    "v_exp_f32 v167, v167\n"
    "v_exp_f32 v168, v168\n"
    "v_exp_f32 v169, v169\n"
    "v_exp_f32 v170, v170\n"
    "v_exp_f32 v171, v171\n"
    "v_pk_add_f32 v[230:231], v[160:161], v[162:163]\n"
    "v_pk_add_f32 v[232:233], v[164:165], v[166:167]\n"
    "v_pk_add_f32 v[234:235], v[168:169], v[170:171]\n"
    "v_pk_add_f32 v[232:233], v[232:233], v[234:235]\n"
    "v_add_f32 v228, v230, v231\n"
    "v_add_f32 v229, v232, v233\n"
    "v_mov_b32 v238, v228\n"
    "v_mov_b32 v239, v229\n"
    "s_nop 0\n"
    "v_permlane16_swap_b32 v228, v238\n"
    "v_permlane16_swap_b32 v229, v239\n"
    "v_add_f32 v228, v228, v238\n"
    "v_add_f32 v229, v229, v239\n"
    "v_mov_b32 v238, v228\n"
    "v_mov_b32 v239, v229\n"
    "s_nop 0\n"
    "v_permlane32_swap_b32 v228, v238\n"
    "v_permlane32_swap_b32 v229, v239\n"
    "v_add_f32 v228, v228, v238\n"
    "v_add_f32 v229, v229, v239\n"
    "s_waitcnt vmcnt(0)\n"
    ".Latt_pollr_%=:\n"
    "v_add_u32 v11, v8, v5\n"
    "v_cmp_gt_i32 vcc, s68, v11\n"
    "s_nop 0\n"
    "s_and_b32 s71, vcc_lo, 0xff\n"
    "s_cbranch_scc1 .Latt_waitr_%=\n"
    ".Latt_gor_%=:\n"
    "buffer_load_dwordx4 v[128:131], v1, s[36:39], s73 offen sc1\n"
    "buffer_load_dwordx4 v[132:135], v1, s[36:39], s73 offen offset:1536 sc1\n"
    "buffer_load_dwordx4 v[136:139], v1, s[36:39], s74 offen sc1\n"
    "buffer_load_dwordx4 v[140:143], v1, s[36:39], s74 offen offset:1536 sc1\n"
    "buffer_load_dwordx4 v[144:147], v0, s[36:39], s73 offen offset:512 sc1\n"
    "buffer_load_dwordx4 v[148:151], v0, s[36:39], s73 offen offset:2048 sc1\n"
    "buffer_load_dwordx4 v[152:155], v0, s[36:39], s74 offen offset:512 sc1\n"
    "buffer_load_dwordx4 v[156:159], v0, s[36:39], s74 offen offset:2048 sc1\n"
    "s_and_b32 s71, vcc_lo, 0xf00\n"
    "s_cselect_b32 s70, 0, 1\n"
    "s_cselect_b32 s71, 0x80000000, 0\n"
    "v_or_b32 v7, s71, v0\n"
    "s_nop 15\n"
    "buffer_load_dwordx4 v[16:19], v7, s[36:39], s6 offen sc1\n"
    "s_nop 15\n"
    "buffer_load_dwordx4 v[20:23], v7, s[36:39], s6 offen offset:1024 sc1\n"
    "s_nop 15\n"
    "buffer_load_dwordx4 v[24:27], v7, s[36:39], s6 offen offset:2048 sc1\n"
    "s_nop 15\n"
    "buffer_load_dwordx4 v[28:31], v7, s[36:39], s6 offen offset:3072 sc1\n"
    "s_waitcnt vmcnt(11)\n"
    "v_mfma_f32_16x16x32_bf16 v[172:175], v[128:131], v[48:51], 0\n"
    "v_mfma_f32_16x16x32_bf16 v[176:179], v[128:131], v[52:55], 0\n"
    "s_waitcnt vmcnt(10)\n"
    "v_mfma_f32_16x16x32_bf16 v[172:175], v[132:135], v[56:59], v[172:175]\n"
    "v_mfma_f32_16x16x32_bf16 v[176:179], v[132:135], v[60:63], v[176:179]\n"
    "s_waitcnt vmcnt(9)\n"
    "v_mfma_f32_16x16x32_bf16 v[172:175], v[136:139], v[64:67], v[172:175]\n"
    "v_mfma_f32_16x16x32_bf16 v[176:179], v[136:139], v[68:71], v[176:179]\n"
    "s_waitcnt vmcnt(8)\n"
    "v_mfma_f32_16x16x32_bf16 v[172:175], v[140:143], v[72:75], v[172:175]\n"
    "v_mfma_f32_16x16x32_bf16 v[176:179], v[140:143], v[76:79], v[176:179]\n"
    "s_waitcnt vmcnt(4)\n"
    "v_mov_b64 v[80:81], v[144:145]\n"
    "v_mov_b64 v[86:87], v[146:147]\n"
    "v_mov_b64 v[92:93], v[148:149]\n"
    "v_mov_b64 v[98:99], v[150:151]\n"
    "v_mov_b64 v[104:105], v[152:153]\n"
    "v_mov_b64 v[110:111], v[154:155]\n"
    "v_mov_b64 v[116:117], v[156:157]\n"
    "v_mov_b64 v[122:123], v[158:159]\n"
    "v_max3_f32 v230, v172, v173, v174\n"
    "v_max3_f32 v232, v176, v177, v178\n"
    "v_max3_f32 v230, v230, v175, v224\n"
    "v_max3_f32 v232, v232, v179, v226\n"
    "v_mov_b32 v238, v230\n"
    "v_mov_b32 v239, v232\n"
    "s_nop 0\n"
    "v_permlane16_swap_b32 v230, v238\n"
    "v_permlane16_swap_b32 v232, v239\n"
    "v_max_f32 v230, v230, v238\n"
    "v_max_f32 v232, v232, v239\n"
    "v_mul_f32 v230, 0xbe0293ee, v230\n"
    "v_mul_f32 v232, 0xbe0293ee, v232\n"
    "v_pk_fma_f32 v[172:173], v[172:173], s[18:19], v[230:231] op_sel_hi:[1,0,0]\n"
    "v_pk_fma_f32 v[174:175], v[174:175], s[18:19], v[230:231] op_sel_hi:[1,0,0]\n"
    "v_pk_fma_f32 v[176:177], v[176:177], s[18:19], v[232:233] op_sel_hi:[1,0,0]\n"
    "v_pk_fma_f32 v[178:179], v[178:179], s[18:19], v[232:233] op_sel_hi:[1,0,0]\n"
    "v_fma_f32 v224, v224, s18, v230\n"
    "v_fma_f32 v226, v226, s18, v232\n"
    "v_exp_f32 v172, v172\n"
    "v_exp_f32 v173, v173\n"
    "v_exp_f32 v174, v174\n"
    "v_exp_f32 v175, v175\n"
    "buffer_load_dwordx4 v[32:35], v7, s[36:39], s7 offen sc1\n"
    "v_exp_f32 v176, v176\n"
    "v_exp_f32 v177, v177\n"
    "v_exp_f32 v178, v178\n"
    "v_exp_f32 v179, v179\n"
    "buffer_load_dwordx4 v[36:39], v7, s[36:39], s7 offen offset:1024 sc1\n"
    "v_exp_f32 v224, v224\n"
    "v_exp_f32 v226, v226\n"
    "v_pk_add_f32 v[230:231], v[172:173], v[174:175]\n"
    "v_pk_add_f32 v[232:233], v[176:177], v[178:179]\n"
    "v_add_f32 v230, v230, v231\n"
    "v_add_f32 v232, v232, v233\n"
    "v_mov_b32 v238, v230\n"
    "v_mov_b32 v239, v232\n"
    "s_nop 0\n"
    "v_permlane16_swap_b32 v230, v238\n"
    "v_permlane16_swap_b32 v232, v239\n"
    "v_add_f32 v230, v230, v238\n"
    "v_add_f32 v232, v232, v239\n"
    "v_fmac_f32 v230, v228, v224\n"
    "v_fmac_f32 v232, v229, v226\n"
    "v_rcp_f32 v230, v230\n"
    "v_rcp_f32 v232, v232\n"
    "buffer_load_dwordx4 v[40:43], v7, s[36:39], s7 offen offset:2048 sc1\n"
    "v_mul_f32 v224, v224, v230\n"
    "v_mul_f32 v226, v226, v232\n"
    "v_pk_mul_f32 v[164:165], v[164:165], v[226:227] op_sel_hi:[1,0]\n"
    "v_pk_mul_f32 v[166:167], v[166:167], v[226:227] op_sel_hi:[1,0]\n"
    "v_pk_mul_f32 v[168:169], v[168:169], v[226:227] op_sel_hi:[1,0]\n"
    "v_pk_mul_f32 v[170:171], v[170:171], v[226:227] op_sel_hi:[1,0]\n"
    "v_pk_mul_f32 v[176:177], v[176:177], v[232:233] op_sel_hi:[1,0]\n"
    "v_pk_mul_f32 v[178:179], v[178:179], v[232:233] op_sel_hi:[1,0]\n"
    "v_pk_mul_f32 v[172:173], v[172:173], v[230:231] op_sel_hi:[1,0]\n"
    "v_pk_mul_f32 v[174:175], v[174:175], v[230:231] op_sel_hi:[1,0]\n"
    "v_pk_mul_f32 v[160:161], v[160:161], v[224:225] op_sel_hi:[1,0]\n"
    "v_pk_mul_f32 v[162:163], v[162:163], v[224:225] op_sel_hi:[1,0]\n"
    "v_cvt_pk_bf16_f32 v216, v164, v165\n"
    "v_cvt_pk_bf16_f32 v217, v166, v167\n"
    "v_cvt_pk_bf16_f32 v218, v168, v169\n"
    "v_cvt_pk_bf16_f32 v219, v170, v171\n"
    "v_cvt_pk_bf16_f32 v220, v176, v177\n"
    "v_cvt_pk_bf16_f32 v221, v178, v179\n"
    "v_cvt_pk_bf16_f32 v212, v172, v173\n"
    "v_cvt_pk_bf16_f32 v213, v174, v175\n"
    "v_cvt_pk_bf16_f32 v214, v160, v161\n"
    "v_cvt_pk_bf16_f32 v215, v162, v163\n"
    "v_mfma_f32_16x16x32_bf16 v[188:191], v[82:85], v[216:219], 0\n"
    "buffer_load_dwordx4 v[44:47], v7, s[36:39], s7 offen offset:3072 sc1\n"
    "v_mfma_f32_16x16x32_bf16 v[192:195], v[88:91], v[216:219], 0\n"
    "v_mfma_f32_16x16x32_bf16 v[180:183], v[80:83], v[212:215], 0\n"
    "v_mfma_f32_16x16x32_bf16 v[184:187], v[86:89], v[212:215], 0\n"
    "buffer_load_dwordx4 v[48:51], v7, s[36:39], s4 offen sc1\n"
    "v_mfma_f32_16x16x16_bf16 v[188:191], v[80:81], v[220:221], v[188:191]\n"
    "v_mfma_f32_16x16x16_bf16 v[192:195], v[86:87], v[220:221], v[192:195]\n"
    "v_mfma_f32_16x16x32_bf16 v[204:207], v[94:97], v[216:219], 0\n"
    "buffer_load_dwordx4 v[52:55], v7, s[36:39], s4 offen offset:1024 sc1\n"
    "v_mfma_f32_16x16x32_bf16 v[208:211], v[100:103], v[216:219], 0\n"
    "v_cvt_pk_bf16_f32 v180, v180, v181\n"
    "v_cvt_pk_bf16_f32 v181, v182, v183\n"
    "v_mfma_f32_16x16x32_bf16 v[196:199], v[92:95], v[212:215], 0\n"
    "v_cvt_pk_bf16_f32 v182, v184, v185\n"
    "v_cvt_pk_bf16_f32 v183, v186, v187\n"
    "v_mfma_f32_16x16x32_bf16 v[200:203], v[98:101], v[212:215], 0\n"
    "buffer_load_dwordx4 v[56:59], v7, s[36:39], s4 offen offset:2048 sc1\n"
    "buffer_store_dwordx4 v[180:183], v0, s[36:39], s75 offen sc1\n"
    "v_cvt_pk_bf16_f32 v188, v188, v189\n"
    "v_mfma_f32_16x16x16_bf16 v[204:207], v[92:93], v[220:221], v[204:207]\n"
    "v_cvt_pk_bf16_f32 v189, v190, v191\n"
    "v_cvt_pk_bf16_f32 v190, v192, v193\n"
    "v_mfma_f32_16x16x16_bf16 v[208:211], v[98:99], v[220:221], v[208:211]\n"
    "v_cvt_pk_bf16_f32 v191, v194, v195\n"
    "buffer_store_dwordx4 v[188:191], v0, s[36:39], s75 offen offset:1024 sc1\n"
    "s_nop 1\n"
    "v_mfma_f32_16x16x32_bf16 v[188:191], v[106:109], v[216:219], 0\n"
    "buffer_load_dwordx4 v[60:63], v7, s[36:39], s4 offen offset:3072 sc1\n"
    "v_mfma_f32_16x16x32_bf16 v[192:195], v[112:115], v[216:219], 0\n"
    "v_cvt_pk_bf16_f32 v196, v196, v197\n"
    "v_cvt_pk_bf16_f32 v197, v198, v199\n"
    "v_mfma_f32_16x16x32_bf16 v[180:183], v[104:107], v[212:215], 0\n"
    "v_cvt_pk_bf16_f32 v198, v200, v201\n"
    "v_cvt_pk_bf16_f32 v199, v202, v203\n"
    "v_mfma_f32_16x16x32_bf16 v[184:187], v[110:113], v[212:215], 0\n"
    "buffer_load_dwordx4 v[64:67], v7, s[36:39], s5 offen sc1\n"
    "buffer_store_dwordx4 v[196:199], v0, s[36:39], s75 offen offset:2048 sc1\n"
    "v_cvt_pk_bf16_f32 v204, v204, v205\n"
    "v_mfma_f32_16x16x16_bf16 v[188:191], v[104:105], v[220:221], v[188:191]\n"
    "v_cvt_pk_bf16_f32 v205, v206, v207\n"
    "v_cvt_pk_bf16_f32 v206, v208, v209\n"
    "v_mfma_f32_16x16x16_bf16 v[192:195], v[110:111], v[220:221], v[192:195]\n"
    "v_cvt_pk_bf16_f32 v207, v210, v211\n"
    "buffer_store_dwordx4 v[204:207], v0, s[36:39], s75 offen offset:3072 sc1\n"
    "s_nop 1\n"
    "v_mfma_f32_16x16x32_bf16 v[204:207], v[118:121], v[216:219], 0\n"
    "buffer_load_dwordx4 v[68:71], v7, s[36:39], s5 offen offset:1024 sc1\n"
    "v_mfma_f32_16x16x32_bf16 v[208:211], v[124:127], v[216:219], 0\n"
    "v_cvt_pk_bf16_f32 v180, v180, v181\n"
    "v_cvt_pk_bf16_f32 v181, v182, v183\n"
    "v_mfma_f32_16x16x32_bf16 v[196:199], v[116:119], v[212:215], 0\n"
    "v_cvt_pk_bf16_f32 v182, v184, v185\n"
    "v_cvt_pk_bf16_f32 v183, v186, v187\n"
    "v_mfma_f32_16x16x32_bf16 v[200:203], v[122:125], v[212:215], 0\n"
    "buffer_load_dwordx4 v[72:75], v7, s[36:39], s5 offen offset:2048 sc1\n"
    "buffer_store_dwordx4 v[180:183], v0, s[36:39], s76 offen sc1\n"
    "v_cvt_pk_bf16_f32 v188, v188, v189\n"
    "v_mfma_f32_16x16x16_bf16 v[204:207], v[116:117], v[220:221], v[204:207]\n"
    "v_cvt_pk_bf16_f32 v189, v190, v191\n"
    "v_cvt_pk_bf16_f32 v190, v192, v193\n"
    "v_mfma_f32_16x16x16_bf16 v[208:211], v[122:123], v[220:221], v[208:211]\n"
    "v_cvt_pk_bf16_f32 v191, v194, v195\n"
    "buffer_store_dwordx4 v[188:191], v0, s[36:39], s76 offen offset:1024 sc1\n"
    "v_cvt_pk_bf16_f32 v196, v196, v197\n"
    "v_cvt_pk_bf16_f32 v197, v198, v199\n"
    "v_cvt_pk_bf16_f32 v198, v200, v201\n"
    "v_cvt_pk_bf16_f32 v199, v202, v203\n"
    "buffer_store_dwordx4 v[196:199], v0, s[36:39], s76 offen offset:2048 sc1\n"
    "v_cvt_pk_bf16_f32 v204, v204, v205\n"
    "v_cvt_pk_bf16_f32 v205, v206, v207\n"
    "v_cvt_pk_bf16_f32 v206, v208, v209\n"
    "v_cvt_pk_bf16_f32 v207, v210, v211\n"
    "buffer_store_dwordx4 v[204:207], v0, s[36:39], s76 offen offset:3072 sc1\n"
    "buffer_load_dwordx4 v[76:79], v7, s[36:39], s5 offen offset:3072 sc1\n"
    "s_nop 7\n"
    "buffer_load_dwordx4 v[82:85], v7, s[36:39], s8 offen sc1\n"
    "s_nop 7\n"
    "buffer_load_dwordx4 v[88:91], v7, s[36:39], s8 offen offset:1024 sc1\n"
    "s_nop 7\n"
    "buffer_load_dwordx4 v[94:97], v7, s[36:39], s8 offen offset:2048 sc1\n"
    "s_nop 7\n"
    "buffer_load_dwordx4 v[100:103], v7, s[36:39], s8 offen offset:3072 sc1\n"
    "s_nop 7\n"
    "buffer_load_dwordx4 v[106:109], v7, s[36:39], s9 offen sc1\n"
    "s_nop 7\n"
    "buffer_load_dwordx4 v[112:115], v7, s[36:39], s9 offen offset:1024 sc1\n"
    "s_nop 7\n"
    "buffer_load_dwordx4 v[118:121], v7, s[36:39], s9 offen offset:2048 sc1\n"
    "s_nop 7\n"
    "buffer_load_dwordx4 v[124:127], v7, s[36:39], s9 offen offset:3072 sc1\n"
    "s_waitcnt vmcnt(9)\n"
    "v_mov_b32 v9, s68\n"
    "s_mov_b64 exec, 15\n"
    "buffer_atomic_smax v9, v2, s[36:39], 0 offen sc1\n"
    "s_mov_b64 exec, -1\n"
    "s_add_u32 s68, s68, 1\n"
    "s_cmp_lg_u32 s70, 0\n"
    "s_cbranch_scc1 .Latt_top_%=\n"
    ".Latt_LOOPE_%=:\n"
    "s_cmp_lg_u32 s68, 64\n"
    "s_cbranch_scc1 .Latt_waitp_%=\n"
    "s_waitcnt vmcnt(0)\n"
    "s_branch .Latt_FIN_%=\n"
    ".Latt_waitp_%=:\n"
    "s_sub_u32 s72, s68, 1\n"
    "v_add_u32 v11, v8, v5\n"
    "v_cmp_gt_i32 vcc, s72, v11\n"
    "s_nop 0\n"
    "s_or_b64 exec, vcc, 1\n"
    "buffer_atomic_smax v8, v4, s[36:39], 0 offen sc0 sc1\n"
    "s_mov_b64 exec, -1\n"
    "s_waitcnt vmcnt(0)\n"
    "v_add_u32 v11, v8, v5\n"
    "v_cmp_gt_i32 vcc, s72, v11\n"
    "s_cbranch_vccz .Latt_gop_%=\n"
    "s_sleep 2\n"
    "s_sub_u32 s69, s69, 1\n"
    "s_cbranch_scc0 .Latt_waitp_%=\n"
    "s_mov_b32 s69, 0\n"
    ".Latt_gop_%=:\n"
    "s_and_b32 s4, s68, 7\n"
    "s_mul_i32 s4, s4, 4194304\n"
    "s_add_u32 s4, s4, s52\n"
    "s_add_u32 s5, s4, 4096\n"
    "s_add_u32 s6, s4, 16384\n"
    "s_add_u32 s7, s4, 20480\n"
    "s_add_u32 s8, s4, 32768\n"
    "s_add_u32 s9, s4, 36864\n"
    "buffer_load_dwordx4 v[16:19], v0, s[36:39], s6 offen sc1\n"
    "buffer_load_dwordx4 v[20:23], v0, s[36:39], s6 offen offset:1024 sc1\n"
    "buffer_load_dwordx4 v[24:27], v0, s[36:39], s6 offen offset:2048 sc1\n"
    "buffer_load_dwordx4 v[28:31], v0, s[36:39], s6 offen offset:3072 sc1\n"
    "buffer_load_dwordx4 v[32:35], v0, s[36:39], s7 offen sc1\n"
    "buffer_load_dwordx4 v[36:39], v0, s[36:39], s7 offen offset:1024 sc1\n"
    "buffer_load_dwordx4 v[40:43], v0, s[36:39], s7 offen offset:2048 sc1\n"
    "buffer_load_dwordx4 v[44:47], v0, s[36:39], s7 offen offset:3072 sc1\n"
    "buffer_load_dwordx4 v[48:51], v0, s[36:39], s4 offen sc1\n"
    "buffer_load_dwordx4 v[52:55], v0, s[36:39], s4 offen offset:1024 sc1\n"
    "buffer_load_dwordx4 v[56:59], v0, s[36:39], s4 offen offset:2048 sc1\n"
    "buffer_load_dwordx4 v[60:63], v0, s[36:39], s4 offen offset:3072 sc1\n"
    "buffer_load_dwordx4 v[64:67], v0, s[36:39], s5 offen sc1\n"
    "buffer_load_dwordx4 v[68:71], v0, s[36:39], s5 offen offset:1024 sc1\n"
    "buffer_load_dwordx4 v[72:75], v0, s[36:39], s5 offen offset:2048 sc1\n"
    "buffer_load_dwordx4 v[76:79], v0, s[36:39], s5 offen offset:3072 sc1\n"
    "buffer_load_dwordx4 v[82:85], v0, s[36:39], s8 offen sc1\n"
    "buffer_load_dwordx4 v[88:91], v0, s[36:39], s8 offen offset:1024 sc1\n"
    "buffer_load_dwordx4 v[94:97], v0, s[36:39], s8 offen offset:2048 sc1\n"
    "buffer_load_dwordx4 v[100:103], v0, s[36:39], s8 offen offset:3072 sc1\n"
    "buffer_load_dwordx4 v[106:109], v0, s[36:39], s9 offen sc1\n"
    "buffer_load_dwordx4 v[112:115], v0, s[36:39], s9 offen offset:1024 sc1\n"
    "buffer_load_dwordx4 v[118:121], v0, s[36:39], s9 offen offset:2048 sc1\n"
    "buffer_load_dwordx4 v[124:127], v0, s[36:39], s9 offen offset:3072 sc1\n"
    "s_waitcnt vmcnt(8)\n"
    "s_branch .Latt_top_%=\n"
    ".Latt_waitr_%=:\n"
    "s_sub_u32 s69, s69, 1\n"
    "s_cbranch_scc1 .Latt_deadr_%=\n"
    "s_sleep 2\n"
    "s_or_b64 exec, vcc, 1\n"
    "buffer_atomic_smax v8, v4, s[36:39], 0 offen sc0 sc1\n"
    "s_mov_b64 exec, -1\n"
    "s_waitcnt vmcnt(0)\n"
    "s_branch .Latt_pollr_%=\n"
    ".Latt_deadr_%=:\n"
    "s_mov_b32 s69, 0\n"
    "s_branch .Latt_gor_%=\n"
    ".Latt_FIN_%=:\n"
  );
}

template <int MODE>
DEVI void red_args(const Params& p, int L, int q, u32 dbg, u64 tag, v16u& sa, v16u& sb, v16u& va, v16u& vb) {
  const int tid = threadIdx.x, lane = tid & 63, w = (int)rfl(tid >> 6);
  const int c = lane & 15, g = lane >> 4;
  const bool rowres = MODE == 0 || (MODE == 1 && L == 0);
  const bool fin = MODE == 2 && L == 1;
  const v4u ws = make_srd(p.ws, WS_END), rx = make_srd(p.x, 2048 * 1152), ro = make_srd(p.out, 2048 * 1152);
  u32 ft = OOB;
  if (lane < 32) {
    if (MODE == 1) { if (lane < 24) ft = mbox(L, lane / 6, SL_UP + lane % 6, 4 * q + w); }
    else if (!fin) ft = mbox(MODE == 0 ? 0 : 1, lane >> 3, SL_P + (lane & 7), (MODE == 0 ? 16 * L : 0) + 4 * q + w);
  } else if (MODE != 0) {
    if (lane < 44) ft = mbox(L, (lane - 32) / 3, (MODE == 1 ? SL_WO : SL_DOWN) + (lane - 32) % 3, (MODE == 1 ? 8 : 24) + 4 * q + w);
    else if (lane == 44 && !rowres) ft = MODE == 1 ? mbox(0, q, SL_REDF, 48 + w) : mbox(L, q, SL_REDA, 48 + w);
  }
  const unsigned long long fm = __ballot(ft != OOB);
  sa = cat4(ws, rowres ? rx : ws, fin ? ro : ws, (v4u){(u32)(2 * tag), (u32)((2 * tag) >> 32), 0u, 0u});
  sb = (v16u){pgb(L, MODE == 1 ? B_PA : B_PF),
              rowres ? 0u : (MODE == 1 ? pgb(1, B_X) : pgb(L, B_X1)), rowres ? 36864u : SLOT, (u32)(rowres ? 63 : (MODE == 1 ? RX2 - 1 : RX - 1)), rowres ? 128u : 1024u,
              MODE == 1 ? pgb(L, B_X1) : pgb(1, B_X), (u32)((MODE == 0 || fin) ? 63 : (MODE == 1 ? RX - 1 : RX2 - 1)),
              fin ? 0u : (MODE == 0 ? OFF_XN0 : (MODE == 1 ? pgb(L, B_XN2) : pgb(1, B_XN))), 4096u,
              rowres ? 0u : ~0u, dbg + w * 64, fin ? 0u : ~0u, (u32)fm, (u32)(fm >> 32), (u32)(L == 0 ? 0 : (L == 1 ? LN_B : LN_C)), (u32)(L == 0 ? LN_B - 1 : (L == 1 ? LN_C - 1 : 63))};
  const int need = w == 0 ? 0x10F : (w == 1 ? 0x23F : (w == 2 ? 0x4F0 : 0xF00));
  const bool in = MODE != 0 && lane < 48 && ((need >> (lane % 12)) & 1);
  const bool back = MODE != 0 && lane >= 48 && lane < 52 && !fin;
  auto frag = [&](int I) { return kb_off(2 * (I >> 1)) + (u32)((I & 1) * 512 + (q >> 1) * 1024 + (c >> 3) * 256 + (8 * (q & 1) + (c & 7)) * 16); };
  auto rowm = [&](int I) { return (u32)((8 * q + (c & 7)) * 1152 + (2 * I + (c >> 3)) * 16); };
  const int I0 = 8 * w + 2 * (g & 1) + (g >> 1);
  const u32 pv = (u32)(q * CHUNK + w * 2048 + lane * 16), pv8 = (u32)(4 * CHUNK + (4 * q + w) * 256 + lane * 16);
  va[0] = mbox(L, q, MODE == 1 ? SL_REDA : SL_REDF, lane);
  va[1] = (u32)(in ? 0 : (back ? (MODE == 1 ? RX : RX2) : 1000));
  va[2] = (u32)g * 5 * CHUNK + frag(8 * w);
  va[3] = (u32)g * 5 * CHUNK + frag(32 + w);
  va[4] = fin ? (u32)(q * 9216 + tid * 16) : frag(I0);
  va[5] = fin ? (w == 0 ? (u32)(q * 9216 + 8192 + tid * 16) : OOB) : (g == 0 ? frag(32 + w) : OOB);
  va[6] = rowres ? rowm(I0) : pv;
  va[7] = g == 0 ? (rowres ? rowm(32 + w) : pv8) : OOB;
  va[8] = pv;
  va[9] = g == 0 ? pv8 : OOB;
  va[10] = fin ? rowm(I0) - q * 9216 : (u32)(((c & 7) * 4 + w) * 8);
  va[11] = fin ? (u32)tid * 16 : (u32)((c & 7) * 32);
  va[12] = fin ? (g == 0 ? rowm(32 + w) - q * 9216 : (u32)(12288 + tid * 16)) : (g < 2 ? 0x3F800000u : 0u);
  va[13] = ft; va[14] = 0; va[15] = 0;
  const u32 one = 0x3F80u << (16 * (c & 1));
#pragma unroll
  for (int d = 0; d < 4; ++d) {
    const bool on = ((c & 7) >> 1) == d;
    vb[d] = (on && c < 8) ? one : 0u;
    vb[4 + d] = (on && c >= 8) ? one : 0u;
    vb[8 + d] = (on && g == 2 * (c >> 3)) ? one : 0u;
    vb[12 + d] = (on && g == 2 * (c >> 3) + 1) ? one : 0u;
  }
#pragma unroll
  for (int i = 0; i < 16; ++i) { sa[i] = rfl(sa[i]); sb[i] = rfl(sb[i]); }
}
#define RUN_RED(TEXT) asm volatile(TEXT : "+{s[36:51]}"(sa), "+{s[52:67]}"(sb), "+{v[0:15]}"(va), "+{v[16:31]}"(vb) :: CLOB_RED)
#define CLOB_RED \
  "v32", "v33", "v34", "v35", "v36", "v37", "v38", "v39", "v40", "v41", "v42", "v43", "v44", "v45", "v46", "v47", "v48", "v49", "v50", "v51", "v52", "v53", "v54", "v55", \
  "v56", "v57", "v58", "v59", "v60", "v61", "v62", "v63", "v64", "v65", "v66", "v67", "v68", "v69", "v70", "v71", "v72", "v73", "v74", "v75", "v76", "v77", "v78", "v79", \
  "v80", "v81", "v82", "v83", "v84", "v85", "v86", "v87", "v88", "v89", "v90", "v91", "v92", "v93", "v94", "v95", "v96", "v97", "v98", "v99", "v100", "v101", "v102", "v103", \
  "v104", "v105", "v106", "v107", "v108", "v109", "v110", "v111", "v112", "v113", "v114", "v115", "v116", "v117", "v118", "v119", "v120", "v121", "v122", "v123", "v124", "v125", "v126", "v127", \
  "v128", "v129", "v130", "v131", "v132", "v133", "v134", "v135", "s68", "s69", "s70", "s71", "s72", "s73", "s74", "s75", "s76", "s77", "s78", "s79", "s80", "s81", "s82", "s83", \
  "s84", "s85", "s86", "s87", "s88", "s89", "s90", "s91", "s92", "s93", "s94", "s95", "s96", "s97", "s98", "s99", "vcc", "scc", "memory"
DEVI void role_ln0(const Params& p, int grp, int q, u32 dbg, u64 tag) {
  v16u sa, sb, va, vb;
  red_args<0>(p, grp, q, dbg, tag, sa, sb, va, vb);
  RUN_RED(
    "s_nop 4\n"
    "s_mov_b32 s69, 0\n"
    "v_mov_b32 v122, -1\n"
    "s_mov_b32 s70, 262144\n"
    "s_add_u32 s69, s66, 0\n"
    "s_and_b32 s73, s69, s55\n"
    "s_mul_i32 s73, s73, s54\n"
    "s_add_u32 s73, s73, s53\n"
    "s_add_u32 s74, s73, s56\n"
    "buffer_load_dwordx4 v[40:43], v6, s[40:43], s73 offen sc1\n"
    "buffer_load_dwordx4 v[44:47], v6, s[40:43], s74 offen sc1\n"
    "buffer_load_dwordx4 v[48:51], v7, s[40:43], s73 offen sc1\n"
    "s_add_u32 s69, s66, 1\n"
    "s_and_b32 s73, s69, s55\n"
    "s_mul_i32 s73, s73, s54\n"
    "s_add_u32 s73, s73, s53\n"
    "s_add_u32 s74, s73, s56\n"
    "buffer_load_dwordx4 v[52:55], v6, s[40:43], s73 offen sc1\n"
    "buffer_load_dwordx4 v[56:59], v6, s[40:43], s74 offen sc1\n"
    "buffer_load_dwordx4 v[60:63], v7, s[40:43], s73 offen sc1\n"
    "s_add_u32 s69, s66, 2\n"
    "s_and_b32 s73, s69, s55\n"
    "s_mul_i32 s73, s73, s54\n"
    "s_add_u32 s73, s73, s53\n"
    "s_add_u32 s74, s73, s56\n"
    "buffer_load_dwordx4 v[68:71], v6, s[40:43], s73 offen sc1\n"
    "buffer_load_dwordx4 v[72:75], v6, s[40:43], s74 offen sc1\n"
    "buffer_load_dwordx4 v[76:79], v7, s[40:43], s73 offen sc1\n"
    "s_add_u32 s69, s66, 3\n"
    "s_and_b32 s73, s69, s55\n"
    "s_mul_i32 s73, s73, s54\n"
    "s_add_u32 s73, s73, s53\n"
    "s_add_u32 s74, s73, s56\n"
    "buffer_load_dwordx4 v[124:127], v6, s[40:43], s73 offen sc1\n"
    "buffer_load_dwordx4 v[128:131], v6, s[40:43], s74 offen sc1\n"
    "buffer_load_dwordx4 v[132:135], v7, s[40:43], s73 offen sc1\n"
    "s_sub_u32 s68, s66, 1\n"
    ".LrLN0_par_%=:\n"
    "buffer_load_dwordx2 v[14:15], off, s[36:39], 0 sc1\n"
    "s_waitcnt vmcnt(0)\n"
    "v_readfirstlane_b32 s75, v14\n"
    "v_readfirstlane_b32 s76, v15\n"
    "s_xor_b32 s75, s75, s48\n"
    "s_xor_b32 s76, s76, s49\n"
    "s_cselect_b32 s76, 2, 0\n"
    "s_or_b32 s76, s76, s75\n"
    "s_cmp_lt_u32 s76, 2\n"
    "s_cbranch_scc1 .LrLN0_par1_%=\n"
    "s_sleep 4\n"
    "s_sub_u32 s70, s70, 1\n"
    "s_cbranch_scc0 .LrLN0_par_%=\n"
    "s_mov_b32 s70, 0\n"
    ".LrLN0_par1_%=:\n"
    "s_and_b32 s75, s75, 1\n"
    "s_lshl_b32 s75, s75, 13\n"
    "s_add_u32 s36, s36, s75\n"
    "s_addc_u32 s37, s37, 0\n"
    "s_and_b32 s76, s75, s61\n"
    "s_add_u32 s40, s40, s76\n"
    "s_addc_u32 s41, s41, 0\n"
    "s_and_b32 s76, s75, s63\n"
    "s_add_u32 s44, s44, s76\n"
    "s_addc_u32 s45, s45, 0\n"
    ".LrLN0_LOOPB_%=:\n"
    ".LrLN0_top_%=:\n"
    "s_add_u32 s68, s68, 1\n"
    "s_add_u32 s69, s68, 4\n"
    "s_and_b32 s75, s68, s58\n"
    "s_mul_i32 s77, s75, 81920\n"
    "s_add_u32 s77, s77, s59\n"
    "s_add_u32 s78, s77, s60\n"
    "s_and_b32 s73, s69, s55\n"
    "s_mul_i32 s73, s73, s54\n"
    "s_add_u32 s73, s73, s53\n"
    "s_add_u32 s74, s73, s56\n"
    "s_waitcnt vmcnt(27)\n"
    "v_mfma_f32_16x16x32_bf16 v[80:83], v[24:27], v[40:43], 0\n"
    "v_mfma_f32_16x16x32_bf16 v[84:87], v[28:31], v[40:43], 0\n"
    "s_waitcnt vmcnt(26)\n"
    "v_mfma_f32_16x16x32_bf16 v[88:91], v[24:27], v[44:47], 0\n"
    "v_mfma_f32_16x16x32_bf16 v[92:95], v[28:31], v[44:47], 0\n"
    "s_waitcnt vmcnt(25)\n"
    "v_mfma_f32_16x16x32_bf16 v[96:99], v[24:27], v[48:51], 0\n"
    "buffer_load_dwordx4 v[40:43], v6, s[40:43], s73 offen sc1\n"
    "buffer_load_dwordx4 v[44:47], v6, s[40:43], s74 offen sc1\n"
    "buffer_load_dwordx4 v[48:51], v7, s[40:43], s73 offen sc1\n"
    "v_pk_add_f32 v[112:113], v[80:81], v[82:83]\n"
    "v_pk_add_f32 v[114:115], v[84:85], v[86:87]\n"
    "v_pk_add_f32 v[112:113], v[112:113], v[88:89]\n"
    "v_pk_add_f32 v[114:115], v[114:115], v[90:91]\n"
    "v_pk_add_f32 v[112:113], v[112:113], v[92:93]\n"
    "v_pk_add_f32 v[114:115], v[114:115], v[94:95]\n"
    "v_pk_add_f32 v[112:113], v[112:113], v[96:97]\n"
    "v_pk_add_f32 v[114:115], v[114:115], v[98:99]\n"
    "v_pk_add_f32 v[112:113], v[112:113], v[114:115]\n"
    "v_add_f32 v112, v112, v113\n"
    "s_nop 1\n"
    "v_add_f32_dpp v14, v112, v112 row_ror:8 row_mask:0xf bank_mask:0xf\n"
    "v_add_f32_dpp v15, v112, v112 row_ror:8 row_mask:0xf bank_mask:0xf\n"
    "s_nop 1\n"
    "v_permlane16_swap_b32 v14, v15\n"
    "v_add_f32 v112, v14, v15\n"
    "v_add_f32 v14, v14, v15\n"
    "s_nop 1\n"
    "v_permlane32_swap_b32 v112, v14\n"
    "v_add_f32 v112, v112, v14\n"
    "v_mul_f32 v112, 0x3be38e39, v112\n"
    "v_mul_f32 v114, v112, v12\n"
    "v_pk_add_f32 v[80:81], v[80:81], v[112:113] op_sel_hi:[1,0] neg_lo:[0,1] neg_hi:[0,1]\n"
    "v_pk_add_f32 v[82:83], v[82:83], v[112:113] op_sel_hi:[1,0] neg_lo:[0,1] neg_hi:[0,1]\n"
    "v_pk_add_f32 v[84:85], v[84:85], v[112:113] op_sel_hi:[1,0] neg_lo:[0,1] neg_hi:[0,1]\n"
    "v_pk_add_f32 v[86:87], v[86:87], v[112:113] op_sel_hi:[1,0] neg_lo:[0,1] neg_hi:[0,1]\n"
    "v_pk_add_f32 v[88:89], v[88:89], v[112:113] op_sel_hi:[1,0] neg_lo:[0,1] neg_hi:[0,1]\n"
    "v_pk_add_f32 v[90:91], v[90:91], v[112:113] op_sel_hi:[1,0] neg_lo:[0,1] neg_hi:[0,1]\n"
    "v_pk_add_f32 v[92:93], v[92:93], v[112:113] op_sel_hi:[1,0] neg_lo:[0,1] neg_hi:[0,1]\n"
    "v_pk_add_f32 v[94:95], v[94:95], v[112:113] op_sel_hi:[1,0] neg_lo:[0,1] neg_hi:[0,1]\n"
    "v_pk_add_f32 v[96:97], v[96:97], v[114:115] op_sel_hi:[1,0] neg_lo:[0,1] neg_hi:[0,1]\n"
    "v_pk_add_f32 v[98:99], v[98:99], v[114:115] op_sel_hi:[1,0] neg_lo:[0,1] neg_hi:[0,1]\n"
    "v_pk_mul_f32 v[116:117], v[80:81], v[80:81]\n"
    "v_pk_mul_f32 v[118:119], v[82:83], v[82:83]\n"
    "v_pk_fma_f32 v[116:117], v[84:85], v[84:85], v[116:117]\n"
    "v_pk_fma_f32 v[118:119], v[86:87], v[86:87], v[118:119]\n"
    "v_pk_fma_f32 v[116:117], v[88:89], v[88:89], v[116:117]\n"
    "v_pk_fma_f32 v[118:119], v[90:91], v[90:91], v[118:119]\n"
    "v_pk_fma_f32 v[116:117], v[92:93], v[92:93], v[116:117]\n"
    "v_pk_fma_f32 v[118:119], v[94:95], v[94:95], v[118:119]\n"
    "v_pk_fma_f32 v[116:117], v[96:97], v[96:97], v[116:117]\n"
    "v_pk_fma_f32 v[118:119], v[98:99], v[98:99], v[118:119]\n"
    "v_pk_add_f32 v[116:117], v[116:117], v[118:119]\n"
    "v_add_f32 v113, v116, v117\n"
    "s_nop 1\n"
    "v_add_f32_dpp v14, v113, v113 row_ror:8 row_mask:0xf bank_mask:0xf\n"
    "v_add_f32_dpp v15, v113, v113 row_ror:8 row_mask:0xf bank_mask:0xf\n"
    "s_nop 1\n"
    "v_permlane16_swap_b32 v14, v15\n"
    "v_add_f32 v113, v14, v15\n"
    "v_add_f32 v14, v14, v15\n"
    "s_nop 1\n"
    "v_permlane32_swap_b32 v113, v14\n"
    "v_add_f32 v113, v113, v14\n"
    "ds_write_b64 v10, v[112:113]\n"
    "s_waitcnt vmcnt(17)\n"
    "s_add_u32 s75, s66, 1\n"
    "s_min_i32 s75, s75, s68\n"
    "s_sub_u32 s76, s68, 2\n"
    "s_max_i32 s75, s75, s76\n"
    "s_sub_u32 s75, s75, 1\n"
    "v_mov_b32 v14, s75\n"
    "s_mov_b64 exec, s[64:65]\n"
    "buffer_atomic_smax v14, v13, s[36:39], 0 offen sc1\n"
    "s_mov_b64 exec, -1\n"
    "s_waitcnt lgkmcnt(0)\n"
    "s_barrier\n"
    "ds_read_b128 v[32:35], v11\n"
    "ds_read_b128 v[36:39], v11 offset:16\n"
    "v_xor_b32 v10, 256, v10\n"
    "v_xor_b32 v11, 256, v11\n"
    "s_waitcnt lgkmcnt(0)\n"
    "v_pk_add_f32 v[114:115], v[32:33], v[34:35]\n"
    "v_pk_add_f32 v[116:117], v[36:37], v[38:39]\n"
    "v_pk_add_f32 v[114:115], v[114:115], v[116:117]\n"
    "v_fmac_f32 v32, 0xbe800000, v114\n"
    "v_fmac_f32 v34, 0xbe800000, v114\n"
    "v_fmac_f32 v36, 0xbe800000, v114\n"
    "v_fmac_f32 v38, 0xbe800000, v114\n"
    "v_fmac_f32 v112, 0xbe800000, v114\n"
    "v_mul_f32 v116, v32, v32\n"
    "v_fmac_f32 v116, v34, v34\n"
    "v_fmac_f32 v116, v36, v36\n"
    "v_fmac_f32 v116, v38, v38\n"
    "v_fmac_f32 v115, 0x43100000, v116\n"
    "v_mov_b32 v116, 0x3727c5ac\n"
    "v_fmac_f32 v116, 0x3ae38e39, v115\n"
    "v_rsq_f32 v116, v116\n"
    "s_nop 0\n"
    "v_mul_f32 v114, v112, v116\n"
    "v_pk_fma_f32 v[80:81], v[80:81], v[116:117], v[114:115] op_sel_hi:[1,0,0]\n"
    "v_pk_fma_f32 v[82:83], v[82:83], v[116:117], v[114:115] op_sel_hi:[1,0,0]\n"
    "v_pk_fma_f32 v[84:85], v[84:85], v[116:117], v[114:115] op_sel_hi:[1,0,0]\n"
    "v_pk_fma_f32 v[86:87], v[86:87], v[116:117], v[114:115] op_sel_hi:[1,0,0]\n"
    "v_pk_fma_f32 v[88:89], v[88:89], v[116:117], v[114:115] op_sel_hi:[1,0,0]\n"
    "v_pk_fma_f32 v[90:91], v[90:91], v[116:117], v[114:115] op_sel_hi:[1,0,0]\n"
    "v_pk_fma_f32 v[92:93], v[92:93], v[116:117], v[114:115] op_sel_hi:[1,0,0]\n"
    "v_pk_fma_f32 v[94:95], v[94:95], v[116:117], v[114:115] op_sel_hi:[1,0,0]\n"
    "v_pk_fma_f32 v[96:97], v[96:97], v[116:117], v[114:115] op_sel_hi:[1,0,0]\n"
    "v_pk_fma_f32 v[98:99], v[98:99], v[116:117], v[114:115] op_sel_hi:[1,0,0]\n"
    "v_cvt_pk_bf16_f32 v100, v80, v81\n"
    "v_cvt_pk_bf16_f32 v101, v82, v83\n"
    "v_cvt_pk_bf16_f32 v102, v84, v85\n"
    "v_cvt_pk_bf16_f32 v103, v86, v87\n"
    "v_cvt_pk_bf16_f32 v104, v88, v89\n"
    "v_cvt_pk_bf16_f32 v105, v90, v91\n"
    "v_cvt_pk_bf16_f32 v106, v92, v93\n"
    "v_cvt_pk_bf16_f32 v107, v94, v95\n"
    "v_cvt_pk_bf16_f32 v108, v96, v97\n"
    "v_cvt_pk_bf16_f32 v109, v98, v99\n"
    "v_permlane16_swap_b32 v100, v102\n"
    "v_permlane16_swap_b32 v101, v103\n"
    "buffer_store_dwordx4 v[100:103], v4, s[44:47], s77 offen sc1\n"
    "v_permlane16_swap_b32 v104, v106\n"
    "v_permlane16_swap_b32 v105, v107\n"
    "buffer_store_dwordx4 v[104:107], v4, s[44:47], s78 offen sc1\n"
    "v_permlane16_swap_b32 v108, v110\n"
    "v_permlane16_swap_b32 v109, v111\n"
    "buffer_store_dwordx4 v[108:111], v5, s[44:47], s77 offen sc1\n"
    "s_cmp_eq_u32 s68, s66\n"
    "s_cbranch_scc1 .LrLN0_first_%=\n"
    ".LrLN0_first1_%=:\n"
    "s_add_u32 s68, s68, 1\n"
    "s_add_u32 s69, s68, 4\n"
    "s_and_b32 s75, s68, s58\n"
    "s_mul_i32 s77, s75, 81920\n"
    "s_add_u32 s77, s77, s59\n"
    "s_add_u32 s78, s77, s60\n"
    "s_and_b32 s73, s69, s55\n"
    "s_mul_i32 s73, s73, s54\n"
    "s_add_u32 s73, s73, s53\n"
    "s_add_u32 s74, s73, s56\n"
    "s_waitcnt vmcnt(27)\n"
    "v_mfma_f32_16x16x32_bf16 v[80:83], v[24:27], v[52:55], 0\n"
    "v_mfma_f32_16x16x32_bf16 v[84:87], v[28:31], v[52:55], 0\n"
    "s_waitcnt vmcnt(26)\n"
    "v_mfma_f32_16x16x32_bf16 v[88:91], v[24:27], v[56:59], 0\n"
    "v_mfma_f32_16x16x32_bf16 v[92:95], v[28:31], v[56:59], 0\n"
    "s_waitcnt vmcnt(25)\n"
    "v_mfma_f32_16x16x32_bf16 v[96:99], v[24:27], v[60:63], 0\n"
    "buffer_load_dwordx4 v[52:55], v6, s[40:43], s73 offen sc1\n"
    "buffer_load_dwordx4 v[56:59], v6, s[40:43], s74 offen sc1\n"
    "buffer_load_dwordx4 v[60:63], v7, s[40:43], s73 offen sc1\n"
    "v_pk_add_f32 v[112:113], v[80:81], v[82:83]\n"
    "v_pk_add_f32 v[114:115], v[84:85], v[86:87]\n"
    "v_pk_add_f32 v[112:113], v[112:113], v[88:89]\n"
    "v_pk_add_f32 v[114:115], v[114:115], v[90:91]\n"
    "v_pk_add_f32 v[112:113], v[112:113], v[92:93]\n"
    "v_pk_add_f32 v[114:115], v[114:115], v[94:95]\n"
    "v_pk_add_f32 v[112:113], v[112:113], v[96:97]\n"
    "v_pk_add_f32 v[114:115], v[114:115], v[98:99]\n"
    "v_pk_add_f32 v[112:113], v[112:113], v[114:115]\n"
    "v_add_f32 v112, v112, v113\n"
    "s_nop 1\n"
    "v_add_f32_dpp v14, v112, v112 row_ror:8 row_mask:0xf bank_mask:0xf\n"
    "v_add_f32_dpp v15, v112, v112 row_ror:8 row_mask:0xf bank_mask:0xf\n"
    "s_nop 1\n"
    "v_permlane16_swap_b32 v14, v15\n"
    "v_add_f32 v112, v14, v15\n"
    "v_add_f32 v14, v14, v15\n"
    "s_nop 1\n"
    "v_permlane32_swap_b32 v112, v14\n"
    "v_add_f32 v112, v112, v14\n"
    "v_mul_f32 v112, 0x3be38e39, v112\n"
    "v_mul_f32 v114, v112, v12\n"
    "v_pk_add_f32 v[80:81], v[80:81], v[112:113] op_sel_hi:[1,0] neg_lo:[0,1] neg_hi:[0,1]\n"
    "v_pk_add_f32 v[82:83], v[82:83], v[112:113] op_sel_hi:[1,0] neg_lo:[0,1] neg_hi:[0,1]\n"
    "v_pk_add_f32 v[84:85], v[84:85], v[112:113] op_sel_hi:[1,0] neg_lo:[0,1] neg_hi:[0,1]\n"
    "v_pk_add_f32 v[86:87], v[86:87], v[112:113] op_sel_hi:[1,0] neg_lo:[0,1] neg_hi:[0,1]\n"
    "v_pk_add_f32 v[88:89], v[88:89], v[112:113] op_sel_hi:[1,0] neg_lo:[0,1] neg_hi:[0,1]\n"
    "v_pk_add_f32 v[90:91], v[90:91], v[112:113] op_sel_hi:[1,0] neg_lo:[0,1] neg_hi:[0,1]\n"
    "v_pk_add_f32 v[92:93], v[92:93], v[112:113] op_sel_hi:[1,0] neg_lo:[0,1] neg_hi:[0,1]\n"
    "v_pk_add_f32 v[94:95], v[94:95], v[112:113] op_sel_hi:[1,0] neg_lo:[0,1] neg_hi:[0,1]\n"
    "v_pk_add_f32 v[96:97], v[96:97], v[114:115] op_sel_hi:[1,0] neg_lo:[0,1] neg_hi:[0,1]\n"
    "v_pk_add_f32 v[98:99], v[98:99], v[114:115] op_sel_hi:[1,0] neg_lo:[0,1] neg_hi:[0,1]\n"
    "v_pk_mul_f32 v[116:117], v[80:81], v[80:81]\n"
    "v_pk_mul_f32 v[118:119], v[82:83], v[82:83]\n"
    "v_pk_fma_f32 v[116:117], v[84:85], v[84:85], v[116:117]\n"
    "v_pk_fma_f32 v[118:119], v[86:87], v[86:87], v[118:119]\n"
    "v_pk_fma_f32 v[116:117], v[88:89], v[88:89], v[116:117]\n"
    "v_pk_fma_f32 v[118:119], v[90:91], v[90:91], v[118:119]\n"
    "v_pk_fma_f32 v[116:117], v[92:93], v[92:93], v[116:117]\n"
    "v_pk_fma_f32 v[118:119], v[94:95], v[94:95], v[118:119]\n"
    "v_pk_fma_f32 v[116:117], v[96:97], v[96:97], v[116:117]\n"
    "v_pk_fma_f32 v[118:119], v[98:99], v[98:99], v[118:119]\n"
    "v_pk_add_f32 v[116:117], v[116:117], v[118:119]\n"
    "v_add_f32 v113, v116, v117\n"
    "s_nop 1\n"
    "v_add_f32_dpp v14, v113, v113 row_ror:8 row_mask:0xf bank_mask:0xf\n"
    "v_add_f32_dpp v15, v113, v113 row_ror:8 row_mask:0xf bank_mask:0xf\n"
    "s_nop 1\n"
    "v_permlane16_swap_b32 v14, v15\n"
    "v_add_f32 v113, v14, v15\n"
    "v_add_f32 v14, v14, v15\n"
    "s_nop 1\n"
    "v_permlane32_swap_b32 v113, v14\n"
    "v_add_f32 v113, v113, v14\n"
    "ds_write_b64 v10, v[112:113]\n"
    "s_waitcnt vmcnt(17)\n"
    "s_add_u32 s75, s66, 1\n"
    "s_min_i32 s75, s75, s68\n"
    "s_sub_u32 s76, s68, 2\n"
    "s_max_i32 s75, s75, s76\n"
    "s_sub_u32 s75, s75, 1\n"
    "v_mov_b32 v14, s75\n"
    "s_mov_b64 exec, s[64:65]\n"
    "buffer_atomic_smax v14, v13, s[36:39], 0 offen sc1\n"
    "s_mov_b64 exec, -1\n"
    "s_waitcnt lgkmcnt(0)\n"
    "s_barrier\n"
    "ds_read_b128 v[32:35], v11\n"
    "ds_read_b128 v[36:39], v11 offset:16\n"
    "v_xor_b32 v10, 256, v10\n"
    "v_xor_b32 v11, 256, v11\n"
    "s_waitcnt lgkmcnt(0)\n"
    "v_pk_add_f32 v[114:115], v[32:33], v[34:35]\n"
    "v_pk_add_f32 v[116:117], v[36:37], v[38:39]\n"
    "v_pk_add_f32 v[114:115], v[114:115], v[116:117]\n"
    "v_fmac_f32 v32, 0xbe800000, v114\n"
    "v_fmac_f32 v34, 0xbe800000, v114\n"
    "v_fmac_f32 v36, 0xbe800000, v114\n"
    "v_fmac_f32 v38, 0xbe800000, v114\n"
    "v_fmac_f32 v112, 0xbe800000, v114\n"
    "v_mul_f32 v116, v32, v32\n"
    "v_fmac_f32 v116, v34, v34\n"
    "v_fmac_f32 v116, v36, v36\n"
    "v_fmac_f32 v116, v38, v38\n"
    "v_fmac_f32 v115, 0x43100000, v116\n"
    "v_mov_b32 v116, 0x3727c5ac\n"
    "v_fmac_f32 v116, 0x3ae38e39, v115\n"
    "v_rsq_f32 v116, v116\n"
    "s_nop 0\n"
    "v_mul_f32 v114, v112, v116\n"
    "v_pk_fma_f32 v[80:81], v[80:81], v[116:117], v[114:115] op_sel_hi:[1,0,0]\n"
    "v_pk_fma_f32 v[82:83], v[82:83], v[116:117], v[114:115] op_sel_hi:[1,0,0]\n"
    "v_pk_fma_f32 v[84:85], v[84:85], v[116:117], v[114:115] op_sel_hi:[1,0,0]\n"
    "v_pk_fma_f32 v[86:87], v[86:87], v[116:117], v[114:115] op_sel_hi:[1,0,0]\n"
    "v_pk_fma_f32 v[88:89], v[88:89], v[116:117], v[114:115] op_sel_hi:[1,0,0]\n"
    "v_pk_fma_f32 v[90:91], v[90:91], v[116:117], v[114:115] op_sel_hi:[1,0,0]\n"
    "v_pk_fma_f32 v[92:93], v[92:93], v[116:117], v[114:115] op_sel_hi:[1,0,0]\n"
    "v_pk_fma_f32 v[94:95], v[94:95], v[116:117], v[114:115] op_sel_hi:[1,0,0]\n"
    "v_pk_fma_f32 v[96:97], v[96:97], v[116:117], v[114:115] op_sel_hi:[1,0,0]\n"
    "v_pk_fma_f32 v[98:99], v[98:99], v[116:117], v[114:115] op_sel_hi:[1,0,0]\n"
    "v_cvt_pk_bf16_f32 v100, v80, v81\n"
    "v_cvt_pk_bf16_f32 v101, v82, v83\n"
    "v_cvt_pk_bf16_f32 v102, v84, v85\n"
    "v_cvt_pk_bf16_f32 v103, v86, v87\n"
    "v_cvt_pk_bf16_f32 v104, v88, v89\n"
    "v_cvt_pk_bf16_f32 v105, v90, v91\n"
    "v_cvt_pk_bf16_f32 v106, v92, v93\n"
    "v_cvt_pk_bf16_f32 v107, v94, v95\n"
    "v_cvt_pk_bf16_f32 v108, v96, v97\n"
    "v_cvt_pk_bf16_f32 v109, v98, v99\n"
    "v_permlane16_swap_b32 v100, v102\n"
    "v_permlane16_swap_b32 v101, v103\n"
    "buffer_store_dwordx4 v[100:103], v4, s[44:47], s77 offen sc1\n"
    "v_permlane16_swap_b32 v104, v106\n"
    "v_permlane16_swap_b32 v105, v107\n"
    "buffer_store_dwordx4 v[104:107], v4, s[44:47], s78 offen sc1\n"
    "v_permlane16_swap_b32 v108, v110\n"
    "v_permlane16_swap_b32 v109, v111\n"
    "buffer_store_dwordx4 v[108:111], v5, s[44:47], s77 offen sc1\n"
    "s_add_u32 s68, s68, 1\n"
    "s_add_u32 s69, s68, 4\n"
    "s_and_b32 s75, s68, s58\n"
    "s_mul_i32 s77, s75, 81920\n"
    "s_add_u32 s77, s77, s59\n"
    "s_add_u32 s78, s77, s60\n"
    "s_and_b32 s73, s69, s55\n"
    "s_mul_i32 s73, s73, s54\n"
    "s_add_u32 s73, s73, s53\n"
    "s_add_u32 s74, s73, s56\n"
    "s_waitcnt vmcnt(27)\n"
    "v_mfma_f32_16x16x32_bf16 v[80:83], v[24:27], v[68:71], 0\n"
    "v_mfma_f32_16x16x32_bf16 v[84:87], v[28:31], v[68:71], 0\n"
    "s_waitcnt vmcnt(26)\n"
    "v_mfma_f32_16x16x32_bf16 v[88:91], v[24:27], v[72:75], 0\n"
    "v_mfma_f32_16x16x32_bf16 v[92:95], v[28:31], v[72:75], 0\n"
    "s_waitcnt vmcnt(25)\n"
    "v_mfma_f32_16x16x32_bf16 v[96:99], v[24:27], v[76:79], 0\n"
    "buffer_load_dwordx4 v[68:71], v6, s[40:43], s73 offen sc1\n"
    "buffer_load_dwordx4 v[72:75], v6, s[40:43], s74 offen sc1\n"
    "buffer_load_dwordx4 v[76:79], v7, s[40:43], s73 offen sc1\n"
    "v_pk_add_f32 v[112:113], v[80:81], v[82:83]\n"
    "v_pk_add_f32 v[114:115], v[84:85], v[86:87]\n"
    "v_pk_add_f32 v[112:113], v[112:113], v[88:89]\n"
    "v_pk_add_f32 v[114:115], v[114:115], v[90:91]\n"
    "v_pk_add_f32 v[112:113], v[112:113], v[92:93]\n"
    "v_pk_add_f32 v[114:115], v[114:115], v[94:95]\n"
    "v_pk_add_f32 v[112:113], v[112:113], v[96:97]\n"
    "v_pk_add_f32 v[114:115], v[114:115], v[98:99]\n"
    "v_pk_add_f32 v[112:113], v[112:113], v[114:115]\n"
    "v_add_f32 v112, v112, v113\n"
    "s_nop 1\n"
    "v_add_f32_dpp v14, v112, v112 row_ror:8 row_mask:0xf bank_mask:0xf\n"
    "v_add_f32_dpp v15, v112, v112 row_ror:8 row_mask:0xf bank_mask:0xf\n"
    "s_nop 1\n"
    "v_permlane16_swap_b32 v14, v15\n"
    "v_add_f32 v112, v14, v15\n"
    "v_add_f32 v14, v14, v15\n"
    "s_nop 1\n"
    "v_permlane32_swap_b32 v112, v14\n"
    "v_add_f32 v112, v112, v14\n"
    "v_mul_f32 v112, 0x3be38e39, v112\n"
    "v_mul_f32 v114, v112, v12\n"
    "v_pk_add_f32 v[80:81], v[80:81], v[112:113] op_sel_hi:[1,0] neg_lo:[0,1] neg_hi:[0,1]\n"
    "v_pk_add_f32 v[82:83], v[82:83], v[112:113] op_sel_hi:[1,0] neg_lo:[0,1] neg_hi:[0,1]\n"
    "v_pk_add_f32 v[84:85], v[84:85], v[112:113] op_sel_hi:[1,0] neg_lo:[0,1] neg_hi:[0,1]\n"
    "v_pk_add_f32 v[86:87], v[86:87], v[112:113] op_sel_hi:[1,0] neg_lo:[0,1] neg_hi:[0,1]\n"
    "v_pk_add_f32 v[88:89], v[88:89], v[112:113] op_sel_hi:[1,0] neg_lo:[0,1] neg_hi:[0,1]\n"
    "v_pk_add_f32 v[90:91], v[90:91], v[112:113] op_sel_hi:[1,0] neg_lo:[0,1] neg_hi:[0,1]\n"
    "v_pk_add_f32 v[92:93], v[92:93], v[112:113] op_sel_hi:[1,0] neg_lo:[0,1] neg_hi:[0,1]\n"
    "v_pk_add_f32 v[94:95], v[94:95], v[112:113] op_sel_hi:[1,0] neg_lo:[0,1] neg_hi:[0,1]\n"
    "v_pk_add_f32 v[96:97], v[96:97], v[114:115] op_sel_hi:[1,0] neg_lo:[0,1] neg_hi:[0,1]\n"
    "v_pk_add_f32 v[98:99], v[98:99], v[114:115] op_sel_hi:[1,0] neg_lo:[0,1] neg_hi:[0,1]\n"
    "v_pk_mul_f32 v[116:117], v[80:81], v[80:81]\n"
    "v_pk_mul_f32 v[118:119], v[82:83], v[82:83]\n"
    "v_pk_fma_f32 v[116:117], v[84:85], v[84:85], v[116:117]\n"
    "v_pk_fma_f32 v[118:119], v[86:87], v[86:87], v[118:119]\n"
    "v_pk_fma_f32 v[116:117], v[88:89], v[88:89], v[116:117]\n"
    "v_pk_fma_f32 v[118:119], v[90:91], v[90:91], v[118:119]\n"
    "v_pk_fma_f32 v[116:117], v[92:93], v[92:93], v[116:117]\n"
    "v_pk_fma_f32 v[118:119], v[94:95], v[94:95], v[118:119]\n"
    "v_pk_fma_f32 v[116:117], v[96:97], v[96:97], v[116:117]\n"
    "v_pk_fma_f32 v[118:119], v[98:99], v[98:99], v[118:119]\n"
    "v_pk_add_f32 v[116:117], v[116:117], v[118:119]\n"
    "v_add_f32 v113, v116, v117\n"
    "s_nop 1\n"
    "v_add_f32_dpp v14, v113, v113 row_ror:8 row_mask:0xf bank_mask:0xf\n"
    "v_add_f32_dpp v15, v113, v113 row_ror:8 row_mask:0xf bank_mask:0xf\n"
    "s_nop 1\n"
    "v_permlane16_swap_b32 v14, v15\n"
    "v_add_f32 v113, v14, v15\n"
    "v_add_f32 v14, v14, v15\n"
    "s_nop 1\n"
    "v_permlane32_swap_b32 v113, v14\n"
    "v_add_f32 v113, v113, v14\n"
    "ds_write_b64 v10, v[112:113]\n"
    "s_waitcnt vmcnt(17)\n"
    "s_add_u32 s75, s66, 1\n"
    "s_min_i32 s75, s75, s68\n"
    "s_sub_u32 s76, s68, 2\n"
    "s_max_i32 s75, s75, s76\n"
    "s_sub_u32 s75, s75, 1\n"
    "v_mov_b32 v14, s75\n"
    "s_mov_b64 exec, s[64:65]\n"
    "buffer_atomic_smax v14, v13, s[36:39], 0 offen sc1\n"
    "s_mov_b64 exec, -1\n"
    "s_waitcnt lgkmcnt(0)\n"
    "s_barrier\n"
    "ds_read_b128 v[32:35], v11\n"
    "ds_read_b128 v[36:39], v11 offset:16\n"
    "v_xor_b32 v10, 256, v10\n"
    "v_xor_b32 v11, 256, v11\n"
    "s_waitcnt lgkmcnt(0)\n"
    "v_pk_add_f32 v[114:115], v[32:33], v[34:35]\n"
    "v_pk_add_f32 v[116:117], v[36:37], v[38:39]\n"
    "v_pk_add_f32 v[114:115], v[114:115], v[116:117]\n"
    "v_fmac_f32 v32, 0xbe800000, v114\n"
    "v_fmac_f32 v34, 0xbe800000, v114\n"
    "v_fmac_f32 v36, 0xbe800000, v114\n"
    "v_fmac_f32 v38, 0xbe800000, v114\n"
    "v_fmac_f32 v112, 0xbe800000, v114\n"
    "v_mul_f32 v116, v32, v32\n"
    "v_fmac_f32 v116, v34, v34\n"
    "v_fmac_f32 v116, v36, v36\n"
    "v_fmac_f32 v116, v38, v38\n"
    "v_fmac_f32 v115, 0x43100000, v116\n"
    "v_mov_b32 v116, 0x3727c5ac\n"
    "v_fmac_f32 v116, 0x3ae38e39, v115\n"
    "v_rsq_f32 v116, v116\n"
    "s_nop 0\n"
    "v_mul_f32 v114, v112, v116\n"
    "v_pk_fma_f32 v[80:81], v[80:81], v[116:117], v[114:115] op_sel_hi:[1,0,0]\n"
    "v_pk_fma_f32 v[82:83], v[82:83], v[116:117], v[114:115] op_sel_hi:[1,0,0]\n"
    "v_pk_fma_f32 v[84:85], v[84:85], v[116:117], v[114:115] op_sel_hi:[1,0,0]\n"
    "v_pk_fma_f32 v[86:87], v[86:87], v[116:117], v[114:115] op_sel_hi:[1,0,0]\n"
    "v_pk_fma_f32 v[88:89], v[88:89], v[116:117], v[114:115] op_sel_hi:[1,0,0]\n"
    "v_pk_fma_f32 v[90:91], v[90:91], v[116:117], v[114:115] op_sel_hi:[1,0,0]\n"
    "v_pk_fma_f32 v[92:93], v[92:93], v[116:117], v[114:115] op_sel_hi:[1,0,0]\n"
    "v_pk_fma_f32 v[94:95], v[94:95], v[116:117], v[114:115] op_sel_hi:[1,0,0]\n"
    "v_pk_fma_f32 v[96:97], v[96:97], v[116:117], v[114:115] op_sel_hi:[1,0,0]\n"
    "v_pk_fma_f32 v[98:99], v[98:99], v[116:117], v[114:115] op_sel_hi:[1,0,0]\n"
    "v_cvt_pk_bf16_f32 v100, v80, v81\n"
    "v_cvt_pk_bf16_f32 v101, v82, v83\n"
    "v_cvt_pk_bf16_f32 v102, v84, v85\n"
    "v_cvt_pk_bf16_f32 v103, v86, v87\n"
    "v_cvt_pk_bf16_f32 v104, v88, v89\n"
    "v_cvt_pk_bf16_f32 v105, v90, v91\n"
    "v_cvt_pk_bf16_f32 v106, v92, v93\n"
    "v_cvt_pk_bf16_f32 v107, v94, v95\n"
    "v_cvt_pk_bf16_f32 v108, v96, v97\n"
    "v_cvt_pk_bf16_f32 v109, v98, v99\n"
    "v_permlane16_swap_b32 v100, v102\n"
    "v_permlane16_swap_b32 v101, v103\n"
    "buffer_store_dwordx4 v[100:103], v4, s[44:47], s77 offen sc1\n"
    "v_permlane16_swap_b32 v104, v106\n"
    "v_permlane16_swap_b32 v105, v107\n"
    "buffer_store_dwordx4 v[104:107], v4, s[44:47], s78 offen sc1\n"
    "v_permlane16_swap_b32 v108, v110\n"
    "v_permlane16_swap_b32 v109, v111\n"
    "buffer_store_dwordx4 v[108:111], v5, s[44:47], s77 offen sc1\n"
    "s_add_u32 s68, s68, 1\n"
    "s_add_u32 s69, s68, 4\n"
    "s_and_b32 s75, s68, s58\n"
    "s_mul_i32 s77, s75, 81920\n"
    "s_add_u32 s77, s77, s59\n"
    "s_add_u32 s78, s77, s60\n"
    "s_and_b32 s73, s69, s55\n"
    "s_mul_i32 s73, s73, s54\n"
    "s_add_u32 s73, s73, s53\n"
    "s_add_u32 s74, s73, s56\n"
    "s_waitcnt vmcnt(27)\n"
    "v_mfma_f32_16x16x32_bf16 v[80:83], v[24:27], v[124:127], 0\n"
    "v_mfma_f32_16x16x32_bf16 v[84:87], v[28:31], v[124:127], 0\n"
    "s_waitcnt vmcnt(26)\n"
    "v_mfma_f32_16x16x32_bf16 v[88:91], v[24:27], v[128:131], 0\n"
    "v_mfma_f32_16x16x32_bf16 v[92:95], v[28:31], v[128:131], 0\n"
    "s_waitcnt vmcnt(25)\n"
    "v_mfma_f32_16x16x32_bf16 v[96:99], v[24:27], v[132:135], 0\n"
    "buffer_load_dwordx4 v[124:127], v6, s[40:43], s73 offen sc1\n"
    "buffer_load_dwordx4 v[128:131], v6, s[40:43], s74 offen sc1\n"
    "buffer_load_dwordx4 v[132:135], v7, s[40:43], s73 offen sc1\n"
    "v_pk_add_f32 v[112:113], v[80:81], v[82:83]\n"
    "v_pk_add_f32 v[114:115], v[84:85], v[86:87]\n"
    "v_pk_add_f32 v[112:113], v[112:113], v[88:89]\n"
    "v_pk_add_f32 v[114:115], v[114:115], v[90:91]\n"
    "v_pk_add_f32 v[112:113], v[112:113], v[92:93]\n"
    "v_pk_add_f32 v[114:115], v[114:115], v[94:95]\n"
    "v_pk_add_f32 v[112:113], v[112:113], v[96:97]\n"
    "v_pk_add_f32 v[114:115], v[114:115], v[98:99]\n"
    "v_pk_add_f32 v[112:113], v[112:113], v[114:115]\n"
    "v_add_f32 v112, v112, v113\n"
    "s_nop 1\n"
    "v_add_f32_dpp v14, v112, v112 row_ror:8 row_mask:0xf bank_mask:0xf\n"
    "v_add_f32_dpp v15, v112, v112 row_ror:8 row_mask:0xf bank_mask:0xf\n"
    "s_nop 1\n"
    "v_permlane16_swap_b32 v14, v15\n"
    "v_add_f32 v112, v14, v15\n"
    "v_add_f32 v14, v14, v15\n"
    "s_nop 1\n"
    "v_permlane32_swap_b32 v112, v14\n"
    "v_add_f32 v112, v112, v14\n"
    "v_mul_f32 v112, 0x3be38e39, v112\n"
    "v_mul_f32 v114, v112, v12\n"
    "v_pk_add_f32 v[80:81], v[80:81], v[112:113] op_sel_hi:[1,0] neg_lo:[0,1] neg_hi:[0,1]\n"
    "v_pk_add_f32 v[82:83], v[82:83], v[112:113] op_sel_hi:[1,0] neg_lo:[0,1] neg_hi:[0,1]\n"
    "v_pk_add_f32 v[84:85], v[84:85], v[112:113] op_sel_hi:[1,0] neg_lo:[0,1] neg_hi:[0,1]\n"
    "v_pk_add_f32 v[86:87], v[86:87], v[112:113] op_sel_hi:[1,0] neg_lo:[0,1] neg_hi:[0,1]\n"
    "v_pk_add_f32 v[88:89], v[88:89], v[112:113] op_sel_hi:[1,0] neg_lo:[0,1] neg_hi:[0,1]\n"
    "v_pk_add_f32 v[90:91], v[90:91], v[112:113] op_sel_hi:[1,0] neg_lo:[0,1] neg_hi:[0,1]\n"
    "v_pk_add_f32 v[92:93], v[92:93], v[112:113] op_sel_hi:[1,0] neg_lo:[0,1] neg_hi:[0,1]\n"
    "v_pk_add_f32 v[94:95], v[94:95], v[112:113] op_sel_hi:[1,0] neg_lo:[0,1] neg_hi:[0,1]\n"
    "v_pk_add_f32 v[96:97], v[96:97], v[114:115] op_sel_hi:[1,0] neg_lo:[0,1] neg_hi:[0,1]\n"
    "v_pk_add_f32 v[98:99], v[98:99], v[114:115] op_sel_hi:[1,0] neg_lo:[0,1] neg_hi:[0,1]\n"
    "v_pk_mul_f32 v[116:117], v[80:81], v[80:81]\n"
    "v_pk_mul_f32 v[118:119], v[82:83], v[82:83]\n"
    "v_pk_fma_f32 v[116:117], v[84:85], v[84:85], v[116:117]\n"
    "v_pk_fma_f32 v[118:119], v[86:87], v[86:87], v[118:119]\n"
    "v_pk_fma_f32 v[116:117], v[88:89], v[88:89], v[116:117]\n"
    "v_pk_fma_f32 v[118:119], v[90:91], v[90:91], v[118:119]\n"
    "v_pk_fma_f32 v[116:117], v[92:93], v[92:93], v[116:117]\n"
    "v_pk_fma_f32 v[118:119], v[94:95], v[94:95], v[118:119]\n"
    "v_pk_fma_f32 v[116:117], v[96:97], v[96:97], v[116:117]\n"
    "v_pk_fma_f32 v[118:119], v[98:99], v[98:99], v[118:119]\n"
    "v_pk_add_f32 v[116:117], v[116:117], v[118:119]\n"
    "v_add_f32 v113, v116, v117\n"
    "s_nop 1\n"
    "v_add_f32_dpp v14, v113, v113 row_ror:8 row_mask:0xf bank_mask:0xf\n"
    "v_add_f32_dpp v15, v113, v113 row_ror:8 row_mask:0xf bank_mask:0xf\n"
    "s_nop 1\n"
    "v_permlane16_swap_b32 v14, v15\n"
    "v_add_f32 v113, v14, v15\n"
    "v_add_f32 v14, v14, v15\n"
    "s_nop 1\n"
    "v_permlane32_swap_b32 v113, v14\n"
    "v_add_f32 v113, v113, v14\n"
    "ds_write_b64 v10, v[112:113]\n"
    "s_waitcnt vmcnt(17)\n"
    "s_add_u32 s75, s66, 1\n"
    "s_min_i32 s75, s75, s68\n"
    "s_sub_u32 s76, s68, 2\n"
    "s_max_i32 s75, s75, s76\n"
    "s_sub_u32 s75, s75, 1\n"
    "v_mov_b32 v14, s75\n"
    "s_mov_b64 exec, s[64:65]\n"
    "buffer_atomic_smax v14, v13, s[36:39], 0 offen sc1\n"
    "s_mov_b64 exec, -1\n"
    "s_waitcnt lgkmcnt(0)\n"
    "s_barrier\n"
    "ds_read_b128 v[32:35], v11\n"
    "ds_read_b128 v[36:39], v11 offset:16\n"
    "v_xor_b32 v10, 256, v10\n"
    "v_xor_b32 v11, 256, v11\n"
    "s_waitcnt lgkmcnt(0)\n"
    "v_pk_add_f32 v[114:115], v[32:33], v[34:35]\n"
    "v_pk_add_f32 v[116:117], v[36:37], v[38:39]\n"
    "v_pk_add_f32 v[114:115], v[114:115], v[116:117]\n"
    "v_fmac_f32 v32, 0xbe800000, v114\n"
    "v_fmac_f32 v34, 0xbe800000, v114\n"
    "v_fmac_f32 v36, 0xbe800000, v114\n"
    "v_fmac_f32 v38, 0xbe800000, v114\n"
    "v_fmac_f32 v112, 0xbe800000, v114\n"
    "v_mul_f32 v116, v32, v32\n"
    "v_fmac_f32 v116, v34, v34\n"
    "v_fmac_f32 v116, v36, v36\n"
    "v_fmac_f32 v116, v38, v38\n"
    "v_fmac_f32 v115, 0x43100000, v116\n"
    "v_mov_b32 v116, 0x3727c5ac\n"
    "v_fmac_f32 v116, 0x3ae38e39, v115\n"
    "v_rsq_f32 v116, v116\n"
    "s_nop 0\n"
    "v_mul_f32 v114, v112, v116\n"
    "v_pk_fma_f32 v[80:81], v[80:81], v[116:117], v[114:115] op_sel_hi:[1,0,0]\n"
    "v_pk_fma_f32 v[82:83], v[82:83], v[116:117], v[114:115] op_sel_hi:[1,0,0]\n"
    "v_pk_fma_f32 v[84:85], v[84:85], v[116:117], v[114:115] op_sel_hi:[1,0,0]\n"
    "v_pk_fma_f32 v[86:87], v[86:87], v[116:117], v[114:115] op_sel_hi:[1,0,0]\n"
    "v_pk_fma_f32 v[88:89], v[88:89], v[116:117], v[114:115] op_sel_hi:[1,0,0]\n"
    "v_pk_fma_f32 v[90:91], v[90:91], v[116:117], v[114:115] op_sel_hi:[1,0,0]\n"
    "v_pk_fma_f32 v[92:93], v[92:93], v[116:117], v[114:115] op_sel_hi:[1,0,0]\n"
    "v_pk_fma_f32 v[94:95], v[94:95], v[116:117], v[114:115] op_sel_hi:[1,0,0]\n"
    "v_pk_fma_f32 v[96:97], v[96:97], v[116:117], v[114:115] op_sel_hi:[1,0,0]\n"
    "v_pk_fma_f32 v[98:99], v[98:99], v[116:117], v[114:115] op_sel_hi:[1,0,0]\n"
    "v_cvt_pk_bf16_f32 v100, v80, v81\n"
    "v_cvt_pk_bf16_f32 v101, v82, v83\n"
    "v_cvt_pk_bf16_f32 v102, v84, v85\n"
    "v_cvt_pk_bf16_f32 v103, v86, v87\n"
    "v_cvt_pk_bf16_f32 v104, v88, v89\n"
    "v_cvt_pk_bf16_f32 v105, v90, v91\n"
    "v_cvt_pk_bf16_f32 v106, v92, v93\n"
    "v_cvt_pk_bf16_f32 v107, v94, v95\n"
    "v_cvt_pk_bf16_f32 v108, v96, v97\n"
    "v_cvt_pk_bf16_f32 v109, v98, v99\n"
    "v_permlane16_swap_b32 v100, v102\n"
    "v_permlane16_swap_b32 v101, v103\n"
    "buffer_store_dwordx4 v[100:103], v4, s[44:47], s77 offen sc1\n"
    "v_permlane16_swap_b32 v104, v106\n"
    "v_permlane16_swap_b32 v105, v107\n"
    "buffer_store_dwordx4 v[104:107], v4, s[44:47], s78 offen sc1\n"
    "v_permlane16_swap_b32 v108, v110\n"
    "v_permlane16_swap_b32 v109, v111\n"
    "buffer_store_dwordx4 v[108:111], v5, s[44:47], s77 offen sc1\n"
    "s_cmp_eq_u32 s68, s67\n"
    "s_cbranch_scc1 .LrLN0_fin_%=\n"
    "s_branch .LrLN0_top_%=\n"
    ".LrLN0_LOOPE_%=:\n"
    ".LrLN0_fin_%=:\n"
    "s_waitcnt vmcnt(0)\n"
    "s_mov_b32 s68, 63\n"
    "v_mov_b32 v14, s68\n"
    "s_mov_b64 exec, s[64:65]\n"
    "buffer_atomic_smax v14, v13, s[36:39], 0 offen sc1\n"
    "s_mov_b64 exec, -1\n"
    "s_waitcnt vmcnt(0)\n"
    "s_barrier\n"
    "s_branch .LrLN0_FIN_%=\n"
    ".LrLN0_first_%=:\n"
    "s_waitcnt vmcnt(0)\n"
    "v_mov_b32 v14, s68\n"
    "s_mov_b64 exec, s[64:65]\n"
    "buffer_atomic_smax v14, v13, s[36:39], 0 offen sc1\n"
    "s_mov_b64 exec, -1\n"
    "s_branch .LrLN0_first1_%=\n"
    ".LrLN0_FIN_%=:\n"
  );
}
DEVI void role_mid(v16u sa, v16u sb, v16u va, v16u vb) {
  RUN_RED(
    "s_nop 4\n"
    "s_mov_b32 s69, 0\n"
    "v_mov_b32 v122, -1\n"
    "s_mov_b32 s70, 262144\n"
    ".LrMID_par_%=:\n"
    "buffer_load_dwordx2 v[14:15], off, s[36:39], 0 sc1\n"
    "s_waitcnt vmcnt(0)\n"
    "v_readfirstlane_b32 s75, v14\n"
    "v_readfirstlane_b32 s76, v15\n"
    "s_xor_b32 s75, s75, s48\n"
    "s_xor_b32 s76, s76, s49\n"
    "s_cselect_b32 s76, 2, 0\n"
    "s_or_b32 s76, s76, s75\n"
    "s_cmp_lt_u32 s76, 2\n"
    "s_cbranch_scc1 .LrMID_par1_%=\n"
    "s_sleep 4\n"
    "s_sub_u32 s70, s70, 1\n"
    "s_cbranch_scc0 .LrMID_par_%=\n"
    "s_mov_b32 s70, 0\n"
    ".LrMID_par1_%=:\n"
    "s_and_b32 s75, s75, 1\n"
    "s_lshl_b32 s75, s75, 13\n"
    "s_add_u32 s36, s36, s75\n"
    "s_addc_u32 s37, s37, 0\n"
    "s_and_b32 s76, s75, s61\n"
    "s_add_u32 s40, s40, s76\n"
    "s_addc_u32 s41, s41, 0\n"
    "s_and_b32 s76, s75, s63\n"
    "s_add_u32 s44, s44, s76\n"
    "s_addc_u32 s45, s45, 0\n"
    "s_branch .LrMID_block_%=\n"
    ".LrMID_LOOPB_%=:\n"
    ".LrMID_top_%=:\n"
    "s_mov_b32 s68, s69\n"
    "s_add_u32 s69, s68, 1\n"
    "s_and_b32 s75, s68, s58\n"
    "s_mul_i32 s77, s75, 4194304\n"
    "s_add_u32 s77, s77, s59\n"
    "s_add_u32 s78, s77, s60\n"
    "s_and_b32 s71, s69, 7\n"
    "s_mul_i32 s71, s71, 4194304\n"
    "s_add_u32 s71, s71, s52\n"
    "s_add_u32 s72, s71, 4096\n"
    "s_and_b32 s73, s69, s55\n"
    "s_mul_i32 s73, s73, s54\n"
    "s_add_u32 s73, s73, s53\n"
    "s_add_u32 s74, s73, s56\n"
    "s_mul_i32 s75, s75, 4194304\n"
    "s_add_u32 s75, s75, s57\n"
    "s_add_u32 s76, s75, 1024\n"
    "s_waitcnt vmcnt(12)\n"
    "v_mfma_f32_16x16x32_bf16 v[80:83], v[16:19], v[32:35], 0\n"
    "s_waitcnt vmcnt(11)\n"
    "v_mfma_f32_16x16x32_bf16 v[80:83], v[20:23], v[36:39], v[80:83]\n"
    "s_waitcnt vmcnt(10)\n"
    "v_mfma_f32_16x16x32_bf16 v[84:87], v[16:19], v[40:43], 0\n"
    "s_waitcnt vmcnt(9)\n"
    "v_mfma_f32_16x16x32_bf16 v[84:87], v[20:23], v[44:47], v[84:87]\n"
    "s_waitcnt vmcnt(8)\n"
    "v_mfma_f32_16x16x32_bf16 v[88:91], v[16:19], v[48:51], 0\n"
    "s_waitcnt vmcnt(7)\n"
    "v_mfma_f32_16x16x32_bf16 v[88:91], v[20:23], v[52:55], v[88:91]\n"
    "s_waitcnt vmcnt(6)\n"
    "v_mfma_f32_16x16x32_bf16 v[92:95], v[16:19], v[56:59], 0\n"
    "s_waitcnt vmcnt(5)\n"
    "v_mfma_f32_16x16x32_bf16 v[92:95], v[20:23], v[60:63], v[92:95]\n"
    "s_waitcnt vmcnt(4)\n"
    "v_mfma_f32_16x16x32_bf16 v[96:99], v[16:19], v[64:67], 0\n"
    "s_waitcnt vmcnt(3)\n"
    "v_mfma_f32_16x16x32_bf16 v[80:83], v[24:27], v[68:71], v[80:83]\n"
    "v_mfma_f32_16x16x32_bf16 v[84:87], v[28:31], v[68:71], v[84:87]\n"
    "s_waitcnt vmcnt(2)\n"
    "v_mfma_f32_16x16x32_bf16 v[88:91], v[24:27], v[72:75], v[88:91]\n"
    "v_mfma_f32_16x16x32_bf16 v[92:95], v[28:31], v[72:75], v[92:95]\n"
    "s_waitcnt vmcnt(1)\n"
    "v_mfma_f32_16x16x32_bf16 v[96:99], v[24:27], v[76:79], v[96:99]\n"
    "v_pk_add_f32 v[112:113], v[80:81], v[82:83]\n"
    "v_pk_add_f32 v[114:115], v[84:85], v[86:87]\n"
    "v_pk_add_f32 v[112:113], v[112:113], v[88:89]\n"
    "v_pk_add_f32 v[114:115], v[114:115], v[90:91]\n"
    "v_pk_add_f32 v[112:113], v[112:113], v[92:93]\n"
    "v_pk_add_f32 v[114:115], v[114:115], v[94:95]\n"
    "s_nop 1\n"
    "v_pk_add_f32 v[112:113], v[112:113], v[96:97]\n"
    "v_pk_add_f32 v[114:115], v[114:115], v[98:99]\n"
    "v_pk_add_f32 v[112:113], v[112:113], v[114:115]\n"
    "v_add_f32 v112, v112, v113\n"
    "v_cvt_pk_bf16_f32 v100, v80, v81\n"
    "v_cvt_pk_bf16_f32 v101, v82, v83\n"
    "v_cvt_pk_bf16_f32 v102, v84, v85\n"
    "v_cvt_pk_bf16_f32 v103, v86, v87\n"
    "v_cvt_pk_bf16_f32 v104, v88, v89\n"
    "v_cvt_pk_bf16_f32 v105, v90, v91\n"
    "v_cvt_pk_bf16_f32 v106, v92, v93\n"
    "v_cvt_pk_bf16_f32 v107, v94, v95\n"
    "v_cvt_pk_bf16_f32 v108, v96, v97\n"
    "v_cvt_pk_bf16_f32 v109, v98, v99\n"
    "v_add_f32_dpp v14, v112, v112 row_ror:8 row_mask:0xf bank_mask:0xf\n"
    "v_add_f32_dpp v15, v112, v112 row_ror:8 row_mask:0xf bank_mask:0xf\n"
    "s_nop 1\n"
    "v_permlane16_swap_b32 v14, v15\n"
    "v_add_f32 v112, v14, v15\n"
    "v_add_f32 v14, v14, v15\n"
    "s_nop 1\n"
    "v_permlane32_swap_b32 v112, v14\n"
    "v_add_f32 v112, v112, v14\n"
    "v_mul_f32 v112, 0x3be38e39, v112\n"
    "v_mul_f32 v114, v112, v12\n"
    "v_pk_add_f32 v[80:81], v[80:81], v[112:113] op_sel_hi:[1,0] neg_lo:[0,1] neg_hi:[0,1]\n"
    "v_pk_add_f32 v[82:83], v[82:83], v[112:113] op_sel_hi:[1,0] neg_lo:[0,1] neg_hi:[0,1]\n"
    "v_pk_add_f32 v[84:85], v[84:85], v[112:113] op_sel_hi:[1,0] neg_lo:[0,1] neg_hi:[0,1]\n"
    "v_pk_add_f32 v[86:87], v[86:87], v[112:113] op_sel_hi:[1,0] neg_lo:[0,1] neg_hi:[0,1]\n"
    "v_pk_add_f32 v[88:89], v[88:89], v[112:113] op_sel_hi:[1,0] neg_lo:[0,1] neg_hi:[0,1]\n"
    "v_pk_add_f32 v[90:91], v[90:91], v[112:113] op_sel_hi:[1,0] neg_lo:[0,1] neg_hi:[0,1]\n"
    "v_pk_add_f32 v[92:93], v[92:93], v[112:113] op_sel_hi:[1,0] neg_lo:[0,1] neg_hi:[0,1]\n"
    "v_pk_add_f32 v[94:95], v[94:95], v[112:113] op_sel_hi:[1,0] neg_lo:[0,1] neg_hi:[0,1]\n"
    "v_pk_add_f32 v[96:97], v[96:97], v[114:115] op_sel_hi:[1,0] neg_lo:[0,1] neg_hi:[0,1]\n"
    "v_pk_add_f32 v[98:99], v[98:99], v[114:115] op_sel_hi:[1,0] neg_lo:[0,1] neg_hi:[0,1]\n"
    "v_pk_mul_f32 v[116:117], v[80:81], v[80:81]\n"
    "v_pk_mul_f32 v[118:119], v[82:83], v[82:83]\n"
    "v_pk_fma_f32 v[116:117], v[84:85], v[84:85], v[116:117]\n"
    "v_pk_fma_f32 v[118:119], v[86:87], v[86:87], v[118:119]\n"
    "v_pk_fma_f32 v[116:117], v[88:89], v[88:89], v[116:117]\n"
    "v_pk_fma_f32 v[118:119], v[90:91], v[90:91], v[118:119]\n"
    "v_pk_fma_f32 v[116:117], v[92:93], v[92:93], v[116:117]\n"
    "v_pk_fma_f32 v[118:119], v[94:95], v[94:95], v[118:119]\n"
    "v_pk_fma_f32 v[116:117], v[96:97], v[96:97], v[116:117]\n"
    "v_pk_fma_f32 v[118:119], v[98:99], v[98:99], v[118:119]\n"
    "v_pk_add_f32 v[116:117], v[116:117], v[118:119]\n"
    "v_add_f32 v113, v116, v117\n"
    "s_nop 1\n"
    "v_add_f32_dpp v14, v113, v113 row_ror:8 row_mask:0xf bank_mask:0xf\n"
    "v_add_f32_dpp v15, v113, v113 row_ror:8 row_mask:0xf bank_mask:0xf\n"
    "s_nop 1\n"
    "v_permlane16_swap_b32 v14, v15\n"
    "v_add_f32 v113, v14, v15\n"
    "v_add_f32 v14, v14, v15\n"
    "s_nop 1\n"
    "v_permlane32_swap_b32 v113, v14\n"
    "v_add_f32 v113, v113, v14\n"
    "ds_write_b64 v10, v[112:113]\n"
    "v_add_u32 v120, v122, v1\n"
    "v_cmp_gt_i32 vcc, s69, v120\n"
    "s_nop 0\n"
    "s_or_b64 exec, vcc, 1\n"
    "buffer_atomic_smax v122, v0, s[36:39], 0 offen sc0 sc1\n"
    "s_mov_b64 exec, -1\n"
    "s_nop 0\n"
    "v_permlane16_swap_b32 v100, v102\n"
    "v_permlane16_swap_b32 v101, v103\n"
    "v_permlane16_swap_b32 v104, v106\n"
    "v_permlane16_swap_b32 v105, v107\n"
    "v_permlane16_swap_b32 v108, v110\n"
    "v_permlane16_swap_b32 v109, v111\n"
    "s_waitcnt lgkmcnt(0)\n"
    "s_barrier\n"
    "ds_read_b128 v[32:35], v11\n"
    "ds_read_b128 v[36:39], v11 offset:16\n"
    "buffer_store_dwordx4 v[100:103], v8, s[36:39], s75 offen sc1\n"
    "buffer_store_dwordx4 v[104:107], v8, s[36:39], s76 offen sc1\n"
    "buffer_store_dwordx4 v[108:111], v9, s[36:39], s75 offen sc1\n"
    "v_xor_b32 v10, 256, v10\n"
    "v_xor_b32 v11, 256, v11\n"
    "s_waitcnt lgkmcnt(0)\n"
    "v_pk_add_f32 v[114:115], v[32:33], v[34:35]\n"
    "v_pk_add_f32 v[116:117], v[36:37], v[38:39]\n"
    "v_pk_add_f32 v[114:115], v[114:115], v[116:117]\n"
    "v_fmac_f32 v32, 0xbe800000, v114\n"
    "v_fmac_f32 v34, 0xbe800000, v114\n"
    "v_fmac_f32 v36, 0xbe800000, v114\n"
    "v_fmac_f32 v38, 0xbe800000, v114\n"
    "v_fmac_f32 v112, 0xbe800000, v114\n"
    "v_mul_f32 v116, v32, v32\n"
    "v_fmac_f32 v116, v34, v34\n"
    "v_fmac_f32 v116, v36, v36\n"
    "v_fmac_f32 v116, v38, v38\n"
    "v_fmac_f32 v115, 0x43100000, v116\n"
    "v_mov_b32 v116, 0x3727c5ac\n"
    "v_fmac_f32 v116, 0x3ae38e39, v115\n"
    "v_rsq_f32 v116, v116\n"
    "s_nop 0\n"
    "v_mul_f32 v114, v112, v116\n"
    "v_pk_fma_f32 v[80:81], v[80:81], v[116:117], v[114:115] op_sel_hi:[1,0,0]\n"
    "v_pk_fma_f32 v[82:83], v[82:83], v[116:117], v[114:115] op_sel_hi:[1,0,0]\n"
    "v_pk_fma_f32 v[84:85], v[84:85], v[116:117], v[114:115] op_sel_hi:[1,0,0]\n"
    "v_pk_fma_f32 v[86:87], v[86:87], v[116:117], v[114:115] op_sel_hi:[1,0,0]\n"
    "v_pk_fma_f32 v[88:89], v[88:89], v[116:117], v[114:115] op_sel_hi:[1,0,0]\n"
    "v_pk_fma_f32 v[90:91], v[90:91], v[116:117], v[114:115] op_sel_hi:[1,0,0]\n"
    "v_pk_fma_f32 v[92:93], v[92:93], v[116:117], v[114:115] op_sel_hi:[1,0,0]\n"
    "v_pk_fma_f32 v[94:95], v[94:95], v[116:117], v[114:115] op_sel_hi:[1,0,0]\n"
    "v_pk_fma_f32 v[96:97], v[96:97], v[116:117], v[114:115] op_sel_hi:[1,0,0]\n"
    "v_pk_fma_f32 v[98:99], v[98:99], v[116:117], v[114:115] op_sel_hi:[1,0,0]\n"
    "v_cvt_pk_bf16_f32 v100, v80, v81\n"
    "v_cvt_pk_bf16_f32 v101, v82, v83\n"
    "v_cvt_pk_bf16_f32 v102, v84, v85\n"
    "v_cvt_pk_bf16_f32 v103, v86, v87\n"
    "v_cvt_pk_bf16_f32 v104, v88, v89\n"
    "v_cvt_pk_bf16_f32 v105, v90, v91\n"
    "v_cvt_pk_bf16_f32 v106, v92, v93\n"
    "v_cvt_pk_bf16_f32 v107, v94, v95\n"
    "v_cvt_pk_bf16_f32 v108, v96, v97\n"
    "v_cvt_pk_bf16_f32 v109, v98, v99\n"
    "v_permlane16_swap_b32 v100, v102\n"
    "v_permlane16_swap_b32 v101, v103\n"
    "buffer_store_dwordx4 v[100:103], v4, s[44:47], s77 offen sc1\n"
    "v_permlane16_swap_b32 v104, v106\n"
    "v_permlane16_swap_b32 v105, v107\n"
    "buffer_store_dwordx4 v[104:107], v4, s[44:47], s78 offen sc1\n"
    "v_permlane16_swap_b32 v108, v110\n"
    "v_permlane16_swap_b32 v109, v111\n"
    "buffer_store_dwordx4 v[108:111], v5, s[44:47], s77 offen sc1\n"
    "s_cmp_eq_u32 s69, 64\n"
    "s_cbranch_scc1 .LrMID_fin_%=\n"
    "s_waitcnt vmcnt(6)\n"
    "v_add_u32 v120, v122, v1\n"
    "v_cmp_gt_i32 vcc, s69, v120\n"
    "s_cbranch_vccnz .LrMID_idle_%=\n"
    "buffer_load_dwordx4 v[32:35], v2, s[36:39], s71 offen sc1\n"
    "buffer_load_dwordx4 v[36:39], v2, s[36:39], s71 offen offset:512 sc1\n"
    "buffer_load_dwordx4 v[40:43], v2, s[36:39], s71 offen offset:2048 sc1\n"
    "buffer_load_dwordx4 v[44:47], v2, s[36:39], s71 offen offset:2560 sc1\n"
    "buffer_load_dwordx4 v[48:51], v2, s[36:39], s72 offen sc1\n"
    "buffer_load_dwordx4 v[52:55], v2, s[36:39], s72 offen offset:512 sc1\n"
    "buffer_load_dwordx4 v[56:59], v2, s[36:39], s72 offen offset:2048 sc1\n"
    "buffer_load_dwordx4 v[60:63], v2, s[36:39], s72 offen offset:2560 sc1\n"
    "buffer_load_dwordx4 v[64:67], v3, s[36:39], s71 offen sc1\n"
    "buffer_load_dwordx4 v[68:71], v6, s[40:43], s73 offen sc1\n"
    "buffer_load_dwordx4 v[72:75], v6, s[40:43], s74 offen sc1\n"
    "buffer_load_dwordx4 v[76:79], v7, s[40:43], s73 offen sc1\n"
    "s_waitcnt vmcnt(12)\n"
    "v_mov_b32 v14, s68\n"
    "s_mov_b64 exec, s[64:65]\n"
    "buffer_atomic_smax v14, v13, s[36:39], 0 offen sc1\n"
    "s_mov_b64 exec, -1\n"
    "s_branch .LrMID_top_%=\n"
    ".LrMID_LOOPE_%=:\n"
    ".LrMID_fin_%=:\n"
    "s_waitcnt vmcnt(0)\n"
    "s_mov_b32 s68, 63\n"
    "v_mov_b32 v14, s68\n"
    "s_mov_b64 exec, s[64:65]\n"
    "buffer_atomic_smax v14, v13, s[36:39], 0 offen sc1\n"
    "s_mov_b64 exec, -1\n"
    "s_waitcnt vmcnt(0)\n"
    "s_barrier\n"
    "s_branch .LrMID_FIN_%=\n"
    ".LrMID_idle_%=:\n"
    "v_add_u32 v120, v122, v1\n"
    "v_cmp_gt_i32 vcc, s69, v120\n"
    "s_nop 0\n"
    "s_or_b64 exec, vcc, 1\n"
    "buffer_atomic_smax v122, v0, s[36:39], 0 offen sc0 sc1\n"
    "s_mov_b64 exec, -1\n"
    "s_waitcnt vmcnt(1)\n"
    "v_mov_b32 v14, s68\n"
    "s_mov_b64 exec, s[64:65]\n"
    "buffer_atomic_smax v14, v13, s[36:39], 0 offen sc1\n"
    "s_mov_b64 exec, -1\n"
    "s_waitcnt vmcnt(1)\n"
    "s_branch .LrMID_block1_%=\n"
    ".LrMID_block_%=:\n"
    "v_add_u32 v120, v122, v1\n"
    "v_cmp_gt_i32 vcc, s69, v120\n"
    "s_nop 0\n"
    "s_or_b64 exec, vcc, 1\n"
    "buffer_atomic_smax v122, v0, s[36:39], 0 offen sc0 sc1\n"
    "s_mov_b64 exec, -1\n"
    "s_waitcnt vmcnt(0)\n"
    ".LrMID_block1_%=:\n"
    "v_add_u32 v120, v122, v1\n"
    "v_cmp_gt_i32 vcc, s69, v120\n"
    "s_cbranch_vccz .LrMID_go_%=\n"
    "s_sleep 2\n"
    "s_sub_u32 s70, s70, 1\n"
    "s_cbranch_scc0 .LrMID_block_%=\n"
    "s_mov_b32 s70, 0\n"
    ".LrMID_go_%=:\n"
    "s_and_b32 s71, s69, 7\n"
    "s_mul_i32 s71, s71, 4194304\n"
    "s_add_u32 s71, s71, s52\n"
    "s_add_u32 s72, s71, 4096\n"
    "s_and_b32 s73, s69, s55\n"
    "s_mul_i32 s73, s73, s54\n"
    "s_add_u32 s73, s73, s53\n"
    "s_add_u32 s74, s73, s56\n"
    "buffer_load_dwordx4 v[32:35], v2, s[36:39], s71 offen sc1\n"
    "buffer_load_dwordx4 v[36:39], v2, s[36:39], s71 offen offset:512 sc1\n"
    "buffer_load_dwordx4 v[40:43], v2, s[36:39], s71 offen offset:2048 sc1\n"
    "buffer_load_dwordx4 v[44:47], v2, s[36:39], s71 offen offset:2560 sc1\n"
    "buffer_load_dwordx4 v[48:51], v2, s[36:39], s72 offen sc1\n"
    "buffer_load_dwordx4 v[52:55], v2, s[36:39], s72 offen offset:512 sc1\n"
    "buffer_load_dwordx4 v[56:59], v2, s[36:39], s72 offen offset:2048 sc1\n"
    "buffer_load_dwordx4 v[60:63], v2, s[36:39], s72 offen offset:2560 sc1\n"
    "buffer_load_dwordx4 v[64:67], v3, s[36:39], s71 offen sc1\n"
    "buffer_load_dwordx4 v[68:71], v6, s[40:43], s73 offen sc1\n"
    "buffer_load_dwordx4 v[72:75], v6, s[40:43], s74 offen sc1\n"
    "buffer_load_dwordx4 v[76:79], v7, s[40:43], s73 offen sc1\n"
    "buffer_load_dword v121, v7, s[40:43], s73 offen\n"
    "s_branch .LrMID_top_%=\n"
    ".LrMID_FIN_%=:\n"
  );
}
DEVI void role_fin(const Params& p, int q, u32 dbg, u64 tag) {
  v16u sa, sb, va, vb;
  red_args<2>(p, 1, q, dbg, tag, sa, sb, va, vb);
  RUN_RED(
    "s_nop 4\n"
    "s_mov_b32 s69, 0\n"
    "v_mov_b32 v122, -1\n"
    "s_mov_b32 s70, 262144\n"
    ".LrFIN_par_%=:\n"
    "buffer_load_dwordx2 v[14:15], off, s[36:39], 0 sc1\n"
    "s_waitcnt vmcnt(0)\n"
    "v_readfirstlane_b32 s75, v14\n"
    "v_readfirstlane_b32 s76, v15\n"
    "s_xor_b32 s75, s75, s48\n"
    "s_xor_b32 s76, s76, s49\n"
    "s_cselect_b32 s76, 2, 0\n"
    "s_or_b32 s76, s76, s75\n"
    "s_cmp_lt_u32 s76, 2\n"
    "s_cbranch_scc1 .LrFIN_par1_%=\n"
    "s_sleep 4\n"
    "s_sub_u32 s70, s70, 1\n"
    "s_cbranch_scc0 .LrFIN_par_%=\n"
    "s_mov_b32 s70, 0\n"
    ".LrFIN_par1_%=:\n"
    "s_and_b32 s75, s75, 1\n"
    "s_lshl_b32 s75, s75, 13\n"
    "s_add_u32 s36, s36, s75\n"
    "s_addc_u32 s37, s37, 0\n"
    "s_and_b32 s76, s75, s61\n"
    "s_add_u32 s40, s40, s76\n"
    "s_addc_u32 s41, s41, 0\n"
    "s_and_b32 s76, s75, s63\n"
    "s_add_u32 s44, s44, s76\n"
    "s_addc_u32 s45, s45, 0\n"
    "s_branch .LrFIN_block_%=\n"
    ".LrFIN_LOOPB_%=:\n"
    ".LrFIN_top_%=:\n"
    "s_mov_b32 s68, s69\n"
    "s_add_u32 s69, s68, 1\n"
    "s_and_b32 s75, s68, s58\n"
    "s_mul_i32 s77, s75, 36864\n"
    "s_add_u32 s77, s77, s59\n"
    "s_add_u32 s78, s77, s60\n"
    "s_and_b32 s71, s69, 7\n"
    "s_mul_i32 s71, s71, 4194304\n"
    "s_add_u32 s71, s71, s52\n"
    "s_add_u32 s72, s71, 4096\n"
    "s_and_b32 s73, s69, s55\n"
    "s_mul_i32 s73, s73, s54\n"
    "s_add_u32 s73, s73, s53\n"
    "s_add_u32 s74, s73, s56\n"
    "s_waitcnt vmcnt(11)\n"
    "v_mfma_f32_16x16x32_bf16 v[80:83], v[16:19], v[32:35], 0\n"
    "s_waitcnt vmcnt(10)\n"
    "v_mfma_f32_16x16x32_bf16 v[80:83], v[20:23], v[36:39], v[80:83]\n"
    "s_waitcnt vmcnt(9)\n"
    "v_mfma_f32_16x16x32_bf16 v[84:87], v[16:19], v[40:43], 0\n"
    "s_waitcnt vmcnt(8)\n"
    "v_mfma_f32_16x16x32_bf16 v[84:87], v[20:23], v[44:47], v[84:87]\n"
    "s_waitcnt vmcnt(7)\n"
    "v_mfma_f32_16x16x32_bf16 v[88:91], v[16:19], v[48:51], 0\n"
    "s_waitcnt vmcnt(6)\n"
    "v_mfma_f32_16x16x32_bf16 v[88:91], v[20:23], v[52:55], v[88:91]\n"
    "s_waitcnt vmcnt(5)\n"
    "v_mfma_f32_16x16x32_bf16 v[92:95], v[16:19], v[56:59], 0\n"
    "s_waitcnt vmcnt(4)\n"
    "v_mfma_f32_16x16x32_bf16 v[92:95], v[20:23], v[60:63], v[92:95]\n"
    "s_waitcnt vmcnt(3)\n"
    "v_mfma_f32_16x16x32_bf16 v[96:99], v[16:19], v[64:67], 0\n"
    "s_waitcnt vmcnt(2)\n"
    "v_mfma_f32_16x16x32_bf16 v[80:83], v[24:27], v[68:71], v[80:83]\n"
    "v_mfma_f32_16x16x32_bf16 v[84:87], v[28:31], v[68:71], v[84:87]\n"
    "s_waitcnt vmcnt(1)\n"
    "v_mfma_f32_16x16x32_bf16 v[88:91], v[24:27], v[72:75], v[88:91]\n"
    "v_mfma_f32_16x16x32_bf16 v[92:95], v[28:31], v[72:75], v[92:95]\n"
    "s_waitcnt vmcnt(0)\n"
    "v_mfma_f32_16x16x32_bf16 v[96:99], v[24:27], v[76:79], v[96:99]\n"
    "v_add_u32 v120, v122, v1\n"
    "v_cmp_gt_i32 vcc, s69, v120\n"
    "s_nop 0\n"
    "s_or_b64 exec, vcc, 1\n"
    "buffer_atomic_smax v122, v0, s[36:39], 0 offen sc0 sc1\n"
    "s_mov_b64 exec, -1\n"
    "v_mov_b32 v14, s68\n"
    "s_mov_b64 exec, s[64:65]\n"
    "buffer_atomic_smax v14, v13, s[36:39], 0 offen sc1\n"
    "s_mov_b64 exec, -1\n"
    "v_cvt_pk_bf16_f32 v100, v80, v81\n"
    "v_cvt_pk_bf16_f32 v101, v82, v83\n"
    "v_cvt_pk_bf16_f32 v102, v84, v85\n"
    "v_cvt_pk_bf16_f32 v103, v86, v87\n"
    "v_cvt_pk_bf16_f32 v104, v88, v89\n"
    "v_cvt_pk_bf16_f32 v105, v90, v91\n"
    "v_cvt_pk_bf16_f32 v106, v92, v93\n"
    "v_cvt_pk_bf16_f32 v107, v94, v95\n"
    "v_cvt_pk_bf16_f32 v108, v96, v97\n"
    "v_cvt_pk_bf16_f32 v109, v98, v99\n"
    "v_permlane16_swap_b32 v100, v102\n"
    "v_permlane16_swap_b32 v101, v103\n"
    "ds_write_b128 v10, v[100:103]\n"
    "v_permlane16_swap_b32 v104, v106\n"
    "v_permlane16_swap_b32 v105, v107\n"
    "ds_write_b128 v10, v[104:107] offset:128\n"
    "v_permlane16_swap_b32 v108, v110\n"
    "v_permlane16_swap_b32 v109, v111\n"
    "ds_write_b128 v12, v[108:111]\n"
    "v_xor_b32 v10, 16384, v10\n"
    "v_xor_b32 v12, 16384, v12\n"
    "s_waitcnt lgkmcnt(0)\n"
    "s_barrier\n"
    "ds_read_b128 v[100:103], v11 offset:0\n"
    "ds_read_b128 v[104:107], v11 offset:4096\n"
    "ds_read_b128 v[108:111], v11 offset:8192\n"
    "v_xor_b32 v11, 16384, v11\n"
    "s_waitcnt lgkmcnt(2)\n"
    "buffer_store_dwordx4 v[100:103], v4, s[44:47], s77 offen sc1\n"
    "s_waitcnt lgkmcnt(1)\n"
    "buffer_store_dwordx4 v[104:107], v4, s[44:47], s78 offen sc1\n"
    "s_waitcnt lgkmcnt(0)\n"
    "buffer_store_dwordx4 v[108:111], v5, s[44:47], s77 offen sc1\n"
    "s_cmp_eq_u32 s69, 64\n"
    "s_cbranch_scc1 .LrFIN_fin_%=\n"
    "s_waitcnt vmcnt(4)\n"
    "v_add_u32 v120, v122, v1\n"
    "v_cmp_gt_i32 vcc, s69, v120\n"
    "s_cbranch_vccnz .LrFIN_idle_%=\n"
    "buffer_load_dwordx4 v[32:35], v2, s[36:39], s71 offen sc1\n"
    "buffer_load_dwordx4 v[36:39], v2, s[36:39], s71 offen offset:512 sc1\n"
    "buffer_load_dwordx4 v[40:43], v2, s[36:39], s71 offen offset:2048 sc1\n"
    "buffer_load_dwordx4 v[44:47], v2, s[36:39], s71 offen offset:2560 sc1\n"
    "buffer_load_dwordx4 v[48:51], v2, s[36:39], s72 offen sc1\n"
    "buffer_load_dwordx4 v[52:55], v2, s[36:39], s72 offen offset:512 sc1\n"
    "buffer_load_dwordx4 v[56:59], v2, s[36:39], s72 offen offset:2048 sc1\n"
    "buffer_load_dwordx4 v[60:63], v2, s[36:39], s72 offen offset:2560 sc1\n"
    "buffer_load_dwordx4 v[64:67], v3, s[36:39], s71 offen sc1\n"
    "buffer_load_dwordx4 v[68:71], v6, s[40:43], s73 offen sc1\n"
    "buffer_load_dwordx4 v[72:75], v6, s[40:43], s74 offen sc1\n"
    "buffer_load_dwordx4 v[76:79], v7, s[40:43], s73 offen sc1\n"
    "s_branch .LrFIN_top_%=\n"
    ".LrFIN_LOOPE_%=:\n"
    ".LrFIN_fin_%=:\n"
    "s_waitcnt vmcnt(0)\n"
    "s_branch .LrFIN_FIN_%=\n"
    ".LrFIN_idle_%=:\n"
    ".LrFIN_block_%=:\n"
    "v_add_u32 v120, v122, v1\n"
    "v_cmp_gt_i32 vcc, s69, v120\n"
    "s_nop 0\n"
    "s_or_b64 exec, vcc, 1\n"
    "buffer_atomic_smax v122, v0, s[36:39], 0 offen sc0 sc1\n"
    "s_mov_b64 exec, -1\n"
    "s_waitcnt vmcnt(0)\n"
    ".LrFIN_block1_%=:\n"
    "v_add_u32 v120, v122, v1\n"
    "v_cmp_gt_i32 vcc, s69, v120\n"
    "s_cbranch_vccz .LrFIN_go_%=\n"
    "s_sleep 2\n"
    "s_sub_u32 s70, s70, 1\n"
    "s_cbranch_scc0 .LrFIN_block_%=\n"
    "s_mov_b32 s70, 0\n"
    ".LrFIN_go_%=:\n"
    "s_and_b32 s71, s69, 7\n"
    "s_mul_i32 s71, s71, 4194304\n"
    "s_add_u32 s71, s71, s52\n"
    "s_add_u32 s72, s71, 4096\n"
    "s_and_b32 s73, s69, s55\n"
    "s_mul_i32 s73, s73, s54\n"
    "s_add_u32 s73, s73, s53\n"
    "s_add_u32 s74, s73, s56\n"
    "buffer_load_dwordx4 v[32:35], v2, s[36:39], s71 offen sc1\n"
    "buffer_load_dwordx4 v[36:39], v2, s[36:39], s71 offen offset:512 sc1\n"
    "buffer_load_dwordx4 v[40:43], v2, s[36:39], s71 offen offset:2048 sc1\n"
    "buffer_load_dwordx4 v[44:47], v2, s[36:39], s71 offen offset:2560 sc1\n"
    "buffer_load_dwordx4 v[48:51], v2, s[36:39], s72 offen sc1\n"
    "buffer_load_dwordx4 v[52:55], v2, s[36:39], s72 offen offset:512 sc1\n"
    "buffer_load_dwordx4 v[56:59], v2, s[36:39], s72 offen offset:2048 sc1\n"
    "buffer_load_dwordx4 v[60:63], v2, s[36:39], s72 offen offset:2560 sc1\n"
    "buffer_load_dwordx4 v[64:67], v3, s[36:39], s71 offen sc1\n"
    "buffer_load_dwordx4 v[68:71], v6, s[40:43], s73 offen sc1\n"
    "buffer_load_dwordx4 v[72:75], v6, s[40:43], s74 offen sc1\n"
    "buffer_load_dwordx4 v[76:79], v7, s[40:43], s73 offen sc1\n"
    "s_branch .LrFIN_top_%=\n"
    ".LrFIN_FIN_%=:\n"
  );
}

__global__ void __launch_bounds__(256, 1) mega(Params p) {
  extern __shared__ __attribute__((aligned(16))) unsigned char smem[];
  const rsrc_t rw = __builtin_amdgcn_make_buffer_rsrc(p.ws, 0, WS_END + 8192, 0x00020000);
  const unsigned long long t00 = __builtin_amdgcn_s_memrealtime();
  if (threadIdx.x < 64) {
    const int lane = threadIdx.x, b = blockIdx.x;
    if (p.role_mask & 0x40000) st16(rw, (v4u){~0u, ~0u, ~0u, ~0u}, lane < 22 ? CHUNK + (u32)b * 352 + lane * 16 : OOB, 0);
    const u64 tg = ld8u(rw, EPOCH) + 1;
    drain();
    if (lane == 0) {
      {
        const v2u tv = {(u32)tg, (u32)(tg >> 32)};
        const u32 a = READY + (u32)b * 8;
        asm volatile("buffer_atomic_swap_x2 %0, %1, %2, 0 offen sc1" :: "v"(tv), "v"(a), "s"(rw) : "memory");
      }
      int rid = (int)(__builtin_amdgcn_s_getreg(20 | (3 << 11)) & 7u) * 32 + (b >> 3);
      for (int k = 0; k < 256; ++k) {
        if (__hip_atomic_exchange((u64*)(p.ws + CLAIM) + ((rid & 31) * 8 + (rid >> 5)), tg, __ATOMIC_RELAXED, __HIP_MEMORY_SCOPE_AGENT) != tg) break;
        rid = (rid + 1) & 255;
      }
      *(volatile v4u*)smem = (v4u){(u32)rid, (u32)tg, (u32)(tg >> 32), 0u};
    }
  }
  __syncthreads();
  const v4u boot = *(volatile v4u*)smem;
  const int rid = (int)rfl(boot.x);
  const u64 tag = (u64)rfl(boot.y) | ((u64)rfl(boot.z) << 32);
  __syncthreads();
  const int L = rid >> 7, j = (rid >> 5) & 3, sl = rid & 31;
  const u32 dbg = OFF_DBG + (u32)rid * 66 * 256;
  auto on = [&](int r) { return ((p.role_mask >> (2 + 8 * L + r)) & 1) != 0; };
  STAMP(65, 8);
  STAMP(65 + 32, 9);
  if (PULL > L && sl >= SL_WO && sl < SL_REDA) pull_weights(p, j, sl - SL_WO);
  if (sl < SL_P) { delay_start(L ? DL_R1 : 0, t00); if (on(1)) role_rec(p, L, j, sl, dbg, tag, smem); }
  else if (sl < SL_ATT) { delay_start(L ? DL_R1 : 0, t00); if (on(0)) role_p(p, L, j, sl - SL_P, dbg, tag); }
  else if (sl < SL_WO) {
    const u32 shift = find_shift(rw, rid == 128 + SL_ATT && threadIdx.x < 64, tag);
    if (p.role_mask & 2) hot_init(p, rw, L, 2 * j + sl - SL_ATT, shift);
    if (on(2)) role_att(p, L, j, sl - SL_ATT, dbg, shift);
  }
  else if (sl < SL_UP) { delay_start(L ? DL_W1 : DL_W0, t00); if (on(3)) role_wo(p, L, j, sl - SL_WO, dbg, tag); }
  else if (sl < SL_DOWN) { delay_start(L ? DL_U1 : DL_U0, t00); if (on(5)) role_up(p, L, j, sl - SL_UP, dbg, tag); }
  else if (sl < SL_REDA) { delay_start(L ? DL_D1 : DL_D0, t00); if (on(6)) role_down(p, L, j, sl - SL_DOWN, dbg, tag); }
  else {
    if (sl == SL_REDA && (p.role_mask & 2)) hot_init(p, rw, L, 8 + j, find_shift(rw, false, tag));
    if ((p.role_mask & 1) && (L == 1 || sl == SL_REDF)) role_ln0(p, L == 0 ? 2 : (sl == SL_REDA ? 1 : 0), j, dbg, tag);
    if (sl == SL_REDF && L == 1) {
      if (on(7)) role_fin(p, j, dbg, tag);
    } else {
      v16u sa, sb, va, vb;
      if (sl == SL_REDA) red_args<1>(p, L, j, dbg, tag, sa, sb, va, vb);
      else red_args<2>(p, 0, j, dbg, tag, sa, sb, va, vb);
      if (on(sl == SL_REDA ? 4 : 7)) role_mid(sa, sb, va, vb);
    }
  }
}

static void fill_params(Params& p, void* const* d_in, void* d_out, void* d_ws) {
  p.x = (const u16*)d_in[0]; p.mem0 = (const u16*)d_in[1];
  p.Wq = (const u16*)d_in[2]; p.Wk = (const u16*)d_in[3]; p.Wv = (const u16*)d_in[4]; p.Wo = (const u16*)d_in[5];
  p.w1 = (const u16*)d_in[8]; p.w3 = (const u16*)d_in[9]; p.w2 = (const u16*)d_in[10];
  p.out = (u16*)d_out; p.ws = (unsigned char*)d_ws;
}

extern "C" void kernel_launch(void* const* d_in, const int* in_sizes, int n_in, void* d_out, int out_size, void* d_ws,
                              size_t ws_size, hipStream_t stream) {
  Params p;
  fill_params(p, d_in, d_out, d_ws);
  (void)hipFuncSetAttribute((const void*)mega, hipFuncAttributeMaxDynamicSharedMemorySize, LDS_BYTES);
  p.role_mask = 0x7FFFF; p.unused = 0;
  mega<<<256, 256, LDS_BYTES, stream>>>(p);
}
```

```cpp
#include <hip/hip_runtime.h>
#include <stdint.h>
#include <type_traits>
#include <utility>

typedef unsigned short u16;
typedef unsigned int u32;
typedef short bf16x8 __attribute__((ext_vector_type(8)));
typedef short bf16x4 __attribute__((ext_vector_type(4)));
typedef float f32x4 __attribute__((ext_vector_type(4)));
typedef float f32x2 __attribute__((ext_vector_type(2)));
typedef __bf16 bf2_t __attribute__((ext_vector_type(2)));
typedef unsigned v16u __attribute__((ext_vector_type(16)));
typedef unsigned v8u __attribute__((ext_vector_type(8)));
typedef unsigned v4u __attribute__((ext_vector_type(4)));
typedef unsigned v2u __attribute__((ext_vector_type(2)));
typedef __amdgpu_buffer_rsrc_t rsrc_t;
#define DEVI __device__ __forceinline__

static constexpr int NSEG = 64;
static constexpr int PSLOW = 1, PSKIP = 2, PULL = 1, RQ = 8, RO = 8, RP = 8, RH = 8, RX = 8, RX2 = 16, LN_B = 28, LN_C = 52, PS_ATT = 3, PS_RED = 3, DL_R1 = 6, DL_W0 = 7, DL_W1 = 12, DL_U0 = 8, DL_U1 = 14, DL_D0 = 11, DL_D1 = 16;

static constexpr u32 PARW = 0, EPOCH = 128, READY = 2048, CLAIM = 4096;
static constexpr u32 CHUNK = 16384;
static constexpr u32 MBOX = CHUNK;
static constexpr u32 INITF = 5 * CHUNK;
static constexpr u32 CTRL_BYTES = 6 * CHUNK + 8192;
static constexpr u32 HOT = 6 * CHUNK, HOT2 = 2u << 20;
static constexpr u32 SLOT = 4u << 20;
static constexpr u32 PGA = SLOT, PGB = SLOT + (2u << 20);
static constexpr int cmax(int a, int b) { return a > b ? a : b; }
static constexpr int NSLOT = cmax(cmax(cmax(RQ, RO), cmax(RP, RH)), cmax(RX, RX2));
DEVI u32 pga(int L, int blk) { return PGA + (u32)blk * CHUNK + (u32)L * 8192; }
DEVI u32 pgb(int L, int blk) { return PGB + (u32)blk * CHUNK + (u32)(1 - L) * 8192; }
static constexpr int B_QKV = 0, B_KVM = 96;
static constexpr int B_O = 0, B_H = 32, B_PA = 68, B_PF = 88, B_XN2 = 108, B_X1 = 113, B_XN = 118, B_X = 123;
static constexpr u32 OFF_XN0 = SLOT * (1 + NSLOT);
static constexpr u32 OFF_DBG = OFF_XN0 + 320 * CHUNK;
static constexpr u32 WS_END = OFF_DBG + 256u * 66 * 256;
DEVI u32 kb_off(int kb) { return (u32)((kb >> 3) * CHUNK + (kb & 7) * 1024); }
static constexpr u32 OOB = 0x80000000u;
static constexpr int LDS_BYTES = 163840;
static constexpr unsigned SPIN_LIMIT = 1u << 18;
static constexpr int SL_REC = 0, SL_P = 8, SL_ATT = 16, SL_WO = 18, SL_UP = 21, SL_DOWN = 27, SL_REDA = 30, SL_REDF = 31;
DEVI u32 mbox(int L, int j, int role, int slot) { return MBOX + (u32)(j * CHUNK + L * 8192 + role * 256 + 4 * slot); }
DEVI u32 mbox_att(int L, int j, int hl, int slot) { return mbox(L, j, SL_ATT + (hl >> 2), 16 * (hl & 3) + slot); }

struct Params {
  const u16* x; const u16* mem0; const u16* Wq; const u16* Wk; const u16* Wv; const u16* Wo;
  const u16* w1; const u16* w3; const u16* w2; u16* out; unsigned char* ws;
  int role_mask; int unused;
};

template <class F, int... I> DEVI void static_for_impl(F&& f, std::integer_sequence<int, I...>) { (f(std::integral_constant<int, I>{}), ...); }
template <int N, class F> DEVI void static_for(F&& f) { static_for_impl(f, std::make_integer_sequence<int, N>{}); }
DEVI float bflo(u32 d) { return __uint_as_float(d << 16); }
DEVI float bfhi(u32 d) { return __uint_as_float(d & 0xffff0000u); }
DEVI u32 pk2(float a, float b) {
  f32x2 f = {a, b};
  bf2_t r = __builtin_convertvector(f, bf2_t);
  return __builtin_bit_cast(u32, r);
}
DEVI bf16x8 as_frag(v4u v) { return __builtin_bit_cast(bf16x8, v); }
DEVI bf16x4 as_frag4(v2u v) { return __builtin_bit_cast(bf16x4, v); }
DEVI f32x4 mfma32(v4u a, v4u b, f32x4 c) { return __builtin_amdgcn_mfma_f32_16x16x32_bf16(as_frag(a), as_frag(b), c, 0, 0, 0); }
DEVI f32x4 mfma16(v2u a, v2u b, f32x4 c) { return __builtin_amdgcn_mfma_f32_16x16x16bf16_1k(as_frag4(a), as_frag4(b), c, 0, 0, 0); }
DEVI v4u ld16(rsrc_t r, u32 voff, u32 soff) { return __builtin_amdgcn_raw_buffer_load_b128(r, voff, soff, 16); }
DEVI v2u ld8(rsrc_t r, u32 voff, u32 soff) { return __builtin_amdgcn_raw_buffer_load_b64(r, voff, soff, 16); }
DEVI u32 ld4(rsrc_t r, u32 voff, u32 soff) { return __builtin_amdgcn_raw_buffer_load_b32(r, voff, soff, 16); }
DEVI void st16(rsrc_t r, v4u v, u32 voff, u32 soff) { __builtin_amdgcn_raw_buffer_store_b128(v, r, voff, soff, 16); }
DEVI void st8(rsrc_t r, v2u v, u32 voff, u32 soff) { __builtin_amdgcn_raw_buffer_store_b64(v, r, voff, soff, 16); }
DEVI void st4(rsrc_t r, u32 v, u32 voff, u32 soff) { __builtin_amdgcn_raw_buffer_store_b32(v, r, voff, soff, 16); }
DEVI void drain() { asm volatile("s_waitcnt vmcnt(0)" ::: "memory"); }
DEVI void cbar() { asm volatile("" ::: "memory"); }
DEVI void sbar() { __builtin_amdgcn_sched_barrier(0); }
DEVI u32 rfl(u32 v) { return (u32)__builtin_amdgcn_readfirstlane((int)v); }
DEVI float xmax16_32(float x) {
  u32 a = __float_as_uint(x);
  auto r = __builtin_amdgcn_permlane16_swap(a, a, false, false);
  x = fmaxf(__uint_as_float(r[0]), __uint_as_float(r[1]));
  a = __float_as_uint(x);
  auto q = __builtin_amdgcn_permlane32_swap(a, a, false, false);
  return fmaxf(__uint_as_float(q[0]), __uint_as_float(q[1]));
}
DEVI float xsum16_32(float x) {
  u32 a = __float_as_uint(x);
  auto r = __builtin_amdgcn_permlane16_swap(a, a, false, false);
  x = __uint_as_float(r[0]) + __uint_as_float(r[1]);
  a = __float_as_uint(x);
  auto q = __builtin_amdgcn_permlane32_swap(a, a, false, false);
  return __uint_as_float(q[0]) + __uint_as_float(q[1]);
}
DEVI float ror8(float x) { return __int_as_float(__builtin_amdgcn_update_dpp(0, __float_as_int(x), 0x128, 0xf, 0xf, false)); }
DEVI v4u swap16(v2u a, v2u b) {
  auto s0 = __builtin_amdgcn_permlane16_swap(a.x, b.x, false, false);
  auto s1 = __builtin_amdgcn_permlane16_swap(a.y, b.y, false, false);
  return (v4u){s0[0], s1[0], s0[1], s1[1]};
}

#define STAMP(row, slot) do { } while (0)

DEVI bool flags_ok(int v, int bias, int s) {
  asm volatile("" : "+v"(v));
  return __all(v + bias >= s);
}
template <int SLP> DEVI void wait_flags(rsrc_t r, u32 addr, int bias, int s, unsigned& budget) {
  for (;;) {
    cbar();
    const int v = (int)ld4(r, addr, 0);
    if (flags_ok(v, bias, s) || budget == 0) break;
    --budget;
    __builtin_amdgcn_s_sleep(SLP);
  }
  cbar();
}
DEVI void publish(rsrc_t r, u32 flag, int s, int lane) {
  drain();
  if (lane == 0) st4(r, (u32)s, flag, 0);
  cbar();
}
DEVI void delay_start(int us, unsigned long long t0) {
  if (us == 0) return;
  while (__builtin_amdgcn_s_memrealtime() - t0 < (unsigned long long)us * 100u) __builtin_amdgcn_s_sleep(16);
}
DEVI void pull_weights(const Params& p, int j, int n) {
  const int tid = threadIdx.x;
  u32 acc = 0;
  {
    const u16* w = n % 3 == 0 ? p.Wq : (n % 3 == 1 ? p.Wk : p.Wv);
    const rsrc_t r = __builtin_amdgcn_make_buffer_rsrc((void*)(w + j * 1024), 0, 576 * 8192 - j * 2048, 0x00020000);
    if (PSLOW) {
#pragma unroll
      for (int k = 0; k < 3; ++k) {
        const int row = (k * 4 + n / 3) * 64 + (tid >> 2);
        acc ^= __builtin_amdgcn_raw_buffer_load_b32(r, row < 576 ? (u32)(row * 8192 + 256 + (tid & 1) * 128 + (tid & 2) * 512) : OOB, 0, 0);
      }
    }
#pragma unroll
    for (int k = PSKIP; k < 9; ++k) {
      const int row = (k * 4 + n / 3) * 16 + (tid >> 4);
      acc ^= __builtin_amdgcn_raw_buffer_load_b32(r, (u32)(row * 8192 + (tid & 15) * 128), 0, 0);
    }
  }
  {
    const rsrc_t r = __builtin_amdgcn_make_buffer_rsrc((void*)(p.Wo + (size_t)j * 1024 * 576), 0, 1024 * 1152, 0x00020000);
#pragma unroll
    for (int k = 0; k < 3; ++k) acc ^= __builtin_amdgcn_raw_buffer_load_b32(r, (u32)((k * 12 + n) * 256 + tid) * 128, 0, 0);
  }
  asm volatile("" :: "v"(acc));
}
DEVI v4u make_srd(const void* p, u32 bytes) {
  const unsigned long long a = (unsigned long long)p;
  return (v4u){rfl((u32)a), rfl((u32)(a >> 32) & 0xffffu), bytes, 0x00020000u};
}
DEVI v16u cat4(v4u a, v4u b, v4u c, v4u d) {
  return (v16u){a.x, a.y, a.z, a.w, b.x, b.y, b.z, b.w, c.x, c.y, c.z, c.w, d.x, d.y, d.z, d.w};
}
DEVI u32 wswz(int j, int g) { return (u32)(4 * ((j >> 1) & 1) ^ 2 * (g & 1)); }
DEVI u32 tr_base(int lane, int chunk) {
  const int g = lane >> 4, q = (lane >> 2) & 3;
  return (u32)(g * 1024 + q * 128) + (((u32)chunk ^ wswz(q, g)) << 4);
}

#define RUN_ASM(TEXT) do { for (int i_ = 0; i_ < 16; ++i_) { sa[i_] = rfl(sa[i_]); sb[i_] = rfl(sb[i_]); } \
    asm volatile(TEXT "s_endpgm\n" :: "{s[36:51]}"(sa), "{s[52:67]}"(sb), "{v[0:15]}"(va) : "v255", "a255", "s99", "vcc", "memory"); \
    __builtin_unreachable(); } while (0)

#define GEMM_DECL v4u g_w0, g_w1, g_w2; u32 g_ldw, g_in_base, g_in_stride, g_in_mask, g_out_base, g_out_kb, g_out_mask, g_flag, g_paddr; int g_pbias, g_pinit = -1
#define GEMM_ARGS g_w0, g_w1, g_w2, g_ldw, g_in_base, g_in_stride, g_in_mask, g_out_base, g_out_kb, g_out_mask, g_flag, g_paddr, g_pbias, g_pinit
DEVI void gemm_args(v4u g_w0, v4u g_w1, v4u g_w2, u32 g_ldw, u32 g_in_base, u32 g_in_stride, u32 g_in_mask, u32 g_out_base, u32 g_out_kb,
                    u32 g_out_mask, u32 g_flag, u32 g_paddr, int g_pbias, int g_pinit, const Params& p, u32 dbg, unsigned long long tag, v16u& sa, v16u& sb, v16u& va) {
  const int tid = threadIdx.x, lane = tid & 63, w = (int)rfl(tid >> 6);
  const int c = lane & 15, gg = lane >> 4, pp = lane & 3;
  const unsigned long long fm = __ballot(g_flag != OOB);
  sa = cat4(make_srd(p.ws, WS_END), g_w0, g_w1, g_w2);
  sb = (v16u){g_in_base, g_in_stride, g_in_mask, g_out_base, SLOT, g_out_mask, 0, (u32)w * 1024, dbg + w * 64, 0, (u32)fm, (u32)(fm >> 32), 0, 0, (u32)(2 * tag), (u32)((2 * tag) >> 32)};
  va[0] = lane * 16;
  va[1] = lane * 16 + w * 2048;
  va[2] = g_paddr;
  va[3] = (u32)g_pbias;
  va[4] = kb_off((int)g_out_kb + 8 + 2 * (w >> 1)) + (u32)((gg & 1) * 1024 + ((2 * (w & 1) + (gg >> 1)) * 16 + c) * 16);
  va[5] = (u32)(8 * w + (lane >> 3)) * g_ldw + ((((u32)lane & 7) ^ wswz(lane >> 3, w)) << 4);
  va[6] = (u32)(w >> 1) * 4096 + tr_base(lane, 4 * (w & 1) + pp);
  va[7] = 2 * 4096 + tr_base(lane, 2 * w + (pp >> 1)) + 8 * (pp & 1);
  for (int i = 8; i < 16; ++i) va[i] = 0;
  va[11] = (u32)g_pinit;
  va[14] = g_flag;
  va[15] = kb_off((int)g_out_kb + 2 * w) + lane * 16;
}

DEVI void role_p(const Params& p, int L, int j, int i, u32 dbg, unsigned long long tag) {
  const int lane = threadIdx.x & 63, w = threadIdx.x >> 6, h = 8 * j + i;
  const u32 wb = 576u * 4096 * 2 - h * 256;
  GEMM_DECL;
  g_w0 = make_srd(p.Wq + h * 128, wb); g_w1 = make_srd(p.Wk + h * 128, wb); g_w2 = make_srd(p.Wv + h * 128, wb);
  g_ldw = 8192;
  g_in_base = L ? pgb(1, B_XN) : OFF_XN0; g_in_stride = L ? SLOT : 5 * CHUNK; g_in_mask = L ? RX2 - 1 : 63;
  g_out_base = pga(L, B_QKV + 3 * h); g_out_kb = 0; g_out_mask = RQ - 1;
  g_flag = lane == 0 ? mbox(L, j, SL_REC + i, w) : (lane == 1 ? mbox_att(L, j, i, 8 + w) : OOB);
  g_paddr = mbox(L, j, SL_P + i, lane);
  g_pbias = lane < (L ? 16 : 48) ? 0 : (lane == 48 ? RQ : 1000);
  g_pinit = lane < 16 || lane >= 48 ? -1 : (lane < 32 ? LN_B - 1 : LN_C - 1);
  v16u sa, sb, va;
  gemm_args(GEMM_ARGS, p, dbg, tag, sa, sb, va);
  RUN_ASM(
    "s_nop 4\n"
    "s_getpc_b64 s[84:85]\n"
    ".LgP_PC_%=:\n"
    "s_and_b32 s85, s85, 0xffff\n"
    "s_mov_b32 s86, .LgP_FIN_%=-.LgP_PC_%=\n"
    "s_mov_b32 s87, 0x20000\n"
    "v_mbcnt_lo_u32_b32 v40, -1, 0\n"
    "v_mbcnt_hi_u32_b32 v40, -1, v40\n"
    "v_lshlrev_b32 v40, 7, v40\n"
    "buffer_load_dword v41, v40, s[84:87], 0 offen\n"
    "v_add_u32 v40, 8192, v40\n"
    "buffer_load_dword v41, v40, s[84:87], 0 offen\n"
    "v_add_u32 v40, 8192, v40\n"
    "buffer_load_dword v41, v40, s[84:87], 0 offen\n"
    "v_add_u32 v8, s59, v0\n"
    "s_mov_b32 s68, 0\n"
    "s_mov_b32 s71, 0\n"
    "s_mov_b32 s78, 65536\n"
    "v_add_u32 v9, s78, v0\n"
    "s_mov_b32 s64, 0xbfb8aa3b\n"
    "s_mov_b32 s72, 262144\n"
    "s_add_u32 m0, s59, 0\n"
    "s_mov_b32 s74, 0\n"
    "buffer_load_dwordx4 v5, s[40:43], s74 offen lds\n"
    "s_add_u32 m0, s59, 4096\n"
    "s_mov_b32 s74, 128\n"
    "buffer_load_dwordx4 v5, s[40:43], s74 offen lds\n"
    "s_add_u32 m0, s59, 8192\n"
    "s_mov_b32 s74, 0\n"
    "buffer_load_dwordx4 v5, s[44:47], s74 offen lds\n"
    "s_add_u32 m0, s59, 12288\n"
    "s_mov_b32 s74, 128\n"
    "buffer_load_dwordx4 v5, s[44:47], s74 offen lds\n"
    "s_add_u32 m0, s59, 16384\n"
    "s_mov_b32 s74, 0\n"
    "buffer_load_dwordx4 v5, s[48:51], s74 offen lds\n"
    "s_add_u32 m0, s59, 20480\n"
    "s_mov_b32 s74, 128\n"
    "buffer_load_dwordx4 v5, s[48:51], s74 offen lds\n"
    "s_add_u32 m0, s59, 24576\n"
    "s_mov_b32 s74, 262144\n"
    "buffer_load_dwordx4 v5, s[40:43], s74 offen lds\n"
    "s_add_u32 m0, s59, 28672\n"
    "s_mov_b32 s74, 262272\n"
    "buffer_load_dwordx4 v5, s[40:43], s74 offen lds\n"
    "s_add_u32 m0, s59, 32768\n"
    "s_mov_b32 s74, 262144\n"
    "buffer_load_dwordx4 v5, s[44:47], s74 offen lds\n"
    "s_add_u32 m0, s59, 36864\n"
    "s_mov_b32 s74, 262272\n"
    "buffer_load_dwordx4 v5, s[44:47], s74 offen lds\n"
    "s_add_u32 m0, s59, 40960\n"
    "s_mov_b32 s74, 262144\n"
    "buffer_load_dwordx4 v5, s[48:51], s74 offen lds\n"
    "s_add_u32 m0, s59, 45056\n"
    "s_mov_b32 s74, 262272\n"
    "buffer_load_dwordx4 v5, s[48:51], s74 offen lds\n"
    "s_add_u32 m0, s59, 49152\n"
    "s_mov_b32 s74, 524288\n"
    "buffer_load_dwordx4 v5, s[40:43], s74 offen lds\n"
    "s_add_u32 m0, s59, 53248\n"
    "s_mov_b32 s74, 524416\n"
    "buffer_load_dwordx4 v5, s[40:43], s74 offen lds\n"
    "s_add_u32 m0, s59, 57344\n"
    "s_mov_b32 s74, 524288\n"
    "buffer_load_dwordx4 v5, s[44:47], s74 offen lds\n"
    "s_add_u32 m0, s59, 61440\n"
    "s_mov_b32 s74, 524416\n"
    "buffer_load_dwordx4 v5, s[44:47], s74 offen lds\n"
    "s_add_u32 m0, s59, 65536\n"
    "s_mov_b32 s74, 524288\n"
    "buffer_load_dwordx4 v5, s[48:51], s74 offen lds\n"
    "s_add_u32 m0, s59, 69632\n"
    "s_mov_b32 s74, 524416\n"
    "buffer_load_dwordx4 v5, s[48:51], s74 offen lds\n"
    "s_add_u32 m0, s59, 73728\n"
    "s_mov_b32 s74, 786432\n"
    "buffer_load_dwordx4 v5, s[40:43], s74 offen lds\n"
    "s_add_u32 m0, s59, 77824\n"
    "s_mov_b32 s74, 786560\n"
    "buffer_load_dwordx4 v5, s[40:43], s74 offen lds\n"
    "s_add_u32 m0, s59, 81920\n"
    "s_mov_b32 s74, 786432\n"
    "buffer_load_dwordx4 v5, s[44:47], s74 offen lds\n"
    "s_add_u32 m0, s59, 86016\n"
    "s_mov_b32 s74, 786560\n"
    "buffer_load_dwordx4 v5, s[44:47], s74 offen lds\n"
    "s_add_u32 m0, s59, 90112\n"
    "s_mov_b32 s74, 786432\n"
    "buffer_load_dwordx4 v5, s[48:51], s74 offen lds\n"
    "s_add_u32 m0, s59, 94208\n"
    "s_mov_b32 s74, 786560\n"
    "buffer_load_dwordx4 v5, s[48:51], s74 offen lds\n"
    "s_add_u32 m0, s59, 98304\n"
    "s_mov_b32 s74, 1048576\n"
    "buffer_load_dwordx4 v5, s[40:43], s74 offen lds\n"
    "s_add_u32 m0, s59, 102400\n"
    "s_mov_b32 s74, 1048704\n"
    "buffer_load_dwordx4 v5, s[40:43], s74 offen lds\n"
    "s_add_u32 m0, s59, 106496\n"
    "s_mov_b32 s74, 1048576\n"
    "buffer_load_dwordx4 v5, s[44:47], s74 offen lds\n"
    "s_add_u32 m0, s59, 110592\n"
    "s_mov_b32 s74, 1048704\n"
    "buffer_load_dwordx4 v5, s[44:47], s74 offen lds\n"
    "s_add_u32 m0, s59, 114688\n"
    "s_mov_b32 s74, 1048576\n"
    "buffer_load_dwordx4 v5, s[48:51], s74 offen lds\n"
    "s_add_u32 m0, s59, 118784\n"
    "s_mov_b32 s74, 1048704\n"
    "buffer_load_dwordx4 v5, s[48:51], s74 offen lds\n"
    "v_add_u32 v16, 49152, v6\n"
    "v_add_u32 v17, 98304, v6\n"
    "v_add_u32 v18, 49152, v7\n"
    "v_add_u32 v19, 98304, v7\n"
    "s_waitcnt vmcnt(24) lgkmcnt(0)\n"
    "s_barrier\n"
    "s_add_u32 m0, s59, 122880\n"
    "s_mov_b32 s74, 1310720\n"
    "buffer_load_dwordx4 v5, s[40:43], s74 offen lds\n"
    "s_add_u32 m0, s59, 126976\n"
    "s_mov_b32 s74, 1310848\n"
    "buffer_load_dwordx4 v5, s[40:43], s74 offen lds\n"
    "s_add_u32 m0, s59, 131072\n"
    "s_mov_b32 s74, 1310720\n"
    "buffer_load_dwordx4 v5, s[44:47], s74 offen lds\n"
    "s_add_u32 m0, s59, 135168\n"
    "s_mov_b32 s74, 1310848\n"
    "buffer_load_dwordx4 v5, s[44:47], s74 offen lds\n"
    "s_add_u32 m0, s59, 139264\n"
    "s_mov_b32 s74, 1310720\n"
    "buffer_load_dwordx4 v5, s[48:51], s74 offen lds\n"
    "s_add_u32 m0, s59, 143360\n"
    "s_mov_b32 s74, 1310848\n"
    "buffer_load_dwordx4 v5, s[48:51], s74 offen lds\n"
    "ds_read_b64_tr_b16 a[0:1], v6 offset:8192\n"
    "ds_read_b64_tr_b16 a[2:3], v6 offset:8704\n"
    "ds_read_b64_tr_b16 a[4:5], v6 offset:8200\n"
    "ds_read_b64_tr_b16 a[6:7], v6 offset:8712\n"
    "ds_read_b64_tr_b16 a[8:9], v6 offset:16384\n"
    "ds_read_b64_tr_b16 a[10:11], v6 offset:16896\n"
    "ds_read_b64_tr_b16 a[12:13], v6 offset:16392\n"
    "ds_read_b64_tr_b16 a[14:15], v6 offset:16904\n"
    "ds_read_b64_tr_b16 a[16:17], v6 offset:0\n"
    "ds_read_b64_tr_b16 a[18:19], v6 offset:512\n"
    "ds_read_b64_tr_b16 a[20:21], v6 offset:8\n"
    "ds_read_b64_tr_b16 a[22:23], v6 offset:520\n"
    "s_waitcnt vmcnt(24) lgkmcnt(0)\n"
    "s_barrier\n"
    "s_add_u32 m0, s59, 0\n"
    "s_mov_b32 s74, 1572864\n"
    "buffer_load_dwordx4 v5, s[40:43], s74 offen lds\n"
    "s_add_u32 m0, s59, 4096\n"
    "s_mov_b32 s74, 1572992\n"
    "buffer_load_dwordx4 v5, s[40:43], s74 offen lds\n"
    "s_add_u32 m0, s59, 8192\n"
    "s_mov_b32 s74, 1572864\n"
    "buffer_load_dwordx4 v5, s[44:47], s74 offen lds\n"
    "s_add_u32 m0, s59, 12288\n"
    "s_mov_b32 s74, 1572992\n"
    "buffer_load_dwordx4 v5, s[44:47], s74 offen lds\n"
    "s_add_u32 m0, s59, 16384\n"
    "s_mov_b32 s74, 1572864\n"
    "buffer_load_dwordx4 v5, s[48:51], s74 offen lds\n"
    "s_add_u32 m0, s59, 20480\n"
    "s_mov_b32 s74, 1572992\n"
    "buffer_load_dwordx4 v5, s[48:51], s74 offen lds\n"
    "ds_read_b64_tr_b16 a[24:25], v6 offset:32768\n"
    "ds_read_b64_tr_b16 a[26:27], v6 offset:33280\n"
    "ds_read_b64_tr_b16 a[28:29], v6 offset:32776\n"
    "ds_read_b64_tr_b16 a[30:31], v6 offset:33288\n"
    "ds_read_b64_tr_b16 a[32:33], v6 offset:40960\n"
    "ds_read_b64_tr_b16 a[34:35], v6 offset:41472\n"
    "ds_read_b64_tr_b16 a[36:37], v6 offset:40968\n"
    "ds_read_b64_tr_b16 a[38:39], v6 offset:41480\n"
    "ds_read_b64_tr_b16 a[40:41], v6 offset:24576\n"
    "ds_read_b64_tr_b16 a[42:43], v6 offset:25088\n"
    "ds_read_b64_tr_b16 a[44:45], v6 offset:24584\n"
    "ds_read_b64_tr_b16 a[46:47], v6 offset:25096\n"
    "s_waitcnt vmcnt(24) lgkmcnt(0)\n"
    "s_barrier\n"
    "s_add_u32 m0, s59, 24576\n"
    "s_mov_b32 s74, 1835008\n"
    "buffer_load_dwordx4 v5, s[40:43], s74 offen lds\n"
    "s_add_u32 m0, s59, 28672\n"
    "s_mov_b32 s74, 1835136\n"
    "buffer_load_dwordx4 v5, s[40:43], s74 offen lds\n"
    "s_add_u32 m0, s59, 32768\n"
    "s_mov_b32 s74, 1835008\n"
    "buffer_load_dwordx4 v5, s[44:47], s74 offen lds\n"
    "s_add_u32 m0, s59, 36864\n"
    "s_mov_b32 s74, 1835136\n"
    "buffer_load_dwordx4 v5, s[44:47], s74 offen lds\n"
    "s_add_u32 m0, s59, 40960\n"
    "s_mov_b32 s74, 1835008\n"
    "buffer_load_dwordx4 v5, s[48:51], s74 offen lds\n"
    "s_add_u32 m0, s59, 45056\n"
    "s_mov_b32 s74, 1835136\n"
    "buffer_load_dwordx4 v5, s[48:51], s74 offen lds\n"
    "ds_read_b64_tr_b16 a[48:49], v16 offset:8192\n"
    "ds_read_b64_tr_b16 a[50:51], v16 offset:8704\n"
    "ds_read_b64_tr_b16 a[52:53], v16 offset:8200\n"
    "ds_read_b64_tr_b16 a[54:55], v16 offset:8712\n"
    "ds_read_b64_tr_b16 a[56:57], v16 offset:16384\n"
    "ds_read_b64_tr_b16 a[58:59], v16 offset:16896\n"
    "ds_read_b64_tr_b16 a[60:61], v16 offset:16392\n"
    "ds_read_b64_tr_b16 a[62:63], v16 offset:16904\n"
    "ds_read_b64_tr_b16 a[64:65], v16 offset:0\n"
    "ds_read_b64_tr_b16 a[66:67], v16 offset:512\n"
    "ds_read_b64_tr_b16 a[68:69], v16 offset:8\n"
    "ds_read_b64_tr_b16 a[70:71], v16 offset:520\n"
    "s_waitcnt vmcnt(24) lgkmcnt(0)\n"
    "s_barrier\n"
    "s_add_u32 m0, s59, 49152\n"
    "s_mov_b32 s74, 2097152\n"
    "buffer_load_dwordx4 v5, s[40:43], s74 offen lds\n"
    "s_add_u32 m0, s59, 53248\n"
    "s_mov_b32 s74, 2097280\n"
    "buffer_load_dwordx4 v5, s[40:43], s74 offen lds\n"
    "s_add_u32 m0, s59, 57344\n"
    "s_mov_b32 s74, 2097152\n"
    "buffer_load_dwordx4 v5, s[44:47], s74 offen lds\n"
    "s_add_u32 m0, s59, 61440\n"
    "s_mov_b32 s74, 2097280\n"
    "buffer_load_dwordx4 v5, s[44:47], s74 offen lds\n"
    "s_add_u32 m0, s59, 65536\n"
    "s_mov_b32 s74, 2097152\n"
    "buffer_load_dwordx4 v5, s[48:51], s74 offen lds\n"
    "s_add_u32 m0, s59, 69632\n"
    "s_mov_b32 s74, 2097280\n"
    "buffer_load_dwordx4 v5, s[48:51], s74 offen lds\n"
    "ds_read_b64_tr_b16 a[72:73], v16 offset:32768\n"
    "ds_read_b64_tr_b16 a[74:75], v16 offset:33280\n"
    "ds_read_b64_tr_b16 a[76:77], v16 offset:32776\n"
    "ds_read_b64_tr_b16 a[78:79], v16 offset:33288\n"
    "ds_read_b64_tr_b16 a[80:81], v16 offset:40960\n"
    "ds_read_b64_tr_b16 a[82:83], v16 offset:41472\n"
    "ds_read_b64_tr_b16 a[84:85], v16 offset:40968\n"
    "ds_read_b64_tr_b16 a[86:87], v16 offset:41480\n"
    "ds_read_b64_tr_b16 a[88:89], v16 offset:24576\n"
    "ds_read_b64_tr_b16 a[90:91], v16 offset:25088\n"
    "ds_read_b64_tr_b16 a[92:93], v16 offset:24584\n"
    "ds_read_b64_tr_b16 a[94:95], v16 offset:25096\n"
    "s_waitcnt vmcnt(24) lgkmcnt(0)\n"
    "s_barrier\n"
    "s_add_u32 m0, s59, 73728\n"
    "s_mov_b32 s74, 2359296\n"
    "buffer_load_dwordx4 v5, s[40:43], s74 offen lds\n"
    "s_add_u32 m0, s59, 77824\n"
    "s_mov_b32 s74, 2359424\n"
    "buffer_load_dwordx4 v5, s[40:43], s74 offen lds\n"
    "s_add_u32 m0, s59, 81920\n"
    "s_mov_b32 s74, 2359296\n"
    "buffer_load_dwordx4 v5, s[44:47], s74 offen lds\n"
    "s_add_u32 m0, s59, 86016\n"
    "s_mov_b32 s74, 2359424\n"
    "buffer_load_dwordx4 v5, s[44:47], s74 offen lds\n"
    "s_add_u32 m0, s59, 90112\n"
    "s_mov_b32 s74, 2359296\n"
    "buffer_load_dwordx4 v5, s[48:51], s74 offen lds\n"
    "s_add_u32 m0, s59, 94208\n"
    "s_mov_b32 s74, 2359424\n"
    "buffer_load_dwordx4 v5, s[48:51], s74 offen lds\n"
    "ds_read_b64_tr_b16 a[96:97], v17 offset:8192\n"
    "ds_read_b64_tr_b16 a[98:99], v17 offset:8704\n"
    "ds_read_b64_tr_b16 a[100:101], v17 offset:8200\n"
    "ds_read_b64_tr_b16 a[102:103], v17 offset:8712\n"
    "ds_read_b64_tr_b16 a[104:105], v17 offset:16384\n"
    "ds_read_b64_tr_b16 a[106:107], v17 offset:16896\n"
    "ds_read_b64_tr_b16 a[108:109], v17 offset:16392\n"
    "ds_read_b64_tr_b16 a[110:111], v17 offset:16904\n"
    "ds_read_b64_tr_b16 a[112:113], v17 offset:0\n"
    "ds_read_b64_tr_b16 a[114:115], v17 offset:512\n"
    "ds_read_b64_tr_b16 a[116:117], v17 offset:8\n"
    "ds_read_b64_tr_b16 a[118:119], v17 offset:520\n"
    "s_waitcnt vmcnt(24) lgkmcnt(0)\n"
    "s_barrier\n"
    "s_add_u32 m0, s59, 98304\n"
    "s_mov_b32 s74, 2621440\n"
    "buffer_load_dwordx4 v5, s[40:43], s74 offen lds\n"
    "s_add_u32 m0, s59, 102400\n"
    "s_mov_b32 s74, 2621568\n"
    "buffer_load_dwordx4 v5, s[40:43], s74 offen lds\n"
    "s_add_u32 m0, s59, 106496\n"
    "s_mov_b32 s74, 2621440\n"
    "buffer_load_dwordx4 v5, s[44:47], s74 offen lds\n"
    "s_add_u32 m0, s59, 110592\n"
    "s_mov_b32 s74, 2621568\n"
    "buffer_load_dwordx4 v5, s[44:47], s74 offen lds\n"
    "s_add_u32 m0, s59, 114688\n"
    "s_mov_b32 s74, 2621440\n"
    "buffer_load_dwordx4 v5, s[48:51], s74 offen lds\n"
    "s_add_u32 m0, s59, 118784\n"
    "s_mov_b32 s74, 2621568\n"
    "buffer_load_dwordx4 v5, s[48:51], s74 offen lds\n"
    "ds_read_b64_tr_b16 a[120:121], v17 offset:32768\n"
    "ds_read_b64_tr_b16 a[122:123], v17 offset:33280\n"
    "ds_read_b64_tr_b16 a[124:125], v17 offset:32776\n"
    "ds_read_b64_tr_b16 a[126:127], v17 offset:33288\n"
    "ds_read_b64_tr_b16 a[128:129], v17 offset:40960\n"
    "ds_read_b64_tr_b16 a[130:131], v17 offset:41472\n"
    "ds_read_b64_tr_b16 a[132:133], v17 offset:40968\n"
    "ds_read_b64_tr_b16 a[134:135], v17 offset:41480\n"
    "ds_read_b64_tr_b16 a[136:137], v17 offset:24576\n"
    "ds_read_b64_tr_b16 a[138:139], v17 offset:25088\n"
    "ds_read_b64_tr_b16 a[140:141], v17 offset:24584\n"
    "ds_read_b64_tr_b16 a[142:143], v17 offset:25096\n"
    "s_waitcnt vmcnt(24) lgkmcnt(0)\n"
    "s_barrier\n"
    "s_add_u32 m0, s59, 122880\n"
    "s_mov_b32 s74, 2883584\n"
    "buffer_load_dwordx4 v5, s[40:43], s74 offen lds\n"
    "s_add_u32 m0, s59, 126976\n"
    "s_mov_b32 s74, 2883712\n"
    "buffer_load_dwordx4 v5, s[40:43], s74 offen lds\n"
    "s_add_u32 m0, s59, 131072\n"
    "s_mov_b32 s74, 2883584\n"
    "buffer_load_dwordx4 v5, s[44:47], s74 offen lds\n"
    "s_add_u32 m0, s59, 135168\n"
    "s_mov_b32 s74, 2883712\n"
    "buffer_load_dwordx4 v5, s[44:47], s74 offen lds\n"
    "s_add_u32 m0, s59, 139264\n"
    "s_mov_b32 s74, 2883584\n"
    "buffer_load_dwordx4 v5, s[48:51], s74 offen lds\n"
    "s_add_u32 m0, s59, 143360\n"
    "s_mov_b32 s74, 2883712\n"
    "buffer_load_dwordx4 v5, s[48:51], s74 offen lds\n"
    "ds_read_b64_tr_b16 a[144:145], v6 offset:8192\n"
    "ds_read_b64_tr_b16 a[146:147], v6 offset:8704\n"
    "ds_read_b64_tr_b16 a[148:149], v6 offset:8200\n"
    "ds_read_b64_tr_b16 a[150:151], v6 offset:8712\n"
    "ds_read_b64_tr_b16 a[152:153], v6 offset:16384\n"
    "ds_read_b64_tr_b16 a[154:155], v6 offset:16896\n"
    "ds_read_b64_tr_b16 a[156:157], v6 offset:16392\n"
    "ds_read_b64_tr_b16 a[158:159], v6 offset:16904\n"
    "ds_read_b64_tr_b16 a[160:161], v6 offset:0\n"
    "ds_read_b64_tr_b16 a[162:163], v6 offset:512\n"
    "ds_read_b64_tr_b16 a[164:165], v6 offset:8\n"
    "ds_read_b64_tr_b16 a[166:167], v6 offset:520\n"
    "s_waitcnt vmcnt(24) lgkmcnt(0)\n"
    "s_barrier\n"
    "s_add_u32 m0, s59, 0\n"
    "s_mov_b32 s74, 3145728\n"
    "buffer_load_dwordx4 v5, s[40:43], s74 offen lds\n"
    "s_add_u32 m0, s59, 4096\n"
    "s_mov_b32 s74, 3145856\n"
    "buffer_load_dwordx4 v5, s[40:43], s74 offen lds\n"
    "s_add_u32 m0, s59, 8192\n"
    "s_mov_b32 s74, 3145728\n"
    "buffer_load_dwordx4 v5, s[44:47], s74 offen lds\n"
    "s_add_u32 m0, s59, 12288\n"
    "s_mov_b32 s74, 3145856\n"
    "buffer_load_dwordx4 v5, s[44:47], s74 offen lds\n"
    "s_add_u32 m0, s59, 16384\n"
    "s_mov_b32 s74, 3145728\n"
    "buffer_load_dwordx4 v5, s[48:51], s74 offen lds\n"
    "s_add_u32 m0, s59, 20480\n"
    "s_mov_b32 s74, 3145856\n"
    "buffer_load_dwordx4 v5, s[48:51], s74 offen lds\n"
    "ds_read_b64_tr_b16 a[168:169], v6 offset:32768\n"
    "ds_read_b64_tr_b16 a[170:171], v6 offset:33280\n"
    "ds_read_b64_tr_b16 a[172:173], v6 offset:32776\n"
    "ds_read_b64_tr_b16 a[174:175], v6 offset:33288\n"
    "ds_read_b64_tr_b16 a[176:177], v6 offset:40960\n"
    "ds_read_b64_tr_b16 a[178:179], v6 offset:41472\n"
    "ds_read_b64_tr_b16 a[180:181], v6 offset:40968\n"
    "ds_read_b64_tr_b16 a[182:183], v6 offset:41480\n"
    "ds_read_b64_tr_b16 a[184:185], v6 offset:24576\n"
    "ds_read_b64_tr_b16 a[186:187], v6 offset:25088\n"
    "ds_read_b64_tr_b16 a[188:189], v6 offset:24584\n"
    "ds_read_b64_tr_b16 a[190:191], v6 offset:25096\n"
    "s_waitcnt vmcnt(24) lgkmcnt(0)\n"
    "s_barrier\n"
    "s_add_u32 m0, s59, 24576\n"
    "s_mov_b32 s74, 3407872\n"
    "buffer_load_dwordx4 v5, s[40:43], s74 offen lds\n"
    "s_add_u32 m0, s59, 28672\n"
    "s_mov_b32 s74, 3408000\n"
    "buffer_load_dwordx4 v5, s[40:43], s74 offen lds\n"
    "s_add_u32 m0, s59, 32768\n"
    "s_mov_b32 s74, 3407872\n"
    "buffer_load_dwordx4 v5, s[44:47], s74 offen lds\n"
    "s_add_u32 m0, s59, 36864\n"
    "s_mov_b32 s74, 3408000\n"
    "buffer_load_dwordx4 v5, s[44:47], s74 offen lds\n"
    "s_add_u32 m0, s59, 40960\n"
    "s_mov_b32 s74, 3407872\n"
    "buffer_load_dwordx4 v5, s[48:51], s74 offen lds\n"
    "s_add_u32 m0, s59, 45056\n"
    "s_mov_b32 s74, 3408000\n"
    "buffer_load_dwordx4 v5, s[48:51], s74 offen lds\n"
    "ds_read_b64_tr_b16 a[192:193], v16 offset:8192\n"
    "ds_read_b64_tr_b16 a[194:195], v16 offset:8704\n"
    "ds_read_b64_tr_b16 a[196:197], v16 offset:8200\n"
    "ds_read_b64_tr_b16 a[198:199], v16 offset:8712\n"
    "ds_read_b64_tr_b16 a[200:201], v16 offset:16384\n"
    "ds_read_b64_tr_b16 a[202:203], v16 offset:16896\n"
    "ds_read_b64_tr_b16 a[204:205], v16 offset:16392\n"
    "ds_read_b64_tr_b16 a[206:207], v16 offset:16904\n"
    "ds_read_b64_tr_b16 a[208:209], v16 offset:0\n"
    "ds_read_b64_tr_b16 a[210:211], v16 offset:512\n"
    "ds_read_b64_tr_b16 a[212:213], v16 offset:8\n"
    "ds_read_b64_tr_b16 a[214:215], v16 offset:520\n"
    "s_waitcnt vmcnt(24) lgkmcnt(0)\n"
    "s_barrier\n"
    "s_add_u32 m0, s59, 49152\n"
    "s_mov_b32 s74, 3670016\n"
    "buffer_load_dwordx4 v5, s[40:43], s74 offen lds\n"
    "s_add_u32 m0, s59, 53248\n"
    "s_mov_b32 s74, 3670144\n"
    "buffer_load_dwordx4 v5, s[40:43], s74 offen lds\n"
    "s_add_u32 m0, s59, 57344\n"
    "s_mov_b32 s74, 3670016\n"
    "buffer_load_dwordx4 v5, s[44:47], s74 offen lds\n"
    "s_add_u32 m0, s59, 61440\n"
    "s_mov_b32 s74, 3670144\n"
    "buffer_load_dwordx4 v5, s[44:47], s74 offen lds\n"
    "s_add_u32 m0, s59, 65536\n"
    "s_mov_b32 s74, 3670016\n"
    "buffer_load_dwordx4 v5, s[48:51], s74 offen lds\n"
    "s_add_u32 m0, s59, 69632\n"
    "s_mov_b32 s74, 3670144\n"
    "buffer_load_dwordx4 v5, s[48:51], s74 offen lds\n"
    "ds_read_b64_tr_b16 a[216:217], v16 offset:32768\n"
    "ds_read_b64_tr_b16 a[218:219], v16 offset:33280\n"
    "ds_read_b64_tr_b16 a[220:221], v16 offset:32776\n"
    "ds_read_b64_tr_b16 a[222:223], v16 offset:33288\n"
    "ds_read_b64_tr_b16 a[224:225], v16 offset:40960\n"
    "ds_read_b64_tr_b16 a[226:227], v16 offset:41472\n"
    "ds_read_b64_tr_b16 a[228:229], v16 offset:40968\n"
    "ds_read_b64_tr_b16 a[230:231], v16 offset:41480\n"
    "ds_read_b64_tr_b16 a[232:233], v16 offset:24576\n"
    "ds_read_b64_tr_b16 a[234:235], v16 offset:25088\n"
    "ds_read_b64_tr_b16 a[236:237], v16 offset:24584\n"
    "ds_read_b64_tr_b16 a[238:239], v16 offset:25096\n"
    "s_waitcnt vmcnt(24) lgkmcnt(0)\n"
    "s_barrier\n"
    "s_add_u32 m0, s59, 73728\n"
    "s_mov_b32 s74, 3932160\n"
    "buffer_load_dwordx4 v5, s[40:43], s74 offen lds\n"
    "s_add_u32 m0, s59, 77824\n"
    "s_mov_b32 s74, 3932288\n"
    "buffer_load_dwordx4 v5, s[40:43], s74 offen lds\n"
    "s_add_u32 m0, s59, 81920\n"
    "s_mov_b32 s74, 3932160\n"
    "buffer_load_dwordx4 v5, s[44:47], s74 offen lds\n"
    "s_add_u32 m0, s59, 86016\n"
    "s_mov_b32 s74, 3932288\n"
    "buffer_load_dwordx4 v5, s[44:47], s74 offen lds\n"
    "s_add_u32 m0, s59, 90112\n"
    "s_mov_b32 s74, 3932160\n"
    "buffer_load_dwordx4 v5, s[48:51], s74 offen lds\n"
    "s_add_u32 m0, s59, 94208\n"
    "s_mov_b32 s74, 3932288\n"
    "buffer_load_dwordx4 v5, s[48:51], s74 offen lds\n"
    "ds_read_b64_tr_b16 a[240:241], v17 offset:8192\n"
    "ds_read_b64_tr_b16 a[242:243], v17 offset:8704\n"
    "ds_read_b64_tr_b16 a[244:245], v17 offset:8200\n"
    "ds_read_b64_tr_b16 a[246:247], v17 offset:8712\n"
    "ds_read_b64_tr_b16 a[248:249], v17 offset:16384\n"
    "ds_read_b64_tr_b16 a[250:251], v17 offset:16896\n"
    "ds_read_b64_tr_b16 a[252:253], v17 offset:16392\n"
    "ds_read_b64_tr_b16 a[254:255], v17 offset:16904\n"
    "ds_read_b64_tr_b16 v[80:81], v17 offset:0\n"
    "ds_read_b64_tr_b16 v[82:83], v17 offset:512\n"
    "ds_read_b64_tr_b16 v[84:85], v17 offset:8\n"
    "ds_read_b64_tr_b16 v[86:87], v17 offset:520\n"
    "s_waitcnt vmcnt(24) lgkmcnt(0)\n"
    "s_barrier\n"
    "s_add_u32 m0, s59, 98304\n"
    "s_mov_b32 s74, 4194304\n"
    "buffer_load_dwordx4 v5, s[40:43], s74 offen lds\n"
    "s_add_u32 m0, s59, 102400\n"
    "s_mov_b32 s74, 4194432\n"
    "buffer_load_dwordx4 v5, s[40:43], s74 offen lds\n"
    "s_add_u32 m0, s59, 106496\n"
    "s_mov_b32 s74, 4194304\n"
    "buffer_load_dwordx4 v5, s[44:47], s74 offen lds\n"
    "s_add_u32 m0, s59, 110592\n"
    "s_mov_b32 s74, 4194432\n"
    "buffer_load_dwordx4 v5, s[44:47], s74 offen lds\n"
    "s_add_u32 m0, s59, 114688\n"
    "s_mov_b32 s74, 4194304\n"
    "buffer_load_dwordx4 v5, s[48:51], s74 offen lds\n"
    "s_add_u32 m0, s59, 118784\n"
    "s_mov_b32 s74, 4194432\n"
    "buffer_load_dwordx4 v5, s[48:51], s74 offen lds\n"
    "ds_read_b64_tr_b16 v[88:89], v17 offset:32768\n"
    "ds_read_b64_tr_b16 v[90:91], v17 offset:33280\n"
    "ds_read_b64_tr_b16 v[92:93], v17 offset:32776\n"
    "ds_read_b64_tr_b16 v[94:95], v17 offset:33288\n"
    "ds_read_b64_tr_b16 v[96:97], v17 offset:40960\n"
    "ds_read_b64_tr_b16 v[98:99], v17 offset:41472\n"
    "ds_read_b64_tr_b16 v[100:101], v17 offset:40968\n"
    "ds_read_b64_tr_b16 v[102:103], v17 offset:41480\n"
    "ds_read_b64_tr_b16 v[104:105], v17 offset:24576\n"
    "ds_read_b64_tr_b16 v[106:107], v17 offset:25088\n"
    "ds_read_b64_tr_b16 v[108:109], v17 offset:24584\n"
    "ds_read_b64_tr_b16 v[110:111], v17 offset:25096\n"
    "s_waitcnt vmcnt(24) lgkmcnt(0)\n"
    "s_barrier\n"
    "s_add_u32 m0, s59, 122880\n"
    "s_mov_b32 s74, 4456448\n"
    "buffer_load_dwordx4 v5, s[40:43], s74 offen lds\n"
    "s_add_u32 m0, s59, 126976\n"
    "s_mov_b32 s74, 4456576\n"
    "buffer_load_dwordx4 v5, s[40:43], s74 offen lds\n"
    "s_add_u32 m0, s59, 131072\n"
    "s_mov_b32 s74, 4456448\n"
    "buffer_load_dwordx4 v5, s[44:47], s74 offen lds\n"
    "s_add_u32 m0, s59, 135168\n"
    "s_mov_b32 s74, 4456576\n"
    "buffer_load_dwordx4 v5, s[44:47], s74 offen lds\n"
    "s_add_u32 m0, s59, 139264\n"
    "s_mov_b32 s74, 4456448\n"
    "buffer_load_dwordx4 v5, s[48:51], s74 offen lds\n"
    "s_add_u32 m0, s59, 143360\n"
    "s_mov_b32 s74, 4456576\n"
    "buffer_load_dwordx4 v5, s[48:51], s74 offen lds\n"
    "buffer_load_dwordx2 v[12:13], off, s[36:39], 0 sc1\n"
    "ds_read_b64_tr_b16 v[112:113], v6 offset:8192\n"
    "ds_read_b64_tr_b16 v[114:115], v6 offset:8704\n"
    "ds_read_b64_tr_b16 v[116:117], v6 offset:8200\n"
    "ds_read_b64_tr_b16 v[118:119], v6 offset:8712\n"
    "ds_read_b64_tr_b16 v[120:121], v6 offset:16384\n"
    "ds_read_b64_tr_b16 v[122:123], v6 offset:16896\n"
    "ds_read_b64_tr_b16 v[124:125], v6 offset:16392\n"
    "ds_read_b64_tr_b16 v[126:127], v6 offset:16904\n"
    "ds_read_b64_tr_b16 v[128:129], v6 offset:0\n"
    "ds_read_b64_tr_b16 v[130:131], v6 offset:512\n"
    "ds_read_b64_tr_b16 v[132:133], v6 offset:8\n"
    "ds_read_b64_tr_b16 v[134:135], v6 offset:520\n"
    "s_waitcnt vmcnt(25) lgkmcnt(0)\n"
    "s_barrier\n"
    "ds_read_b64_tr_b16 v[136:137], v6 offset:32768\n"
    "ds_read_b64_tr_b16 v[138:139], v6 offset:33280\n"
    "ds_read_b64_tr_b16 v[140:141], v6 offset:32776\n"
    "ds_read_b64_tr_b16 v[142:143], v6 offset:33288\n"
    "ds_read_b64_tr_b16 v[144:145], v6 offset:40960\n"
    "ds_read_b64_tr_b16 v[146:147], v6 offset:41472\n"
    "ds_read_b64_tr_b16 v[148:149], v6 offset:40968\n"
    "ds_read_b64_tr_b16 v[150:151], v6 offset:41480\n"
    "ds_read_b64_tr_b16 v[152:153], v6 offset:24576\n"
    "ds_read_b64_tr_b16 v[154:155], v6 offset:25088\n"
    "ds_read_b64_tr_b16 v[156:157], v6 offset:24584\n"
    "ds_read_b64_tr_b16 v[158:159], v6 offset:25096\n"
    "s_waitcnt vmcnt(19) lgkmcnt(0)\n"
    "s_barrier\n"
    "ds_read_b64_tr_b16 v[160:161], v16 offset:8192\n"
    "ds_read_b64_tr_b16 v[162:163], v16 offset:8704\n"
    "ds_read_b64_tr_b16 v[164:165], v16 offset:8200\n"
    "ds_read_b64_tr_b16 v[166:167], v16 offset:8712\n"
    "ds_read_b64_tr_b16 v[168:169], v16 offset:16384\n"
    "ds_read_b64_tr_b16 v[170:171], v16 offset:16896\n"
    "ds_read_b64_tr_b16 v[172:173], v16 offset:16392\n"
    "ds_read_b64_tr_b16 v[174:175], v16 offset:16904\n"
    "ds_read_b64_tr_b16 v[176:177], v16 offset:0\n"
    "ds_read_b64_tr_b16 v[178:179], v16 offset:512\n"
    "ds_read_b64_tr_b16 v[180:181], v16 offset:8\n"
    "ds_read_b64_tr_b16 v[182:183], v16 offset:520\n"
    "s_waitcnt vmcnt(13) lgkmcnt(0)\n"
    "s_barrier\n"
    "ds_read_b64_tr_b16 v[184:185], v16 offset:32768\n"
    "ds_read_b64_tr_b16 v[186:187], v16 offset:33280\n"
    "ds_read_b64_tr_b16 v[188:189], v16 offset:32776\n"
    "ds_read_b64_tr_b16 v[190:191], v16 offset:33288\n"
    "ds_read_b64_tr_b16 v[192:193], v16 offset:40960\n"
    "ds_read_b64_tr_b16 v[194:195], v16 offset:41472\n"
    "ds_read_b64_tr_b16 v[196:197], v16 offset:40968\n"
    "ds_read_b64_tr_b16 v[198:199], v16 offset:41480\n"
    "ds_read_b64_tr_b16 v[200:201], v16 offset:24576\n"
    "ds_read_b64_tr_b16 v[202:203], v16 offset:25088\n"
    "ds_read_b64_tr_b16 v[204:205], v16 offset:24584\n"
    "ds_read_b64_tr_b16 v[206:207], v16 offset:25096\n"
    "s_waitcnt vmcnt(7) lgkmcnt(0)\n"
    "s_barrier\n"
    "ds_read_b64_tr_b16 v[208:209], v17 offset:8192\n"
    "ds_read_b64_tr_b16 v[210:211], v17 offset:8704\n"
    "ds_read_b64_tr_b16 v[212:213], v17 offset:8200\n"
    "ds_read_b64_tr_b16 v[214:215], v17 offset:8712\n"
    "ds_read_b64_tr_b16 v[216:217], v17 offset:16384\n"
    "ds_read_b64_tr_b16 v[218:219], v17 offset:16896\n"
    "ds_read_b64_tr_b16 v[220:221], v17 offset:16392\n"
    "ds_read_b64_tr_b16 v[222:223], v17 offset:16904\n"
    "ds_read_b64_tr_b16 v[224:225], v17 offset:0\n"
    "ds_read_b64_tr_b16 v[226:227], v17 offset:512\n"
    "ds_read_b64_tr_b16 v[228:229], v17 offset:8\n"
    "ds_read_b64_tr_b16 v[230:231], v17 offset:520\n"
    "s_waitcnt vmcnt(0) lgkmcnt(0)\n"
    "s_barrier\n"
    ".LgP_par_%=:\n"
    "v_readfirstlane_b32 s74, v12\n"
    "v_readfirstlane_b32 s75, v13\n"
    "s_xor_b32 s74, s74, s66\n"
    "s_xor_b32 s75, s75, s67\n"
    "s_cselect_b32 s75, 2, 0\n"
    "s_or_b32 s75, s75, s74\n"
    "s_cmp_lt_u32 s75, 2\n"
    "s_cbranch_scc1 .LgP_par1_%=\n"
    "s_sleep 4\n"
    "buffer_load_dwordx2 v[12:13], off, s[36:39], 0 sc1\n"
    "s_waitcnt vmcnt(0)\n"
    "s_sub_u32 s72, s72, 1\n"
    "s_cbranch_scc0 .LgP_par_%=\n"
    "s_mov_b32 s72, 0\n"
    ".LgP_par1_%=:\n"
    "s_and_b32 s74, s74, 1\n"
    "s_lshl_b32 s74, s74, 13\n"
    "s_add_u32 s36, s36, s74\n"
    "s_addc_u32 s37, s37, 0\n"
    "v_add_u32 v13, v11, v3\n"
    "v_cmp_gt_i32 vcc, s71, v13\n"
    "s_nop 0\n"
    "s_or_b64 exec, vcc, 1\n"
    "buffer_atomic_smax v11, v2, s[36:39], 0 offen sc0 sc1\n"
    "s_mov_b64 exec, -1\n"
    "s_getpc_b64 s[84:85]\n"
    ".LgP_PC2_%=:\n"
    "s_and_b32 s85, s85, 0xffff\n"
    "s_mov_b32 s86, .LgP_FIN_%=-.LgP_PC2_%=\n"
    "s_mov_b32 s87, 0x20000\n"
    "v_mbcnt_lo_u32_b32 v42, -1, 0\n"
    "v_mbcnt_hi_u32_b32 v42, -1, v42\n"
    "v_lshlrev_b32 v42, 7, v42\n"
    "buffer_load_dword v43, v42, s[84:87], 0 offen\n"
    "v_add_u32 v42, 8192, v42\n"
    "buffer_load_dword v43, v42, s[84:87], 0 offen\n"
    "v_add_u32 v42, 8192, v42\n"
    "buffer_load_dword v43, v42, s[84:87], 0 offen\n"
    "ds_read_b64_tr_b16 v[232:233], v17 offset:32768\n"
    "ds_read_b64_tr_b16 v[234:235], v17 offset:33280\n"
    "ds_read_b64_tr_b16 v[236:237], v17 offset:32776\n"
    "ds_read_b64_tr_b16 v[238:239], v17 offset:33288\n"
    "ds_read_b64_tr_b16 v[240:241], v17 offset:40960\n"
    "ds_read_b64_tr_b16 v[242:243], v17 offset:41472\n"
    "ds_read_b64_tr_b16 v[244:245], v17 offset:40968\n"
    "ds_read_b64_tr_b16 v[246:247], v17 offset:41480\n"
    "ds_read_b64_tr_b16 v[248:249], v17 offset:24576\n"
    "ds_read_b64_tr_b16 v[250:251], v17 offset:25088\n"
    "ds_read_b64_tr_b16 v[252:253], v17 offset:24584\n"
    "ds_read_b64_tr_b16 v[254:255], v17 offset:25096\n"
    "s_waitcnt lgkmcnt(0)\n"
    "s_barrier\n"
    "s_waitcnt vmcnt(0)\n"
    "v_mov_b32 v5, v11\n"
    "v_mov_b32 v12, -1\n"
    "s_branch .LgP_block1_%=\n"
    ".LgP_LOOPB_%=:\n"
    ".LgP_head_%=:\n"
    "v_mfma_f32_16x16x32_bf16 v[48:51], v[16:19], a[8:11], 0\n"
    "s_waitcnt lgkmcnt(2)\n"
    "v_mfma_f32_16x16x32_bf16 v[56:59], v[16:19], a[12:15], 0\n"
    "ds_read_b128 v[16:19], v9 offset:4096\n"
    "v_cvt_pk_bf16_f32 v40, v40, v41\n"
    "v_mfma_f32_16x16x32_bf16 v[52:55], v[20:23], a[8:11], 0\n"
    "s_waitcnt lgkmcnt(2)\n"
    "v_cvt_pk_bf16_f32 v41, v42, v43\n"
    "v_cvt_pk_bf16_f32 v42, v44, v45\n"
    "v_mfma_f32_16x16x32_bf16 v[60:63], v[20:23], a[12:15], 0\n"
    "ds_read_b128 v[20:23], v9 offset:5120\n"
    "v_cvt_pk_bf16_f32 v43, v46, v47\n"
    "v_mfma_f32_16x16x32_bf16 v[48:51], v[24:27], a[32:35], v[48:51]\n"
    "s_waitcnt lgkmcnt(2)\n"
    "buffer_store_dwordx4 v[40:43], v1, s[36:39], s79 offen offset:1024 sc1\n"
    "v_cvt_pk_bf16_f32 v72, v72, v73\n"
    "v_mfma_f32_16x16x32_bf16 v[56:59], v[24:27], a[36:39], v[56:59]\n"
    "ds_read_b128 v[24:27], v9 offset:0\n"
    "v_cvt_pk_bf16_f32 v73, v74, v75\n"
    "v_mfma_f32_16x16x32_bf16 v[52:55], v[28:31], a[32:35], v[52:55]\n"
    "s_waitcnt lgkmcnt(2)\n"
    "v_cvt_pk_bf16_f32 v74, v76, v77\n"
    "v_cvt_pk_bf16_f32 v75, v78, v79\n"
    "v_mfma_f32_16x16x32_bf16 v[60:63], v[28:31], a[36:39], v[60:63]\n"
    "ds_read_b128 v[28:31], v9 offset:2048\n"
    "v_mfma_f32_16x16x32_bf16 v[48:51], v[16:19], a[56:59], v[48:51]\n"
    "s_waitcnt lgkmcnt(2)\n"
    "v_mfma_f32_16x16x32_bf16 v[56:59], v[16:19], a[60:63], v[56:59]\n"
    "ds_read_b128 v[16:19], v9 offset:4096\n"
    "v_mfma_f32_16x16x32_bf16 v[52:55], v[20:23], a[56:59], v[52:55]\n"
    "s_waitcnt lgkmcnt(2)\n"
    "buffer_store_dwordx4 v[72:75], v1, s[36:39], s73 offen offset:1024 sc1\n"
    "v_mfma_f32_16x16x32_bf16 v[60:63], v[20:23], a[60:63], v[60:63]\n"
    "ds_read_b128 v[20:23], v9 offset:1024\n"
    "v_mfma_f32_16x16x32_bf16 v[32:35], a[0:3], v[24:27], 0\n"
    "v_add_u32 v13, v11, v3\n"
    "v_mfma_f32_16x16x32_bf16 v[36:39], a[4:7], v[24:27], 0\n"
    "v_cmp_gt_i32 vcc, s71, v13\n"
    "v_mfma_f32_16x16x32_bf16 v[64:67], a[16:19], v[24:27], 0\n"
    "s_waitcnt lgkmcnt(2)\n"
    "s_and_b32 s76, s71, s54\n"
    "v_mfma_f32_16x16x32_bf16 v[68:71], a[20:23], v[24:27], 0\n"
    "ds_read_b128 v[24:27], v9 offset:3072\n"
    "v_mfma_f32_16x16x32_bf16 v[32:35], a[24:27], v[28:31], v[32:35]\n"
    "s_or_b64 exec, vcc, 1\n"
    "buffer_atomic_smax v11, v2, s[36:39], 0 offen sc0 sc1\n"
    "s_mov_b64 exec, -1\n"
    "v_mfma_f32_16x16x32_bf16 v[36:39], a[28:31], v[28:31], v[36:39]\n"
    "s_mul_i32 s76, s76, s53\n"
    "v_mfma_f32_16x16x32_bf16 v[64:67], a[40:43], v[28:31], v[64:67]\n"
    "s_waitcnt lgkmcnt(2)\n"
    "s_add_u32 s76, s76, s52\n"
    "v_mfma_f32_16x16x32_bf16 v[68:71], a[44:47], v[28:31], v[68:71]\n"
    "ds_read_b128 v[28:31], v9 offset:5120\n"
    "v_mfma_f32_16x16x32_bf16 v[32:35], a[48:51], v[16:19], v[32:35]\n"
    "s_and_b32 s77, s71, 1\n"
    "v_mfma_f32_16x16x32_bf16 v[36:39], a[52:55], v[16:19], v[36:39]\n"
    "s_mul_i32 s77, s77, 65536\n"
    "v_mfma_f32_16x16x32_bf16 v[64:67], a[64:67], v[16:19], v[64:67]\n"
    "s_waitcnt lgkmcnt(2)\n"
    "s_add_u32 s77, s77, s59\n"
    "v_mfma_f32_16x16x32_bf16 v[68:71], a[68:71], v[16:19], v[68:71]\n"
    "ds_read_b128 v[16:19], v9 offset:6144\n"
    "v_mfma_f32_16x16x32_bf16 v[40:43], a[0:3], v[20:23], 0\n"
    "s_and_b32 s73, s68, s57\n"
    "v_mfma_f32_16x16x32_bf16 v[44:47], a[4:7], v[20:23], 0\n"
    "s_mul_i32 s73, s73, s56\n"
    "v_mfma_f32_16x16x32_bf16 v[72:75], a[16:19], v[20:23], 0\n"
    "s_waitcnt lgkmcnt(2)\n"
    "s_add_u32 s73, s73, s55\n"
    "v_mfma_f32_16x16x32_bf16 v[76:79], a[20:23], v[20:23], 0\n"
    "ds_read_b128 v[20:23], v9 offset:7168\n"
    "v_mfma_f32_16x16x32_bf16 v[40:43], a[24:27], v[24:27], v[40:43]\n"
    "s_add_u32 s79, s73, 16384\n"
    "v_mfma_f32_16x16x32_bf16 v[44:47], a[28:31], v[24:27], v[44:47]\n"
    "s_add_u32 s80, s73, 32768\n"
    "v_mfma_f32_16x16x32_bf16 v[72:75], a[40:43], v[24:27], v[72:75]\n"
    "s_waitcnt lgkmcnt(2)\n"
    "v_mfma_f32_16x16x32_bf16 v[76:79], a[44:47], v[24:27], v[76:79]\n"
    "ds_read_b128 v[24:27], v9 offset:8192\n"
    "v_mfma_f32_16x16x32_bf16 v[40:43], a[48:51], v[28:31], v[40:43]\n"
    "v_mfma_f32_16x16x32_bf16 v[44:47], a[52:55], v[28:31], v[44:47]\n"
    "v_add_u32 v13, v5, v3\n"
    "v_mfma_f32_16x16x32_bf16 v[72:75], a[64:67], v[28:31], v[72:75]\n"
    "s_waitcnt lgkmcnt(2)\n"
    "v_cmp_gt_i32 vcc, s71, v13\n"
    "v_mfma_f32_16x16x32_bf16 v[76:79], a[68:71], v[28:31], v[76:79]\n"
    "ds_read_b128 v[28:31], v9 offset:9216\n"
    "v_mfma_f32_16x16x32_bf16 v[32:35], a[72:75], v[16:19], v[32:35]\n"
    "s_or_b64 exec, vcc, 1\n"
    "buffer_atomic_smax v5, v2, s[36:39], 0 offen sc0 sc1\n"
    "s_mov_b64 exec, -1\n"
    "v_mfma_f32_16x16x32_bf16 v[36:39], a[76:79], v[16:19], v[36:39]\n"
    "v_mfma_f32_16x16x32_bf16 v[48:51], v[16:19], a[80:83], v[48:51]\n"
    "v_mfma_f32_16x16x32_bf16 v[56:59], v[16:19], a[84:87], v[56:59]\n"
    "v_mfma_f32_16x16x32_bf16 v[64:67], a[88:91], v[16:19], v[64:67]\n"
    "s_waitcnt lgkmcnt(2)\n"
    "v_mfma_f32_16x16x32_bf16 v[68:71], a[92:95], v[16:19], v[68:71]\n"
    "ds_read_b128 v[16:19], v9 offset:10240\n"
    "v_mfma_f32_16x16x32_bf16 v[40:43], a[72:75], v[20:23], v[40:43]\n"
    "v_mfma_f32_16x16x32_bf16 v[44:47], a[76:79], v[20:23], v[44:47]\n"
    "v_mfma_f32_16x16x32_bf16 v[52:55], v[20:23], a[80:83], v[52:55]\n"
    "v_mfma_f32_16x16x32_bf16 v[60:63], v[20:23], a[84:87], v[60:63]\n"
    "v_mfma_f32_16x16x32_bf16 v[72:75], a[88:91], v[20:23], v[72:75]\n"
    "s_waitcnt lgkmcnt(2)\n"
    "v_mfma_f32_16x16x32_bf16 v[76:79], a[92:95], v[20:23], v[76:79]\n"
    "ds_read_b128 v[20:23], v9 offset:11264\n"
    "v_mfma_f32_16x16x32_bf16 v[32:35], a[96:99], v[24:27], v[32:35]\n"
    "v_mfma_f32_16x16x32_bf16 v[36:39], a[100:103], v[24:27], v[36:39]\n"
    "v_mfma_f32_16x16x32_bf16 v[48:51], v[24:27], a[104:107], v[48:51]\n"
    "v_mfma_f32_16x16x32_bf16 v[56:59], v[24:27], a[108:111], v[56:59]\n"
    "s_waitcnt vmcnt(1)\n"
    "v_add_u32 v13, v11, v3\n"
    "v_mfma_f32_16x16x32_bf16 v[64:67], a[112:115], v[24:27], v[64:67]\n"
    "s_waitcnt lgkmcnt(2)\n"
    "buffer_atomic_smax v12, v14, s[36:39], 0 offen sc1\n"
    "v_cmp_gt_i32 vcc, s71, v13\n"
    "v_mfma_f32_16x16x32_bf16 v[68:71], a[116:119], v[24:27], v[68:71]\n"
    "ds_read_b128 v[24:27], v9 offset:12288\n"
    "v_mfma_f32_16x16x32_bf16 v[40:43], a[96:99], v[28:31], v[40:43]\n"
    "v_mfma_f32_16x16x32_bf16 v[44:47], a[100:103], v[28:31], v[44:47]\n"
    ".LgP_mid_%=:\n"
    "s_cbranch_vccnz .LgP_poll0_%=\n"
    "v_mfma_f32_16x16x32_bf16 v[52:55], v[28:31], a[104:107], v[52:55]\n"
    "s_add_u32 m0, s77, s59\n"
    "s_add_u32 s77, s77, 8192\n"
    "buffer_load_dwordx4 v1, s[36:39], s76 offen sc1 lds\n"
    "v_mfma_f32_16x16x32_bf16 v[60:63], v[28:31], a[108:111], v[60:63]\n"
    "v_mfma_f32_16x16x32_bf16 v[72:75], a[112:115], v[28:31], v[72:75]\n"
    "s_waitcnt lgkmcnt(2)\n"
    "v_mfma_f32_16x16x32_bf16 v[76:79], a[116:119], v[28:31], v[76:79]\n"
    "ds_read_b128 v[28:31], v9 offset:13312\n"
    "v_mfma_f32_16x16x32_bf16 v[32:35], a[120:123], v[16:19], v[32:35]\n"
    "v_mfma_f32_16x16x32_bf16 v[36:39], a[124:127], v[16:19], v[36:39]\n"
    "v_mfma_f32_16x16x32_bf16 v[48:51], v[16:19], a[128:131], v[48:51]\n"
    "buffer_load_dwordx4 v1, s[36:39], s76 offen offset:1024 sc1 lds\n"
    "s_add_u32 s76, s76, 16384\n"
    "v_mfma_f32_16x16x32_bf16 v[56:59], v[16:19], a[132:135], v[56:59]\n"
    "v_mfma_f32_16x16x32_bf16 v[64:67], a[136:139], v[16:19], v[64:67]\n"
    "s_waitcnt lgkmcnt(2)\n"
    "v_mfma_f32_16x16x32_bf16 v[68:71], a[140:143], v[16:19], v[68:71]\n"
    "ds_read_b128 v[16:19], v9 offset:14336\n"
    "v_mfma_f32_16x16x32_bf16 v[40:43], a[120:123], v[20:23], v[40:43]\n"
    "v_mfma_f32_16x16x32_bf16 v[44:47], a[124:127], v[20:23], v[44:47]\n"
    "v_mfma_f32_16x16x32_bf16 v[52:55], v[20:23], a[128:131], v[52:55]\n"
    "s_add_u32 m0, s77, s59\n"
    "s_add_u32 s77, s77, 8192\n"
    "buffer_load_dwordx4 v1, s[36:39], s76 offen sc1 lds\n"
    "v_mfma_f32_16x16x32_bf16 v[60:63], v[20:23], a[132:135], v[60:63]\n"
    "v_mfma_f32_16x16x32_bf16 v[72:75], a[136:139], v[20:23], v[72:75]\n"
    "s_waitcnt lgkmcnt(2)\n"
    "v_mfma_f32_16x16x32_bf16 v[76:79], a[140:143], v[20:23], v[76:79]\n"
    "ds_read_b128 v[20:23], v9 offset:15360\n"
    "v_mfma_f32_16x16x32_bf16 v[32:35], a[144:147], v[24:27], v[32:35]\n"
    "v_mfma_f32_16x16x32_bf16 v[36:39], a[148:151], v[24:27], v[36:39]\n"
    "v_mfma_f32_16x16x32_bf16 v[48:51], v[24:27], a[152:155], v[48:51]\n"
    "v_mfma_f32_16x16x32_bf16 v[56:59], v[24:27], a[156:159], v[56:59]\n"
    "buffer_load_dwordx4 v1, s[36:39], s76 offen offset:1024 sc1 lds\n"
    "s_add_u32 s76, s76, 16384\n"
    "v_mfma_f32_16x16x32_bf16 v[64:67], a[160:163], v[24:27], v[64:67]\n"
    "s_waitcnt lgkmcnt(2)\n"
    "v_mfma_f32_16x16x32_bf16 v[68:71], a[164:167], v[24:27], v[68:71]\n"
    "ds_read_b128 v[24:27], v9 offset:16384\n"
    "v_mfma_f32_16x16x32_bf16 v[40:43], a[144:147], v[28:31], v[40:43]\n"
    "v_mfma_f32_16x16x32_bf16 v[44:47], a[148:151], v[28:31], v[44:47]\n"
    "v_mfma_f32_16x16x32_bf16 v[52:55], v[28:31], a[152:155], v[52:55]\n"
    "v_mfma_f32_16x16x32_bf16 v[60:63], v[28:31], a[156:159], v[60:63]\n"
    "s_add_u32 m0, s77, s59\n"
    "s_add_u32 s77, s77, 8192\n"
    "buffer_load_dwordx4 v1, s[36:39], s76 offen sc1 lds\n"
    "v_mfma_f32_16x16x32_bf16 v[72:75], a[160:163], v[28:31], v[72:75]\n"
    "s_waitcnt lgkmcnt(2)\n"
    "v_mfma_f32_16x16x32_bf16 v[76:79], a[164:167], v[28:31], v[76:79]\n"
    "ds_read_b128 v[28:31], v9 offset:17408\n"
    "v_mfma_f32_16x16x32_bf16 v[32:35], a[168:171], v[16:19], v[32:35]\n"
    "v_mfma_f32_16x16x32_bf16 v[36:39], a[172:175], v[16:19], v[36:39]\n"
    "v_mfma_f32_16x16x32_bf16 v[48:51], v[16:19], a[176:179], v[48:51]\n"
    "v_mfma_f32_16x16x32_bf16 v[56:59], v[16:19], a[180:183], v[56:59]\n"
    "v_mfma_f32_16x16x32_bf16 v[64:67], a[184:187], v[16:19], v[64:67]\n"
    "s_waitcnt lgkmcnt(2)\n"
    "buffer_load_dwordx4 v1, s[36:39], s76 offen offset:1024 sc1 lds\n"
    "s_add_u32 s76, s76, 16384\n"
    "v_mfma_f32_16x16x32_bf16 v[68:71], a[188:191], v[16:19], v[68:71]\n"
    "ds_read_b128 v[16:19], v9 offset:18432\n"
    "v_mfma_f32_16x16x32_bf16 v[40:43], a[168:171], v[20:23], v[40:43]\n"
    "v_mfma_f32_16x16x32_bf16 v[44:47], a[172:175], v[20:23], v[44:47]\n"
    "v_mfma_f32_16x16x32_bf16 v[52:55], v[20:23], a[176:179], v[52:55]\n"
    "v_mfma_f32_16x16x32_bf16 v[60:63], v[20:23], a[180:183], v[60:63]\n"
    "v_mfma_f32_16x16x32_bf16 v[72:75], a[184:187], v[20:23], v[72:75]\n"
    "s_waitcnt lgkmcnt(2)\n"
    "s_add_u32 m0, s77, s59\n"
    "s_add_u32 s77, s77, 8192\n"
    "buffer_load_dwordx4 v1, s[36:39], s76 offen sc1 lds\n"
    "v_mfma_f32_16x16x32_bf16 v[76:79], a[188:191], v[20:23], v[76:79]\n"
    "ds_read_b128 v[20:23], v9 offset:19456\n"
    "v_mfma_f32_16x16x32_bf16 v[32:35], a[192:195], v[24:27], v[32:35]\n"
    "v_mfma_f32_16x16x32_bf16 v[36:39], a[196:199], v[24:27], v[36:39]\n"
    "v_mfma_f32_16x16x32_bf16 v[48:51], v[24:27], a[200:203], v[48:51]\n"
    "v_mfma_f32_16x16x32_bf16 v[56:59], v[24:27], a[204:207], v[56:59]\n"
    "v_mfma_f32_16x16x32_bf16 v[64:67], a[208:211], v[24:27], v[64:67]\n"
    "s_waitcnt lgkmcnt(2)\n"
    "v_mfma_f32_16x16x32_bf16 v[68:71], a[212:215], v[24:27], v[68:71]\n"
    "ds_read_b128 v[24:27], v9 offset:20480\n"
    "v_mfma_f32_16x16x32_bf16 v[40:43], a[192:195], v[28:31], v[40:43]\n"
    "buffer_load_dwordx4 v1, s[36:39], s76 offen offset:1024 sc1 lds\n"
    "s_add_u32 s76, s76, 16384\n"
    "v_mfma_f32_16x16x32_bf16 v[44:47], a[196:199], v[28:31], v[44:47]\n"
    "v_mfma_f32_16x16x32_bf16 v[52:55], v[28:31], a[200:203], v[52:55]\n"
    "v_mfma_f32_16x16x32_bf16 v[60:63], v[28:31], a[204:207], v[60:63]\n"
    "v_mfma_f32_16x16x32_bf16 v[72:75], a[208:211], v[28:31], v[72:75]\n"
    "s_waitcnt lgkmcnt(2)\n"
    "v_mfma_f32_16x16x32_bf16 v[76:79], a[212:215], v[28:31], v[76:79]\n"
    "ds_read_b128 v[28:31], v9 offset:21504\n"
    "v_mfma_f32_16x16x32_bf16 v[32:35], a[216:219], v[16:19], v[32:35]\n"
    "s_mov_b32 m0, s77\n"
    "s_nop 0\n"
    "buffer_load_dwordx4 v8, s[36:39], s76 offen sc1 lds\n"
    "v_mfma_f32_16x16x32_bf16 v[36:39], a[220:223], v[16:19], v[36:39]\n"
    "v_mfma_f32_16x16x32_bf16 v[48:51], v[16:19], a[224:227], v[48:51]\n"
    "v_mfma_f32_16x16x32_bf16 v[56:59], v[16:19], a[228:231], v[56:59]\n"
    "v_mfma_f32_16x16x32_bf16 v[64:67], a[232:235], v[16:19], v[64:67]\n"
    "s_waitcnt lgkmcnt(2)\n"
    "v_mfma_f32_16x16x32_bf16 v[68:71], a[236:239], v[16:19], v[68:71]\n"
    "ds_read_b128 v[16:19], v9 offset:22528\n"
    "v_mfma_f32_16x16x32_bf16 v[40:43], a[216:219], v[20:23], v[40:43]\n"
    "v_mfma_f32_16x16x32_bf16 v[44:47], a[220:223], v[20:23], v[44:47]\n"
    ".LgP_q116_%=:\n"
    "v_mfma_f32_16x16x32_bf16 v[52:55], v[20:23], a[224:227], v[52:55]\n"
    "v_mfma_f32_16x16x32_bf16 v[60:63], v[20:23], a[228:231], v[60:63]\n"
    "v_mfma_f32_16x16x32_bf16 v[72:75], a[232:235], v[20:23], v[72:75]\n"
    "s_waitcnt lgkmcnt(2)\n"
    "v_mfma_f32_16x16x32_bf16 v[76:79], a[236:239], v[20:23], v[76:79]\n"
    "ds_read_b128 v[20:23], v9 offset:23552\n"
    "v_mfma_f32_16x16x32_bf16 v[32:35], a[240:243], v[24:27], v[32:35]\n"
    "v_mfma_f32_16x16x32_bf16 v[36:39], a[244:247], v[24:27], v[36:39]\n"
    "v_mfma_f32_16x16x32_bf16 v[48:51], v[24:27], a[248:251], v[48:51]\n"
    "v_mfma_f32_16x16x32_bf16 v[56:59], v[24:27], a[252:255], v[56:59]\n"
    "v_mfma_f32_16x16x32_bf16 v[64:67], v[80:83], v[24:27], v[64:67]\n"
    "s_waitcnt lgkmcnt(2)\n"
    "v_mfma_f32_16x16x32_bf16 v[68:71], v[84:87], v[24:27], v[68:71]\n"
    "ds_read_b128 v[24:27], v9 offset:24576\n"
    "v_mfma_f32_16x16x32_bf16 v[40:43], a[240:243], v[28:31], v[40:43]\n"
    "v_mfma_f32_16x16x32_bf16 v[44:47], a[244:247], v[28:31], v[44:47]\n"
    "v_mfma_f32_16x16x32_bf16 v[52:55], v[28:31], a[248:251], v[52:55]\n"
    "v_mfma_f32_16x16x32_bf16 v[60:63], v[28:31], a[252:255], v[60:63]\n"
    "v_mfma_f32_16x16x32_bf16 v[72:75], v[80:83], v[28:31], v[72:75]\n"
    "s_waitcnt lgkmcnt(2)\n"
    "v_mfma_f32_16x16x32_bf16 v[76:79], v[84:87], v[28:31], v[76:79]\n"
    "ds_read_b128 v[28:31], v9 offset:25600\n"
    "v_mfma_f32_16x16x32_bf16 v[32:35], v[88:91], v[16:19], v[32:35]\n"
    "v_mfma_f32_16x16x32_bf16 v[36:39], v[92:95], v[16:19], v[36:39]\n"
    "v_mfma_f32_16x16x32_bf16 v[48:51], v[16:19], v[96:99], v[48:51]\n"
    "v_mfma_f32_16x16x32_bf16 v[56:59], v[16:19], v[100:103], v[56:59]\n"
    ".LgP_q136_%=:\n"
    "v_mfma_f32_16x16x32_bf16 v[64:67], v[104:107], v[16:19], v[64:67]\n"
    "s_waitcnt lgkmcnt(2)\n"
    "v_mfma_f32_16x16x32_bf16 v[68:71], v[108:111], v[16:19], v[68:71]\n"
    "ds_read_b128 v[16:19], v9 offset:26624\n"
    "v_mfma_f32_16x16x32_bf16 v[40:43], v[88:91], v[20:23], v[40:43]\n"
    "v_mfma_f32_16x16x32_bf16 v[44:47], v[92:95], v[20:23], v[44:47]\n"
    "v_mfma_f32_16x16x32_bf16 v[52:55], v[20:23], v[96:99], v[52:55]\n"
    "v_mfma_f32_16x16x32_bf16 v[60:63], v[20:23], v[100:103], v[60:63]\n"
    "v_mfma_f32_16x16x32_bf16 v[72:75], v[104:107], v[20:23], v[72:75]\n"
    "s_waitcnt lgkmcnt(2)\n"
    "v_mfma_f32_16x16x32_bf16 v[76:79], v[108:111], v[20:23], v[76:79]\n"
    "ds_read_b128 v[20:23], v9 offset:27648\n"
    "v_mfma_f32_16x16x32_bf16 v[32:35], v[112:115], v[24:27], v[32:35]\n"
    "v_mfma_f32_16x16x32_bf16 v[36:39], v[116:119], v[24:27], v[36:39]\n"
    "v_mfma_f32_16x16x32_bf16 v[48:51], v[24:27], v[120:123], v[48:51]\n"
    "v_mfma_f32_16x16x32_bf16 v[56:59], v[24:27], v[124:127], v[56:59]\n"
    "v_mfma_f32_16x16x32_bf16 v[64:67], v[128:131], v[24:27], v[64:67]\n"
    "s_waitcnt lgkmcnt(2)\n"
    "v_mfma_f32_16x16x32_bf16 v[68:71], v[132:135], v[24:27], v[68:71]\n"
    "ds_read_b128 v[24:27], v9 offset:28672\n"
    "v_mfma_f32_16x16x32_bf16 v[40:43], v[112:115], v[28:31], v[40:43]\n"
    "v_mfma_f32_16x16x32_bf16 v[44:47], v[116:119], v[28:31], v[44:47]\n"
    "v_mfma_f32_16x16x32_bf16 v[52:55], v[28:31], v[120:123], v[52:55]\n"
    "v_mfma_f32_16x16x32_bf16 v[60:63], v[28:31], v[124:127], v[60:63]\n"
    "v_mfma_f32_16x16x32_bf16 v[72:75], v[128:131], v[28:31], v[72:75]\n"
    "s_waitcnt lgkmcnt(2)\n"
    "v_mfma_f32_16x16x32_bf16 v[76:79], v[132:135], v[28:31], v[76:79]\n"
    "ds_read_b128 v[28:31], v9 offset:29696\n"
    ".LgP_q156_%=:\n"
    "v_mfma_f32_16x16x32_bf16 v[32:35], v[136:139], v[16:19], v[32:35]\n"
    "v_mov_b32 v12, s68\n"
    "v_mfma_f32_16x16x32_bf16 v[36:39], v[140:143], v[16:19], v[36:39]\n"
    "s_mov_b32 s68, s71\n"
    "v_mfma_f32_16x16x32_bf16 v[48:51], v[16:19], v[144:147], v[48:51]\n"
    "s_add_u32 s71, s68, 1\n"
    "v_mfma_f32_16x16x32_bf16 v[56:59], v[16:19], v[148:151], v[56:59]\n"
    "v_mfma_f32_16x16x32_bf16 v[64:67], v[152:155], v[16:19], v[64:67]\n"
    "s_waitcnt lgkmcnt(2)\n"
    "v_mfma_f32_16x16x32_bf16 v[68:71], v[156:159], v[16:19], v[68:71]\n"
    "ds_read_b128 v[16:19], v9 offset:30720\n"
    "v_mfma_f32_16x16x32_bf16 v[40:43], v[136:139], v[20:23], v[40:43]\n"
    "v_mfma_f32_16x16x32_bf16 v[44:47], v[140:143], v[20:23], v[44:47]\n"
    "v_mfma_f32_16x16x32_bf16 v[52:55], v[20:23], v[144:147], v[52:55]\n"
    "v_mfma_f32_16x16x32_bf16 v[60:63], v[20:23], v[148:151], v[60:63]\n"
    "v_mfma_f32_16x16x32_bf16 v[72:75], v[152:155], v[20:23], v[72:75]\n"
    "s_waitcnt lgkmcnt(2)\n"
    "v_mfma_f32_16x16x32_bf16 v[76:79], v[156:159], v[20:23], v[76:79]\n"
    "ds_read_b128 v[20:23], v9 offset:31744\n"
    "v_mfma_f32_16x16x32_bf16 v[32:35], v[160:163], v[24:27], v[32:35]\n"
    "v_mfma_f32_16x16x32_bf16 v[36:39], v[164:167], v[24:27], v[36:39]\n"
    "v_mfma_f32_16x16x32_bf16 v[48:51], v[24:27], v[168:171], v[48:51]\n"
    "v_mfma_f32_16x16x32_bf16 v[56:59], v[24:27], v[172:175], v[56:59]\n"
    "v_mfma_f32_16x16x32_bf16 v[64:67], v[176:179], v[24:27], v[64:67]\n"
    "s_waitcnt lgkmcnt(2)\n"
    "v_mfma_f32_16x16x32_bf16 v[68:71], v[180:183], v[24:27], v[68:71]\n"
    "ds_read_b128 v[24:27], v9 offset:32768\n"
    "v_mfma_f32_16x16x32_bf16 v[40:43], v[160:163], v[28:31], v[40:43]\n"
    "v_mfma_f32_16x16x32_bf16 v[44:47], v[164:167], v[28:31], v[44:47]\n"
    "v_mfma_f32_16x16x32_bf16 v[52:55], v[28:31], v[168:171], v[52:55]\n"
    "v_mfma_f32_16x16x32_bf16 v[60:63], v[28:31], v[172:175], v[60:63]\n"
    "v_mfma_f32_16x16x32_bf16 v[72:75], v[176:179], v[28:31], v[72:75]\n"
    "s_waitcnt lgkmcnt(2)\n"
    "v_mfma_f32_16x16x32_bf16 v[76:79], v[180:183], v[28:31], v[76:79]\n"
    "ds_read_b128 v[28:31], v9 offset:33792\n"
    "v_mfma_f32_16x16x32_bf16 v[48:51], v[16:19], v[192:195], v[48:51]\n"
    "s_waitcnt lgkmcnt(2)\n"
    "v_mfma_f32_16x16x32_bf16 v[56:59], v[16:19], v[196:199], v[56:59]\n"
    "ds_read_b128 v[16:19], v9 offset:34816\n"
    "v_mfma_f32_16x16x32_bf16 v[52:55], v[20:23], v[192:195], v[52:55]\n"
    "s_waitcnt lgkmcnt(2)\n"
    "v_mfma_f32_16x16x32_bf16 v[60:63], v[20:23], v[196:199], v[60:63]\n"
    "ds_read_b128 v[20:23], v9 offset:35840\n"
    "v_mfma_f32_16x16x32_bf16 v[48:51], v[24:27], v[216:219], v[48:51]\n"
    "s_waitcnt lgkmcnt(2)\n"
    "v_mfma_f32_16x16x32_bf16 v[56:59], v[24:27], v[220:223], v[56:59]\n"
    "ds_read_b128 v[24:27], v9 offset:30720\n"
    "v_mfma_f32_16x16x32_bf16 v[52:55], v[28:31], v[216:219], v[52:55]\n"
    "s_waitcnt lgkmcnt(2)\n"
    "v_mfma_f32_16x16x32_bf16 v[60:63], v[28:31], v[220:223], v[60:63]\n"
    "ds_read_b128 v[28:31], v9 offset:32768\n"
    "v_mfma_f32_16x16x32_bf16 v[48:51], v[16:19], v[240:243], v[48:51]\n"
    "s_waitcnt lgkmcnt(2)\n"
    "v_mfma_f32_16x16x32_bf16 v[56:59], v[16:19], v[244:247], v[56:59]\n"
    "ds_read_b128 v[16:19], v9 offset:34816\n"
    "v_mfma_f32_16x16x32_bf16 v[52:55], v[20:23], v[240:243], v[52:55]\n"
    "s_waitcnt lgkmcnt(2)\n"
    "v_mfma_f32_16x16x32_bf16 v[60:63], v[20:23], v[244:247], v[60:63]\n"
    "ds_read_b128 v[20:23], v9 offset:31744\n"
    "v_mfma_f32_16x16x32_bf16 v[32:35], v[184:187], v[24:27], v[32:35]\n"
    "v_mfma_f32_16x16x32_bf16 v[36:39], v[188:191], v[24:27], v[36:39]\n"
    "v_cvt_pk_bf16_f32 v48, v48, v49\n"
    "v_cvt_pk_bf16_f32 v49, v50, v51\n"
    "v_mfma_f32_16x16x32_bf16 v[64:67], v[200:203], v[24:27], v[64:67]\n"
    "s_waitcnt lgkmcnt(2)\n"
    "v_cvt_pk_bf16_f32 v50, v52, v53\n"
    "v_cvt_pk_bf16_f32 v51, v54, v55\n"
    "v_mfma_f32_16x16x32_bf16 v[68:71], v[204:207], v[24:27], v[68:71]\n"
    "ds_read_b128 v[24:27], v9 offset:33792\n"
    "v_cvt_pk_bf16_f32 v56, v56, v57\n"
    "v_mfma_f32_16x16x32_bf16 v[32:35], v[208:211], v[28:31], v[32:35]\n"
    "buffer_store_dwordx4 v[48:51], v1, s[36:39], s80 offen sc1\n"
    "v_cvt_pk_bf16_f32 v57, v58, v59\n"
    "v_mfma_f32_16x16x32_bf16 v[36:39], v[212:215], v[28:31], v[36:39]\n"
    "v_cvt_pk_bf16_f32 v58, v60, v61\n"
    "v_cvt_pk_bf16_f32 v59, v62, v63\n"
    "v_mfma_f32_16x16x32_bf16 v[64:67], v[224:227], v[28:31], v[64:67]\n"
    "s_waitcnt lgkmcnt(2)\n"
    "v_mfma_f32_16x16x32_bf16 v[68:71], v[228:231], v[28:31], v[68:71]\n"
    "ds_read_b128 v[28:31], v9 offset:35840\n"
    "v_mfma_f32_16x16x32_bf16 v[32:35], v[232:235], v[16:19], v[32:35]\n"
    "v_mfma_f32_16x16x32_bf16 v[36:39], v[236:239], v[16:19], v[36:39]\n"
    "buffer_store_dwordx4 v[56:59], v1, s[36:39], s80 offen offset:1024 sc1\n"
    "v_mfma_f32_16x16x32_bf16 v[64:67], v[248:251], v[16:19], v[64:67]\n"
    "s_waitcnt lgkmcnt(2)\n"
    "v_xor_b32 v9, s78, v9\n"
    "v_mfma_f32_16x16x32_bf16 v[68:71], v[252:255], v[16:19], v[68:71]\n"
    "s_waitcnt vmcnt(2) lgkmcnt(0)\n"
    "s_barrier\n"
    "ds_read_b128 v[16:19], v9 offset:0\n"
    "v_mfma_f32_16x16x32_bf16 v[40:43], v[184:187], v[20:23], v[40:43]\n"
    "v_mfma_f32_16x16x32_bf16 v[44:47], v[188:191], v[20:23], v[44:47]\n"
    "v_cvt_pk_bf16_f32 v32, v32, v33\n"
    "v_cvt_pk_bf16_f32 v33, v34, v35\n"
    "v_mfma_f32_16x16x32_bf16 v[72:75], v[200:203], v[20:23], v[72:75]\n"
    "v_cvt_pk_bf16_f32 v34, v36, v37\n"
    "v_cvt_pk_bf16_f32 v35, v38, v39\n"
    "v_mfma_f32_16x16x32_bf16 v[76:79], v[204:207], v[20:23], v[76:79]\n"
    "ds_read_b128 v[20:23], v9 offset:1024\n"
    "v_cvt_pk_bf16_f32 v64, v64, v65\n"
    "v_mfma_f32_16x16x32_bf16 v[40:43], v[208:211], v[24:27], v[40:43]\n"
    "buffer_store_dwordx4 v[32:35], v1, s[36:39], s79 offen sc1\n"
    "v_cvt_pk_bf16_f32 v65, v66, v67\n"
    "v_mfma_f32_16x16x32_bf16 v[44:47], v[212:215], v[24:27], v[44:47]\n"
    "v_cvt_pk_bf16_f32 v66, v68, v69\n"
    "v_cvt_pk_bf16_f32 v67, v70, v71\n"
    "v_mfma_f32_16x16x32_bf16 v[72:75], v[224:227], v[24:27], v[72:75]\n"
    "v_mfma_f32_16x16x32_bf16 v[76:79], v[228:231], v[24:27], v[76:79]\n"
    "ds_read_b128 v[24:27], v9 offset:2048\n"
    "v_mfma_f32_16x16x32_bf16 v[40:43], v[232:235], v[28:31], v[40:43]\n"
    "v_mfma_f32_16x16x32_bf16 v[44:47], v[236:239], v[28:31], v[44:47]\n"
    "buffer_store_dwordx4 v[64:67], v1, s[36:39], s73 offen sc1\n"
    "v_mfma_f32_16x16x32_bf16 v[72:75], v[248:251], v[28:31], v[72:75]\n"
    "s_waitcnt lgkmcnt(2)\n"
    "v_mfma_f32_16x16x32_bf16 v[76:79], v[252:255], v[28:31], v[76:79]\n"
    "ds_read_b128 v[28:31], v9 offset:3072\n"
    "s_branch .LgP_head_%=\n"
    ".LgP_LOOPE_%=:\n"
    ".LgP_done_%=:\n"
    "s_waitcnt vmcnt(0)\n"
    "buffer_atomic_smax v12, v14, s[36:39], 0 offen sc1\n"
    "s_waitcnt vmcnt(0)\n"
    "s_branch .LgP_FIN_%=\n"
    ".LgP_poll0_%=:\n"
    "v_mfma_f32_16x16x32_bf16 v[52:55], v[28:31], a[104:107], v[52:55]\n"
    "s_or_b64 exec, vcc, 1\n"
    "buffer_atomic_smax v11, v2, s[36:39], 0 offen sc0 sc1\n"
    "s_mov_b64 exec, -1\n"
    "v_mfma_f32_16x16x32_bf16 v[60:63], v[28:31], a[108:111], v[60:63]\n"
    "v_mfma_f32_16x16x32_bf16 v[72:75], a[112:115], v[28:31], v[72:75]\n"
    "s_waitcnt lgkmcnt(2)\n"
    "v_mfma_f32_16x16x32_bf16 v[76:79], a[116:119], v[28:31], v[76:79]\n"
    "ds_read_b128 v[28:31], v9 offset:13312\n"
    "v_mfma_f32_16x16x32_bf16 v[32:35], a[120:123], v[16:19], v[32:35]\n"
    "v_mfma_f32_16x16x32_bf16 v[36:39], a[124:127], v[16:19], v[36:39]\n"
    "v_mfma_f32_16x16x32_bf16 v[48:51], v[16:19], a[128:131], v[48:51]\n"
    "v_mfma_f32_16x16x32_bf16 v[56:59], v[16:19], a[132:135], v[56:59]\n"
    "v_mfma_f32_16x16x32_bf16 v[64:67], a[136:139], v[16:19], v[64:67]\n"
    "s_waitcnt lgkmcnt(2)\n"
    "v_mfma_f32_16x16x32_bf16 v[68:71], a[140:143], v[16:19], v[68:71]\n"
    "ds_read_b128 v[16:19], v9 offset:14336\n"
    "v_mfma_f32_16x16x32_bf16 v[40:43], a[120:123], v[20:23], v[40:43]\n"
    "v_mfma_f32_16x16x32_bf16 v[44:47], a[124:127], v[20:23], v[44:47]\n"
    "v_mfma_f32_16x16x32_bf16 v[52:55], v[20:23], a[128:131], v[52:55]\n"
    "v_mfma_f32_16x16x32_bf16 v[60:63], v[20:23], a[132:135], v[60:63]\n"
    "v_mfma_f32_16x16x32_bf16 v[72:75], a[136:139], v[20:23], v[72:75]\n"
    "s_waitcnt lgkmcnt(2)\n"
    "v_mfma_f32_16x16x32_bf16 v[76:79], a[140:143], v[20:23], v[76:79]\n"
    "ds_read_b128 v[20:23], v9 offset:15360\n"
    "s_waitcnt vmcnt(2)\n"
    "v_add_u32 v13, v5, v3\n"
    "v_mfma_f32_16x16x32_bf16 v[32:35], a[144:147], v[24:27], v[32:35]\n"
    "v_cmp_gt_i32 vcc, s71, v13\n"
    "v_mfma_f32_16x16x32_bf16 v[36:39], a[148:151], v[24:27], v[36:39]\n"
    "v_mfma_f32_16x16x32_bf16 v[48:51], v[24:27], a[152:155], v[48:51]\n"
    "v_mfma_f32_16x16x32_bf16 v[56:59], v[24:27], a[156:159], v[56:59]\n"
    "s_cbranch_vccz .LgP_dma76_%=\n"
    "v_mfma_f32_16x16x32_bf16 v[64:67], a[160:163], v[24:27], v[64:67]\n"
    "s_waitcnt lgkmcnt(2)\n"
    "s_or_b64 exec, vcc, 1\n"
    "buffer_atomic_smax v5, v2, s[36:39], 0 offen sc0 sc1\n"
    "s_mov_b64 exec, -1\n"
    "v_mfma_f32_16x16x32_bf16 v[68:71], a[164:167], v[24:27], v[68:71]\n"
    "ds_read_b128 v[24:27], v9 offset:16384\n"
    "v_mfma_f32_16x16x32_bf16 v[40:43], a[144:147], v[28:31], v[40:43]\n"
    "v_mfma_f32_16x16x32_bf16 v[44:47], a[148:151], v[28:31], v[44:47]\n"
    "v_mfma_f32_16x16x32_bf16 v[52:55], v[28:31], a[152:155], v[52:55]\n"
    "v_mfma_f32_16x16x32_bf16 v[60:63], v[28:31], a[156:159], v[60:63]\n"
    "v_mfma_f32_16x16x32_bf16 v[72:75], a[160:163], v[28:31], v[72:75]\n"
    "s_waitcnt lgkmcnt(2)\n"
    "v_mfma_f32_16x16x32_bf16 v[76:79], a[164:167], v[28:31], v[76:79]\n"
    "ds_read_b128 v[28:31], v9 offset:17408\n"
    "v_mfma_f32_16x16x32_bf16 v[32:35], a[168:171], v[16:19], v[32:35]\n"
    "v_mfma_f32_16x16x32_bf16 v[36:39], a[172:175], v[16:19], v[36:39]\n"
    "v_mfma_f32_16x16x32_bf16 v[48:51], v[16:19], a[176:179], v[48:51]\n"
    "v_mfma_f32_16x16x32_bf16 v[56:59], v[16:19], a[180:183], v[56:59]\n"
    "v_mfma_f32_16x16x32_bf16 v[64:67], a[184:187], v[16:19], v[64:67]\n"
    "s_waitcnt lgkmcnt(2)\n"
    "v_mfma_f32_16x16x32_bf16 v[68:71], a[188:191], v[16:19], v[68:71]\n"
    "ds_read_b128 v[16:19], v9 offset:18432\n"
    "v_mfma_f32_16x16x32_bf16 v[40:43], a[168:171], v[20:23], v[40:43]\n"
    "v_mfma_f32_16x16x32_bf16 v[44:47], a[172:175], v[20:23], v[44:47]\n"
    "s_waitcnt vmcnt(1)\n"
    "v_add_u32 v13, v11, v3\n"
    "v_mfma_f32_16x16x32_bf16 v[52:55], v[20:23], a[176:179], v[52:55]\n"
    "v_cmp_gt_i32 vcc, s71, v13\n"
    "v_mfma_f32_16x16x32_bf16 v[60:63], v[20:23], a[180:183], v[60:63]\n"
    "v_mfma_f32_16x16x32_bf16 v[72:75], a[184:187], v[20:23], v[72:75]\n"
    "s_waitcnt lgkmcnt(2)\n"
    "v_mfma_f32_16x16x32_bf16 v[76:79], a[188:191], v[20:23], v[76:79]\n"
    "ds_read_b128 v[20:23], v9 offset:19456\n"
    "s_cbranch_vccz .LgP_dma96_%=\n"
    "v_mfma_f32_16x16x32_bf16 v[32:35], a[192:195], v[24:27], v[32:35]\n"
    "s_or_b64 exec, vcc, 1\n"
    "buffer_atomic_smax v11, v2, s[36:39], 0 offen sc0 sc1\n"
    "s_mov_b64 exec, -1\n"
    "v_mfma_f32_16x16x32_bf16 v[36:39], a[196:199], v[24:27], v[36:39]\n"
    "v_mfma_f32_16x16x32_bf16 v[48:51], v[24:27], a[200:203], v[48:51]\n"
    "v_mfma_f32_16x16x32_bf16 v[56:59], v[24:27], a[204:207], v[56:59]\n"
    "v_mfma_f32_16x16x32_bf16 v[64:67], a[208:211], v[24:27], v[64:67]\n"
    "s_waitcnt lgkmcnt(2)\n"
    "v_mfma_f32_16x16x32_bf16 v[68:71], a[212:215], v[24:27], v[68:71]\n"
    "ds_read_b128 v[24:27], v9 offset:20480\n"
    "v_mfma_f32_16x16x32_bf16 v[40:43], a[192:195], v[28:31], v[40:43]\n"
    "v_mfma_f32_16x16x32_bf16 v[44:47], a[196:199], v[28:31], v[44:47]\n"
    "v_mfma_f32_16x16x32_bf16 v[52:55], v[28:31], a[200:203], v[52:55]\n"
    "v_mfma_f32_16x16x32_bf16 v[60:63], v[28:31], a[204:207], v[60:63]\n"
    "v_mfma_f32_16x16x32_bf16 v[72:75], a[208:211], v[28:31], v[72:75]\n"
    "s_waitcnt lgkmcnt(2)\n"
    "v_mfma_f32_16x16x32_bf16 v[76:79], a[212:215], v[28:31], v[76:79]\n"
    "ds_read_b128 v[28:31], v9 offset:21504\n"
    "v_mfma_f32_16x16x32_bf16 v[32:35], a[216:219], v[16:19], v[32:35]\n"
    "v_mfma_f32_16x16x32_bf16 v[36:39], a[220:223], v[16:19], v[36:39]\n"
    "v_mfma_f32_16x16x32_bf16 v[48:51], v[16:19], a[224:227], v[48:51]\n"
    "v_mfma_f32_16x16x32_bf16 v[56:59], v[16:19], a[228:231], v[56:59]\n"
    "s_waitcnt vmcnt(1)\n"
    "v_add_u32 v13, v5, v3\n"
    "v_mfma_f32_16x16x32_bf16 v[64:67], a[232:235], v[16:19], v[64:67]\n"
    "s_waitcnt lgkmcnt(2)\n"
    "v_cmp_gt_i32 vcc, s71, v13\n"
    "v_mfma_f32_16x16x32_bf16 v[68:71], a[236:239], v[16:19], v[68:71]\n"
    "ds_read_b128 v[16:19], v9 offset:22528\n"
    "v_mfma_f32_16x16x32_bf16 v[40:43], a[216:219], v[20:23], v[40:43]\n"
    "v_mfma_f32_16x16x32_bf16 v[44:47], a[220:223], v[20:23], v[44:47]\n"
    "s_cbranch_vccz .LgP_dma116_%=\n"
    "v_mfma_f32_16x16x32_bf16 v[52:55], v[20:23], a[224:227], v[52:55]\n"
    "s_or_b64 exec, vcc, 1\n"
    "buffer_atomic_smax v5, v2, s[36:39], 0 offen sc0 sc1\n"
    "s_mov_b64 exec, -1\n"
    "v_mfma_f32_16x16x32_bf16 v[60:63], v[20:23], a[228:231], v[60:63]\n"
    "v_mfma_f32_16x16x32_bf16 v[72:75], a[232:235], v[20:23], v[72:75]\n"
    "s_waitcnt lgkmcnt(2)\n"
    "v_mfma_f32_16x16x32_bf16 v[76:79], a[236:239], v[20:23], v[76:79]\n"
    "ds_read_b128 v[20:23], v9 offset:23552\n"
    "v_mfma_f32_16x16x32_bf16 v[32:35], a[240:243], v[24:27], v[32:35]\n"
    "v_mfma_f32_16x16x32_bf16 v[36:39], a[244:247], v[24:27], v[36:39]\n"
    "v_mfma_f32_16x16x32_bf16 v[48:51], v[24:27], a[248:251], v[48:51]\n"
    "v_mfma_f32_16x16x32_bf16 v[56:59], v[24:27], a[252:255], v[56:59]\n"
    "v_mfma_f32_16x16x32_bf16 v[64:67], v[80:83], v[24:27], v[64:67]\n"
    "s_waitcnt lgkmcnt(2)\n"
    "v_mfma_f32_16x16x32_bf16 v[68:71], v[84:87], v[24:27], v[68:71]\n"
    "ds_read_b128 v[24:27], v9 offset:24576\n"
    "v_mfma_f32_16x16x32_bf16 v[40:43], a[240:243], v[28:31], v[40:43]\n"
    "v_mfma_f32_16x16x32_bf16 v[44:47], a[244:247], v[28:31], v[44:47]\n"
    "v_mfma_f32_16x16x32_bf16 v[52:55], v[28:31], a[248:251], v[52:55]\n"
    "v_mfma_f32_16x16x32_bf16 v[60:63], v[28:31], a[252:255], v[60:63]\n"
    "v_mfma_f32_16x16x32_bf16 v[72:75], v[80:83], v[28:31], v[72:75]\n"
    "s_waitcnt lgkmcnt(2)\n"
    "v_mfma_f32_16x16x32_bf16 v[76:79], v[84:87], v[28:31], v[76:79]\n"
    "ds_read_b128 v[28:31], v9 offset:25600\n"
    "s_waitcnt vmcnt(1)\n"
    "v_add_u32 v13, v11, v3\n"
    "v_mfma_f32_16x16x32_bf16 v[32:35], v[88:91], v[16:19], v[32:35]\n"
    "v_cmp_gt_i32 vcc, s71, v13\n"
    "v_mfma_f32_16x16x32_bf16 v[36:39], v[92:95], v[16:19], v[36:39]\n"
    "v_mfma_f32_16x16x32_bf16 v[48:51], v[16:19], v[96:99], v[48:51]\n"
    "v_mfma_f32_16x16x32_bf16 v[56:59], v[16:19], v[100:103], v[56:59]\n"
    "s_cbranch_vccz .LgP_dma136_%=\n"
    "v_mfma_f32_16x16x32_bf16 v[64:67], v[104:107], v[16:19], v[64:67]\n"
    "s_waitcnt lgkmcnt(2)\n"
    "s_or_b64 exec, vcc, 1\n"
    "buffer_atomic_smax v11, v2, s[36:39], 0 offen sc0 sc1\n"
    "s_mov_b64 exec, -1\n"
    "v_mfma_f32_16x16x32_bf16 v[68:71], v[108:111], v[16:19], v[68:71]\n"
    "ds_read_b128 v[16:19], v9 offset:26624\n"
    "v_mfma_f32_16x16x32_bf16 v[40:43], v[88:91], v[20:23], v[40:43]\n"
    "v_mfma_f32_16x16x32_bf16 v[44:47], v[92:95], v[20:23], v[44:47]\n"
    "v_mfma_f32_16x16x32_bf16 v[52:55], v[20:23], v[96:99], v[52:55]\n"
    "v_mfma_f32_16x16x32_bf16 v[60:63], v[20:23], v[100:103], v[60:63]\n"
    "v_mfma_f32_16x16x32_bf16 v[72:75], v[104:107], v[20:23], v[72:75]\n"
    "s_waitcnt lgkmcnt(2)\n"
    "v_mfma_f32_16x16x32_bf16 v[76:79], v[108:111], v[20:23], v[76:79]\n"
    "ds_read_b128 v[20:23], v9 offset:27648\n"
    "v_mfma_f32_16x16x32_bf16 v[32:35], v[112:115], v[24:27], v[32:35]\n"
    "v_mfma_f32_16x16x32_bf16 v[36:39], v[116:119], v[24:27], v[36:39]\n"
    "v_mfma_f32_16x16x32_bf16 v[48:51], v[24:27], v[120:123], v[48:51]\n"
    "v_mfma_f32_16x16x32_bf16 v[56:59], v[24:27], v[124:127], v[56:59]\n"
    "v_mfma_f32_16x16x32_bf16 v[64:67], v[128:131], v[24:27], v[64:67]\n"
    "s_waitcnt lgkmcnt(2)\n"
    "v_mfma_f32_16x16x32_bf16 v[68:71], v[132:135], v[24:27], v[68:71]\n"
    "ds_read_b128 v[24:27], v9 offset:28672\n"
    "v_mfma_f32_16x16x32_bf16 v[40:43], v[112:115], v[28:31], v[40:43]\n"
    "v_mfma_f32_16x16x32_bf16 v[44:47], v[116:119], v[28:31], v[44:47]\n"
    "s_waitcnt vmcnt(1)\n"
    "v_add_u32 v13, v5, v3\n"
    "v_mfma_f32_16x16x32_bf16 v[52:55], v[28:31], v[120:123], v[52:55]\n"
    "v_cmp_gt_i32 vcc, s71, v13\n"
    "v_mfma_f32_16x16x32_bf16 v[60:63], v[28:31], v[124:127], v[60:63]\n"
    "v_mfma_f32_16x16x32_bf16 v[72:75], v[128:131], v[28:31], v[72:75]\n"
    "s_waitcnt lgkmcnt(2)\n"
    "v_mfma_f32_16x16x32_bf16 v[76:79], v[132:135], v[28:31], v[76:79]\n"
    "ds_read_b128 v[28:31], v9 offset:29696\n"
    "s_cbranch_vccz .LgP_dma156_%=\n"
    "v_mfma_f32_16x16x32_bf16 v[32:35], v[136:139], v[16:19], v[32:35]\n"
    "s_or_b64 exec, vcc, 1\n"
    "buffer_atomic_smax v5, v2, s[36:39], 0 offen sc0 sc1\n"
    "s_mov_b64 exec, -1\n"
    "v_mfma_f32_16x16x32_bf16 v[36:39], v[140:143], v[16:19], v[36:39]\n"
    "v_mfma_f32_16x16x32_bf16 v[48:51], v[16:19], v[144:147], v[48:51]\n"
    "v_mfma_f32_16x16x32_bf16 v[56:59], v[16:19], v[148:151], v[56:59]\n"
    "v_mfma_f32_16x16x32_bf16 v[64:67], v[152:155], v[16:19], v[64:67]\n"
    "s_waitcnt lgkmcnt(2)\n"
    "v_mfma_f32_16x16x32_bf16 v[68:71], v[156:159], v[16:19], v[68:71]\n"
    "ds_read_b128 v[16:19], v9 offset:30720\n"
    "v_mfma_f32_16x16x32_bf16 v[40:43], v[136:139], v[20:23], v[40:43]\n"
    "v_mfma_f32_16x16x32_bf16 v[44:47], v[140:143], v[20:23], v[44:47]\n"
    "v_mfma_f32_16x16x32_bf16 v[52:55], v[20:23], v[144:147], v[52:55]\n"
    "v_mfma_f32_16x16x32_bf16 v[60:63], v[20:23], v[148:151], v[60:63]\n"
    "v_mfma_f32_16x16x32_bf16 v[72:75], v[152:155], v[20:23], v[72:75]\n"
    "s_waitcnt lgkmcnt(2)\n"
    "v_mfma_f32_16x16x32_bf16 v[76:79], v[156:159], v[20:23], v[76:79]\n"
    "ds_read_b128 v[20:23], v9 offset:31744\n"
    "v_mfma_f32_16x16x32_bf16 v[32:35], v[160:163], v[24:27], v[32:35]\n"
    "v_mfma_f32_16x16x32_bf16 v[36:39], v[164:167], v[24:27], v[36:39]\n"
    "v_mfma_f32_16x16x32_bf16 v[48:51], v[24:27], v[168:171], v[48:51]\n"
    "v_mfma_f32_16x16x32_bf16 v[56:59], v[24:27], v[172:175], v[56:59]\n"
    "s_waitcnt vmcnt(1)\n"
    "v_add_u32 v13, v11, v3\n"
    "v_mfma_f32_16x16x32_bf16 v[64:67], v[176:179], v[24:27], v[64:67]\n"
    "s_waitcnt lgkmcnt(2)\n"
    "v_cmp_gt_i32 vcc, s71, v13\n"
    "v_mfma_f32_16x16x32_bf16 v[68:71], v[180:183], v[24:27], v[68:71]\n"
    "ds_read_b128 v[24:27], v9 offset:32768\n"
    "v_mfma_f32_16x16x32_bf16 v[40:43], v[160:163], v[28:31], v[40:43]\n"
    "v_mfma_f32_16x16x32_bf16 v[44:47], v[164:167], v[28:31], v[44:47]\n"
    "s_cbranch_vccz .LgP_dma176_%=\n"
    "v_mfma_f32_16x16x32_bf16 v[52:55], v[28:31], v[168:171], v[52:55]\n"
    "s_or_b64 exec, vcc, 1\n"
    "buffer_atomic_smax v11, v2, s[36:39], 0 offen sc0 sc1\n"
    "s_mov_b64 exec, -1\n"
    "v_mfma_f32_16x16x32_bf16 v[60:63], v[28:31], v[172:175], v[60:63]\n"
    "v_mfma_f32_16x16x32_bf16 v[72:75], v[176:179], v[28:31], v[72:75]\n"
    "s_waitcnt lgkmcnt(2)\n"
    "v_mfma_f32_16x16x32_bf16 v[76:79], v[180:183], v[28:31], v[76:79]\n"
    "ds_read_b128 v[28:31], v9 offset:33792\n"
    "v_mfma_f32_16x16x32_bf16 v[48:51], v[16:19], v[192:195], v[48:51]\n"
    "s_waitcnt lgkmcnt(2)\n"
    "v_mfma_f32_16x16x32_bf16 v[56:59], v[16:19], v[196:199], v[56:59]\n"
    "ds_read_b128 v[16:19], v9 offset:34816\n"
    "v_mfma_f32_16x16x32_bf16 v[52:55], v[20:23], v[192:195], v[52:55]\n"
    "s_waitcnt lgkmcnt(2)\n"
    "v_mfma_f32_16x16x32_bf16 v[60:63], v[20:23], v[196:199], v[60:63]\n"
    "ds_read_b128 v[20:23], v9 offset:35840\n"
    "v_mfma_f32_16x16x32_bf16 v[48:51], v[24:27], v[216:219], v[48:51]\n"
    "s_waitcnt lgkmcnt(2)\n"
    "v_mfma_f32_16x16x32_bf16 v[56:59], v[24:27], v[220:223], v[56:59]\n"
    "ds_read_b128 v[24:27], v9 offset:30720\n"
    "v_mfma_f32_16x16x32_bf16 v[52:55], v[28:31], v[216:219], v[52:55]\n"
    "s_waitcnt lgkmcnt(2)\n"
    "v_mfma_f32_16x16x32_bf16 v[60:63], v[28:31], v[220:223], v[60:63]\n"
    "ds_read_b128 v[28:31], v9 offset:32768\n"
    "v_mfma_f32_16x16x32_bf16 v[48:51], v[16:19], v[240:243], v[48:51]\n"
    "s_waitcnt lgkmcnt(2)\n"
    "v_mfma_f32_16x16x32_bf16 v[56:59], v[16:19], v[244:247], v[56:59]\n"
    "ds_read_b128 v[16:19], v9 offset:34816\n"
    "v_mfma_f32_16x16x32_bf16 v[52:55], v[20:23], v[240:243], v[52:55]\n"
    "s_waitcnt lgkmcnt(2)\n"
    "v_mfma_f32_16x16x32_bf16 v[60:63], v[20:23], v[244:247], v[60:63]\n"
    "ds_read_b128 v[20:23], v9 offset:31744\n"
    "s_waitcnt vmcnt(1)\n"
    "v_add_u32 v13, v5, v3\n"
    "v_mfma_f32_16x16x32_bf16 v[32:35], v[184:187], v[24:27], v[32:35]\n"
    "v_cmp_gt_i32 vcc, s71, v13\n"
    "v_mfma_f32_16x16x32_bf16 v[36:39], v[188:191], v[24:27], v[36:39]\n"
    "v_cvt_pk_bf16_f32 v48, v48, v49\n"
    "v_cvt_pk_bf16_f32 v49, v50, v51\n"
    "v_mfma_f32_16x16x32_bf16 v[64:67], v[200:203], v[24:27], v[64:67]\n"
    "s_waitcnt lgkmcnt(2)\n"
    "v_cvt_pk_bf16_f32 v50, v52, v53\n"
    "v_cvt_pk_bf16_f32 v51, v54, v55\n"
    "v_mfma_f32_16x16x32_bf16 v[68:71], v[204:207], v[24:27], v[68:71]\n"
    "ds_read_b128 v[24:27], v9 offset:33792\n"
    "v_cvt_pk_bf16_f32 v56, v56, v57\n"
    "s_cbranch_vccz .LgP_dma196_%=\n"
    "v_mfma_f32_16x16x32_bf16 v[32:35], v[208:211], v[28:31], v[32:35]\n"
    "s_or_b64 exec, vcc, 1\n"
    "buffer_atomic_smax v5, v2, s[36:39], 0 offen sc0 sc1\n"
    "s_mov_b64 exec, -1\n"
    "buffer_store_dwordx4 v[48:51], v1, s[36:39], s80 offen sc1\n"
    "v_cvt_pk_bf16_f32 v57, v58, v59\n"
    "v_mfma_f32_16x16x32_bf16 v[36:39], v[212:215], v[28:31], v[36:39]\n"
    "v_cvt_pk_bf16_f32 v58, v60, v61\n"
    "v_cvt_pk_bf16_f32 v59, v62, v63\n"
    "v_mfma_f32_16x16x32_bf16 v[64:67], v[224:227], v[28:31], v[64:67]\n"
    "s_waitcnt lgkmcnt(2)\n"
    "v_mfma_f32_16x16x32_bf16 v[68:71], v[228:231], v[28:31], v[68:71]\n"
    "ds_read_b128 v[28:31], v9 offset:35840\n"
    "v_mfma_f32_16x16x32_bf16 v[32:35], v[232:235], v[16:19], v[32:35]\n"
    "v_mfma_f32_16x16x32_bf16 v[36:39], v[236:239], v[16:19], v[36:39]\n"
    "buffer_store_dwordx4 v[56:59], v1, s[36:39], s80 offen offset:1024 sc1\n"
    "v_mfma_f32_16x16x32_bf16 v[64:67], v[248:251], v[16:19], v[64:67]\n"
    "s_waitcnt lgkmcnt(2)\n"
    "v_mfma_f32_16x16x32_bf16 v[68:71], v[252:255], v[16:19], v[68:71]\n"
    "v_mfma_f32_16x16x32_bf16 v[40:43], v[184:187], v[20:23], v[40:43]\n"
    "v_mfma_f32_16x16x32_bf16 v[44:47], v[188:191], v[20:23], v[44:47]\n"
    "v_cvt_pk_bf16_f32 v32, v32, v33\n"
    "v_cvt_pk_bf16_f32 v33, v34, v35\n"
    "v_mfma_f32_16x16x32_bf16 v[72:75], v[200:203], v[20:23], v[72:75]\n"
    "s_waitcnt lgkmcnt(1)\n"
    "v_cvt_pk_bf16_f32 v34, v36, v37\n"
    "v_cvt_pk_bf16_f32 v35, v38, v39\n"
    "v_mfma_f32_16x16x32_bf16 v[76:79], v[204:207], v[20:23], v[76:79]\n"
    "v_cvt_pk_bf16_f32 v64, v64, v65\n"
    "v_mfma_f32_16x16x32_bf16 v[40:43], v[208:211], v[24:27], v[40:43]\n"
    "buffer_store_dwordx4 v[32:35], v1, s[36:39], s79 offen sc1\n"
    "v_cvt_pk_bf16_f32 v65, v66, v67\n"
    "v_mfma_f32_16x16x32_bf16 v[44:47], v[212:215], v[24:27], v[44:47]\n"
    "v_cvt_pk_bf16_f32 v66, v68, v69\n"
    "v_cvt_pk_bf16_f32 v67, v70, v71\n"
    "v_mfma_f32_16x16x32_bf16 v[72:75], v[224:227], v[24:27], v[72:75]\n"
    "s_waitcnt lgkmcnt(0)\n"
    "v_mfma_f32_16x16x32_bf16 v[76:79], v[228:231], v[24:27], v[76:79]\n"
    "v_mfma_f32_16x16x32_bf16 v[40:43], v[232:235], v[28:31], v[40:43]\n"
    "v_mfma_f32_16x16x32_bf16 v[44:47], v[236:239], v[28:31], v[44:47]\n"
    "buffer_store_dwordx4 v[64:67], v1, s[36:39], s73 offen sc1\n"
    "v_mfma_f32_16x16x32_bf16 v[72:75], v[248:251], v[28:31], v[72:75]\n"
    "v_mfma_f32_16x16x32_bf16 v[76:79], v[252:255], v[28:31], v[76:79]\n"
    "v_cvt_pk_bf16_f32 v40, v40, v41\n"
    "v_cvt_pk_bf16_f32 v41, v42, v43\n"
    "v_cvt_pk_bf16_f32 v42, v44, v45\n"
    "v_cvt_pk_bf16_f32 v43, v46, v47\n"
    "buffer_store_dwordx4 v[40:43], v1, s[36:39], s79 offen offset:1024 sc1\n"
    "v_cvt_pk_bf16_f32 v72, v72, v73\n"
    "v_cvt_pk_bf16_f32 v73, v74, v75\n"
    "s_nop 0\n"
    "v_cvt_pk_bf16_f32 v74, v76, v77\n"
    "v_cvt_pk_bf16_f32 v75, v78, v79\n"
    "buffer_store_dwordx4 v[72:75], v1, s[36:39], s73 offen offset:1024 sc1\n"
    "v_mov_b32 v12, s68\n"
    "s_mov_b32 s68, s71\n"
    "s_cmp_eq_u32 s68, 64\n"
    "s_cbranch_scc1 .LgP_done_%=\n"
    "s_waitcnt vmcnt(7)\n"
    "v_add_u32 v13, v11, v3\n"
    "v_cmp_gt_i32 vcc, s71, v13\n"
    "s_cbranch_vccnz .LgP_idle_%=\n"
    "s_add_u32 m0, s77, s59\n"
    "s_add_u32 s77, s77, 8192\n"
    "buffer_load_dwordx4 v1, s[36:39], s76 offen sc1 lds\n"
    "buffer_load_dwordx4 v1, s[36:39], s76 offen offset:1024 sc1 lds\n"
    "s_add_u32 s76, s76, 16384\n"
    "s_add_u32 m0, s77, s59\n"
    "s_add_u32 s77, s77, 8192\n"
    "buffer_load_dwordx4 v1, s[36:39], s76 offen sc1 lds\n"
    "buffer_load_dwordx4 v1, s[36:39], s76 offen offset:1024 sc1 lds\n"
    "s_add_u32 s76, s76, 16384\n"
    "s_add_u32 m0, s77, s59\n"
    "s_add_u32 s77, s77, 8192\n"
    "buffer_load_dwordx4 v1, s[36:39], s76 offen sc1 lds\n"
    "buffer_load_dwordx4 v1, s[36:39], s76 offen offset:1024 sc1 lds\n"
    "s_add_u32 s76, s76, 16384\n"
    "s_add_u32 m0, s77, s59\n"
    "s_add_u32 s77, s77, 8192\n"
    "buffer_load_dwordx4 v1, s[36:39], s76 offen sc1 lds\n"
    "buffer_load_dwordx4 v1, s[36:39], s76 offen offset:1024 sc1 lds\n"
    "s_add_u32 s76, s76, 16384\n"
    "s_mov_b32 m0, s77\n"
    "s_nop 0\n"
    "buffer_load_dwordx4 v8, s[36:39], s76 offen sc1 lds\n"
    ".LgP_late_%=:\n"
    "s_waitcnt vmcnt(9)\n"
    "buffer_atomic_smax v12, v14, s[36:39], 0 offen sc1\n"
    "s_waitcnt vmcnt(1)\n"
    ".LgP_bar_%=:\n"
    "v_xor_b32 v9, s78, v9\n"
    "s_add_u32 s71, s68, 1\n"
    "s_barrier\n"
    "ds_read_b128 v[16:19], v9 offset:0\n"
    "ds_read_b128 v[20:23], v9 offset:1024\n"
    "ds_read_b128 v[24:27], v9 offset:2048\n"
    "ds_read_b128 v[28:31], v9 offset:3072\n"
    "v_add_u32 v13, v11, v3\n"
    "v_cmp_gt_i32 vcc, s71, v13\n"
    "s_nop 0\n"
    "s_or_b64 exec, vcc, 1\n"
    "buffer_atomic_smax v11, v2, s[36:39], 0 offen sc0 sc1\n"
    "s_mov_b64 exec, -1\n"
    "s_waitcnt lgkmcnt(3)\n"
    "v_mfma_f32_16x16x32_bf16 v[48:51], v[16:19], a[8:11], 0\n"
    "s_waitcnt lgkmcnt(2)\n"
    "v_mfma_f32_16x16x32_bf16 v[56:59], v[16:19], a[12:15], 0\n"
    "ds_read_b128 v[16:19], v9 offset:4096\n"
    "v_mfma_f32_16x16x32_bf16 v[52:55], v[20:23], a[8:11], 0\n"
    "s_waitcnt lgkmcnt(2)\n"
    "v_mfma_f32_16x16x32_bf16 v[60:63], v[20:23], a[12:15], 0\n"
    "ds_read_b128 v[20:23], v9 offset:5120\n"
    "v_mfma_f32_16x16x32_bf16 v[48:51], v[24:27], a[32:35], v[48:51]\n"
    "s_waitcnt lgkmcnt(2)\n"
    "v_mfma_f32_16x16x32_bf16 v[56:59], v[24:27], a[36:39], v[56:59]\n"
    "ds_read_b128 v[24:27], v9 offset:0\n"
    "v_mfma_f32_16x16x32_bf16 v[52:55], v[28:31], a[32:35], v[52:55]\n"
    "s_waitcnt lgkmcnt(2)\n"
    "v_mfma_f32_16x16x32_bf16 v[60:63], v[28:31], a[36:39], v[60:63]\n"
    "ds_read_b128 v[28:31], v9 offset:2048\n"
    "v_mfma_f32_16x16x32_bf16 v[48:51], v[16:19], a[56:59], v[48:51]\n"
    "s_waitcnt lgkmcnt(2)\n"
    "v_mfma_f32_16x16x32_bf16 v[56:59], v[16:19], a[60:63], v[56:59]\n"
    "ds_read_b128 v[16:19], v9 offset:4096\n"
    "v_mfma_f32_16x16x32_bf16 v[52:55], v[20:23], a[56:59], v[52:55]\n"
    "s_waitcnt lgkmcnt(2)\n"
    "s_waitcnt vmcnt(1)\n"
    "buffer_atomic_smax v12, v14, s[36:39], 0 offen sc1\n"
    "v_mfma_f32_16x16x32_bf16 v[60:63], v[20:23], a[60:63], v[60:63]\n"
    "ds_read_b128 v[20:23], v9 offset:1024\n"
    "v_mfma_f32_16x16x32_bf16 v[32:35], a[0:3], v[24:27], 0\n"
    "s_and_b32 s76, s71, s54\n"
    "v_mfma_f32_16x16x32_bf16 v[36:39], a[4:7], v[24:27], 0\n"
    "s_mul_i32 s76, s76, s53\n"
    "v_mfma_f32_16x16x32_bf16 v[64:67], a[16:19], v[24:27], 0\n"
    "s_waitcnt lgkmcnt(2)\n"
    "s_add_u32 s76, s76, s52\n"
    "v_mfma_f32_16x16x32_bf16 v[68:71], a[20:23], v[24:27], 0\n"
    "ds_read_b128 v[24:27], v9 offset:3072\n"
    "v_mfma_f32_16x16x32_bf16 v[32:35], a[24:27], v[28:31], v[32:35]\n"
    "s_and_b32 s77, s71, 1\n"
    "v_mfma_f32_16x16x32_bf16 v[36:39], a[28:31], v[28:31], v[36:39]\n"
    "s_mul_i32 s77, s77, 65536\n"
    "v_mfma_f32_16x16x32_bf16 v[64:67], a[40:43], v[28:31], v[64:67]\n"
    "s_waitcnt lgkmcnt(2)\n"
    "s_add_u32 s77, s77, s59\n"
    "v_mfma_f32_16x16x32_bf16 v[68:71], a[44:47], v[28:31], v[68:71]\n"
    "ds_read_b128 v[28:31], v9 offset:5120\n"
    "v_mfma_f32_16x16x32_bf16 v[32:35], a[48:51], v[16:19], v[32:35]\n"
    "s_and_b32 s73, s68, s57\n"
    "v_mfma_f32_16x16x32_bf16 v[36:39], a[52:55], v[16:19], v[36:39]\n"
    "s_mul_i32 s73, s73, s56\n"
    "v_mfma_f32_16x16x32_bf16 v[64:67], a[64:67], v[16:19], v[64:67]\n"
    "s_waitcnt lgkmcnt(2)\n"
    "s_add_u32 s73, s73, s55\n"
    "v_mfma_f32_16x16x32_bf16 v[68:71], a[68:71], v[16:19], v[68:71]\n"
    "ds_read_b128 v[16:19], v9 offset:6144\n"
    "v_mfma_f32_16x16x32_bf16 v[40:43], a[0:3], v[20:23], 0\n"
    "s_add_u32 s79, s73, 16384\n"
    "v_mfma_f32_16x16x32_bf16 v[44:47], a[4:7], v[20:23], 0\n"
    "s_add_u32 s80, s73, 32768\n"
    "v_mfma_f32_16x16x32_bf16 v[72:75], a[16:19], v[20:23], 0\n"
    "s_waitcnt lgkmcnt(2)\n"
    "v_mfma_f32_16x16x32_bf16 v[76:79], a[20:23], v[20:23], 0\n"
    "ds_read_b128 v[20:23], v9 offset:7168\n"
    "v_mfma_f32_16x16x32_bf16 v[40:43], a[24:27], v[24:27], v[40:43]\n"
    "v_mfma_f32_16x16x32_bf16 v[44:47], a[28:31], v[24:27], v[44:47]\n"
    "v_mfma_f32_16x16x32_bf16 v[72:75], a[40:43], v[24:27], v[72:75]\n"
    "s_waitcnt lgkmcnt(2)\n"
    "v_mfma_f32_16x16x32_bf16 v[76:79], a[44:47], v[24:27], v[76:79]\n"
    "ds_read_b128 v[24:27], v9 offset:8192\n"
    "v_mfma_f32_16x16x32_bf16 v[40:43], a[48:51], v[28:31], v[40:43]\n"
    "v_mfma_f32_16x16x32_bf16 v[44:47], a[52:55], v[28:31], v[44:47]\n"
    "v_add_u32 v13, v5, v3\n"
    "v_mfma_f32_16x16x32_bf16 v[72:75], a[64:67], v[28:31], v[72:75]\n"
    "s_waitcnt lgkmcnt(2)\n"
    "v_cmp_gt_i32 vcc, s71, v13\n"
    "v_mfma_f32_16x16x32_bf16 v[76:79], a[68:71], v[28:31], v[76:79]\n"
    "ds_read_b128 v[28:31], v9 offset:9216\n"
    "v_mfma_f32_16x16x32_bf16 v[32:35], a[72:75], v[16:19], v[32:35]\n"
    "s_or_b64 exec, vcc, 1\n"
    "buffer_atomic_smax v5, v2, s[36:39], 0 offen sc0 sc1\n"
    "s_mov_b64 exec, -1\n"
    "v_mfma_f32_16x16x32_bf16 v[36:39], a[76:79], v[16:19], v[36:39]\n"
    "v_mfma_f32_16x16x32_bf16 v[48:51], v[16:19], a[80:83], v[48:51]\n"
    "v_mfma_f32_16x16x32_bf16 v[56:59], v[16:19], a[84:87], v[56:59]\n"
    "v_mfma_f32_16x16x32_bf16 v[64:67], a[88:91], v[16:19], v[64:67]\n"
    "s_waitcnt lgkmcnt(2)\n"
    "v_mfma_f32_16x16x32_bf16 v[68:71], a[92:95], v[16:19], v[68:71]\n"
    "ds_read_b128 v[16:19], v9 offset:10240\n"
    "v_mfma_f32_16x16x32_bf16 v[40:43], a[72:75], v[20:23], v[40:43]\n"
    "v_mfma_f32_16x16x32_bf16 v[44:47], a[76:79], v[20:23], v[44:47]\n"
    "v_mfma_f32_16x16x32_bf16 v[52:55], v[20:23], a[80:83], v[52:55]\n"
    "v_mfma_f32_16x16x32_bf16 v[60:63], v[20:23], a[84:87], v[60:63]\n"
    "v_mfma_f32_16x16x32_bf16 v[72:75], a[88:91], v[20:23], v[72:75]\n"
    "s_waitcnt lgkmcnt(2)\n"
    "v_mfma_f32_16x16x32_bf16 v[76:79], a[92:95], v[20:23], v[76:79]\n"
    "ds_read_b128 v[20:23], v9 offset:11264\n"
    "v_mfma_f32_16x16x32_bf16 v[32:35], a[96:99], v[24:27], v[32:35]\n"
    "v_mfma_f32_16x16x32_bf16 v[36:39], a[100:103], v[24:27], v[36:39]\n"
    "v_mfma_f32_16x16x32_bf16 v[48:51], v[24:27], a[104:107], v[48:51]\n"
    "v_mfma_f32_16x16x32_bf16 v[56:59], v[24:27], a[108:111], v[56:59]\n"
    "s_waitcnt vmcnt(1)\n"
    "v_add_u32 v13, v11, v3\n"
    "v_mfma_f32_16x16x32_bf16 v[64:67], a[112:115], v[24:27], v[64:67]\n"
    "s_waitcnt lgkmcnt(2)\n"
    "buffer_atomic_smax v12, v14, s[36:39], 0 offen sc1\n"
    "v_cmp_gt_i32 vcc, s71, v13\n"
    "v_mfma_f32_16x16x32_bf16 v[68:71], a[116:119], v[24:27], v[68:71]\n"
    "ds_read_b128 v[24:27], v9 offset:12288\n"
    "v_mfma_f32_16x16x32_bf16 v[40:43], a[96:99], v[28:31], v[40:43]\n"
    "v_mfma_f32_16x16x32_bf16 v[44:47], a[100:103], v[28:31], v[44:47]\n"
    "s_branch .LgP_mid_%=\n"
    ".LgP_dma76_%=:\n"
    "v_mfma_f32_16x16x32_bf16 v[64:67], a[160:163], v[24:27], v[64:67]\n"
    "s_waitcnt lgkmcnt(2)\n"
    "s_add_u32 m0, s77, s59\n"
    "s_add_u32 s77, s77, 8192\n"
    "buffer_load_dwordx4 v1, s[36:39], s76 offen sc1 lds\n"
    "v_mfma_f32_16x16x32_bf16 v[68:71], a[164:167], v[24:27], v[68:71]\n"
    "ds_read_b128 v[24:27], v9 offset:16384\n"
    "v_mfma_f32_16x16x32_bf16 v[40:43], a[144:147], v[28:31], v[40:43]\n"
    "v_mfma_f32_16x16x32_bf16 v[44:47], a[148:151], v[28:31], v[44:47]\n"
    "v_mfma_f32_16x16x32_bf16 v[52:55], v[28:31], a[152:155], v[52:55]\n"
    "buffer_load_dwordx4 v1, s[36:39], s76 offen offset:1024 sc1 lds\n"
    "s_add_u32 s76, s76, 16384\n"
    "v_mfma_f32_16x16x32_bf16 v[60:63], v[28:31], a[156:159], v[60:63]\n"
    "v_mfma_f32_16x16x32_bf16 v[72:75], a[160:163], v[28:31], v[72:75]\n"
    "s_waitcnt lgkmcnt(2)\n"
    "v_mfma_f32_16x16x32_bf16 v[76:79], a[164:167], v[28:31], v[76:79]\n"
    "ds_read_b128 v[28:31], v9 offset:17408\n"
    "v_mfma_f32_16x16x32_bf16 v[32:35], a[168:171], v[16:19], v[32:35]\n"
    "v_mfma_f32_16x16x32_bf16 v[36:39], a[172:175], v[16:19], v[36:39]\n"
    "s_add_u32 m0, s77, s59\n"
    "s_add_u32 s77, s77, 8192\n"
    "buffer_load_dwordx4 v1, s[36:39], s76 offen sc1 lds\n"
    "v_mfma_f32_16x16x32_bf16 v[48:51], v[16:19], a[176:179], v[48:51]\n"
    "v_mfma_f32_16x16x32_bf16 v[56:59], v[16:19], a[180:183], v[56:59]\n"
    "v_mfma_f32_16x16x32_bf16 v[64:67], a[184:187], v[16:19], v[64:67]\n"
    "s_waitcnt lgkmcnt(2)\n"
    "buffer_load_dwordx4 v1, s[36:39], s76 offen offset:1024 sc1 lds\n"
    "s_add_u32 s76, s76, 16384\n"
    "v_mfma_f32_16x16x32_bf16 v[68:71], a[188:191], v[16:19], v[68:71]\n"
    "ds_read_b128 v[16:19], v9 offset:18432\n"
    "v_mfma_f32_16x16x32_bf16 v[40:43], a[168:171], v[20:23], v[40:43]\n"
    "v_mfma_f32_16x16x32_bf16 v[44:47], a[172:175], v[20:23], v[44:47]\n"
    "v_mfma_f32_16x16x32_bf16 v[52:55], v[20:23], a[176:179], v[52:55]\n"
    "v_mfma_f32_16x16x32_bf16 v[60:63], v[20:23], a[180:183], v[60:63]\n"
    "s_add_u32 m0, s77, s59\n"
    "s_add_u32 s77, s77, 8192\n"
    "buffer_load_dwordx4 v1, s[36:39], s76 offen sc1 lds\n"
    "v_mfma_f32_16x16x32_bf16 v[72:75], a[184:187], v[20:23], v[72:75]\n"
    "s_waitcnt lgkmcnt(2)\n"
    "v_mfma_f32_16x16x32_bf16 v[76:79], a[188:191], v[20:23], v[76:79]\n"
    "ds_read_b128 v[20:23], v9 offset:19456\n"
    "v_mfma_f32_16x16x32_bf16 v[32:35], a[192:195], v[24:27], v[32:35]\n"
    "v_mfma_f32_16x16x32_bf16 v[36:39], a[196:199], v[24:27], v[36:39]\n"
    "buffer_load_dwordx4 v1, s[36:39], s76 offen offset:1024 sc1 lds\n"
    "s_add_u32 s76, s76, 16384\n"
    "v_mfma_f32_16x16x32_bf16 v[48:51], v[24:27], a[200:203], v[48:51]\n"
    "v_mfma_f32_16x16x32_bf16 v[56:59], v[24:27], a[204:207], v[56:59]\n"
    "v_mfma_f32_16x16x32_bf16 v[64:67], a[208:211], v[24:27], v[64:67]\n"
    "s_waitcnt lgkmcnt(2)\n"
    "v_mfma_f32_16x16x32_bf16 v[68:71], a[212:215], v[24:27], v[68:71]\n"
    "ds_read_b128 v[24:27], v9 offset:20480\n"
    "v_mfma_f32_16x16x32_bf16 v[40:43], a[192:195], v[28:31], v[40:43]\n"
    "s_add_u32 m0, s77, s59\n"
    "s_add_u32 s77, s77, 8192\n"
    "buffer_load_dwordx4 v1, s[36:39], s76 offen sc1 lds\n"
    "v_mfma_f32_16x16x32_bf16 v[44:47], a[196:199], v[28:31], v[44:47]\n"
    "v_mfma_f32_16x16x32_bf16 v[52:55], v[28:31], a[200:203], v[52:55]\n"
    "v_mfma_f32_16x16x32_bf16 v[60:63], v[28:31], a[204:207], v[60:63]\n"
    "buffer_load_dwordx4 v1, s[36:39], s76 offen offset:1024 sc1 lds\n"
    "s_add_u32 s76, s76, 16384\n"
    "v_mfma_f32_16x16x32_bf16 v[72:75], a[208:211], v[28:31], v[72:75]\n"
    "s_waitcnt lgkmcnt(2)\n"
    "v_mfma_f32_16x16x32_bf16 v[76:79], a[212:215], v[28:31], v[76:79]\n"
    "ds_read_b128 v[28:31], v9 offset:21504\n"
    "v_mfma_f32_16x16x32_bf16 v[32:35], a[216:219], v[16:19], v[32:35]\n"
    "v_mfma_f32_16x16x32_bf16 v[36:39], a[220:223], v[16:19], v[36:39]\n"
    "v_mfma_f32_16x16x32_bf16 v[48:51], v[16:19], a[224:227], v[48:51]\n"
    "s_mov_b32 m0, s77\n"
    "s_nop 0\n"
    "buffer_load_dwordx4 v8, s[36:39], s76 offen sc1 lds\n"
    "v_mfma_f32_16x16x32_bf16 v[56:59], v[16:19], a[228:231], v[56:59]\n"
    "v_mfma_f32_16x16x32_bf16 v[64:67], a[232:235], v[16:19], v[64:67]\n"
    "s_waitcnt lgkmcnt(2)\n"
    "v_mfma_f32_16x16x32_bf16 v[68:71], a[236:239], v[16:19], v[68:71]\n"
    "ds_read_b128 v[16:19], v9 offset:22528\n"
    "v_mfma_f32_16x16x32_bf16 v[40:43], a[216:219], v[20:23], v[40:43]\n"
    "v_mfma_f32_16x16x32_bf16 v[44:47], a[220:223], v[20:23], v[44:47]\n"
    "s_branch .LgP_q116_%=\n"
    ".LgP_dma96_%=:\n"
    "v_mfma_f32_16x16x32_bf16 v[32:35], a[192:195], v[24:27], v[32:35]\n"
    "s_add_u32 m0, s77, s59\n"
    "s_add_u32 s77, s77, 8192\n"
    "buffer_load_dwordx4 v1, s[36:39], s76 offen sc1 lds\n"
    "v_mfma_f32_16x16x32_bf16 v[36:39], a[196:199], v[24:27], v[36:39]\n"
    "v_mfma_f32_16x16x32_bf16 v[48:51], v[24:27], a[200:203], v[48:51]\n"
    "v_mfma_f32_16x16x32_bf16 v[56:59], v[24:27], a[204:207], v[56:59]\n"
    "buffer_load_dwordx4 v1, s[36:39], s76 offen offset:1024 sc1 lds\n"
    "s_add_u32 s76, s76, 16384\n"
    "v_mfma_f32_16x16x32_bf16 v[64:67], a[208:211], v[24:27], v[64:67]\n"
    "s_waitcnt lgkmcnt(2)\n"
    "v_mfma_f32_16x16x32_bf16 v[68:71], a[212:215], v[24:27], v[68:71]\n"
    "ds_read_b128 v[24:27], v9 offset:20480\n"
    "v_mfma_f32_16x16x32_bf16 v[40:43], a[192:195], v[28:31], v[40:43]\n"
    "v_mfma_f32_16x16x32_bf16 v[44:47], a[196:199], v[28:31], v[44:47]\n"
    "v_mfma_f32_16x16x32_bf16 v[52:55], v[28:31], a[200:203], v[52:55]\n"
    "s_add_u32 m0, s77, s59\n"
    "s_add_u32 s77, s77, 8192\n"
    "buffer_load_dwordx4 v1, s[36:39], s76 offen sc1 lds\n"
    "v_mfma_f32_16x16x32_bf16 v[60:63], v[28:31], a[204:207], v[60:63]\n"
    "v_mfma_f32_16x16x32_bf16 v[72:75], a[208:211], v[28:31], v[72:75]\n"
    "s_waitcnt lgkmcnt(2)\n"
    "v_mfma_f32_16x16x32_bf16 v[76:79], a[212:215], v[28:31], v[76:79]\n"
    "ds_read_b128 v[28:31], v9 offset:21504\n"
    "v_mfma_f32_16x16x32_bf16 v[32:35], a[216:219], v[16:19], v[32:35]\n"
    "v_mfma_f32_16x16x32_bf16 v[36:39], a[220:223], v[16:19], v[36:39]\n"
    "buffer_load_dwordx4 v1, s[36:39], s76 offen offset:1024 sc1 lds\n"
    "s_add_u32 s76, s76, 16384\n"
    "v_mfma_f32_16x16x32_bf16 v[48:51], v[16:19], a[224:227], v[48:51]\n"
    "v_mfma_f32_16x16x32_bf16 v[56:59], v[16:19], a[228:231], v[56:59]\n"
    "v_mfma_f32_16x16x32_bf16 v[64:67], a[232:235], v[16:19], v[64:67]\n"
    "s_waitcnt lgkmcnt(2)\n"
    "s_add_u32 m0, s77, s59\n"
    "s_add_u32 s77, s77, 8192\n"
    "buffer_load_dwordx4 v1, s[36:39], s76 offen sc1 lds\n"
    "v_mfma_f32_16x16x32_bf16 v[68:71], a[236:239], v[16:19], v[68:71]\n"
    "ds_read_b128 v[16:19], v9 offset:22528\n"
    "v_mfma_f32_16x16x32_bf16 v[40:43], a[216:219], v[20:23], v[40:43]\n"
    "v_mfma_f32_16x16x32_bf16 v[44:47], a[220:223], v[20:23], v[44:47]\n"
    "v_mfma_f32_16x16x32_bf16 v[52:55], v[20:23], a[224:227], v[52:55]\n"
    "v_mfma_f32_16x16x32_bf16 v[60:63], v[20:23], a[228:231], v[60:63]\n"
    "buffer_load_dwordx4 v1, s[36:39], s76 offen offset:1024 sc1 lds\n"
    "s_add_u32 s76, s76, 16384\n"
    "v_mfma_f32_16x16x32_bf16 v[72:75], a[232:235], v[20:23], v[72:75]\n"
    "s_waitcnt lgkmcnt(2)\n"
    "v_mfma_f32_16x16x32_bf16 v[76:79], a[236:239], v[20:23], v[76:79]\n"
    "ds_read_b128 v[20:23], v9 offset:23552\n"
    "v_mfma_f32_16x16x32_bf16 v[32:35], a[240:243], v[24:27], v[32:35]\n"
    "v_mfma_f32_16x16x32_bf16 v[36:39], a[244:247], v[24:27], v[36:39]\n"
    "v_mfma_f32_16x16x32_bf16 v[48:51], v[24:27], a[248:251], v[48:51]\n"
    "s_add_u32 m0, s77, s59\n"
    "s_add_u32 s77, s77, 8192\n"
    "buffer_load_dwordx4 v1, s[36:39], s76 offen sc1 lds\n"
    "v_mfma_f32_16x16x32_bf16 v[56:59], v[24:27], a[252:255], v[56:59]\n"
    "v_mfma_f32_16x16x32_bf16 v[64:67], v[80:83], v[24:27], v[64:67]\n"
    "s_waitcnt lgkmcnt(2)\n"
    "v_mfma_f32_16x16x32_bf16 v[68:71], v[84:87], v[24:27], v[68:71]\n"
    "ds_read_b128 v[24:27], v9 offset:24576\n"
    "v_mfma_f32_16x16x32_bf16 v[40:43], a[240:243], v[28:31], v[40:43]\n"
    "buffer_load_dwordx4 v1, s[36:39], s76 offen offset:1024 sc1 lds\n"
    "s_add_u32 s76, s76, 16384\n"
    "v_mfma_f32_16x16x32_bf16 v[44:47], a[244:247], v[28:31], v[44:47]\n"
    "v_mfma_f32_16x16x32_bf16 v[52:55], v[28:31], a[248:251], v[52:55]\n"
    "v_mfma_f32_16x16x32_bf16 v[60:63], v[28:31], a[252:255], v[60:63]\n"
    "v_mfma_f32_16x16x32_bf16 v[72:75], v[80:83], v[28:31], v[72:75]\n"
    "s_waitcnt lgkmcnt(2)\n"
    "s_mov_b32 m0, s77\n"
    "s_nop 0\n"
    "buffer_load_dwordx4 v8, s[36:39], s76 offen sc1 lds\n"
    "v_mfma_f32_16x16x32_bf16 v[76:79], v[84:87], v[28:31], v[76:79]\n"
    "ds_read_b128 v[28:31], v9 offset:25600\n"
    "v_mfma_f32_16x16x32_bf16 v[32:35], v[88:91], v[16:19], v[32:35]\n"
    "v_mfma_f32_16x16x32_bf16 v[36:39], v[92:95], v[16:19], v[36:39]\n"
    "v_mfma_f32_16x16x32_bf16 v[48:51], v[16:19], v[96:99], v[48:51]\n"
    "v_mfma_f32_16x16x32_bf16 v[56:59], v[16:19], v[100:103], v[56:59]\n"
    "s_branch .LgP_q136_%=\n"
    ".LgP_dma116_%=:\n"
    "v_mfma_f32_16x16x32_bf16 v[52:55], v[20:23], a[224:227], v[52:55]\n"
    "s_add_u32 m0, s77, s59\n"
    "s_add_u32 s77, s77, 8192\n"
    "buffer_load_dwordx4 v1, s[36:39], s76 offen sc1 lds\n"
    "v_mfma_f32_16x16x32_bf16 v[60:63], v[20:23], a[228:231], v[60:63]\n"
    "v_mfma_f32_16x16x32_bf16 v[72:75], a[232:235], v[20:23], v[72:75]\n"
    "s_waitcnt lgkmcnt(2)\n"
    "v_mfma_f32_16x16x32_bf16 v[76:79], a[236:239], v[20:23], v[76:79]\n"
    "ds_read_b128 v[20:23], v9 offset:23552\n"
    "v_mfma_f32_16x16x32_bf16 v[32:35], a[240:243], v[24:27], v[32:35]\n"
    "buffer_load_dwordx4 v1, s[36:39], s76 offen offset:1024 sc1 lds\n"
    "s_add_u32 s76, s76, 16384\n"
    "v_mfma_f32_16x16x32_bf16 v[36:39], a[244:247], v[24:27], v[36:39]\n"
    "v_mfma_f32_16x16x32_bf16 v[48:51], v[24:27], a[248:251], v[48:51]\n"
    "v_mfma_f32_16x16x32_bf16 v[56:59], v[24:27], a[252:255], v[56:59]\n"
    "v_mfma_f32_16x16x32_bf16 v[64:67], v[80:83], v[24:27], v[64:67]\n"
    "s_waitcnt lgkmcnt(2)\n"
    "s_add_u32 m0, s77, s59\n"
    "s_add_u32 s77, s77, 8192\n"
    "buffer_load_dwordx4 v1, s[36:39], s76 offen sc1 lds\n"
    "v_mfma_f32_16x16x32_bf16 v[68:71], v[84:87], v[24:27], v[68:71]\n"
    "ds_read_b128 v[24:27], v9 offset:24576\n"
    "v_mfma_f32_16x16x32_bf16 v[40:43], a[240:243], v[28:31], v[40:43]\n"
    "v_mfma_f32_16x16x32_bf16 v[44:47], a[244:247], v[28:31], v[44:47]\n"
    "v_mfma_f32_16x16x32_bf16 v[52:55], v[28:31], a[248:251], v[52:55]\n"
    "v_mfma_f32_16x16x32_bf16 v[60:63], v[28:31], a[252:255], v[60:63]\n"
    "buffer_load_dwordx4 v1, s[36:39], s76 offen offset:1024 sc1 lds\n"
    "s_add_u32 s76, s76, 16384\n"
    "v_mfma_f32_16x16x32_bf16 v[72:75], v[80:83], v[28:31], v[72:75]\n"
    "s_waitcnt lgkmcnt(2)\n"
    "v_mfma_f32_16x16x32_bf16 v[76:79], v[84:87], v[28:31], v[76:79]\n"
    "ds_read_b128 v[28:31], v9 offset:25600\n"
    "v_mfma_f32_16x16x32_bf16 v[32:35], v[88:91], v[16:19], v[32:35]\n"
    "v_mfma_f32_16x16x32_bf16 v[36:39], v[92:95], v[16:19], v[36:39]\n"
    "s_add_u32 m0, s77, s59\n"
    "s_add_u32 s77, s77, 8192\n"
    "buffer_load_dwordx4 v1, s[36:39], s76 offen sc1 lds\n"
    "v_mfma_f32_16x16x32_bf16 v[48:51], v[16:19], v[96:99], v[48:51]\n"
    "v_mfma_f32_16x16x32_bf16 v[56:59], v[16:19], v[100:103], v[56:59]\n"
    "v_mfma_f32_16x16x32_bf16 v[64:67], v[104:107], v[16:19], v[64:67]\n"
    "s_waitcnt lgkmcnt(2)\n"
    "v_mfma_f32_16x16x32_bf16 v[68:71], v[108:111], v[16:19], v[68:71]\n"
    "ds_read_b128 v[16:19], v9 offset:26624\n"
    "v_mfma_f32_16x16x32_bf16 v[40:43], v[88:91], v[20:23], v[40:43]\n"
    "buffer_load_dwordx4 v1, s[36:39], s76 offen offset:1024 sc1 lds\n"
    "s_add_u32 s76, s76, 16384\n"
    "v_mfma_f32_16x16x32_bf16 v[44:47], v[92:95], v[20:23], v[44:47]\n"
    "v_mfma_f32_16x16x32_bf16 v[52:55], v[20:23], v[96:99], v[52:55]\n"
    "v_mfma_f32_16x16x32_bf16 v[60:63], v[20:23], v[100:103], v[60:63]\n"
    "v_mfma_f32_16x16x32_bf16 v[72:75], v[104:107], v[20:23], v[72:75]\n"
    "s_waitcnt lgkmcnt(2)\n"
    "s_add_u32 m0, s77, s59\n"
    "s_add_u32 s77, s77, 8192\n"
    "buffer_load_dwordx4 v1, s[36:39], s76 offen sc1 lds\n"
    "v_mfma_f32_16x16x32_bf16 v[76:79], v[108:111], v[20:23], v[76:79]\n"
    "ds_read_b128 v[20:23], v9 offset:27648\n"
    "v_mfma_f32_16x16x32_bf16 v[32:35], v[112:115], v[24:27], v[32:35]\n"
    "v_mfma_f32_16x16x32_bf16 v[36:39], v[116:119], v[24:27], v[36:39]\n"
    "v_mfma_f32_16x16x32_bf16 v[48:51], v[24:27], v[120:123], v[48:51]\n"
    "buffer_load_dwordx4 v1, s[36:39], s76 offen offset:1024 sc1 lds\n"
    "s_add_u32 s76, s76, 16384\n"
    "v_mfma_f32_16x16x32_bf16 v[56:59], v[24:27], v[124:127], v[56:59]\n"
    "v_mfma_f32_16x16x32_bf16 v[64:67], v[128:131], v[24:27], v[64:67]\n"
    "s_waitcnt lgkmcnt(2)\n"
    "v_mfma_f32_16x16x32_bf16 v[68:71], v[132:135], v[24:27], v[68:71]\n"
    "ds_read_b128 v[24:27], v9 offset:28672\n"
    "v_mfma_f32_16x16x32_bf16 v[40:43], v[112:115], v[28:31], v[40:43]\n"
    "v_mfma_f32_16x16x32_bf16 v[44:47], v[116:119], v[28:31], v[44:47]\n"
    "s_mov_b32 m0, s77\n"
    "s_nop 0\n"
    "buffer_load_dwordx4 v8, s[36:39], s76 offen sc1 lds\n"
    "v_mfma_f32_16x16x32_bf16 v[52:55], v[28:31], v[120:123], v[52:55]\n"
    "v_mfma_f32_16x16x32_bf16 v[60:63], v[28:31], v[124:127], v[60:63]\n"
    "v_mfma_f32_16x16x32_bf16 v[72:75], v[128:131], v[28:31], v[72:75]\n"
    "s_waitcnt lgkmcnt(2)\n"
    "v_mfma_f32_16x16x32_bf16 v[76:79], v[132:135], v[28:31], v[76:79]\n"
    "ds_read_b128 v[28:31], v9 offset:29696\n"
    "s_branch .LgP_q156_%=\n"
    ".LgP_dma136_%=:\n"
    "v_mfma_f32_16x16x32_bf16 v[64:67], v[104:107], v[16:19], v[64:67]\n"
    "s_waitcnt lgkmcnt(2)\n"
    "s_add_u32 m0, s77, s59\n"
    "s_add_u32 s77, s77, 8192\n"
    "buffer_load_dwordx4 v1, s[36:39], s76 offen sc1 lds\n"
    "v_mfma_f32_16x16x32_bf16 v[68:71], v[108:111], v[16:19], v[68:71]\n"
    "ds_read_b128 v[16:19], v9 offset:26624\n"
    "v_mfma_f32_16x16x32_bf16 v[40:43], v[88:91], v[20:23], v[40:43]\n"
    "v_mfma_f32_16x16x32_bf16 v[44:47], v[92:95], v[20:23], v[44:47]\n"
    "v_mfma_f32_16x16x32_bf16 v[52:55], v[20:23], v[96:99], v[52:55]\n"
    "buffer_load_dwordx4 v1, s[36:39], s76 offen offset:1024 sc1 lds\n"
    "s_add_u32 s76, s76, 16384\n"
    "v_mfma_f32_16x16x32_bf16 v[60:63], v[20:23], v[100:103], v[60:63]\n"
    "v_mfma_f32_16x16x32_bf16 v[72:75], v[104:107], v[20:23], v[72:75]\n"
    "s_waitcnt lgkmcnt(2)\n"
    "v_mfma_f32_16x16x32_bf16 v[76:79], v[108:111], v[20:23], v[76:79]\n"
    "ds_read_b128 v[20:23], v9 offset:27648\n"
    "v_mfma_f32_16x16x32_bf16 v[32:35], v[112:115], v[24:27], v[32:35]\n"
    "v_mfma_f32_16x16x32_bf16 v[36:39], v[116:119], v[24:27], v[36:39]\n"
    "s_add_u32 m0, s77, s59\n"
    "s_add_u32 s77, s77, 8192\n"
    "buffer_load_dwordx4 v1, s[36:39], s76 offen sc1 lds\n"
    "v_mfma_f32_16x16x32_bf16 v[48:51], v[24:27], v[120:123], v[48:51]\n"
    "v_mfma_f32_16x16x32_bf16 v[56:59], v[24:27], v[124:127], v[56:59]\n"
    "v_mfma_f32_16x16x32_bf16 v[64:67], v[128:131], v[24:27], v[64:67]\n"
    "s_waitcnt lgkmcnt(2)\n"
    "buffer_load_dwordx4 v1, s[36:39], s76 offen offset:1024 sc1 lds\n"
    "s_add_u32 s76, s76, 16384\n"
    "v_mfma_f32_16x16x32_bf16 v[68:71], v[132:135], v[24:27], v[68:71]\n"
    "ds_read_b128 v[24:27], v9 offset:28672\n"
    "v_mfma_f32_16x16x32_bf16 v[40:43], v[112:115], v[28:31], v[40:43]\n"
    "v_mfma_f32_16x16x32_bf16 v[44:47], v[116:119], v[28:31], v[44:47]\n"
    "v_mfma_f32_16x16x32_bf16 v[52:55], v[28:31], v[120:123], v[52:55]\n"
    "v_mfma_f32_16x16x32_bf16 v[60:63], v[28:31], v[124:127], v[60:63]\n"
    "s_add_u32 m0, s77, s59\n"
    "s_add_u32 s77, s77, 8192\n"
    "buffer_load_dwordx4 v1, s[36:39], s76 offen sc1 lds\n"
    "v_mfma_f32_16x16x32_bf16 v[72:75], v[128:131], v[28:31], v[72:75]\n"
    "s_waitcnt lgkmcnt(2)\n"
    "v_mfma_f32_16x16x32_bf16 v[76:79], v[132:135], v[28:31], v[76:79]\n"
    "ds_read_b128 v[28:31], v9 offset:29696\n"
    "v_mfma_f32_16x16x32_bf16 v[32:35], v[136:139], v[16:19], v[32:35]\n"
    "v_mfma_f32_16x16x32_bf16 v[36:39], v[140:143], v[16:19], v[36:39]\n"
    "buffer_load_dwordx4 v1, s[36:39], s76 offen offset:1024 sc1 lds\n"
    "s_add_u32 s76, s76, 16384\n"
    "v_mfma_f32_16x16x32_bf16 v[48:51], v[16:19], v[144:147], v[48:51]\n"
    "v_mfma_f32_16x16x32_bf16 v[56:59], v[16:19], v[148:151], v[56:59]\n"
    "v_mfma_f32_16x16x32_bf16 v[64:67], v[152:155], v[16:19], v[64:67]\n"
    "s_waitcnt lgkmcnt(2)\n"
    "v_mfma_f32_16x16x32_bf16 v[68:71], v[156:159], v[16:19], v[68:71]\n"
    "ds_read_b128 v[16:19], v9 offset:30720\n"
    "v_mfma_f32_16x16x32_bf16 v[40:43], v[136:139], v[20:23], v[40:43]\n"
    "s_add_u32 m0, s77, s59\n"
    "s_add_u32 s77, s77, 8192\n"
    "buffer_load_dwordx4 v1, s[36:39], s76 offen sc1 lds\n"
    "v_mfma_f32_16x16x32_bf16 v[44:47], v[140:143], v[20:23], v[44:47]\n"
    "v_mfma_f32_16x16x32_bf16 v[52:55], v[20:23], v[144:147], v[52:55]\n"
    "v_mfma_f32_16x16x32_bf16 v[60:63], v[20:23], v[148:151], v[60:63]\n"
    "buffer_load_dwordx4 v1, s[36:39], s76 offen offset:1024 sc1 lds\n"
    "s_add_u32 s76, s76, 16384\n"
    "v_mfma_f32_16x16x32_bf16 v[72:75], v[152:155], v[20:23], v[72:75]\n"
    "s_waitcnt lgkmcnt(2)\n"
    "v_mfma_f32_16x16x32_bf16 v[76:79], v[156:159], v[20:23], v[76:79]\n"
    "ds_read_b128 v[20:23], v9 offset:31744\n"
    "v_mfma_f32_16x16x32_bf16 v[32:35], v[160:163], v[24:27], v[32:35]\n"
    "v_mfma_f32_16x16x32_bf16 v[36:39], v[164:167], v[24:27], v[36:39]\n"
    "v_mfma_f32_16x16x32_bf16 v[48:51], v[24:27], v[168:171], v[48:51]\n"
    "s_mov_b32 m0, s77\n"
    "s_nop 0\n"
    "buffer_load_dwordx4 v8, s[36:39], s76 offen sc1 lds\n"
    "v_mfma_f32_16x16x32_bf16 v[56:59], v[24:27], v[172:175], v[56:59]\n"
    "v_mfma_f32_16x16x32_bf16 v[64:67], v[176:179], v[24:27], v[64:67]\n"
    "s_waitcnt lgkmcnt(2)\n"
    "v_mfma_f32_16x16x32_bf16 v[68:71], v[180:183], v[24:27], v[68:71]\n"
    "ds_read_b128 v[24:27], v9 offset:32768\n"
    "v_mfma_f32_16x16x32_bf16 v[40:43], v[160:163], v[28:31], v[40:43]\n"
    "v_mfma_f32_16x16x32_bf16 v[44:47], v[164:167], v[28:31], v[44:47]\n"
    "v_mfma_f32_16x16x32_bf16 v[52:55], v[28:31], v[168:171], v[52:55]\n"
    "v_mfma_f32_16x16x32_bf16 v[60:63], v[28:31], v[172:175], v[60:63]\n"
    "v_mfma_f32_16x16x32_bf16 v[72:75], v[176:179], v[28:31], v[72:75]\n"
    "s_waitcnt lgkmcnt(2)\n"
    "v_mfma_f32_16x16x32_bf16 v[76:79], v[180:183], v[28:31], v[76:79]\n"
    "ds_read_b128 v[28:31], v9 offset:33792\n"
    "v_mfma_f32_16x16x32_bf16 v[48:51], v[16:19], v[192:195], v[48:51]\n"
    "s_waitcnt lgkmcnt(2)\n"
    "v_mfma_f32_16x16x32_bf16 v[56:59], v[16:19], v[196:199], v[56:59]\n"
    "ds_read_b128 v[16:19], v9 offset:34816\n"
    "v_mfma_f32_16x16x32_bf16 v[52:55], v[20:23], v[192:195], v[52:55]\n"
    "s_waitcnt lgkmcnt(2)\n"
    "v_mfma_f32_16x16x32_bf16 v[60:63], v[20:23], v[196:199], v[60:63]\n"
    "ds_read_b128 v[20:23], v9 offset:35840\n"
    "v_mfma_f32_16x16x32_bf16 v[48:51], v[24:27], v[216:219], v[48:51]\n"
    "s_waitcnt lgkmcnt(2)\n"
    "v_mfma_f32_16x16x32_bf16 v[56:59], v[24:27], v[220:223], v[56:59]\n"
    "ds_read_b128 v[24:27], v9 offset:30720\n"
    "v_mfma_f32_16x16x32_bf16 v[52:55], v[28:31], v[216:219], v[52:55]\n"
    "s_waitcnt lgkmcnt(2)\n"
    "v_mfma_f32_16x16x32_bf16 v[60:63], v[28:31], v[220:223], v[60:63]\n"
    "ds_read_b128 v[28:31], v9 offset:32768\n"
    "v_mfma_f32_16x16x32_bf16 v[48:51], v[16:19], v[240:243], v[48:51]\n"
    "s_waitcnt lgkmcnt(2)\n"
    "v_mfma_f32_16x16x32_bf16 v[56:59], v[16:19], v[244:247], v[56:59]\n"
    "ds_read_b128 v[16:19], v9 offset:34816\n"
    "v_mfma_f32_16x16x32_bf16 v[52:55], v[20:23], v[240:243], v[52:55]\n"
    "s_waitcnt lgkmcnt(2)\n"
    "v_mfma_f32_16x16x32_bf16 v[60:63], v[20:23], v[244:247], v[60:63]\n"
    "ds_read_b128 v[20:23], v9 offset:31744\n"
    "v_mfma_f32_16x16x32_bf16 v[32:35], v[184:187], v[24:27], v[32:35]\n"
    "v_mfma_f32_16x16x32_bf16 v[36:39], v[188:191], v[24:27], v[36:39]\n"
    "v_cvt_pk_bf16_f32 v48, v48, v49\n"
    "v_cvt_pk_bf16_f32 v49, v50, v51\n"
    "v_mfma_f32_16x16x32_bf16 v[64:67], v[200:203], v[24:27], v[64:67]\n"
    "s_waitcnt lgkmcnt(2)\n"
    "v_cvt_pk_bf16_f32 v50, v52, v53\n"
    "v_cvt_pk_bf16_f32 v51, v54, v55\n"
    "v_mfma_f32_16x16x32_bf16 v[68:71], v[204:207], v[24:27], v[68:71]\n"
    "ds_read_b128 v[24:27], v9 offset:33792\n"
    "v_cvt_pk_bf16_f32 v56, v56, v57\n"
    "v_mfma_f32_16x16x32_bf16 v[32:35], v[208:211], v[28:31], v[32:35]\n"
    "buffer_store_dwordx4 v[48:51], v1, s[36:39], s80 offen sc1\n"
    "v_cvt_pk_bf16_f32 v57, v58, v59\n"
    "v_mfma_f32_16x16x32_bf16 v[36:39], v[212:215], v[28:31], v[36:39]\n"
    "v_cvt_pk_bf16_f32 v58, v60, v61\n"
    "v_cvt_pk_bf16_f32 v59, v62, v63\n"
    "v_mfma_f32_16x16x32_bf16 v[64:67], v[224:227], v[28:31], v[64:67]\n"
    "s_waitcnt lgkmcnt(2)\n"
    "v_mfma_f32_16x16x32_bf16 v[68:71], v[228:231], v[28:31], v[68:71]\n"
    "ds_read_b128 v[28:31], v9 offset:35840\n"
    "v_mfma_f32_16x16x32_bf16 v[32:35], v[232:235], v[16:19], v[32:35]\n"
    "v_mfma_f32_16x16x32_bf16 v[36:39], v[236:239], v[16:19], v[36:39]\n"
    "buffer_store_dwordx4 v[56:59], v1, s[36:39], s80 offen offset:1024 sc1\n"
    "v_mfma_f32_16x16x32_bf16 v[64:67], v[248:251], v[16:19], v[64:67]\n"
    "s_waitcnt lgkmcnt(2)\n"
    "v_mfma_f32_16x16x32_bf16 v[68:71], v[252:255], v[16:19], v[68:71]\n"
    "v_mfma_f32_16x16x32_bf16 v[40:43], v[184:187], v[20:23], v[40:43]\n"
    "v_mfma_f32_16x16x32_bf16 v[44:47], v[188:191], v[20:23], v[44:47]\n"
    "v_cvt_pk_bf16_f32 v32, v32, v33\n"
    "v_cvt_pk_bf16_f32 v33, v34, v35\n"
    "v_mfma_f32_16x16x32_bf16 v[72:75], v[200:203], v[20:23], v[72:75]\n"
    "s_waitcnt lgkmcnt(1)\n"
    "v_cvt_pk_bf16_f32 v34, v36, v37\n"
    "v_cvt_pk_bf16_f32 v35, v38, v39\n"
    "v_mfma_f32_16x16x32_bf16 v[76:79], v[204:207], v[20:23], v[76:79]\n"
    "v_cvt_pk_bf16_f32 v64, v64, v65\n"
    "v_mfma_f32_16x16x32_bf16 v[40:43], v[208:211], v[24:27], v[40:43]\n"
    "buffer_store_dwordx4 v[32:35], v1, s[36:39], s79 offen sc1\n"
    "v_cvt_pk_bf16_f32 v65, v66, v67\n"
    "v_mfma_f32_16x16x32_bf16 v[44:47], v[212:215], v[24:27], v[44:47]\n"
    "v_cvt_pk_bf16_f32 v66, v68, v69\n"
    "v_cvt_pk_bf16_f32 v67, v70, v71\n"
    "v_mfma_f32_16x16x32_bf16 v[72:75], v[224:227], v[24:27], v[72:75]\n"
    "s_waitcnt lgkmcnt(0)\n"
    "v_mfma_f32_16x16x32_bf16 v[76:79], v[228:231], v[24:27], v[76:79]\n"
    "v_mfma_f32_16x16x32_bf16 v[40:43], v[232:235], v[28:31], v[40:43]\n"
    "v_mfma_f32_16x16x32_bf16 v[44:47], v[236:239], v[28:31], v[44:47]\n"
    "buffer_store_dwordx4 v[64:67], v1, s[36:39], s73 offen sc1\n"
    "v_mfma_f32_16x16x32_bf16 v[72:75], v[248:251], v[28:31], v[72:75]\n"
    "v_mfma_f32_16x16x32_bf16 v[76:79], v[252:255], v[28:31], v[76:79]\n"
    "v_cvt_pk_bf16_f32 v40, v40, v41\n"
    "v_cvt_pk_bf16_f32 v41, v42, v43\n"
    "v_cvt_pk_bf16_f32 v42, v44, v45\n"
    "v_cvt_pk_bf16_f32 v43, v46, v47\n"
    "buffer_store_dwordx4 v[40:43], v1, s[36:39], s79 offen offset:1024 sc1\n"
    "v_cvt_pk_bf16_f32 v72, v72, v73\n"
    "v_cvt_pk_bf16_f32 v73, v74, v75\n"
    "s_nop 0\n"
    "v_cvt_pk_bf16_f32 v74, v76, v77\n"
    "v_cvt_pk_bf16_f32 v75, v78, v79\n"
    "buffer_store_dwordx4 v[72:75], v1, s[36:39], s73 offen offset:1024 sc1\n"
    "v_mov_b32 v12, s68\n"
    "s_mov_b32 s68, s71\n"
    "s_waitcnt vmcnt(6)\n"
    "s_branch .LgP_bar_%=\n"
    ".LgP_dma156_%=:\n"
    "v_mfma_f32_16x16x32_bf16 v[32:35], v[136:139], v[16:19], v[32:35]\n"
    "s_add_u32 m0, s77, s59\n"
    "s_add_u32 s77, s77, 8192\n"
    "buffer_load_dwordx4 v1, s[36:39], s76 offen sc1 lds\n"
    "v_mfma_f32_16x16x32_bf16 v[36:39], v[140:143], v[16:19], v[36:39]\n"
    "v_mfma_f32_16x16x32_bf16 v[48:51], v[16:19], v[144:147], v[48:51]\n"
    "v_mfma_f32_16x16x32_bf16 v[56:59], v[16:19], v[148:151], v[56:59]\n"
    "buffer_load_dwordx4 v1, s[36:39], s76 offen offset:1024 sc1 lds\n"
    "s_add_u32 s76, s76, 16384\n"
    "v_mfma_f32_16x16x32_bf16 v[64:67], v[152:155], v[16:19], v[64:67]\n"
    "s_waitcnt lgkmcnt(2)\n"
    "v_mfma_f32_16x16x32_bf16 v[68:71], v[156:159], v[16:19], v[68:71]\n"
    "ds_read_b128 v[16:19], v9 offset:30720\n"
    "v_mfma_f32_16x16x32_bf16 v[40:43], v[136:139], v[20:23], v[40:43]\n"
    "v_mfma_f32_16x16x32_bf16 v[44:47], v[140:143], v[20:23], v[44:47]\n"
    "s_add_u32 m0, s77, s59\n"
    "s_add_u32 s77, s77, 8192\n"
    "buffer_load_dwordx4 v1, s[36:39], s76 offen sc1 lds\n"
    "v_mfma_f32_16x16x32_bf16 v[52:55], v[20:23], v[144:147], v[52:55]\n"
    "v_mfma_f32_16x16x32_bf16 v[60:63], v[20:23], v[148:151], v[60:63]\n"
    "v_mfma_f32_16x16x32_bf16 v[72:75], v[152:155], v[20:23], v[72:75]\n"
    "s_waitcnt lgkmcnt(2)\n"
    "buffer_load_dwordx4 v1, s[36:39], s76 offen offset:1024 sc1 lds\n"
    "s_add_u32 s76, s76, 16384\n"
    "v_mfma_f32_16x16x32_bf16 v[76:79], v[156:159], v[20:23], v[76:79]\n"
    "ds_read_b128 v[20:23], v9 offset:31744\n"
    "v_mfma_f32_16x16x32_bf16 v[32:35], v[160:163], v[24:27], v[32:35]\n"
    "v_mfma_f32_16x16x32_bf16 v[36:39], v[164:167], v[24:27], v[36:39]\n"
    "v_mfma_f32_16x16x32_bf16 v[48:51], v[24:27], v[168:171], v[48:51]\n"
    "s_add_u32 m0, s77, s59\n"
    "s_add_u32 s77, s77, 8192\n"
    "buffer_load_dwordx4 v1, s[36:39], s76 offen sc1 lds\n"
    "v_mfma_f32_16x16x32_bf16 v[56:59], v[24:27], v[172:175], v[56:59]\n"
    "v_mfma_f32_16x16x32_bf16 v[64:67], v[176:179], v[24:27], v[64:67]\n"
    "s_waitcnt lgkmcnt(2)\n"
    "v_mfma_f32_16x16x32_bf16 v[68:71], v[180:183], v[24:27], v[68:71]\n"
    "ds_read_b128 v[24:27], v9 offset:32768\n"
    "v_mfma_f32_16x16x32_bf16 v[40:43], v[160:163], v[28:31], v[40:43]\n"
    "v_mfma_f32_16x16x32_bf16 v[44:47], v[164:167], v[28:31], v[44:47]\n"
    "buffer_load_dwordx4 v1, s[36:39], s76 offen offset:1024 sc1 lds\n"
    "s_add_u32 s76, s76, 16384\n"
    "v_mfma_f32_16x16x32_bf16 v[52:55], v[28:31], v[168:171], v[52:55]\n"
    "v_mfma_f32_16x16x32_bf16 v[60:63], v[28:31], v[172:175], v[60:63]\n"
    "v_mfma_f32_16x16x32_bf16 v[72:75], v[176:179], v[28:31], v[72:75]\n"
    "s_waitcnt lgkmcnt(2)\n"
    "s_add_u32 m0, s77, s59\n"
    "s_add_u32 s77, s77, 8192\n"
    "buffer_load_dwordx4 v1, s[36:39], s76 offen sc1 lds\n"
    "v_mfma_f32_16x16x32_bf16 v[76:79], v[180:183], v[28:31], v[76:79]\n"
    "ds_read_b128 v[28:31], v9 offset:33792\n"
    "v_mfma_f32_16x16x32_bf16 v[48:51], v[16:19], v[192:195], v[48:51]\n"
    "s_waitcnt lgkmcnt(2)\n"
    "v_mfma_f32_16x16x32_bf16 v[56:59], v[16:19], v[196:199], v[56:59]\n"
    "ds_read_b128 v[16:19], v9 offset:34816\n"
    "v_mfma_f32_16x16x32_bf16 v[52:55], v[20:23], v[192:195], v[52:55]\n"
    "s_waitcnt lgkmcnt(2)\n"
    "v_mfma_f32_16x16x32_bf16 v[60:63], v[20:23], v[196:199], v[60:63]\n"
    "ds_read_b128 v[20:23], v9 offset:35840\n"
    "v_mfma_f32_16x16x32_bf16 v[48:51], v[24:27], v[216:219], v[48:51]\n"
    "s_waitcnt lgkmcnt(2)\n"
    "buffer_load_dwordx4 v1, s[36:39], s76 offen offset:1024 sc1 lds\n"
    "s_add_u32 s76, s76, 16384\n"
    "v_mfma_f32_16x16x32_bf16 v[56:59], v[24:27], v[220:223], v[56:59]\n"
    "ds_read_b128 v[24:27], v9 offset:30720\n"
    "v_mfma_f32_16x16x32_bf16 v[52:55], v[28:31], v[216:219], v[52:55]\n"
    "s_waitcnt lgkmcnt(2)\n"
    "v_mfma_f32_16x16x32_bf16 v[60:63], v[28:31], v[220:223], v[60:63]\n"
    "ds_read_b128 v[28:31], v9 offset:32768\n"
    "v_mfma_f32_16x16x32_bf16 v[48:51], v[16:19], v[240:243], v[48:51]\n"
    "s_waitcnt lgkmcnt(2)\n"
    "v_mfma_f32_16x16x32_bf16 v[56:59], v[16:19], v[244:247], v[56:59]\n"
    "ds_read_b128 v[16:19], v9 offset:34816\n"
    "v_mfma_f32_16x16x32_bf16 v[52:55], v[20:23], v[240:243], v[52:55]\n"
    "s_waitcnt lgkmcnt(2)\n"
    "s_mov_b32 m0, s77\n"
    "s_nop 0\n"
    "buffer_load_dwordx4 v8, s[36:39], s76 offen sc1 lds\n"
    "v_mfma_f32_16x16x32_bf16 v[60:63], v[20:23], v[244:247], v[60:63]\n"
    "ds_read_b128 v[20:23], v9 offset:31744\n"
    "v_mfma_f32_16x16x32_bf16 v[32:35], v[184:187], v[24:27], v[32:35]\n"
    "v_mfma_f32_16x16x32_bf16 v[36:39], v[188:191], v[24:27], v[36:39]\n"
    "v_cvt_pk_bf16_f32 v48, v48, v49\n"
    "v_cvt_pk_bf16_f32 v49, v50, v51\n"
    "v_mfma_f32_16x16x32_bf16 v[64:67], v[200:203], v[24:27], v[64:67]\n"
    "s_waitcnt lgkmcnt(2)\n"
    "v_cvt_pk_bf16_f32 v50, v52, v53\n"
    "v_cvt_pk_bf16_f32 v51, v54, v55\n"
    "v_mfma_f32_16x16x32_bf16 v[68:71], v[204:207], v[24:27], v[68:71]\n"
    "ds_read_b128 v[24:27], v9 offset:33792\n"
    "v_cvt_pk_bf16_f32 v56, v56, v57\n"
    "v_mfma_f32_16x16x32_bf16 v[32:35], v[208:211], v[28:31], v[32:35]\n"
    "buffer_store_dwordx4 v[48:51], v1, s[36:39], s80 offen sc1\n"
    "v_cvt_pk_bf16_f32 v57, v58, v59\n"
    "v_mfma_f32_16x16x32_bf16 v[36:39], v[212:215], v[28:31], v[36:39]\n"
    "v_cvt_pk_bf16_f32 v58, v60, v61\n"
    "v_cvt_pk_bf16_f32 v59, v62, v63\n"
    "v_mfma_f32_16x16x32_bf16 v[64:67], v[224:227], v[28:31], v[64:67]\n"
    "s_waitcnt lgkmcnt(2)\n"
    "v_mfma_f32_16x16x32_bf16 v[68:71], v[228:231], v[28:31], v[68:71]\n"
    "ds_read_b128 v[28:31], v9 offset:35840\n"
    "v_mfma_f32_16x16x32_bf16 v[32:35], v[232:235], v[16:19], v[32:35]\n"
    "v_mfma_f32_16x16x32_bf16 v[36:39], v[236:239], v[16:19], v[36:39]\n"
    "buffer_store_dwordx4 v[56:59], v1, s[36:39], s80 offen offset:1024 sc1\n"
    "v_mfma_f32_16x16x32_bf16 v[64:67], v[248:251], v[16:19], v[64:67]\n"
    "s_waitcnt lgkmcnt(2)\n"
    "v_mfma_f32_16x16x32_bf16 v[68:71], v[252:255], v[16:19], v[68:71]\n"
    "v_mfma_f32_16x16x32_bf16 v[40:43], v[184:187], v[20:23], v[40:43]\n"
    "v_mfma_f32_16x16x32_bf16 v[44:47], v[188:191], v[20:23], v[44:47]\n"
    "v_cvt_pk_bf16_f32 v32, v32, v33\n"
    "v_cvt_pk_bf16_f32 v33, v34, v35\n"
    "v_mfma_f32_16x16x32_bf16 v[72:75], v[200:203], v[20:23], v[72:75]\n"
    "s_waitcnt lgkmcnt(1)\n"
    "v_cvt_pk_bf16_f32 v34, v36, v37\n"
    "v_cvt_pk_bf16_f32 v35, v38, v39\n"
    "v_mfma_f32_16x16x32_bf16 v[76:79], v[204:207], v[20:23], v[76:79]\n"
    "v_cvt_pk_bf16_f32 v64, v64, v65\n"
    "v_mfma_f32_16x16x32_bf16 v[40:43], v[208:211], v[24:27], v[40:43]\n"
    "buffer_store_dwordx4 v[32:35], v1, s[36:39], s79 offen sc1\n"
    "v_cvt_pk_bf16_f32 v65, v66, v67\n"
    "v_mfma_f32_16x16x32_bf16 v[44:47], v[212:215], v[24:27], v[44:47]\n"
    "v_cvt_pk_bf16_f32 v66, v68, v69\n"
    "v_cvt_pk_bf16_f32 v67, v70, v71\n"
    "v_mfma_f32_16x16x32_bf16 v[72:75], v[224:227], v[24:27], v[72:75]\n"
    "s_waitcnt lgkmcnt(0)\n"
    "v_mfma_f32_16x16x32_bf16 v[76:79], v[228:231], v[24:27], v[76:79]\n"
    "v_mfma_f32_16x16x32_bf16 v[40:43], v[232:235], v[28:31], v[40:43]\n"
    "v_mfma_f32_16x16x32_bf16 v[44:47], v[236:239], v[28:31], v[44:47]\n"
    "buffer_store_dwordx4 v[64:67], v1, s[36:39], s73 offen sc1\n"
    "v_mfma_f32_16x16x32_bf16 v[72:75], v[248:251], v[28:31], v[72:75]\n"
    "v_mfma_f32_16x16x32_bf16 v[76:79], v[252:255], v[28:31], v[76:79]\n"
    "v_cvt_pk_bf16_f32 v40, v40, v41\n"
    "v_cvt_pk_bf16_f32 v41, v42, v43\n"
    "v_cvt_pk_bf16_f32 v42, v44, v45\n"
    "v_cvt_pk_bf16_f32 v43, v46, v47\n"
    "buffer_store_dwordx4 v[40:43], v1, s[36:39], s79 offen offset:1024 sc1\n"
    "v_cvt_pk_bf16_f32 v72, v72, v73\n"
    "v_cvt_pk_bf16_f32 v73, v74, v75\n"
    "s_nop 0\n"
    "v_cvt_pk_bf16_f32 v74, v76, v77\n"
    "v_cvt_pk_bf16_f32 v75, v78, v79\n"
    "buffer_store_dwordx4 v[72:75], v1, s[36:39], s73 offen offset:1024 sc1\n"
    "v_mov_b32 v12, s68\n"
    "s_mov_b32 s68, s71\n"
    "s_waitcnt vmcnt(6)\n"
    "s_branch .LgP_bar_%=\n"
    ".LgP_dma176_%=:\n"
    "v_mfma_f32_16x16x32_bf16 v[52:55], v[28:31], v[168:171], v[52:55]\n"
    "s_add_u32 m0, s77, s59\n"
    "s_add_u32 s77, s77, 8192\n"
    "buffer_load_dwordx4 v1, s[36:39], s76 offen sc1 lds\n"
    "v_mfma_f32_16x16x32_bf16 v[60:63], v[28:31], v[172:175], v[60:63]\n"
    "v_mfma_f32_16x16x32_bf16 v[72:75], v[176:179], v[28:31], v[72:75]\n"
    "s_waitcnt lgkmcnt(2)\n"
    "v_mfma_f32_16x16x32_bf16 v[76:79], v[180:183], v[28:31], v[76:79]\n"
    "ds_read_b128 v[28:31], v9 offset:33792\n"
    "v_mfma_f32_16x16x32_bf16 v[48:51], v[16:19], v[192:195], v[48:51]\n"
    "s_waitcnt lgkmcnt(2)\n"
    "buffer_load_dwordx4 v1, s[36:39], s76 offen offset:1024 sc1 lds\n"
    "s_add_u32 s76, s76, 16384\n"
    "v_mfma_f32_16x16x32_bf16 v[56:59], v[16:19], v[196:199], v[56:59]\n"
    "ds_read_b128 v[16:19], v9 offset:34816\n"
    "v_mfma_f32_16x16x32_bf16 v[52:55], v[20:23], v[192:195], v[52:55]\n"
    "s_waitcnt lgkmcnt(2)\n"
    "v_mfma_f32_16x16x32_bf16 v[60:63], v[20:23], v[196:199], v[60:63]\n"
    "ds_read_b128 v[20:23], v9 offset:35840\n"
    "v_mfma_f32_16x16x32_bf16 v[48:51], v[24:27], v[216:219], v[48:51]\n"
    "s_waitcnt lgkmcnt(2)\n"
    "v_mfma_f32_16x16x32_bf16 v[56:59], v[24:27], v[220:223], v[56:59]\n"
    "ds_read_b128 v[24:27], v9 offset:30720\n"
    "v_mfma_f32_16x16x32_bf16 v[52:55], v[28:31], v[216:219], v[52:55]\n"
    "s_waitcnt lgkmcnt(2)\n"
    "s_add_u32 m0, s77, s59\n"
    "s_add_u32 s77, s77, 8192\n"
    "buffer_load_dwordx4 v1, s[36:39], s76 offen sc1 lds\n"
    "v_mfma_f32_16x16x32_bf16 v[60:63], v[28:31], v[220:223], v[60:63]\n"
    "ds_read_b128 v[28:31], v9 offset:32768\n"
    "v_mfma_f32_16x16x32_bf16 v[48:51], v[16:19], v[240:243], v[48:51]\n"
    "s_waitcnt lgkmcnt(2)\n"
    "v_mfma_f32_16x16x32_bf16 v[56:59], v[16:19], v[244:247], v[56:59]\n"
    "ds_read_b128 v[16:19], v9 offset:34816\n"
    "v_mfma_f32_16x16x32_bf16 v[52:55], v[20:23], v[240:243], v[52:55]\n"
    "s_waitcnt lgkmcnt(2)\n"
    "v_mfma_f32_16x16x32_bf16 v[60:63], v[20:23], v[244:247], v[60:63]\n"
    "ds_read_b128 v[20:23], v9 offset:31744\n"
    "v_mfma_f32_16x16x32_bf16 v[32:35], v[184:187], v[24:27], v[32:35]\n"
    "buffer_load_dwordx4 v1, s[36:39], s76 offen offset:1024 sc1 lds\n"
    "s_add_u32 s76, s76, 16384\n"
    "v_mfma_f32_16x16x32_bf16 v[36:39], v[188:191], v[24:27], v[36:39]\n"
    "v_cvt_pk_bf16_f32 v48, v48, v49\n"
    "v_cvt_pk_bf16_f32 v49, v50, v51\n"
    "v_mfma_f32_16x16x32_bf16 v[64:67], v[200:203], v[24:27], v[64:67]\n"
    "s_waitcnt lgkmcnt(2)\n"
    "v_cvt_pk_bf16_f32 v50, v52, v53\n"
    "v_cvt_pk_bf16_f32 v51, v54, v55\n"
    "v_mfma_f32_16x16x32_bf16 v[68:71], v[204:207], v[24:27], v[68:71]\n"
    "ds_read_b128 v[24:27], v9 offset:33792\n"
    "v_cvt_pk_bf16_f32 v56, v56, v57\n"
    "v_mfma_f32_16x16x32_bf16 v[32:35], v[208:211], v[28:31], v[32:35]\n"
    "s_add_u32 m0, s77, s59\n"
    "s_add_u32 s77, s77, 8192\n"
    "buffer_load_dwordx4 v1, s[36:39], s76 offen sc1 lds\n"
    "buffer_store_dwordx4 v[48:51], v1, s[36:39], s80 offen sc1\n"
    "v_cvt_pk_bf16_f32 v57, v58, v59\n"
    "v_mfma_f32_16x16x32_bf16 v[36:39], v[212:215], v[28:31], v[36:39]\n"
    "v_cvt_pk_bf16_f32 v58, v60, v61\n"
    "v_cvt_pk_bf16_f32 v59, v62, v63\n"
    "v_mfma_f32_16x16x32_bf16 v[64:67], v[224:227], v[28:31], v[64:67]\n"
    "s_waitcnt lgkmcnt(2)\n"
    "v_mfma_f32_16x16x32_bf16 v[68:71], v[228:231], v[28:31], v[68:71]\n"
    "ds_read_b128 v[28:31], v9 offset:35840\n"
    "v_mfma_f32_16x16x32_bf16 v[32:35], v[232:235], v[16:19], v[32:35]\n"
    "buffer_load_dwordx4 v1, s[36:39], s76 offen offset:1024 sc1 lds\n"
    "s_add_u32 s76, s76, 16384\n"
    "v_mfma_f32_16x16x32_bf16 v[36:39], v[236:239], v[16:19], v[36:39]\n"
    "buffer_store_dwordx4 v[56:59], v1, s[36:39], s80 offen offset:1024 sc1\n"
    "v_mfma_f32_16x16x32_bf16 v[64:67], v[248:251], v[16:19], v[64:67]\n"
    "s_waitcnt lgkmcnt(2)\n"
    "v_mfma_f32_16x16x32_bf16 v[68:71], v[252:255], v[16:19], v[68:71]\n"
    "v_mfma_f32_16x16x32_bf16 v[40:43], v[184:187], v[20:23], v[40:43]\n"
    "s_add_u32 m0, s77, s59\n"
    "s_add_u32 s77, s77, 8192\n"
    "buffer_load_dwordx4 v1, s[36:39], s76 offen sc1 lds\n"
    "v_mfma_f32_16x16x32_bf16 v[44:47], v[188:191], v[20:23], v[44:47]\n"
    "v_cvt_pk_bf16_f32 v32, v32, v33\n"
    "v_cvt_pk_bf16_f32 v33, v34, v35\n"
    "v_mfma_f32_16x16x32_bf16 v[72:75], v[200:203], v[20:23], v[72:75]\n"
    "s_waitcnt lgkmcnt(1)\n"
    "v_cvt_pk_bf16_f32 v34, v36, v37\n"
    "v_cvt_pk_bf16_f32 v35, v38, v39\n"
    "v_mfma_f32_16x16x32_bf16 v[76:79], v[204:207], v[20:23], v[76:79]\n"
    "v_cvt_pk_bf16_f32 v64, v64, v65\n"
    "v_mfma_f32_16x16x32_bf16 v[40:43], v[208:211], v[24:27], v[40:43]\n"
    "buffer_load_dwordx4 v1, s[36:39], s76 offen offset:1024 sc1 lds\n"
    "s_add_u32 s76, s76, 16384\n"
    "buffer_store_dwordx4 v[32:35], v1, s[36:39], s79 offen sc1\n"
    "v_cvt_pk_bf16_f32 v65, v66, v67\n"
    "v_mfma_f32_16x16x32_bf16 v[44:47], v[212:215], v[24:27], v[44:47]\n"
    "v_cvt_pk_bf16_f32 v66, v68, v69\n"
    "v_cvt_pk_bf16_f32 v67, v70, v71\n"
    "v_mfma_f32_16x16x32_bf16 v[72:75], v[224:227], v[24:27], v[72:75]\n"
    "s_waitcnt lgkmcnt(0)\n"
    "v_mfma_f32_16x16x32_bf16 v[76:79], v[228:231], v[24:27], v[76:79]\n"
    "v_mfma_f32_16x16x32_bf16 v[40:43], v[232:235], v[28:31], v[40:43]\n"
    "s_mov_b32 m0, s77\n"
    "s_nop 0\n"
    "buffer_load_dwordx4 v8, s[36:39], s76 offen sc1 lds\n"
    "v_mfma_f32_16x16x32_bf16 v[44:47], v[236:239], v[28:31], v[44:47]\n"
    "buffer_store_dwordx4 v[64:67], v1, s[36:39], s73 offen sc1\n"
    "v_mfma_f32_16x16x32_bf16 v[72:75], v[248:251], v[28:31], v[72:75]\n"
    "v_mfma_f32_16x16x32_bf16 v[76:79], v[252:255], v[28:31], v[76:79]\n"
    "v_cvt_pk_bf16_f32 v40, v40, v41\n"
    "v_cvt_pk_bf16_f32 v41, v42, v43\n"
    "v_cvt_pk_bf16_f32 v42, v44, v45\n"
    "v_cvt_pk_bf16_f32 v43, v46, v47\n"
    "buffer_store_dwordx4 v[40:43], v1, s[36:39], s79 offen offset:1024 sc1\n"
    "v_cvt_pk_bf16_f32 v72, v72, v73\n"
    "v_cvt_pk_bf16_f32 v73, v74, v75\n"
    "s_nop 0\n"
    "v_cvt_pk_bf16_f32 v74, v76, v77\n"
    "v_cvt_pk_bf16_f32 v75, v78, v79\n"
    "buffer_store_dwordx4 v[72:75], v1, s[36:39], s73 offen offset:1024 sc1\n"
    "v_mov_b32 v12, s68\n"
    "s_mov_b32 s68, s71\n"
    "s_waitcnt vmcnt(3)\n"
    "s_branch .LgP_bar_%=\n"
    ".LgP_dma196_%=:\n"
    "v_mfma_f32_16x16x32_bf16 v[32:35], v[208:211], v[28:31], v[32:35]\n"
    "s_add_u32 m0, s77, s59\n"
    "s_add_u32 s77, s77, 8192\n"
    "buffer_load_dwordx4 v1, s[36:39], s76 offen sc1 lds\n"
    "buffer_store_dwordx4 v[48:51], v1, s[36:39], s80 offen sc1\n"
    "v_cvt_pk_bf16_f32 v57, v58, v59\n"
    "v_mfma_f32_16x16x32_bf16 v[36:39], v[212:215], v[28:31], v[36:39]\n"
    "v_cvt_pk_bf16_f32 v58, v60, v61\n"
    "v_cvt_pk_bf16_f32 v59, v62, v63\n"
    "v_mfma_f32_16x16x32_bf16 v[64:67], v[224:227], v[28:31], v[64:67]\n"
    "s_waitcnt lgkmcnt(2)\n"
    "v_mfma_f32_16x16x32_bf16 v[68:71], v[228:231], v[28:31], v[68:71]\n"
    "ds_read_b128 v[28:31], v9 offset:35840\n"
    "v_mfma_f32_16x16x32_bf16 v[32:35], v[232:235], v[16:19], v[32:35]\n"
    "buffer_load_dwordx4 v1, s[36:39], s76 offen offset:1024 sc1 lds\n"
    "s_add_u32 s76, s76, 16384\n"
    "v_mfma_f32_16x16x32_bf16 v[36:39], v[236:239], v[16:19], v[36:39]\n"
    "buffer_store_dwordx4 v[56:59], v1, s[36:39], s80 offen offset:1024 sc1\n"
    "v_mfma_f32_16x16x32_bf16 v[64:67], v[248:251], v[16:19], v[64:67]\n"
    "s_waitcnt lgkmcnt(2)\n"
    "v_mfma_f32_16x16x32_bf16 v[68:71], v[252:255], v[16:19], v[68:71]\n"
    "v_mfma_f32_16x16x32_bf16 v[40:43], v[184:187], v[20:23], v[40:43]\n"
    "v_mfma_f32_16x16x32_bf16 v[44:47], v[188:191], v[20:23], v[44:47]\n"
    "s_add_u32 m0, s77, s59\n"
    "s_add_u32 s77, s77, 8192\n"
    "buffer_load_dwordx4 v1, s[36:39], s76 offen sc1 lds\n"
    "v_cvt_pk_bf16_f32 v32, v32, v33\n"
    "v_cvt_pk_bf16_f32 v33, v34, v35\n"
    "v_mfma_f32_16x16x32_bf16 v[72:75], v[200:203], v[20:23], v[72:75]\n"
    "s_waitcnt lgkmcnt(1)\n"
    "v_cvt_pk_bf16_f32 v34, v36, v37\n"
    "v_cvt_pk_bf16_f32 v35, v38, v39\n"
    "v_mfma_f32_16x16x32_bf16 v[76:79], v[204:207], v[20:23], v[76:79]\n"
    "v_cvt_pk_bf16_f32 v64, v64, v65\n"
    "v_mfma_f32_16x16x32_bf16 v[40:43], v[208:211], v[24:27], v[40:43]\n"
    "buffer_store_dwordx4 v[32:35], v1, s[36:39], s79 offen sc1\n"
    "v_cvt_pk_bf16_f32 v65, v66, v67\n"
    "v_mfma_f32_16x16x32_bf16 v[44:47], v[212:215], v[24:27], v[44:47]\n"
    "v_cvt_pk_bf16_f32 v66, v68, v69\n"
    "v_cvt_pk_bf16_f32 v67, v70, v71\n"
    "v_mfma_f32_16x16x32_bf16 v[72:75], v[224:227], v[24:27], v[72:75]\n"
    "s_waitcnt lgkmcnt(0)\n"
    "buffer_load_dwordx4 v1, s[36:39], s76 offen offset:1024 sc1 lds\n"
    "s_add_u32 s76, s76, 16384\n"
    "v_mfma_f32_16x16x32_bf16 v[76:79], v[228:231], v[24:27], v[76:79]\n"
    "v_mfma_f32_16x16x32_bf16 v[40:43], v[232:235], v[28:31], v[40:43]\n"
    "v_mfma_f32_16x16x32_bf16 v[44:47], v[236:239], v[28:31], v[44:47]\n"
    "buffer_store_dwordx4 v[64:67], v1, s[36:39], s73 offen sc1\n"
    "v_mfma_f32_16x16x32_bf16 v[72:75], v[248:251], v[28:31], v[72:75]\n"
    "v_mfma_f32_16x16x32_bf16 v[76:79], v[252:255], v[28:31], v[76:79]\n"
    "v_cvt_pk_bf16_f32 v40, v40, v41\n"
    "v_cvt_pk_bf16_f32 v41, v42, v43\n"
    "v_cvt_pk_bf16_f32 v42, v44, v45\n"
    "v_cvt_pk_bf16_f32 v43, v46, v47\n"
    "buffer_store_dwordx4 v[40:43], v1, s[36:39], s79 offen offset:1024 sc1\n"
    "v_cvt_pk_bf16_f32 v72, v72, v73\n"
    "v_cvt_pk_bf16_f32 v73, v74, v75\n"
    "s_nop 0\n"
    "v_cvt_pk_bf16_f32 v74, v76, v77\n"
    "v_cvt_pk_bf16_f32 v75, v78, v79\n"
    "buffer_store_dwordx4 v[72:75], v1, s[36:39], s73 offen offset:1024 sc1\n"
    "v_mov_b32 v12, s68\n"
    "s_mov_b32 s68, s71\n"
    "s_add_u32 m0, s77, s59\n"
    "s_add_u32 s77, s77, 8192\n"
    "buffer_load_dwordx4 v1, s[36:39], s76 offen sc1 lds\n"
    "buffer_load_dwordx4 v1, s[36:39], s76 offen offset:1024 sc1 lds\n"
    "s_add_u32 s76, s76, 16384\n"
    "s_add_u32 m0, s77, s59\n"
    "s_add_u32 s77, s77, 8192\n"
    "buffer_load_dwordx4 v1, s[36:39], s76 offen sc1 lds\n"
    "buffer_load_dwordx4 v1, s[36:39], s76 offen offset:1024 sc1 lds\n"
    "s_add_u32 s76, s76, 16384\n"
    "s_mov_b32 m0, s77\n"
    "s_nop 0\n"
    "buffer_load_dwordx4 v8, s[36:39], s76 offen sc1 lds\n"
    "s_waitcnt vmcnt(5)\n"
    "buffer_atomic_smax v12, v14, s[36:39], 0 offen sc1\n"
    "s_waitcnt vmcnt(1)\n"
    "s_branch .LgP_bar_%=\n"
    ".LgP_idle_%=:\n"
    "s_or_b64 exec, vcc, 1\n"
    "buffer_atomic_smax v11, v2, s[36:39], 0 offen sc0 sc1\n"
    "s_mov_b64 exec, -1\n"
    "s_waitcnt vmcnt(1)\n"
    "buffer_atomic_smax v12, v14, s[36:39], 0 offen sc1\n"
    "s_branch .LgP_block0_%=\n"
    ".LgP_block_%=:\n"
    "v_add_u32 v13, v11, v3\n"
    "v_cmp_gt_i32 vcc, s71, v13\n"
    "s_nop 0\n"
    "s_or_b64 exec, vcc, 1\n"
    "buffer_atomic_smax v11, v2, s[36:39], 0 offen sc0 sc1\n"
    "s_mov_b64 exec, -1\n"
    "s_sleep 3\n"
    "s_waitcnt vmcnt(1)\n"
    ".LgP_block0_%=:\n"
    "v_add_u32 v13, v5, v3\n"
    "v_cmp_gt_i32 vcc, s71, v13\n"
    "s_cbranch_vccz .LgP_go_%=\n"
    "v_add_u32 v13, v5, v3\n"
    "v_cmp_gt_i32 vcc, s71, v13\n"
    "s_nop 0\n"
    "s_or_b64 exec, vcc, 1\n"
    "buffer_atomic_smax v5, v2, s[36:39], 0 offen sc0 sc1\n"
    "s_mov_b64 exec, -1\n"
    "s_sleep 3\n"
    "s_waitcnt vmcnt(1)\n"
    ".LgP_block1_%=:\n"
    "v_add_u32 v13, v11, v3\n"
    "v_cmp_gt_i32 vcc, s71, v13\n"
    "s_cbranch_vccz .LgP_go_%=\n"
    "s_sub_u32 s72, s72, 1\n"
    "s_cbranch_scc0 .LgP_block_%=\n"
    "s_mov_b32 s72, 0\n"
    ".LgP_go_%=:\n"
    "s_and_b32 s76, s71, s54\n"
    "s_mul_i32 s76, s76, s53\n"
    "s_add_u32 s76, s76, s52\n"
    "s_and_b32 s77, s71, 1\n"
    "s_mul_i32 s77, s77, 65536\n"
    "s_add_u32 s77, s77, s59\n"
    "s_add_u32 m0, s77, s59\n"
    "s_add_u32 s77, s77, 8192\n"
    "buffer_load_dwordx4 v1, s[36:39], s76 offen sc1 lds\n"
    "buffer_load_dwordx4 v1, s[36:39], s76 offen offset:1024 sc1 lds\n"
    "s_add_u32 s76, s76, 16384\n"
    "s_add_u32 m0, s77, s59\n"
    "s_add_u32 s77, s77, 8192\n"
    "buffer_load_dwordx4 v1, s[36:39], s76 offen sc1 lds\n"
    "buffer_load_dwordx4 v1, s[36:39], s76 offen offset:1024 sc1 lds\n"
    "s_add_u32 s76, s76, 16384\n"
    "s_add_u32 m0, s77, s59\n"
    "s_add_u32 s77, s77, 8192\n"
    "buffer_load_dwordx4 v1, s[36:39], s76 offen sc1 lds\n"
    "buffer_load_dwordx4 v1, s[36:39], s76 offen offset:1024 sc1 lds\n"
    "s_add_u32 s76, s76, 16384\n"
    "s_add_u32 m0, s77, s59\n"
    "s_add_u32 s77, s77, 8192\n"
    "buffer_load_dwordx4 v1, s[36:39], s76 offen sc1 lds\n"
    "buffer_load_dwordx4 v1, s[36:39], s76 offen offset:1024 sc1 lds\n"
    "s_add_u32 s76, s76, 16384\n"
    "s_mov_b32 m0, s77\n"
    "s_nop 0\n"
    "buffer_load_dwordx4 v8, s[36:39], s76 offen sc1 lds\n"
    "s_waitcnt vmcnt(0)\n"
    "s_branch .LgP_bar_%=\n"
    ".LgP_FIN_%=:\n"
  );
}
DEVI void role_up(const Params& p, int L, int j, int i, u32 dbg, unsigned long long tag) {
  const int lane = threadIdx.x & 63, w = threadIdx.x >> 6, col = (6 * j + i) * 192;
  const u32 wb = 576u * 4608 * 2 - col * 2;
  GEMM_DECL;
  g_w0 = make_srd(p.w1 + col, wb); g_w1 = make_srd(p.w3 + col, wb); g_w2 = g_w1;
  g_ldw = 9216;
  g_in_base = pgb(L, B_XN2); g_in_stride = SLOT; g_in_mask = RX - 1;
  g_out_base = pgb(L, B_H + 9 * j); g_out_kb = 12 * i; g_out_mask = RH - 1;
  g_flag = lane < 3 ? mbox(L, j, SL_DOWN + lane, 4 * i + w) : OOB;
  g_paddr = mbox(L, j, SL_UP + i, lane);
  g_pbias = lane < 16 ? 0 : (lane < 19 ? RH : 1000);
  v16u sa, sb, va;
  gemm_args(GEMM_ARGS, p, dbg, tag, sa, sb, va);
  RUN_ASM(
    "s_nop 4\n"
    "s_getpc_b64 s[84:85]\n"
    ".LgUP_PC_%=:\n"
    "s_and_b32 s85, s85, 0xffff\n"
    "s_mov_b32 s86, .LgUP_FIN_%=-.LgUP_PC_%=\n"
    "s_mov_b32 s87, 0x20000\n"
    "v_mbcnt_lo_u32_b32 v40, -1, 0\n"
    "v_mbcnt_hi_u32_b32 v40, -1, v40\n"
    "v_lshlrev_b32 v40, 7, v40\n"
    "buffer_load_dword v41, v40, s[84:87], 0 offen\n"
    "v_add_u32 v40, 8192, v40\n"
    "buffer_load_dword v41, v40, s[84:87], 0 offen\n"
    "v_add_u32 v40, 8192, v40\n"
    "buffer_load_dword v41, v40, s[84:87], 0 offen\n"
    "v_add_u32 v8, s59, v0\n"
    "s_mov_b32 s68, 0\n"
    "s_mov_b32 s71, 0\n"
    "s_mov_b32 s78, 65536\n"
    "v_add_u32 v9, s78, v0\n"
    "s_mov_b32 s64, 0xbfb8aa3b\n"
    "s_mov_b32 s72, 262144\n"
    "s_add_u32 m0, s59, 0\n"
    "s_mov_b32 s74, 0\n"
    "buffer_load_dwordx4 v5, s[40:43], s74 offen lds\n"
    "s_add_u32 m0, s59, 4096\n"
    "s_mov_b32 s74, 128\n"
    "buffer_load_dwordx4 v5, s[40:43], s74 offen lds\n"
    "s_add_u32 m0, s59, 8192\n"
    "s_mov_b32 s74, 256\n"
    "buffer_load_dwordx4 v5, s[40:43], s74 offen lds\n"
    "s_add_u32 m0, s59, 12288\n"
    "s_mov_b32 s74, 0\n"
    "buffer_load_dwordx4 v5, s[44:47], s74 offen lds\n"
    "s_add_u32 m0, s59, 16384\n"
    "s_mov_b32 s74, 128\n"
    "buffer_load_dwordx4 v5, s[44:47], s74 offen lds\n"
    "s_add_u32 m0, s59, 20480\n"
    "s_mov_b32 s74, 256\n"
    "buffer_load_dwordx4 v5, s[44:47], s74 offen lds\n"
    "s_add_u32 m0, s59, 24576\n"
    "s_mov_b32 s74, 294912\n"
    "buffer_load_dwordx4 v5, s[40:43], s74 offen lds\n"
    "s_add_u32 m0, s59, 28672\n"
    "s_mov_b32 s74, 295040\n"
    "buffer_load_dwordx4 v5, s[40:43], s74 offen lds\n"
    "s_add_u32 m0, s59, 32768\n"
    "s_mov_b32 s74, 295168\n"
    "buffer_load_dwordx4 v5, s[40:43], s74 offen lds\n"
    "s_add_u32 m0, s59, 36864\n"
    "s_mov_b32 s74, 294912\n"
    "buffer_load_dwordx4 v5, s[44:47], s74 offen lds\n"
    "s_add_u32 m0, s59, 40960\n"
    "s_mov_b32 s74, 295040\n"
    "buffer_load_dwordx4 v5, s[44:47], s74 offen lds\n"
    "s_add_u32 m0, s59, 45056\n"
    "s_mov_b32 s74, 295168\n"
    "buffer_load_dwordx4 v5, s[44:47], s74 offen lds\n"
    "s_add_u32 m0, s59, 49152\n"
    "s_mov_b32 s74, 589824\n"
    "buffer_load_dwordx4 v5, s[40:43], s74 offen lds\n"
    "s_add_u32 m0, s59, 53248\n"
    "s_mov_b32 s74, 589952\n"
    "buffer_load_dwordx4 v5, s[40:43], s74 offen lds\n"
    "s_add_u32 m0, s59, 57344\n"
    "s_mov_b32 s74, 590080\n"
    "buffer_load_dwordx4 v5, s[40:43], s74 offen lds\n"
    "s_add_u32 m0, s59, 61440\n"
    "s_mov_b32 s74, 589824\n"
    "buffer_load_dwordx4 v5, s[44:47], s74 offen lds\n"
    "s_add_u32 m0, s59, 65536\n"
    "s_mov_b32 s74, 589952\n"
    "buffer_load_dwordx4 v5, s[44:47], s74 offen lds\n"
    "s_add_u32 m0, s59, 69632\n"
    "s_mov_b32 s74, 590080\n"
    "buffer_load_dwordx4 v5, s[44:47], s74 offen lds\n"
    "s_add_u32 m0, s59, 73728\n"
    "s_mov_b32 s74, 884736\n"
    "buffer_load_dwordx4 v5, s[40:43], s74 offen lds\n"
    "s_add_u32 m0, s59, 77824\n"
    "s_mov_b32 s74, 884864\n"
    "buffer_load_dwordx4 v5, s[40:43], s74 offen lds\n"
    "s_add_u32 m0, s59, 81920\n"
    "s_mov_b32 s74, 884992\n"
    "buffer_load_dwordx4 v5, s[40:43], s74 offen lds\n"
    "s_add_u32 m0, s59, 86016\n"
    "s_mov_b32 s74, 884736\n"
    "buffer_load_dwordx4 v5, s[44:47], s74 offen lds\n"
    "s_add_u32 m0, s59, 90112\n"
    "s_mov_b32 s74, 884864\n"
    "buffer_load_dwordx4 v5, s[44:47], s74 offen lds\n"
    "s_add_u32 m0, s59, 94208\n"
    "s_mov_b32 s74, 884992\n"
    "buffer_load_dwordx4 v5, s[44:47], s74 offen lds\n"
    "s_add_u32 m0, s59, 98304\n"
    "s_mov_b32 s74, 1179648\n"
    "buffer_load_dwordx4 v5, s[40:43], s74 offen lds\n"
    "s_add_u32 m0, s59, 102400\n"
    "s_mov_b32 s74, 1179776\n"
    "buffer_load_dwordx4 v5, s[40:43], s74 offen lds\n"
    "s_add_u32 m0, s59, 106496\n"
    "s_mov_b32 s74, 1179904\n"
    "buffer_load_dwordx4 v5, s[40:43], s74 offen lds\n"
    "s_add_u32 m0, s59, 110592\n"
    "s_mov_b32 s74, 1179648\n"
    "buffer_load_dwordx4 v5, s[44:47], s74 offen lds\n"
    "s_add_u32 m0, s59, 114688\n"
    "s_mov_b32 s74, 1179776\n"
    "buffer_load_dwordx4 v5, s[44:47], s74 offen lds\n"
    "s_add_u32 m0, s59, 118784\n"
    "s_mov_b32 s74, 1179904\n"
    "buffer_load_dwordx4 v5, s[44:47], s74 offen lds\n"
    "v_add_u32 v16, 49152, v6\n"
    "v_add_u32 v17, 98304, v6\n"
    "v_add_u32 v18, 49152, v7\n"
    "v_add_u32 v19, 98304, v7\n"
    "s_waitcnt vmcnt(24) lgkmcnt(0)\n"
    "s_barrier\n"
    "s_add_u32 m0, s59, 122880\n"
    "s_mov_b32 s74, 1474560\n"
    "buffer_load_dwordx4 v5, s[40:43], s74 offen lds\n"
    "s_add_u32 m0, s59, 126976\n"
    "s_mov_b32 s74, 1474688\n"
    "buffer_load_dwordx4 v5, s[40:43], s74 offen lds\n"
    "s_add_u32 m0, s59, 131072\n"
    "s_mov_b32 s74, 1474816\n"
    "buffer_load_dwordx4 v5, s[40:43], s74 offen lds\n"
    "s_add_u32 m0, s59, 135168\n"
    "s_mov_b32 s74, 1474560\n"
    "buffer_load_dwordx4 v5, s[44:47], s74 offen lds\n"
    "s_add_u32 m0, s59, 139264\n"
    "s_mov_b32 s74, 1474688\n"
    "buffer_load_dwordx4 v5, s[44:47], s74 offen lds\n"
    "s_add_u32 m0, s59, 143360\n"
    "s_mov_b32 s74, 1474816\n"
    "buffer_load_dwordx4 v5, s[44:47], s74 offen lds\n"
    "ds_read_b64_tr_b16 a[0:1], v6 offset:0\n"
    "ds_read_b64_tr_b16 a[2:3], v6 offset:512\n"
    "ds_read_b64_tr_b16 a[4:5], v6 offset:8\n"
    "ds_read_b64_tr_b16 a[6:7], v6 offset:520\n"
    "ds_read_b64_tr_b16 a[8:9], v7 offset:0\n"
    "ds_read_b64_tr_b16 a[10:11], v7 offset:512\n"
    "ds_read_b64_tr_b16 a[12:13], v6 offset:12288\n"
    "ds_read_b64_tr_b16 a[14:15], v6 offset:12800\n"
    "ds_read_b64_tr_b16 a[16:17], v6 offset:12296\n"
    "ds_read_b64_tr_b16 a[18:19], v6 offset:12808\n"
    "ds_read_b64_tr_b16 a[20:21], v7 offset:12288\n"
    "ds_read_b64_tr_b16 a[22:23], v7 offset:12800\n"
    "s_waitcnt vmcnt(24) lgkmcnt(0)\n"
    "s_barrier\n"
    "s_add_u32 m0, s59, 0\n"
    "s_mov_b32 s74, 1769472\n"
    "buffer_load_dwordx4 v5, s[40:43], s74 offen lds\n"
    "s_add_u32 m0, s59, 4096\n"
    "s_mov_b32 s74, 1769600\n"
    "buffer_load_dwordx4 v5, s[40:43], s74 offen lds\n"
    "s_add_u32 m0, s59, 8192\n"
    "s_mov_b32 s74, 1769728\n"
    "buffer_load_dwordx4 v5, s[40:43], s74 offen lds\n"
    "s_add_u32 m0, s59, 12288\n"
    "s_mov_b32 s74, 1769472\n"
    "buffer_load_dwordx4 v5, s[44:47], s74 offen lds\n"
    "s_add_u32 m0, s59, 16384\n"
    "s_mov_b32 s74, 1769600\n"
    "buffer_load_dwordx4 v5, s[44:47], s74 offen lds\n"
    "s_add_u32 m0, s59, 20480\n"
    "s_mov_b32 s74, 1769728\n"
    "buffer_load_dwordx4 v5, s[44:47], s74 offen lds\n"
    "ds_read_b64_tr_b16 a[24:25], v6 offset:24576\n"
    "ds_read_b64_tr_b16 a[26:27], v6 offset:25088\n"
    "ds_read_b64_tr_b16 a[28:29], v6 offset:24584\n"
    "ds_read_b64_tr_b16 a[30:31], v6 offset:25096\n"
    "ds_read_b64_tr_b16 a[32:33], v7 offset:24576\n"
    "ds_read_b64_tr_b16 a[34:35], v7 offset:25088\n"
    "ds_read_b64_tr_b16 a[36:37], v6 offset:36864\n"
    "ds_read_b64_tr_b16 a[38:39], v6 offset:37376\n"
    "ds_read_b64_tr_b16 a[40:41], v6 offset:36872\n"
    "ds_read_b64_tr_b16 a[42:43], v6 offset:37384\n"
    "ds_read_b64_tr_b16 a[44:45], v7 offset:36864\n"
    "ds_read_b64_tr_b16 a[46:47], v7 offset:37376\n"
    "s_waitcnt vmcnt(24) lgkmcnt(0)\n"
    "s_barrier\n"
    "s_add_u32 m0, s59, 24576\n"
    "s_mov_b32 s74, 2064384\n"
    "buffer_load_dwordx4 v5, s[40:43], s74 offen lds\n"
    "s_add_u32 m0, s59, 28672\n"
    "s_mov_b32 s74, 2064512\n"
    "buffer_load_dwordx4 v5, s[40:43], s74 offen lds\n"
    "s_add_u32 m0, s59, 32768\n"
    "s_mov_b32 s74, 2064640\n"
    "buffer_load_dwordx4 v5, s[40:43], s74 offen lds\n"
    "s_add_u32 m0, s59, 36864\n"
    "s_mov_b32 s74, 2064384\n"
    "buffer_load_dwordx4 v5, s[44:47], s74 offen lds\n"
    "s_add_u32 m0, s59, 40960\n"
    "s_mov_b32 s74, 2064512\n"
    "buffer_load_dwordx4 v5, s[44:47], s74 offen lds\n"
    "s_add_u32 m0, s59, 45056\n"
    "s_mov_b32 s74, 2064640\n"
    "buffer_load_dwordx4 v5, s[44:47], s74 offen lds\n"
    "ds_read_b64_tr_b16 a[48:49], v16 offset:0\n"
    "ds_read_b64_tr_b16 a[50:51], v16 offset:512\n"
    "ds_read_b64_tr_b16 a[52:53], v16 offset:8\n"
    "ds_read_b64_tr_b16 a[54:55], v16 offset:520\n"
    "ds_read_b64_tr_b16 a[56:57], v18 offset:0\n"
    "ds_read_b64_tr_b16 a[58:59], v18 offset:512\n"
    "ds_read_b64_tr_b16 a[60:61], v16 offset:12288\n"
    "ds_read_b64_tr_b16 a[62:63], v16 offset:12800\n"
    "ds_read_b64_tr_b16 a[64:65], v16 offset:12296\n"
    "ds_read_b64_tr_b16 a[66:67], v16 offset:12808\n"
    "ds_read_b64_tr_b16 a[68:69], v18 offset:12288\n"
    "ds_read_b64_tr_b16 a[70:71], v18 offset:12800\n"
    "s_waitcnt vmcnt(24) lgkmcnt(0)\n"
    "s_barrier\n"
    "s_add_u32 m0, s59, 49152\n"
    "s_mov_b32 s74, 2359296\n"
    "buffer_load_dwordx4 v5, s[40:43], s74 offen lds\n"
    "s_add_u32 m0, s59, 53248\n"
    "s_mov_b32 s74, 2359424\n"
    "buffer_load_dwordx4 v5, s[40:43], s74 offen lds\n"
    "s_add_u32 m0, s59, 57344\n"
    "s_mov_b32 s74, 2359552\n"
    "buffer_load_dwordx4 v5, s[40:43], s74 offen lds\n"
    "s_add_u32 m0, s59, 61440\n"
    "s_mov_b32 s74, 2359296\n"
    "buffer_load_dwordx4 v5, s[44:47], s74 offen lds\n"
    "s_add_u32 m0, s59, 65536\n"
    "s_mov_b32 s74, 2359424\n"
    "buffer_load_dwordx4 v5, s[44:47], s74 offen lds\n"
    "s_add_u32 m0, s59, 69632\n"
    "s_mov_b32 s74, 2359552\n"
    "buffer_load_dwordx4 v5, s[44:47], s74 offen lds\n"
    "ds_read_b64_tr_b16 a[72:73], v16 offset:24576\n"
    "ds_read_b64_tr_b16 a[74:75], v16 offset:25088\n"
    "ds_read_b64_tr_b16 a[76:77], v16 offset:24584\n"
    "ds_read_b64_tr_b16 a[78:79], v16 offset:25096\n"
    "ds_read_b64_tr_b16 a[80:81], v18 offset:24576\n"
    "ds_read_b64_tr_b16 a[82:83], v18 offset:25088\n"
    "ds_read_b64_tr_b16 a[84:85], v16 offset:36864\n"
    "ds_read_b64_tr_b16 a[86:87], v16 offset:37376\n"
    "ds_read_b64_tr_b16 a[88:89], v16 offset:36872\n"
    "ds_read_b64_tr_b16 a[90:91], v16 offset:37384\n"
    "ds_read_b64_tr_b16 a[92:93], v18 offset:36864\n"
    "ds_read_b64_tr_b16 a[94:95], v18 offset:37376\n"
    "s_waitcnt vmcnt(24) lgkmcnt(0)\n"
    "s_barrier\n"
    "s_add_u32 m0, s59, 73728\n"
    "s_mov_b32 s74, 2654208\n"
    "buffer_load_dwordx4 v5, s[40:43], s74 offen lds\n"
    "s_add_u32 m0, s59, 77824\n"
    "s_mov_b32 s74, 2654336\n"
    "buffer_load_dwordx4 v5, s[40:43], s74 offen lds\n"
    "s_add_u32 m0, s59, 81920\n"
    "s_mov_b32 s74, 2654464\n"
    "buffer_load_dwordx4 v5, s[40:43], s74 offen lds\n"
    "s_add_u32 m0, s59, 86016\n"
    "s_mov_b32 s74, 2654208\n"
    "buffer_load_dwordx4 v5, s[44:47], s74 offen lds\n"
    "s_add_u32 m0, s59, 90112\n"
    "s_mov_b32 s74, 2654336\n"
    "buffer_load_dwordx4 v5, s[44:47], s74 offen lds\n"
    "s_add_u32 m0, s59, 94208\n"
    "s_mov_b32 s74, 2654464\n"
    "buffer_load_dwordx4 v5, s[44:47], s74 offen lds\n"
    "ds_read_b64_tr_b16 a[96:97], v17 offset:0\n"
    "ds_read_b64_tr_b16 a[98:99], v17 offset:512\n"
    "ds_read_b64_tr_b16 a[100:101], v17 offset:8\n"
    "ds_read_b64_tr_b16 a[102:103], v17 offset:520\n"
    "ds_read_b64_tr_b16 a[104:105], v19 offset:0\n"
    "ds_read_b64_tr_b16 a[106:107], v19 offset:512\n"
    "ds_read_b64_tr_b16 a[108:109], v17 offset:12288\n"
    "ds_read_b64_tr_b16 a[110:111], v17 offset:12800\n"
    "ds_read_b64_tr_b16 a[112:113], v17 offset:12296\n"
    "ds_read_b64_tr_b16 a[114:115], v17 offset:12808\n"
    "ds_read_b64_tr_b16 a[116:117], v19 offset:12288\n"
    "ds_read_b64_tr_b16 a[118:119], v19 offset:12800\n"
    "s_waitcnt vmcnt(24) lgkmcnt(0)\n"
    "s_barrier\n"
    "s_add_u32 m0, s59, 98304\n"
    "s_mov_b32 s74, 2949120\n"
    "buffer_load_dwordx4 v5, s[40:43], s74 offen lds\n"
    "s_add_u32 m0, s59, 102400\n"
    "s_mov_b32 s74, 2949248\n"
    "buffer_load_dwordx4 v5, s[40:43], s74 offen lds\n"
    "s_add_u32 m0, s59, 106496\n"
    "s_mov_b32 s74, 2949376\n"
    "buffer_load_dwordx4 v5, s[40:43], s74 offen lds\n"
    "s_add_u32 m0, s59, 110592\n"
    "s_mov_b32 s74, 2949120\n"
    "buffer_load_dwordx4 v5, s[44:47], s74 offen lds\n"
    "s_add_u32 m0, s59, 114688\n"
    "s_mov_b32 s74, 2949248\n"
    "buffer_load_dwordx4 v5, s[44:47], s74 offen lds\n"
    "s_add_u32 m0, s59, 118784\n"
    "s_mov_b32 s74, 2949376\n"
    "buffer_load_dwordx4 v5, s[44:47], s74 offen lds\n"
    "ds_read_b64_tr_b16 a[120:121], v17 offset:24576\n"
    "ds_read_b64_tr_b16 a[122:123], v17 offset:25088\n"
    "ds_read_b64_tr_b16 a[124:125], v17 offset:24584\n"
    "ds_read_b64_tr_b16 a[126:127], v17 offset:25096\n"
    "ds_read_b64_tr_b16 a[128:129], v19 offset:24576\n"
    "ds_read_b64_tr_b16 a[130:131], v19 offset:25088\n"
    "ds_read_b64_tr_b16 a[132:133], v17 offset:36864\n"
    "ds_read_b64_tr_b16 a[134:135], v17 offset:37376\n"
    "ds_read_b64_tr_b16 a[136:137], v17 offset:36872\n"
    "ds_read_b64_tr_b16 a[138:139], v17 offset:37384\n"
    "ds_read_b64_tr_b16 a[140:141], v19 offset:36864\n"
    "ds_read_b64_tr_b16 a[142:143], v19 offset:37376\n"
    "s_waitcnt vmcnt(24) lgkmcnt(0)\n"
    "s_barrier\n"
    "s_add_u32 m0, s59, 122880\n"
    "s_mov_b32 s74, 3244032\n"
    "buffer_load_dwordx4 v5, s[40:43], s74 offen lds\n"
    "s_add_u32 m0, s59, 126976\n"
    "s_mov_b32 s74, 3244160\n"
    "buffer_load_dwordx4 v5, s[40:43], s74 offen lds\n"
    "s_add_u32 m0, s59, 131072\n"
    "s_mov_b32 s74, 3244288\n"
    "buffer_load_dwordx4 v5, s[40:43], s74 offen lds\n"
    "s_add_u32 m0, s59, 135168\n"
    "s_mov_b32 s74, 3244032\n"
    "buffer_load_dwordx4 v5, s[44:47], s74 offen lds\n"
    "s_add_u32 m0, s59, 139264\n"
    "s_mov_b32 s74, 3244160\n"
    "buffer_load_dwordx4 v5, s[44:47], s74 offen lds\n"
    "s_add_u32 m0, s59, 143360\n"
    "s_mov_b32 s74, 3244288\n"
    "buffer_load_dwordx4 v5, s[44:47], s74 offen lds\n"
    "ds_read_b64_tr_b16 a[144:145], v6 offset:0\n"
    "ds_read_b64_tr_b16 a[146:147], v6 offset:512\n"
    "ds_read_b64_tr_b16 a[148:149], v6 offset:8\n"
    "ds_read_b64_tr_b16 a[150:151], v6 offset:520\n"
    "ds_read_b64_tr_b16 a[152:153], v7 offset:0\n"
    "ds_read_b64_tr_b16 a[154:155], v7 offset:512\n"
    "ds_read_b64_tr_b16 a[156:157], v6 offset:12288\n"
    "ds_read_b64_tr_b16 a[158:159], v6 offset:12800\n"
    "ds_read_b64_tr_b16 a[160:161], v6 offset:12296\n"
    "ds_read_b64_tr_b16 a[162:163], v6 offset:12808\n"
    "ds_read_b64_tr_b16 a[164:165], v7 offset:12288\n"
    "ds_read_b64_tr_b16 a[166:167], v7 offset:12800\n"
    "s_waitcnt vmcnt(24) lgkmcnt(0)\n"
    "s_barrier\n"
    "s_add_u32 m0, s59, 0\n"
    "s_mov_b32 s74, 3538944\n"
    "buffer_load_dwordx4 v5, s[40:43], s74 offen lds\n"
    "s_add_u32 m0, s59, 4096\n"
    "s_mov_b32 s74, 3539072\n"
    "buffer_load_dwordx4 v5, s[40:43], s74 offen lds\n"
    "s_add_u32 m0, s59, 8192\n"
    "s_mov_b32 s74, 3539200\n"
    "buffer_load_dwordx4 v5, s[40:43], s74 offen lds\n"
    "s_add_u32 m0, s59, 12288\n"
    "s_mov_b32 s74, 3538944\n"
    "buffer_load_dwordx4 v5, s[44:47], s74 offen lds\n"
    "s_add_u32 m0, s59, 16384\n"
    "s_mov_b32 s74, 3539072\n"
    "buffer_load_dwordx4 v5, s[44:47], s74 offen lds\n"
    "s_add_u32 m0, s59, 20480\n"
    "s_mov_b32 s74, 3539200\n"
    "buffer_load_dwordx4 v5, s[44:47], s74 offen lds\n"
    "ds_read_b64_tr_b16 a[168:169], v6 offset:24576\n"
    "ds_read_b64_tr_b16 a[170:171], v6 offset:25088\n"
    "ds_read_b64_tr_b16 a[172:173], v6 offset:24584\n"
    "ds_read_b64_tr_b16 a[174:175], v6 offset:25096\n"
    "ds_read_b64_tr_b16 a[176:177], v7 offset:24576\n"
    "ds_read_b64_tr_b16 a[178:179], v7 offset:25088\n"
    "ds_read_b64_tr_b16 a[180:181], v6 offset:36864\n"
    "ds_read_b64_tr_b16 a[182:183], v6 offset:37376\n"
    "ds_read_b64_tr_b16 a[184:185], v6 offset:36872\n"
    "ds_read_b64_tr_b16 a[186:187], v6 offset:37384\n"
    "ds_read_b64_tr_b16 a[188:189], v7 offset:36864\n"
    "ds_read_b64_tr_b16 a[190:191], v7 offset:37376\n"
    "s_waitcnt vmcnt(24) lgkmcnt(0)\n"
    "s_barrier\n"
    "s_add_u32 m0, s59, 24576\n"
    "s_mov_b32 s74, 3833856\n"
    "buffer_load_dwordx4 v5, s[40:43], s74 offen lds\n"
    "s_add_u32 m0, s59, 28672\n"
    "s_mov_b32 s74, 3833984\n"
    "buffer_load_dwordx4 v5, s[40:43], s74 offen lds\n"
    "s_add_u32 m0, s59, 32768\n"
    "s_mov_b32 s74, 3834112\n"
    "buffer_load_dwordx4 v5, s[40:43], s74 offen lds\n"
    "s_add_u32 m0, s59, 36864\n"
    "s_mov_b32 s74, 3833856\n"
    "buffer_load_dwordx4 v5, s[44:47], s74 offen lds\n"
    "s_add_u32 m0, s59, 40960\n"
    "s_mov_b32 s74, 3833984\n"
    "buffer_load_dwordx4 v5, s[44:47], s74 offen lds\n"
    "s_add_u32 m0, s59, 45056\n"
    "s_mov_b32 s74, 3834112\n"
    "buffer_load_dwordx4 v5, s[44:47], s74 offen lds\n"
    "ds_read_b64_tr_b16 a[192:193], v16 offset:0\n"
    "ds_read_b64_tr_b16 a[194:195], v16 offset:512\n"
    "ds_read_b64_tr_b16 a[196:197], v16 offset:8\n"
    "ds_read_b64_tr_b16 a[198:199], v16 offset:520\n"
    "ds_read_b64_tr_b16 a[200:201], v18 offset:0\n"
    "ds_read_b64_tr_b16 a[202:203], v18 offset:512\n"
    "ds_read_b64_tr_b16 a[204:205], v16 offset:12288\n"
    "ds_read_b64_tr_b16 a[206:207], v16 offset:12800\n"
    "ds_read_b64_tr_b16 a[208:209], v16 offset:12296\n"
    "ds_read_b64_tr_b16 a[210:211], v16 offset:12808\n"
    "ds_read_b64_tr_b16 a[212:213], v18 offset:12288\n"
    "ds_read_b64_tr_b16 a[214:215], v18 offset:12800\n"
    "s_waitcnt vmcnt(24) lgkmcnt(0)\n"
    "s_barrier\n"
    "s_add_u32 m0, s59, 49152\n"
    "s_mov_b32 s74, 4128768\n"
    "buffer_load_dwordx4 v5, s[40:43], s74 offen lds\n"
    "s_add_u32 m0, s59, 53248\n"
    "s_mov_b32 s74, 4128896\n"
    "buffer_load_dwordx4 v5, s[40:43], s74 offen lds\n"
    "s_add_u32 m0, s59, 57344\n"
    "s_mov_b32 s74, 4129024\n"
    "buffer_load_dwordx4 v5, s[40:43], s74 offen lds\n"
    "s_add_u32 m0, s59, 61440\n"
    "s_mov_b32 s74, 4128768\n"
    "buffer_load_dwordx4 v5, s[44:47], s74 offen lds\n"
    "s_add_u32 m0, s59, 65536\n"
    "s_mov_b32 s74, 4128896\n"
    "buffer_load_dwordx4 v5, s[44:47], s74 offen lds\n"
    "s_add_u32 m0, s59, 69632\n"
    "s_mov_b32 s74, 4129024\n"
    "buffer_load_dwordx4 v5, s[44:47], s74 offen lds\n"
    "ds_read_b64_tr_b16 a[216:217], v16 offset:24576\n"
    "ds_read_b64_tr_b16 a[218:219], v16 offset:25088\n"
    "ds_read_b64_tr_b16 a[220:221], v16 offset:24584\n"
    "ds_read_b64_tr_b16 a[222:223], v16 offset:25096\n"
    "ds_read_b64_tr_b16 a[224:225], v18 offset:24576\n"
    "ds_read_b64_tr_b16 a[226:227], v18 offset:25088\n"
    "ds_read_b64_tr_b16 a[228:229], v16 offset:36864\n"
    "ds_read_b64_tr_b16 a[230:231], v16 offset:37376\n"
    "ds_read_b64_tr_b16 a[232:233], v16 offset:36872\n"
    "ds_read_b64_tr_b16 a[234:235], v16 offset:37384\n"
    "ds_read_b64_tr_b16 a[236:237], v18 offset:36864\n"
    "ds_read_b64_tr_b16 a[238:239], v18 offset:37376\n"
    "s_waitcnt vmcnt(24) lgkmcnt(0)\n"
    "s_barrier\n"
    "s_add_u32 m0, s59, 73728\n"
    "s_mov_b32 s74, 4423680\n"
    "buffer_load_dwordx4 v5, s[40:43], s74 offen lds\n"
    "s_add_u32 m0, s59, 77824\n"
    "s_mov_b32 s74, 4423808\n"
    "buffer_load_dwordx4 v5, s[40:43], s74 offen lds\n"
    "s_add_u32 m0, s59, 81920\n"
    "s_mov_b32 s74, 4423936\n"
    "buffer_load_dwordx4 v5, s[40:43], s74 offen lds\n"
    "s_add_u32 m0, s59, 86016\n"
    "s_mov_b32 s74, 4423680\n"
    "buffer_load_dwordx4 v5, s[44:47], s74 offen lds\n"
    "s_add_u32 m0, s59, 90112\n"
    "s_mov_b32 s74, 4423808\n"
    "buffer_load_dwordx4 v5, s[44:47], s74 offen lds\n"
    "s_add_u32 m0, s59, 94208\n"
    "s_mov_b32 s74, 4423936\n"
    "buffer_load_dwordx4 v5, s[44:47], s74 offen lds\n"
    "ds_read_b64_tr_b16 a[240:241], v17 offset:0\n"
    "ds_read_b64_tr_b16 a[242:243], v17 offset:512\n"
    "ds_read_b64_tr_b16 a[244:245], v17 offset:8\n"
    "ds_read_b64_tr_b16 a[246:247], v17 offset:520\n"
    "ds_read_b64_tr_b16 a[248:249], v19 offset:0\n"
    "ds_read_b64_tr_b16 a[250:251], v19 offset:512\n"
    "ds_read_b64_tr_b16 a[252:253], v17 offset:12288\n"
    "ds_read_b64_tr_b16 a[254:255], v17 offset:12800\n"
    "ds_read_b64_tr_b16 v[80:81], v17 offset:12296\n"
    "ds_read_b64_tr_b16 v[82:83], v17 offset:12808\n"
    "ds_read_b64_tr_b16 v[84:85], v19 offset:12288\n"
    "ds_read_b64_tr_b16 v[86:87], v19 offset:12800\n"
    "s_waitcnt vmcnt(24) lgkmcnt(0)\n"
    "s_barrier\n"
    "s_add_u32 m0, s59, 98304\n"
    "s_mov_b32 s74, 4718592\n"
    "buffer_load_dwordx4 v5, s[40:43], s74 offen lds\n"
    "s_add_u32 m0, s59, 102400\n"
    "s_mov_b32 s74, 4718720\n"
    "buffer_load_dwordx4 v5, s[40:43], s74 offen lds\n"
    "s_add_u32 m0, s59, 106496\n"
    "s_mov_b32 s74, 4718848\n"
    "buffer_load_dwordx4 v5, s[40:43], s74 offen lds\n"
    "s_add_u32 m0, s59, 110592\n"
    "s_mov_b32 s74, 4718592\n"
    "buffer_load_dwordx4 v5, s[44:47], s74 offen lds\n"
    "s_add_u32 m0, s59, 114688\n"
    "s_mov_b32 s74, 4718720\n"
    "buffer_load_dwordx4 v5, s[44:47], s74 offen lds\n"
    "s_add_u32 m0, s59, 118784\n"
    "s_mov_b32 s74, 4718848\n"
    "buffer_load_dwordx4 v5, s[44:47], s74 offen lds\n"
    "ds_read_b64_tr_b16 v[88:89], v17 offset:24576\n"
    "ds_read_b64_tr_b16 v[90:91], v17 offset:25088\n"
    "ds_read_b64_tr_b16 v[92:93], v17 offset:24584\n"
    "ds_read_b64_tr_b16 v[94:95], v17 offset:25096\n"
    "ds_read_b64_tr_b16 v[96:97], v19 offset:24576\n"
    "ds_read_b64_tr_b16 v[98:99], v19 offset:25088\n"
    "ds_read_b64_tr_b16 v[100:101], v17 offset:36864\n"
    "ds_read_b64_tr_b16 v[102:103], v17 offset:37376\n"
    "ds_read_b64_tr_b16 v[104:105], v17 offset:36872\n"
    "ds_read_b64_tr_b16 v[106:107], v17 offset:37384\n"
    "ds_read_b64_tr_b16 v[108:109], v19 offset:36864\n"
    "ds_read_b64_tr_b16 v[110:111], v19 offset:37376\n"
    "s_waitcnt vmcnt(24) lgkmcnt(0)\n"
    "s_barrier\n"
    "s_add_u32 m0, s59, 122880\n"
    "s_mov_b32 s74, 5013504\n"
    "buffer_load_dwordx4 v5, s[40:43], s74 offen lds\n"
    "s_add_u32 m0, s59, 126976\n"
    "s_mov_b32 s74, 5013632\n"
    "buffer_load_dwordx4 v5, s[40:43], s74 offen lds\n"
    "s_add_u32 m0, s59, 131072\n"
    "s_mov_b32 s74, 5013760\n"
    "buffer_load_dwordx4 v5, s[40:43], s74 offen lds\n"
    "s_add_u32 m0, s59, 135168\n"
    "s_mov_b32 s74, 5013504\n"
    "buffer_load_dwordx4 v5, s[44:47], s74 offen lds\n"
    "s_add_u32 m0, s59, 139264\n"
    "s_mov_b32 s74, 5013632\n"
    "buffer_load_dwordx4 v5, s[44:47], s74 offen lds\n"
    "s_add_u32 m0, s59, 143360\n"
    "s_mov_b32 s74, 5013760\n"
    "buffer_load_dwordx4 v5, s[44:47], s74 offen lds\n"
    "buffer_load_dwordx2 v[12:13], off, s[36:39], 0 sc1\n"
    "ds_read_b64_tr_b16 v[112:113], v6 offset:0\n"
    "ds_read_b64_tr_b16 v[114:115], v6 offset:512\n"
    "ds_read_b64_tr_b16 v[116:117], v6 offset:8\n"
    "ds_read_b64_tr_b16 v[118:119], v6 offset:520\n"
    "ds_read_b64_tr_b16 v[120:121], v7 offset:0\n"
    "ds_read_b64_tr_b16 v[122:123], v7 offset:512\n"
    "ds_read_b64_tr_b16 v[124:125], v6 offset:12288\n"
    "ds_read_b64_tr_b16 v[126:127], v6 offset:12800\n"
    "ds_read_b64_tr_b16 v[128:129], v6 offset:12296\n"
    "ds_read_b64_tr_b16 v[130:131], v6 offset:12808\n"
    "ds_read_b64_tr_b16 v[132:133], v7 offset:12288\n"
    "ds_read_b64_tr_b16 v[134:135], v7 offset:12800\n"
    "s_waitcnt vmcnt(25) lgkmcnt(0)\n"
    "s_barrier\n"
    "ds_read_b64_tr_b16 v[136:137], v6 offset:24576\n"
    "ds_read_b64_tr_b16 v[138:139], v6 offset:25088\n"
    "ds_read_b64_tr_b16 v[140:141], v6 offset:24584\n"
    "ds_read_b64_tr_b16 v[142:143], v6 offset:25096\n"
    "ds_read_b64_tr_b16 v[144:145], v7 offset:24576\n"
    "ds_read_b64_tr_b16 v[146:147], v7 offset:25088\n"
    "ds_read_b64_tr_b16 v[148:149], v6 offset:36864\n"
    "ds_read_b64_tr_b16 v[150:151], v6 offset:37376\n"
    "ds_read_b64_tr_b16 v[152:153], v6 offset:36872\n"
    "ds_read_b64_tr_b16 v[154:155], v6 offset:37384\n"
    "ds_read_b64_tr_b16 v[156:157], v7 offset:36864\n"
    "ds_read_b64_tr_b16 v[158:159], v7 offset:37376\n"
    "s_waitcnt vmcnt(19) lgkmcnt(0)\n"
    "s_barrier\n"
    "ds_read_b64_tr_b16 v[160:161], v16 offset:0\n"
    "ds_read_b64_tr_b16 v[162:163], v16 offset:512\n"
    "ds_read_b64_tr_b16 v[164:165], v16 offset:8\n"
    "ds_read_b64_tr_b16 v[166:167], v16 offset:520\n"
    "ds_read_b64_tr_b16 v[168:169], v18 offset:0\n"
    "ds_read_b64_tr_b16 v[170:171], v18 offset:512\n"
    "ds_read_b64_tr_b16 v[172:173], v16 offset:12288\n"
    "ds_read_b64_tr_b16 v[174:175], v16 offset:12800\n"
    "ds_read_b64_tr_b16 v[176:177], v16 offset:12296\n"
    "ds_read_b64_tr_b16 v[178:179], v16 offset:12808\n"
    "ds_read_b64_tr_b16 v[180:181], v18 offset:12288\n"
    "ds_read_b64_tr_b16 v[182:183], v18 offset:12800\n"
    "s_waitcnt vmcnt(13) lgkmcnt(0)\n"
    "s_barrier\n"
    "ds_read_b64_tr_b16 v[184:185], v16 offset:24576\n"
    "ds_read_b64_tr_b16 v[186:187], v16 offset:25088\n"
    "ds_read_b64_tr_b16 v[188:189], v16 offset:24584\n"
    "ds_read_b64_tr_b16 v[190:191], v16 offset:25096\n"
    "ds_read_b64_tr_b16 v[192:193], v18 offset:24576\n"
    "ds_read_b64_tr_b16 v[194:195], v18 offset:25088\n"
    "ds_read_b64_tr_b16 v[196:197], v16 offset:36864\n"
    "ds_read_b64_tr_b16 v[198:199], v16 offset:37376\n"
    "ds_read_b64_tr_b16 v[200:201], v16 offset:36872\n"
    "ds_read_b64_tr_b16 v[202:203], v16 offset:37384\n"
    "ds_read_b64_tr_b16 v[204:205], v18 offset:36864\n"
    "ds_read_b64_tr_b16 v[206:207], v18 offset:37376\n"
    "s_waitcnt vmcnt(7) lgkmcnt(0)\n"
    "s_barrier\n"
    "ds_read_b64_tr_b16 v[208:209], v17 offset:0\n"
    "ds_read_b64_tr_b16 v[210:211], v17 offset:512\n"
    "ds_read_b64_tr_b16 v[212:213], v17 offset:8\n"
    "ds_read_b64_tr_b16 v[214:215], v17 offset:520\n"
    "ds_read_b64_tr_b16 v[216:217], v19 offset:0\n"
    "ds_read_b64_tr_b16 v[218:219], v19 offset:512\n"
    "ds_read_b64_tr_b16 v[220:221], v17 offset:12288\n"
    "ds_read_b64_tr_b16 v[222:223], v17 offset:12800\n"
    "ds_read_b64_tr_b16 v[224:225], v17 offset:12296\n"
    "ds_read_b64_tr_b16 v[226:227], v17 offset:12808\n"
    "ds_read_b64_tr_b16 v[228:229], v19 offset:12288\n"
    "ds_read_b64_tr_b16 v[230:231], v19 offset:12800\n"
    "s_waitcnt vmcnt(0) lgkmcnt(0)\n"
    "s_barrier\n"
    ".LgUP_par_%=:\n"
    "v_readfirstlane_b32 s74, v12\n"
    "v_readfirstlane_b32 s75, v13\n"
    "s_xor_b32 s74, s74, s66\n"
    "s_xor_b32 s75, s75, s67\n"
    "s_cselect_b32 s75, 2, 0\n"
    "s_or_b32 s75, s75, s74\n"
    "s_cmp_lt_u32 s75, 2\n"
    "s_cbranch_scc1 .LgUP_par1_%=\n"
    "s_sleep 4\n"
    "buffer_load_dwordx2 v[12:13], off, s[36:39], 0 sc1\n"
    "s_waitcnt vmcnt(0)\n"
    "s_sub_u32 s72, s72, 1\n"
    "s_cbranch_scc0 .LgUP_par_%=\n"
    "s_mov_b32 s72, 0\n"
    ".LgUP_par1_%=:\n"
    "s_and_b32 s74, s74, 1\n"
    "s_lshl_b32 s74, s74, 13\n"
    "s_add_u32 s36, s36, s74\n"
    "s_addc_u32 s37, s37, 0\n"
    "v_add_u32 v13, v11, v3\n"
    "v_cmp_gt_i32 vcc, s71, v13\n"
    "s_nop 0\n"
    "s_or_b64 exec, vcc, 1\n"
    "buffer_atomic_smax v11, v2, s[36:39], 0 offen sc0 sc1\n"
    "s_mov_b64 exec, -1\n"
    "s_getpc_b64 s[84:85]\n"
    ".LgUP_PC2_%=:\n"
    "s_and_b32 s85, s85, 0xffff\n"
    "s_mov_b32 s86, .LgUP_FIN_%=-.LgUP_PC2_%=\n"
    "s_mov_b32 s87, 0x20000\n"
    "v_mbcnt_lo_u32_b32 v42, -1, 0\n"
    "v_mbcnt_hi_u32_b32 v42, -1, v42\n"
    "v_lshlrev_b32 v42, 7, v42\n"
    "buffer_load_dword v43, v42, s[84:87], 0 offen\n"
    "v_add_u32 v42, 8192, v42\n"
    "buffer_load_dword v43, v42, s[84:87], 0 offen\n"
    "v_add_u32 v42, 8192, v42\n"
    "buffer_load_dword v43, v42, s[84:87], 0 offen\n"
    "ds_read_b64_tr_b16 v[232:233], v17 offset:24576\n"
    "ds_read_b64_tr_b16 v[234:235], v17 offset:25088\n"
    "ds_read_b64_tr_b16 v[236:237], v17 offset:24584\n"
    "ds_read_b64_tr_b16 v[238:239], v17 offset:25096\n"
    "ds_read_b64_tr_b16 v[240:241], v19 offset:24576\n"
    "ds_read_b64_tr_b16 v[242:243], v19 offset:25088\n"
    "ds_read_b64_tr_b16 v[244:245], v17 offset:36864\n"
    "ds_read_b64_tr_b16 v[246:247], v17 offset:37376\n"
    "ds_read_b64_tr_b16 v[248:249], v17 offset:36872\n"
    "ds_read_b64_tr_b16 v[250:251], v17 offset:37384\n"
    "ds_read_b64_tr_b16 v[252:253], v19 offset:36864\n"
    "ds_read_b64_tr_b16 v[254:255], v19 offset:37376\n"
    "s_waitcnt lgkmcnt(0)\n"
    "s_barrier\n"
    "s_waitcnt vmcnt(0)\n"
    "v_mov_b32 v5, v11\n"
    "v_mov_b32 v12, -1\n"
    "s_branch .LgUP_block1_%=\n"
    ".LgUP_LOOPB_%=:\n"
    ".LgUP_head_%=:\n"
    "v_mfma_f32_16x16x32_bf16 v[48:51], a[8:11], v[16:19], 0\n"
    "s_waitcnt lgkmcnt(2)\n"
    "v_mfma_f32_16x16x32_bf16 v[72:75], a[20:23], v[16:19], 0\n"
    "ds_read_b128 v[16:19], v9 offset:4096\n"
    "v_mul_f32 v64, v40, v64\n"
    "v_mfma_f32_16x16x32_bf16 v[52:55], a[8:11], v[20:23], 0\n"
    "s_waitcnt lgkmcnt(2)\n"
    "v_mul_f32 v65, v41, v65\n"
    "v_mul_f32 v66, v42, v66\n"
    "v_mfma_f32_16x16x32_bf16 v[76:79], a[20:23], v[20:23], 0\n"
    "ds_read_b128 v[20:23], v9 offset:5120\n"
    "v_mul_f32 v67, v43, v67\n"
    "v_mfma_f32_16x16x32_bf16 v[48:51], a[32:35], v[24:27], v[48:51]\n"
    "s_waitcnt lgkmcnt(2)\n"
    "v_mul_f32 v68, v44, v68\n"
    "v_mul_f32 v69, v45, v69\n"
    "v_mfma_f32_16x16x32_bf16 v[72:75], a[44:47], v[24:27], v[72:75]\n"
    "ds_read_b128 v[24:27], v9 offset:6144\n"
    "v_mul_f32 v70, v46, v70\n"
    "v_mfma_f32_16x16x32_bf16 v[52:55], a[32:35], v[28:31], v[52:55]\n"
    "s_waitcnt lgkmcnt(2)\n"
    "v_mul_f32 v71, v47, v71\n"
    "v_mul_f32 v40, 0xbfb8aa3b, v40\n"
    "v_mfma_f32_16x16x32_bf16 v[76:79], a[44:47], v[28:31], v[76:79]\n"
    "ds_read_b128 v[28:31], v9 offset:7168\n"
    "v_mul_f32 v41, 0xbfb8aa3b, v41\n"
    "v_mfma_f32_16x16x32_bf16 v[48:51], a[56:59], v[16:19], v[48:51]\n"
    "s_waitcnt lgkmcnt(2)\n"
    "v_exp_f32 v40, v40\n"
    "v_mfma_f32_16x16x32_bf16 v[72:75], a[68:71], v[16:19], v[72:75]\n"
    "ds_read_b128 v[16:19], v9 offset:8192\n"
    "v_mul_f32 v42, 0xbfb8aa3b, v42\n"
    "v_mfma_f32_16x16x32_bf16 v[52:55], a[56:59], v[20:23], v[52:55]\n"
    "s_waitcnt lgkmcnt(2)\n"
    "v_exp_f32 v41, v41\n"
    "v_mfma_f32_16x16x32_bf16 v[76:79], a[68:71], v[20:23], v[76:79]\n"
    "ds_read_b128 v[20:23], v9 offset:9216\n"
    "v_mul_f32 v43, 0xbfb8aa3b, v43\n"
    "v_mfma_f32_16x16x32_bf16 v[48:51], a[80:83], v[24:27], v[48:51]\n"
    "s_waitcnt lgkmcnt(2)\n"
    "v_exp_f32 v42, v42\n"
    "v_mfma_f32_16x16x32_bf16 v[72:75], a[92:95], v[24:27], v[72:75]\n"
    "ds_read_b128 v[24:27], v9 offset:10240\n"
    "v_mul_f32 v44, 0xbfb8aa3b, v44\n"
    "v_mfma_f32_16x16x32_bf16 v[52:55], a[80:83], v[28:31], v[52:55]\n"
    "s_waitcnt lgkmcnt(2)\n"
    "v_exp_f32 v43, v43\n"
    "v_mfma_f32_16x16x32_bf16 v[76:79], a[92:95], v[28:31], v[76:79]\n"
    "ds_read_b128 v[28:31], v9 offset:11264\n"
    "v_mul_f32 v45, 0xbfb8aa3b, v45\n"
    "v_mfma_f32_16x16x32_bf16 v[48:51], a[104:107], v[16:19], v[48:51]\n"
    "s_waitcnt lgkmcnt(2)\n"
    "v_exp_f32 v44, v44\n"
    "v_mfma_f32_16x16x32_bf16 v[72:75], a[116:119], v[16:19], v[72:75]\n"
    "ds_read_b128 v[16:19], v9 offset:12288\n"
    "v_mul_f32 v46, 0xbfb8aa3b, v46\n"
    "v_mfma_f32_16x16x32_bf16 v[52:55], a[104:107], v[20:23], v[52:55]\n"
    "s_waitcnt lgkmcnt(2)\n"
    "v_exp_f32 v45, v45\n"
    "v_mfma_f32_16x16x32_bf16 v[76:79], a[116:119], v[20:23], v[76:79]\n"
    "ds_read_b128 v[20:23], v9 offset:13312\n"
    "v_mul_f32 v47, 0xbfb8aa3b, v47\n"
    "v_mfma_f32_16x16x32_bf16 v[48:51], a[128:131], v[24:27], v[48:51]\n"
    "s_waitcnt lgkmcnt(2)\n"
    "v_exp_f32 v46, v46\n"
    "v_mfma_f32_16x16x32_bf16 v[72:75], a[140:143], v[24:27], v[72:75]\n"
    "ds_read_b128 v[24:27], v9 offset:14336\n"
    "v_add_f32 v40, 1.0, v40\n"
    "v_mfma_f32_16x16x32_bf16 v[52:55], a[128:131], v[28:31], v[52:55]\n"
    "s_waitcnt lgkmcnt(2)\n"
    "v_exp_f32 v47, v47\n"
    "v_mfma_f32_16x16x32_bf16 v[76:79], a[140:143], v[28:31], v[76:79]\n"
    "ds_read_b128 v[28:31], v9 offset:15360\n"
    "v_add_f32 v41, 1.0, v41\n"
    "v_mfma_f32_16x16x32_bf16 v[48:51], a[152:155], v[16:19], v[48:51]\n"
    "s_waitcnt lgkmcnt(2)\n"
    "v_rcp_f32 v40, v40\n"
    "v_mfma_f32_16x16x32_bf16 v[72:75], a[164:167], v[16:19], v[72:75]\n"
    "ds_read_b128 v[16:19], v9 offset:16384\n"
    "v_add_f32 v42, 1.0, v42\n"
    "v_mfma_f32_16x16x32_bf16 v[52:55], a[152:155], v[20:23], v[52:55]\n"
    "s_waitcnt lgkmcnt(2)\n"
    "v_rcp_f32 v41, v41\n"
    "v_mfma_f32_16x16x32_bf16 v[76:79], a[164:167], v[20:23], v[76:79]\n"
    "ds_read_b128 v[20:23], v9 offset:17408\n"
    "v_add_f32 v43, 1.0, v43\n"
    "v_mfma_f32_16x16x32_bf16 v[48:51], a[176:179], v[24:27], v[48:51]\n"
    "s_waitcnt lgkmcnt(2)\n"
    "v_rcp_f32 v42, v42\n"
    "v_mfma_f32_16x16x32_bf16 v[72:75], a[188:191], v[24:27], v[72:75]\n"
    "ds_read_b128 v[24:27], v9 offset:18432\n"
    "v_add_f32 v44, 1.0, v44\n"
    "v_mfma_f32_16x16x32_bf16 v[52:55], a[176:179], v[28:31], v[52:55]\n"
    "s_waitcnt lgkmcnt(2)\n"
    "v_rcp_f32 v43, v43\n"
    "v_mfma_f32_16x16x32_bf16 v[76:79], a[188:191], v[28:31], v[76:79]\n"
    "ds_read_b128 v[28:31], v9 offset:19456\n"
    "v_add_f32 v45, 1.0, v45\n"
    "v_mfma_f32_16x16x32_bf16 v[48:51], a[200:203], v[16:19], v[48:51]\n"
    "s_waitcnt lgkmcnt(2)\n"
    "v_rcp_f32 v44, v44\n"
    "v_mfma_f32_16x16x32_bf16 v[72:75], a[212:215], v[16:19], v[72:75]\n"
    "ds_read_b128 v[16:19], v9 offset:20480\n"
    "v_add_f32 v46, 1.0, v46\n"
    "v_mfma_f32_16x16x32_bf16 v[52:55], a[200:203], v[20:23], v[52:55]\n"
    "s_waitcnt lgkmcnt(2)\n"
    "v_rcp_f32 v45, v45\n"
    "v_mfma_f32_16x16x32_bf16 v[76:79], a[212:215], v[20:23], v[76:79]\n"
    "ds_read_b128 v[20:23], v9 offset:21504\n"
    "v_add_f32 v47, 1.0, v47\n"
    "v_mfma_f32_16x16x32_bf16 v[48:51], a[224:227], v[24:27], v[48:51]\n"
    "s_waitcnt lgkmcnt(2)\n"
    "v_rcp_f32 v46, v46\n"
    "v_mfma_f32_16x16x32_bf16 v[72:75], a[236:239], v[24:27], v[72:75]\n"
    "ds_read_b128 v[24:27], v9 offset:22528\n"
    "v_mul_f32 v40, v40, v64\n"
    "v_mfma_f32_16x16x32_bf16 v[52:55], a[224:227], v[28:31], v[52:55]\n"
    "s_waitcnt lgkmcnt(2)\n"
    "v_rcp_f32 v47, v47\n"
    "v_mfma_f32_16x16x32_bf16 v[76:79], a[236:239], v[28:31], v[76:79]\n"
    "ds_read_b128 v[28:31], v9 offset:23552\n"
    "v_mul_f32 v41, v41, v65\n"
    "v_mfma_f32_16x16x32_bf16 v[48:51], a[248:251], v[16:19], v[48:51]\n"
    "s_waitcnt lgkmcnt(2)\n"
    "v_mul_f32 v42, v42, v66\n"
    "v_mul_f32 v43, v43, v67\n"
    "v_add_u32 v13, v11, v3\n"
    "v_mfma_f32_16x16x32_bf16 v[72:75], v[84:87], v[16:19], v[72:75]\n"
    "ds_read_b128 v[16:19], v9 offset:24576\n"
    "v_mul_f32 v44, v44, v68\n"
    "v_cmp_gt_i32 vcc, s71, v13\n"
    "v_mfma_f32_16x16x32_bf16 v[52:55], a[248:251], v[20:23], v[52:55]\n"
    "s_waitcnt lgkmcnt(2)\n"
    "v_mul_f32 v45, v45, v69\n"
    "v_mul_f32 v46, v46, v70\n"
    "v_mfma_f32_16x16x32_bf16 v[76:79], v[84:87], v[20:23], v[76:79]\n"
    "ds_read_b128 v[20:23], v9 offset:25600\n"
    "v_mul_f32 v47, v47, v71\n"
    "v_mfma_f32_16x16x32_bf16 v[48:51], v[96:99], v[24:27], v[48:51]\n"
    "s_waitcnt lgkmcnt(2)\n"
    "s_or_b64 exec, vcc, 1\n"
    "buffer_atomic_smax v11, v2, s[36:39], 0 offen sc0 sc1\n"
    "s_mov_b64 exec, -1\n"
    "v_cvt_pk_bf16_f32 v40, v40, v41\n"
    "v_cvt_pk_bf16_f32 v41, v42, v43\n"
    "v_mfma_f32_16x16x32_bf16 v[72:75], v[108:111], v[24:27], v[72:75]\n"
    "ds_read_b128 v[24:27], v9 offset:26624\n"
    "v_cvt_pk_bf16_f32 v42, v44, v45\n"
    "v_mfma_f32_16x16x32_bf16 v[52:55], v[96:99], v[28:31], v[52:55]\n"
    "s_waitcnt lgkmcnt(2)\n"
    "v_cvt_pk_bf16_f32 v43, v46, v47\n"
    "v_mfma_f32_16x16x32_bf16 v[76:79], v[108:111], v[28:31], v[76:79]\n"
    "ds_read_b128 v[28:31], v9 offset:27648\n"
    "v_mfma_f32_16x16x32_bf16 v[48:51], v[120:123], v[16:19], v[48:51]\n"
    "s_waitcnt lgkmcnt(2)\n"
    "buffer_store_dwordx4 v[40:43], v15, s[36:39], s73 offen offset:1024 sc1\n"
    "v_mfma_f32_16x16x32_bf16 v[72:75], v[132:135], v[16:19], v[72:75]\n"
    "ds_read_b128 v[16:19], v9 offset:28672\n"
    "v_mfma_f32_16x16x32_bf16 v[52:55], v[120:123], v[20:23], v[52:55]\n"
    "s_waitcnt lgkmcnt(2)\n"
    "s_and_b32 s76, s71, s54\n"
    "v_mfma_f32_16x16x32_bf16 v[76:79], v[132:135], v[20:23], v[76:79]\n"
    "ds_read_b128 v[20:23], v9 offset:29696\n"
    "v_mfma_f32_16x16x32_bf16 v[48:51], v[144:147], v[24:27], v[48:51]\n"
    "s_waitcnt lgkmcnt(2)\n"
    "s_mul_i32 s76, s76, s53\n"
    "v_mfma_f32_16x16x32_bf16 v[72:75], v[156:159], v[24:27], v[72:75]\n"
    "ds_read_b128 v[24:27], v9 offset:30720\n"
    "v_mfma_f32_16x16x32_bf16 v[52:55], v[144:147], v[28:31], v[52:55]\n"
    "s_waitcnt lgkmcnt(2)\n"
    "s_add_u32 s76, s76, s52\n"
    "v_mfma_f32_16x16x32_bf16 v[76:79], v[156:159], v[28:31], v[76:79]\n"
    "ds_read_b128 v[28:31], v9 offset:31744\n"
    "v_mfma_f32_16x16x32_bf16 v[48:51], v[168:171], v[16:19], v[48:51]\n"
    "s_waitcnt lgkmcnt(2)\n"
    "s_and_b32 s77, s71, 1\n"
    "v_mfma_f32_16x16x32_bf16 v[72:75], v[180:183], v[16:19], v[72:75]\n"
    "ds_read_b128 v[16:19], v9 offset:32768\n"
    "v_mfma_f32_16x16x32_bf16 v[52:55], v[168:171], v[20:23], v[52:55]\n"
    "s_waitcnt lgkmcnt(2)\n"
    "s_mul_i32 s77, s77, 65536\n"
    "v_mfma_f32_16x16x32_bf16 v[76:79], v[180:183], v[20:23], v[76:79]\n"
    "ds_read_b128 v[20:23], v9 offset:33792\n"
    "v_mfma_f32_16x16x32_bf16 v[48:51], v[192:195], v[24:27], v[48:51]\n"
    "s_waitcnt lgkmcnt(2)\n"
    "s_add_u32 s77, s77, s59\n"
    "v_mfma_f32_16x16x32_bf16 v[72:75], v[204:207], v[24:27], v[72:75]\n"
    "ds_read_b128 v[24:27], v9 offset:34816\n"
    "v_mfma_f32_16x16x32_bf16 v[52:55], v[192:195], v[28:31], v[52:55]\n"
    "s_waitcnt lgkmcnt(2)\n"
    "s_and_b32 s73, s68, s57\n"
    "v_mfma_f32_16x16x32_bf16 v[76:79], v[204:207], v[28:31], v[76:79]\n"
    "ds_read_b128 v[28:31], v9 offset:35840\n"
    "v_mfma_f32_16x16x32_bf16 v[48:51], v[216:219], v[16:19], v[48:51]\n"
    "s_waitcnt lgkmcnt(2)\n"
    "s_mul_i32 s73, s73, s56\n"
    "v_mfma_f32_16x16x32_bf16 v[72:75], v[228:231], v[16:19], v[72:75]\n"
    "ds_read_b128 v[16:19], v9 offset:0\n"
    "v_add_u32 v13, v5, v3\n"
    "v_mfma_f32_16x16x32_bf16 v[52:55], v[216:219], v[20:23], v[52:55]\n"
    "s_waitcnt lgkmcnt(2)\n"
    "v_cmp_gt_i32 vcc, s71, v13\n"
    "v_mfma_f32_16x16x32_bf16 v[76:79], v[228:231], v[20:23], v[76:79]\n"
    "ds_read_b128 v[20:23], v9 offset:2048\n"
    "v_mfma_f32_16x16x32_bf16 v[48:51], v[240:243], v[24:27], v[48:51]\n"
    "s_waitcnt lgkmcnt(2)\n"
    "s_or_b64 exec, vcc, 1\n"
    "buffer_atomic_smax v5, v2, s[36:39], 0 offen sc0 sc1\n"
    "s_mov_b64 exec, -1\n"
    "v_mfma_f32_16x16x32_bf16 v[72:75], v[252:255], v[24:27], v[72:75]\n"
    "ds_read_b128 v[24:27], v9 offset:4096\n"
    "v_mfma_f32_16x16x32_bf16 v[52:55], v[240:243], v[28:31], v[52:55]\n"
    "s_waitcnt lgkmcnt(2)\n"
    "s_add_u32 s73, s73, s55\n"
    "v_mfma_f32_16x16x32_bf16 v[76:79], v[252:255], v[28:31], v[76:79]\n"
    "ds_read_b128 v[28:31], v9 offset:6144\n"
    "v_mfma_f32_16x16x32_bf16 v[32:35], a[0:3], v[16:19], 0\n"
    "v_mfma_f32_16x16x32_bf16 v[36:39], a[4:7], v[16:19], 0\n"
    "v_mul_f32 v72, v48, v72\n"
    "v_mul_f32 v73, v49, v73\n"
    "v_mfma_f32_16x16x32_bf16 v[56:59], a[12:15], v[16:19], 0\n"
    "s_waitcnt lgkmcnt(2)\n"
    "v_mul_f32 v74, v50, v74\n"
    "v_mul_f32 v75, v51, v75\n"
    "v_mfma_f32_16x16x32_bf16 v[60:63], a[16:19], v[16:19], 0\n"
    "ds_read_b128 v[16:19], v9 offset:8192\n"
    "v_mul_f32 v76, v52, v76\n"
    "v_mfma_f32_16x16x32_bf16 v[32:35], a[24:27], v[20:23], v[32:35]\n"
    "v_mul_f32 v77, v53, v77\n"
    "v_mul_f32 v78, v54, v78\n"
    "v_mfma_f32_16x16x32_bf16 v[36:39], a[28:31], v[20:23], v[36:39]\n"
    "v_mul_f32 v79, v55, v79\n"
    "v_mul_f32 v48, 0xbfb8aa3b, v48\n"
    "v_mfma_f32_16x16x32_bf16 v[56:59], a[36:39], v[20:23], v[56:59]\n"
    "s_waitcnt lgkmcnt(2)\n"
    "v_exp_f32 v48, v48\n"
    "v_mfma_f32_16x16x32_bf16 v[60:63], a[40:43], v[20:23], v[60:63]\n"
    "ds_read_b128 v[20:23], v9 offset:10240\n"
    "v_mul_f32 v49, 0xbfb8aa3b, v49\n"
    "v_mfma_f32_16x16x32_bf16 v[32:35], a[48:51], v[24:27], v[32:35]\n"
    "v_exp_f32 v49, v49\n"
    "v_mfma_f32_16x16x32_bf16 v[36:39], a[52:55], v[24:27], v[36:39]\n"
    "v_mul_f32 v50, 0xbfb8aa3b, v50\n"
    "v_mul_f32 v51, 0xbfb8aa3b, v51\n"
    "v_mfma_f32_16x16x32_bf16 v[56:59], a[60:63], v[24:27], v[56:59]\n"
    "s_waitcnt lgkmcnt(2)\n"
    "v_exp_f32 v50, v50\n"
    "v_mfma_f32_16x16x32_bf16 v[60:63], a[64:67], v[24:27], v[60:63]\n"
    "ds_read_b128 v[24:27], v9 offset:12288\n"
    "s_waitcnt vmcnt(1)\n"
    "v_mul_f32 v52, 0xbfb8aa3b, v52\n"
    "v_add_u32 v13, v11, v3\n"
    "v_mfma_f32_16x16x32_bf16 v[32:35], a[72:75], v[28:31], v[32:35]\n"
    "buffer_atomic_smax v12, v14, s[36:39], 0 offen sc1\n"
    "v_exp_f32 v51, v51\n"
    "v_cmp_gt_i32 vcc, s71, v13\n"
    "v_mfma_f32_16x16x32_bf16 v[36:39], a[76:79], v[28:31], v[36:39]\n"
    "v_exp_f32 v52, v52\n"
    "v_mfma_f32_16x16x32_bf16 v[56:59], a[84:87], v[28:31], v[56:59]\n"
    "s_waitcnt lgkmcnt(2)\n"
    "v_mul_f32 v53, 0xbfb8aa3b, v53\n"
    "v_mul_f32 v54, 0xbfb8aa3b, v54\n"
    "v_mfma_f32_16x16x32_bf16 v[60:63], a[88:91], v[28:31], v[60:63]\n"
    "ds_read_b128 v[28:31], v9 offset:14336\n"
    "v_mul_f32 v55, 0xbfb8aa3b, v55\n"
    ".LgUP_mid_%=:\n"
    "s_cbranch_vccnz .LgUP_poll0_%=\n"
    "v_mfma_f32_16x16x32_bf16 v[32:35], a[96:99], v[16:19], v[32:35]\n"
    "s_add_u32 m0, s77, s59\n"
    "s_add_u32 s77, s77, 8192\n"
    "buffer_load_dwordx4 v1, s[36:39], s76 offen sc1 lds\n"
    "v_exp_f32 v53, v53\n"
    "v_mfma_f32_16x16x32_bf16 v[36:39], a[100:103], v[16:19], v[36:39]\n"
    "v_exp_f32 v54, v54\n"
    "v_mfma_f32_16x16x32_bf16 v[56:59], a[108:111], v[16:19], v[56:59]\n"
    "s_waitcnt lgkmcnt(2)\n"
    "v_exp_f32 v55, v55\n"
    "v_mfma_f32_16x16x32_bf16 v[60:63], a[112:115], v[16:19], v[60:63]\n"
    "ds_read_b128 v[16:19], v9 offset:16384\n"
    "v_add_f32 v48, 1.0, v48\n"
    "v_mfma_f32_16x16x32_bf16 v[32:35], a[120:123], v[20:23], v[32:35]\n"
    "v_rcp_f32 v48, v48\n"
    "v_mfma_f32_16x16x32_bf16 v[36:39], a[124:127], v[20:23], v[36:39]\n"
    "v_add_f32 v49, 1.0, v49\n"
    "v_add_f32 v50, 1.0, v50\n"
    "v_mfma_f32_16x16x32_bf16 v[56:59], a[132:135], v[20:23], v[56:59]\n"
    "s_waitcnt lgkmcnt(2)\n"
    "buffer_load_dwordx4 v1, s[36:39], s76 offen offset:1024 sc1 lds\n"
    "s_add_u32 s76, s76, 16384\n"
    "v_rcp_f32 v49, v49\n"
    "v_mfma_f32_16x16x32_bf16 v[60:63], a[136:139], v[20:23], v[60:63]\n"
    "ds_read_b128 v[20:23], v9 offset:18432\n"
    "v_add_f32 v51, 1.0, v51\n"
    "v_mfma_f32_16x16x32_bf16 v[32:35], a[144:147], v[24:27], v[32:35]\n"
    "v_rcp_f32 v50, v50\n"
    "v_mfma_f32_16x16x32_bf16 v[36:39], a[148:151], v[24:27], v[36:39]\n"
    "v_rcp_f32 v51, v51\n"
    "v_mfma_f32_16x16x32_bf16 v[56:59], a[156:159], v[24:27], v[56:59]\n"
    "s_waitcnt lgkmcnt(2)\n"
    "v_add_f32 v52, 1.0, v52\n"
    "v_add_f32 v53, 1.0, v53\n"
    "v_mfma_f32_16x16x32_bf16 v[60:63], a[160:163], v[24:27], v[60:63]\n"
    "ds_read_b128 v[24:27], v9 offset:20480\n"
    "v_add_f32 v54, 1.0, v54\n"
    "v_mfma_f32_16x16x32_bf16 v[32:35], a[168:171], v[28:31], v[32:35]\n"
    "v_rcp_f32 v52, v52\n"
    "v_mfma_f32_16x16x32_bf16 v[36:39], a[172:175], v[28:31], v[36:39]\n"
    "s_add_u32 m0, s77, s59\n"
    "s_add_u32 s77, s77, 8192\n"
    "buffer_load_dwordx4 v1, s[36:39], s76 offen sc1 lds\n"
    "v_rcp_f32 v53, v53\n"
    "v_mfma_f32_16x16x32_bf16 v[56:59], a[180:183], v[28:31], v[56:59]\n"
    "s_waitcnt lgkmcnt(2)\n"
    "v_rcp_f32 v54, v54\n"
    "v_mfma_f32_16x16x32_bf16 v[60:63], a[184:187], v[28:31], v[60:63]\n"
    "ds_read_b128 v[28:31], v9 offset:22528\n"
    "v_add_f32 v55, 1.0, v55\n"
    "v_mfma_f32_16x16x32_bf16 v[32:35], a[192:195], v[16:19], v[32:35]\n"
    "v_rcp_f32 v55, v55\n"
    "v_mfma_f32_16x16x32_bf16 v[36:39], a[196:199], v[16:19], v[36:39]\n"
    "v_mul_f32 v48, v48, v72\n"
    "v_mul_f32 v49, v49, v73\n"
    "v_mfma_f32_16x16x32_bf16 v[56:59], a[204:207], v[16:19], v[56:59]\n"
    "s_waitcnt lgkmcnt(2)\n"
    "v_mul_f32 v50, v50, v74\n"
    "v_mul_f32 v51, v51, v75\n"
    "v_mfma_f32_16x16x32_bf16 v[60:63], a[208:211], v[16:19], v[60:63]\n"
    "ds_read_b128 v[16:19], v9 offset:24576\n"
    "v_mul_f32 v52, v52, v76\n"
    "v_mfma_f32_16x16x32_bf16 v[32:35], a[216:219], v[20:23], v[32:35]\n"
    "buffer_load_dwordx4 v1, s[36:39], s76 offen offset:1024 sc1 lds\n"
    "s_add_u32 s76, s76, 16384\n"
    "v_mul_f32 v53, v53, v77\n"
    "v_mul_f32 v54, v54, v78\n"
    "v_mfma_f32_16x16x32_bf16 v[36:39], a[220:223], v[20:23], v[36:39]\n"
    "v_mul_f32 v55, v55, v79\n"
    "v_cvt_pk_bf16_f32 v48, v48, v49\n"
    "v_mfma_f32_16x16x32_bf16 v[56:59], a[228:231], v[20:23], v[56:59]\n"
    "s_waitcnt lgkmcnt(2)\n"
    "v_cvt_pk_bf16_f32 v49, v50, v51\n"
    "v_cvt_pk_bf16_f32 v50, v52, v53\n"
    "v_mfma_f32_16x16x32_bf16 v[60:63], a[232:235], v[20:23], v[60:63]\n"
    "ds_read_b128 v[20:23], v9 offset:26624\n"
    "v_cvt_pk_bf16_f32 v51, v54, v55\n"
    "v_mfma_f32_16x16x32_bf16 v[32:35], a[240:243], v[24:27], v[32:35]\n"
    "s_nop 0\n"
    "v_mfma_f32_16x16x32_bf16 v[36:39], a[244:247], v[24:27], v[36:39]\n"
    "v_permlane16_swap_b32 v48, v50\n"
    "v_mfma_f32_16x16x32_bf16 v[56:59], a[252:255], v[24:27], v[56:59]\n"
    "s_waitcnt lgkmcnt(2)\n"
    "s_add_u32 m0, s77, s59\n"
    "s_add_u32 s77, s77, 8192\n"
    "buffer_load_dwordx4 v1, s[36:39], s76 offen sc1 lds\n"
    "v_permlane16_swap_b32 v49, v51\n"
    "v_mfma_f32_16x16x32_bf16 v[60:63], v[80:83], v[24:27], v[60:63]\n"
    "ds_read_b128 v[24:27], v9 offset:28672\n"
    "v_mfma_f32_16x16x32_bf16 v[32:35], v[88:91], v[28:31], v[32:35]\n"
    "v_mfma_f32_16x16x32_bf16 v[36:39], v[92:95], v[28:31], v[36:39]\n"
    "v_mfma_f32_16x16x32_bf16 v[56:59], v[100:103], v[28:31], v[56:59]\n"
    "s_waitcnt lgkmcnt(2)\n"
    "v_mfma_f32_16x16x32_bf16 v[60:63], v[104:107], v[28:31], v[60:63]\n"
    "ds_read_b128 v[28:31], v9 offset:30720\n"
    "v_mfma_f32_16x16x32_bf16 v[32:35], v[112:115], v[16:19], v[32:35]\n"
    "v_mfma_f32_16x16x32_bf16 v[36:39], v[116:119], v[16:19], v[36:39]\n"
    "buffer_load_dwordx4 v1, s[36:39], s76 offen offset:1024 sc1 lds\n"
    "s_add_u32 s76, s76, 16384\n"
    "v_mfma_f32_16x16x32_bf16 v[56:59], v[124:127], v[16:19], v[56:59]\n"
    "s_waitcnt lgkmcnt(2)\n"
    "v_mfma_f32_16x16x32_bf16 v[60:63], v[128:131], v[16:19], v[60:63]\n"
    "ds_read_b128 v[16:19], v9 offset:32768\n"
    "v_mfma_f32_16x16x32_bf16 v[32:35], v[136:139], v[20:23], v[32:35]\n"
    "v_mfma_f32_16x16x32_bf16 v[36:39], v[140:143], v[20:23], v[36:39]\n"
    "v_mfma_f32_16x16x32_bf16 v[56:59], v[148:151], v[20:23], v[56:59]\n"
    "s_waitcnt lgkmcnt(2)\n"
    "v_mfma_f32_16x16x32_bf16 v[60:63], v[152:155], v[20:23], v[60:63]\n"
    "ds_read_b128 v[20:23], v9 offset:34816\n"
    "v_mfma_f32_16x16x32_bf16 v[32:35], v[160:163], v[24:27], v[32:35]\n"
    "s_add_u32 m0, s77, s59\n"
    "s_add_u32 s77, s77, 8192\n"
    "buffer_load_dwordx4 v1, s[36:39], s76 offen sc1 lds\n"
    "v_mfma_f32_16x16x32_bf16 v[36:39], v[164:167], v[24:27], v[36:39]\n"
    "v_mfma_f32_16x16x32_bf16 v[56:59], v[172:175], v[24:27], v[56:59]\n"
    "s_waitcnt lgkmcnt(2)\n"
    "v_mfma_f32_16x16x32_bf16 v[60:63], v[176:179], v[24:27], v[60:63]\n"
    "ds_read_b128 v[24:27], v9 offset:1024\n"
    "v_mfma_f32_16x16x32_bf16 v[32:35], v[184:187], v[28:31], v[32:35]\n"
    "v_mfma_f32_16x16x32_bf16 v[36:39], v[188:191], v[28:31], v[36:39]\n"
    "v_mfma_f32_16x16x32_bf16 v[56:59], v[196:199], v[28:31], v[56:59]\n"
    "s_waitcnt lgkmcnt(2)\n"
    "buffer_load_dwordx4 v1, s[36:39], s76 offen offset:1024 sc1 lds\n"
    "s_add_u32 s76, s76, 16384\n"
    "v_mfma_f32_16x16x32_bf16 v[60:63], v[200:203], v[28:31], v[60:63]\n"
    "ds_read_b128 v[28:31], v9 offset:3072\n"
    "v_mfma_f32_16x16x32_bf16 v[32:35], v[208:211], v[16:19], v[32:35]\n"
    "v_mfma_f32_16x16x32_bf16 v[36:39], v[212:215], v[16:19], v[36:39]\n"
    "v_mfma_f32_16x16x32_bf16 v[56:59], v[220:223], v[16:19], v[56:59]\n"
    "s_waitcnt lgkmcnt(2)\n"
    "v_mfma_f32_16x16x32_bf16 v[60:63], v[224:227], v[16:19], v[60:63]\n"
    "ds_read_b128 v[16:19], v9 offset:5120\n"
    "v_mfma_f32_16x16x32_bf16 v[32:35], v[232:235], v[20:23], v[32:35]\n"
    "v_mfma_f32_16x16x32_bf16 v[36:39], v[236:239], v[20:23], v[36:39]\n"
    "s_mov_b32 m0, s77\n"
    "s_nop 0\n"
    "buffer_load_dwordx4 v8, s[36:39], s76 offen sc1 lds\n"
    "v_mfma_f32_16x16x32_bf16 v[56:59], v[244:247], v[20:23], v[56:59]\n"
    "s_waitcnt lgkmcnt(2)\n"
    "v_mfma_f32_16x16x32_bf16 v[60:63], v[248:251], v[20:23], v[60:63]\n"
    "ds_read_b128 v[20:23], v9 offset:7168\n"
    "v_mfma_f32_16x16x32_bf16 v[40:43], a[0:3], v[24:27], 0\n"
    "v_mfma_f32_16x16x32_bf16 v[44:47], a[4:7], v[24:27], 0\n"
    "v_mul_f32 v56, v32, v56\n"
    "v_mul_f32 v57, v33, v57\n"
    "v_mfma_f32_16x16x32_bf16 v[64:67], a[12:15], v[24:27], 0\n"
    "s_waitcnt lgkmcnt(2)\n"
    "v_mul_f32 v58, v34, v58\n"
    "v_mul_f32 v59, v35, v59\n"
    "v_mfma_f32_16x16x32_bf16 v[68:71], a[16:19], v[24:27], 0\n"
    "ds_read_b128 v[24:27], v9 offset:9216\n"
    "v_mul_f32 v60, v36, v60\n"
    ".LgUP_q148_%=:\n"
    "v_mfma_f32_16x16x32_bf16 v[40:43], a[24:27], v[28:31], v[40:43]\n"
    "v_mul_f32 v61, v37, v61\n"
    "v_mul_f32 v62, v38, v62\n"
    "v_mfma_f32_16x16x32_bf16 v[44:47], a[28:31], v[28:31], v[44:47]\n"
    "v_mul_f32 v63, v39, v63\n"
    "v_mul_f32 v32, 0xbfb8aa3b, v32\n"
    "v_mfma_f32_16x16x32_bf16 v[64:67], a[36:39], v[28:31], v[64:67]\n"
    "s_waitcnt lgkmcnt(2)\n"
    "v_exp_f32 v32, v32\n"
    "v_mfma_f32_16x16x32_bf16 v[68:71], a[40:43], v[28:31], v[68:71]\n"
    "ds_read_b128 v[28:31], v9 offset:11264\n"
    "v_mul_f32 v33, 0xbfb8aa3b, v33\n"
    "v_mfma_f32_16x16x32_bf16 v[40:43], a[48:51], v[16:19], v[40:43]\n"
    "v_exp_f32 v33, v33\n"
    "v_mfma_f32_16x16x32_bf16 v[44:47], a[52:55], v[16:19], v[44:47]\n"
    "v_mul_f32 v34, 0xbfb8aa3b, v34\n"
    "v_mul_f32 v35, 0xbfb8aa3b, v35\n"
    "v_mfma_f32_16x16x32_bf16 v[64:67], a[60:63], v[16:19], v[64:67]\n"
    "s_waitcnt lgkmcnt(2)\n"
    "v_exp_f32 v34, v34\n"
    "v_mfma_f32_16x16x32_bf16 v[68:71], a[64:67], v[16:19], v[68:71]\n"
    "ds_read_b128 v[16:19], v9 offset:13312\n"
    "v_mul_f32 v36, 0xbfb8aa3b, v36\n"
    "v_mfma_f32_16x16x32_bf16 v[40:43], a[72:75], v[20:23], v[40:43]\n"
    "v_exp_f32 v35, v35\n"
    "v_mfma_f32_16x16x32_bf16 v[44:47], a[76:79], v[20:23], v[44:47]\n"
    "v_exp_f32 v36, v36\n"
    "v_mfma_f32_16x16x32_bf16 v[64:67], a[84:87], v[20:23], v[64:67]\n"
    "s_waitcnt lgkmcnt(2)\n"
    "v_mul_f32 v37, 0xbfb8aa3b, v37\n"
    "v_mul_f32 v38, 0xbfb8aa3b, v38\n"
    "v_mfma_f32_16x16x32_bf16 v[68:71], a[88:91], v[20:23], v[68:71]\n"
    "ds_read_b128 v[20:23], v9 offset:15360\n"
    "v_mul_f32 v39, 0xbfb8aa3b, v39\n"
    "v_mfma_f32_16x16x32_bf16 v[40:43], a[96:99], v[24:27], v[40:43]\n"
    "v_exp_f32 v37, v37\n"
    "v_mfma_f32_16x16x32_bf16 v[44:47], a[100:103], v[24:27], v[44:47]\n"
    "v_exp_f32 v38, v38\n"
    "v_mfma_f32_16x16x32_bf16 v[64:67], a[108:111], v[24:27], v[64:67]\n"
    "s_waitcnt lgkmcnt(2)\n"
    "v_exp_f32 v39, v39\n"
    "v_mfma_f32_16x16x32_bf16 v[68:71], a[112:115], v[24:27], v[68:71]\n"
    "ds_read_b128 v[24:27], v9 offset:17408\n"
    "v_add_f32 v32, 1.0, v32\n"
    "v_mfma_f32_16x16x32_bf16 v[40:43], a[120:123], v[28:31], v[40:43]\n"
    "v_rcp_f32 v32, v32\n"
    "v_mfma_f32_16x16x32_bf16 v[44:47], a[124:127], v[28:31], v[44:47]\n"
    "v_add_f32 v33, 1.0, v33\n"
    "v_add_f32 v34, 1.0, v34\n"
    "v_mfma_f32_16x16x32_bf16 v[64:67], a[132:135], v[28:31], v[64:67]\n"
    "s_waitcnt lgkmcnt(2)\n"
    "v_rcp_f32 v33, v33\n"
    "v_mfma_f32_16x16x32_bf16 v[68:71], a[136:139], v[28:31], v[68:71]\n"
    "ds_read_b128 v[28:31], v9 offset:19456\n"
    "v_add_f32 v35, 1.0, v35\n"
    ".LgUP_q168_%=:\n"
    "v_mfma_f32_16x16x32_bf16 v[40:43], a[144:147], v[16:19], v[40:43]\n"
    "buffer_store_dwordx4 v[48:51], v4, s[36:39], s73 offen sc1\n"
    "v_add_f32 v36, 1.0, v36\n"
    "v_mov_b32 v12, s68\n"
    "v_mfma_f32_16x16x32_bf16 v[44:47], a[148:151], v[16:19], v[44:47]\n"
    "s_mov_b32 s68, s71\n"
    "v_rcp_f32 v34, v34\n"
    "v_mfma_f32_16x16x32_bf16 v[64:67], a[156:159], v[16:19], v[64:67]\n"
    "s_waitcnt lgkmcnt(2)\n"
    "v_rcp_f32 v35, v35\n"
    "v_mfma_f32_16x16x32_bf16 v[68:71], a[160:163], v[16:19], v[68:71]\n"
    "ds_read_b128 v[16:19], v9 offset:21504\n"
    "v_add_f32 v37, 1.0, v37\n"
    "v_mfma_f32_16x16x32_bf16 v[40:43], a[168:171], v[20:23], v[40:43]\n"
    "s_add_u32 s71, s68, 1\n"
    "v_rcp_f32 v36, v36\n"
    "v_mfma_f32_16x16x32_bf16 v[44:47], a[172:175], v[20:23], v[44:47]\n"
    "v_rcp_f32 v37, v37\n"
    "v_mfma_f32_16x16x32_bf16 v[64:67], a[180:183], v[20:23], v[64:67]\n"
    "s_waitcnt lgkmcnt(2)\n"
    "v_add_f32 v38, 1.0, v38\n"
    "v_add_f32 v39, 1.0, v39\n"
    "v_mfma_f32_16x16x32_bf16 v[68:71], a[184:187], v[20:23], v[68:71]\n"
    "ds_read_b128 v[20:23], v9 offset:23552\n"
    "v_mul_f32 v32, v32, v56\n"
    "v_mfma_f32_16x16x32_bf16 v[40:43], a[192:195], v[24:27], v[40:43]\n"
    "v_rcp_f32 v38, v38\n"
    "v_mfma_f32_16x16x32_bf16 v[44:47], a[196:199], v[24:27], v[44:47]\n"
    "v_rcp_f32 v39, v39\n"
    "v_mfma_f32_16x16x32_bf16 v[64:67], a[204:207], v[24:27], v[64:67]\n"
    "s_waitcnt lgkmcnt(2)\n"
    "v_mul_f32 v33, v33, v57\n"
    "v_mul_f32 v34, v34, v58\n"
    "v_mfma_f32_16x16x32_bf16 v[68:71], a[208:211], v[24:27], v[68:71]\n"
    "ds_read_b128 v[24:27], v9 offset:25600\n"
    "v_mul_f32 v35, v35, v59\n"
    "v_mfma_f32_16x16x32_bf16 v[40:43], a[216:219], v[28:31], v[40:43]\n"
    "v_mul_f32 v36, v36, v60\n"
    "v_mul_f32 v37, v37, v61\n"
    "v_mfma_f32_16x16x32_bf16 v[44:47], a[220:223], v[28:31], v[44:47]\n"
    "v_mul_f32 v38, v38, v62\n"
    "v_mul_f32 v39, v39, v63\n"
    "v_mfma_f32_16x16x32_bf16 v[64:67], a[228:231], v[28:31], v[64:67]\n"
    "s_waitcnt lgkmcnt(2)\n"
    "v_cvt_pk_bf16_f32 v32, v32, v33\n"
    "v_cvt_pk_bf16_f32 v33, v34, v35\n"
    "v_mfma_f32_16x16x32_bf16 v[68:71], a[232:235], v[28:31], v[68:71]\n"
    "ds_read_b128 v[28:31], v9 offset:27648\n"
    "v_cvt_pk_bf16_f32 v34, v36, v37\n"
    "v_mfma_f32_16x16x32_bf16 v[40:43], a[240:243], v[16:19], v[40:43]\n"
    "v_cvt_pk_bf16_f32 v35, v38, v39\n"
    "v_mfma_f32_16x16x32_bf16 v[44:47], a[244:247], v[16:19], v[44:47]\n"
    "buffer_store_dwordx4 v[32:35], v15, s[36:39], s73 offen sc1\n"
    "v_mfma_f32_16x16x32_bf16 v[64:67], a[252:255], v[16:19], v[64:67]\n"
    "s_waitcnt lgkmcnt(2)\n"
    "v_mfma_f32_16x16x32_bf16 v[68:71], v[80:83], v[16:19], v[68:71]\n"
    "ds_read_b128 v[16:19], v9 offset:29696\n"
    "v_mfma_f32_16x16x32_bf16 v[40:43], v[88:91], v[20:23], v[40:43]\n"
    "v_mfma_f32_16x16x32_bf16 v[44:47], v[92:95], v[20:23], v[44:47]\n"
    "v_mfma_f32_16x16x32_bf16 v[64:67], v[100:103], v[20:23], v[64:67]\n"
    "s_waitcnt lgkmcnt(2)\n"
    "v_mfma_f32_16x16x32_bf16 v[68:71], v[104:107], v[20:23], v[68:71]\n"
    "ds_read_b128 v[20:23], v9 offset:31744\n"
    "v_mfma_f32_16x16x32_bf16 v[40:43], v[112:115], v[24:27], v[40:43]\n"
    "v_mfma_f32_16x16x32_bf16 v[44:47], v[116:119], v[24:27], v[44:47]\n"
    "v_mfma_f32_16x16x32_bf16 v[64:67], v[124:127], v[24:27], v[64:67]\n"
    "s_waitcnt lgkmcnt(2)\n"
    "v_mfma_f32_16x16x32_bf16 v[68:71], v[128:131], v[24:27], v[68:71]\n"
    "ds_read_b128 v[24:27], v9 offset:33792\n"
    "v_mfma_f32_16x16x32_bf16 v[40:43], v[136:139], v[28:31], v[40:43]\n"
    "v_mfma_f32_16x16x32_bf16 v[44:47], v[140:143], v[28:31], v[44:47]\n"
    "v_mfma_f32_16x16x32_bf16 v[64:67], v[148:151], v[28:31], v[64:67]\n"
    "s_waitcnt lgkmcnt(2)\n"
    "v_mfma_f32_16x16x32_bf16 v[68:71], v[152:155], v[28:31], v[68:71]\n"
    "ds_read_b128 v[28:31], v9 offset:35840\n"
    "v_mfma_f32_16x16x32_bf16 v[40:43], v[160:163], v[16:19], v[40:43]\n"
    "v_mfma_f32_16x16x32_bf16 v[44:47], v[164:167], v[16:19], v[44:47]\n"
    "v_mfma_f32_16x16x32_bf16 v[64:67], v[172:175], v[16:19], v[64:67]\n"
    "s_waitcnt lgkmcnt(2)\n"
    "v_xor_b32 v9, s78, v9\n"
    "v_mfma_f32_16x16x32_bf16 v[68:71], v[176:179], v[16:19], v[68:71]\n"
    "s_waitcnt vmcnt(2) lgkmcnt(0)\n"
    "s_barrier\n"
    "ds_read_b128 v[16:19], v9 offset:0\n"
    "v_mfma_f32_16x16x32_bf16 v[40:43], v[184:187], v[20:23], v[40:43]\n"
    "v_mfma_f32_16x16x32_bf16 v[44:47], v[188:191], v[20:23], v[44:47]\n"
    "v_mfma_f32_16x16x32_bf16 v[64:67], v[196:199], v[20:23], v[64:67]\n"
    "v_mfma_f32_16x16x32_bf16 v[68:71], v[200:203], v[20:23], v[68:71]\n"
    "ds_read_b128 v[20:23], v9 offset:1024\n"
    "v_mfma_f32_16x16x32_bf16 v[40:43], v[208:211], v[24:27], v[40:43]\n"
    "v_mfma_f32_16x16x32_bf16 v[44:47], v[212:215], v[24:27], v[44:47]\n"
    "v_mfma_f32_16x16x32_bf16 v[64:67], v[220:223], v[24:27], v[64:67]\n"
    "v_mfma_f32_16x16x32_bf16 v[68:71], v[224:227], v[24:27], v[68:71]\n"
    "ds_read_b128 v[24:27], v9 offset:2048\n"
    "v_mfma_f32_16x16x32_bf16 v[40:43], v[232:235], v[28:31], v[40:43]\n"
    "v_mfma_f32_16x16x32_bf16 v[44:47], v[236:239], v[28:31], v[44:47]\n"
    "v_mfma_f32_16x16x32_bf16 v[64:67], v[244:247], v[28:31], v[64:67]\n"
    "s_waitcnt lgkmcnt(2)\n"
    "v_mfma_f32_16x16x32_bf16 v[68:71], v[248:251], v[28:31], v[68:71]\n"
    "ds_read_b128 v[28:31], v9 offset:3072\n"
    "s_branch .LgUP_head_%=\n"
    ".LgUP_LOOPE_%=:\n"
    ".LgUP_done_%=:\n"
    "s_waitcnt vmcnt(0)\n"
    "buffer_atomic_smax v12, v14, s[36:39], 0 offen sc1\n"
    "s_waitcnt vmcnt(0)\n"
    "s_branch .LgUP_FIN_%=\n"
    ".LgUP_poll0_%=:\n"
    "v_mfma_f32_16x16x32_bf16 v[32:35], a[96:99], v[16:19], v[32:35]\n"
    "s_or_b64 exec, vcc, 1\n"
    "buffer_atomic_smax v11, v2, s[36:39], 0 offen sc0 sc1\n"
    "s_mov_b64 exec, -1\n"
    "v_exp_f32 v53, v53\n"
    "v_mfma_f32_16x16x32_bf16 v[36:39], a[100:103], v[16:19], v[36:39]\n"
    "v_exp_f32 v54, v54\n"
    "v_mfma_f32_16x16x32_bf16 v[56:59], a[108:111], v[16:19], v[56:59]\n"
    "s_waitcnt lgkmcnt(2)\n"
    "v_exp_f32 v55, v55\n"
    "v_mfma_f32_16x16x32_bf16 v[60:63], a[112:115], v[16:19], v[60:63]\n"
    "ds_read_b128 v[16:19], v9 offset:16384\n"
    "v_add_f32 v48, 1.0, v48\n"
    "v_mfma_f32_16x16x32_bf16 v[32:35], a[120:123], v[20:23], v[32:35]\n"
    "v_rcp_f32 v48, v48\n"
    "v_mfma_f32_16x16x32_bf16 v[36:39], a[124:127], v[20:23], v[36:39]\n"
    "v_add_f32 v49, 1.0, v49\n"
    "v_add_f32 v50, 1.0, v50\n"
    "v_mfma_f32_16x16x32_bf16 v[56:59], a[132:135], v[20:23], v[56:59]\n"
    "s_waitcnt lgkmcnt(2)\n"
    "v_rcp_f32 v49, v49\n"
    "v_mfma_f32_16x16x32_bf16 v[60:63], a[136:139], v[20:23], v[60:63]\n"
    "ds_read_b128 v[20:23], v9 offset:18432\n"
    "v_add_f32 v51, 1.0, v51\n"
    "v_mfma_f32_16x16x32_bf16 v[32:35], a[144:147], v[24:27], v[32:35]\n"
    "v_rcp_f32 v50, v50\n"
    "v_mfma_f32_16x16x32_bf16 v[36:39], a[148:151], v[24:27], v[36:39]\n"
    "v_rcp_f32 v51, v51\n"
    "v_mfma_f32_16x16x32_bf16 v[56:59], a[156:159], v[24:27], v[56:59]\n"
    "s_waitcnt lgkmcnt(2)\n"
    "v_add_f32 v52, 1.0, v52\n"
    "v_add_f32 v53, 1.0, v53\n"
    "v_mfma_f32_16x16x32_bf16 v[60:63], a[160:163], v[24:27], v[60:63]\n"
    "ds_read_b128 v[24:27], v9 offset:20480\n"
    "v_add_f32 v54, 1.0, v54\n"
    "v_mfma_f32_16x16x32_bf16 v[32:35], a[168:171], v[28:31], v[32:35]\n"
    "v_rcp_f32 v52, v52\n"
    "v_mfma_f32_16x16x32_bf16 v[36:39], a[172:175], v[28:31], v[36:39]\n"
    "v_rcp_f32 v53, v53\n"
    "v_mfma_f32_16x16x32_bf16 v[56:59], a[180:183], v[28:31], v[56:59]\n"
    "s_waitcnt lgkmcnt(2)\n"
    "v_rcp_f32 v54, v54\n"
    "v_mfma_f32_16x16x32_bf16 v[60:63], a[184:187], v[28:31], v[60:63]\n"
    "ds_read_b128 v[28:31], v9 offset:22528\n"
    "s_waitcnt vmcnt(2)\n"
    "v_add_f32 v55, 1.0, v55\n"
    "v_add_u32 v13, v5, v3\n"
    "v_mfma_f32_16x16x32_bf16 v[32:35], a[192:195], v[16:19], v[32:35]\n"
    "v_rcp_f32 v55, v55\n"
    "v_cmp_gt_i32 vcc, s71, v13\n"
    "v_mfma_f32_16x16x32_bf16 v[36:39], a[196:199], v[16:19], v[36:39]\n"
    "v_mul_f32 v48, v48, v72\n"
    "v_mul_f32 v49, v49, v73\n"
    "v_mfma_f32_16x16x32_bf16 v[56:59], a[204:207], v[16:19], v[56:59]\n"
    "s_waitcnt lgkmcnt(2)\n"
    "v_mul_f32 v50, v50, v74\n"
    "v_mul_f32 v51, v51, v75\n"
    "v_mfma_f32_16x16x32_bf16 v[60:63], a[208:211], v[16:19], v[60:63]\n"
    "ds_read_b128 v[16:19], v9 offset:24576\n"
    "v_mul_f32 v52, v52, v76\n"
    "s_cbranch_vccz .LgUP_dma108_%=\n"
    "v_mfma_f32_16x16x32_bf16 v[32:35], a[216:219], v[20:23], v[32:35]\n"
    "s_or_b64 exec, vcc, 1\n"
    "buffer_atomic_smax v5, v2, s[36:39], 0 offen sc0 sc1\n"
    "s_mov_b64 exec, -1\n"
    "v_mul_f32 v53, v53, v77\n"
    "v_mul_f32 v54, v54, v78\n"
    "v_mfma_f32_16x16x32_bf16 v[36:39], a[220:223], v[20:23], v[36:39]\n"
    "v_mul_f32 v55, v55, v79\n"
    "v_cvt_pk_bf16_f32 v48, v48, v49\n"
    "v_mfma_f32_16x16x32_bf16 v[56:59], a[228:231], v[20:23], v[56:59]\n"
    "s_waitcnt lgkmcnt(2)\n"
    "v_cvt_pk_bf16_f32 v49, v50, v51\n"
    "v_cvt_pk_bf16_f32 v50, v52, v53\n"
    "v_mfma_f32_16x16x32_bf16 v[60:63], a[232:235], v[20:23], v[60:63]\n"
    "ds_read_b128 v[20:23], v9 offset:26624\n"
    "v_cvt_pk_bf16_f32 v51, v54, v55\n"
    "v_mfma_f32_16x16x32_bf16 v[32:35], a[240:243], v[24:27], v[32:35]\n"
    "s_nop 0\n"
    "v_mfma_f32_16x16x32_bf16 v[36:39], a[244:247], v[24:27], v[36:39]\n"
    "v_permlane16_swap_b32 v48, v50\n"
    "v_mfma_f32_16x16x32_bf16 v[56:59], a[252:255], v[24:27], v[56:59]\n"
    "s_waitcnt lgkmcnt(2)\n"
    "v_permlane16_swap_b32 v49, v51\n"
    "v_mfma_f32_16x16x32_bf16 v[60:63], v[80:83], v[24:27], v[60:63]\n"
    "ds_read_b128 v[24:27], v9 offset:28672\n"
    "v_mfma_f32_16x16x32_bf16 v[32:35], v[88:91], v[28:31], v[32:35]\n"
    "v_mfma_f32_16x16x32_bf16 v[36:39], v[92:95], v[28:31], v[36:39]\n"
    "v_mfma_f32_16x16x32_bf16 v[56:59], v[100:103], v[28:31], v[56:59]\n"
    "s_waitcnt lgkmcnt(2)\n"
    "v_mfma_f32_16x16x32_bf16 v[60:63], v[104:107], v[28:31], v[60:63]\n"
    "ds_read_b128 v[28:31], v9 offset:30720\n"
    "v_mfma_f32_16x16x32_bf16 v[32:35], v[112:115], v[16:19], v[32:35]\n"
    "v_mfma_f32_16x16x32_bf16 v[36:39], v[116:119], v[16:19], v[36:39]\n"
    "v_mfma_f32_16x16x32_bf16 v[56:59], v[124:127], v[16:19], v[56:59]\n"
    "s_waitcnt lgkmcnt(2)\n"
    "v_mfma_f32_16x16x32_bf16 v[60:63], v[128:131], v[16:19], v[60:63]\n"
    "ds_read_b128 v[16:19], v9 offset:32768\n"
    "s_waitcnt vmcnt(1)\n"
    "v_add_u32 v13, v11, v3\n"
    "v_mfma_f32_16x16x32_bf16 v[32:35], v[136:139], v[20:23], v[32:35]\n"
    "v_cmp_gt_i32 vcc, s71, v13\n"
    "v_mfma_f32_16x16x32_bf16 v[36:39], v[140:143], v[20:23], v[36:39]\n"
    "v_mfma_f32_16x16x32_bf16 v[56:59], v[148:151], v[20:23], v[56:59]\n"
    "s_waitcnt lgkmcnt(2)\n"
    "v_mfma_f32_16x16x32_bf16 v[60:63], v[152:155], v[20:23], v[60:63]\n"
    "ds_read_b128 v[20:23], v9 offset:34816\n"
    "s_cbranch_vccz .LgUP_dma128_%=\n"
    "v_mfma_f32_16x16x32_bf16 v[32:35], v[160:163], v[24:27], v[32:35]\n"
    "s_or_b64 exec, vcc, 1\n"
    "buffer_atomic_smax v11, v2, s[36:39], 0 offen sc0 sc1\n"
    "s_mov_b64 exec, -1\n"
    "v_mfma_f32_16x16x32_bf16 v[36:39], v[164:167], v[24:27], v[36:39]\n"
    "v_mfma_f32_16x16x32_bf16 v[56:59], v[172:175], v[24:27], v[56:59]\n"
    "s_waitcnt lgkmcnt(2)\n"
    "v_mfma_f32_16x16x32_bf16 v[60:63], v[176:179], v[24:27], v[60:63]\n"
    "ds_read_b128 v[24:27], v9 offset:1024\n"
    "v_mfma_f32_16x16x32_bf16 v[32:35], v[184:187], v[28:31], v[32:35]\n"
    "v_mfma_f32_16x16x32_bf16 v[36:39], v[188:191], v[28:31], v[36:39]\n"
    "v_mfma_f32_16x16x32_bf16 v[56:59], v[196:199], v[28:31], v[56:59]\n"
    "s_waitcnt lgkmcnt(2)\n"
    "v_mfma_f32_16x16x32_bf16 v[60:63], v[200:203], v[28:31], v[60:63]\n"
    "ds_read_b128 v[28:31], v9 offset:3072\n"
    "v_mfma_f32_16x16x32_bf16 v[32:35], v[208:211], v[16:19], v[32:35]\n"
    "v_mfma_f32_16x16x32_bf16 v[36:39], v[212:215], v[16:19], v[36:39]\n"
    "v_mfma_f32_16x16x32_bf16 v[56:59], v[220:223], v[16:19], v[56:59]\n"
    "s_waitcnt lgkmcnt(2)\n"
    "v_mfma_f32_16x16x32_bf16 v[60:63], v[224:227], v[16:19], v[60:63]\n"
    "ds_read_b128 v[16:19], v9 offset:5120\n"
    "v_mfma_f32_16x16x32_bf16 v[32:35], v[232:235], v[20:23], v[32:35]\n"
    "v_mfma_f32_16x16x32_bf16 v[36:39], v[236:239], v[20:23], v[36:39]\n"
    "v_mfma_f32_16x16x32_bf16 v[56:59], v[244:247], v[20:23], v[56:59]\n"
    "s_waitcnt lgkmcnt(2)\n"
    "v_mfma_f32_16x16x32_bf16 v[60:63], v[248:251], v[20:23], v[60:63]\n"
    "ds_read_b128 v[20:23], v9 offset:7168\n"
    "s_waitcnt vmcnt(1)\n"
    "v_add_u32 v13, v5, v3\n"
    "v_mfma_f32_16x16x32_bf16 v[40:43], a[0:3], v[24:27], 0\n"
    "v_cmp_gt_i32 vcc, s71, v13\n"
    "v_mfma_f32_16x16x32_bf16 v[44:47], a[4:7], v[24:27], 0\n"
    "v_mul_f32 v56, v32, v56\n"
    "v_mul_f32 v57, v33, v57\n"
    "v_mfma_f32_16x16x32_bf16 v[64:67], a[12:15], v[24:27], 0\n"
    "s_waitcnt lgkmcnt(2)\n"
    "v_mul_f32 v58, v34, v58\n"
    "v_mul_f32 v59, v35, v59\n"
    "v_mfma_f32_16x16x32_bf16 v[68:71], a[16:19], v[24:27], 0\n"
    "ds_read_b128 v[24:27], v9 offset:9216\n"
    "v_mul_f32 v60, v36, v60\n"
    "s_cbranch_vccz .LgUP_dma148_%=\n"
    "v_mfma_f32_16x16x32_bf16 v[40:43], a[24:27], v[28:31], v[40:43]\n"
    "s_or_b64 exec, vcc, 1\n"
    "buffer_atomic_smax v5, v2, s[36:39], 0 offen sc0 sc1\n"
    "s_mov_b64 exec, -1\n"
    "v_mul_f32 v61, v37, v61\n"
    "v_mul_f32 v62, v38, v62\n"
    "v_mfma_f32_16x16x32_bf16 v[44:47], a[28:31], v[28:31], v[44:47]\n"
    "v_mul_f32 v63, v39, v63\n"
    "v_mul_f32 v32, 0xbfb8aa3b, v32\n"
    "v_mfma_f32_16x16x32_bf16 v[64:67], a[36:39], v[28:31], v[64:67]\n"
    "s_waitcnt lgkmcnt(2)\n"
    "v_exp_f32 v32, v32\n"
    "v_mfma_f32_16x16x32_bf16 v[68:71], a[40:43], v[28:31], v[68:71]\n"
    "ds_read_b128 v[28:31], v9 offset:11264\n"
    "v_mul_f32 v33, 0xbfb8aa3b, v33\n"
    "v_mfma_f32_16x16x32_bf16 v[40:43], a[48:51], v[16:19], v[40:43]\n"
    "v_exp_f32 v33, v33\n"
    "v_mfma_f32_16x16x32_bf16 v[44:47], a[52:55], v[16:19], v[44:47]\n"
    "v_mul_f32 v34, 0xbfb8aa3b, v34\n"
    "v_mul_f32 v35, 0xbfb8aa3b, v35\n"
    "v_mfma_f32_16x16x32_bf16 v[64:67], a[60:63], v[16:19], v[64:67]\n"
    "s_waitcnt lgkmcnt(2)\n"
    "v_exp_f32 v34, v34\n"
    "v_mfma_f32_16x16x32_bf16 v[68:71], a[64:67], v[16:19], v[68:71]\n"
    "ds_read_b128 v[16:19], v9 offset:13312\n"
    "v_mul_f32 v36, 0xbfb8aa3b, v36\n"
    "v_mfma_f32_16x16x32_bf16 v[40:43], a[72:75], v[20:23], v[40:43]\n"
    "v_exp_f32 v35, v35\n"
    "v_mfma_f32_16x16x32_bf16 v[44:47], a[76:79], v[20:23], v[44:47]\n"
    "v_exp_f32 v36, v36\n"
    "v_mfma_f32_16x16x32_bf16 v[64:67], a[84:87], v[20:23], v[64:67]\n"
    "s_waitcnt lgkmcnt(2)\n"
    "v_mul_f32 v37, 0xbfb8aa3b, v37\n"
    "v_mul_f32 v38, 0xbfb8aa3b, v38\n"
    "v_mfma_f32_16x16x32_bf16 v[68:71], a[88:91], v[20:23], v[68:71]\n"
    "ds_read_b128 v[20:23], v9 offset:15360\n"
    "v_mul_f32 v39, 0xbfb8aa3b, v39\n"
    "v_mfma_f32_16x16x32_bf16 v[40:43], a[96:99], v[24:27], v[40:43]\n"
    "v_exp_f32 v37, v37\n"
    "v_mfma_f32_16x16x32_bf16 v[44:47], a[100:103], v[24:27], v[44:47]\n"
    "v_exp_f32 v38, v38\n"
    "v_mfma_f32_16x16x32_bf16 v[64:67], a[108:111], v[24:27], v[64:67]\n"
    "s_waitcnt lgkmcnt(2)\n"
    "v_exp_f32 v39, v39\n"
    "v_mfma_f32_16x16x32_bf16 v[68:71], a[112:115], v[24:27], v[68:71]\n"
    "ds_read_b128 v[24:27], v9 offset:17408\n"
    "s_waitcnt vmcnt(1)\n"
    "v_add_f32 v32, 1.0, v32\n"
    "v_add_u32 v13, v11, v3\n"
    "v_mfma_f32_16x16x32_bf16 v[40:43], a[120:123], v[28:31], v[40:43]\n"
    "v_rcp_f32 v32, v32\n"
    "v_cmp_gt_i32 vcc, s71, v13\n"
    "v_mfma_f32_16x16x32_bf16 v[44:47], a[124:127], v[28:31], v[44:47]\n"
    "v_add_f32 v33, 1.0, v33\n"
    "v_add_f32 v34, 1.0, v34\n"
    "v_mfma_f32_16x16x32_bf16 v[64:67], a[132:135], v[28:31], v[64:67]\n"
    "s_waitcnt lgkmcnt(2)\n"
    "v_rcp_f32 v33, v33\n"
    "v_mfma_f32_16x16x32_bf16 v[68:71], a[136:139], v[28:31], v[68:71]\n"
    "ds_read_b128 v[28:31], v9 offset:19456\n"
    "v_add_f32 v35, 1.0, v35\n"
    "s_cbranch_vccz .LgUP_dma168_%=\n"
    "v_mfma_f32_16x16x32_bf16 v[40:43], a[144:147], v[16:19], v[40:43]\n"
    "s_or_b64 exec, vcc, 1\n"
    "buffer_atomic_smax v11, v2, s[36:39], 0 offen sc0 sc1\n"
    "s_mov_b64 exec, -1\n"
    "buffer_store_dwordx4 v[48:51], v4, s[36:39], s73 offen sc1\n"
    "v_add_f32 v36, 1.0, v36\n"
    "v_mfma_f32_16x16x32_bf16 v[44:47], a[148:151], v[16:19], v[44:47]\n"
    "v_rcp_f32 v34, v34\n"
    "v_mfma_f32_16x16x32_bf16 v[64:67], a[156:159], v[16:19], v[64:67]\n"
    "s_waitcnt lgkmcnt(2)\n"
    "v_rcp_f32 v35, v35\n"
    "v_mfma_f32_16x16x32_bf16 v[68:71], a[160:163], v[16:19], v[68:71]\n"
    "ds_read_b128 v[16:19], v9 offset:21504\n"
    "v_add_f32 v37, 1.0, v37\n"
    "v_mfma_f32_16x16x32_bf16 v[40:43], a[168:171], v[20:23], v[40:43]\n"
    "v_rcp_f32 v36, v36\n"
    "v_mfma_f32_16x16x32_bf16 v[44:47], a[172:175], v[20:23], v[44:47]\n"
    "v_rcp_f32 v37, v37\n"
    "v_mfma_f32_16x16x32_bf16 v[64:67], a[180:183], v[20:23], v[64:67]\n"
    "s_waitcnt lgkmcnt(2)\n"
    "v_add_f32 v38, 1.0, v38\n"
    "v_add_f32 v39, 1.0, v39\n"
    "v_mfma_f32_16x16x32_bf16 v[68:71], a[184:187], v[20:23], v[68:71]\n"
    "ds_read_b128 v[20:23], v9 offset:23552\n"
    "v_mul_f32 v32, v32, v56\n"
    "v_mfma_f32_16x16x32_bf16 v[40:43], a[192:195], v[24:27], v[40:43]\n"
    "v_rcp_f32 v38, v38\n"
    "v_mfma_f32_16x16x32_bf16 v[44:47], a[196:199], v[24:27], v[44:47]\n"
    "v_rcp_f32 v39, v39\n"
    "v_mfma_f32_16x16x32_bf16 v[64:67], a[204:207], v[24:27], v[64:67]\n"
    "s_waitcnt lgkmcnt(2)\n"
    "v_mul_f32 v33, v33, v57\n"
    "v_mul_f32 v34, v34, v58\n"
    "v_mfma_f32_16x16x32_bf16 v[68:71], a[208:211], v[24:27], v[68:71]\n"
    "ds_read_b128 v[24:27], v9 offset:25600\n"
    "v_mul_f32 v35, v35, v59\n"
    "v_mfma_f32_16x16x32_bf16 v[40:43], a[216:219], v[28:31], v[40:43]\n"
    "v_mul_f32 v36, v36, v60\n"
    "v_mul_f32 v37, v37, v61\n"
    "v_mfma_f32_16x16x32_bf16 v[44:47], a[220:223], v[28:31], v[44:47]\n"
    "v_mul_f32 v38, v38, v62\n"
    "v_mul_f32 v39, v39, v63\n"
    "v_mfma_f32_16x16x32_bf16 v[64:67], a[228:231], v[28:31], v[64:67]\n"
    "s_waitcnt lgkmcnt(2)\n"
    "v_cvt_pk_bf16_f32 v32, v32, v33\n"
    "v_cvt_pk_bf16_f32 v33, v34, v35\n"
    "v_mfma_f32_16x16x32_bf16 v[68:71], a[232:235], v[28:31], v[68:71]\n"
    "ds_read_b128 v[28:31], v9 offset:27648\n"
    "s_waitcnt vmcnt(2)\n"
    "v_cvt_pk_bf16_f32 v34, v36, v37\n"
    "v_add_u32 v13, v5, v3\n"
    "v_mfma_f32_16x16x32_bf16 v[40:43], a[240:243], v[16:19], v[40:43]\n"
    "v_cvt_pk_bf16_f32 v35, v38, v39\n"
    "v_cmp_gt_i32 vcc, s71, v13\n"
    "v_mfma_f32_16x16x32_bf16 v[44:47], a[244:247], v[16:19], v[44:47]\n"
    "buffer_store_dwordx4 v[32:35], v15, s[36:39], s73 offen sc1\n"
    "v_mfma_f32_16x16x32_bf16 v[64:67], a[252:255], v[16:19], v[64:67]\n"
    "s_waitcnt lgkmcnt(2)\n"
    "v_mfma_f32_16x16x32_bf16 v[68:71], v[80:83], v[16:19], v[68:71]\n"
    "ds_read_b128 v[16:19], v9 offset:29696\n"
    "s_cbranch_vccz .LgUP_dma188_%=\n"
    "v_mfma_f32_16x16x32_bf16 v[40:43], v[88:91], v[20:23], v[40:43]\n"
    "s_or_b64 exec, vcc, 1\n"
    "buffer_atomic_smax v5, v2, s[36:39], 0 offen sc0 sc1\n"
    "s_mov_b64 exec, -1\n"
    "v_mfma_f32_16x16x32_bf16 v[44:47], v[92:95], v[20:23], v[44:47]\n"
    "v_mfma_f32_16x16x32_bf16 v[64:67], v[100:103], v[20:23], v[64:67]\n"
    "s_waitcnt lgkmcnt(2)\n"
    "v_mfma_f32_16x16x32_bf16 v[68:71], v[104:107], v[20:23], v[68:71]\n"
    "ds_read_b128 v[20:23], v9 offset:31744\n"
    "v_mfma_f32_16x16x32_bf16 v[40:43], v[112:115], v[24:27], v[40:43]\n"
    "v_mfma_f32_16x16x32_bf16 v[44:47], v[116:119], v[24:27], v[44:47]\n"
    "v_mfma_f32_16x16x32_bf16 v[64:67], v[124:127], v[24:27], v[64:67]\n"
    "s_waitcnt lgkmcnt(2)\n"
    "v_mfma_f32_16x16x32_bf16 v[68:71], v[128:131], v[24:27], v[68:71]\n"
    "ds_read_b128 v[24:27], v9 offset:33792\n"
    "v_mfma_f32_16x16x32_bf16 v[40:43], v[136:139], v[28:31], v[40:43]\n"
    "v_mfma_f32_16x16x32_bf16 v[44:47], v[140:143], v[28:31], v[44:47]\n"
    "v_mfma_f32_16x16x32_bf16 v[64:67], v[148:151], v[28:31], v[64:67]\n"
    "s_waitcnt lgkmcnt(2)\n"
    "v_mfma_f32_16x16x32_bf16 v[68:71], v[152:155], v[28:31], v[68:71]\n"
    "ds_read_b128 v[28:31], v9 offset:35840\n"
    "v_mfma_f32_16x16x32_bf16 v[40:43], v[160:163], v[16:19], v[40:43]\n"
    "v_mfma_f32_16x16x32_bf16 v[44:47], v[164:167], v[16:19], v[44:47]\n"
    "v_mfma_f32_16x16x32_bf16 v[64:67], v[172:175], v[16:19], v[64:67]\n"
    "s_waitcnt lgkmcnt(2)\n"
    "v_mfma_f32_16x16x32_bf16 v[68:71], v[176:179], v[16:19], v[68:71]\n"
    "s_waitcnt vmcnt(3)\n"
    "v_add_u32 v13, v11, v3\n"
    "v_mfma_f32_16x16x32_bf16 v[40:43], v[184:187], v[20:23], v[40:43]\n"
    "v_cmp_gt_i32 vcc, s71, v13\n"
    "v_mfma_f32_16x16x32_bf16 v[44:47], v[188:191], v[20:23], v[44:47]\n"
    "v_mfma_f32_16x16x32_bf16 v[64:67], v[196:199], v[20:23], v[64:67]\n"
    "s_waitcnt lgkmcnt(1)\n"
    "v_mfma_f32_16x16x32_bf16 v[68:71], v[200:203], v[20:23], v[68:71]\n"
    "s_cbranch_vccz .LgUP_dma208_%=\n"
    "v_mfma_f32_16x16x32_bf16 v[40:43], v[208:211], v[24:27], v[40:43]\n"
    "s_or_b64 exec, vcc, 1\n"
    "buffer_atomic_smax v11, v2, s[36:39], 0 offen sc0 sc1\n"
    "s_mov_b64 exec, -1\n"
    "v_mfma_f32_16x16x32_bf16 v[44:47], v[212:215], v[24:27], v[44:47]\n"
    "v_mfma_f32_16x16x32_bf16 v[64:67], v[220:223], v[24:27], v[64:67]\n"
    "s_waitcnt lgkmcnt(0)\n"
    "v_mfma_f32_16x16x32_bf16 v[68:71], v[224:227], v[24:27], v[68:71]\n"
    "v_mfma_f32_16x16x32_bf16 v[40:43], v[232:235], v[28:31], v[40:43]\n"
    "v_mfma_f32_16x16x32_bf16 v[44:47], v[236:239], v[28:31], v[44:47]\n"
    "v_mfma_f32_16x16x32_bf16 v[64:67], v[244:247], v[28:31], v[64:67]\n"
    "v_mfma_f32_16x16x32_bf16 v[68:71], v[248:251], v[28:31], v[68:71]\n"
    "s_nop 3\n"
    "v_pk_mul_f32 v[64:65], v[40:41], v[64:65]\n"
    "v_pk_mul_f32 v[66:67], v[42:43], v[66:67]\n"
    "s_nop 1\n"
    "v_pk_mul_f32 v[68:69], v[44:45], v[68:69]\n"
    "v_pk_mul_f32 v[70:71], v[46:47], v[70:71]\n"
    "v_pk_mul_f32 v[40:41], v[40:41], s[64:65] op_sel_hi:[1,0]\n"
    "v_pk_mul_f32 v[42:43], v[42:43], s[64:65] op_sel_hi:[1,0]\n"
    "v_pk_mul_f32 v[44:45], v[44:45], s[64:65] op_sel_hi:[1,0]\n"
    "v_pk_mul_f32 v[46:47], v[46:47], s[64:65] op_sel_hi:[1,0]\n"
    "v_exp_f32 v40, v40\n"
    "v_exp_f32 v41, v41\n"
    "v_exp_f32 v42, v42\n"
    "v_exp_f32 v43, v43\n"
    "v_exp_f32 v44, v44\n"
    "v_exp_f32 v45, v45\n"
    "v_exp_f32 v46, v46\n"
    "v_exp_f32 v47, v47\n"
    "v_pk_add_f32 v[40:41], v[40:41], 1.0 op_sel_hi:[1,0]\n"
    "v_pk_add_f32 v[42:43], v[42:43], 1.0 op_sel_hi:[1,0]\n"
    "v_pk_add_f32 v[44:45], v[44:45], 1.0 op_sel_hi:[1,0]\n"
    "v_pk_add_f32 v[46:47], v[46:47], 1.0 op_sel_hi:[1,0]\n"
    "v_rcp_f32 v40, v40\n"
    "v_rcp_f32 v41, v41\n"
    "v_rcp_f32 v42, v42\n"
    "v_rcp_f32 v43, v43\n"
    "v_rcp_f32 v44, v44\n"
    "v_rcp_f32 v45, v45\n"
    "v_rcp_f32 v46, v46\n"
    "v_rcp_f32 v47, v47\n"
    "v_pk_mul_f32 v[40:41], v[40:41], v[64:65]\n"
    "v_pk_mul_f32 v[42:43], v[42:43], v[66:67]\n"
    "v_pk_mul_f32 v[44:45], v[44:45], v[68:69]\n"
    "v_pk_mul_f32 v[46:47], v[46:47], v[70:71]\n"
    "v_cvt_pk_bf16_f32 v40, v40, v41\n"
    "v_cvt_pk_bf16_f32 v41, v42, v43\n"
    "v_cvt_pk_bf16_f32 v42, v44, v45\n"
    "v_cvt_pk_bf16_f32 v43, v46, v47\n"
    "buffer_store_dwordx4 v[40:43], v15, s[36:39], s73 offen offset:1024 sc1\n"
    "v_mov_b32 v12, s68\n"
    "s_mov_b32 s68, s71\n"
    "s_cmp_eq_u32 s68, 64\n"
    "s_cbranch_scc1 .LgUP_done_%=\n"
    "s_waitcnt vmcnt(2)\n"
    "v_add_u32 v13, v5, v3\n"
    "v_cmp_gt_i32 vcc, s71, v13\n"
    "s_cbranch_vccnz .LgUP_idle_%=\n"
    "s_add_u32 m0, s77, s59\n"
    "s_add_u32 s77, s77, 8192\n"
    "buffer_load_dwordx4 v1, s[36:39], s76 offen sc1 lds\n"
    "buffer_load_dwordx4 v1, s[36:39], s76 offen offset:1024 sc1 lds\n"
    "s_add_u32 s76, s76, 16384\n"
    "s_add_u32 m0, s77, s59\n"
    "s_add_u32 s77, s77, 8192\n"
    "buffer_load_dwordx4 v1, s[36:39], s76 offen sc1 lds\n"
    "buffer_load_dwordx4 v1, s[36:39], s76 offen offset:1024 sc1 lds\n"
    "s_add_u32 s76, s76, 16384\n"
    "s_add_u32 m0, s77, s59\n"
    "s_add_u32 s77, s77, 8192\n"
    "buffer_load_dwordx4 v1, s[36:39], s76 offen sc1 lds\n"
    "buffer_load_dwordx4 v1, s[36:39], s76 offen offset:1024 sc1 lds\n"
    "s_add_u32 s76, s76, 16384\n"
    "s_add_u32 m0, s77, s59\n"
    "s_add_u32 s77, s77, 8192\n"
    "buffer_load_dwordx4 v1, s[36:39], s76 offen sc1 lds\n"
    "buffer_load_dwordx4 v1, s[36:39], s76 offen offset:1024 sc1 lds\n"
    "s_add_u32 s76, s76, 16384\n"
    "s_mov_b32 m0, s77\n"
    "s_nop 0\n"
    "buffer_load_dwordx4 v8, s[36:39], s76 offen sc1 lds\n"
    ".LgUP_late_%=:\n"
    "s_waitcnt vmcnt(9)\n"
    "buffer_atomic_smax v12, v14, s[36:39], 0 offen sc1\n"
    "s_waitcnt vmcnt(1)\n"
    ".LgUP_bar_%=:\n"
    "v_xor_b32 v9, s78, v9\n"
    "s_add_u32 s71, s68, 1\n"
    "s_barrier\n"
    "ds_read_b128 v[16:19], v9 offset:0\n"
    "ds_read_b128 v[20:23], v9 offset:1024\n"
    "ds_read_b128 v[24:27], v9 offset:2048\n"
    "ds_read_b128 v[28:31], v9 offset:3072\n"
    "v_add_u32 v13, v11, v3\n"
    "v_cmp_gt_i32 vcc, s71, v13\n"
    "s_nop 0\n"
    "s_or_b64 exec, vcc, 1\n"
    "buffer_atomic_smax v11, v2, s[36:39], 0 offen sc0 sc1\n"
    "s_mov_b64 exec, -1\n"
    "s_waitcnt lgkmcnt(3)\n"
    "v_mfma_f32_16x16x32_bf16 v[48:51], a[8:11], v[16:19], 0\n"
    "s_waitcnt lgkmcnt(2)\n"
    "v_mfma_f32_16x16x32_bf16 v[72:75], a[20:23], v[16:19], 0\n"
    "ds_read_b128 v[16:19], v9 offset:4096\n"
    "v_mfma_f32_16x16x32_bf16 v[52:55], a[8:11], v[20:23], 0\n"
    "s_waitcnt lgkmcnt(2)\n"
    "v_mfma_f32_16x16x32_bf16 v[76:79], a[20:23], v[20:23], 0\n"
    "ds_read_b128 v[20:23], v9 offset:5120\n"
    "v_mfma_f32_16x16x32_bf16 v[48:51], a[32:35], v[24:27], v[48:51]\n"
    "s_waitcnt lgkmcnt(2)\n"
    "v_mfma_f32_16x16x32_bf16 v[72:75], a[44:47], v[24:27], v[72:75]\n"
    "ds_read_b128 v[24:27], v9 offset:6144\n"
    "v_mfma_f32_16x16x32_bf16 v[52:55], a[32:35], v[28:31], v[52:55]\n"
    "s_waitcnt lgkmcnt(2)\n"
    "v_mfma_f32_16x16x32_bf16 v[76:79], a[44:47], v[28:31], v[76:79]\n"
    "ds_read_b128 v[28:31], v9 offset:7168\n"
    "v_mfma_f32_16x16x32_bf16 v[48:51], a[56:59], v[16:19], v[48:51]\n"
    "s_waitcnt lgkmcnt(2)\n"
    "v_mfma_f32_16x16x32_bf16 v[72:75], a[68:71], v[16:19], v[72:75]\n"
    "ds_read_b128 v[16:19], v9 offset:8192\n"
    "v_mfma_f32_16x16x32_bf16 v[52:55], a[56:59], v[20:23], v[52:55]\n"
    "s_waitcnt lgkmcnt(2)\n"
    "s_waitcnt vmcnt(1)\n"
    "buffer_atomic_smax v12, v14, s[36:39], 0 offen sc1\n"
    "v_mfma_f32_16x16x32_bf16 v[76:79], a[68:71], v[20:23], v[76:79]\n"
    "ds_read_b128 v[20:23], v9 offset:9216\n"
    "v_mfma_f32_16x16x32_bf16 v[48:51], a[80:83], v[24:27], v[48:51]\n"
    "s_waitcnt lgkmcnt(2)\n"
    "v_mfma_f32_16x16x32_bf16 v[72:75], a[92:95], v[24:27], v[72:75]\n"
    "ds_read_b128 v[24:27], v9 offset:10240\n"
    "v_mfma_f32_16x16x32_bf16 v[52:55], a[80:83], v[28:31], v[52:55]\n"
    "s_waitcnt lgkmcnt(2)\n"
    "v_mfma_f32_16x16x32_bf16 v[76:79], a[92:95], v[28:31], v[76:79]\n"
    "ds_read_b128 v[28:31], v9 offset:11264\n"
    "v_mfma_f32_16x16x32_bf16 v[48:51], a[104:107], v[16:19], v[48:51]\n"
    "s_waitcnt lgkmcnt(2)\n"
    "v_mfma_f32_16x16x32_bf16 v[72:75], a[116:119], v[16:19], v[72:75]\n"
    "ds_read_b128 v[16:19], v9 offset:12288\n"
    "v_mfma_f32_16x16x32_bf16 v[52:55], a[104:107], v[20:23], v[52:55]\n"
    "s_waitcnt lgkmcnt(2)\n"
    "v_mfma_f32_16x16x32_bf16 v[76:79], a[116:119], v[20:23], v[76:79]\n"
    "ds_read_b128 v[20:23], v9 offset:13312\n"
    "v_mfma_f32_16x16x32_bf16 v[48:51], a[128:131], v[24:27], v[48:51]\n"
    "s_waitcnt lgkmcnt(2)\n"
    "v_mfma_f32_16x16x32_bf16 v[72:75], a[140:143], v[24:27], v[72:75]\n"
    "ds_read_b128 v[24:27], v9 offset:14336\n"
    "v_mfma_f32_16x16x32_bf16 v[52:55], a[128:131], v[28:31], v[52:55]\n"
    "s_waitcnt lgkmcnt(2)\n"
    "v_mfma_f32_16x16x32_bf16 v[76:79], a[140:143], v[28:31], v[76:79]\n"
    "ds_read_b128 v[28:31], v9 offset:15360\n"
    "v_mfma_f32_16x16x32_bf16 v[48:51], a[152:155], v[16:19], v[48:51]\n"
    "s_waitcnt lgkmcnt(2)\n"
    "v_mfma_f32_16x16x32_bf16 v[72:75], a[164:167], v[16:19], v[72:75]\n"
    "ds_read_b128 v[16:19], v9 offset:16384\n"
    "v_mfma_f32_16x16x32_bf16 v[52:55], a[152:155], v[20:23], v[52:55]\n"
    "s_waitcnt lgkmcnt(2)\n"
    "v_mfma_f32_16x16x32_bf16 v[76:79], a[164:167], v[20:23], v[76:79]\n"
    "ds_read_b128 v[20:23], v9 offset:17408\n"
    "v_mfma_f32_16x16x32_bf16 v[48:51], a[176:179], v[24:27], v[48:51]\n"
    "s_waitcnt lgkmcnt(2)\n"
    "v_mfma_f32_16x16x32_bf16 v[72:75], a[188:191], v[24:27], v[72:75]\n"
    "ds_read_b128 v[24:27], v9 offset:18432\n"
    "v_mfma_f32_16x16x32_bf16 v[52:55], a[176:179], v[28:31], v[52:55]\n"
    "s_waitcnt lgkmcnt(2)\n"
    "v_mfma_f32_16x16x32_bf16 v[76:79], a[188:191], v[28:31], v[76:79]\n"
    "ds_read_b128 v[28:31], v9 offset:19456\n"
    "v_mfma_f32_16x16x32_bf16 v[48:51], a[200:203], v[16:19], v[48:51]\n"
    "s_waitcnt lgkmcnt(2)\n"
    "v_mfma_f32_16x16x32_bf16 v[72:75], a[212:215], v[16:19], v[72:75]\n"
    "ds_read_b128 v[16:19], v9 offset:20480\n"
    "v_mfma_f32_16x16x32_bf16 v[52:55], a[200:203], v[20:23], v[52:55]\n"
    "s_waitcnt lgkmcnt(2)\n"
    "v_mfma_f32_16x16x32_bf16 v[76:79], a[212:215], v[20:23], v[76:79]\n"
    "ds_read_b128 v[20:23], v9 offset:21504\n"
    "v_mfma_f32_16x16x32_bf16 v[48:51], a[224:227], v[24:27], v[48:51]\n"
    "s_waitcnt lgkmcnt(2)\n"
    "v_mfma_f32_16x16x32_bf16 v[72:75], a[236:239], v[24:27], v[72:75]\n"
    "ds_read_b128 v[24:27], v9 offset:22528\n"
    "v_mfma_f32_16x16x32_bf16 v[52:55], a[224:227], v[28:31], v[52:55]\n"
    "s_waitcnt lgkmcnt(2)\n"
    "v_mfma_f32_16x16x32_bf16 v[76:79], a[236:239], v[28:31], v[76:79]\n"
    "ds_read_b128 v[28:31], v9 offset:23552\n"
    "v_mfma_f32_16x16x32_bf16 v[48:51], a[248:251], v[16:19], v[48:51]\n"
    "s_waitcnt lgkmcnt(2)\n"
    "v_mfma_f32_16x16x32_bf16 v[72:75], v[84:87], v[16:19], v[72:75]\n"
    "ds_read_b128 v[16:19], v9 offset:24576\n"
    "v_mfma_f32_16x16x32_bf16 v[52:55], a[248:251], v[20:23], v[52:55]\n"
    "s_waitcnt lgkmcnt(2)\n"
    "v_mfma_f32_16x16x32_bf16 v[76:79], v[84:87], v[20:23], v[76:79]\n"
    "ds_read_b128 v[20:23], v9 offset:25600\n"
    "v_mfma_f32_16x16x32_bf16 v[48:51], v[96:99], v[24:27], v[48:51]\n"
    "s_waitcnt lgkmcnt(2)\n"
    "v_mfma_f32_16x16x32_bf16 v[72:75], v[108:111], v[24:27], v[72:75]\n"
    "ds_read_b128 v[24:27], v9 offset:26624\n"
    "v_mfma_f32_16x16x32_bf16 v[52:55], v[96:99], v[28:31], v[52:55]\n"
    "s_waitcnt lgkmcnt(2)\n"
    "v_mfma_f32_16x16x32_bf16 v[76:79], v[108:111], v[28:31], v[76:79]\n"
    "ds_read_b128 v[28:31], v9 offset:27648\n"
    "v_mfma_f32_16x16x32_bf16 v[48:51], v[120:123], v[16:19], v[48:51]\n"
    "s_waitcnt lgkmcnt(2)\n"
    "v_mfma_f32_16x16x32_bf16 v[72:75], v[132:135], v[16:19], v[72:75]\n"
    "ds_read_b128 v[16:19], v9 offset:28672\n"
    "v_mfma_f32_16x16x32_bf16 v[52:55], v[120:123], v[20:23], v[52:55]\n"
    "s_waitcnt lgkmcnt(2)\n"
    "s_and_b32 s76, s71, s54\n"
    "v_mfma_f32_16x16x32_bf16 v[76:79], v[132:135], v[20:23], v[76:79]\n"
    "ds_read_b128 v[20:23], v9 offset:29696\n"
    "v_mfma_f32_16x16x32_bf16 v[48:51], v[144:147], v[24:27], v[48:51]\n"
    "s_waitcnt lgkmcnt(2)\n"
    "s_mul_i32 s76, s76, s53\n"
    "v_mfma_f32_16x16x32_bf16 v[72:75], v[156:159], v[24:27], v[72:75]\n"
    "ds_read_b128 v[24:27], v9 offset:30720\n"
    "v_mfma_f32_16x16x32_bf16 v[52:55], v[144:147], v[28:31], v[52:55]\n"
    "s_waitcnt lgkmcnt(2)\n"
    "s_add_u32 s76, s76, s52\n"
    "v_mfma_f32_16x16x32_bf16 v[76:79], v[156:159], v[28:31], v[76:79]\n"
    "ds_read_b128 v[28:31], v9 offset:31744\n"
    "v_mfma_f32_16x16x32_bf16 v[48:51], v[168:171], v[16:19], v[48:51]\n"
    "s_waitcnt lgkmcnt(2)\n"
    "s_and_b32 s77, s71, 1\n"
    "v_mfma_f32_16x16x32_bf16 v[72:75], v[180:183], v[16:19], v[72:75]\n"
    "ds_read_b128 v[16:19], v9 offset:32768\n"
    "v_mfma_f32_16x16x32_bf16 v[52:55], v[168:171], v[20:23], v[52:55]\n"
    "s_waitcnt lgkmcnt(2)\n"
    "s_mul_i32 s77, s77, 65536\n"
    "v_mfma_f32_16x16x32_bf16 v[76:79], v[180:183], v[20:23], v[76:79]\n"
    "ds_read_b128 v[20:23], v9 offset:33792\n"
    "v_mfma_f32_16x16x32_bf16 v[48:51], v[192:195], v[24:27], v[48:51]\n"
    "s_waitcnt lgkmcnt(2)\n"
    "s_add_u32 s77, s77, s59\n"
    "v_mfma_f32_16x16x32_bf16 v[72:75], v[204:207], v[24:27], v[72:75]\n"
    "ds_read_b128 v[24:27], v9 offset:34816\n"
    "v_mfma_f32_16x16x32_bf16 v[52:55], v[192:195], v[28:31], v[52:55]\n"
    "s_waitcnt lgkmcnt(2)\n"
    "s_and_b32 s73, s68, s57\n"
    "v_mfma_f32_16x16x32_bf16 v[76:79], v[204:207], v[28:31], v[76:79]\n"
    "ds_read_b128 v[28:31], v9 offset:35840\n"
    "v_mfma_f32_16x16x32_bf16 v[48:51], v[216:219], v[16:19], v[48:51]\n"
    "s_waitcnt lgkmcnt(2)\n"
    "s_mul_i32 s73, s73, s56\n"
    "v_mfma_f32_16x16x32_bf16 v[72:75], v[228:231], v[16:19], v[72:75]\n"
    "ds_read_b128 v[16:19], v9 offset:0\n"
    "v_add_u32 v13, v5, v3\n"
    "v_mfma_f32_16x16x32_bf16 v[52:55], v[216:219], v[20:23], v[52:55]\n"
    "s_waitcnt lgkmcnt(2)\n"
    "v_cmp_gt_i32 vcc, s71, v13\n"
    "v_mfma_f32_16x16x32_bf16 v[76:79], v[228:231], v[20:23], v[76:79]\n"
    "ds_read_b128 v[20:23], v9 offset:2048\n"
    "v_mfma_f32_16x16x32_bf16 v[48:51], v[240:243], v[24:27], v[48:51]\n"
    "s_waitcnt lgkmcnt(2)\n"
    "s_or_b64 exec, vcc, 1\n"
    "buffer_atomic_smax v5, v2, s[36:39], 0 offen sc0 sc1\n"
    "s_mov_b64 exec, -1\n"
    "v_mfma_f32_16x16x32_bf16 v[72:75], v[252:255], v[24:27], v[72:75]\n"
    "ds_read_b128 v[24:27], v9 offset:4096\n"
    "v_mfma_f32_16x16x32_bf16 v[52:55], v[240:243], v[28:31], v[52:55]\n"
    "s_waitcnt lgkmcnt(2)\n"
    "s_add_u32 s73, s73, s55\n"
    "v_mfma_f32_16x16x32_bf16 v[76:79], v[252:255], v[28:31], v[76:79]\n"
    "ds_read_b128 v[28:31], v9 offset:6144\n"
    "v_mfma_f32_16x16x32_bf16 v[32:35], a[0:3], v[16:19], 0\n"
    "v_mfma_f32_16x16x32_bf16 v[36:39], a[4:7], v[16:19], 0\n"
    "v_mul_f32 v72, v48, v72\n"
    "v_mul_f32 v73, v49, v73\n"
    "v_mfma_f32_16x16x32_bf16 v[56:59], a[12:15], v[16:19], 0\n"
    "s_waitcnt lgkmcnt(2)\n"
    "v_mul_f32 v74, v50, v74\n"
    "v_mul_f32 v75, v51, v75\n"
    "v_mfma_f32_16x16x32_bf16 v[60:63], a[16:19], v[16:19], 0\n"
    "ds_read_b128 v[16:19], v9 offset:8192\n"
    "v_mul_f32 v76, v52, v76\n"
    "v_mfma_f32_16x16x32_bf16 v[32:35], a[24:27], v[20:23], v[32:35]\n"
    "v_mul_f32 v77, v53, v77\n"
    "v_mul_f32 v78, v54, v78\n"
    "v_mfma_f32_16x16x32_bf16 v[36:39], a[28:31], v[20:23], v[36:39]\n"
    "v_mul_f32 v79, v55, v79\n"
    "v_mul_f32 v48, 0xbfb8aa3b, v48\n"
    "v_mfma_f32_16x16x32_bf16 v[56:59], a[36:39], v[20:23], v[56:59]\n"
    "s_waitcnt lgkmcnt(2)\n"
    "v_exp_f32 v48, v48\n"
    "v_mfma_f32_16x16x32_bf16 v[60:63], a[40:43], v[20:23], v[60:63]\n"
    "ds_read_b128 v[20:23], v9 offset:10240\n"
    "v_mul_f32 v49, 0xbfb8aa3b, v49\n"
    "v_mfma_f32_16x16x32_bf16 v[32:35], a[48:51], v[24:27], v[32:35]\n"
    "v_exp_f32 v49, v49\n"
    "v_mfma_f32_16x16x32_bf16 v[36:39], a[52:55], v[24:27], v[36:39]\n"
    "v_mul_f32 v50, 0xbfb8aa3b, v50\n"
    "v_mul_f32 v51, 0xbfb8aa3b, v51\n"
    "v_mfma_f32_16x16x32_bf16 v[56:59], a[60:63], v[24:27], v[56:59]\n"
    "s_waitcnt lgkmcnt(2)\n"
    "v_exp_f32 v50, v50\n"
    "v_mfma_f32_16x16x32_bf16 v[60:63], a[64:67], v[24:27], v[60:63]\n"
    "ds_read_b128 v[24:27], v9 offset:12288\n"
    "s_waitcnt vmcnt(1)\n"
    "v_mul_f32 v52, 0xbfb8aa3b, v52\n"
    "v_add_u32 v13, v11, v3\n"
    "v_mfma_f32_16x16x32_bf16 v[32:35], a[72:75], v[28:31], v[32:35]\n"
    "buffer_atomic_smax v12, v14, s[36:39], 0 offen sc1\n"
    "v_exp_f32 v51, v51\n"
    "v_cmp_gt_i32 vcc, s71, v13\n"
    "v_mfma_f32_16x16x32_bf16 v[36:39], a[76:79], v[28:31], v[36:39]\n"
    "v_exp_f32 v52, v52\n"
    "v_mfma_f32_16x16x32_bf16 v[56:59], a[84:87], v[28:31], v[56:59]\n"
    "s_waitcnt lgkmcnt(2)\n"
    "v_mul_f32 v53, 0xbfb8aa3b, v53\n"
    "v_mul_f32 v54, 0xbfb8aa3b, v54\n"
    "v_mfma_f32_16x16x32_bf16 v[60:63], a[88:91], v[28:31], v[60:63]\n"
    "ds_read_b128 v[28:31], v9 offset:14336\n"
    "v_mul_f32 v55, 0xbfb8aa3b, v55\n"
    "s_branch .LgUP_mid_%=\n"
    ".LgUP_dma108_%=:\n"
    "v_mfma_f32_16x16x32_bf16 v[32:35], a[216:219], v[20:23], v[32:35]\n"
    "s_add_u32 m0, s77, s59\n"
    "s_add_u32 s77, s77, 8192\n"
    "buffer_load_dwordx4 v1, s[36:39], s76 offen sc1 lds\n"
    "v_mul_f32 v53, v53, v77\n"
    "v_mul_f32 v54, v54, v78\n"
    "v_mfma_f32_16x16x32_bf16 v[36:39], a[220:223], v[20:23], v[36:39]\n"
    "v_mul_f32 v55, v55, v79\n"
    "v_cvt_pk_bf16_f32 v48, v48, v49\n"
    "v_mfma_f32_16x16x32_bf16 v[56:59], a[228:231], v[20:23], v[56:59]\n"
    "s_waitcnt lgkmcnt(2)\n"
    "v_cvt_pk_bf16_f32 v49, v50, v51\n"
    "v_cvt_pk_bf16_f32 v50, v52, v53\n"
    "v_mfma_f32_16x16x32_bf16 v[60:63], a[232:235], v[20:23], v[60:63]\n"
    "ds_read_b128 v[20:23], v9 offset:26624\n"
    "v_cvt_pk_bf16_f32 v51, v54, v55\n"
    "v_mfma_f32_16x16x32_bf16 v[32:35], a[240:243], v[24:27], v[32:35]\n"
    "buffer_load_dwordx4 v1, s[36:39], s76 offen offset:1024 sc1 lds\n"
    "s_add_u32 s76, s76, 16384\n"
    "s_nop 0\n"
    "v_mfma_f32_16x16x32_bf16 v[36:39], a[244:247], v[24:27], v[36:39]\n"
    "v_permlane16_swap_b32 v48, v50\n"
    "v_mfma_f32_16x16x32_bf16 v[56:59], a[252:255], v[24:27], v[56:59]\n"
    "s_waitcnt lgkmcnt(2)\n"
    "v_permlane16_swap_b32 v49, v51\n"
    "v_mfma_f32_16x16x32_bf16 v[60:63], v[80:83], v[24:27], v[60:63]\n"
    "ds_read_b128 v[24:27], v9 offset:28672\n"
    "v_mfma_f32_16x16x32_bf16 v[32:35], v[88:91], v[28:31], v[32:35]\n"
    "s_add_u32 m0, s77, s59\n"
    "s_add_u32 s77, s77, 8192\n"
    "buffer_load_dwordx4 v1, s[36:39], s76 offen sc1 lds\n"
    "v_mfma_f32_16x16x32_bf16 v[36:39], v[92:95], v[28:31], v[36:39]\n"
    "v_mfma_f32_16x16x32_bf16 v[56:59], v[100:103], v[28:31], v[56:59]\n"
    "s_waitcnt lgkmcnt(2)\n"
    "v_mfma_f32_16x16x32_bf16 v[60:63], v[104:107], v[28:31], v[60:63]\n"
    "ds_read_b128 v[28:31], v9 offset:30720\n"
    "v_mfma_f32_16x16x32_bf16 v[32:35], v[112:115], v[16:19], v[32:35]\n"
    "v_mfma_f32_16x16x32_bf16 v[36:39], v[116:119], v[16:19], v[36:39]\n"
    "buffer_load_dwordx4 v1, s[36:39], s76 offen offset:1024 sc1 lds\n"
    "s_add_u32 s76, s76, 16384\n"
    "v_mfma_f32_16x16x32_bf16 v[56:59], v[124:127], v[16:19], v[56:59]\n"
    "s_waitcnt lgkmcnt(2)\n"
    "v_mfma_f32_16x16x32_bf16 v[60:63], v[128:131], v[16:19], v[60:63]\n"
    "ds_read_b128 v[16:19], v9 offset:32768\n"
    "v_mfma_f32_16x16x32_bf16 v[32:35], v[136:139], v[20:23], v[32:35]\n"
    "v_mfma_f32_16x16x32_bf16 v[36:39], v[140:143], v[20:23], v[36:39]\n"
    "s_add_u32 m0, s77, s59\n"
    "s_add_u32 s77, s77, 8192\n"
    "buffer_load_dwordx4 v1, s[36:39], s76 offen sc1 lds\n"
    "v_mfma_f32_16x16x32_bf16 v[56:59], v[148:151], v[20:23], v[56:59]\n"
    "s_waitcnt lgkmcnt(2)\n"
    "v_mfma_f32_16x16x32_bf16 v[60:63], v[152:155], v[20:23], v[60:63]\n"
    "ds_read_b128 v[20:23], v9 offset:34816\n"
    "v_mfma_f32_16x16x32_bf16 v[32:35], v[160:163], v[24:27], v[32:35]\n"
    "v_mfma_f32_16x16x32_bf16 v[36:39], v[164:167], v[24:27], v[36:39]\n"
    "buffer_load_dwordx4 v1, s[36:39], s76 offen offset:1024 sc1 lds\n"
    "s_add_u32 s76, s76, 16384\n"
    "v_mfma_f32_16x16x32_bf16 v[56:59], v[172:175], v[24:27], v[56:59]\n"
    "s_waitcnt lgkmcnt(2)\n"
    "v_mfma_f32_16x16x32_bf16 v[60:63], v[176:179], v[24:27], v[60:63]\n"
    "ds_read_b128 v[24:27], v9 offset:1024\n"
    "v_mfma_f32_16x16x32_bf16 v[32:35], v[184:187], v[28:31], v[32:35]\n"
    "v_mfma_f32_16x16x32_bf16 v[36:39], v[188:191], v[28:31], v[36:39]\n"
    "v_mfma_f32_16x16x32_bf16 v[56:59], v[196:199], v[28:31], v[56:59]\n"
    "s_waitcnt lgkmcnt(2)\n"
    "s_add_u32 m0, s77, s59\n"
    "s_add_u32 s77, s77, 8192\n"
    "buffer_load_dwordx4 v1, s[36:39], s76 offen sc1 lds\n"
    "v_mfma_f32_16x16x32_bf16 v[60:63], v[200:203], v[28:31], v[60:63]\n"
    "ds_read_b128 v[28:31], v9 offset:3072\n"
    "v_mfma_f32_16x16x32_bf16 v[32:35], v[208:211], v[16:19], v[32:35]\n"
    "v_mfma_f32_16x16x32_bf16 v[36:39], v[212:215], v[16:19], v[36:39]\n"
    "v_mfma_f32_16x16x32_bf16 v[56:59], v[220:223], v[16:19], v[56:59]\n"
    "s_waitcnt lgkmcnt(2)\n"
    "buffer_load_dwordx4 v1, s[36:39], s76 offen offset:1024 sc1 lds\n"
    "s_add_u32 s76, s76, 16384\n"
    "v_mfma_f32_16x16x32_bf16 v[60:63], v[224:227], v[16:19], v[60:63]\n"
    "ds_read_b128 v[16:19], v9 offset:5120\n"
    "v_mfma_f32_16x16x32_bf16 v[32:35], v[232:235], v[20:23], v[32:35]\n"
    "v_mfma_f32_16x16x32_bf16 v[36:39], v[236:239], v[20:23], v[36:39]\n"
    "v_mfma_f32_16x16x32_bf16 v[56:59], v[244:247], v[20:23], v[56:59]\n"
    "s_waitcnt lgkmcnt(2)\n"
    "s_mov_b32 m0, s77\n"
    "s_nop 0\n"
    "buffer_load_dwordx4 v8, s[36:39], s76 offen sc1 lds\n"
    "v_mfma_f32_16x16x32_bf16 v[60:63], v[248:251], v[20:23], v[60:63]\n"
    "ds_read_b128 v[20:23], v9 offset:7168\n"
    "v_mfma_f32_16x16x32_bf16 v[40:43], a[0:3], v[24:27], 0\n"
    "v_mfma_f32_16x16x32_bf16 v[44:47], a[4:7], v[24:27], 0\n"
    "v_mul_f32 v56, v32, v56\n"
    "v_mul_f32 v57, v33, v57\n"
    "v_mfma_f32_16x16x32_bf16 v[64:67], a[12:15], v[24:27], 0\n"
    "s_waitcnt lgkmcnt(2)\n"
    "v_mul_f32 v58, v34, v58\n"
    "v_mul_f32 v59, v35, v59\n"
    "v_mfma_f32_16x16x32_bf16 v[68:71], a[16:19], v[24:27], 0\n"
    "ds_read_b128 v[24:27], v9 offset:9216\n"
    "v_mul_f32 v60, v36, v60\n"
    "s_branch .LgUP_q148_%=\n"
    ".LgUP_dma128_%=:\n"
    "v_mfma_f32_16x16x32_bf16 v[32:35], v[160:163], v[24:27], v[32:35]\n"
    "s_add_u32 m0, s77, s59\n"
    "s_add_u32 s77, s77, 8192\n"
    "buffer_load_dwordx4 v1, s[36:39], s76 offen sc1 lds\n"
    "v_mfma_f32_16x16x32_bf16 v[36:39], v[164:167], v[24:27], v[36:39]\n"
    "v_mfma_f32_16x16x32_bf16 v[56:59], v[172:175], v[24:27], v[56:59]\n"
    "s_waitcnt lgkmcnt(2)\n"
    "v_mfma_f32_16x16x32_bf16 v[60:63], v[176:179], v[24:27], v[60:63]\n"
    "ds_read_b128 v[24:27], v9 offset:1024\n"
    "v_mfma_f32_16x16x32_bf16 v[32:35], v[184:187], v[28:31], v[32:35]\n"
    "buffer_load_dwordx4 v1, s[36:39], s76 offen offset:1024 sc1 lds\n"
    "s_add_u32 s76, s76, 16384\n"
    "v_mfma_f32_16x16x32_bf16 v[36:39], v[188:191], v[28:31], v[36:39]\n"
    "v_mfma_f32_16x16x32_bf16 v[56:59], v[196:199], v[28:31], v[56:59]\n"
    "s_waitcnt lgkmcnt(2)\n"
    "v_mfma_f32_16x16x32_bf16 v[60:63], v[200:203], v[28:31], v[60:63]\n"
    "ds_read_b128 v[28:31], v9 offset:3072\n"
    "v_mfma_f32_16x16x32_bf16 v[32:35], v[208:211], v[16:19], v[32:35]\n"
    "s_add_u32 m0, s77, s59\n"
    "s_add_u32 s77, s77, 8192\n"
    "buffer_load_dwordx4 v1, s[36:39], s76 offen sc1 lds\n"
    "v_mfma_f32_16x16x32_bf16 v[36:39], v[212:215], v[16:19], v[36:39]\n"
    "v_mfma_f32_16x16x32_bf16 v[56:59], v[220:223], v[16:19], v[56:59]\n"
    "s_waitcnt lgkmcnt(2)\n"
    "v_mfma_f32_16x16x32_bf16 v[60:63], v[224:227], v[16:19], v[60:63]\n"
    "ds_read_b128 v[16:19], v9 offset:5120\n"
    "v_mfma_f32_16x16x32_bf16 v[32:35], v[232:235], v[20:23], v[32:35]\n"
    "v_mfma_f32_16x16x32_bf16 v[36:39], v[236:239], v[20:23], v[36:39]\n"
    "buffer_load_dwordx4 v1, s[36:39], s76 offen offset:1024 sc1 lds\n"
    "s_add_u32 s76, s76, 16384\n"
    "v_mfma_f32_16x16x32_bf16 v[56:59], v[244:247], v[20:23], v[56:59]\n"
    "s_waitcnt lgkmcnt(2)\n"
    "v_mfma_f32_16x16x32_bf16 v[60:63], v[248:251], v[20:23], v[60:63]\n"
    "ds_read_b128 v[20:23], v9 offset:7168\n"
    "v_mfma_f32_16x16x32_bf16 v[40:43], a[0:3], v[24:27], 0\n"
    "v_mfma_f32_16x16x32_bf16 v[44:47], a[4:7], v[24:27], 0\n"
    "s_add_u32 m0, s77, s59\n"
    "s_add_u32 s77, s77, 8192\n"
    "buffer_load_dwordx4 v1, s[36:39], s76 offen sc1 lds\n"
    "v_mul_f32 v56, v32, v56\n"
    "v_mul_f32 v57, v33, v57\n"
    "v_mfma_f32_16x16x32_bf16 v[64:67], a[12:15], v[24:27], 0\n"
    "s_waitcnt lgkmcnt(2)\n"
    "v_mul_f32 v58, v34, v58\n"
    "v_mul_f32 v59, v35, v59\n"
    "v_mfma_f32_16x16x32_bf16 v[68:71], a[16:19], v[24:27], 0\n"
    "ds_read_b128 v[24:27], v9 offset:9216\n"
    "v_mul_f32 v60, v36, v60\n"
    "v_mfma_f32_16x16x32_bf16 v[40:43], a[24:27], v[28:31], v[40:43]\n"
    "v_mul_f32 v61, v37, v61\n"
    "v_mul_f32 v62, v38, v62\n"
    "v_mfma_f32_16x16x32_bf16 v[44:47], a[28:31], v[28:31], v[44:47]\n"
    "buffer_load_dwordx4 v1, s[36:39], s76 offen offset:1024 sc1 lds\n"
    "s_add_u32 s76, s76, 16384\n"
    "v_mul_f32 v63, v39, v63\n"
    "v_mul_f32 v32, 0xbfb8aa3b, v32\n"
    "v_mfma_f32_16x16x32_bf16 v[64:67], a[36:39], v[28:31], v[64:67]\n"
    "s_waitcnt lgkmcnt(2)\n"
    "v_exp_f32 v32, v32\n"
    "v_mfma_f32_16x16x32_bf16 v[68:71], a[40:43], v[28:31], v[68:71]\n"
    "ds_read_b128 v[28:31], v9 offset:11264\n"
    "v_mul_f32 v33, 0xbfb8aa3b, v33\n"
    "v_mfma_f32_16x16x32_bf16 v[40:43], a[48:51], v[16:19], v[40:43]\n"
    "v_exp_f32 v33, v33\n"
    "v_mfma_f32_16x16x32_bf16 v[44:47], a[52:55], v[16:19], v[44:47]\n"
    "v_mul_f32 v34, 0xbfb8aa3b, v34\n"
    "v_mul_f32 v35, 0xbfb8aa3b, v35\n"
    "v_mfma_f32_16x16x32_bf16 v[64:67], a[60:63], v[16:19], v[64:67]\n"
    "s_waitcnt lgkmcnt(2)\n"
    "s_add_u32 m0, s77, s59\n"
    "s_add_u32 s77, s77, 8192\n"
    "buffer_load_dwordx4 v1, s[36:39], s76 offen sc1 lds\n"
    "v_exp_f32 v34, v34\n"
    "v_mfma_f32_16x16x32_bf16 v[68:71], a[64:67], v[16:19], v[68:71]\n"
    "ds_read_b128 v[16:19], v9 offset:13312\n"
    "v_mul_f32 v36, 0xbfb8aa3b, v36\n"
    "v_mfma_f32_16x16x32_bf16 v[40:43], a[72:75], v[20:23], v[40:43]\n"
    "v_exp_f32 v35, v35\n"
    "v_mfma_f32_16x16x32_bf16 v[44:47], a[76:79], v[20:23], v[44:47]\n"
    "v_exp_f32 v36, v36\n"
    "v_mfma_f32_16x16x32_bf16 v[64:67], a[84:87], v[20:23], v[64:67]\n"
    "s_waitcnt lgkmcnt(2)\n"
    "buffer_load_dwordx4 v1, s[36:39], s76 offen offset:1024 sc1 lds\n"
    "s_add_u32 s76, s76, 16384\n"
    "v_mul_f32 v37, 0xbfb8aa3b, v37\n"
    "v_mul_f32 v38, 0xbfb8aa3b, v38\n"
    "v_mfma_f32_16x16x32_bf16 v[68:71], a[88:91], v[20:23], v[68:71]\n"
    "ds_read_b128 v[20:23], v9 offset:15360\n"
    "v_mul_f32 v39, 0xbfb8aa3b, v39\n"
    "v_mfma_f32_16x16x32_bf16 v[40:43], a[96:99], v[24:27], v[40:43]\n"
    "v_exp_f32 v37, v37\n"
    "v_mfma_f32_16x16x32_bf16 v[44:47], a[100:103], v[24:27], v[44:47]\n"
    "v_exp_f32 v38, v38\n"
    "v_mfma_f32_16x16x32_bf16 v[64:67], a[108:111], v[24:27], v[64:67]\n"
    "s_waitcnt lgkmcnt(2)\n"
    "s_mov_b32 m0, s77\n"
    "s_nop 0\n"
    "buffer_load_dwordx4 v8, s[36:39], s76 offen sc1 lds\n"
    "v_exp_f32 v39, v39\n"
    "v_mfma_f32_16x16x32_bf16 v[68:71], a[112:115], v[24:27], v[68:71]\n"
    "ds_read_b128 v[24:27], v9 offset:17408\n"
    "v_add_f32 v32, 1.0, v32\n"
    "v_mfma_f32_16x16x32_bf16 v[40:43], a[120:123], v[28:31], v[40:43]\n"
    "v_rcp_f32 v32, v32\n"
    "v_mfma_f32_16x16x32_bf16 v[44:47], a[124:127], v[28:31], v[44:47]\n"
    "v_add_f32 v33, 1.0, v33\n"
    "v_add_f32 v34, 1.0, v34\n"
    "v_mfma_f32_16x16x32_bf16 v[64:67], a[132:135], v[28:31], v[64:67]\n"
    "s_waitcnt lgkmcnt(2)\n"
    "v_rcp_f32 v33, v33\n"
    "v_mfma_f32_16x16x32_bf16 v[68:71], a[136:139], v[28:31], v[68:71]\n"
    "ds_read_b128 v[28:31], v9 offset:19456\n"
    "v_add_f32 v35, 1.0, v35\n"
    "s_branch .LgUP_q168_%=\n"
    ".LgUP_dma148_%=:\n"
    "v_mfma_f32_16x16x32_bf16 v[40:43], a[24:27], v[28:31], v[40:43]\n"
    "s_add_u32 m0, s77, s59\n"
    "s_add_u32 s77, s77, 8192\n"
    "buffer_load_dwordx4 v1, s[36:39], s76 offen sc1 lds\n"
    "v_mul_f32 v61, v37, v61\n"
    "v_mul_f32 v62, v38, v62\n"
    "v_mfma_f32_16x16x32_bf16 v[44:47], a[28:31], v[28:31], v[44:47]\n"
    "v_mul_f32 v63, v39, v63\n"
    "v_mul_f32 v32, 0xbfb8aa3b, v32\n"
    "v_mfma_f32_16x16x32_bf16 v[64:67], a[36:39], v[28:31], v[64:67]\n"
    "s_waitcnt lgkmcnt(2)\n"
    "v_exp_f32 v32, v32\n"
    "v_mfma_f32_16x16x32_bf16 v[68:71], a[40:43], v[28:31], v[68:71]\n"
    "ds_read_b128 v[28:31], v9 offset:11264\n"
    "v_mul_f32 v33, 0xbfb8aa3b, v33\n"
    "v_mfma_f32_16x16x32_bf16 v[40:43], a[48:51], v[16:19], v[40:43]\n"
    "buffer_load_dwordx4 v1, s[36:39], s76 offen offset:1024 sc1 lds\n"
    "s_add_u32 s76, s76, 16384\n"
    "v_exp_f32 v33, v33\n"
    "v_mfma_f32_16x16x32_bf16 v[44:47], a[52:55], v[16:19], v[44:47]\n"
    "v_mul_f32 v34, 0xbfb8aa3b, v34\n"
    "v_mul_f32 v35, 0xbfb8aa3b, v35\n"
    "v_mfma_f32_16x16x32_bf16 v[64:67], a[60:63], v[16:19], v[64:67]\n"
    "s_waitcnt lgkmcnt(2)\n"
    "v_exp_f32 v34, v34\n"
    "v_mfma_f32_16x16x32_bf16 v[68:71], a[64:67], v[16:19], v[68:71]\n"
    "ds_read_b128 v[16:19], v9 offset:13312\n"
    "v_mul_f32 v36, 0xbfb8aa3b, v36\n"
    "v_mfma_f32_16x16x32_bf16 v[40:43], a[72:75], v[20:23], v[40:43]\n"
    "s_add_u32 m0, s77, s59\n"
    "s_add_u32 s77, s77, 8192\n"
    "buffer_load_dwordx4 v1, s[36:39], s76 offen sc1 lds\n"
    "v_exp_f32 v35, v35\n"
    "v_mfma_f32_16x16x32_bf16 v[44:47], a[76:79], v[20:23], v[44:47]\n"
    "v_exp_f32 v36, v36\n"
    "v_mfma_f32_16x16x32_bf16 v[64:67], a[84:87], v[20:23], v[64:67]\n"
    "s_waitcnt lgkmcnt(2)\n"
    "v_mul_f32 v37, 0xbfb8aa3b, v37\n"
    "v_mul_f32 v38, 0xbfb8aa3b, v38\n"
    "v_mfma_f32_16x16x32_bf16 v[68:71], a[88:91], v[20:23], v[68:71]\n"
    "ds_read_b128 v[20:23], v9 offset:15360\n"
    "v_mul_f32 v39, 0xbfb8aa3b, v39\n"
    "v_mfma_f32_16x16x32_bf16 v[40:43], a[96:99], v[24:27], v[40:43]\n"
    "v_exp_f32 v37, v37\n"
    "v_mfma_f32_16x16x32_bf16 v[44:47], a[100:103], v[24:27], v[44:47]\n"
    "buffer_load_dwordx4 v1, s[36:39], s76 offen offset:1024 sc1 lds\n"
    "s_add_u32 s76, s76, 16384\n"
    "v_exp_f32 v38, v38\n"
    "v_mfma_f32_16x16x32_bf16 v[64:67], a[108:111], v[24:27], v[64:67]\n"
    "s_waitcnt lgkmcnt(2)\n"
    "v_exp_f32 v39, v39\n"
    "v_mfma_f32_16x16x32_bf16 v[68:71], a[112:115], v[24:27], v[68:71]\n"
    "ds_read_b128 v[24:27], v9 offset:17408\n"
    "v_add_f32 v32, 1.0, v32\n"
    "v_mfma_f32_16x16x32_bf16 v[40:43], a[120:123], v[28:31], v[40:43]\n"
    "v_rcp_f32 v32, v32\n"
    "v_mfma_f32_16x16x32_bf16 v[44:47], a[124:127], v[28:31], v[44:47]\n"
    "s_add_u32 m0, s77, s59\n"
    "s_add_u32 s77, s77, 8192\n"
    "buffer_load_dwordx4 v1, s[36:39], s76 offen sc1 lds\n"
    "v_add_f32 v33, 1.0, v33\n"
    "v_add_f32 v34, 1.0, v34\n"
    "v_mfma_f32_16x16x32_bf16 v[64:67], a[132:135], v[28:31], v[64:67]\n"
    "s_waitcnt lgkmcnt(2)\n"
    "v_rcp_f32 v33, v33\n"
    "v_mfma_f32_16x16x32_bf16 v[68:71], a[136:139], v[28:31], v[68:71]\n"
    "ds_read_b128 v[28:31], v9 offset:19456\n"
    "v_add_f32 v35, 1.0, v35\n"
    "v_mfma_f32_16x16x32_bf16 v[40:43], a[144:147], v[16:19], v[40:43]\n"
    "buffer_store_dwordx4 v[48:51], v4, s[36:39], s73 offen sc1\n"
    "v_add_f32 v36, 1.0, v36\n"
    "v_mfma_f32_16x16x32_bf16 v[44:47], a[148:151], v[16:19], v[44:47]\n"
    "buffer_load_dwordx4 v1, s[36:39], s76 offen offset:1024 sc1 lds\n"
    "s_add_u32 s76, s76, 16384\n"
    "v_rcp_f32 v34, v34\n"
    "v_mfma_f32_16x16x32_bf16 v[64:67], a[156:159], v[16:19], v[64:67]\n"
    "s_waitcnt lgkmcnt(2)\n"
    "v_rcp_f32 v35, v35\n"
    "v_mfma_f32_16x16x32_bf16 v[68:71], a[160:163], v[16:19], v[68:71]\n"
    "ds_read_b128 v[16:19], v9 offset:21504\n"
    "v_add_f32 v37, 1.0, v37\n"
    "v_mfma_f32_16x16x32_bf16 v[40:43], a[168:171], v[20:23], v[40:43]\n"
    "v_rcp_f32 v36, v36\n"
    "v_mfma_f32_16x16x32_bf16 v[44:47], a[172:175], v[20:23], v[44:47]\n"
    "v_rcp_f32 v37, v37\n"
    "v_mfma_f32_16x16x32_bf16 v[64:67], a[180:183], v[20:23], v[64:67]\n"
    "s_waitcnt lgkmcnt(2)\n"
    "s_add_u32 m0, s77, s59\n"
    "s_add_u32 s77, s77, 8192\n"
    "buffer_load_dwordx4 v1, s[36:39], s76 offen sc1 lds\n"
    "v_add_f32 v38, 1.0, v38\n"
    "v_add_f32 v39, 1.0, v39\n"
    "v_mfma_f32_16x16x32_bf16 v[68:71], a[184:187], v[20:23], v[68:71]\n"
    "ds_read_b128 v[20:23], v9 offset:23552\n"
    "v_mul_f32 v32, v32, v56\n"
    "v_mfma_f32_16x16x32_bf16 v[40:43], a[192:195], v[24:27], v[40:43]\n"
    "v_rcp_f32 v38, v38\n"
    "v_mfma_f32_16x16x32_bf16 v[44:47], a[196:199], v[24:27], v[44:47]\n"
    "v_rcp_f32 v39, v39\n"
    "v_mfma_f32_16x16x32_bf16 v[64:67], a[204:207], v[24:27], v[64:67]\n"
    "s_waitcnt lgkmcnt(2)\n"
    "buffer_load_dwordx4 v1, s[36:39], s76 offen offset:1024 sc1 lds\n"
    "s_add_u32 s76, s76, 16384\n"
    "v_mul_f32 v33, v33, v57\n"
    "v_mul_f32 v34, v34, v58\n"
    "v_mfma_f32_16x16x32_bf16 v[68:71], a[208:211], v[24:27], v[68:71]\n"
    "ds_read_b128 v[24:27], v9 offset:25600\n"
    "v_mul_f32 v35, v35, v59\n"
    "v_mfma_f32_16x16x32_bf16 v[40:43], a[216:219], v[28:31], v[40:43]\n"
    "v_mul_f32 v36, v36, v60\n"
    "v_mul_f32 v37, v37, v61\n"
    "v_mfma_f32_16x16x32_bf16 v[44:47], a[220:223], v[28:31], v[44:47]\n"
    "v_mul_f32 v38, v38, v62\n"
    "v_mul_f32 v39, v39, v63\n"
    "v_mfma_f32_16x16x32_bf16 v[64:67], a[228:231], v[28:31], v[64:67]\n"
    "s_waitcnt lgkmcnt(2)\n"
    "s_mov_b32 m0, s77\n"
    "s_nop 0\n"
    "buffer_load_dwordx4 v8, s[36:39], s76 offen sc1 lds\n"
    "v_cvt_pk_bf16_f32 v32, v32, v33\n"
    "v_cvt_pk_bf16_f32 v33, v34, v35\n"
    "v_mfma_f32_16x16x32_bf16 v[68:71], a[232:235], v[28:31], v[68:71]\n"
    "ds_read_b128 v[28:31], v9 offset:27648\n"
    "v_cvt_pk_bf16_f32 v34, v36, v37\n"
    "v_mfma_f32_16x16x32_bf16 v[40:43], a[240:243], v[16:19], v[40:43]\n"
    "v_cvt_pk_bf16_f32 v35, v38, v39\n"
    "v_mfma_f32_16x16x32_bf16 v[44:47], a[244:247], v[16:19], v[44:47]\n"
    "buffer_store_dwordx4 v[32:35], v15, s[36:39], s73 offen sc1\n"
    "v_mfma_f32_16x16x32_bf16 v[64:67], a[252:255], v[16:19], v[64:67]\n"
    "s_waitcnt lgkmcnt(2)\n"
    "v_mfma_f32_16x16x32_bf16 v[68:71], v[80:83], v[16:19], v[68:71]\n"
    "ds_read_b128 v[16:19], v9 offset:29696\n"
    "v_mfma_f32_16x16x32_bf16 v[40:43], v[88:91], v[20:23], v[40:43]\n"
    "v_mfma_f32_16x16x32_bf16 v[44:47], v[92:95], v[20:23], v[44:47]\n"
    "v_mfma_f32_16x16x32_bf16 v[64:67], v[100:103], v[20:23], v[64:67]\n"
    "s_waitcnt lgkmcnt(2)\n"
    "v_mfma_f32_16x16x32_bf16 v[68:71], v[104:107], v[20:23], v[68:71]\n"
    "ds_read_b128 v[20:23], v9 offset:31744\n"
    "v_mfma_f32_16x16x32_bf16 v[40:43], v[112:115], v[24:27], v[40:43]\n"
    "v_mfma_f32_16x16x32_bf16 v[44:47], v[116:119], v[24:27], v[44:47]\n"
    "v_mfma_f32_16x16x32_bf16 v[64:67], v[124:127], v[24:27], v[64:67]\n"
    "s_waitcnt lgkmcnt(2)\n"
    "v_mfma_f32_16x16x32_bf16 v[68:71], v[128:131], v[24:27], v[68:71]\n"
    "ds_read_b128 v[24:27], v9 offset:33792\n"
    "v_mfma_f32_16x16x32_bf16 v[40:43], v[136:139], v[28:31], v[40:43]\n"
    "v_mfma_f32_16x16x32_bf16 v[44:47], v[140:143], v[28:31], v[44:47]\n"
    "v_mfma_f32_16x16x32_bf16 v[64:67], v[148:151], v[28:31], v[64:67]\n"
    "s_waitcnt lgkmcnt(2)\n"
    "v_mfma_f32_16x16x32_bf16 v[68:71], v[152:155], v[28:31], v[68:71]\n"
    "ds_read_b128 v[28:31], v9 offset:35840\n"
    "v_mfma_f32_16x16x32_bf16 v[40:43], v[160:163], v[16:19], v[40:43]\n"
    "v_mfma_f32_16x16x32_bf16 v[44:47], v[164:167], v[16:19], v[44:47]\n"
    "v_mfma_f32_16x16x32_bf16 v[64:67], v[172:175], v[16:19], v[64:67]\n"
    "s_waitcnt lgkmcnt(2)\n"
    "v_mfma_f32_16x16x32_bf16 v[68:71], v[176:179], v[16:19], v[68:71]\n"
    "v_mfma_f32_16x16x32_bf16 v[40:43], v[184:187], v[20:23], v[40:43]\n"
    "v_mfma_f32_16x16x32_bf16 v[44:47], v[188:191], v[20:23], v[44:47]\n"
    "v_mfma_f32_16x16x32_bf16 v[64:67], v[196:199], v[20:23], v[64:67]\n"
    "s_waitcnt lgkmcnt(1)\n"
    "v_mfma_f32_16x16x32_bf16 v[68:71], v[200:203], v[20:23], v[68:71]\n"
    "v_mfma_f32_16x16x32_bf16 v[40:43], v[208:211], v[24:27], v[40:43]\n"
    "v_mfma_f32_16x16x32_bf16 v[44:47], v[212:215], v[24:27], v[44:47]\n"
    "v_mfma_f32_16x16x32_bf16 v[64:67], v[220:223], v[24:27], v[64:67]\n"
    "s_waitcnt lgkmcnt(0)\n"
    "v_mfma_f32_16x16x32_bf16 v[68:71], v[224:227], v[24:27], v[68:71]\n"
    "v_mfma_f32_16x16x32_bf16 v[40:43], v[232:235], v[28:31], v[40:43]\n"
    "v_mfma_f32_16x16x32_bf16 v[44:47], v[236:239], v[28:31], v[44:47]\n"
    "v_mfma_f32_16x16x32_bf16 v[64:67], v[244:247], v[28:31], v[64:67]\n"
    "v_mfma_f32_16x16x32_bf16 v[68:71], v[248:251], v[28:31], v[68:71]\n"
    "s_nop 3\n"
    "v_pk_mul_f32 v[64:65], v[40:41], v[64:65]\n"
    "v_pk_mul_f32 v[66:67], v[42:43], v[66:67]\n"
    "s_nop 1\n"
    "v_pk_mul_f32 v[68:69], v[44:45], v[68:69]\n"
    "v_pk_mul_f32 v[70:71], v[46:47], v[70:71]\n"
    "v_pk_mul_f32 v[40:41], v[40:41], s[64:65] op_sel_hi:[1,0]\n"
    "v_pk_mul_f32 v[42:43], v[42:43], s[64:65] op_sel_hi:[1,0]\n"
    "v_pk_mul_f32 v[44:45], v[44:45], s[64:65] op_sel_hi:[1,0]\n"
    "v_pk_mul_f32 v[46:47], v[46:47], s[64:65] op_sel_hi:[1,0]\n"
    "v_exp_f32 v40, v40\n"
    "v_exp_f32 v41, v41\n"
    "v_exp_f32 v42, v42\n"
    "v_exp_f32 v43, v43\n"
    "v_exp_f32 v44, v44\n"
    "v_exp_f32 v45, v45\n"
    "v_exp_f32 v46, v46\n"
    "v_exp_f32 v47, v47\n"
    "v_pk_add_f32 v[40:41], v[40:41], 1.0 op_sel_hi:[1,0]\n"
    "v_pk_add_f32 v[42:43], v[42:43], 1.0 op_sel_hi:[1,0]\n"
    "v_pk_add_f32 v[44:45], v[44:45], 1.0 op_sel_hi:[1,0]\n"
    "v_pk_add_f32 v[46:47], v[46:47], 1.0 op_sel_hi:[1,0]\n"
    "v_rcp_f32 v40, v40\n"
    "v_rcp_f32 v41, v41\n"
    "v_rcp_f32 v42, v42\n"
    "v_rcp_f32 v43, v43\n"
    "v_rcp_f32 v44, v44\n"
    "v_rcp_f32 v45, v45\n"
    "v_rcp_f32 v46, v46\n"
    "v_rcp_f32 v47, v47\n"
    "v_pk_mul_f32 v[40:41], v[40:41], v[64:65]\n"
    "v_pk_mul_f32 v[42:43], v[42:43], v[66:67]\n"
    "v_pk_mul_f32 v[44:45], v[44:45], v[68:69]\n"
    "v_pk_mul_f32 v[46:47], v[46:47], v[70:71]\n"
    "v_cvt_pk_bf16_f32 v40, v40, v41\n"
    "v_cvt_pk_bf16_f32 v41, v42, v43\n"
    "v_cvt_pk_bf16_f32 v42, v44, v45\n"
    "v_cvt_pk_bf16_f32 v43, v46, v47\n"
    "buffer_store_dwordx4 v[40:43], v15, s[36:39], s73 offen offset:1024 sc1\n"
    "v_mov_b32 v12, s68\n"
    "s_mov_b32 s68, s71\n"
    "s_waitcnt vmcnt(2)\n"
    "s_branch .LgUP_bar_%=\n"
    ".LgUP_dma168_%=:\n"
    "v_mfma_f32_16x16x32_bf16 v[40:43], a[144:147], v[16:19], v[40:43]\n"
    "s_add_u32 m0, s77, s59\n"
    "s_add_u32 s77, s77, 8192\n"
    "buffer_load_dwordx4 v1, s[36:39], s76 offen sc1 lds\n"
    "buffer_store_dwordx4 v[48:51], v4, s[36:39], s73 offen sc1\n"
    "v_add_f32 v36, 1.0, v36\n"
    "v_mfma_f32_16x16x32_bf16 v[44:47], a[148:151], v[16:19], v[44:47]\n"
    "v_rcp_f32 v34, v34\n"
    "v_mfma_f32_16x16x32_bf16 v[64:67], a[156:159], v[16:19], v[64:67]\n"
    "s_waitcnt lgkmcnt(2)\n"
    "v_rcp_f32 v35, v35\n"
    "v_mfma_f32_16x16x32_bf16 v[68:71], a[160:163], v[16:19], v[68:71]\n"
    "ds_read_b128 v[16:19], v9 offset:21504\n"
    "v_add_f32 v37, 1.0, v37\n"
    "v_mfma_f32_16x16x32_bf16 v[40:43], a[168:171], v[20:23], v[40:43]\n"
    "buffer_load_dwordx4 v1, s[36:39], s76 offen offset:1024 sc1 lds\n"
    "s_add_u32 s76, s76, 16384\n"
    "v_rcp_f32 v36, v36\n"
    "v_mfma_f32_16x16x32_bf16 v[44:47], a[172:175], v[20:23], v[44:47]\n"
    "v_rcp_f32 v37, v37\n"
    "v_mfma_f32_16x16x32_bf16 v[64:67], a[180:183], v[20:23], v[64:67]\n"
    "s_waitcnt lgkmcnt(2)\n"
    "v_add_f32 v38, 1.0, v38\n"
    "v_add_f32 v39, 1.0, v39\n"
    "v_mfma_f32_16x16x32_bf16 v[68:71], a[184:187], v[20:23], v[68:71]\n"
    "ds_read_b128 v[20:23], v9 offset:23552\n"
    "v_mul_f32 v32, v32, v56\n"
    "v_mfma_f32_16x16x32_bf16 v[40:43], a[192:195], v[24:27], v[40:43]\n"
    "s_add_u32 m0, s77, s59\n"
    "s_add_u32 s77, s77, 8192\n"
    "buffer_load_dwordx4 v1, s[36:39], s76 offen sc1 lds\n"
    "v_rcp_f32 v38, v38\n"
    "v_mfma_f32_16x16x32_bf16 v[44:47], a[196:199], v[24:27], v[44:47]\n"
    "v_rcp_f32 v39, v39\n"
    "v_mfma_f32_16x16x32_bf16 v[64:67], a[204:207], v[24:27], v[64:67]\n"
    "s_waitcnt lgkmcnt(2)\n"
    "v_mul_f32 v33, v33, v57\n"
    "v_mul_f32 v34, v34, v58\n"
    "v_mfma_f32_16x16x32_bf16 v[68:71], a[208:211], v[24:27], v[68:71]\n"
    "ds_read_b128 v[24:27], v9 offset:25600\n"
    "v_mul_f32 v35, v35, v59\n"
    "v_mfma_f32_16x16x32_bf16 v[40:43], a[216:219], v[28:31], v[40:43]\n"
    "v_mul_f32 v36, v36, v60\n"
    "v_mul_f32 v37, v37, v61\n"
    "v_mfma_f32_16x16x32_bf16 v[44:47], a[220:223], v[28:31], v[44:47]\n"
    "buffer_load_dwordx4 v1, s[36:39], s76 offen offset:1024 sc1 lds\n"
    "s_add_u32 s76, s76, 16384\n"
    "v_mul_f32 v38, v38, v62\n"
    "v_mul_f32 v39, v39, v63\n"
    "v_mfma_f32_16x16x32_bf16 v[64:67], a[228:231], v[28:31], v[64:67]\n"
    "s_waitcnt lgkmcnt(2)\n"
    "v_cvt_pk_bf16_f32 v32, v32, v33\n"
    "v_cvt_pk_bf16_f32 v33, v34, v35\n"
    "v_mfma_f32_16x16x32_bf16 v[68:71], a[232:235], v[28:31], v[68:71]\n"
    "ds_read_b128 v[28:31], v9 offset:27648\n"
    "v_cvt_pk_bf16_f32 v34, v36, v37\n"
    "v_mfma_f32_16x16x32_bf16 v[40:43], a[240:243], v[16:19], v[40:43]\n"
    "v_cvt_pk_bf16_f32 v35, v38, v39\n"
    "v_mfma_f32_16x16x32_bf16 v[44:47], a[244:247], v[16:19], v[44:47]\n"
    "s_add_u32 m0, s77, s59\n"
    "s_add_u32 s77, s77, 8192\n"
    "buffer_load_dwordx4 v1, s[36:39], s76 offen sc1 lds\n"
    "buffer_store_dwordx4 v[32:35], v15, s[36:39], s73 offen sc1\n"
    "v_mfma_f32_16x16x32_bf16 v[64:67], a[252:255], v[16:19], v[64:67]\n"
    "s_waitcnt lgkmcnt(2)\n"
    "v_mfma_f32_16x16x32_bf16 v[68:71], v[80:83], v[16:19], v[68:71]\n"
    "ds_read_b128 v[16:19], v9 offset:29696\n"
    "v_mfma_f32_16x16x32_bf16 v[40:43], v[88:91], v[20:23], v[40:43]\n"
    "v_mfma_f32_16x16x32_bf16 v[44:47], v[92:95], v[20:23], v[44:47]\n"
    "buffer_load_dwordx4 v1, s[36:39], s76 offen offset:1024 sc1 lds\n"
    "s_add_u32 s76, s76, 16384\n"
    "v_mfma_f32_16x16x32_bf16 v[64:67], v[100:103], v[20:23], v[64:67]\n"
    "s_waitcnt lgkmcnt(2)\n"
    "v_mfma_f32_16x16x32_bf16 v[68:71], v[104:107], v[20:23], v[68:71]\n"
    "ds_read_b128 v[20:23], v9 offset:31744\n"
    "v_mfma_f32_16x16x32_bf16 v[40:43], v[112:115], v[24:27], v[40:43]\n"
    "v_mfma_f32_16x16x32_bf16 v[44:47], v[116:119], v[24:27], v[44:47]\n"
    "v_mfma_f32_16x16x32_bf16 v[64:67], v[124:127], v[24:27], v[64:67]\n"
    "s_waitcnt lgkmcnt(2)\n"
    "s_add_u32 m0, s77, s59\n"
    "s_add_u32 s77, s77, 8192\n"
    "buffer_load_dwordx4 v1, s[36:39], s76 offen sc1 lds\n"
    "v_mfma_f32_16x16x32_bf16 v[68:71], v[128:131], v[24:27], v[68:71]\n"
    "ds_read_b128 v[24:27], v9 offset:33792\n"
    "v_mfma_f32_16x16x32_bf16 v[40:43], v[136:139], v[28:31], v[40:43]\n"
    "v_mfma_f32_16x16x32_bf16 v[44:47], v[140:143], v[28:31], v[44:47]\n"
    "v_mfma_f32_16x16x32_bf16 v[64:67], v[148:151], v[28:31], v[64:67]\n"
    "s_waitcnt lgkmcnt(2)\n"
    "buffer_load_dwordx4 v1, s[36:39], s76 offen offset:1024 sc1 lds\n"
    "s_add_u32 s76, s76, 16384\n"
    "v_mfma_f32_16x16x32_bf16 v[68:71], v[152:155], v[28:31], v[68:71]\n"
    "ds_read_b128 v[28:31], v9 offset:35840\n"
    "v_mfma_f32_16x16x32_bf16 v[40:43], v[160:163], v[16:19], v[40:43]\n"
    "v_mfma_f32_16x16x32_bf16 v[44:47], v[164:167], v[16:19], v[44:47]\n"
    "v_mfma_f32_16x16x32_bf16 v[64:67], v[172:175], v[16:19], v[64:67]\n"
    "s_waitcnt lgkmcnt(2)\n"
    "s_mov_b32 m0, s77\n"
    "s_nop 0\n"
    "buffer_load_dwordx4 v8, s[36:39], s76 offen sc1 lds\n"
    "v_mfma_f32_16x16x32_bf16 v[68:71], v[176:179], v[16:19], v[68:71]\n"
    "v_mfma_f32_16x16x32_bf16 v[40:43], v[184:187], v[20:23], v[40:43]\n"
    "v_mfma_f32_16x16x32_bf16 v[44:47], v[188:191], v[20:23], v[44:47]\n"
    "v_mfma_f32_16x16x32_bf16 v[64:67], v[196:199], v[20:23], v[64:67]\n"
    "s_waitcnt lgkmcnt(1)\n"
    "v_mfma_f32_16x16x32_bf16 v[68:71], v[200:203], v[20:23], v[68:71]\n"
    "v_mfma_f32_16x16x32_bf16 v[40:43], v[208:211], v[24:27], v[40:43]\n"
    "v_mfma_f32_16x16x32_bf16 v[44:47], v[212:215], v[24:27], v[44:47]\n"
    "v_mfma_f32_16x16x32_bf16 v[64:67], v[220:223], v[24:27], v[64:67]\n"
    "s_waitcnt lgkmcnt(0)\n"
    "v_mfma_f32_16x16x32_bf16 v[68:71], v[224:227], v[24:27], v[68:71]\n"
    "v_mfma_f32_16x16x32_bf16 v[40:43], v[232:235], v[28:31], v[40:43]\n"
    "v_mfma_f32_16x16x32_bf16 v[44:47], v[236:239], v[28:31], v[44:47]\n"
    "v_mfma_f32_16x16x32_bf16 v[64:67], v[244:247], v[28:31], v[64:67]\n"
    "v_mfma_f32_16x16x32_bf16 v[68:71], v[248:251], v[28:31], v[68:71]\n"
    "s_nop 3\n"
    "v_pk_mul_f32 v[64:65], v[40:41], v[64:65]\n"
    "v_pk_mul_f32 v[66:67], v[42:43], v[66:67]\n"
    "s_nop 1\n"
    "v_pk_mul_f32 v[68:69], v[44:45], v[68:69]\n"
    "v_pk_mul_f32 v[70:71], v[46:47], v[70:71]\n"
    "v_pk_mul_f32 v[40:41], v[40:41], s[64:65] op_sel_hi:[1,0]\n"
    "v_pk_mul_f32 v[42:43], v[42:43], s[64:65] op_sel_hi:[1,0]\n"
    "v_pk_mul_f32 v[44:45], v[44:45], s[64:65] op_sel_hi:[1,0]\n"
    "v_pk_mul_f32 v[46:47], v[46:47], s[64:65] op_sel_hi:[1,0]\n"
    "v_exp_f32 v40, v40\n"
    "v_exp_f32 v41, v41\n"
    "v_exp_f32 v42, v42\n"
    "v_exp_f32 v43, v43\n"
    "v_exp_f32 v44, v44\n"
    "v_exp_f32 v45, v45\n"
    "v_exp_f32 v46, v46\n"
    "v_exp_f32 v47, v47\n"
    "v_pk_add_f32 v[40:41], v[40:41], 1.0 op_sel_hi:[1,0]\n"
    "v_pk_add_f32 v[42:43], v[42:43], 1.0 op_sel_hi:[1,0]\n"
    "v_pk_add_f32 v[44:45], v[44:45], 1.0 op_sel_hi:[1,0]\n"
    "v_pk_add_f32 v[46:47], v[46:47], 1.0 op_sel_hi:[1,0]\n"
    "v_rcp_f32 v40, v40\n"
    "v_rcp_f32 v41, v41\n"
    "v_rcp_f32 v42, v42\n"
    "v_rcp_f32 v43, v43\n"
    "v_rcp_f32 v44, v44\n"
    "v_rcp_f32 v45, v45\n"
    "v_rcp_f32 v46, v46\n"
    "v_rcp_f32 v47, v47\n"
    "v_pk_mul_f32 v[40:41], v[40:41], v[64:65]\n"
    "v_pk_mul_f32 v[42:43], v[42:43], v[66:67]\n"
    "v_pk_mul_f32 v[44:45], v[44:45], v[68:69]\n"
    "v_pk_mul_f32 v[46:47], v[46:47], v[70:71]\n"
    "v_cvt_pk_bf16_f32 v40, v40, v41\n"
    "v_cvt_pk_bf16_f32 v41, v42, v43\n"
    "v_cvt_pk_bf16_f32 v42, v44, v45\n"
    "v_cvt_pk_bf16_f32 v43, v46, v47\n"
    "buffer_store_dwordx4 v[40:43], v15, s[36:39], s73 offen offset:1024 sc1\n"
    "v_mov_b32 v12, s68\n"
    "s_mov_b32 s68, s71\n"
    "s_waitcnt vmcnt(1)\n"
    "s_branch .LgUP_bar_%=\n"
    ".LgUP_dma188_%=:\n"
    "v_mfma_f32_16x16x32_bf16 v[40:43], v[88:91], v[20:23], v[40:43]\n"
    "s_add_u32 m0, s77, s59\n"
    "s_add_u32 s77, s77, 8192\n"
    "buffer_load_dwordx4 v1, s[36:39], s76 offen sc1 lds\n"
    "v_mfma_f32_16x16x32_bf16 v[44:47], v[92:95], v[20:23], v[44:47]\n"
    "v_mfma_f32_16x16x32_bf16 v[64:67], v[100:103], v[20:23], v[64:67]\n"
    "s_waitcnt lgkmcnt(2)\n"
    "v_mfma_f32_16x16x32_bf16 v[68:71], v[104:107], v[20:23], v[68:71]\n"
    "ds_read_b128 v[20:23], v9 offset:31744\n"
    "v_mfma_f32_16x16x32_bf16 v[40:43], v[112:115], v[24:27], v[40:43]\n"
    "buffer_load_dwordx4 v1, s[36:39], s76 offen offset:1024 sc1 lds\n"
    "s_add_u32 s76, s76, 16384\n"
    "v_mfma_f32_16x16x32_bf16 v[44:47], v[116:119], v[24:27], v[44:47]\n"
    "v_mfma_f32_16x16x32_bf16 v[64:67], v[124:127], v[24:27], v[64:67]\n"
    "s_waitcnt lgkmcnt(2)\n"
    "v_mfma_f32_16x16x32_bf16 v[68:71], v[128:131], v[24:27], v[68:71]\n"
    "ds_read_b128 v[24:27], v9 offset:33792\n"
    "v_mfma_f32_16x16x32_bf16 v[40:43], v[136:139], v[28:31], v[40:43]\n"
    "v_mfma_f32_16x16x32_bf16 v[44:47], v[140:143], v[28:31], v[44:47]\n"
    "s_add_u32 m0, s77, s59\n"
    "s_add_u32 s77, s77, 8192\n"
    "buffer_load_dwordx4 v1, s[36:39], s76 offen sc1 lds\n"
    "v_mfma_f32_16x16x32_bf16 v[64:67], v[148:151], v[28:31], v[64:67]\n"
    "s_waitcnt lgkmcnt(2)\n"
    "v_mfma_f32_16x16x32_bf16 v[68:71], v[152:155], v[28:31], v[68:71]\n"
    "ds_read_b128 v[28:31], v9 offset:35840\n"
    "v_mfma_f32_16x16x32_bf16 v[40:43], v[160:163], v[16:19], v[40:43]\n"
    "v_mfma_f32_16x16x32_bf16 v[44:47], v[164:167], v[16:19], v[44:47]\n"
    "buffer_load_dwordx4 v1, s[36:39], s76 offen offset:1024 sc1 lds\n"
    "s_add_u32 s76, s76, 16384\n"
    "v_mfma_f32_16x16x32_bf16 v[64:67], v[172:175], v[16:19], v[64:67]\n"
    "s_waitcnt lgkmcnt(2)\n"
    "v_mfma_f32_16x16x32_bf16 v[68:71], v[176:179], v[16:19], v[68:71]\n"
    "v_mfma_f32_16x16x32_bf16 v[40:43], v[184:187], v[20:23], v[40:43]\n"
    "v_mfma_f32_16x16x32_bf16 v[44:47], v[188:191], v[20:23], v[44:47]\n"
    "v_mfma_f32_16x16x32_bf16 v[64:67], v[196:199], v[20:23], v[64:67]\n"
    "s_waitcnt lgkmcnt(1)\n"
    "s_add_u32 m0, s77, s59\n"
    "s_add_u32 s77, s77, 8192\n"
    "buffer_load_dwordx4 v1, s[36:39], s76 offen sc1 lds\n"
    "v_mfma_f32_16x16x32_bf16 v[68:71], v[200:203], v[20:23], v[68:71]\n"
    "v_mfma_f32_16x16x32_bf16 v[40:43], v[208:211], v[24:27], v[40:43]\n"
    "v_mfma_f32_16x16x32_bf16 v[44:47], v[212:215], v[24:27], v[44:47]\n"
    "v_mfma_f32_16x16x32_bf16 v[64:67], v[220:223], v[24:27], v[64:67]\n"
    "s_waitcnt lgkmcnt(0)\n"
    "buffer_load_dwordx4 v1, s[36:39], s76 offen offset:1024 sc1 lds\n"
    "s_add_u32 s76, s76, 16384\n"
    "v_mfma_f32_16x16x32_bf16 v[68:71], v[224:227], v[24:27], v[68:71]\n"
    "v_mfma_f32_16x16x32_bf16 v[40:43], v[232:235], v[28:31], v[40:43]\n"
    "v_mfma_f32_16x16x32_bf16 v[44:47], v[236:239], v[28:31], v[44:47]\n"
    "v_mfma_f32_16x16x32_bf16 v[64:67], v[244:247], v[28:31], v[64:67]\n"
    "v_mfma_f32_16x16x32_bf16 v[68:71], v[248:251], v[28:31], v[68:71]\n"
    "s_nop 3\n"
    "v_pk_mul_f32 v[64:65], v[40:41], v[64:65]\n"
    "v_pk_mul_f32 v[66:67], v[42:43], v[66:67]\n"
    "s_nop 1\n"
    "v_pk_mul_f32 v[68:69], v[44:45], v[68:69]\n"
    "v_pk_mul_f32 v[70:71], v[46:47], v[70:71]\n"
    "v_pk_mul_f32 v[40:41], v[40:41], s[64:65] op_sel_hi:[1,0]\n"
    "v_pk_mul_f32 v[42:43], v[42:43], s[64:65] op_sel_hi:[1,0]\n"
    "v_pk_mul_f32 v[44:45], v[44:45], s[64:65] op_sel_hi:[1,0]\n"
    "v_pk_mul_f32 v[46:47], v[46:47], s[64:65] op_sel_hi:[1,0]\n"
    "v_exp_f32 v40, v40\n"
    "v_exp_f32 v41, v41\n"
    "v_exp_f32 v42, v42\n"
    "v_exp_f32 v43, v43\n"
    "v_exp_f32 v44, v44\n"
    "v_exp_f32 v45, v45\n"
    "v_exp_f32 v46, v46\n"
    "v_exp_f32 v47, v47\n"
    "v_pk_add_f32 v[40:41], v[40:41], 1.0 op_sel_hi:[1,0]\n"
    "v_pk_add_f32 v[42:43], v[42:43], 1.0 op_sel_hi:[1,0]\n"
    "v_pk_add_f32 v[44:45], v[44:45], 1.0 op_sel_hi:[1,0]\n"
    "v_pk_add_f32 v[46:47], v[46:47], 1.0 op_sel_hi:[1,0]\n"
    "v_rcp_f32 v40, v40\n"
    "v_rcp_f32 v41, v41\n"
    "v_rcp_f32 v42, v42\n"
    "v_rcp_f32 v43, v43\n"
    "v_rcp_f32 v44, v44\n"
    "v_rcp_f32 v45, v45\n"
    "v_rcp_f32 v46, v46\n"
    "v_rcp_f32 v47, v47\n"
    "v_pk_mul_f32 v[40:41], v[40:41], v[64:65]\n"
    "v_pk_mul_f32 v[42:43], v[42:43], v[66:67]\n"
    "v_pk_mul_f32 v[44:45], v[44:45], v[68:69]\n"
    "v_pk_mul_f32 v[46:47], v[46:47], v[70:71]\n"
    "v_cvt_pk_bf16_f32 v40, v40, v41\n"
    "v_cvt_pk_bf16_f32 v41, v42, v43\n"
    "v_cvt_pk_bf16_f32 v42, v44, v45\n"
    "v_cvt_pk_bf16_f32 v43, v46, v47\n"
    "buffer_store_dwordx4 v[40:43], v15, s[36:39], s73 offen offset:1024 sc1\n"
    "v_mov_b32 v12, s68\n"
    "s_mov_b32 s68, s71\n"
    "s_add_u32 m0, s77, s59\n"
    "s_add_u32 s77, s77, 8192\n"
    "buffer_load_dwordx4 v1, s[36:39], s76 offen sc1 lds\n"
    "buffer_load_dwordx4 v1, s[36:39], s76 offen offset:1024 sc1 lds\n"
    "s_add_u32 s76, s76, 16384\n"
    "s_mov_b32 m0, s77\n"
    "s_nop 0\n"
    "buffer_load_dwordx4 v8, s[36:39], s76 offen sc1 lds\n"
    "s_waitcnt vmcnt(3)\n"
    "buffer_atomic_smax v12, v14, s[36:39], 0 offen sc1\n"
    "s_waitcnt vmcnt(1)\n"
    "s_branch .LgUP_bar_%=\n"
    ".LgUP_dma208_%=:\n"
    "v_mfma_f32_16x16x32_bf16 v[40:43], v[208:211], v[24:27], v[40:43]\n"
    "s_add_u32 m0, s77, s59\n"
    "s_add_u32 s77, s77, 8192\n"
    "buffer_load_dwordx4 v1, s[36:39], s76 offen sc1 lds\n"
    "v_mfma_f32_16x16x32_bf16 v[44:47], v[212:215], v[24:27], v[44:47]\n"
    "v_mfma_f32_16x16x32_bf16 v[64:67], v[220:223], v[24:27], v[64:67]\n"
    "s_waitcnt lgkmcnt(0)\n"
    "v_mfma_f32_16x16x32_bf16 v[68:71], v[224:227], v[24:27], v[68:71]\n"
    "v_mfma_f32_16x16x32_bf16 v[40:43], v[232:235], v[28:31], v[40:43]\n"
    "v_mfma_f32_16x16x32_bf16 v[44:47], v[236:239], v[28:31], v[44:47]\n"
    "buffer_load_dwordx4 v1, s[36:39], s76 offen offset:1024 sc1 lds\n"
    "s_add_u32 s76, s76, 16384\n"
    "v_mfma_f32_16x16x32_bf16 v[64:67], v[244:247], v[28:31], v[64:67]\n"
    "v_mfma_f32_16x16x32_bf16 v[68:71], v[248:251], v[28:31], v[68:71]\n"
    "s_nop 3\n"
    "v_pk_mul_f32 v[64:65], v[40:41], v[64:65]\n"
    "v_pk_mul_f32 v[66:67], v[42:43], v[66:67]\n"
    "s_nop 1\n"
    "v_pk_mul_f32 v[68:69], v[44:45], v[68:69]\n"
    "v_pk_mul_f32 v[70:71], v[46:47], v[70:71]\n"
    "v_pk_mul_f32 v[40:41], v[40:41], s[64:65] op_sel_hi:[1,0]\n"
    "v_pk_mul_f32 v[42:43], v[42:43], s[64:65] op_sel_hi:[1,0]\n"
    "v_pk_mul_f32 v[44:45], v[44:45], s[64:65] op_sel_hi:[1,0]\n"
    "v_pk_mul_f32 v[46:47], v[46:47], s[64:65] op_sel_hi:[1,0]\n"
    "v_exp_f32 v40, v40\n"
    "v_exp_f32 v41, v41\n"
    "v_exp_f32 v42, v42\n"
    "v_exp_f32 v43, v43\n"
    "v_exp_f32 v44, v44\n"
    "v_exp_f32 v45, v45\n"
    "v_exp_f32 v46, v46\n"
    "v_exp_f32 v47, v47\n"
    "v_pk_add_f32 v[40:41], v[40:41], 1.0 op_sel_hi:[1,0]\n"
    "v_pk_add_f32 v[42:43], v[42:43], 1.0 op_sel_hi:[1,0]\n"
    "v_pk_add_f32 v[44:45], v[44:45], 1.0 op_sel_hi:[1,0]\n"
    "v_pk_add_f32 v[46:47], v[46:47], 1.0 op_sel_hi:[1,0]\n"
    "v_rcp_f32 v40, v40\n"
    "v_rcp_f32 v41, v41\n"
    "v_rcp_f32 v42, v42\n"
    "v_rcp_f32 v43, v43\n"
    "v_rcp_f32 v44, v44\n"
    "v_rcp_f32 v45, v45\n"
    "v_rcp_f32 v46, v46\n"
    "v_rcp_f32 v47, v47\n"
    "v_pk_mul_f32 v[40:41], v[40:41], v[64:65]\n"
    "v_pk_mul_f32 v[42:43], v[42:43], v[66:67]\n"
    "v_pk_mul_f32 v[44:45], v[44:45], v[68:69]\n"
    "v_pk_mul_f32 v[46:47], v[46:47], v[70:71]\n"
    "v_cvt_pk_bf16_f32 v40, v40, v41\n"
    "v_cvt_pk_bf16_f32 v41, v42, v43\n"
    "v_cvt_pk_bf16_f32 v42, v44, v45\n"
    "v_cvt_pk_bf16_f32 v43, v46, v47\n"
    "buffer_store_dwordx4 v[40:43], v15, s[36:39], s73 offen offset:1024 sc1\n"
    "v_mov_b32 v12, s68\n"
    "s_mov_b32 s68, s71\n"
    "s_add_u32 m0, s77, s59\n"
    "s_add_u32 s77, s77, 8192\n"
    "buffer_load_dwordx4 v1, s[36:39], s76 offen sc1 lds\n"
    "buffer_load_dwordx4 v1, s[36:39], s76 offen offset:1024 sc1 lds\n"
    "s_add_u32 s76, s76, 16384\n"
    "s_add_u32 m0, s77, s59\n"
    "s_add_u32 s77, s77, 8192\n"
    "buffer_load_dwordx4 v1, s[36:39], s76 offen sc1 lds\n"
    "buffer_load_dwordx4 v1, s[36:39], s76 offen offset:1024 sc1 lds\n"
    "s_add_u32 s76, s76, 16384\n"
    "s_add_u32 m0, s77, s59\n"
    "s_add_u32 s77, s77, 8192\n"
    "buffer_load_dwordx4 v1, s[36:39], s76 offen sc1 lds\n"
    "buffer_load_dwordx4 v1, s[36:39], s76 offen offset:1024 sc1 lds\n"
    "s_add_u32 s76, s76, 16384\n"
    "s_mov_b32 m0, s77\n"
    "s_nop 0\n"
    "buffer_load_dwordx4 v8, s[36:39], s76 offen sc1 lds\n"
    "s_waitcnt vmcnt(7)\n"
    "buffer_atomic_smax v12, v14, s[36:39], 0 offen sc1\n"
    "s_waitcnt vmcnt(1)\n"
    "s_branch .LgUP_bar_%=\n"
    ".LgUP_idle_%=:\n"
    "s_or_b64 exec, vcc, 1\n"
    "buffer_atomic_smax v5, v2, s[36:39], 0 offen sc0 sc1\n"
    "s_mov_b64 exec, -1\n"
    "s_waitcnt vmcnt(1)\n"
    "buffer_atomic_smax v12, v14, s[36:39], 0 offen sc1\n"
    "s_branch .LgUP_block1_%=\n"
    ".LgUP_block_%=:\n"
    "v_add_u32 v13, v11, v3\n"
    "v_cmp_gt_i32 vcc, s71, v13\n"
    "s_nop 0\n"
    "s_or_b64 exec, vcc, 1\n"
    "buffer_atomic_smax v11, v2, s[36:39], 0 offen sc0 sc1\n"
    "s_mov_b64 exec, -1\n"
    "s_sleep 3\n"
    "s_waitcnt vmcnt(1)\n"
    ".LgUP_block0_%=:\n"
    "v_add_u32 v13, v5, v3\n"
    "v_cmp_gt_i32 vcc, s71, v13\n"
    "s_cbranch_vccz .LgUP_go_%=\n"
    "v_add_u32 v13, v5, v3\n"
    "v_cmp_gt_i32 vcc, s71, v13\n"
    "s_nop 0\n"
    "s_or_b64 exec, vcc, 1\n"
    "buffer_atomic_smax v5, v2, s[36:39], 0 offen sc0 sc1\n"
    "s_mov_b64 exec, -1\n"
    "s_sleep 3\n"
    "s_waitcnt vmcnt(1)\n"
    ".LgUP_block1_%=:\n"
    "v_add_u32 v13, v11, v3\n"
    "v_cmp_gt_i32 vcc, s71, v13\n"
    "s_cbranch_vccz .LgUP_go_%=\n"
    "s_sub_u32 s72, s72, 1\n"
    "s_cbranch_scc0 .LgUP_block_%=\n"
    "s_mov_b32 s72, 0\n"
    ".LgUP_go_%=:\n"
    "s_and_b32 s76, s71, s54\n"
    "s_mul_i32 s76, s76, s53\n"
    "s_add_u32 s76, s76, s52\n"
    "s_and_b32 s77, s71, 1\n"
    "s_mul_i32 s77, s77, 65536\n"
    "s_add_u32 s77, s77, s59\n"
    "s_add_u32 m0, s77, s59\n"
    "s_add_u32 s77, s77, 8192\n"
    "buffer_load_dwordx4 v1, s[36:39], s76 offen sc1 lds\n"
    "buffer_load_dwordx4 v1, s[36:39], s76 offen offset:1024 sc1 lds\n"
    "s_add_u32 s76, s76, 16384\n"
    "s_add_u32 m0, s77, s59\n"
    "s_add_u32 s77, s77, 8192\n"
    "buffer_load_dwordx4 v1, s[36:39], s76 offen sc1 lds\n"
    "buffer_load_dwordx4 v1, s[36:39], s76 offen offset:1024 sc1 lds\n"
    "s_add_u32 s76, s76, 16384\n"
    "s_add_u32 m0, s77, s59\n"
    "s_add_u32 s77, s77, 8192\n"
    "buffer_load_dwordx4 v1, s[36:39], s76 offen sc1 lds\n"
    "buffer_load_dwordx4 v1, s[36:39], s76 offen offset:1024 sc1 lds\n"
    "s_add_u32 s76, s76, 16384\n"
    "s_add_u32 m0, s77, s59\n"
    "s_add_u32 s77, s77, 8192\n"
    "buffer_load_dwordx4 v1, s[36:39], s76 offen sc1 lds\n"
    "buffer_load_dwordx4 v1, s[36:39], s76 offen offset:1024 sc1 lds\n"
    "s_add_u32 s76, s76, 16384\n"
    "s_mov_b32 m0, s77\n"
    "s_nop 0\n"
    "buffer_load_dwordx4 v8, s[36:39], s76 offen sc1 lds\n"
    "s_waitcnt vmcnt(0)\n"
    "s_branch .LgUP_bar_%=\n"
    ".LgUP_FIN_%=:\n"
  );
}
DEVI void role_wo(const Params& p, int L, int j, int i, u32 dbg, unsigned long long tag) {
  const int lane = threadIdx.x & 63, w = threadIdx.x >> 6, e0 = j * 1024 * 576 + i * 192;
  GEMM_DECL;
  g_w0 = make_srd(p.Wo + e0, 4096u * 576 * 2 - e0 * 2); g_w1 = g_w0; g_w2 = g_w0;
  g_ldw = 1152;
  g_in_base = pgb(L, B_O + 8 * j); g_in_stride = SLOT; g_in_mask = RO - 1;
  g_out_base = pgb(L, B_PA + 5 * j); g_out_kb = 12 * i; g_out_mask = RP - 1;
  g_flag = lane < 4 ? mbox(L, lane, SL_REDA, 12 * j + 4 * i + w) : ((lane < 12 && w == 0) ? mbox_att(L, j, lane - 4, 4 + i) : OOB);
  g_paddr = mbox(L, j, SL_WO + i, lane);
  g_pbias = lane < 8 ? 0 : (lane < 24 ? RP : 1000);
  v16u sa, sb, va;
  gemm_args(GEMM_ARGS, p, dbg, tag, sa, sb, va);
  RUN_ASM(
    "s_nop 4\n"
    "s_getpc_b64 s[84:85]\n"
    ".LgWO_PC_%=:\n"
    "s_and_b32 s85, s85, 0xffff\n"
    "s_mov_b32 s86, .LgWO_FIN_%=-.LgWO_PC_%=\n"
    "s_mov_b32 s87, 0x20000\n"
    "v_mbcnt_lo_u32_b32 v40, -1, 0\n"
    "v_mbcnt_hi_u32_b32 v40, -1, v40\n"
    "v_lshlrev_b32 v40, 7, v40\n"
    "buffer_load_dword v41, v40, s[84:87], 0 offen\n"
    "v_add_u32 v40, 8192, v40\n"
    "buffer_load_dword v41, v40, s[84:87], 0 offen\n"
    "v_add_u32 v40, 8192, v40\n"
    "buffer_load_dword v41, v40, s[84:87], 0 offen\n"
    "v_add_u32 v8, s59, v0\n"
    "s_mov_b32 s68, 0\n"
    "s_mov_b32 s71, 0\n"
    "s_mov_b32 s78, 65536\n"
    "v_add_u32 v9, s78, v0\n"
    "s_mov_b32 s64, 0xbfb8aa3b\n"
    "s_mov_b32 s72, 262144\n"
    "s_add_u32 m0, s59, 0\n"
    "s_mov_b32 s74, 0\n"
    "buffer_load_dwordx4 v5, s[40:43], s74 offen lds\n"
    "s_add_u32 m0, s59, 4096\n"
    "s_mov_b32 s74, 128\n"
    "buffer_load_dwordx4 v5, s[40:43], s74 offen lds\n"
    "s_add_u32 m0, s59, 8192\n"
    "s_mov_b32 s74, 256\n"
    "buffer_load_dwordx4 v5, s[40:43], s74 offen lds\n"
    "s_add_u32 m0, s59, 12288\n"
    "s_mov_b32 s74, 36864\n"
    "buffer_load_dwordx4 v5, s[40:43], s74 offen lds\n"
    "s_add_u32 m0, s59, 16384\n"
    "s_mov_b32 s74, 36992\n"
    "buffer_load_dwordx4 v5, s[40:43], s74 offen lds\n"
    "s_add_u32 m0, s59, 20480\n"
    "s_mov_b32 s74, 37120\n"
    "buffer_load_dwordx4 v5, s[40:43], s74 offen lds\n"
    "s_add_u32 m0, s59, 24576\n"
    "s_mov_b32 s74, 73728\n"
    "buffer_load_dwordx4 v5, s[40:43], s74 offen lds\n"
    "s_add_u32 m0, s59, 28672\n"
    "s_mov_b32 s74, 73856\n"
    "buffer_load_dwordx4 v5, s[40:43], s74 offen lds\n"
    "s_add_u32 m0, s59, 32768\n"
    "s_mov_b32 s74, 73984\n"
    "buffer_load_dwordx4 v5, s[40:43], s74 offen lds\n"
    "s_add_u32 m0, s59, 36864\n"
    "s_mov_b32 s74, 110592\n"
    "buffer_load_dwordx4 v5, s[40:43], s74 offen lds\n"
    "s_add_u32 m0, s59, 40960\n"
    "s_mov_b32 s74, 110720\n"
    "buffer_load_dwordx4 v5, s[40:43], s74 offen lds\n"
    "s_add_u32 m0, s59, 45056\n"
    "s_mov_b32 s74, 110848\n"
    "buffer_load_dwordx4 v5, s[40:43], s74 offen lds\n"
    "s_add_u32 m0, s59, 49152\n"
    "s_mov_b32 s74, 147456\n"
    "buffer_load_dwordx4 v5, s[40:43], s74 offen lds\n"
    "s_add_u32 m0, s59, 53248\n"
    "s_mov_b32 s74, 147584\n"
    "buffer_load_dwordx4 v5, s[40:43], s74 offen lds\n"
    "s_add_u32 m0, s59, 57344\n"
    "s_mov_b32 s74, 147712\n"
    "buffer_load_dwordx4 v5, s[40:43], s74 offen lds\n"
    "s_add_u32 m0, s59, 61440\n"
    "s_mov_b32 s74, 184320\n"
    "buffer_load_dwordx4 v5, s[40:43], s74 offen lds\n"
    "s_add_u32 m0, s59, 65536\n"
    "s_mov_b32 s74, 184448\n"
    "buffer_load_dwordx4 v5, s[40:43], s74 offen lds\n"
    "s_add_u32 m0, s59, 69632\n"
    "s_mov_b32 s74, 184576\n"
    "buffer_load_dwordx4 v5, s[40:43], s74 offen lds\n"
    "s_add_u32 m0, s59, 73728\n"
    "s_mov_b32 s74, 221184\n"
    "buffer_load_dwordx4 v5, s[40:43], s74 offen lds\n"
    "s_add_u32 m0, s59, 77824\n"
    "s_mov_b32 s74, 221312\n"
    "buffer_load_dwordx4 v5, s[40:43], s74 offen lds\n"
    "s_add_u32 m0, s59, 81920\n"
    "s_mov_b32 s74, 221440\n"
    "buffer_load_dwordx4 v5, s[40:43], s74 offen lds\n"
    "s_add_u32 m0, s59, 86016\n"
    "s_mov_b32 s74, 258048\n"
    "buffer_load_dwordx4 v5, s[40:43], s74 offen lds\n"
    "s_add_u32 m0, s59, 90112\n"
    "s_mov_b32 s74, 258176\n"
    "buffer_load_dwordx4 v5, s[40:43], s74 offen lds\n"
    "s_add_u32 m0, s59, 94208\n"
    "s_mov_b32 s74, 258304\n"
    "buffer_load_dwordx4 v5, s[40:43], s74 offen lds\n"
    "s_add_u32 m0, s59, 98304\n"
    "s_mov_b32 s74, 294912\n"
    "buffer_load_dwordx4 v5, s[40:43], s74 offen lds\n"
    "s_add_u32 m0, s59, 102400\n"
    "s_mov_b32 s74, 295040\n"
    "buffer_load_dwordx4 v5, s[40:43], s74 offen lds\n"
    "s_add_u32 m0, s59, 106496\n"
    "s_mov_b32 s74, 295168\n"
    "buffer_load_dwordx4 v5, s[40:43], s74 offen lds\n"
    "s_add_u32 m0, s59, 110592\n"
    "s_mov_b32 s74, 331776\n"
    "buffer_load_dwordx4 v5, s[40:43], s74 offen lds\n"
    "s_add_u32 m0, s59, 114688\n"
    "s_mov_b32 s74, 331904\n"
    "buffer_load_dwordx4 v5, s[40:43], s74 offen lds\n"
    "s_add_u32 m0, s59, 118784\n"
    "s_mov_b32 s74, 332032\n"
    "buffer_load_dwordx4 v5, s[40:43], s74 offen lds\n"
    "v_add_u32 v16, 49152, v6\n"
    "v_add_u32 v17, 98304, v6\n"
    "v_add_u32 v18, 49152, v7\n"
    "v_add_u32 v19, 98304, v7\n"
    "s_waitcnt vmcnt(24) lgkmcnt(0)\n"
    "s_barrier\n"
    "s_add_u32 m0, s59, 122880\n"
    "s_mov_b32 s74, 368640\n"
    "buffer_load_dwordx4 v5, s[40:43], s74 offen lds\n"
    "s_add_u32 m0, s59, 126976\n"
    "s_mov_b32 s74, 368768\n"
    "buffer_load_dwordx4 v5, s[40:43], s74 offen lds\n"
    "s_add_u32 m0, s59, 131072\n"
    "s_mov_b32 s74, 368896\n"
    "buffer_load_dwordx4 v5, s[40:43], s74 offen lds\n"
    "s_add_u32 m0, s59, 135168\n"
    "s_mov_b32 s74, 405504\n"
    "buffer_load_dwordx4 v5, s[40:43], s74 offen lds\n"
    "s_add_u32 m0, s59, 139264\n"
    "s_mov_b32 s74, 405632\n"
    "buffer_load_dwordx4 v5, s[40:43], s74 offen lds\n"
    "s_add_u32 m0, s59, 143360\n"
    "s_mov_b32 s74, 405760\n"
    "buffer_load_dwordx4 v5, s[40:43], s74 offen lds\n"
    "ds_read_b64_tr_b16 a[0:1], v6 offset:0\n"
    "ds_read_b64_tr_b16 a[2:3], v6 offset:512\n"
    "ds_read_b64_tr_b16 a[4:5], v6 offset:8\n"
    "ds_read_b64_tr_b16 a[6:7], v6 offset:520\n"
    "ds_read_b64_tr_b16 a[8:9], v7 offset:0\n"
    "ds_read_b64_tr_b16 a[10:11], v7 offset:512\n"
    "ds_read_b64_tr_b16 a[12:13], v6 offset:12288\n"
    "ds_read_b64_tr_b16 a[14:15], v6 offset:12800\n"
    "ds_read_b64_tr_b16 a[16:17], v6 offset:12296\n"
    "ds_read_b64_tr_b16 a[18:19], v6 offset:12808\n"
    "ds_read_b64_tr_b16 a[20:21], v7 offset:12288\n"
    "ds_read_b64_tr_b16 a[22:23], v7 offset:12800\n"
    "s_waitcnt vmcnt(24) lgkmcnt(0)\n"
    "s_barrier\n"
    "s_add_u32 m0, s59, 0\n"
    "s_mov_b32 s74, 442368\n"
    "buffer_load_dwordx4 v5, s[40:43], s74 offen lds\n"
    "s_add_u32 m0, s59, 4096\n"
    "s_mov_b32 s74, 442496\n"
    "buffer_load_dwordx4 v5, s[40:43], s74 offen lds\n"
    "s_add_u32 m0, s59, 8192\n"
    "s_mov_b32 s74, 442624\n"
    "buffer_load_dwordx4 v5, s[40:43], s74 offen lds\n"
    "s_add_u32 m0, s59, 12288\n"
    "s_mov_b32 s74, 479232\n"
    "buffer_load_dwordx4 v5, s[40:43], s74 offen lds\n"
    "s_add_u32 m0, s59, 16384\n"
    "s_mov_b32 s74, 479360\n"
    "buffer_load_dwordx4 v5, s[40:43], s74 offen lds\n"
    "s_add_u32 m0, s59, 20480\n"
    "s_mov_b32 s74, 479488\n"
    "buffer_load_dwordx4 v5, s[40:43], s74 offen lds\n"
    "ds_read_b64_tr_b16 a[24:25], v6 offset:24576\n"
    "ds_read_b64_tr_b16 a[26:27], v6 offset:25088\n"
    "ds_read_b64_tr_b16 a[28:29], v6 offset:24584\n"
    "ds_read_b64_tr_b16 a[30:31], v6 offset:25096\n"
    "ds_read_b64_tr_b16 a[32:33], v7 offset:24576\n"
    "ds_read_b64_tr_b16 a[34:35], v7 offset:25088\n"
    "ds_read_b64_tr_b16 a[36:37], v6 offset:36864\n"
    "ds_read_b64_tr_b16 a[38:39], v6 offset:37376\n"
    "ds_read_b64_tr_b16 a[40:41], v6 offset:36872\n"
    "ds_read_b64_tr_b16 a[42:43], v6 offset:37384\n"
    "ds_read_b64_tr_b16 a[44:45], v7 offset:36864\n"
    "ds_read_b64_tr_b16 a[46:47], v7 offset:37376\n"
    "s_waitcnt vmcnt(24) lgkmcnt(0)\n"
    "s_barrier\n"
    "s_add_u32 m0, s59, 24576\n"
    "s_mov_b32 s74, 516096\n"
    "buffer_load_dwordx4 v5, s[40:43], s74 offen lds\n"
    "s_add_u32 m0, s59, 28672\n"
    "s_mov_b32 s74, 516224\n"
    "buffer_load_dwordx4 v5, s[40:43], s74 offen lds\n"
    "s_add_u32 m0, s59, 32768\n"
    "s_mov_b32 s74, 516352\n"
    "buffer_load_dwordx4 v5, s[40:43], s74 offen lds\n"
    "s_add_u32 m0, s59, 36864\n"
    "s_mov_b32 s74, 552960\n"
    "buffer_load_dwordx4 v5, s[40:43], s74 offen lds\n"
    "s_add_u32 m0, s59, 40960\n"
    "s_mov_b32 s74, 553088\n"
    "buffer_load_dwordx4 v5, s[40:43], s74 offen lds\n"
    "s_add_u32 m0, s59, 45056\n"
    "s_mov_b32 s74, 553216\n"
    "buffer_load_dwordx4 v5, s[40:43], s74 offen lds\n"
    "ds_read_b64_tr_b16 a[48:49], v16 offset:0\n"
    "ds_read_b64_tr_b16 a[50:51], v16 offset:512\n"
    "ds_read_b64_tr_b16 a[52:53], v16 offset:8\n"
    "ds_read_b64_tr_b16 a[54:55], v16 offset:520\n"
    "ds_read_b64_tr_b16 a[56:57], v18 offset:0\n"
    "ds_read_b64_tr_b16 a[58:59], v18 offset:512\n"
    "ds_read_b64_tr_b16 a[60:61], v16 offset:12288\n"
    "ds_read_b64_tr_b16 a[62:63], v16 offset:12800\n"
    "ds_read_b64_tr_b16 a[64:65], v16 offset:12296\n"
    "ds_read_b64_tr_b16 a[66:67], v16 offset:12808\n"
    "ds_read_b64_tr_b16 a[68:69], v18 offset:12288\n"
    "ds_read_b64_tr_b16 a[70:71], v18 offset:12800\n"
    "s_waitcnt vmcnt(24) lgkmcnt(0)\n"
    "s_barrier\n"
    "s_add_u32 m0, s59, 49152\n"
    "s_mov_b32 s74, 589824\n"
    "buffer_load_dwordx4 v5, s[40:43], s74 offen lds\n"
    "s_add_u32 m0, s59, 53248\n"
    "s_mov_b32 s74, 589952\n"
    "buffer_load_dwordx4 v5, s[40:43], s74 offen lds\n"
    "s_add_u32 m0, s59, 57344\n"
    "s_mov_b32 s74, 590080\n"
    "buffer_load_dwordx4 v5, s[40:43], s74 offen lds\n"
    "s_add_u32 m0, s59, 61440\n"
    "s_mov_b32 s74, 626688\n"
    "buffer_load_dwordx4 v5, s[40:43], s74 offen lds\n"
    "s_add_u32 m0, s59, 65536\n"
    "s_mov_b32 s74, 626816\n"
    "buffer_load_dwordx4 v5, s[40:43], s74 offen lds\n"
    "s_add_u32 m0, s59, 69632\n"
    "s_mov_b32 s74, 626944\n"
    "buffer_load_dwordx4 v5, s[40:43], s74 offen lds\n"
    "ds_read_b64_tr_b16 a[72:73], v16 offset:24576\n"
    "ds_read_b64_tr_b16 a[74:75], v16 offset:25088\n"
    "ds_read_b64_tr_b16 a[76:77], v16 offset:24584\n"
    "ds_read_b64_tr_b16 a[78:79], v16 offset:25096\n"
    "ds_read_b64_tr_b16 a[80:81], v18 offset:24576\n"
    "ds_read_b64_tr_b16 a[82:83], v18 offset:25088\n"
    "ds_read_b64_tr_b16 a[84:85], v16 offset:36864\n"
    "ds_read_b64_tr_b16 a[86:87], v16 offset:37376\n"
    "ds_read_b64_tr_b16 a[88:89], v16 offset:36872\n"
    "ds_read_b64_tr_b16 a[90:91], v16 offset:37384\n"
    "ds_read_b64_tr_b16 a[92:93], v18 offset:36864\n"
    "ds_read_b64_tr_b16 a[94:95], v18 offset:37376\n"
    "s_waitcnt vmcnt(24) lgkmcnt(0)\n"
    "s_barrier\n"
    "s_add_u32 m0, s59, 73728\n"
    "s_mov_b32 s74, 663552\n"
    "buffer_load_dwordx4 v5, s[40:43], s74 offen lds\n"
    "s_add_u32 m0, s59, 77824\n"
    "s_mov_b32 s74, 663680\n"
    "buffer_load_dwordx4 v5, s[40:43], s74 offen lds\n"
    "s_add_u32 m0, s59, 81920\n"
    "s_mov_b32 s74, 663808\n"
    "buffer_load_dwordx4 v5, s[40:43], s74 offen lds\n"
    "s_add_u32 m0, s59, 86016\n"
    "s_mov_b32 s74, 700416\n"
    "buffer_load_dwordx4 v5, s[40:43], s74 offen lds\n"
    "s_add_u32 m0, s59, 90112\n"
    "s_mov_b32 s74, 700544\n"
    "buffer_load_dwordx4 v5, s[40:43], s74 offen lds\n"
    "s_add_u32 m0, s59, 94208\n"
    "s_mov_b32 s74, 700672\n"
    "buffer_load_dwordx4 v5, s[40:43], s74 offen lds\n"
    "ds_read_b64_tr_b16 a[96:97], v17 offset:0\n"
    "ds_read_b64_tr_b16 a[98:99], v17 offset:512\n"
    "ds_read_b64_tr_b16 a[100:101], v17 offset:8\n"
    "ds_read_b64_tr_b16 a[102:103], v17 offset:520\n"
    "ds_read_b64_tr_b16 a[104:105], v19 offset:0\n"
    "ds_read_b64_tr_b16 a[106:107], v19 offset:512\n"
    "ds_read_b64_tr_b16 a[108:109], v17 offset:12288\n"
    "ds_read_b64_tr_b16 a[110:111], v17 offset:12800\n"
    "ds_read_b64_tr_b16 a[112:113], v17 offset:12296\n"
    "ds_read_b64_tr_b16 a[114:115], v17 offset:12808\n"
    "ds_read_b64_tr_b16 a[116:117], v19 offset:12288\n"
    "ds_read_b64_tr_b16 a[118:119], v19 offset:12800\n"
    "s_waitcnt vmcnt(24) lgkmcnt(0)\n"
    "s_barrier\n"
    "s_add_u32 m0, s59, 98304\n"
    "s_mov_b32 s74, 737280\n"
    "buffer_load_dwordx4 v5, s[40:43], s74 offen lds\n"
    "s_add_u32 m0, s59, 102400\n"
    "s_mov_b32 s74, 737408\n"
    "buffer_load_dwordx4 v5, s[40:43], s74 offen lds\n"
    "s_add_u32 m0, s59, 106496\n"
    "s_mov_b32 s74, 737536\n"
    "buffer_load_dwordx4 v5, s[40:43], s74 offen lds\n"
    "s_add_u32 m0, s59, 110592\n"
    "s_mov_b32 s74, 774144\n"
    "buffer_load_dwordx4 v5, s[40:43], s74 offen lds\n"
    "s_add_u32 m0, s59, 114688\n"
    "s_mov_b32 s74, 774272\n"
    "buffer_load_dwordx4 v5, s[40:43], s74 offen lds\n"
    "s_add_u32 m0, s59, 118784\n"
    "s_mov_b32 s74, 774400\n"
    "buffer_load_dwordx4 v5, s[40:43], s74 offen lds\n"
    "ds_read_b64_tr_b16 a[120:121], v17 offset:24576\n"
    "ds_read_b64_tr_b16 a[122:123], v17 offset:25088\n"
    "ds_read_b64_tr_b16 a[124:125], v17 offset:24584\n"
    "ds_read_b64_tr_b16 a[126:127], v17 offset:25096\n"
    "ds_read_b64_tr_b16 a[128:129], v19 offset:24576\n"
    "ds_read_b64_tr_b16 a[130:131], v19 offset:25088\n"
    "ds_read_b64_tr_b16 a[132:133], v17 offset:36864\n"
    "ds_read_b64_tr_b16 a[134:135], v17 offset:37376\n"
    "ds_read_b64_tr_b16 a[136:137], v17 offset:36872\n"
    "ds_read_b64_tr_b16 a[138:139], v17 offset:37384\n"
    "ds_read_b64_tr_b16 a[140:141], v19 offset:36864\n"
    "ds_read_b64_tr_b16 a[142:143], v19 offset:37376\n"
    "s_waitcnt vmcnt(24) lgkmcnt(0)\n"
    "s_barrier\n"
    "s_add_u32 m0, s59, 122880\n"
    "s_mov_b32 s74, 811008\n"
    "buffer_load_dwordx4 v5, s[40:43], s74 offen lds\n"
    "s_add_u32 m0, s59, 126976\n"
    "s_mov_b32 s74, 811136\n"
    "buffer_load_dwordx4 v5, s[40:43], s74 offen lds\n"
    "s_add_u32 m0, s59, 131072\n"
    "s_mov_b32 s74, 811264\n"
    "buffer_load_dwordx4 v5, s[40:43], s74 offen lds\n"
    "s_add_u32 m0, s59, 135168\n"
    "s_mov_b32 s74, 847872\n"
    "buffer_load_dwordx4 v5, s[40:43], s74 offen lds\n"
    "s_add_u32 m0, s59, 139264\n"
    "s_mov_b32 s74, 848000\n"
    "buffer_load_dwordx4 v5, s[40:43], s74 offen lds\n"
    "s_add_u32 m0, s59, 143360\n"
    "s_mov_b32 s74, 848128\n"
    "buffer_load_dwordx4 v5, s[40:43], s74 offen lds\n"
    "ds_read_b64_tr_b16 a[144:145], v6 offset:0\n"
    "ds_read_b64_tr_b16 a[146:147], v6 offset:512\n"
    "ds_read_b64_tr_b16 a[148:149], v6 offset:8\n"
    "ds_read_b64_tr_b16 a[150:151], v6 offset:520\n"
    "ds_read_b64_tr_b16 a[152:153], v7 offset:0\n"
    "ds_read_b64_tr_b16 a[154:155], v7 offset:512\n"
    "ds_read_b64_tr_b16 a[156:157], v6 offset:12288\n"
    "ds_read_b64_tr_b16 a[158:159], v6 offset:12800\n"
    "ds_read_b64_tr_b16 a[160:161], v6 offset:12296\n"
    "ds_read_b64_tr_b16 a[162:163], v6 offset:12808\n"
    "ds_read_b64_tr_b16 a[164:165], v7 offset:12288\n"
    "ds_read_b64_tr_b16 a[166:167], v7 offset:12800\n"
    "s_waitcnt vmcnt(24) lgkmcnt(0)\n"
    "s_barrier\n"
    "s_add_u32 m0, s59, 0\n"
    "s_mov_b32 s74, 884736\n"
    "buffer_load_dwordx4 v5, s[40:43], s74 offen lds\n"
    "s_add_u32 m0, s59, 4096\n"
    "s_mov_b32 s74, 884864\n"
    "buffer_load_dwordx4 v5, s[40:43], s74 offen lds\n"
    "s_add_u32 m0, s59, 8192\n"
    "s_mov_b32 s74, 884992\n"
    "buffer_load_dwordx4 v5, s[40:43], s74 offen lds\n"
    "s_add_u32 m0, s59, 12288\n"
    "s_mov_b32 s74, 921600\n"
    "buffer_load_dwordx4 v5, s[40:43], s74 offen lds\n"
    "s_add_u32 m0, s59, 16384\n"
    "s_mov_b32 s74, 921728\n"
    "buffer_load_dwordx4 v5, s[40:43], s74 offen lds\n"
    "s_add_u32 m0, s59, 20480\n"
    "s_mov_b32 s74, 921856\n"
    "buffer_load_dwordx4 v5, s[40:43], s74 offen lds\n"
    "ds_read_b64_tr_b16 a[168:169], v6 offset:24576\n"
    "ds_read_b64_tr_b16 a[170:171], v6 offset:25088\n"
    "ds_read_b64_tr_b16 a[172:173], v6 offset:24584\n"
    "ds_read_b64_tr_b16 a[174:175], v6 offset:25096\n"
    "ds_read_b64_tr_b16 a[176:177], v7 offset:24576\n"
    "ds_read_b64_tr_b16 a[178:179], v7 offset:25088\n"
    "ds_read_b64_tr_b16 a[180:181], v6 offset:36864\n"
    "ds_read_b64_tr_b16 a[182:183], v6 offset:37376\n"
    "ds_read_b64_tr_b16 a[184:185], v6 offset:36872\n"
    "ds_read_b64_tr_b16 a[186:187], v6 offset:37384\n"
    "ds_read_b64_tr_b16 a[188:189], v7 offset:36864\n"
    "ds_read_b64_tr_b16 a[190:191], v7 offset:37376\n"
    "s_waitcnt vmcnt(24) lgkmcnt(0)\n"
    "s_barrier\n"
    "s_add_u32 m0, s59, 24576\n"
    "s_mov_b32 s74, 958464\n"
    "buffer_load_dwordx4 v5, s[40:43], s74 offen lds\n"
    "s_add_u32 m0, s59, 28672\n"
    "s_mov_b32 s74, 958592\n"
    "buffer_load_dwordx4 v5, s[40:43], s74 offen lds\n"
    "s_add_u32 m0, s59, 32768\n"
    "s_mov_b32 s74, 958720\n"
    "buffer_load_dwordx4 v5, s[40:43], s74 offen lds\n"
    "s_add_u32 m0, s59, 36864\n"
    "s_mov_b32 s74, 995328\n"
    "buffer_load_dwordx4 v5, s[40:43], s74 offen lds\n"
    "s_add_u32 m0, s59, 40960\n"
    "s_mov_b32 s74, 995456\n"
    "buffer_load_dwordx4 v5, s[40:43], s74 offen lds\n"
    "s_add_u32 m0, s59, 45056\n"
    "s_mov_b32 s74, 995584\n"
    "buffer_load_dwordx4 v5, s[40:43], s74 offen lds\n"
    "ds_read_b64_tr_b16 a[192:193], v16 offset:0\n"
    "ds_read_b64_tr_b16 a[194:195], v16 offset:512\n"
    "ds_read_b64_tr_b16 a[196:197], v16 offset:8\n"
    "ds_read_b64_tr_b16 a[198:199], v16 offset:520\n"
    "ds_read_b64_tr_b16 a[200:201], v18 offset:0\n"
    "ds_read_b64_tr_b16 a[202:203], v18 offset:512\n"
    "ds_read_b64_tr_b16 a[204:205], v16 offset:12288\n"
    "ds_read_b64_tr_b16 a[206:207], v16 offset:12800\n"
    "ds_read_b64_tr_b16 a[208:209], v16 offset:12296\n"
    "ds_read_b64_tr_b16 a[210:211], v16 offset:12808\n"
    "ds_read_b64_tr_b16 a[212:213], v18 offset:12288\n"
    "ds_read_b64_tr_b16 a[214:215], v18 offset:12800\n"
    "s_waitcnt vmcnt(24) lgkmcnt(0)\n"
    "s_barrier\n"
    "s_add_u32 m0, s59, 49152\n"
    "s_mov_b32 s74, 1032192\n"
    "buffer_load_dwordx4 v5, s[40:43], s74 offen lds\n"
    "s_add_u32 m0, s59, 53248\n"
    "s_mov_b32 s74, 1032320\n"
    "buffer_load_dwordx4 v5, s[40:43], s74 offen lds\n"
    "s_add_u32 m0, s59, 57344\n"
    "s_mov_b32 s74, 1032448\n"
    "buffer_load_dwordx4 v5, s[40:43], s74 offen lds\n"
    "s_add_u32 m0, s59, 61440\n"
    "s_mov_b32 s74, 1069056\n"
    "buffer_load_dwordx4 v5, s[40:43], s74 offen lds\n"
    "s_add_u32 m0, s59, 65536\n"
    "s_mov_b32 s74, 1069184\n"
    "buffer_load_dwordx4 v5, s[40:43], s74 offen lds\n"
    "s_add_u32 m0, s59, 69632\n"
    "s_mov_b32 s74, 1069312\n"
    "buffer_load_dwordx4 v5, s[40:43], s74 offen lds\n"
    "ds_read_b64_tr_b16 a[216:217], v16 offset:24576\n"
    "ds_read_b64_tr_b16 a[218:219], v16 offset:25088\n"
    "ds_read_b64_tr_b16 a[220:221], v16 offset:24584\n"
    "ds_read_b64_tr_b16 a[222:223], v16 offset:25096\n"
    "ds_read_b64_tr_b16 a[224:225], v18 offset:24576\n"
    "ds_read_b64_tr_b16 a[226:227], v18 offset:25088\n"
    "ds_read_b64_tr_b16 a[228:229], v16 offset:36864\n"
    "ds_read_b64_tr_b16 a[230:231], v16 offset:37376\n"
    "ds_read_b64_tr_b16 a[232:233], v16 offset:36872\n"
    "ds_read_b64_tr_b16 a[234:235], v16 offset:37384\n"
    "ds_read_b64_tr_b16 a[236:237], v18 offset:36864\n"
    "ds_read_b64_tr_b16 a[238:239], v18 offset:37376\n"
    "s_waitcnt vmcnt(24) lgkmcnt(0)\n"
    "s_barrier\n"
    "s_add_u32 m0, s59, 73728\n"
    "s_mov_b32 s74, 1105920\n"
    "buffer_load_dwordx4 v5, s[40:43], s74 offen lds\n"
    "s_add_u32 m0, s59, 77824\n"
    "s_mov_b32 s74, 1106048\n"
    "buffer_load_dwordx4 v5, s[40:43], s74 offen lds\n"
    "s_add_u32 m0, s59, 81920\n"
    "s_mov_b32 s74, 1106176\n"
    "buffer_load_dwordx4 v5, s[40:43], s74 offen lds\n"
    "s_add_u32 m0, s59, 86016\n"
    "s_mov_b32 s74, 1142784\n"
    "buffer_load_dwordx4 v5, s[40:43], s74 offen lds\n"
    "s_add_u32 m0, s59, 90112\n"
    "s_mov_b32 s74, 1142912\n"
    "buffer_load_dwordx4 v5, s[40:43], s74 offen lds\n"
    "s_add_u32 m0, s59, 94208\n"
    "s_mov_b32 s74, 1143040\n"
    "buffer_load_dwordx4 v5, s[40:43], s74 offen lds\n"
    "buffer_load_dwordx2 v[12:13], off, s[36:39], 0 sc1\n"
    "ds_read_b64_tr_b16 a[240:241], v17 offset:0\n"
    "ds_read_b64_tr_b16 a[242:243], v17 offset:512\n"
    "ds_read_b64_tr_b16 a[244:245], v17 offset:8\n"
    "ds_read_b64_tr_b16 a[246:247], v17 offset:520\n"
    "ds_read_b64_tr_b16 a[248:249], v19 offset:0\n"
    "ds_read_b64_tr_b16 a[250:251], v19 offset:512\n"
    "ds_read_b64_tr_b16 a[252:253], v17 offset:12288\n"
    "ds_read_b64_tr_b16 a[254:255], v17 offset:12800\n"
    "ds_read_b64_tr_b16 v[128:129], v17 offset:12296\n"
    "ds_read_b64_tr_b16 v[130:131], v17 offset:12808\n"
    "ds_read_b64_tr_b16 v[132:133], v19 offset:12288\n"
    "ds_read_b64_tr_b16 v[134:135], v19 offset:12800\n"
    "s_waitcnt vmcnt(25) lgkmcnt(0)\n"
    "s_barrier\n"
    "ds_read_b64_tr_b16 v[136:137], v17 offset:24576\n"
    "ds_read_b64_tr_b16 v[138:139], v17 offset:25088\n"
    "ds_read_b64_tr_b16 v[140:141], v17 offset:24584\n"
    "ds_read_b64_tr_b16 v[142:143], v17 offset:25096\n"
    "ds_read_b64_tr_b16 v[144:145], v19 offset:24576\n"
    "ds_read_b64_tr_b16 v[146:147], v19 offset:25088\n"
    "ds_read_b64_tr_b16 v[148:149], v17 offset:36864\n"
    "ds_read_b64_tr_b16 v[150:151], v17 offset:37376\n"
    "ds_read_b64_tr_b16 v[152:153], v17 offset:36872\n"
    "ds_read_b64_tr_b16 v[154:155], v17 offset:37384\n"
    "ds_read_b64_tr_b16 v[156:157], v19 offset:36864\n"
    "ds_read_b64_tr_b16 v[158:159], v19 offset:37376\n"
    "s_waitcnt vmcnt(19) lgkmcnt(0)\n"
    "s_barrier\n"
    "ds_read_b64_tr_b16 v[160:161], v6 offset:0\n"
    "ds_read_b64_tr_b16 v[162:163], v6 offset:512\n"
    "ds_read_b64_tr_b16 v[164:165], v6 offset:8\n"
    "ds_read_b64_tr_b16 v[166:167], v6 offset:520\n"
    "ds_read_b64_tr_b16 v[168:169], v7 offset:0\n"
    "ds_read_b64_tr_b16 v[170:171], v7 offset:512\n"
    "ds_read_b64_tr_b16 v[172:173], v6 offset:12288\n"
    "ds_read_b64_tr_b16 v[174:175], v6 offset:12800\n"
    "ds_read_b64_tr_b16 v[176:177], v6 offset:12296\n"
    "ds_read_b64_tr_b16 v[178:179], v6 offset:12808\n"
    "ds_read_b64_tr_b16 v[180:181], v7 offset:12288\n"
    "ds_read_b64_tr_b16 v[182:183], v7 offset:12800\n"
    "s_waitcnt vmcnt(13) lgkmcnt(0)\n"
    "s_barrier\n"
    "ds_read_b64_tr_b16 v[184:185], v6 offset:24576\n"
    "ds_read_b64_tr_b16 v[186:187], v6 offset:25088\n"
    "ds_read_b64_tr_b16 v[188:189], v6 offset:24584\n"
    "ds_read_b64_tr_b16 v[190:191], v6 offset:25096\n"
    "ds_read_b64_tr_b16 v[192:193], v7 offset:24576\n"
    "ds_read_b64_tr_b16 v[194:195], v7 offset:25088\n"
    "ds_read_b64_tr_b16 v[196:197], v6 offset:36864\n"
    "ds_read_b64_tr_b16 v[198:199], v6 offset:37376\n"
    "ds_read_b64_tr_b16 v[200:201], v6 offset:36872\n"
    "ds_read_b64_tr_b16 v[202:203], v6 offset:37384\n"
    "ds_read_b64_tr_b16 v[204:205], v7 offset:36864\n"
    "ds_read_b64_tr_b16 v[206:207], v7 offset:37376\n"
    "s_waitcnt vmcnt(7) lgkmcnt(0)\n"
    "s_barrier\n"
    "ds_read_b64_tr_b16 v[208:209], v16 offset:0\n"
    "ds_read_b64_tr_b16 v[210:211], v16 offset:512\n"
    "ds_read_b64_tr_b16 v[212:213], v16 offset:8\n"
    "ds_read_b64_tr_b16 v[214:215], v16 offset:520\n"
    "ds_read_b64_tr_b16 v[216:217], v18 offset:0\n"
    "ds_read_b64_tr_b16 v[218:219], v18 offset:512\n"
    "ds_read_b64_tr_b16 v[220:221], v16 offset:12288\n"
    "ds_read_b64_tr_b16 v[222:223], v16 offset:12800\n"
    "ds_read_b64_tr_b16 v[224:225], v16 offset:12296\n"
    "ds_read_b64_tr_b16 v[226:227], v16 offset:12808\n"
    "ds_read_b64_tr_b16 v[228:229], v18 offset:12288\n"
    "ds_read_b64_tr_b16 v[230:231], v18 offset:12800\n"
    "s_waitcnt vmcnt(0) lgkmcnt(0)\n"
    "s_barrier\n"
    ".LgWO_par_%=:\n"
    "v_readfirstlane_b32 s74, v12\n"
    "v_readfirstlane_b32 s75, v13\n"
    "s_xor_b32 s74, s74, s66\n"
    "s_xor_b32 s75, s75, s67\n"
    "s_cselect_b32 s75, 2, 0\n"
    "s_or_b32 s75, s75, s74\n"
    "s_cmp_lt_u32 s75, 2\n"
    "s_cbranch_scc1 .LgWO_par1_%=\n"
    "s_sleep 4\n"
    "buffer_load_dwordx2 v[12:13], off, s[36:39], 0 sc1\n"
    "s_waitcnt vmcnt(0)\n"
    "s_sub_u32 s72, s72, 1\n"
    "s_cbranch_scc0 .LgWO_par_%=\n"
    "s_mov_b32 s72, 0\n"
    ".LgWO_par1_%=:\n"
    "s_and_b32 s74, s74, 1\n"
    "s_lshl_b32 s74, s74, 13\n"
    "s_add_u32 s36, s36, s74\n"
    "s_addc_u32 s37, s37, 0\n"
    "v_add_u32 v13, v11, v3\n"
    "v_cmp_gt_i32 vcc, s71, v13\n"
    "s_nop 0\n"
    "s_or_b64 exec, vcc, 1\n"
    "buffer_atomic_smax v11, v2, s[36:39], 0 offen sc0 sc1\n"
    "s_mov_b64 exec, -1\n"
    "s_getpc_b64 s[84:85]\n"
    ".LgWO_PC2_%=:\n"
    "s_and_b32 s85, s85, 0xffff\n"
    "s_mov_b32 s86, .LgWO_FIN_%=-.LgWO_PC2_%=\n"
    "s_mov_b32 s87, 0x20000\n"
    "v_mbcnt_lo_u32_b32 v42, -1, 0\n"
    "v_mbcnt_hi_u32_b32 v42, -1, v42\n"
    "v_lshlrev_b32 v42, 7, v42\n"
    "buffer_load_dword v43, v42, s[84:87], 0 offen\n"
    "v_add_u32 v42, 8192, v42\n"
    "buffer_load_dword v43, v42, s[84:87], 0 offen\n"
    "v_add_u32 v42, 8192, v42\n"
    "buffer_load_dword v43, v42, s[84:87], 0 offen\n"
    "ds_read_b64_tr_b16 v[232:233], v16 offset:24576\n"
    "ds_read_b64_tr_b16 v[234:235], v16 offset:25088\n"
    "ds_read_b64_tr_b16 v[236:237], v16 offset:24584\n"
    "ds_read_b64_tr_b16 v[238:239], v16 offset:25096\n"
    "ds_read_b64_tr_b16 v[240:241], v18 offset:24576\n"
    "ds_read_b64_tr_b16 v[242:243], v18 offset:25088\n"
    "ds_read_b64_tr_b16 v[244:245], v16 offset:36864\n"
    "ds_read_b64_tr_b16 v[246:247], v16 offset:37376\n"
    "ds_read_b64_tr_b16 v[248:249], v16 offset:36872\n"
    "ds_read_b64_tr_b16 v[250:251], v16 offset:37384\n"
    "ds_read_b64_tr_b16 v[252:253], v18 offset:36864\n"
    "ds_read_b64_tr_b16 v[254:255], v18 offset:37376\n"
    "s_waitcnt lgkmcnt(0)\n"
    "s_barrier\n"
    "s_waitcnt vmcnt(0)\n"
    "v_mov_b32 v5, v11\n"
    "v_mov_b32 v12, -1\n"
    "s_branch .LgWO_block1_%=\n"
    ".LgWO_LOOPB_%=:\n"
    ".LgWO_head_%=:\n"
    "v_mfma_f32_16x16x32_bf16 v[64:67], a[8:11], v[16:19], 0\n"
    "s_waitcnt lgkmcnt(6)\n"
    "ds_read_b128 v[16:19], v9 offset:0\n"
    "v_mfma_f32_16x16x32_bf16 v[68:71], a[8:11], v[20:23], 0\n"
    "s_waitcnt lgkmcnt(6)\n"
    "ds_read_b128 v[20:23], v9 offset:2048\n"
    "v_mfma_f32_16x16x32_bf16 v[64:67], a[20:23], v[24:27], v[64:67]\n"
    "s_waitcnt lgkmcnt(6)\n"
    "ds_read_b128 v[24:27], v9 offset:4096\n"
    "v_mfma_f32_16x16x32_bf16 v[68:71], a[20:23], v[28:31], v[68:71]\n"
    "s_waitcnt lgkmcnt(6)\n"
    "ds_read_b128 v[28:31], v9 offset:6144\n"
    "v_mfma_f32_16x16x32_bf16 v[64:67], a[32:35], v[32:35], v[64:67]\n"
    "s_waitcnt lgkmcnt(6)\n"
    "ds_read_b128 v[32:35], v9 offset:1024\n"
    "v_add_u32 v13, v11, v3\n"
    "v_mfma_f32_16x16x32_bf16 v[68:71], a[32:35], v[36:39], v[68:71]\n"
    "s_waitcnt lgkmcnt(6)\n"
    "ds_read_b128 v[36:39], v9 offset:3072\n"
    "v_cmp_gt_i32 vcc, s71, v13\n"
    "v_mfma_f32_16x16x32_bf16 v[64:67], a[44:47], v[40:43], v[64:67]\n"
    "s_waitcnt lgkmcnt(6)\n"
    "ds_read_b128 v[40:43], v9 offset:5120\n"
    "v_mfma_f32_16x16x32_bf16 v[68:71], a[44:47], v[44:47], v[68:71]\n"
    "s_waitcnt lgkmcnt(6)\n"
    "ds_read_b128 v[44:47], v9 offset:7168\n"
    "v_mfma_f32_16x16x32_bf16 v[48:51], a[0:3], v[16:19], 0\n"
    "s_waitcnt lgkmcnt(6)\n"
    "s_or_b64 exec, vcc, 1\n"
    "buffer_atomic_smax v11, v2, s[36:39], 0 offen sc0 sc1\n"
    "s_mov_b64 exec, -1\n"
    "v_cvt_pk_bf16_f32 v56, v56, v57\n"
    "v_cvt_pk_bf16_f32 v57, v58, v59\n"
    "v_mfma_f32_16x16x32_bf16 v[52:55], a[4:7], v[16:19], 0\n"
    "ds_read_b128 v[16:19], v9 offset:8192\n"
    "v_cvt_pk_bf16_f32 v58, v60, v61\n"
    "v_mfma_f32_16x16x32_bf16 v[48:51], a[12:15], v[20:23], v[48:51]\n"
    "s_waitcnt lgkmcnt(6)\n"
    "v_cvt_pk_bf16_f32 v59, v62, v63\n"
    "v_mfma_f32_16x16x32_bf16 v[52:55], a[16:19], v[20:23], v[52:55]\n"
    "ds_read_b128 v[20:23], v9 offset:9216\n"
    "v_mfma_f32_16x16x32_bf16 v[48:51], a[24:27], v[24:27], v[48:51]\n"
    "s_waitcnt lgkmcnt(6)\n"
    "buffer_store_dwordx4 v[56:59], v15, s[36:39], s73 offen offset:1024 sc1\n"
    "v_mfma_f32_16x16x32_bf16 v[52:55], a[28:31], v[24:27], v[52:55]\n"
    "ds_read_b128 v[24:27], v9 offset:10240\n"
    "v_mfma_f32_16x16x32_bf16 v[48:51], a[36:39], v[28:31], v[48:51]\n"
    "s_waitcnt lgkmcnt(6)\n"
    "s_and_b32 s76, s71, s54\n"
    "v_mfma_f32_16x16x32_bf16 v[52:55], a[40:43], v[28:31], v[52:55]\n"
    "ds_read_b128 v[28:31], v9 offset:11264\n"
    "v_mfma_f32_16x16x32_bf16 v[56:59], a[0:3], v[32:35], 0\n"
    "s_waitcnt lgkmcnt(6)\n"
    "s_mul_i32 s76, s76, s53\n"
    "v_mfma_f32_16x16x32_bf16 v[60:63], a[4:7], v[32:35], 0\n"
    "ds_read_b128 v[32:35], v9 offset:12288\n"
    "v_mfma_f32_16x16x32_bf16 v[56:59], a[12:15], v[36:39], v[56:59]\n"
    "s_waitcnt lgkmcnt(6)\n"
    "s_add_u32 s76, s76, s52\n"
    "v_mfma_f32_16x16x32_bf16 v[60:63], a[16:19], v[36:39], v[60:63]\n"
    "ds_read_b128 v[36:39], v9 offset:13312\n"
    "v_mfma_f32_16x16x32_bf16 v[56:59], a[24:27], v[40:43], v[56:59]\n"
    "s_waitcnt lgkmcnt(6)\n"
    "s_and_b32 s77, s71, 1\n"
    "v_mfma_f32_16x16x32_bf16 v[60:63], a[28:31], v[40:43], v[60:63]\n"
    "ds_read_b128 v[40:43], v9 offset:14336\n"
    "v_mfma_f32_16x16x32_bf16 v[56:59], a[36:39], v[44:47], v[56:59]\n"
    "s_waitcnt lgkmcnt(6)\n"
    "s_mul_i32 s77, s77, 65536\n"
    "v_mfma_f32_16x16x32_bf16 v[60:63], a[40:43], v[44:47], v[60:63]\n"
    "ds_read_b128 v[44:47], v9 offset:15360\n"
    "v_mfma_f32_16x16x32_bf16 v[48:51], a[48:51], v[16:19], v[48:51]\n"
    "s_add_u32 s77, s77, s59\n"
    "v_mfma_f32_16x16x32_bf16 v[52:55], a[52:55], v[16:19], v[52:55]\n"
    "s_waitcnt lgkmcnt(6)\n"
    "s_and_b32 s73, s68, s57\n"
    "v_mfma_f32_16x16x32_bf16 v[64:67], a[56:59], v[16:19], v[64:67]\n"
    "ds_read_b128 v[16:19], v9 offset:16384\n"
    "v_mfma_f32_16x16x32_bf16 v[56:59], a[48:51], v[20:23], v[56:59]\n"
    "s_mul_i32 s73, s73, s56\n"
    "v_mfma_f32_16x16x32_bf16 v[60:63], a[52:55], v[20:23], v[60:63]\n"
    "s_waitcnt lgkmcnt(6)\n"
    "s_add_u32 s73, s73, s55\n"
    "v_mfma_f32_16x16x32_bf16 v[68:71], a[56:59], v[20:23], v[68:71]\n"
    "ds_read_b128 v[20:23], v9 offset:17408\n"
    "v_add_u32 v13, v5, v3\n"
    "v_mfma_f32_16x16x32_bf16 v[48:51], a[60:63], v[24:27], v[48:51]\n"
    "v_cmp_gt_i32 vcc, s71, v13\n"
    "v_mfma_f32_16x16x32_bf16 v[52:55], a[64:67], v[24:27], v[52:55]\n"
    "s_waitcnt lgkmcnt(6)\n"
    "v_mfma_f32_16x16x32_bf16 v[64:67], a[68:71], v[24:27], v[64:67]\n"
    "ds_read_b128 v[24:27], v9 offset:18432\n"
    "s_or_b64 exec, vcc, 1\n"
    "buffer_atomic_smax v5, v2, s[36:39], 0 offen sc0 sc1\n"
    "s_mov_b64 exec, -1\n"
    "v_mfma_f32_16x16x32_bf16 v[56:59], a[60:63], v[28:31], v[56:59]\n"
    "v_mfma_f32_16x16x32_bf16 v[60:63], a[64:67], v[28:31], v[60:63]\n"
    "s_waitcnt lgkmcnt(6)\n"
    "v_mfma_f32_16x16x32_bf16 v[68:71], a[68:71], v[28:31], v[68:71]\n"
    "ds_read_b128 v[28:31], v9 offset:19456\n"
    "v_mfma_f32_16x16x32_bf16 v[48:51], a[72:75], v[32:35], v[48:51]\n"
    "v_mfma_f32_16x16x32_bf16 v[52:55], a[76:79], v[32:35], v[52:55]\n"
    "s_waitcnt lgkmcnt(6)\n"
    "v_mfma_f32_16x16x32_bf16 v[64:67], a[80:83], v[32:35], v[64:67]\n"
    "ds_read_b128 v[32:35], v9 offset:20480\n"
    "v_mfma_f32_16x16x32_bf16 v[56:59], a[72:75], v[36:39], v[56:59]\n"
    "v_mfma_f32_16x16x32_bf16 v[60:63], a[76:79], v[36:39], v[60:63]\n"
    "s_waitcnt lgkmcnt(6)\n"
    "v_mfma_f32_16x16x32_bf16 v[68:71], a[80:83], v[36:39], v[68:71]\n"
    "ds_read_b128 v[36:39], v9 offset:21504\n"
    "v_mfma_f32_16x16x32_bf16 v[48:51], a[84:87], v[40:43], v[48:51]\n"
    "v_mfma_f32_16x16x32_bf16 v[52:55], a[88:91], v[40:43], v[52:55]\n"
    "s_waitcnt lgkmcnt(6)\n"
    "v_mfma_f32_16x16x32_bf16 v[64:67], a[92:95], v[40:43], v[64:67]\n"
    "ds_read_b128 v[40:43], v9 offset:22528\n"
    "v_mfma_f32_16x16x32_bf16 v[56:59], a[84:87], v[44:47], v[56:59]\n"
    "v_mfma_f32_16x16x32_bf16 v[60:63], a[88:91], v[44:47], v[60:63]\n"
    "s_waitcnt lgkmcnt(6)\n"
    "v_mfma_f32_16x16x32_bf16 v[68:71], a[92:95], v[44:47], v[68:71]\n"
    "ds_read_b128 v[44:47], v9 offset:23552\n"
    "s_waitcnt vmcnt(1)\n"
    "v_add_u32 v13, v11, v3\n"
    "v_mfma_f32_16x16x32_bf16 v[48:51], a[96:99], v[16:19], v[48:51]\n"
    "buffer_atomic_smax v12, v14, s[36:39], 0 offen sc1\n"
    "v_cmp_gt_i32 vcc, s71, v13\n"
    "v_mfma_f32_16x16x32_bf16 v[52:55], a[100:103], v[16:19], v[52:55]\n"
    "s_waitcnt lgkmcnt(6)\n"
    "v_add_u32 v10, 49152, v9\n"
    "v_mfma_f32_16x16x32_bf16 v[64:67], a[104:107], v[16:19], v[64:67]\n"
    "ds_read_b128 v[16:19], v9 offset:24576\n"
    "v_mfma_f32_16x16x32_bf16 v[56:59], a[96:99], v[20:23], v[56:59]\n"
    ".LgWO_mid_%=:\n"
    "s_cbranch_vccnz .LgWO_poll0_%=\n"
    "v_mfma_f32_16x16x32_bf16 v[60:63], a[100:103], v[20:23], v[60:63]\n"
    "s_waitcnt lgkmcnt(6)\n"
    "s_add_u32 m0, s77, s59\n"
    "s_add_u32 s77, s77, 8192\n"
    "buffer_load_dwordx4 v1, s[36:39], s76 offen sc1 lds\n"
    "v_mfma_f32_16x16x32_bf16 v[68:71], a[104:107], v[20:23], v[68:71]\n"
    "ds_read_b128 v[20:23], v9 offset:25600\n"
    "v_mfma_f32_16x16x32_bf16 v[48:51], a[108:111], v[24:27], v[48:51]\n"
    "v_mfma_f32_16x16x32_bf16 v[52:55], a[112:115], v[24:27], v[52:55]\n"
    "s_waitcnt lgkmcnt(6)\n"
    "v_mfma_f32_16x16x32_bf16 v[64:67], a[116:119], v[24:27], v[64:67]\n"
    "ds_read_b128 v[24:27], v9 offset:26624\n"
    "v_mfma_f32_16x16x32_bf16 v[56:59], a[108:111], v[28:31], v[56:59]\n"
    "buffer_load_dwordx4 v1, s[36:39], s76 offen offset:1024 sc1 lds\n"
    "s_add_u32 s76, s76, 16384\n"
    "v_mfma_f32_16x16x32_bf16 v[60:63], a[112:115], v[28:31], v[60:63]\n"
    "s_waitcnt lgkmcnt(6)\n"
    "v_mfma_f32_16x16x32_bf16 v[68:71], a[116:119], v[28:31], v[68:71]\n"
    "ds_read_b128 v[28:31], v9 offset:27648\n"
    "v_mfma_f32_16x16x32_bf16 v[48:51], a[120:123], v[32:35], v[48:51]\n"
    "v_mfma_f32_16x16x32_bf16 v[52:55], a[124:127], v[32:35], v[52:55]\n"
    "s_waitcnt lgkmcnt(6)\n"
    "s_add_u32 m0, s77, s59\n"
    "s_add_u32 s77, s77, 8192\n"
    "buffer_load_dwordx4 v1, s[36:39], s76 offen sc1 lds\n"
    "v_mfma_f32_16x16x32_bf16 v[64:67], a[128:131], v[32:35], v[64:67]\n"
    "ds_read_b128 v[32:35], v9 offset:28672\n"
    "v_mfma_f32_16x16x32_bf16 v[56:59], a[120:123], v[36:39], v[56:59]\n"
    "v_mfma_f32_16x16x32_bf16 v[60:63], a[124:127], v[36:39], v[60:63]\n"
    "s_waitcnt lgkmcnt(6)\n"
    "v_mfma_f32_16x16x32_bf16 v[68:71], a[128:131], v[36:39], v[68:71]\n"
    "ds_read_b128 v[36:39], v9 offset:29696\n"
    "v_mfma_f32_16x16x32_bf16 v[48:51], a[132:135], v[40:43], v[48:51]\n"
    "buffer_load_dwordx4 v1, s[36:39], s76 offen offset:1024 sc1 lds\n"
    "s_add_u32 s76, s76, 16384\n"
    "v_mfma_f32_16x16x32_bf16 v[52:55], a[136:139], v[40:43], v[52:55]\n"
    "s_waitcnt lgkmcnt(6)\n"
    "v_mfma_f32_16x16x32_bf16 v[64:67], a[140:143], v[40:43], v[64:67]\n"
    "ds_read_b128 v[40:43], v9 offset:30720\n"
    "v_mfma_f32_16x16x32_bf16 v[56:59], a[132:135], v[44:47], v[56:59]\n"
    "v_mfma_f32_16x16x32_bf16 v[60:63], a[136:139], v[44:47], v[60:63]\n"
    "s_waitcnt lgkmcnt(6)\n"
    "v_mfma_f32_16x16x32_bf16 v[68:71], a[140:143], v[44:47], v[68:71]\n"
    "ds_read_b128 v[44:47], v9 offset:31744\n"
    "v_mfma_f32_16x16x32_bf16 v[48:51], a[144:147], v[16:19], v[48:51]\n"
    "s_add_u32 m0, s77, s59\n"
    "s_add_u32 s77, s77, 8192\n"
    "buffer_load_dwordx4 v1, s[36:39], s76 offen sc1 lds\n"
    "v_mfma_f32_16x16x32_bf16 v[52:55], a[148:151], v[16:19], v[52:55]\n"
    "s_waitcnt lgkmcnt(6)\n"
    "v_mfma_f32_16x16x32_bf16 v[64:67], a[152:155], v[16:19], v[64:67]\n"
    "ds_read_b128 v[16:19], v9 offset:32768\n"
    "v_mfma_f32_16x16x32_bf16 v[56:59], a[144:147], v[20:23], v[56:59]\n"
    "v_mfma_f32_16x16x32_bf16 v[60:63], a[148:151], v[20:23], v[60:63]\n"
    "s_waitcnt lgkmcnt(6)\n"
    "buffer_load_dwordx4 v1, s[36:39], s76 offen offset:1024 sc1 lds\n"
    "s_add_u32 s76, s76, 16384\n"
    "v_mfma_f32_16x16x32_bf16 v[68:71], a[152:155], v[20:23], v[68:71]\n"
    "ds_read_b128 v[20:23], v9 offset:33792\n"
    "v_mfma_f32_16x16x32_bf16 v[48:51], a[156:159], v[24:27], v[48:51]\n"
    "v_mfma_f32_16x16x32_bf16 v[52:55], a[160:163], v[24:27], v[52:55]\n"
    "s_waitcnt lgkmcnt(6)\n"
    "v_mfma_f32_16x16x32_bf16 v[64:67], a[164:167], v[24:27], v[64:67]\n"
    "ds_read_b128 v[24:27], v9 offset:34816\n"
    "v_mfma_f32_16x16x32_bf16 v[56:59], a[156:159], v[28:31], v[56:59]\n"
    "s_add_u32 m0, s77, s59\n"
    "s_add_u32 s77, s77, 8192\n"
    "buffer_load_dwordx4 v1, s[36:39], s76 offen sc1 lds\n"
    "v_mfma_f32_16x16x32_bf16 v[60:63], a[160:163], v[28:31], v[60:63]\n"
    "s_waitcnt lgkmcnt(6)\n"
    "v_mfma_f32_16x16x32_bf16 v[68:71], a[164:167], v[28:31], v[68:71]\n"
    "ds_read_b128 v[28:31], v9 offset:35840\n"
    "v_mfma_f32_16x16x32_bf16 v[48:51], a[168:171], v[32:35], v[48:51]\n"
    "v_mfma_f32_16x16x32_bf16 v[52:55], a[172:175], v[32:35], v[52:55]\n"
    "s_waitcnt lgkmcnt(6)\n"
    "v_mfma_f32_16x16x32_bf16 v[64:67], a[176:179], v[32:35], v[64:67]\n"
    "ds_read_b128 v[32:35], v9 offset:36864\n"
    "v_mfma_f32_16x16x32_bf16 v[56:59], a[168:171], v[36:39], v[56:59]\n"
    "buffer_load_dwordx4 v1, s[36:39], s76 offen offset:1024 sc1 lds\n"
    "s_add_u32 s76, s76, 16384\n"
    "v_mfma_f32_16x16x32_bf16 v[60:63], a[172:175], v[36:39], v[60:63]\n"
    "s_waitcnt lgkmcnt(6)\n"
    "v_mfma_f32_16x16x32_bf16 v[68:71], a[176:179], v[36:39], v[68:71]\n"
    "ds_read_b128 v[36:39], v9 offset:37888\n"
    "v_mfma_f32_16x16x32_bf16 v[48:51], a[180:183], v[40:43], v[48:51]\n"
    "v_mfma_f32_16x16x32_bf16 v[52:55], a[184:187], v[40:43], v[52:55]\n"
    "s_waitcnt lgkmcnt(6)\n"
    "s_add_u32 m0, s77, s59\n"
    "s_add_u32 s77, s77, 8192\n"
    "buffer_load_dwordx4 v1, s[36:39], s76 offen sc1 lds\n"
    "v_mfma_f32_16x16x32_bf16 v[64:67], a[188:191], v[40:43], v[64:67]\n"
    "ds_read_b128 v[40:43], v9 offset:38912\n"
    "v_mfma_f32_16x16x32_bf16 v[56:59], a[180:183], v[44:47], v[56:59]\n"
    "v_mfma_f32_16x16x32_bf16 v[60:63], a[184:187], v[44:47], v[60:63]\n"
    "s_waitcnt lgkmcnt(6)\n"
    "v_mfma_f32_16x16x32_bf16 v[68:71], a[188:191], v[44:47], v[68:71]\n"
    "ds_read_b128 v[44:47], v9 offset:39936\n"
    "v_mfma_f32_16x16x32_bf16 v[48:51], a[192:195], v[16:19], v[48:51]\n"
    "buffer_load_dwordx4 v1, s[36:39], s76 offen offset:1024 sc1 lds\n"
    "s_add_u32 s76, s76, 16384\n"
    "v_mfma_f32_16x16x32_bf16 v[52:55], a[196:199], v[16:19], v[52:55]\n"
    "s_waitcnt lgkmcnt(6)\n"
    "v_mfma_f32_16x16x32_bf16 v[64:67], a[200:203], v[16:19], v[64:67]\n"
    "ds_read_b128 v[16:19], v9 offset:40960\n"
    "v_mfma_f32_16x16x32_bf16 v[56:59], a[192:195], v[20:23], v[56:59]\n"
    "v_mfma_f32_16x16x32_bf16 v[60:63], a[196:199], v[20:23], v[60:63]\n"
    "s_waitcnt lgkmcnt(6)\n"
    "v_mfma_f32_16x16x32_bf16 v[68:71], a[200:203], v[20:23], v[68:71]\n"
    "ds_read_b128 v[20:23], v9 offset:41984\n"
    "v_mfma_f32_16x16x32_bf16 v[48:51], a[204:207], v[24:27], v[48:51]\n"
    "s_add_u32 m0, s77, s59\n"
    "s_add_u32 s77, s77, 8192\n"
    "buffer_load_dwordx4 v1, s[36:39], s76 offen sc1 lds\n"
    "v_mfma_f32_16x16x32_bf16 v[52:55], a[208:211], v[24:27], v[52:55]\n"
    "s_waitcnt lgkmcnt(6)\n"
    "v_mfma_f32_16x16x32_bf16 v[64:67], a[212:215], v[24:27], v[64:67]\n"
    "ds_read_b128 v[24:27], v9 offset:43008\n"
    "v_mfma_f32_16x16x32_bf16 v[56:59], a[204:207], v[28:31], v[56:59]\n"
    "v_mfma_f32_16x16x32_bf16 v[60:63], a[208:211], v[28:31], v[60:63]\n"
    "s_waitcnt lgkmcnt(6)\n"
    "buffer_load_dwordx4 v1, s[36:39], s76 offen offset:1024 sc1 lds\n"
    "s_add_u32 s76, s76, 16384\n"
    "v_mfma_f32_16x16x32_bf16 v[68:71], a[212:215], v[28:31], v[68:71]\n"
    "ds_read_b128 v[28:31], v9 offset:44032\n"
    "v_mfma_f32_16x16x32_bf16 v[48:51], a[216:219], v[32:35], v[48:51]\n"
    "v_mfma_f32_16x16x32_bf16 v[52:55], a[220:223], v[32:35], v[52:55]\n"
    "s_waitcnt lgkmcnt(6)\n"
    "v_mfma_f32_16x16x32_bf16 v[64:67], a[224:227], v[32:35], v[64:67]\n"
    "ds_read_b128 v[32:35], v9 offset:45056\n"
    "v_mfma_f32_16x16x32_bf16 v[56:59], a[216:219], v[36:39], v[56:59]\n"
    "s_add_u32 m0, s77, s59\n"
    "s_add_u32 s77, s77, 8192\n"
    "buffer_load_dwordx4 v1, s[36:39], s76 offen sc1 lds\n"
    "v_mfma_f32_16x16x32_bf16 v[60:63], a[220:223], v[36:39], v[60:63]\n"
    "s_waitcnt lgkmcnt(6)\n"
    "v_mfma_f32_16x16x32_bf16 v[68:71], a[224:227], v[36:39], v[68:71]\n"
    "ds_read_b128 v[36:39], v9 offset:46080\n"
    "v_mfma_f32_16x16x32_bf16 v[48:51], a[228:231], v[40:43], v[48:51]\n"
    "v_mfma_f32_16x16x32_bf16 v[52:55], a[232:235], v[40:43], v[52:55]\n"
    "s_waitcnt lgkmcnt(6)\n"
    "v_mfma_f32_16x16x32_bf16 v[64:67], a[236:239], v[40:43], v[64:67]\n"
    "ds_read_b128 v[40:43], v9 offset:47104\n"
    "v_mfma_f32_16x16x32_bf16 v[56:59], a[228:231], v[44:47], v[56:59]\n"
    "buffer_load_dwordx4 v1, s[36:39], s76 offen offset:1024 sc1 lds\n"
    "s_add_u32 s76, s76, 16384\n"
    "v_mfma_f32_16x16x32_bf16 v[60:63], a[232:235], v[44:47], v[60:63]\n"
    "s_waitcnt lgkmcnt(6)\n"
    "v_mfma_f32_16x16x32_bf16 v[68:71], a[236:239], v[44:47], v[68:71]\n"
    "ds_read_b128 v[44:47], v9 offset:48128\n"
    "v_mfma_f32_16x16x32_bf16 v[48:51], a[240:243], v[16:19], v[48:51]\n"
    "v_mfma_f32_16x16x32_bf16 v[52:55], a[244:247], v[16:19], v[52:55]\n"
    "s_waitcnt lgkmcnt(6)\n"
    "s_add_u32 m0, s77, s59\n"
    "s_add_u32 s77, s77, 8192\n"
    "buffer_load_dwordx4 v1, s[36:39], s76 offen sc1 lds\n"
    "v_mfma_f32_16x16x32_bf16 v[64:67], a[248:251], v[16:19], v[64:67]\n"
    "ds_read_b128 v[16:19], v10 offset:0\n"
    "v_mfma_f32_16x16x32_bf16 v[56:59], a[240:243], v[20:23], v[56:59]\n"
    "v_mfma_f32_16x16x32_bf16 v[60:63], a[244:247], v[20:23], v[60:63]\n"
    "s_waitcnt lgkmcnt(6)\n"
    "v_mfma_f32_16x16x32_bf16 v[68:71], a[248:251], v[20:23], v[68:71]\n"
    "ds_read_b128 v[20:23], v10 offset:1024\n"
    "v_mfma_f32_16x16x32_bf16 v[48:51], a[252:255], v[24:27], v[48:51]\n"
    "buffer_load_dwordx4 v1, s[36:39], s76 offen offset:1024 sc1 lds\n"
    "s_add_u32 s76, s76, 16384\n"
    "v_mfma_f32_16x16x32_bf16 v[52:55], v[128:131], v[24:27], v[52:55]\n"
    "s_waitcnt lgkmcnt(6)\n"
    "v_mfma_f32_16x16x32_bf16 v[64:67], v[132:135], v[24:27], v[64:67]\n"
    "ds_read_b128 v[24:27], v10 offset:2048\n"
    "v_mfma_f32_16x16x32_bf16 v[56:59], a[252:255], v[28:31], v[56:59]\n"
    "v_mfma_f32_16x16x32_bf16 v[60:63], v[128:131], v[28:31], v[60:63]\n"
    "s_waitcnt lgkmcnt(6)\n"
    "v_mfma_f32_16x16x32_bf16 v[68:71], v[132:135], v[28:31], v[68:71]\n"
    "ds_read_b128 v[28:31], v10 offset:3072\n"
    ".LgWO_q132_%=:\n"
    "v_mfma_f32_16x16x32_bf16 v[48:51], v[136:139], v[32:35], v[48:51]\n"
    "v_mov_b32 v12, s68\n"
    "v_mfma_f32_16x16x32_bf16 v[52:55], v[140:143], v[32:35], v[52:55]\n"
    "s_waitcnt lgkmcnt(6)\n"
    "s_mov_b32 s68, s71\n"
    "v_mfma_f32_16x16x32_bf16 v[64:67], v[144:147], v[32:35], v[64:67]\n"
    "ds_read_b128 v[32:35], v10 offset:4096\n"
    "v_mfma_f32_16x16x32_bf16 v[56:59], v[136:139], v[36:39], v[56:59]\n"
    "s_add_u32 s71, s68, 1\n"
    "v_mfma_f32_16x16x32_bf16 v[60:63], v[140:143], v[36:39], v[60:63]\n"
    "s_waitcnt lgkmcnt(6)\n"
    "v_mfma_f32_16x16x32_bf16 v[68:71], v[144:147], v[36:39], v[68:71]\n"
    "ds_read_b128 v[36:39], v10 offset:5120\n"
    "v_mfma_f32_16x16x32_bf16 v[48:51], v[148:151], v[40:43], v[48:51]\n"
    "v_mfma_f32_16x16x32_bf16 v[52:55], v[152:155], v[40:43], v[52:55]\n"
    "s_waitcnt lgkmcnt(6)\n"
    "v_mfma_f32_16x16x32_bf16 v[64:67], v[156:159], v[40:43], v[64:67]\n"
    "ds_read_b128 v[40:43], v10 offset:6144\n"
    "v_mfma_f32_16x16x32_bf16 v[56:59], v[148:151], v[44:47], v[56:59]\n"
    "v_mfma_f32_16x16x32_bf16 v[60:63], v[152:155], v[44:47], v[60:63]\n"
    "s_waitcnt lgkmcnt(6)\n"
    "v_mfma_f32_16x16x32_bf16 v[68:71], v[156:159], v[44:47], v[68:71]\n"
    "ds_read_b128 v[44:47], v10 offset:7168\n"
    "v_mfma_f32_16x16x32_bf16 v[48:51], v[160:163], v[16:19], v[48:51]\n"
    "v_mfma_f32_16x16x32_bf16 v[52:55], v[164:167], v[16:19], v[52:55]\n"
    "s_waitcnt lgkmcnt(6)\n"
    "v_mfma_f32_16x16x32_bf16 v[64:67], v[168:171], v[16:19], v[64:67]\n"
    "ds_read_b128 v[16:19], v10 offset:8192\n"
    "v_mfma_f32_16x16x32_bf16 v[56:59], v[160:163], v[20:23], v[56:59]\n"
    "v_mfma_f32_16x16x32_bf16 v[60:63], v[164:167], v[20:23], v[60:63]\n"
    "s_waitcnt lgkmcnt(6)\n"
    "v_mfma_f32_16x16x32_bf16 v[68:71], v[168:171], v[20:23], v[68:71]\n"
    "ds_read_b128 v[20:23], v10 offset:9216\n"
    "v_mfma_f32_16x16x32_bf16 v[48:51], v[172:175], v[24:27], v[48:51]\n"
    "v_mfma_f32_16x16x32_bf16 v[52:55], v[176:179], v[24:27], v[52:55]\n"
    "s_waitcnt lgkmcnt(6)\n"
    "v_mfma_f32_16x16x32_bf16 v[64:67], v[180:183], v[24:27], v[64:67]\n"
    "ds_read_b128 v[24:27], v10 offset:10240\n"
    "v_mfma_f32_16x16x32_bf16 v[56:59], v[172:175], v[28:31], v[56:59]\n"
    "v_mfma_f32_16x16x32_bf16 v[60:63], v[176:179], v[28:31], v[60:63]\n"
    "s_waitcnt lgkmcnt(6)\n"
    "v_mfma_f32_16x16x32_bf16 v[68:71], v[180:183], v[28:31], v[68:71]\n"
    "ds_read_b128 v[28:31], v10 offset:11264\n"
    "v_mfma_f32_16x16x32_bf16 v[48:51], v[184:187], v[32:35], v[48:51]\n"
    "v_mfma_f32_16x16x32_bf16 v[52:55], v[188:191], v[32:35], v[52:55]\n"
    "s_waitcnt lgkmcnt(6)\n"
    "v_mfma_f32_16x16x32_bf16 v[64:67], v[192:195], v[32:35], v[64:67]\n"
    "ds_read_b128 v[32:35], v10 offset:12288\n"
    "v_mfma_f32_16x16x32_bf16 v[56:59], v[184:187], v[36:39], v[56:59]\n"
    "v_mfma_f32_16x16x32_bf16 v[60:63], v[188:191], v[36:39], v[60:63]\n"
    "s_waitcnt lgkmcnt(6)\n"
    "v_mfma_f32_16x16x32_bf16 v[68:71], v[192:195], v[36:39], v[68:71]\n"
    "ds_read_b128 v[36:39], v10 offset:13312\n"
    "v_mfma_f32_16x16x32_bf16 v[48:51], v[196:199], v[40:43], v[48:51]\n"
    "v_mfma_f32_16x16x32_bf16 v[52:55], v[200:203], v[40:43], v[52:55]\n"
    "s_waitcnt lgkmcnt(6)\n"
    "v_mfma_f32_16x16x32_bf16 v[64:67], v[204:207], v[40:43], v[64:67]\n"
    "ds_read_b128 v[40:43], v10 offset:14336\n"
    "v_mfma_f32_16x16x32_bf16 v[56:59], v[196:199], v[44:47], v[56:59]\n"
    "v_mfma_f32_16x16x32_bf16 v[60:63], v[200:203], v[44:47], v[60:63]\n"
    "s_waitcnt lgkmcnt(6)\n"
    "v_mfma_f32_16x16x32_bf16 v[68:71], v[204:207], v[44:47], v[68:71]\n"
    "ds_read_b128 v[44:47], v10 offset:15360\n"
    "v_mfma_f32_16x16x32_bf16 v[64:67], v[216:219], v[16:19], v[64:67]\n"
    "s_waitcnt lgkmcnt(6)\n"
    "ds_read_b128 v[16:19], v10 offset:8192\n"
    "v_mfma_f32_16x16x32_bf16 v[68:71], v[216:219], v[20:23], v[68:71]\n"
    "s_waitcnt lgkmcnt(6)\n"
    "ds_read_b128 v[20:23], v10 offset:10240\n"
    "v_mfma_f32_16x16x32_bf16 v[64:67], v[228:231], v[24:27], v[64:67]\n"
    "s_waitcnt lgkmcnt(6)\n"
    "ds_read_b128 v[24:27], v10 offset:12288\n"
    "v_mfma_f32_16x16x32_bf16 v[68:71], v[228:231], v[28:31], v[68:71]\n"
    "s_waitcnt lgkmcnt(6)\n"
    "ds_read_b128 v[28:31], v10 offset:14336\n"
    "v_mfma_f32_16x16x32_bf16 v[64:67], v[240:243], v[32:35], v[64:67]\n"
    "s_waitcnt lgkmcnt(6)\n"
    "ds_read_b128 v[32:35], v10 offset:9216\n"
    "v_mfma_f32_16x16x32_bf16 v[68:71], v[240:243], v[36:39], v[68:71]\n"
    "s_waitcnt lgkmcnt(6)\n"
    "ds_read_b128 v[36:39], v10 offset:11264\n"
    "v_mfma_f32_16x16x32_bf16 v[64:67], v[252:255], v[40:43], v[64:67]\n"
    "s_waitcnt lgkmcnt(6)\n"
    "ds_read_b128 v[40:43], v10 offset:13312\n"
    "v_mfma_f32_16x16x32_bf16 v[68:71], v[252:255], v[44:47], v[68:71]\n"
    "s_waitcnt lgkmcnt(6)\n"
    "ds_read_b128 v[44:47], v10 offset:15360\n"
    "v_mfma_f32_16x16x32_bf16 v[48:51], v[208:211], v[16:19], v[48:51]\n"
    "v_mfma_f32_16x16x32_bf16 v[52:55], v[212:215], v[16:19], v[52:55]\n"
    "s_waitcnt lgkmcnt(6)\n"
    "v_cvt_pk_bf16_f32 v64, v64, v65\n"
    "v_cvt_pk_bf16_f32 v65, v66, v67\n"
    "v_mfma_f32_16x16x32_bf16 v[48:51], v[220:223], v[20:23], v[48:51]\n"
    "s_waitcnt lgkmcnt(5)\n"
    "v_cvt_pk_bf16_f32 v66, v68, v69\n"
    "v_cvt_pk_bf16_f32 v67, v70, v71\n"
    "v_xor_b32 v9, s78, v9\n"
    "v_mfma_f32_16x16x32_bf16 v[52:55], v[224:227], v[20:23], v[52:55]\n"
    "s_waitcnt vmcnt(0) lgkmcnt(0)\n"
    "s_barrier\n"
    "ds_read_b128 v[16:19], v9 offset:0\n"
    "s_nop 0\n"
    "v_mfma_f32_16x16x32_bf16 v[48:51], v[232:235], v[24:27], v[48:51]\n"
    "ds_read_b128 v[20:23], v9 offset:1024\n"
    "v_mfma_f32_16x16x32_bf16 v[52:55], v[236:239], v[24:27], v[52:55]\n"
    "ds_read_b128 v[24:27], v9 offset:2048\n"
    "v_mfma_f32_16x16x32_bf16 v[48:51], v[244:247], v[28:31], v[48:51]\n"
    "v_permlane16_swap_b32 v64, v66\n"
    "v_mfma_f32_16x16x32_bf16 v[52:55], v[248:251], v[28:31], v[52:55]\n"
    "ds_read_b128 v[28:31], v9 offset:3072\n"
    "v_mfma_f32_16x16x32_bf16 v[56:59], v[208:211], v[32:35], v[56:59]\n"
    "v_permlane16_swap_b32 v65, v67\n"
    "v_mfma_f32_16x16x32_bf16 v[60:63], v[212:215], v[32:35], v[60:63]\n"
    "ds_read_b128 v[32:35], v9 offset:4096\n"
    "v_cvt_pk_bf16_f32 v48, v48, v49\n"
    "v_mfma_f32_16x16x32_bf16 v[56:59], v[220:223], v[36:39], v[56:59]\n"
    "buffer_store_dwordx4 v[64:67], v4, s[36:39], s73 offen sc1\n"
    "v_cvt_pk_bf16_f32 v49, v50, v51\n"
    "v_mfma_f32_16x16x32_bf16 v[60:63], v[224:227], v[36:39], v[60:63]\n"
    "ds_read_b128 v[36:39], v9 offset:5120\n"
    "v_cvt_pk_bf16_f32 v50, v52, v53\n"
    "v_mfma_f32_16x16x32_bf16 v[56:59], v[232:235], v[40:43], v[56:59]\n"
    "v_cvt_pk_bf16_f32 v51, v54, v55\n"
    "v_mfma_f32_16x16x32_bf16 v[60:63], v[236:239], v[40:43], v[60:63]\n"
    "ds_read_b128 v[40:43], v9 offset:6144\n"
    "v_mfma_f32_16x16x32_bf16 v[56:59], v[244:247], v[44:47], v[56:59]\n"
    "s_waitcnt lgkmcnt(6)\n"
    "v_mfma_f32_16x16x32_bf16 v[60:63], v[248:251], v[44:47], v[60:63]\n"
    "ds_read_b128 v[44:47], v9 offset:7168\n"
    "buffer_store_dwordx4 v[48:51], v15, s[36:39], s73 offen sc1\n"
    "s_branch .LgWO_head_%=\n"
    ".LgWO_LOOPE_%=:\n"
    ".LgWO_done_%=:\n"
    "s_waitcnt vmcnt(0)\n"
    "buffer_atomic_smax v12, v14, s[36:39], 0 offen sc1\n"
    "s_waitcnt vmcnt(0)\n"
    "s_branch .LgWO_FIN_%=\n"
    ".LgWO_poll0_%=:\n"
    "v_mfma_f32_16x16x32_bf16 v[60:63], a[100:103], v[20:23], v[60:63]\n"
    "s_waitcnt lgkmcnt(6)\n"
    "s_or_b64 exec, vcc, 1\n"
    "buffer_atomic_smax v11, v2, s[36:39], 0 offen sc0 sc1\n"
    "s_mov_b64 exec, -1\n"
    "v_mfma_f32_16x16x32_bf16 v[68:71], a[104:107], v[20:23], v[68:71]\n"
    "ds_read_b128 v[20:23], v9 offset:25600\n"
    "v_mfma_f32_16x16x32_bf16 v[48:51], a[108:111], v[24:27], v[48:51]\n"
    "v_mfma_f32_16x16x32_bf16 v[52:55], a[112:115], v[24:27], v[52:55]\n"
    "s_waitcnt lgkmcnt(6)\n"
    "v_mfma_f32_16x16x32_bf16 v[64:67], a[116:119], v[24:27], v[64:67]\n"
    "ds_read_b128 v[24:27], v9 offset:26624\n"
    "v_mfma_f32_16x16x32_bf16 v[56:59], a[108:111], v[28:31], v[56:59]\n"
    "v_mfma_f32_16x16x32_bf16 v[60:63], a[112:115], v[28:31], v[60:63]\n"
    "s_waitcnt lgkmcnt(6)\n"
    "v_mfma_f32_16x16x32_bf16 v[68:71], a[116:119], v[28:31], v[68:71]\n"
    "ds_read_b128 v[28:31], v9 offset:27648\n"
    "v_mfma_f32_16x16x32_bf16 v[48:51], a[120:123], v[32:35], v[48:51]\n"
    "v_mfma_f32_16x16x32_bf16 v[52:55], a[124:127], v[32:35], v[52:55]\n"
    "s_waitcnt lgkmcnt(6)\n"
    "v_mfma_f32_16x16x32_bf16 v[64:67], a[128:131], v[32:35], v[64:67]\n"
    "ds_read_b128 v[32:35], v9 offset:28672\n"
    "v_mfma_f32_16x16x32_bf16 v[56:59], a[120:123], v[36:39], v[56:59]\n"
    "v_mfma_f32_16x16x32_bf16 v[60:63], a[124:127], v[36:39], v[60:63]\n"
    "s_waitcnt lgkmcnt(6)\n"
    "v_mfma_f32_16x16x32_bf16 v[68:71], a[128:131], v[36:39], v[68:71]\n"
    "ds_read_b128 v[36:39], v9 offset:29696\n"
    "v_mfma_f32_16x16x32_bf16 v[48:51], a[132:135], v[40:43], v[48:51]\n"
    "v_mfma_f32_16x16x32_bf16 v[52:55], a[136:139], v[40:43], v[52:55]\n"
    "s_waitcnt lgkmcnt(6)\n"
    "s_waitcnt vmcnt(2)\n"
    "v_add_u32 v13, v5, v3\n"
    "v_mfma_f32_16x16x32_bf16 v[64:67], a[140:143], v[40:43], v[64:67]\n"
    "ds_read_b128 v[40:43], v9 offset:30720\n"
    "v_cmp_gt_i32 vcc, s71, v13\n"
    "v_mfma_f32_16x16x32_bf16 v[56:59], a[132:135], v[44:47], v[56:59]\n"
    "v_mfma_f32_16x16x32_bf16 v[60:63], a[136:139], v[44:47], v[60:63]\n"
    "s_waitcnt lgkmcnt(6)\n"
    "v_mfma_f32_16x16x32_bf16 v[68:71], a[140:143], v[44:47], v[68:71]\n"
    "ds_read_b128 v[44:47], v9 offset:31744\n"
    "s_cbranch_vccz .LgWO_dma72_%=\n"
    "v_mfma_f32_16x16x32_bf16 v[48:51], a[144:147], v[16:19], v[48:51]\n"
    "s_or_b64 exec, vcc, 1\n"
    "buffer_atomic_smax v5, v2, s[36:39], 0 offen sc0 sc1\n"
    "s_mov_b64 exec, -1\n"
    "v_mfma_f32_16x16x32_bf16 v[52:55], a[148:151], v[16:19], v[52:55]\n"
    "s_waitcnt lgkmcnt(6)\n"
    "v_mfma_f32_16x16x32_bf16 v[64:67], a[152:155], v[16:19], v[64:67]\n"
    "ds_read_b128 v[16:19], v9 offset:32768\n"
    "v_mfma_f32_16x16x32_bf16 v[56:59], a[144:147], v[20:23], v[56:59]\n"
    "v_mfma_f32_16x16x32_bf16 v[60:63], a[148:151], v[20:23], v[60:63]\n"
    "s_waitcnt lgkmcnt(6)\n"
    "v_mfma_f32_16x16x32_bf16 v[68:71], a[152:155], v[20:23], v[68:71]\n"
    "ds_read_b128 v[20:23], v9 offset:33792\n"
    "v_mfma_f32_16x16x32_bf16 v[48:51], a[156:159], v[24:27], v[48:51]\n"
    "v_mfma_f32_16x16x32_bf16 v[52:55], a[160:163], v[24:27], v[52:55]\n"
    "s_waitcnt lgkmcnt(6)\n"
    "v_mfma_f32_16x16x32_bf16 v[64:67], a[164:167], v[24:27], v[64:67]\n"
    "ds_read_b128 v[24:27], v9 offset:34816\n"
    "v_mfma_f32_16x16x32_bf16 v[56:59], a[156:159], v[28:31], v[56:59]\n"
    "v_mfma_f32_16x16x32_bf16 v[60:63], a[160:163], v[28:31], v[60:63]\n"
    "s_waitcnt lgkmcnt(6)\n"
    "v_mfma_f32_16x16x32_bf16 v[68:71], a[164:167], v[28:31], v[68:71]\n"
    "ds_read_b128 v[28:31], v9 offset:35840\n"
    "v_mfma_f32_16x16x32_bf16 v[48:51], a[168:171], v[32:35], v[48:51]\n"
    "v_mfma_f32_16x16x32_bf16 v[52:55], a[172:175], v[32:35], v[52:55]\n"
    "s_waitcnt lgkmcnt(6)\n"
    "v_mfma_f32_16x16x32_bf16 v[64:67], a[176:179], v[32:35], v[64:67]\n"
    "ds_read_b128 v[32:35], v9 offset:36864\n"
    "v_mfma_f32_16x16x32_bf16 v[56:59], a[168:171], v[36:39], v[56:59]\n"
    "s_waitcnt vmcnt(1)\n"
    "v_add_u32 v13, v11, v3\n"
    "v_mfma_f32_16x16x32_bf16 v[60:63], a[172:175], v[36:39], v[60:63]\n"
    "s_waitcnt lgkmcnt(6)\n"
    "v_cmp_gt_i32 vcc, s71, v13\n"
    "v_mfma_f32_16x16x32_bf16 v[68:71], a[176:179], v[36:39], v[68:71]\n"
    "ds_read_b128 v[36:39], v9 offset:37888\n"
    "v_mfma_f32_16x16x32_bf16 v[48:51], a[180:183], v[40:43], v[48:51]\n"
    "v_mfma_f32_16x16x32_bf16 v[52:55], a[184:187], v[40:43], v[52:55]\n"
    "s_waitcnt lgkmcnt(6)\n"
    "s_cbranch_vccz .LgWO_dma92_%=\n"
    "v_mfma_f32_16x16x32_bf16 v[64:67], a[188:191], v[40:43], v[64:67]\n"
    "ds_read_b128 v[40:43], v9 offset:38912\n"
    "s_or_b64 exec, vcc, 1\n"
    "buffer_atomic_smax v11, v2, s[36:39], 0 offen sc0 sc1\n"
    "s_mov_b64 exec, -1\n"
    "v_mfma_f32_16x16x32_bf16 v[56:59], a[180:183], v[44:47], v[56:59]\n"
    "v_mfma_f32_16x16x32_bf16 v[60:63], a[184:187], v[44:47], v[60:63]\n"
    "s_waitcnt lgkmcnt(6)\n"
    "v_mfma_f32_16x16x32_bf16 v[68:71], a[188:191], v[44:47], v[68:71]\n"
    "ds_read_b128 v[44:47], v9 offset:39936\n"
    "v_mfma_f32_16x16x32_bf16 v[48:51], a[192:195], v[16:19], v[48:51]\n"
    "v_mfma_f32_16x16x32_bf16 v[52:55], a[196:199], v[16:19], v[52:55]\n"
    "s_waitcnt lgkmcnt(6)\n"
    "v_mfma_f32_16x16x32_bf16 v[64:67], a[200:203], v[16:19], v[64:67]\n"
    "ds_read_b128 v[16:19], v9 offset:40960\n"
    "v_mfma_f32_16x16x32_bf16 v[56:59], a[192:195], v[20:23], v[56:59]\n"
    "v_mfma_f32_16x16x32_bf16 v[60:63], a[196:199], v[20:23], v[60:63]\n"
    "s_waitcnt lgkmcnt(6)\n"
    "v_mfma_f32_16x16x32_bf16 v[68:71], a[200:203], v[20:23], v[68:71]\n"
    "ds_read_b128 v[20:23], v9 offset:41984\n"
    "v_mfma_f32_16x16x32_bf16 v[48:51], a[204:207], v[24:27], v[48:51]\n"
    "v_mfma_f32_16x16x32_bf16 v[52:55], a[208:211], v[24:27], v[52:55]\n"
    "s_waitcnt lgkmcnt(6)\n"
    "v_mfma_f32_16x16x32_bf16 v[64:67], a[212:215], v[24:27], v[64:67]\n"
    "ds_read_b128 v[24:27], v9 offset:43008\n"
    "v_mfma_f32_16x16x32_bf16 v[56:59], a[204:207], v[28:31], v[56:59]\n"
    "v_mfma_f32_16x16x32_bf16 v[60:63], a[208:211], v[28:31], v[60:63]\n"
    "s_waitcnt lgkmcnt(6)\n"
    "v_mfma_f32_16x16x32_bf16 v[68:71], a[212:215], v[28:31], v[68:71]\n"
    "ds_read_b128 v[28:31], v9 offset:44032\n"
    "s_waitcnt vmcnt(1)\n"
    "v_add_u32 v13, v5, v3\n"
    "v_mfma_f32_16x16x32_bf16 v[48:51], a[216:219], v[32:35], v[48:51]\n"
    "v_cmp_gt_i32 vcc, s71, v13\n"
    "v_mfma_f32_16x16x32_bf16 v[52:55], a[220:223], v[32:35], v[52:55]\n"
    "s_waitcnt lgkmcnt(6)\n"
    "v_mfma_f32_16x16x32_bf16 v[64:67], a[224:227], v[32:35], v[64:67]\n"
    "ds_read_b128 v[32:35], v9 offset:45056\n"
    "v_mfma_f32_16x16x32_bf16 v[56:59], a[216:219], v[36:39], v[56:59]\n"
    "s_cbranch_vccz .LgWO_dma112_%=\n"
    "v_mfma_f32_16x16x32_bf16 v[60:63], a[220:223], v[36:39], v[60:63]\n"
    "s_waitcnt lgkmcnt(6)\n"
    "s_or_b64 exec, vcc, 1\n"
    "buffer_atomic_smax v5, v2, s[36:39], 0 offen sc0 sc1\n"
    "s_mov_b64 exec, -1\n"
    "v_mfma_f32_16x16x32_bf16 v[68:71], a[224:227], v[36:39], v[68:71]\n"
    "ds_read_b128 v[36:39], v9 offset:46080\n"
    "v_mfma_f32_16x16x32_bf16 v[48:51], a[228:231], v[40:43], v[48:51]\n"
    "v_mfma_f32_16x16x32_bf16 v[52:55], a[232:235], v[40:43], v[52:55]\n"
    "s_waitcnt lgkmcnt(6)\n"
    "v_mfma_f32_16x16x32_bf16 v[64:67], a[236:239], v[40:43], v[64:67]\n"
    "ds_read_b128 v[40:43], v9 offset:47104\n"
    "v_mfma_f32_16x16x32_bf16 v[56:59], a[228:231], v[44:47], v[56:59]\n"
    "v_mfma_f32_16x16x32_bf16 v[60:63], a[232:235], v[44:47], v[60:63]\n"
    "s_waitcnt lgkmcnt(6)\n"
    "v_mfma_f32_16x16x32_bf16 v[68:71], a[236:239], v[44:47], v[68:71]\n"
    "ds_read_b128 v[44:47], v9 offset:48128\n"
    "v_mfma_f32_16x16x32_bf16 v[48:51], a[240:243], v[16:19], v[48:51]\n"
    "v_mfma_f32_16x16x32_bf16 v[52:55], a[244:247], v[16:19], v[52:55]\n"
    "s_waitcnt lgkmcnt(6)\n"
    "v_mfma_f32_16x16x32_bf16 v[64:67], a[248:251], v[16:19], v[64:67]\n"
    "ds_read_b128 v[16:19], v10 offset:0\n"
    "v_mfma_f32_16x16x32_bf16 v[56:59], a[240:243], v[20:23], v[56:59]\n"
    "v_mfma_f32_16x16x32_bf16 v[60:63], a[244:247], v[20:23], v[60:63]\n"
    "s_waitcnt lgkmcnt(6)\n"
    "v_mfma_f32_16x16x32_bf16 v[68:71], a[248:251], v[20:23], v[68:71]\n"
    "ds_read_b128 v[20:23], v10 offset:1024\n"
    "v_mfma_f32_16x16x32_bf16 v[48:51], a[252:255], v[24:27], v[48:51]\n"
    "v_mfma_f32_16x16x32_bf16 v[52:55], v[128:131], v[24:27], v[52:55]\n"
    "s_waitcnt lgkmcnt(6)\n"
    "s_waitcnt vmcnt(1)\n"
    "v_add_u32 v13, v11, v3\n"
    "v_mfma_f32_16x16x32_bf16 v[64:67], v[132:135], v[24:27], v[64:67]\n"
    "ds_read_b128 v[24:27], v10 offset:2048\n"
    "v_cmp_gt_i32 vcc, s71, v13\n"
    "v_mfma_f32_16x16x32_bf16 v[56:59], a[252:255], v[28:31], v[56:59]\n"
    "v_mfma_f32_16x16x32_bf16 v[60:63], v[128:131], v[28:31], v[60:63]\n"
    "s_waitcnt lgkmcnt(6)\n"
    "v_mfma_f32_16x16x32_bf16 v[68:71], v[132:135], v[28:31], v[68:71]\n"
    "ds_read_b128 v[28:31], v10 offset:3072\n"
    "s_cbranch_vccz .LgWO_dma132_%=\n"
    "v_mfma_f32_16x16x32_bf16 v[48:51], v[136:139], v[32:35], v[48:51]\n"
    "s_or_b64 exec, vcc, 1\n"
    "buffer_atomic_smax v11, v2, s[36:39], 0 offen sc0 sc1\n"
    "s_mov_b64 exec, -1\n"
    "v_mfma_f32_16x16x32_bf16 v[52:55], v[140:143], v[32:35], v[52:55]\n"
    "s_waitcnt lgkmcnt(6)\n"
    "v_mfma_f32_16x16x32_bf16 v[64:67], v[144:147], v[32:35], v[64:67]\n"
    "ds_read_b128 v[32:35], v10 offset:4096\n"
    "v_mfma_f32_16x16x32_bf16 v[56:59], v[136:139], v[36:39], v[56:59]\n"
    "v_mfma_f32_16x16x32_bf16 v[60:63], v[140:143], v[36:39], v[60:63]\n"
    "s_waitcnt lgkmcnt(6)\n"
    "v_mfma_f32_16x16x32_bf16 v[68:71], v[144:147], v[36:39], v[68:71]\n"
    "ds_read_b128 v[36:39], v10 offset:5120\n"
    "v_mfma_f32_16x16x32_bf16 v[48:51], v[148:151], v[40:43], v[48:51]\n"
    "v_mfma_f32_16x16x32_bf16 v[52:55], v[152:155], v[40:43], v[52:55]\n"
    "s_waitcnt lgkmcnt(6)\n"
    "v_mfma_f32_16x16x32_bf16 v[64:67], v[156:159], v[40:43], v[64:67]\n"
    "ds_read_b128 v[40:43], v10 offset:6144\n"
    "v_mfma_f32_16x16x32_bf16 v[56:59], v[148:151], v[44:47], v[56:59]\n"
    "v_mfma_f32_16x16x32_bf16 v[60:63], v[152:155], v[44:47], v[60:63]\n"
    "s_waitcnt lgkmcnt(6)\n"
    "v_mfma_f32_16x16x32_bf16 v[68:71], v[156:159], v[44:47], v[68:71]\n"
    "ds_read_b128 v[44:47], v10 offset:7168\n"
    "v_mfma_f32_16x16x32_bf16 v[48:51], v[160:163], v[16:19], v[48:51]\n"
    "v_mfma_f32_16x16x32_bf16 v[52:55], v[164:167], v[16:19], v[52:55]\n"
    "s_waitcnt lgkmcnt(6)\n"
    "v_mfma_f32_16x16x32_bf16 v[64:67], v[168:171], v[16:19], v[64:67]\n"
    "ds_read_b128 v[16:19], v10 offset:8192\n"
    "v_mfma_f32_16x16x32_bf16 v[56:59], v[160:163], v[20:23], v[56:59]\n"
    "s_waitcnt vmcnt(1)\n"
    "v_add_u32 v13, v5, v3\n"
    "v_mfma_f32_16x16x32_bf16 v[60:63], v[164:167], v[20:23], v[60:63]\n"
    "s_waitcnt lgkmcnt(6)\n"
    "v_cmp_gt_i32 vcc, s71, v13\n"
    "v_mfma_f32_16x16x32_bf16 v[68:71], v[168:171], v[20:23], v[68:71]\n"
    "ds_read_b128 v[20:23], v10 offset:9216\n"
    "v_mfma_f32_16x16x32_bf16 v[48:51], v[172:175], v[24:27], v[48:51]\n"
    "v_mfma_f32_16x16x32_bf16 v[52:55], v[176:179], v[24:27], v[52:55]\n"
    "s_waitcnt lgkmcnt(6)\n"
    "s_cbranch_vccz .LgWO_dma152_%=\n"
    "v_mfma_f32_16x16x32_bf16 v[64:67], v[180:183], v[24:27], v[64:67]\n"
    "ds_read_b128 v[24:27], v10 offset:10240\n"
    "s_or_b64 exec, vcc, 1\n"
    "buffer_atomic_smax v5, v2, s[36:39], 0 offen sc0 sc1\n"
    "s_mov_b64 exec, -1\n"
    "v_mfma_f32_16x16x32_bf16 v[56:59], v[172:175], v[28:31], v[56:59]\n"
    "v_mfma_f32_16x16x32_bf16 v[60:63], v[176:179], v[28:31], v[60:63]\n"
    "s_waitcnt lgkmcnt(6)\n"
    "v_mfma_f32_16x16x32_bf16 v[68:71], v[180:183], v[28:31], v[68:71]\n"
    "ds_read_b128 v[28:31], v10 offset:11264\n"
    "v_mfma_f32_16x16x32_bf16 v[48:51], v[184:187], v[32:35], v[48:51]\n"
    "v_mfma_f32_16x16x32_bf16 v[52:55], v[188:191], v[32:35], v[52:55]\n"
    "s_waitcnt lgkmcnt(6)\n"
    "v_mfma_f32_16x16x32_bf16 v[64:67], v[192:195], v[32:35], v[64:67]\n"
    "ds_read_b128 v[32:35], v10 offset:12288\n"
    "v_mfma_f32_16x16x32_bf16 v[56:59], v[184:187], v[36:39], v[56:59]\n"
    "v_mfma_f32_16x16x32_bf16 v[60:63], v[188:191], v[36:39], v[60:63]\n"
    "s_waitcnt lgkmcnt(6)\n"
    "v_mfma_f32_16x16x32_bf16 v[68:71], v[192:195], v[36:39], v[68:71]\n"
    "ds_read_b128 v[36:39], v10 offset:13312\n"
    "v_mfma_f32_16x16x32_bf16 v[48:51], v[196:199], v[40:43], v[48:51]\n"
    "v_mfma_f32_16x16x32_bf16 v[52:55], v[200:203], v[40:43], v[52:55]\n"
    "s_waitcnt lgkmcnt(6)\n"
    "v_mfma_f32_16x16x32_bf16 v[64:67], v[204:207], v[40:43], v[64:67]\n"
    "ds_read_b128 v[40:43], v10 offset:14336\n"
    "v_mfma_f32_16x16x32_bf16 v[56:59], v[196:199], v[44:47], v[56:59]\n"
    "v_mfma_f32_16x16x32_bf16 v[60:63], v[200:203], v[44:47], v[60:63]\n"
    "s_waitcnt lgkmcnt(6)\n"
    "v_mfma_f32_16x16x32_bf16 v[68:71], v[204:207], v[44:47], v[68:71]\n"
    "ds_read_b128 v[44:47], v10 offset:15360\n"
    "s_waitcnt vmcnt(1)\n"
    "v_add_u32 v13, v11, v3\n"
    "v_mfma_f32_16x16x32_bf16 v[64:67], v[216:219], v[16:19], v[64:67]\n"
    "s_waitcnt lgkmcnt(6)\n"
    "ds_read_b128 v[16:19], v10 offset:8192\n"
    "v_cmp_gt_i32 vcc, s71, v13\n"
    "v_mfma_f32_16x16x32_bf16 v[68:71], v[216:219], v[20:23], v[68:71]\n"
    "s_waitcnt lgkmcnt(6)\n"
    "ds_read_b128 v[20:23], v10 offset:10240\n"
    "v_mfma_f32_16x16x32_bf16 v[64:67], v[228:231], v[24:27], v[64:67]\n"
    "s_waitcnt lgkmcnt(6)\n"
    "ds_read_b128 v[24:27], v10 offset:12288\n"
    "v_mfma_f32_16x16x32_bf16 v[68:71], v[228:231], v[28:31], v[68:71]\n"
    "s_waitcnt lgkmcnt(6)\n"
    "ds_read_b128 v[28:31], v10 offset:14336\n"
    "s_cbranch_vccz .LgWO_dma172_%=\n"
    "v_mfma_f32_16x16x32_bf16 v[64:67], v[240:243], v[32:35], v[64:67]\n"
    "s_waitcnt lgkmcnt(6)\n"
    "ds_read_b128 v[32:35], v10 offset:9216\n"
    "s_or_b64 exec, vcc, 1\n"
    "buffer_atomic_smax v11, v2, s[36:39], 0 offen sc0 sc1\n"
    "s_mov_b64 exec, -1\n"
    "v_mfma_f32_16x16x32_bf16 v[68:71], v[240:243], v[36:39], v[68:71]\n"
    "s_waitcnt lgkmcnt(6)\n"
    "ds_read_b128 v[36:39], v10 offset:11264\n"
    "v_mfma_f32_16x16x32_bf16 v[64:67], v[252:255], v[40:43], v[64:67]\n"
    "s_waitcnt lgkmcnt(6)\n"
    "ds_read_b128 v[40:43], v10 offset:13312\n"
    "v_mfma_f32_16x16x32_bf16 v[68:71], v[252:255], v[44:47], v[68:71]\n"
    "s_waitcnt lgkmcnt(6)\n"
    "ds_read_b128 v[44:47], v10 offset:15360\n"
    "v_mfma_f32_16x16x32_bf16 v[48:51], v[208:211], v[16:19], v[48:51]\n"
    "v_mfma_f32_16x16x32_bf16 v[52:55], v[212:215], v[16:19], v[52:55]\n"
    "s_waitcnt lgkmcnt(6)\n"
    "v_cvt_pk_bf16_f32 v64, v64, v65\n"
    "v_cvt_pk_bf16_f32 v65, v66, v67\n"
    "v_mfma_f32_16x16x32_bf16 v[48:51], v[220:223], v[20:23], v[48:51]\n"
    "s_waitcnt lgkmcnt(5)\n"
    "v_cvt_pk_bf16_f32 v66, v68, v69\n"
    "v_cvt_pk_bf16_f32 v67, v70, v71\n"
    "v_mfma_f32_16x16x32_bf16 v[52:55], v[224:227], v[20:23], v[52:55]\n"
    "s_nop 0\n"
    "v_mfma_f32_16x16x32_bf16 v[48:51], v[232:235], v[24:27], v[48:51]\n"
    "v_mfma_f32_16x16x32_bf16 v[52:55], v[236:239], v[24:27], v[52:55]\n"
    "s_waitcnt lgkmcnt(4)\n"
    "v_mfma_f32_16x16x32_bf16 v[48:51], v[244:247], v[28:31], v[48:51]\n"
    "s_waitcnt lgkmcnt(3)\n"
    "v_permlane16_swap_b32 v64, v66\n"
    "v_mfma_f32_16x16x32_bf16 v[52:55], v[248:251], v[28:31], v[52:55]\n"
    "v_mfma_f32_16x16x32_bf16 v[56:59], v[208:211], v[32:35], v[56:59]\n"
    "s_waitcnt lgkmcnt(2)\n"
    "v_permlane16_swap_b32 v65, v67\n"
    "v_mfma_f32_16x16x32_bf16 v[60:63], v[212:215], v[32:35], v[60:63]\n"
    "v_cvt_pk_bf16_f32 v48, v48, v49\n"
    "v_mfma_f32_16x16x32_bf16 v[56:59], v[220:223], v[36:39], v[56:59]\n"
    "s_waitcnt lgkmcnt(1)\n"
    "buffer_store_dwordx4 v[64:67], v4, s[36:39], s73 offen sc1\n"
    "v_cvt_pk_bf16_f32 v49, v50, v51\n"
    "v_mfma_f32_16x16x32_bf16 v[60:63], v[224:227], v[36:39], v[60:63]\n"
    "v_cvt_pk_bf16_f32 v50, v52, v53\n"
    "v_mfma_f32_16x16x32_bf16 v[56:59], v[232:235], v[40:43], v[56:59]\n"
    "s_waitcnt lgkmcnt(0)\n"
    "v_cvt_pk_bf16_f32 v51, v54, v55\n"
    "v_mfma_f32_16x16x32_bf16 v[60:63], v[236:239], v[40:43], v[60:63]\n"
    "v_mfma_f32_16x16x32_bf16 v[56:59], v[244:247], v[44:47], v[56:59]\n"
    "v_mfma_f32_16x16x32_bf16 v[60:63], v[248:251], v[44:47], v[60:63]\n"
    "buffer_store_dwordx4 v[48:51], v15, s[36:39], s73 offen sc1\n"
    "s_nop 2\n"
    "v_cvt_pk_bf16_f32 v56, v56, v57\n"
    "v_cvt_pk_bf16_f32 v57, v58, v59\n"
    "s_nop 1\n"
    "v_cvt_pk_bf16_f32 v58, v60, v61\n"
    "v_cvt_pk_bf16_f32 v59, v62, v63\n"
    "buffer_store_dwordx4 v[56:59], v15, s[36:39], s73 offen offset:1024 sc1\n"
    "v_mov_b32 v12, s68\n"
    "s_mov_b32 s68, s71\n"
    "s_cmp_eq_u32 s68, 64\n"
    "s_cbranch_scc1 .LgWO_done_%=\n"
    "s_waitcnt vmcnt(4)\n"
    "v_add_u32 v13, v5, v3\n"
    "v_cmp_gt_i32 vcc, s71, v13\n"
    "s_cbranch_vccnz .LgWO_idle_%=\n"
    "s_add_u32 m0, s77, s59\n"
    "s_add_u32 s77, s77, 8192\n"
    "buffer_load_dwordx4 v1, s[36:39], s76 offen sc1 lds\n"
    "buffer_load_dwordx4 v1, s[36:39], s76 offen offset:1024 sc1 lds\n"
    "s_add_u32 s76, s76, 16384\n"
    "s_add_u32 m0, s77, s59\n"
    "s_add_u32 s77, s77, 8192\n"
    "buffer_load_dwordx4 v1, s[36:39], s76 offen sc1 lds\n"
    "buffer_load_dwordx4 v1, s[36:39], s76 offen offset:1024 sc1 lds\n"
    "s_add_u32 s76, s76, 16384\n"
    "s_add_u32 m0, s77, s59\n"
    "s_add_u32 s77, s77, 8192\n"
    "buffer_load_dwordx4 v1, s[36:39], s76 offen sc1 lds\n"
    "buffer_load_dwordx4 v1, s[36:39], s76 offen offset:1024 sc1 lds\n"
    "s_add_u32 s76, s76, 16384\n"
    "s_add_u32 m0, s77, s59\n"
    "s_add_u32 s77, s77, 8192\n"
    "buffer_load_dwordx4 v1, s[36:39], s76 offen sc1 lds\n"
    "buffer_load_dwordx4 v1, s[36:39], s76 offen offset:1024 sc1 lds\n"
    "s_add_u32 s76, s76, 16384\n"
    "s_add_u32 m0, s77, s59\n"
    "s_add_u32 s77, s77, 8192\n"
    "buffer_load_dwordx4 v1, s[36:39], s76 offen sc1 lds\n"
    "buffer_load_dwordx4 v1, s[36:39], s76 offen offset:1024 sc1 lds\n"
    "s_add_u32 s76, s76, 16384\n"
    "s_add_u32 m0, s77, s59\n"
    "s_add_u32 s77, s77, 8192\n"
    "buffer_load_dwordx4 v1, s[36:39], s76 offen sc1 lds\n"
    "buffer_load_dwordx4 v1, s[36:39], s76 offen offset:1024 sc1 lds\n"
    "s_add_u32 s76, s76, 16384\n"
    "s_add_u32 m0, s77, s59\n"
    "s_add_u32 s77, s77, 8192\n"
    "buffer_load_dwordx4 v1, s[36:39], s76 offen sc1 lds\n"
    "buffer_load_dwordx4 v1, s[36:39], s76 offen offset:1024 sc1 lds\n"
    "s_add_u32 s76, s76, 16384\n"
    "s_add_u32 m0, s77, s59\n"
    "s_add_u32 s77, s77, 8192\n"
    "buffer_load_dwordx4 v1, s[36:39], s76 offen sc1 lds\n"
    "buffer_load_dwordx4 v1, s[36:39], s76 offen offset:1024 sc1 lds\n"
    "s_add_u32 s76, s76, 16384\n"
    ".LgWO_late_%=:\n"
    "s_waitcnt vmcnt(16)\n"
    "buffer_atomic_smax v12, v14, s[36:39], 0 offen sc1\n"
    "s_waitcnt vmcnt(1)\n"
    ".LgWO_bar_%=:\n"
    "v_xor_b32 v9, s78, v9\n"
    "s_add_u32 s71, s68, 1\n"
    "s_barrier\n"
    "ds_read_b128 v[16:19], v9 offset:0\n"
    "ds_read_b128 v[20:23], v9 offset:1024\n"
    "ds_read_b128 v[24:27], v9 offset:2048\n"
    "ds_read_b128 v[28:31], v9 offset:3072\n"
    "ds_read_b128 v[32:35], v9 offset:4096\n"
    "ds_read_b128 v[36:39], v9 offset:5120\n"
    "ds_read_b128 v[40:43], v9 offset:6144\n"
    "ds_read_b128 v[44:47], v9 offset:7168\n"
    "v_add_u32 v13, v11, v3\n"
    "v_cmp_gt_i32 vcc, s71, v13\n"
    "s_nop 0\n"
    "s_or_b64 exec, vcc, 1\n"
    "buffer_atomic_smax v11, v2, s[36:39], 0 offen sc0 sc1\n"
    "s_mov_b64 exec, -1\n"
    "s_waitcnt lgkmcnt(7)\n"
    "v_mfma_f32_16x16x32_bf16 v[64:67], a[8:11], v[16:19], 0\n"
    "s_waitcnt lgkmcnt(6)\n"
    "ds_read_b128 v[16:19], v9 offset:0\n"
    "v_mfma_f32_16x16x32_bf16 v[68:71], a[8:11], v[20:23], 0\n"
    "s_waitcnt lgkmcnt(6)\n"
    "ds_read_b128 v[20:23], v9 offset:2048\n"
    "v_mfma_f32_16x16x32_bf16 v[64:67], a[20:23], v[24:27], v[64:67]\n"
    "s_waitcnt lgkmcnt(6)\n"
    "ds_read_b128 v[24:27], v9 offset:4096\n"
    "v_mfma_f32_16x16x32_bf16 v[68:71], a[20:23], v[28:31], v[68:71]\n"
    "s_waitcnt lgkmcnt(6)\n"
    "ds_read_b128 v[28:31], v9 offset:6144\n"
    "v_mfma_f32_16x16x32_bf16 v[64:67], a[32:35], v[32:35], v[64:67]\n"
    "s_waitcnt lgkmcnt(6)\n"
    "ds_read_b128 v[32:35], v9 offset:1024\n"
    "v_mfma_f32_16x16x32_bf16 v[68:71], a[32:35], v[36:39], v[68:71]\n"
    "s_waitcnt lgkmcnt(6)\n"
    "ds_read_b128 v[36:39], v9 offset:3072\n"
    "v_mfma_f32_16x16x32_bf16 v[64:67], a[44:47], v[40:43], v[64:67]\n"
    "s_waitcnt lgkmcnt(6)\n"
    "ds_read_b128 v[40:43], v9 offset:5120\n"
    "v_mfma_f32_16x16x32_bf16 v[68:71], a[44:47], v[44:47], v[68:71]\n"
    "s_waitcnt lgkmcnt(6)\n"
    "ds_read_b128 v[44:47], v9 offset:7168\n"
    "v_mfma_f32_16x16x32_bf16 v[48:51], a[0:3], v[16:19], 0\n"
    "s_waitcnt lgkmcnt(6)\n"
    "v_mfma_f32_16x16x32_bf16 v[52:55], a[4:7], v[16:19], 0\n"
    "ds_read_b128 v[16:19], v9 offset:8192\n"
    "v_mfma_f32_16x16x32_bf16 v[48:51], a[12:15], v[20:23], v[48:51]\n"
    "s_waitcnt lgkmcnt(6)\n"
    "s_waitcnt vmcnt(1)\n"
    "buffer_atomic_smax v12, v14, s[36:39], 0 offen sc1\n"
    "v_mfma_f32_16x16x32_bf16 v[52:55], a[16:19], v[20:23], v[52:55]\n"
    "ds_read_b128 v[20:23], v9 offset:9216\n"
    "v_mfma_f32_16x16x32_bf16 v[48:51], a[24:27], v[24:27], v[48:51]\n"
    "s_waitcnt lgkmcnt(6)\n"
    "v_mfma_f32_16x16x32_bf16 v[52:55], a[28:31], v[24:27], v[52:55]\n"
    "ds_read_b128 v[24:27], v9 offset:10240\n"
    "v_mfma_f32_16x16x32_bf16 v[48:51], a[36:39], v[28:31], v[48:51]\n"
    "s_waitcnt lgkmcnt(6)\n"
    "s_and_b32 s76, s71, s54\n"
    "v_mfma_f32_16x16x32_bf16 v[52:55], a[40:43], v[28:31], v[52:55]\n"
    "ds_read_b128 v[28:31], v9 offset:11264\n"
    "v_mfma_f32_16x16x32_bf16 v[56:59], a[0:3], v[32:35], 0\n"
    "s_waitcnt lgkmcnt(6)\n"
    "s_mul_i32 s76, s76, s53\n"
    "v_mfma_f32_16x16x32_bf16 v[60:63], a[4:7], v[32:35], 0\n"
    "ds_read_b128 v[32:35], v9 offset:12288\n"
    "v_mfma_f32_16x16x32_bf16 v[56:59], a[12:15], v[36:39], v[56:59]\n"
    "s_waitcnt lgkmcnt(6)\n"
    "s_add_u32 s76, s76, s52\n"
    "v_mfma_f32_16x16x32_bf16 v[60:63], a[16:19], v[36:39], v[60:63]\n"
    "ds_read_b128 v[36:39], v9 offset:13312\n"
    "v_mfma_f32_16x16x32_bf16 v[56:59], a[24:27], v[40:43], v[56:59]\n"
    "s_waitcnt lgkmcnt(6)\n"
    "s_and_b32 s77, s71, 1\n"
    "v_mfma_f32_16x16x32_bf16 v[60:63], a[28:31], v[40:43], v[60:63]\n"
    "ds_read_b128 v[40:43], v9 offset:14336\n"
    "v_mfma_f32_16x16x32_bf16 v[56:59], a[36:39], v[44:47], v[56:59]\n"
    "s_waitcnt lgkmcnt(6)\n"
    "s_mul_i32 s77, s77, 65536\n"
    "v_mfma_f32_16x16x32_bf16 v[60:63], a[40:43], v[44:47], v[60:63]\n"
    "ds_read_b128 v[44:47], v9 offset:15360\n"
    "v_mfma_f32_16x16x32_bf16 v[48:51], a[48:51], v[16:19], v[48:51]\n"
    "s_add_u32 s77, s77, s59\n"
    "v_mfma_f32_16x16x32_bf16 v[52:55], a[52:55], v[16:19], v[52:55]\n"
    "s_waitcnt lgkmcnt(6)\n"
    "s_and_b32 s73, s68, s57\n"
    "v_mfma_f32_16x16x32_bf16 v[64:67], a[56:59], v[16:19], v[64:67]\n"
    "ds_read_b128 v[16:19], v9 offset:16384\n"
    "v_mfma_f32_16x16x32_bf16 v[56:59], a[48:51], v[20:23], v[56:59]\n"
    "s_mul_i32 s73, s73, s56\n"
    "v_mfma_f32_16x16x32_bf16 v[60:63], a[52:55], v[20:23], v[60:63]\n"
    "s_waitcnt lgkmcnt(6)\n"
    "s_add_u32 s73, s73, s55\n"
    "v_mfma_f32_16x16x32_bf16 v[68:71], a[56:59], v[20:23], v[68:71]\n"
    "ds_read_b128 v[20:23], v9 offset:17408\n"
    "v_add_u32 v13, v5, v3\n"
    "v_mfma_f32_16x16x32_bf16 v[48:51], a[60:63], v[24:27], v[48:51]\n"
    "v_cmp_gt_i32 vcc, s71, v13\n"
    "v_mfma_f32_16x16x32_bf16 v[52:55], a[64:67], v[24:27], v[52:55]\n"
    "s_waitcnt lgkmcnt(6)\n"
    "v_mfma_f32_16x16x32_bf16 v[64:67], a[68:71], v[24:27], v[64:67]\n"
    "ds_read_b128 v[24:27], v9 offset:18432\n"
    "s_or_b64 exec, vcc, 1\n"
    "buffer_atomic_smax v5, v2, s[36:39], 0 offen sc0 sc1\n"
    "s_mov_b64 exec, -1\n"
    "v_mfma_f32_16x16x32_bf16 v[56:59], a[60:63], v[28:31], v[56:59]\n"
    "v_mfma_f32_16x16x32_bf16 v[60:63], a[64:67], v[28:31], v[60:63]\n"
    "s_waitcnt lgkmcnt(6)\n"
    "v_mfma_f32_16x16x32_bf16 v[68:71], a[68:71], v[28:31], v[68:71]\n"
    "ds_read_b128 v[28:31], v9 offset:19456\n"
    "v_mfma_f32_16x16x32_bf16 v[48:51], a[72:75], v[32:35], v[48:51]\n"
    "v_mfma_f32_16x16x32_bf16 v[52:55], a[76:79], v[32:35], v[52:55]\n"
    "s_waitcnt lgkmcnt(6)\n"
    "v_mfma_f32_16x16x32_bf16 v[64:67], a[80:83], v[32:35], v[64:67]\n"
    "ds_read_b128 v[32:35], v9 offset:20480\n"
    "v_mfma_f32_16x16x32_bf16 v[56:59], a[72:75], v[36:39], v[56:59]\n"
    "v_mfma_f32_16x16x32_bf16 v[60:63], a[76:79], v[36:39], v[60:63]\n"
    "s_waitcnt lgkmcnt(6)\n"
    "v_mfma_f32_16x16x32_bf16 v[68:71], a[80:83], v[36:39], v[68:71]\n"
    "ds_read_b128 v[36:39], v9 offset:21504\n"
    "v_mfma_f32_16x16x32_bf16 v[48:51], a[84:87], v[40:43], v[48:51]\n"
    "v_mfma_f32_16x16x32_bf16 v[52:55], a[88:91], v[40:43], v[52:55]\n"
    "s_waitcnt lgkmcnt(6)\n"
    "v_mfma_f32_16x16x32_bf16 v[64:67], a[92:95], v[40:43], v[64:67]\n"
    "ds_read_b128 v[40:43], v9 offset:22528\n"
    "v_mfma_f32_16x16x32_bf16 v[56:59], a[84:87], v[44:47], v[56:59]\n"
    "v_mfma_f32_16x16x32_bf16 v[60:63], a[88:91], v[44:47], v[60:63]\n"
    "s_waitcnt lgkmcnt(6)\n"
    "v_mfma_f32_16x16x32_bf16 v[68:71], a[92:95], v[44:47], v[68:71]\n"
    "ds_read_b128 v[44:47], v9 offset:23552\n"
    "s_waitcnt vmcnt(1)\n"
    "v_add_u32 v13, v11, v3\n"
    "v_mfma_f32_16x16x32_bf16 v[48:51], a[96:99], v[16:19], v[48:51]\n"
    "buffer_atomic_smax v12, v14, s[36:39], 0 offen sc1\n"
    "v_cmp_gt_i32 vcc, s71, v13\n"
    "v_mfma_f32_16x16x32_bf16 v[52:55], a[100:103], v[16:19], v[52:55]\n"
    "s_waitcnt lgkmcnt(6)\n"
    "v_add_u32 v10, 49152, v9\n"
    "v_mfma_f32_16x16x32_bf16 v[64:67], a[104:107], v[16:19], v[64:67]\n"
    "ds_read_b128 v[16:19], v9 offset:24576\n"
    "v_mfma_f32_16x16x32_bf16 v[56:59], a[96:99], v[20:23], v[56:59]\n"
    "s_branch .LgWO_mid_%=\n"
    ".LgWO_dma72_%=:\n"
    "v_mfma_f32_16x16x32_bf16 v[48:51], a[144:147], v[16:19], v[48:51]\n"
    "s_add_u32 m0, s77, s59\n"
    "s_add_u32 s77, s77, 8192\n"
    "buffer_load_dwordx4 v1, s[36:39], s76 offen sc1 lds\n"
    "v_mfma_f32_16x16x32_bf16 v[52:55], a[148:151], v[16:19], v[52:55]\n"
    "s_waitcnt lgkmcnt(6)\n"
    "v_mfma_f32_16x16x32_bf16 v[64:67], a[152:155], v[16:19], v[64:67]\n"
    "ds_read_b128 v[16:19], v9 offset:32768\n"
    "v_mfma_f32_16x16x32_bf16 v[56:59], a[144:147], v[20:23], v[56:59]\n"
    "buffer_load_dwordx4 v1, s[36:39], s76 offen offset:1024 sc1 lds\n"
    "s_add_u32 s76, s76, 16384\n"
    "v_mfma_f32_16x16x32_bf16 v[60:63], a[148:151], v[20:23], v[60:63]\n"
    "s_waitcnt lgkmcnt(6)\n"
    "v_mfma_f32_16x16x32_bf16 v[68:71], a[152:155], v[20:23], v[68:71]\n"
    "ds_read_b128 v[20:23], v9 offset:33792\n"
    "v_mfma_f32_16x16x32_bf16 v[48:51], a[156:159], v[24:27], v[48:51]\n"
    "v_mfma_f32_16x16x32_bf16 v[52:55], a[160:163], v[24:27], v[52:55]\n"
    "s_waitcnt lgkmcnt(6)\n"
    "s_add_u32 m0, s77, s59\n"
    "s_add_u32 s77, s77, 8192\n"
    "buffer_load_dwordx4 v1, s[36:39], s76 offen sc1 lds\n"
    "v_mfma_f32_16x16x32_bf16 v[64:67], a[164:167], v[24:27], v[64:67]\n"
    "ds_read_b128 v[24:27], v9 offset:34816\n"
    "v_mfma_f32_16x16x32_bf16 v[56:59], a[156:159], v[28:31], v[56:59]\n"
    "v_mfma_f32_16x16x32_bf16 v[60:63], a[160:163], v[28:31], v[60:63]\n"
    "s_waitcnt lgkmcnt(6)\n"
    "buffer_load_dwordx4 v1, s[36:39], s76 offen offset:1024 sc1 lds\n"
    "s_add_u32 s76, s76, 16384\n"
    "v_mfma_f32_16x16x32_bf16 v[68:71], a[164:167], v[28:31], v[68:71]\n"
    "ds_read_b128 v[28:31], v9 offset:35840\n"
    "v_mfma_f32_16x16x32_bf16 v[48:51], a[168:171], v[32:35], v[48:51]\n"
    "v_mfma_f32_16x16x32_bf16 v[52:55], a[172:175], v[32:35], v[52:55]\n"
    "s_waitcnt lgkmcnt(6)\n"
    "v_mfma_f32_16x16x32_bf16 v[64:67], a[176:179], v[32:35], v[64:67]\n"
    "ds_read_b128 v[32:35], v9 offset:36864\n"
    "v_mfma_f32_16x16x32_bf16 v[56:59], a[168:171], v[36:39], v[56:59]\n"
    "s_add_u32 m0, s77, s59\n"
    "s_add_u32 s77, s77, 8192\n"
    "buffer_load_dwordx4 v1, s[36:39], s76 offen sc1 lds\n"
    "v_mfma_f32_16x16x32_bf16 v[60:63], a[172:175], v[36:39], v[60:63]\n"
    "s_waitcnt lgkmcnt(6)\n"
    "v_mfma_f32_16x16x32_bf16 v[68:71], a[176:179], v[36:39], v[68:71]\n"
    "ds_read_b128 v[36:39], v9 offset:37888\n"
    "v_mfma_f32_16x16x32_bf16 v[48:51], a[180:183], v[40:43], v[48:51]\n"
    "buffer_load_dwordx4 v1, s[36:39], s76 offen offset:1024 sc1 lds\n"
    "s_add_u32 s76, s76, 16384\n"
    "v_mfma_f32_16x16x32_bf16 v[52:55], a[184:187], v[40:43], v[52:55]\n"
    "s_waitcnt lgkmcnt(6)\n"
    "v_mfma_f32_16x16x32_bf16 v[64:67], a[188:191], v[40:43], v[64:67]\n"
    "ds_read_b128 v[40:43], v9 offset:38912\n"
    "v_mfma_f32_16x16x32_bf16 v[56:59], a[180:183], v[44:47], v[56:59]\n"
    "v_mfma_f32_16x16x32_bf16 v[60:63], a[184:187], v[44:47], v[60:63]\n"
    "s_waitcnt lgkmcnt(6)\n"
    "s_add_u32 m0, s77, s59\n"
    "s_add_u32 s77, s77, 8192\n"
    "buffer_load_dwordx4 v1, s[36:39], s76 offen sc1 lds\n"
    "v_mfma_f32_16x16x32_bf16 v[68:71], a[188:191], v[44:47], v[68:71]\n"
    "ds_read_b128 v[44:47], v9 offset:39936\n"
    "v_mfma_f32_16x16x32_bf16 v[48:51], a[192:195], v[16:19], v[48:51]\n"
    "v_mfma_f32_16x16x32_bf16 v[52:55], a[196:199], v[16:19], v[52:55]\n"
    "s_waitcnt lgkmcnt(6)\n"
    "buffer_load_dwordx4 v1, s[36:39], s76 offen offset:1024 sc1 lds\n"
    "s_add_u32 s76, s76, 16384\n"
    "v_mfma_f32_16x16x32_bf16 v[64:67], a[200:203], v[16:19], v[64:67]\n"
    "ds_read_b128 v[16:19], v9 offset:40960\n"
    "v_mfma_f32_16x16x32_bf16 v[56:59], a[192:195], v[20:23], v[56:59]\n"
    "v_mfma_f32_16x16x32_bf16 v[60:63], a[196:199], v[20:23], v[60:63]\n"
    "s_waitcnt lgkmcnt(6)\n"
    "v_mfma_f32_16x16x32_bf16 v[68:71], a[200:203], v[20:23], v[68:71]\n"
    "ds_read_b128 v[20:23], v9 offset:41984\n"
    "v_mfma_f32_16x16x32_bf16 v[48:51], a[204:207], v[24:27], v[48:51]\n"
    "s_add_u32 m0, s77, s59\n"
    "s_add_u32 s77, s77, 8192\n"
    "buffer_load_dwordx4 v1, s[36:39], s76 offen sc1 lds\n"
    "v_mfma_f32_16x16x32_bf16 v[52:55], a[208:211], v[24:27], v[52:55]\n"
    "s_waitcnt lgkmcnt(6)\n"
    "v_mfma_f32_16x16x32_bf16 v[64:67], a[212:215], v[24:27], v[64:67]\n"
    "ds_read_b128 v[24:27], v9 offset:43008\n"
    "v_mfma_f32_16x16x32_bf16 v[56:59], a[204:207], v[28:31], v[56:59]\n"
    "buffer_load_dwordx4 v1, s[36:39], s76 offen offset:1024 sc1 lds\n"
    "s_add_u32 s76, s76, 16384\n"
    "v_mfma_f32_16x16x32_bf16 v[60:63], a[208:211], v[28:31], v[60:63]\n"
    "s_waitcnt lgkmcnt(6)\n"
    "v_mfma_f32_16x16x32_bf16 v[68:71], a[212:215], v[28:31], v[68:71]\n"
    "ds_read_b128 v[28:31], v9 offset:44032\n"
    "v_mfma_f32_16x16x32_bf16 v[48:51], a[216:219], v[32:35], v[48:51]\n"
    "v_mfma_f32_16x16x32_bf16 v[52:55], a[220:223], v[32:35], v[52:55]\n"
    "s_waitcnt lgkmcnt(6)\n"
    "s_add_u32 m0, s77, s59\n"
    "s_add_u32 s77, s77, 8192\n"
    "buffer_load_dwordx4 v1, s[36:39], s76 offen sc1 lds\n"
    "v_mfma_f32_16x16x32_bf16 v[64:67], a[224:227], v[32:35], v[64:67]\n"
    "ds_read_b128 v[32:35], v9 offset:45056\n"
    "v_mfma_f32_16x16x32_bf16 v[56:59], a[216:219], v[36:39], v[56:59]\n"
    "v_mfma_f32_16x16x32_bf16 v[60:63], a[220:223], v[36:39], v[60:63]\n"
    "s_waitcnt lgkmcnt(6)\n"
    "buffer_load_dwordx4 v1, s[36:39], s76 offen offset:1024 sc1 lds\n"
    "s_add_u32 s76, s76, 16384\n"
    "v_mfma_f32_16x16x32_bf16 v[68:71], a[224:227], v[36:39], v[68:71]\n"
    "ds_read_b128 v[36:39], v9 offset:46080\n"
    "v_mfma_f32_16x16x32_bf16 v[48:51], a[228:231], v[40:43], v[48:51]\n"
    "v_mfma_f32_16x16x32_bf16 v[52:55], a[232:235], v[40:43], v[52:55]\n"
    "s_waitcnt lgkmcnt(6)\n"
    "v_mfma_f32_16x16x32_bf16 v[64:67], a[236:239], v[40:43], v[64:67]\n"
    "ds_read_b128 v[40:43], v9 offset:47104\n"
    "v_mfma_f32_16x16x32_bf16 v[56:59], a[228:231], v[44:47], v[56:59]\n"
    "s_add_u32 m0, s77, s59\n"
    "s_add_u32 s77, s77, 8192\n"
    "buffer_load_dwordx4 v1, s[36:39], s76 offen sc1 lds\n"
    "v_mfma_f32_16x16x32_bf16 v[60:63], a[232:235], v[44:47], v[60:63]\n"
    "s_waitcnt lgkmcnt(6)\n"
    "v_mfma_f32_16x16x32_bf16 v[68:71], a[236:239], v[44:47], v[68:71]\n"
    "ds_read_b128 v[44:47], v9 offset:48128\n"
    "v_mfma_f32_16x16x32_bf16 v[48:51], a[240:243], v[16:19], v[48:51]\n"
    "buffer_load_dwordx4 v1, s[36:39], s76 offen offset:1024 sc1 lds\n"
    "s_add_u32 s76, s76, 16384\n"
    "v_mfma_f32_16x16x32_bf16 v[52:55], a[244:247], v[16:19], v[52:55]\n"
    "s_waitcnt lgkmcnt(6)\n"
    "v_mfma_f32_16x16x32_bf16 v[64:67], a[248:251], v[16:19], v[64:67]\n"
    "ds_read_b128 v[16:19], v10 offset:0\n"
    "v_mfma_f32_16x16x32_bf16 v[56:59], a[240:243], v[20:23], v[56:59]\n"
    "v_mfma_f32_16x16x32_bf16 v[60:63], a[244:247], v[20:23], v[60:63]\n"
    "s_waitcnt lgkmcnt(6)\n"
    "s_add_u32 m0, s77, s59\n"
    "s_add_u32 s77, s77, 8192\n"
    "buffer_load_dwordx4 v1, s[36:39], s76 offen sc1 lds\n"
    "v_mfma_f32_16x16x32_bf16 v[68:71], a[248:251], v[20:23], v[68:71]\n"
    "ds_read_b128 v[20:23], v10 offset:1024\n"
    "v_mfma_f32_16x16x32_bf16 v[48:51], a[252:255], v[24:27], v[48:51]\n"
    "v_mfma_f32_16x16x32_bf16 v[52:55], v[128:131], v[24:27], v[52:55]\n"
    "s_waitcnt lgkmcnt(6)\n"
    "buffer_load_dwordx4 v1, s[36:39], s76 offen offset:1024 sc1 lds\n"
    "s_add_u32 s76, s76, 16384\n"
    "v_mfma_f32_16x16x32_bf16 v[64:67], v[132:135], v[24:27], v[64:67]\n"
    "ds_read_b128 v[24:27], v10 offset:2048\n"
    "v_mfma_f32_16x16x32_bf16 v[56:59], a[252:255], v[28:31], v[56:59]\n"
    "v_mfma_f32_16x16x32_bf16 v[60:63], v[128:131], v[28:31], v[60:63]\n"
    "s_waitcnt lgkmcnt(6)\n"
    "v_mfma_f32_16x16x32_bf16 v[68:71], v[132:135], v[28:31], v[68:71]\n"
    "ds_read_b128 v[28:31], v10 offset:3072\n"
    "s_branch .LgWO_q132_%=\n"
    ".LgWO_dma92_%=:\n"
    "v_mfma_f32_16x16x32_bf16 v[64:67], a[188:191], v[40:43], v[64:67]\n"
    "ds_read_b128 v[40:43], v9 offset:38912\n"
    "v_mfma_f32_16x16x32_bf16 v[56:59], a[180:183], v[44:47], v[56:59]\n"
    "s_add_u32 m0, s77, s59\n"
    "s_add_u32 s77, s77, 8192\n"
    "buffer_load_dwordx4 v1, s[36:39], s76 offen sc1 lds\n"
    "v_mfma_f32_16x16x32_bf16 v[60:63], a[184:187], v[44:47], v[60:63]\n"
    "s_waitcnt lgkmcnt(6)\n"
    "v_mfma_f32_16x16x32_bf16 v[68:71], a[188:191], v[44:47], v[68:71]\n"
    "ds_read_b128 v[44:47], v9 offset:39936\n"
    "v_mfma_f32_16x16x32_bf16 v[48:51], a[192:195], v[16:19], v[48:51]\n"
    "buffer_load_dwordx4 v1, s[36:39], s76 offen offset:1024 sc1 lds\n"
    "s_add_u32 s76, s76, 16384\n"
    "v_mfma_f32_16x16x32_bf16 v[52:55], a[196:199], v[16:19], v[52:55]\n"
    "s_waitcnt lgkmcnt(6)\n"
    "v_mfma_f32_16x16x32_bf16 v[64:67], a[200:203], v[16:19], v[64:67]\n"
    "ds_read_b128 v[16:19], v9 offset:40960\n"
    "v_mfma_f32_16x16x32_bf16 v[56:59], a[192:195], v[20:23], v[56:59]\n"
    "s_add_u32 m0, s77, s59\n"
    "s_add_u32 s77, s77, 8192\n"
    "buffer_load_dwordx4 v1, s[36:39], s76 offen sc1 lds\n"
    "v_mfma_f32_16x16x32_bf16 v[60:63], a[196:199], v[20:23], v[60:63]\n"
    "s_waitcnt lgkmcnt(6)\n"
    "v_mfma_f32_16x16x32_bf16 v[68:71], a[200:203], v[20:23], v[68:71]\n"
    "ds_read_b128 v[20:23], v9 offset:41984\n"
    "v_mfma_f32_16x16x32_bf16 v[48:51], a[204:207], v[24:27], v[48:51]\n"
    "v_mfma_f32_16x16x32_bf16 v[52:55], a[208:211], v[24:27], v[52:55]\n"
    "s_waitcnt lgkmcnt(6)\n"
    "buffer_load_dwordx4 v1, s[36:39], s76 offen offset:1024 sc1 lds\n"
    "s_add_u32 s76, s76, 16384\n"
    "v_mfma_f32_16x16x32_bf16 v[64:67], a[212:215], v[24:27], v[64:67]\n"
    "ds_read_b128 v[24:27], v9 offset:43008\n"
    "v_mfma_f32_16x16x32_bf16 v[56:59], a[204:207], v[28:31], v[56:59]\n"
    "v_mfma_f32_16x16x32_bf16 v[60:63], a[208:211], v[28:31], v[60:63]\n"
    "s_waitcnt lgkmcnt(6)\n"
    "s_add_u32 m0, s77, s59\n"
    "s_add_u32 s77, s77, 8192\n"
    "buffer_load_dwordx4 v1, s[36:39], s76 offen sc1 lds\n"
    "v_mfma_f32_16x16x32_bf16 v[68:71], a[212:215], v[28:31], v[68:71]\n"
    "ds_read_b128 v[28:31], v9 offset:44032\n"
    "v_mfma_f32_16x16x32_bf16 v[48:51], a[216:219], v[32:35], v[48:51]\n"
    "v_mfma_f32_16x16x32_bf16 v[52:55], a[220:223], v[32:35], v[52:55]\n"
    "s_waitcnt lgkmcnt(6)\n"
    "v_mfma_f32_16x16x32_bf16 v[64:67], a[224:227], v[32:35], v[64:67]\n"
    "ds_read_b128 v[32:35], v9 offset:45056\n"
    "v_mfma_f32_16x16x32_bf16 v[56:59], a[216:219], v[36:39], v[56:59]\n"
    "buffer_load_dwordx4 v1, s[36:39], s76 offen offset:1024 sc1 lds\n"
    "s_add_u32 s76, s76, 16384\n"
    "v_mfma_f32_16x16x32_bf16 v[60:63], a[220:223], v[36:39], v[60:63]\n"
    "s_waitcnt lgkmcnt(6)\n"
    "v_mfma_f32_16x16x32_bf16 v[68:71], a[224:227], v[36:39], v[68:71]\n"
    "ds_read_b128 v[36:39], v9 offset:46080\n"
    "v_mfma_f32_16x16x32_bf16 v[48:51], a[228:231], v[40:43], v[48:51]\n"
    "s_add_u32 m0, s77, s59\n"
    "s_add_u32 s77, s77, 8192\n"
    "buffer_load_dwordx4 v1, s[36:39], s76 offen sc1 lds\n"
    "v_mfma_f32_16x16x32_bf16 v[52:55], a[232:235], v[40:43], v[52:55]\n"
    "s_waitcnt lgkmcnt(6)\n"
    "v_mfma_f32_16x16x32_bf16 v[64:67], a[236:239], v[40:43], v[64:67]\n"
    "ds_read_b128 v[40:43], v9 offset:47104\n"
    "v_mfma_f32_16x16x32_bf16 v[56:59], a[228:231], v[44:47], v[56:59]\n"
    "v_mfma_f32_16x16x32_bf16 v[60:63], a[232:235], v[44:47], v[60:63]\n"
    "s_waitcnt lgkmcnt(6)\n"
    "buffer_load_dwordx4 v1, s[36:39], s76 offen offset:1024 sc1 lds\n"
    "s_add_u32 s76, s76, 16384\n"
    "v_mfma_f32_16x16x32_bf16 v[68:71], a[236:239], v[44:47], v[68:71]\n"
    "ds_read_b128 v[44:47], v9 offset:48128\n"
    "v_mfma_f32_16x16x32_bf16 v[48:51], a[240:243], v[16:19], v[48:51]\n"
    "v_mfma_f32_16x16x32_bf16 v[52:55], a[244:247], v[16:19], v[52:55]\n"
    "s_waitcnt lgkmcnt(6)\n"
    "s_add_u32 m0, s77, s59\n"
    "s_add_u32 s77, s77, 8192\n"
    "buffer_load_dwordx4 v1, s[36:39], s76 offen sc1 lds\n"
    "v_mfma_f32_16x16x32_bf16 v[64:67], a[248:251], v[16:19], v[64:67]\n"
    "ds_read_b128 v[16:19], v10 offset:0\n"
    "v_mfma_f32_16x16x32_bf16 v[56:59], a[240:243], v[20:23], v[56:59]\n"
    "v_mfma_f32_16x16x32_bf16 v[60:63], a[244:247], v[20:23], v[60:63]\n"
    "s_waitcnt lgkmcnt(6)\n"
    "buffer_load_dwordx4 v1, s[36:39], s76 offen offset:1024 sc1 lds\n"
    "s_add_u32 s76, s76, 16384\n"
    "v_mfma_f32_16x16x32_bf16 v[68:71], a[248:251], v[20:23], v[68:71]\n"
    "ds_read_b128 v[20:23], v10 offset:1024\n"
    "v_mfma_f32_16x16x32_bf16 v[48:51], a[252:255], v[24:27], v[48:51]\n"
    "v_mfma_f32_16x16x32_bf16 v[52:55], v[128:131], v[24:27], v[52:55]\n"
    "s_waitcnt lgkmcnt(6)\n"
    "v_mfma_f32_16x16x32_bf16 v[64:67], v[132:135], v[24:27], v[64:67]\n"
    "ds_read_b128 v[24:27], v10 offset:2048\n"
    "v_mfma_f32_16x16x32_bf16 v[56:59], a[252:255], v[28:31], v[56:59]\n"
    "s_add_u32 m0, s77, s59\n"
    "s_add_u32 s77, s77, 8192\n"
    "buffer_load_dwordx4 v1, s[36:39], s76 offen sc1 lds\n"
    "v_mfma_f32_16x16x32_bf16 v[60:63], v[128:131], v[28:31], v[60:63]\n"
    "s_waitcnt lgkmcnt(6)\n"
    "v_mfma_f32_16x16x32_bf16 v[68:71], v[132:135], v[28:31], v[68:71]\n"
    "ds_read_b128 v[28:31], v10 offset:3072\n"
    "v_mfma_f32_16x16x32_bf16 v[48:51], v[136:139], v[32:35], v[48:51]\n"
    "buffer_load_dwordx4 v1, s[36:39], s76 offen offset:1024 sc1 lds\n"
    "s_add_u32 s76, s76, 16384\n"
    "v_mfma_f32_16x16x32_bf16 v[52:55], v[140:143], v[32:35], v[52:55]\n"
    "s_waitcnt lgkmcnt(6)\n"
    "v_mfma_f32_16x16x32_bf16 v[64:67], v[144:147], v[32:35], v[64:67]\n"
    "ds_read_b128 v[32:35], v10 offset:4096\n"
    "v_mfma_f32_16x16x32_bf16 v[56:59], v[136:139], v[36:39], v[56:59]\n"
    "v_mfma_f32_16x16x32_bf16 v[60:63], v[140:143], v[36:39], v[60:63]\n"
    "s_waitcnt lgkmcnt(6)\n"
    "s_add_u32 m0, s77, s59\n"
    "s_add_u32 s77, s77, 8192\n"
    "buffer_load_dwordx4 v1, s[36:39], s76 offen sc1 lds\n"
    "v_mfma_f32_16x16x32_bf16 v[68:71], v[144:147], v[36:39], v[68:71]\n"
    "ds_read_b128 v[36:39], v10 offset:5120\n"
    "v_mfma_f32_16x16x32_bf16 v[48:51], v[148:151], v[40:43], v[48:51]\n"
    "v_mfma_f32_16x16x32_bf16 v[52:55], v[152:155], v[40:43], v[52:55]\n"
    "s_waitcnt lgkmcnt(6)\n"
    "buffer_load_dwordx4 v1, s[36:39], s76 offen offset:1024 sc1 lds\n"
    "s_add_u32 s76, s76, 16384\n"
    "v_mfma_f32_16x16x32_bf16 v[64:67], v[156:159], v[40:43], v[64:67]\n"
    "ds_read_b128 v[40:43], v10 offset:6144\n"
    "v_mfma_f32_16x16x32_bf16 v[56:59], v[148:151], v[44:47], v[56:59]\n"
    "v_mfma_f32_16x16x32_bf16 v[60:63], v[152:155], v[44:47], v[60:63]\n"
    "s_waitcnt lgkmcnt(6)\n"
    "v_mfma_f32_16x16x32_bf16 v[68:71], v[156:159], v[44:47], v[68:71]\n"
    "ds_read_b128 v[44:47], v10 offset:7168\n"
    "v_mfma_f32_16x16x32_bf16 v[48:51], v[160:163], v[16:19], v[48:51]\n"
    "s_add_u32 m0, s77, s59\n"
    "s_add_u32 s77, s77, 8192\n"
    "buffer_load_dwordx4 v1, s[36:39], s76 offen sc1 lds\n"
    "v_mfma_f32_16x16x32_bf16 v[52:55], v[164:167], v[16:19], v[52:55]\n"
    "s_waitcnt lgkmcnt(6)\n"
    "v_mfma_f32_16x16x32_bf16 v[64:67], v[168:171], v[16:19], v[64:67]\n"
    "ds_read_b128 v[16:19], v10 offset:8192\n"
    "v_mfma_f32_16x16x32_bf16 v[56:59], v[160:163], v[20:23], v[56:59]\n"
    "buffer_load_dwordx4 v1, s[36:39], s76 offen offset:1024 sc1 lds\n"
    "s_add_u32 s76, s76, 16384\n"
    "v_mfma_f32_16x16x32_bf16 v[60:63], v[164:167], v[20:23], v[60:63]\n"
    "s_waitcnt lgkmcnt(6)\n"
    "v_mfma_f32_16x16x32_bf16 v[68:71], v[168:171], v[20:23], v[68:71]\n"
    "ds_read_b128 v[20:23], v10 offset:9216\n"
    "v_mfma_f32_16x16x32_bf16 v[48:51], v[172:175], v[24:27], v[48:51]\n"
    "v_mfma_f32_16x16x32_bf16 v[52:55], v[176:179], v[24:27], v[52:55]\n"
    "s_waitcnt lgkmcnt(6)\n"
    "v_mfma_f32_16x16x32_bf16 v[64:67], v[180:183], v[24:27], v[64:67]\n"
    "ds_read_b128 v[24:27], v10 offset:10240\n"
    "v_mfma_f32_16x16x32_bf16 v[56:59], v[172:175], v[28:31], v[56:59]\n"
    "v_mfma_f32_16x16x32_bf16 v[60:63], v[176:179], v[28:31], v[60:63]\n"
    "s_waitcnt lgkmcnt(6)\n"
    "v_mfma_f32_16x16x32_bf16 v[68:71], v[180:183], v[28:31], v[68:71]\n"
    "ds_read_b128 v[28:31], v10 offset:11264\n"
    "v_mfma_f32_16x16x32_bf16 v[48:51], v[184:187], v[32:35], v[48:51]\n"
    "v_mfma_f32_16x16x32_bf16 v[52:55], v[188:191], v[32:35], v[52:55]\n"
    "s_waitcnt lgkmcnt(6)\n"
    "v_mfma_f32_16x16x32_bf16 v[64:67], v[192:195], v[32:35], v[64:67]\n"
    "ds_read_b128 v[32:35], v10 offset:12288\n"
    "v_mfma_f32_16x16x32_bf16 v[56:59], v[184:187], v[36:39], v[56:59]\n"
    "v_mfma_f32_16x16x32_bf16 v[60:63], v[188:191], v[36:39], v[60:63]\n"
    "s_waitcnt lgkmcnt(6)\n"
    "v_mfma_f32_16x16x32_bf16 v[68:71], v[192:195], v[36:39], v[68:71]\n"
    "ds_read_b128 v[36:39], v10 offset:13312\n"
    "v_mfma_f32_16x16x32_bf16 v[48:51], v[196:199], v[40:43], v[48:51]\n"
    "v_mfma_f32_16x16x32_bf16 v[52:55], v[200:203], v[40:43], v[52:55]\n"
    "s_waitcnt lgkmcnt(6)\n"
    "v_mfma_f32_16x16x32_bf16 v[64:67], v[204:207], v[40:43], v[64:67]\n"
    "ds_read_b128 v[40:43], v10 offset:14336\n"
    "v_mfma_f32_16x16x32_bf16 v[56:59], v[196:199], v[44:47], v[56:59]\n"
    "v_mfma_f32_16x16x32_bf16 v[60:63], v[200:203], v[44:47], v[60:63]\n"
    "s_waitcnt lgkmcnt(6)\n"
    "v_mfma_f32_16x16x32_bf16 v[68:71], v[204:207], v[44:47], v[68:71]\n"
    "ds_read_b128 v[44:47], v10 offset:15360\n"
    "v_mfma_f32_16x16x32_bf16 v[64:67], v[216:219], v[16:19], v[64:67]\n"
    "s_waitcnt lgkmcnt(6)\n"
    "ds_read_b128 v[16:19], v10 offset:8192\n"
    "v_mfma_f32_16x16x32_bf16 v[68:71], v[216:219], v[20:23], v[68:71]\n"
    "s_waitcnt lgkmcnt(6)\n"
    "ds_read_b128 v[20:23], v10 offset:10240\n"
    "v_mfma_f32_16x16x32_bf16 v[64:67], v[228:231], v[24:27], v[64:67]\n"
    "s_waitcnt lgkmcnt(6)\n"
    "ds_read_b128 v[24:27], v10 offset:12288\n"
    "v_mfma_f32_16x16x32_bf16 v[68:71], v[228:231], v[28:31], v[68:71]\n"
    "s_waitcnt lgkmcnt(6)\n"
    "ds_read_b128 v[28:31], v10 offset:14336\n"
    "v_mfma_f32_16x16x32_bf16 v[64:67], v[240:243], v[32:35], v[64:67]\n"
    "s_waitcnt lgkmcnt(6)\n"
    "ds_read_b128 v[32:35], v10 offset:9216\n"
    "v_mfma_f32_16x16x32_bf16 v[68:71], v[240:243], v[36:39], v[68:71]\n"
    "s_waitcnt lgkmcnt(6)\n"
    "ds_read_b128 v[36:39], v10 offset:11264\n"
    "v_mfma_f32_16x16x32_bf16 v[64:67], v[252:255], v[40:43], v[64:67]\n"
    "s_waitcnt lgkmcnt(6)\n"
    "ds_read_b128 v[40:43], v10 offset:13312\n"
    "v_mfma_f32_16x16x32_bf16 v[68:71], v[252:255], v[44:47], v[68:71]\n"
    "s_waitcnt lgkmcnt(6)\n"
    "ds_read_b128 v[44:47], v10 offset:15360\n"
    "v_mfma_f32_16x16x32_bf16 v[48:51], v[208:211], v[16:19], v[48:51]\n"
    "v_mfma_f32_16x16x32_bf16 v[52:55], v[212:215], v[16:19], v[52:55]\n"
    "s_waitcnt lgkmcnt(6)\n"
    "v_cvt_pk_bf16_f32 v64, v64, v65\n"
    "v_cvt_pk_bf16_f32 v65, v66, v67\n"
    "v_mfma_f32_16x16x32_bf16 v[48:51], v[220:223], v[20:23], v[48:51]\n"
    "s_waitcnt lgkmcnt(5)\n"
    "v_cvt_pk_bf16_f32 v66, v68, v69\n"
    "v_cvt_pk_bf16_f32 v67, v70, v71\n"
    "v_mfma_f32_16x16x32_bf16 v[52:55], v[224:227], v[20:23], v[52:55]\n"
    "s_nop 0\n"
    "v_mfma_f32_16x16x32_bf16 v[48:51], v[232:235], v[24:27], v[48:51]\n"
    "v_mfma_f32_16x16x32_bf16 v[52:55], v[236:239], v[24:27], v[52:55]\n"
    "s_waitcnt lgkmcnt(4)\n"
    "v_mfma_f32_16x16x32_bf16 v[48:51], v[244:247], v[28:31], v[48:51]\n"
    "s_waitcnt lgkmcnt(3)\n"
    "v_permlane16_swap_b32 v64, v66\n"
    "v_mfma_f32_16x16x32_bf16 v[52:55], v[248:251], v[28:31], v[52:55]\n"
    "v_mfma_f32_16x16x32_bf16 v[56:59], v[208:211], v[32:35], v[56:59]\n"
    "s_waitcnt lgkmcnt(2)\n"
    "v_permlane16_swap_b32 v65, v67\n"
    "v_mfma_f32_16x16x32_bf16 v[60:63], v[212:215], v[32:35], v[60:63]\n"
    "v_cvt_pk_bf16_f32 v48, v48, v49\n"
    "v_mfma_f32_16x16x32_bf16 v[56:59], v[220:223], v[36:39], v[56:59]\n"
    "s_waitcnt lgkmcnt(1)\n"
    "buffer_store_dwordx4 v[64:67], v4, s[36:39], s73 offen sc1\n"
    "v_cvt_pk_bf16_f32 v49, v50, v51\n"
    "v_mfma_f32_16x16x32_bf16 v[60:63], v[224:227], v[36:39], v[60:63]\n"
    "v_cvt_pk_bf16_f32 v50, v52, v53\n"
    "v_mfma_f32_16x16x32_bf16 v[56:59], v[232:235], v[40:43], v[56:59]\n"
    "s_waitcnt lgkmcnt(0)\n"
    "v_cvt_pk_bf16_f32 v51, v54, v55\n"
    "v_mfma_f32_16x16x32_bf16 v[60:63], v[236:239], v[40:43], v[60:63]\n"
    "v_mfma_f32_16x16x32_bf16 v[56:59], v[244:247], v[44:47], v[56:59]\n"
    "v_mfma_f32_16x16x32_bf16 v[60:63], v[248:251], v[44:47], v[60:63]\n"
    "buffer_store_dwordx4 v[48:51], v15, s[36:39], s73 offen sc1\n"
    "s_nop 2\n"
    "v_cvt_pk_bf16_f32 v56, v56, v57\n"
    "v_cvt_pk_bf16_f32 v57, v58, v59\n"
    "s_nop 1\n"
    "v_cvt_pk_bf16_f32 v58, v60, v61\n"
    "v_cvt_pk_bf16_f32 v59, v62, v63\n"
    "buffer_store_dwordx4 v[56:59], v15, s[36:39], s73 offen offset:1024 sc1\n"
    "v_mov_b32 v12, s68\n"
    "s_mov_b32 s68, s71\n"
    "s_waitcnt vmcnt(3)\n"
    "s_branch .LgWO_bar_%=\n"
    ".LgWO_dma112_%=:\n"
    "v_mfma_f32_16x16x32_bf16 v[60:63], a[220:223], v[36:39], v[60:63]\n"
    "s_waitcnt lgkmcnt(6)\n"
    "s_add_u32 m0, s77, s59\n"
    "s_add_u32 s77, s77, 8192\n"
    "buffer_load_dwordx4 v1, s[36:39], s76 offen sc1 lds\n"
    "v_mfma_f32_16x16x32_bf16 v[68:71], a[224:227], v[36:39], v[68:71]\n"
    "ds_read_b128 v[36:39], v9 offset:46080\n"
    "v_mfma_f32_16x16x32_bf16 v[48:51], a[228:231], v[40:43], v[48:51]\n"
    "v_mfma_f32_16x16x32_bf16 v[52:55], a[232:235], v[40:43], v[52:55]\n"
    "s_waitcnt lgkmcnt(6)\n"
    "buffer_load_dwordx4 v1, s[36:39], s76 offen offset:1024 sc1 lds\n"
    "s_add_u32 s76, s76, 16384\n"
    "v_mfma_f32_16x16x32_bf16 v[64:67], a[236:239], v[40:43], v[64:67]\n"
    "ds_read_b128 v[40:43], v9 offset:47104\n"
    "v_mfma_f32_16x16x32_bf16 v[56:59], a[228:231], v[44:47], v[56:59]\n"
    "v_mfma_f32_16x16x32_bf16 v[60:63], a[232:235], v[44:47], v[60:63]\n"
    "s_waitcnt lgkmcnt(6)\n"
    "s_add_u32 m0, s77, s59\n"
    "s_add_u32 s77, s77, 8192\n"
    "buffer_load_dwordx4 v1, s[36:39], s76 offen sc1 lds\n"
    "v_mfma_f32_16x16x32_bf16 v[68:71], a[236:239], v[44:47], v[68:71]\n"
    "ds_read_b128 v[44:47], v9 offset:48128\n"
    "v_mfma_f32_16x16x32_bf16 v[48:51], a[240:243], v[16:19], v[48:51]\n"
    "v_mfma_f32_16x16x32_bf16 v[52:55], a[244:247], v[16:19], v[52:55]\n"
    "s_waitcnt lgkmcnt(6)\n"
    "buffer_load_dwordx4 v1, s[36:39], s76 offen offset:1024 sc1 lds\n"
    "s_add_u32 s76, s76, 16384\n"
    "v_mfma_f32_16x16x32_bf16 v[64:67], a[248:251], v[16:19], v[64:67]\n"
    "ds_read_b128 v[16:19], v10 offset:0\n"
    "v_mfma_f32_16x16x32_bf16 v[56:59], a[240:243], v[20:23], v[56:59]\n"
    "v_mfma_f32_16x16x32_bf16 v[60:63], a[244:247], v[20:23], v[60:63]\n"
    "s_waitcnt lgkmcnt(6)\n"
    "v_mfma_f32_16x16x32_bf16 v[68:71], a[248:251], v[20:23], v[68:71]\n"
    "ds_read_b128 v[20:23], v10 offset:1024\n"
    "v_mfma_f32_16x16x32_bf16 v[48:51], a[252:255], v[24:27], v[48:51]\n"
    "s_add_u32 m0, s77, s59\n"
    "s_add_u32 s77, s77, 8192\n"
    "buffer_load_dwordx4 v1, s[36:39], s76 offen sc1 lds\n"
    "v_mfma_f32_16x16x32_bf16 v[52:55], v[128:131], v[24:27], v[52:55]\n"
    "s_waitcnt lgkmcnt(6)\n"
    "v_mfma_f32_16x16x32_bf16 v[64:67], v[132:135], v[24:27], v[64:67]\n"
    "ds_read_b128 v[24:27], v10 offset:2048\n"
    "v_mfma_f32_16x16x32_bf16 v[56:59], a[252:255], v[28:31], v[56:59]\n"
    "buffer_load_dwordx4 v1, s[36:39], s76 offen offset:1024 sc1 lds\n"
    "s_add_u32 s76, s76, 16384\n"
    "v_mfma_f32_16x16x32_bf16 v[60:63], v[128:131], v[28:31], v[60:63]\n"
    "s_waitcnt lgkmcnt(6)\n"
    "v_mfma_f32_16x16x32_bf16 v[68:71], v[132:135], v[28:31], v[68:71]\n"
    "ds_read_b128 v[28:31], v10 offset:3072\n"
    "v_mfma_f32_16x16x32_bf16 v[48:51], v[136:139], v[32:35], v[48:51]\n"
    "s_add_u32 m0, s77, s59\n"
    "s_add_u32 s77, s77, 8192\n"
    "buffer_load_dwordx4 v1, s[36:39], s76 offen sc1 lds\n"
    "v_mfma_f32_16x16x32_bf16 v[52:55], v[140:143], v[32:35], v[52:55]\n"
    "s_waitcnt lgkmcnt(6)\n"
    "v_mfma_f32_16x16x32_bf16 v[64:67], v[144:147], v[32:35], v[64:67]\n"
    "ds_read_b128 v[32:35], v10 offset:4096\n"
    "v_mfma_f32_16x16x32_bf16 v[56:59], v[136:139], v[36:39], v[56:59]\n"
    "v_mfma_f32_16x16x32_bf16 v[60:63], v[140:143], v[36:39], v[60:63]\n"
    "s_waitcnt lgkmcnt(6)\n"
    "buffer_load_dwordx4 v1, s[36:39], s76 offen offset:1024 sc1 lds\n"
    "s_add_u32 s76, s76, 16384\n"
    "v_mfma_f32_16x16x32_bf16 v[68:71], v[144:147], v[36:39], v[68:71]\n"
    "ds_read_b128 v[36:39], v10 offset:5120\n"
    "v_mfma_f32_16x16x32_bf16 v[48:51], v[148:151], v[40:43], v[48:51]\n"
    "v_mfma_f32_16x16x32_bf16 v[52:55], v[152:155], v[40:43], v[52:55]\n"
    "s_waitcnt lgkmcnt(6)\n"
    "s_add_u32 m0, s77, s59\n"
    "s_add_u32 s77, s77, 8192\n"
    "buffer_load_dwordx4 v1, s[36:39], s76 offen sc1 lds\n"
    "v_mfma_f32_16x16x32_bf16 v[64:67], v[156:159], v[40:43], v[64:67]\n"
    "ds_read_b128 v[40:43], v10 offset:6144\n"
    "v_mfma_f32_16x16x32_bf16 v[56:59], v[148:151], v[44:47], v[56:59]\n"
    "v_mfma_f32_16x16x32_bf16 v[60:63], v[152:155], v[44:47], v[60:63]\n"
    "s_waitcnt lgkmcnt(6)\n"
    "buffer_load_dwordx4 v1, s[36:39], s76 offen offset:1024 sc1 lds\n"
    "s_add_u32 s76, s76, 16384\n"
    "v_mfma_f32_16x16x32_bf16 v[68:71], v[156:159], v[44:47], v[68:71]\n"
    "ds_read_b128 v[44:47], v10 offset:7168\n"
    "v_mfma_f32_16x16x32_bf16 v[48:51], v[160:163], v[16:19], v[48:51]\n"
    "v_mfma_f32_16x16x32_bf16 v[52:55], v[164:167], v[16:19], v[52:55]\n"
    "s_waitcnt lgkmcnt(6)\n"
    "v_mfma_f32_16x16x32_bf16 v[64:67], v[168:171], v[16:19], v[64:67]\n"
    "ds_read_b128 v[16:19], v10 offset:8192\n"
    "v_mfma_f32_16x16x32_bf16 v[56:59], v[160:163], v[20:23], v[56:59]\n"
    "s_add_u32 m0, s77, s59\n"
    "s_add_u32 s77, s77, 8192\n"
    "buffer_load_dwordx4 v1, s[36:39], s76 offen sc1 lds\n"
    "v_mfma_f32_16x16x32_bf16 v[60:63], v[164:167], v[20:23], v[60:63]\n"
    "s_waitcnt lgkmcnt(6)\n"
    "v_mfma_f32_16x16x32_bf16 v[68:71], v[168:171], v[20:23], v[68:71]\n"
    "ds_read_b128 v[20:23], v10 offset:9216\n"
    "v_mfma_f32_16x16x32_bf16 v[48:51], v[172:175], v[24:27], v[48:51]\n"
    "buffer_load_dwordx4 v1, s[36:39], s76 offen offset:1024 sc1 lds\n"
    "s_add_u32 s76, s76, 16384\n"
    "v_mfma_f32_16x16x32_bf16 v[52:55], v[176:179], v[24:27], v[52:55]\n"
    "s_waitcnt lgkmcnt(6)\n"
    "v_mfma_f32_16x16x32_bf16 v[64:67], v[180:183], v[24:27], v[64:67]\n"
    "ds_read_b128 v[24:27], v10 offset:10240\n"
    "v_mfma_f32_16x16x32_bf16 v[56:59], v[172:175], v[28:31], v[56:59]\n"
    "s_add_u32 m0, s77, s59\n"
    "s_add_u32 s77, s77, 8192\n"
    "buffer_load_dwordx4 v1, s[36:39], s76 offen sc1 lds\n"
    "v_mfma_f32_16x16x32_bf16 v[60:63], v[176:179], v[28:31], v[60:63]\n"
    "s_waitcnt lgkmcnt(6)\n"
    "v_mfma_f32_16x16x32_bf16 v[68:71], v[180:183], v[28:31], v[68:71]\n"
    "ds_read_b128 v[28:31], v10 offset:11264\n"
    "v_mfma_f32_16x16x32_bf16 v[48:51], v[184:187], v[32:35], v[48:51]\n"
    "v_mfma_f32_16x16x32_bf16 v[52:55], v[188:191], v[32:35], v[52:55]\n"
    "s_waitcnt lgkmcnt(6)\n"
    "buffer_load_dwordx4 v1, s[36:39], s76 offen offset:1024 sc1 lds\n"
    "s_add_u32 s76, s76, 16384\n"
    "v_mfma_f32_16x16x32_bf16 v[64:67], v[192:195], v[32:35], v[64:67]\n"
    "ds_read_b128 v[32:35], v10 offset:12288\n"
    "v_mfma_f32_16x16x32_bf16 v[56:59], v[184:187], v[36:39], v[56:59]\n"
    "v_mfma_f32_16x16x32_bf16 v[60:63], v[188:191], v[36:39], v[60:63]\n"
    "s_waitcnt lgkmcnt(6)\n"
    "s_add_u32 m0, s77, s59\n"
    "s_add_u32 s77, s77, 8192\n"
    "buffer_load_dwordx4 v1, s[36:39], s76 offen sc1 lds\n"
    "v_mfma_f32_16x16x32_bf16 v[68:71], v[192:195], v[36:39], v[68:71]\n"
    "ds_read_b128 v[36:39], v10 offset:13312\n"
    "v_mfma_f32_16x16x32_bf16 v[48:51], v[196:199], v[40:43], v[48:51]\n"
    "v_mfma_f32_16x16x32_bf16 v[52:55], v[200:203], v[40:43], v[52:55]\n"
    "s_waitcnt lgkmcnt(6)\n"
    "buffer_load_dwordx4 v1, s[36:39], s76 offen offset:1024 sc1 lds\n"
    "s_add_u32 s76, s76, 16384\n"
    "v_mfma_f32_16x16x32_bf16 v[64:67], v[204:207], v[40:43], v[64:67]\n"
    "ds_read_b128 v[40:43], v10 offset:14336\n"
    "v_mfma_f32_16x16x32_bf16 v[56:59], v[196:199], v[44:47], v[56:59]\n"
    "v_mfma_f32_16x16x32_bf16 v[60:63], v[200:203], v[44:47], v[60:63]\n"
    "s_waitcnt lgkmcnt(6)\n"
    "v_mfma_f32_16x16x32_bf16 v[68:71], v[204:207], v[44:47], v[68:71]\n"
    "ds_read_b128 v[44:47], v10 offset:15360\n"
    "v_mfma_f32_16x16x32_bf16 v[64:67], v[216:219], v[16:19], v[64:67]\n"
    "s_waitcnt lgkmcnt(6)\n"
    "ds_read_b128 v[16:19], v10 offset:8192\n"
    "v_mfma_f32_16x16x32_bf16 v[68:71], v[216:219], v[20:23], v[68:71]\n"
    "s_waitcnt lgkmcnt(6)\n"
    "ds_read_b128 v[20:23], v10 offset:10240\n"
    "v_mfma_f32_16x16x32_bf16 v[64:67], v[228:231], v[24:27], v[64:67]\n"
    "s_waitcnt lgkmcnt(6)\n"
    "ds_read_b128 v[24:27], v10 offset:12288\n"
    "v_mfma_f32_16x16x32_bf16 v[68:71], v[228:231], v[28:31], v[68:71]\n"
    "s_waitcnt lgkmcnt(6)\n"
    "ds_read_b128 v[28:31], v10 offset:14336\n"
    "v_mfma_f32_16x16x32_bf16 v[64:67], v[240:243], v[32:35], v[64:67]\n"
    "s_waitcnt lgkmcnt(6)\n"
    "ds_read_b128 v[32:35], v10 offset:9216\n"
    "v_mfma_f32_16x16x32_bf16 v[68:71], v[240:243], v[36:39], v[68:71]\n"
    "s_waitcnt lgkmcnt(6)\n"
    "ds_read_b128 v[36:39], v10 offset:11264\n"
    "v_mfma_f32_16x16x32_bf16 v[64:67], v[252:255], v[40:43], v[64:67]\n"
    "s_waitcnt lgkmcnt(6)\n"
    "ds_read_b128 v[40:43], v10 offset:13312\n"
    "v_mfma_f32_16x16x32_bf16 v[68:71], v[252:255], v[44:47], v[68:71]\n"
    "s_waitcnt lgkmcnt(6)\n"
    "ds_read_b128 v[44:47], v10 offset:15360\n"
    "v_mfma_f32_16x16x32_bf16 v[48:51], v[208:211], v[16:19], v[48:51]\n"
    "v_mfma_f32_16x16x32_bf16 v[52:55], v[212:215], v[16:19], v[52:55]\n"
    "s_waitcnt lgkmcnt(6)\n"
    "v_cvt_pk_bf16_f32 v64, v64, v65\n"
    "v_cvt_pk_bf16_f32 v65, v66, v67\n"
    "v_mfma_f32_16x16x32_bf16 v[48:51], v[220:223], v[20:23], v[48:51]\n"
    "s_waitcnt lgkmcnt(5)\n"
    "v_cvt_pk_bf16_f32 v66, v68, v69\n"
    "v_cvt_pk_bf16_f32 v67, v70, v71\n"
    "v_mfma_f32_16x16x32_bf16 v[52:55], v[224:227], v[20:23], v[52:55]\n"
    "s_nop 0\n"
    "v_mfma_f32_16x16x32_bf16 v[48:51], v[232:235], v[24:27], v[48:51]\n"
    "v_mfma_f32_16x16x32_bf16 v[52:55], v[236:239], v[24:27], v[52:55]\n"
    "s_waitcnt lgkmcnt(4)\n"
    "v_mfma_f32_16x16x32_bf16 v[48:51], v[244:247], v[28:31], v[48:51]\n"
    "s_waitcnt lgkmcnt(3)\n"
    "v_permlane16_swap_b32 v64, v66\n"
    "v_mfma_f32_16x16x32_bf16 v[52:55], v[248:251], v[28:31], v[52:55]\n"
    "v_mfma_f32_16x16x32_bf16 v[56:59], v[208:211], v[32:35], v[56:59]\n"
    "s_waitcnt lgkmcnt(2)\n"
    "v_permlane16_swap_b32 v65, v67\n"
    "v_mfma_f32_16x16x32_bf16 v[60:63], v[212:215], v[32:35], v[60:63]\n"
    "v_cvt_pk_bf16_f32 v48, v48, v49\n"
    "v_mfma_f32_16x16x32_bf16 v[56:59], v[220:223], v[36:39], v[56:59]\n"
    "s_waitcnt lgkmcnt(1)\n"
    "buffer_store_dwordx4 v[64:67], v4, s[36:39], s73 offen sc1\n"
    "v_cvt_pk_bf16_f32 v49, v50, v51\n"
    "v_mfma_f32_16x16x32_bf16 v[60:63], v[224:227], v[36:39], v[60:63]\n"
    "v_cvt_pk_bf16_f32 v50, v52, v53\n"
    "v_mfma_f32_16x16x32_bf16 v[56:59], v[232:235], v[40:43], v[56:59]\n"
    "s_waitcnt lgkmcnt(0)\n"
    "v_cvt_pk_bf16_f32 v51, v54, v55\n"
    "v_mfma_f32_16x16x32_bf16 v[60:63], v[236:239], v[40:43], v[60:63]\n"
    "v_mfma_f32_16x16x32_bf16 v[56:59], v[244:247], v[44:47], v[56:59]\n"
    "v_mfma_f32_16x16x32_bf16 v[60:63], v[248:251], v[44:47], v[60:63]\n"
    "buffer_store_dwordx4 v[48:51], v15, s[36:39], s73 offen sc1\n"
    "s_nop 2\n"
    "v_cvt_pk_bf16_f32 v56, v56, v57\n"
    "v_cvt_pk_bf16_f32 v57, v58, v59\n"
    "s_nop 1\n"
    "v_cvt_pk_bf16_f32 v58, v60, v61\n"
    "v_cvt_pk_bf16_f32 v59, v62, v63\n"
    "buffer_store_dwordx4 v[56:59], v15, s[36:39], s73 offen offset:1024 sc1\n"
    "v_mov_b32 v12, s68\n"
    "s_mov_b32 s68, s71\n"
    "s_waitcnt vmcnt(3)\n"
    "s_branch .LgWO_bar_%=\n"
    ".LgWO_dma132_%=:\n"
    "v_mfma_f32_16x16x32_bf16 v[48:51], v[136:139], v[32:35], v[48:51]\n"
    "s_add_u32 m0, s77, s59\n"
    "s_add_u32 s77, s77, 8192\n"
    "buffer_load_dwordx4 v1, s[36:39], s76 offen sc1 lds\n"
    "v_mfma_f32_16x16x32_bf16 v[52:55], v[140:143], v[32:35], v[52:55]\n"
    "s_waitcnt lgkmcnt(6)\n"
    "v_mfma_f32_16x16x32_bf16 v[64:67], v[144:147], v[32:35], v[64:67]\n"
    "ds_read_b128 v[32:35], v10 offset:4096\n"
    "v_mfma_f32_16x16x32_bf16 v[56:59], v[136:139], v[36:39], v[56:59]\n"
    "buffer_load_dwordx4 v1, s[36:39], s76 offen offset:1024 sc1 lds\n"
    "s_add_u32 s76, s76, 16384\n"
    "v_mfma_f32_16x16x32_bf16 v[60:63], v[140:143], v[36:39], v[60:63]\n"
    "s_waitcnt lgkmcnt(6)\n"
    "v_mfma_f32_16x16x32_bf16 v[68:71], v[144:147], v[36:39], v[68:71]\n"
    "ds_read_b128 v[36:39], v10 offset:5120\n"
    "v_mfma_f32_16x16x32_bf16 v[48:51], v[148:151], v[40:43], v[48:51]\n"
    "s_add_u32 m0, s77, s59\n"
    "s_add_u32 s77, s77, 8192\n"
    "buffer_load_dwordx4 v1, s[36:39], s76 offen sc1 lds\n"
    "v_mfma_f32_16x16x32_bf16 v[52:55], v[152:155], v[40:43], v[52:55]\n"
    "s_waitcnt lgkmcnt(6)\n"
    "v_mfma_f32_16x16x32_bf16 v[64:67], v[156:159], v[40:43], v[64:67]\n"
    "ds_read_b128 v[40:43], v10 offset:6144\n"
    "v_mfma_f32_16x16x32_bf16 v[56:59], v[148:151], v[44:47], v[56:59]\n"
    "buffer_load_dwordx4 v1, s[36:39], s76 offen offset:1024 sc1 lds\n"
    "s_add_u32 s76, s76, 16384\n"
    "v_mfma_f32_16x16x32_bf16 v[60:63], v[152:155], v[44:47], v[60:63]\n"
    "s_waitcnt lgkmcnt(6)\n"
    "v_mfma_f32_16x16x32_bf16 v[68:71], v[156:159], v[44:47], v[68:71]\n"
    "ds_read_b128 v[44:47], v10 offset:7168\n"
    "v_mfma_f32_16x16x32_bf16 v[48:51], v[160:163], v[16:19], v[48:51]\n"
    "s_add_u32 m0, s77, s59\n"
    "s_add_u32 s77, s77, 8192\n"
    "buffer_load_dwordx4 v1, s[36:39], s76 offen sc1 lds\n"
    "v_mfma_f32_16x16x32_bf16 v[52:55], v[164:167], v[16:19], v[52:55]\n"
    "s_waitcnt lgkmcnt(6)\n"
    "v_mfma_f32_16x16x32_bf16 v[64:67], v[168:171], v[16:19], v[64:67]\n"
    "ds_read_b128 v[16:19], v10 offset:8192\n"
    "v_mfma_f32_16x16x32_bf16 v[56:59], v[160:163], v[20:23], v[56:59]\n"
    "buffer_load_dwordx4 v1, s[36:39], s76 offen offset:1024 sc1 lds\n"
    "s_add_u32 s76, s76, 16384\n"
    "v_mfma_f32_16x16x32_bf16 v[60:63], v[164:167], v[20:23], v[60:63]\n"
    "s_waitcnt lgkmcnt(6)\n"
    "v_mfma_f32_16x16x32_bf16 v[68:71], v[168:171], v[20:23], v[68:71]\n"
    "ds_read_b128 v[20:23], v10 offset:9216\n"
    "v_mfma_f32_16x16x32_bf16 v[48:51], v[172:175], v[24:27], v[48:51]\n"
    "s_add_u32 m0, s77, s59\n"
    "s_add_u32 s77, s77, 8192\n"
    "buffer_load_dwordx4 v1, s[36:39], s76 offen sc1 lds\n"
    "v_mfma_f32_16x16x32_bf16 v[52:55], v[176:179], v[24:27], v[52:55]\n"
    "s_waitcnt lgkmcnt(6)\n"
    "v_mfma_f32_16x16x32_bf16 v[64:67], v[180:183], v[24:27], v[64:67]\n"
    "ds_read_b128 v[24:27], v10 offset:10240\n"
    "v_mfma_f32_16x16x32_bf16 v[56:59], v[172:175], v[28:31], v[56:59]\n"
    "buffer_load_dwordx4 v1, s[36:39], s76 offen offset:1024 sc1 lds\n"
    "s_add_u32 s76, s76, 16384\n"
    "v_mfma_f32_16x16x32_bf16 v[60:63], v[176:179], v[28:31], v[60:63]\n"
    "s_waitcnt lgkmcnt(6)\n"
    "v_mfma_f32_16x16x32_bf16 v[68:71], v[180:183], v[28:31], v[68:71]\n"
    "ds_read_b128 v[28:31], v10 offset:11264\n"
    "v_mfma_f32_16x16x32_bf16 v[48:51], v[184:187], v[32:35], v[48:51]\n"
    "s_add_u32 m0, s77, s59\n"
    "s_add_u32 s77, s77, 8192\n"
    "buffer_load_dwordx4 v1, s[36:39], s76 offen sc1 lds\n"
    "v_mfma_f32_16x16x32_bf16 v[52:55], v[188:191], v[32:35], v[52:55]\n"
    "s_waitcnt lgkmcnt(6)\n"
    "v_mfma_f32_16x16x32_bf16 v[64:67], v[192:195], v[32:35], v[64:67]\n"
    "ds_read_b128 v[32:35], v10 offset:12288\n"
    "v_mfma_f32_16x16x32_bf16 v[56:59], v[184:187], v[36:39], v[56:59]\n"
    "buffer_load_dwordx4 v1, s[36:39], s76 offen offset:1024 sc1 lds\n"
    "s_add_u32 s76, s76, 16384\n"
    "v_mfma_f32_16x16x32_bf16 v[60:63], v[188:191], v[36:39], v[60:63]\n"
    "s_waitcnt lgkmcnt(6)\n"
    "v_mfma_f32_16x16x32_bf16 v[68:71], v[192:195], v[36:39], v[68:71]\n"
    "ds_read_b128 v[36:39], v10 offset:13312\n"
    "v_mfma_f32_16x16x32_bf16 v[48:51], v[196:199], v[40:43], v[48:51]\n"
    "s_add_u32 m0, s77, s59\n"
    "s_add_u32 s77, s77, 8192\n"
    "buffer_load_dwordx4 v1, s[36:39], s76 offen sc1 lds\n"
    "v_mfma_f32_16x16x32_bf16 v[52:55], v[200:203], v[40:43], v[52:55]\n"
    "s_waitcnt lgkmcnt(6)\n"
    "v_mfma_f32_16x16x32_bf16 v[64:67], v[204:207], v[40:43], v[64:67]\n"
    "ds_read_b128 v[40:43], v10 offset:14336\n"
    "v_mfma_f32_16x16x32_bf16 v[56:59], v[196:199], v[44:47], v[56:59]\n"
    "buffer_load_dwordx4 v1, s[36:39], s76 offen offset:1024 sc1 lds\n"
    "s_add_u32 s76, s76, 16384\n"
    "v_mfma_f32_16x16x32_bf16 v[60:63], v[200:203], v[44:47], v[60:63]\n"
    "s_waitcnt lgkmcnt(6)\n"
    "v_mfma_f32_16x16x32_bf16 v[68:71], v[204:207], v[44:47], v[68:71]\n"
    "ds_read_b128 v[44:47], v10 offset:15360\n"
    "v_mfma_f32_16x16x32_bf16 v[64:67], v[216:219], v[16:19], v[64:67]\n"
    "s_waitcnt lgkmcnt(6)\n"
    "ds_read_b128 v[16:19], v10 offset:8192\n"
    "v_mfma_f32_16x16x32_bf16 v[68:71], v[216:219], v[20:23], v[68:71]\n"
    "s_waitcnt lgkmcnt(6)\n"
    "ds_read_b128 v[20:23], v10 offset:10240\n"
    "v_mfma_f32_16x16x32_bf16 v[64:67], v[228:231], v[24:27], v[64:67]\n"
    "s_waitcnt lgkmcnt(6)\n"
    "ds_read_b128 v[24:27], v10 offset:12288\n"
    "v_mfma_f32_16x16x32_bf16 v[68:71], v[228:231], v[28:31], v[68:71]\n"
    "s_waitcnt lgkmcnt(6)\n"
    "ds_read_b128 v[28:31], v10 offset:14336\n"
    "v_mfma_f32_16x16x32_bf16 v[64:67], v[240:243], v[32:35], v[64:67]\n"
    "s_waitcnt lgkmcnt(6)\n"
    "ds_read_b128 v[32:35], v10 offset:9216\n"
    "v_mfma_f32_16x16x32_bf16 v[68:71], v[240:243], v[36:39], v[68:71]\n"
    "s_waitcnt lgkmcnt(6)\n"
    "ds_read_b128 v[36:39], v10 offset:11264\n"
    "v_mfma_f32_16x16x32_bf16 v[64:67], v[252:255], v[40:43], v[64:67]\n"
    "s_waitcnt lgkmcnt(6)\n"
    "ds_read_b128 v[40:43], v10 offset:13312\n"
    "v_mfma_f32_16x16x32_bf16 v[68:71], v[252:255], v[44:47], v[68:71]\n"
    "s_waitcnt lgkmcnt(6)\n"
    "ds_read_b128 v[44:47], v10 offset:15360\n"
    "v_mfma_f32_16x16x32_bf16 v[48:51], v[208:211], v[16:19], v[48:51]\n"
    "s_add_u32 m0, s77, s59\n"
    "s_add_u32 s77, s77, 8192\n"
    "buffer_load_dwordx4 v1, s[36:39], s76 offen sc1 lds\n"
    "v_mfma_f32_16x16x32_bf16 v[52:55], v[212:215], v[16:19], v[52:55]\n"
    "s_waitcnt lgkmcnt(6)\n"
    "v_cvt_pk_bf16_f32 v64, v64, v65\n"
    "v_cvt_pk_bf16_f32 v65, v66, v67\n"
    "v_mfma_f32_16x16x32_bf16 v[48:51], v[220:223], v[20:23], v[48:51]\n"
    "s_waitcnt lgkmcnt(5)\n"
    "buffer_load_dwordx4 v1, s[36:39], s76 offen offset:1024 sc1 lds\n"
    "s_add_u32 s76, s76, 16384\n"
    "v_cvt_pk_bf16_f32 v66, v68, v69\n"
    "v_cvt_pk_bf16_f32 v67, v70, v71\n"
    "v_mfma_f32_16x16x32_bf16 v[52:55], v[224:227], v[20:23], v[52:55]\n"
    "s_nop 0\n"
    "v_mfma_f32_16x16x32_bf16 v[48:51], v[232:235], v[24:27], v[48:51]\n"
    "v_mfma_f32_16x16x32_bf16 v[52:55], v[236:239], v[24:27], v[52:55]\n"
    "s_waitcnt lgkmcnt(4)\n"
    "v_mfma_f32_16x16x32_bf16 v[48:51], v[244:247], v[28:31], v[48:51]\n"
    "s_waitcnt lgkmcnt(3)\n"
    "v_permlane16_swap_b32 v64, v66\n"
    "v_mfma_f32_16x16x32_bf16 v[52:55], v[248:251], v[28:31], v[52:55]\n"
    "v_mfma_f32_16x16x32_bf16 v[56:59], v[208:211], v[32:35], v[56:59]\n"
    "s_waitcnt lgkmcnt(2)\n"
    "s_add_u32 m0, s77, s59\n"
    "s_add_u32 s77, s77, 8192\n"
    "buffer_load_dwordx4 v1, s[36:39], s76 offen sc1 lds\n"
    "v_permlane16_swap_b32 v65, v67\n"
    "v_mfma_f32_16x16x32_bf16 v[60:63], v[212:215], v[32:35], v[60:63]\n"
    "v_cvt_pk_bf16_f32 v48, v48, v49\n"
    "v_mfma_f32_16x16x32_bf16 v[56:59], v[220:223], v[36:39], v[56:59]\n"
    "s_waitcnt lgkmcnt(1)\n"
    "buffer_store_dwordx4 v[64:67], v4, s[36:39], s73 offen sc1\n"
    "v_cvt_pk_bf16_f32 v49, v50, v51\n"
    "v_mfma_f32_16x16x32_bf16 v[60:63], v[224:227], v[36:39], v[60:63]\n"
    "v_cvt_pk_bf16_f32 v50, v52, v53\n"
    "v_mfma_f32_16x16x32_bf16 v[56:59], v[232:235], v[40:43], v[56:59]\n"
    "s_waitcnt lgkmcnt(0)\n"
    "buffer_load_dwordx4 v1, s[36:39], s76 offen offset:1024 sc1 lds\n"
    "s_add_u32 s76, s76, 16384\n"
    "v_cvt_pk_bf16_f32 v51, v54, v55\n"
    "v_mfma_f32_16x16x32_bf16 v[60:63], v[236:239], v[40:43], v[60:63]\n"
    "v_mfma_f32_16x16x32_bf16 v[56:59], v[244:247], v[44:47], v[56:59]\n"
    "v_mfma_f32_16x16x32_bf16 v[60:63], v[248:251], v[44:47], v[60:63]\n"
    "buffer_store_dwordx4 v[48:51], v15, s[36:39], s73 offen sc1\n"
    "s_nop 2\n"
    "v_cvt_pk_bf16_f32 v56, v56, v57\n"
    "v_cvt_pk_bf16_f32 v57, v58, v59\n"
    "s_nop 1\n"
    "v_cvt_pk_bf16_f32 v58, v60, v61\n"
    "v_cvt_pk_bf16_f32 v59, v62, v63\n"
    "buffer_store_dwordx4 v[56:59], v15, s[36:39], s73 offen offset:1024 sc1\n"
    "v_mov_b32 v12, s68\n"
    "s_mov_b32 s68, s71\n"
    "s_waitcnt vmcnt(2)\n"
    "s_branch .LgWO_bar_%=\n"
    ".LgWO_dma152_%=:\n"
    "v_mfma_f32_16x16x32_bf16 v[64:67], v[180:183], v[24:27], v[64:67]\n"
    "ds_read_b128 v[24:27], v10 offset:10240\n"
    "v_mfma_f32_16x16x32_bf16 v[56:59], v[172:175], v[28:31], v[56:59]\n"
    "s_add_u32 m0, s77, s59\n"
    "s_add_u32 s77, s77, 8192\n"
    "buffer_load_dwordx4 v1, s[36:39], s76 offen sc1 lds\n"
    "v_mfma_f32_16x16x32_bf16 v[60:63], v[176:179], v[28:31], v[60:63]\n"
    "s_waitcnt lgkmcnt(6)\n"
    "v_mfma_f32_16x16x32_bf16 v[68:71], v[180:183], v[28:31], v[68:71]\n"
    "ds_read_b128 v[28:31], v10 offset:11264\n"
    "v_mfma_f32_16x16x32_bf16 v[48:51], v[184:187], v[32:35], v[48:51]\n"
    "buffer_load_dwordx4 v1, s[36:39], s76 offen offset:1024 sc1 lds\n"
    "s_add_u32 s76, s76, 16384\n"
    "v_mfma_f32_16x16x32_bf16 v[52:55], v[188:191], v[32:35], v[52:55]\n"
    "s_waitcnt lgkmcnt(6)\n"
    "v_mfma_f32_16x16x32_bf16 v[64:67], v[192:195], v[32:35], v[64:67]\n"
    "ds_read_b128 v[32:35], v10 offset:12288\n"
    "v_mfma_f32_16x16x32_bf16 v[56:59], v[184:187], v[36:39], v[56:59]\n"
    "s_add_u32 m0, s77, s59\n"
    "s_add_u32 s77, s77, 8192\n"
    "buffer_load_dwordx4 v1, s[36:39], s76 offen sc1 lds\n"
    "v_mfma_f32_16x16x32_bf16 v[60:63], v[188:191], v[36:39], v[60:63]\n"
    "s_waitcnt lgkmcnt(6)\n"
    "v_mfma_f32_16x16x32_bf16 v[68:71], v[192:195], v[36:39], v[68:71]\n"
    "ds_read_b128 v[36:39], v10 offset:13312\n"
    "v_mfma_f32_16x16x32_bf16 v[48:51], v[196:199], v[40:43], v[48:51]\n"
    "buffer_load_dwordx4 v1, s[36:39], s76 offen offset:1024 sc1 lds\n"
    "s_add_u32 s76, s76, 16384\n"
    "v_mfma_f32_16x16x32_bf16 v[52:55], v[200:203], v[40:43], v[52:55]\n"
    "s_waitcnt lgkmcnt(6)\n"
    "v_mfma_f32_16x16x32_bf16 v[64:67], v[204:207], v[40:43], v[64:67]\n"
    "ds_read_b128 v[40:43], v10 offset:14336\n"
    "v_mfma_f32_16x16x32_bf16 v[56:59], v[196:199], v[44:47], v[56:59]\n"
    "v_mfma_f32_16x16x32_bf16 v[60:63], v[200:203], v[44:47], v[60:63]\n"
    "s_waitcnt lgkmcnt(6)\n"
    "s_add_u32 m0, s77, s59\n"
    "s_add_u32 s77, s77, 8192\n"
    "buffer_load_dwordx4 v1, s[36:39], s76 offen sc1 lds\n"
    "v_mfma_f32_16x16x32_bf16 v[68:71], v[204:207], v[44:47], v[68:71]\n"
    "ds_read_b128 v[44:47], v10 offset:15360\n"
    "v_mfma_f32_16x16x32_bf16 v[64:67], v[216:219], v[16:19], v[64:67]\n"
    "s_waitcnt lgkmcnt(6)\n"
    "ds_read_b128 v[16:19], v10 offset:8192\n"
    "v_mfma_f32_16x16x32_bf16 v[68:71], v[216:219], v[20:23], v[68:71]\n"
    "s_waitcnt lgkmcnt(6)\n"
    "ds_read_b128 v[20:23], v10 offset:10240\n"
    "v_mfma_f32_16x16x32_bf16 v[64:67], v[228:231], v[24:27], v[64:67]\n"
    "s_waitcnt lgkmcnt(6)\n"
    "ds_read_b128 v[24:27], v10 offset:12288\n"
    "v_mfma_f32_16x16x32_bf16 v[68:71], v[228:231], v[28:31], v[68:71]\n"
    "s_waitcnt lgkmcnt(6)\n"
    "ds_read_b128 v[28:31], v10 offset:14336\n"
    "v_mfma_f32_16x16x32_bf16 v[64:67], v[240:243], v[32:35], v[64:67]\n"
    "s_waitcnt lgkmcnt(6)\n"
    "ds_read_b128 v[32:35], v10 offset:9216\n"
    "v_mfma_f32_16x16x32_bf16 v[68:71], v[240:243], v[36:39], v[68:71]\n"
    "s_waitcnt lgkmcnt(6)\n"
    "ds_read_b128 v[36:39], v10 offset:11264\n"
    "v_mfma_f32_16x16x32_bf16 v[64:67], v[252:255], v[40:43], v[64:67]\n"
    "s_waitcnt lgkmcnt(6)\n"
    "ds_read_b128 v[40:43], v10 offset:13312\n"
    "v_mfma_f32_16x16x32_bf16 v[68:71], v[252:255], v[44:47], v[68:71]\n"
    "s_waitcnt lgkmcnt(6)\n"
    "ds_read_b128 v[44:47], v10 offset:15360\n"
    "v_mfma_f32_16x16x32_bf16 v[48:51], v[208:211], v[16:19], v[48:51]\n"
    "v_mfma_f32_16x16x32_bf16 v[52:55], v[212:215], v[16:19], v[52:55]\n"
    "s_waitcnt lgkmcnt(6)\n"
    "buffer_load_dwordx4 v1, s[36:39], s76 offen offset:1024 sc1 lds\n"
    "s_add_u32 s76, s76, 16384\n"
    "v_cvt_pk_bf16_f32 v64, v64, v65\n"
    "v_cvt_pk_bf16_f32 v65, v66, v67\n"
    "v_mfma_f32_16x16x32_bf16 v[48:51], v[220:223], v[20:23], v[48:51]\n"
    "s_waitcnt lgkmcnt(5)\n"
    "v_cvt_pk_bf16_f32 v66, v68, v69\n"
    "v_cvt_pk_bf16_f32 v67, v70, v71\n"
    "v_mfma_f32_16x16x32_bf16 v[52:55], v[224:227], v[20:23], v[52:55]\n"
    "s_nop 0\n"
    "v_mfma_f32_16x16x32_bf16 v[48:51], v[232:235], v[24:27], v[48:51]\n"
    "v_mfma_f32_16x16x32_bf16 v[52:55], v[236:239], v[24:27], v[52:55]\n"
    "s_waitcnt lgkmcnt(4)\n"
    "v_mfma_f32_16x16x32_bf16 v[48:51], v[244:247], v[28:31], v[48:51]\n"
    "s_waitcnt lgkmcnt(3)\n"
    "s_add_u32 m0, s77, s59\n"
    "s_add_u32 s77, s77, 8192\n"
    "buffer_load_dwordx4 v1, s[36:39], s76 offen sc1 lds\n"
    "v_permlane16_swap_b32 v64, v66\n"
    "v_mfma_f32_16x16x32_bf16 v[52:55], v[248:251], v[28:31], v[52:55]\n"
    "v_mfma_f32_16x16x32_bf16 v[56:59], v[208:211], v[32:35], v[56:59]\n"
    "s_waitcnt lgkmcnt(2)\n"
    "v_permlane16_swap_b32 v65, v67\n"
    "v_mfma_f32_16x16x32_bf16 v[60:63], v[212:215], v[32:35], v[60:63]\n"
    "v_cvt_pk_bf16_f32 v48, v48, v49\n"
    "v_mfma_f32_16x16x32_bf16 v[56:59], v[220:223], v[36:39], v[56:59]\n"
    "s_waitcnt lgkmcnt(1)\n"
    "buffer_store_dwordx4 v[64:67], v4, s[36:39], s73 offen sc1\n"
    "v_cvt_pk_bf16_f32 v49, v50, v51\n"
    "v_mfma_f32_16x16x32_bf16 v[60:63], v[224:227], v[36:39], v[60:63]\n"
    "v_cvt_pk_bf16_f32 v50, v52, v53\n"
    "v_mfma_f32_16x16x32_bf16 v[56:59], v[232:235], v[40:43], v[56:59]\n"
    "s_waitcnt lgkmcnt(0)\n"
    "buffer_load_dwordx4 v1, s[36:39], s76 offen offset:1024 sc1 lds\n"
    "s_add_u32 s76, s76, 16384\n"
    "v_cvt_pk_bf16_f32 v51, v54, v55\n"
    "v_mfma_f32_16x16x32_bf16 v[60:63], v[236:239], v[40:43], v[60:63]\n"
    "v_mfma_f32_16x16x32_bf16 v[56:59], v[244:247], v[44:47], v[56:59]\n"
    "v_mfma_f32_16x16x32_bf16 v[60:63], v[248:251], v[44:47], v[60:63]\n"
    "buffer_store_dwordx4 v[48:51], v15, s[36:39], s73 offen sc1\n"
    "s_nop 2\n"
    "v_cvt_pk_bf16_f32 v56, v56, v57\n"
    "v_cvt_pk_bf16_f32 v57, v58, v59\n"
    "s_nop 1\n"
    "v_cvt_pk_bf16_f32 v58, v60, v61\n"
    "v_cvt_pk_bf16_f32 v59, v62, v63\n"
    "buffer_store_dwordx4 v[56:59], v15, s[36:39], s73 offen offset:1024 sc1\n"
    "v_mov_b32 v12, s68\n"
    "s_mov_b32 s68, s71\n"
    "s_add_u32 m0, s77, s59\n"
    "s_add_u32 s77, s77, 8192\n"
    "buffer_load_dwordx4 v1, s[36:39], s76 offen sc1 lds\n"
    "buffer_load_dwordx4 v1, s[36:39], s76 offen offset:1024 sc1 lds\n"
    "s_add_u32 s76, s76, 16384\n"
    "s_add_u32 m0, s77, s59\n"
    "s_add_u32 s77, s77, 8192\n"
    "buffer_load_dwordx4 v1, s[36:39], s76 offen sc1 lds\n"
    "buffer_load_dwordx4 v1, s[36:39], s76 offen offset:1024 sc1 lds\n"
    "s_add_u32 s76, s76, 16384\n"
    "s_add_u32 m0, s77, s59\n"
    "s_add_u32 s77, s77, 8192\n"
    "buffer_load_dwordx4 v1, s[36:39], s76 offen sc1 lds\n"
    "buffer_load_dwordx4 v1, s[36:39], s76 offen offset:1024 sc1 lds\n"
    "s_add_u32 s76, s76, 16384\n"
    "s_add_u32 m0, s77, s59\n"
    "s_add_u32 s77, s77, 8192\n"
    "buffer_load_dwordx4 v1, s[36:39], s76 offen sc1 lds\n"
    "buffer_load_dwordx4 v1, s[36:39], s76 offen offset:1024 sc1 lds\n"
    "s_add_u32 s76, s76, 16384\n"
    "s_waitcnt vmcnt(8)\n"
    "buffer_atomic_smax v12, v14, s[36:39], 0 offen sc1\n"
    "s_waitcnt vmcnt(1)\n"
    "s_branch .LgWO_bar_%=\n"
    ".LgWO_dma172_%=:\n"
    "v_mfma_f32_16x16x32_bf16 v[64:67], v[240:243], v[32:35], v[64:67]\n"
    "s_waitcnt lgkmcnt(6)\n"
    "ds_read_b128 v[32:35], v10 offset:9216\n"
    "v_mfma_f32_16x16x32_bf16 v[68:71], v[240:243], v[36:39], v[68:71]\n"
    "s_waitcnt lgkmcnt(6)\n"
    "ds_read_b128 v[36:39], v10 offset:11264\n"
    "v_mfma_f32_16x16x32_bf16 v[64:67], v[252:255], v[40:43], v[64:67]\n"
    "s_waitcnt lgkmcnt(6)\n"
    "ds_read_b128 v[40:43], v10 offset:13312\n"
    "v_mfma_f32_16x16x32_bf16 v[68:71], v[252:255], v[44:47], v[68:71]\n"
    "s_waitcnt lgkmcnt(6)\n"
    "ds_read_b128 v[44:47], v10 offset:15360\n"
    "v_mfma_f32_16x16x32_bf16 v[48:51], v[208:211], v[16:19], v[48:51]\n"
    "s_add_u32 m0, s77, s59\n"
    "s_add_u32 s77, s77, 8192\n"
    "buffer_load_dwordx4 v1, s[36:39], s76 offen sc1 lds\n"
    "v_mfma_f32_16x16x32_bf16 v[52:55], v[212:215], v[16:19], v[52:55]\n"
    "s_waitcnt lgkmcnt(6)\n"
    "v_cvt_pk_bf16_f32 v64, v64, v65\n"
    "v_cvt_pk_bf16_f32 v65, v66, v67\n"
    "v_mfma_f32_16x16x32_bf16 v[48:51], v[220:223], v[20:23], v[48:51]\n"
    "s_waitcnt lgkmcnt(5)\n"
    "buffer_load_dwordx4 v1, s[36:39], s76 offen offset:1024 sc1 lds\n"
    "s_add_u32 s76, s76, 16384\n"
    "v_cvt_pk_bf16_f32 v66, v68, v69\n"
    "v_cvt_pk_bf16_f32 v67, v70, v71\n"
    "v_mfma_f32_16x16x32_bf16 v[52:55], v[224:227], v[20:23], v[52:55]\n"
    "s_nop 0\n"
    "v_mfma_f32_16x16x32_bf16 v[48:51], v[232:235], v[24:27], v[48:51]\n"
    "v_mfma_f32_16x16x32_bf16 v[52:55], v[236:239], v[24:27], v[52:55]\n"
    "s_waitcnt lgkmcnt(4)\n"
    "v_mfma_f32_16x16x32_bf16 v[48:51], v[244:247], v[28:31], v[48:51]\n"
    "s_waitcnt lgkmcnt(3)\n"
    "v_permlane16_swap_b32 v64, v66\n"
    "v_mfma_f32_16x16x32_bf16 v[52:55], v[248:251], v[28:31], v[52:55]\n"
    "v_mfma_f32_16x16x32_bf16 v[56:59], v[208:211], v[32:35], v[56:59]\n"
    "s_waitcnt lgkmcnt(2)\n"
    "v_permlane16_swap_b32 v65, v67\n"
    "v_mfma_f32_16x16x32_bf16 v[60:63], v[212:215], v[32:35], v[60:63]\n"
    "v_cvt_pk_bf16_f32 v48, v48, v49\n"
    "v_mfma_f32_16x16x32_bf16 v[56:59], v[220:223], v[36:39], v[56:59]\n"
    "s_waitcnt lgkmcnt(1)\n"
    "s_add_u32 m0, s77, s59\n"
    "s_add_u32 s77, s77, 8192\n"
    "buffer_load_dwordx4 v1, s[36:39], s76 offen sc1 lds\n"
    "buffer_store_dwordx4 v[64:67], v4, s[36:39], s73 offen sc1\n"
    "v_cvt_pk_bf16_f32 v49, v50, v51\n"
    "v_mfma_f32_16x16x32_bf16 v[60:63], v[224:227], v[36:39], v[60:63]\n"
    "v_cvt_pk_bf16_f32 v50, v52, v53\n"
    "v_mfma_f32_16x16x32_bf16 v[56:59], v[232:235], v[40:43], v[56:59]\n"
    "s_waitcnt lgkmcnt(0)\n"
    "v_cvt_pk_bf16_f32 v51, v54, v55\n"
    "v_mfma_f32_16x16x32_bf16 v[60:63], v[236:239], v[40:43], v[60:63]\n"
    "v_mfma_f32_16x16x32_bf16 v[56:59], v[244:247], v[44:47], v[56:59]\n"
    "v_mfma_f32_16x16x32_bf16 v[60:63], v[248:251], v[44:47], v[60:63]\n"
    "buffer_store_dwordx4 v[48:51], v15, s[36:39], s73 offen sc1\n"
    "s_nop 2\n"
    "v_cvt_pk_bf16_f32 v56, v56, v57\n"
    "v_cvt_pk_bf16_f32 v57, v58, v59\n"
    "s_nop 1\n"
    "v_cvt_pk_bf16_f32 v58, v60, v61\n"
    "v_cvt_pk_bf16_f32 v59, v62, v63\n"
    "buffer_store_dwordx4 v[56:59], v15, s[36:39], s73 offen offset:1024 sc1\n"
    "v_mov_b32 v12, s68\n"
    "s_mov_b32 s68, s71\n"
    "buffer_load_dwordx4 v1, s[36:39], s76 offen offset:1024 sc1 lds\n"
    "s_add_u32 s76, s76, 16384\n"
    "s_add_u32 m0, s77, s59\n"
    "s_add_u32 s77, s77, 8192\n"
    "buffer_load_dwordx4 v1, s[36:39], s76 offen sc1 lds\n"
    "buffer_load_dwordx4 v1, s[36:39], s76 offen offset:1024 sc1 lds\n"
    "s_add_u32 s76, s76, 16384\n"
    "s_add_u32 m0, s77, s59\n"
    "s_add_u32 s77, s77, 8192\n"
    "buffer_load_dwordx4 v1, s[36:39], s76 offen sc1 lds\n"
    "buffer_load_dwordx4 v1, s[36:39], s76 offen offset:1024 sc1 lds\n"
    "s_add_u32 s76, s76, 16384\n"
    "s_add_u32 m0, s77, s59\n"
    "s_add_u32 s77, s77, 8192\n"
    "buffer_load_dwordx4 v1, s[36:39], s76 offen sc1 lds\n"
    "buffer_load_dwordx4 v1, s[36:39], s76 offen offset:1024 sc1 lds\n"
    "s_add_u32 s76, s76, 16384\n"
    "s_add_u32 m0, s77, s59\n"
    "s_add_u32 s77, s77, 8192\n"
    "buffer_load_dwordx4 v1, s[36:39], s76 offen sc1 lds\n"
    "buffer_load_dwordx4 v1, s[36:39], s76 offen offset:1024 sc1 lds\n"
    "s_add_u32 s76, s76, 16384\n"
    "s_add_u32 m0, s77, s59\n"
    "s_add_u32 s77, s77, 8192\n"
    "buffer_load_dwordx4 v1, s[36:39], s76 offen sc1 lds\n"
    "buffer_load_dwordx4 v1, s[36:39], s76 offen offset:1024 sc1 lds\n"
    "s_add_u32 s76, s76, 16384\n"
    "s_add_u32 m0, s77, s59\n"
    "s_add_u32 s77, s77, 8192\n"
    "buffer_load_dwordx4 v1, s[36:39], s76 offen sc1 lds\n"
    "buffer_load_dwordx4 v1, s[36:39], s76 offen offset:1024 sc1 lds\n"
    "s_add_u32 s76, s76, 16384\n"
    "s_waitcnt vmcnt(13)\n"
    "buffer_atomic_smax v12, v14, s[36:39], 0 offen sc1\n"
    "s_waitcnt vmcnt(1)\n"
    "s_branch .LgWO_bar_%=\n"
    ".LgWO_idle_%=:\n"
    "s_or_b64 exec, vcc, 1\n"
    "buffer_atomic_smax v5, v2, s[36:39], 0 offen sc0 sc1\n"
    "s_mov_b64 exec, -1\n"
    "s_waitcnt vmcnt(1)\n"
    "buffer_atomic_smax v12, v14, s[36:39], 0 offen sc1\n"
    "s_branch .LgWO_block1_%=\n"
    ".LgWO_block_%=:\n"
    "v_add_u32 v13, v11, v3\n"
    "v_cmp_gt_i32 vcc, s71, v13\n"
    "s_nop 0\n"
    "s_or_b64 exec, vcc, 1\n"
    "buffer_atomic_smax v11, v2, s[36:39], 0 offen sc0 sc1\n"
    "s_mov_b64 exec, -1\n"
    "s_sleep 3\n"
    "s_waitcnt vmcnt(1)\n"
    ".LgWO_block0_%=:\n"
    "v_add_u32 v13, v5, v3\n"
    "v_cmp_gt_i32 vcc, s71, v13\n"
    "s_cbranch_vccz .LgWO_go_%=\n"
    "v_add_u32 v13, v5, v3\n"
    "v_cmp_gt_i32 vcc, s71, v13\n"
    "s_nop 0\n"
    "s_or_b64 exec, vcc, 1\n"
    "buffer_atomic_smax v5, v2, s[36:39], 0 offen sc0 sc1\n"
    "s_mov_b64 exec, -1\n"
    "s_sleep 3\n"
    "s_waitcnt vmcnt(1)\n"
    ".LgWO_block1_%=:\n"
    "v_add_u32 v13, v11, v3\n"
    "v_cmp_gt_i32 vcc, s71, v13\n"
    "s_cbranch_vccz .LgWO_go_%=\n"
    "s_sub_u32 s72, s72, 1\n"
    "s_cbranch_scc0 .LgWO_block_%=\n"
    "s_mov_b32 s72, 0\n"
    ".LgWO_go_%=:\n"
    "s_and_b32 s76, s71, s54\n"
    "s_mul_i32 s76, s76, s53\n"
    "s_add_u32 s76, s76, s52\n"
    "s_and_b32 s77, s71, 1\n"
    "s_mul_i32 s77, s77, 65536\n"
    "s_add_u32 s77, s77, s59\n"
    "s_add_u32 m0, s77, s59\n"
    "s_add_u32 s77, s77, 8192\n"
    "buffer_load_dwordx4 v1, s[36:39], s76 offen sc1 lds\n"
    "buffer_load_dwordx4 v1, s[36:39], s76 offen offset:1024 sc1 lds\n"
    "s_add_u32 s76, s76, 16384\n"
    "s_add_u32 m0, s77, s59\n"
    "s_add_u32 s77, s77, 8192\n"
    "buffer_load_dwordx4 v1, s[36:39], s76 offen sc1 lds\n"
    "buffer_load_dwordx4 v1, s[36:39], s76 offen offset:1024 sc1 lds\n"
    "s_add_u32 s76, s76, 16384\n"
    "s_add_u32 m0, s77, s59\n"
    "s_add_u32 s77, s77, 8192\n"
    "buffer_load_dwordx4 v1, s[36:39], s76 offen sc1 lds\n"
    "buffer_load_dwordx4 v1, s[36:39], s76 offen offset:1024 sc1 lds\n"
    "s_add_u32 s76, s76, 16384\n"
    "s_add_u32 m0, s77, s59\n"
    "s_add_u32 s77, s77, 8192\n"
    "buffer_load_dwordx4 v1, s[36:39], s76 offen sc1 lds\n"
    "buffer_load_dwordx4 v1, s[36:39], s76 offen offset:1024 sc1 lds\n"
    "s_add_u32 s76, s76, 16384\n"
    "s_add_u32 m0, s77, s59\n"
    "s_add_u32 s77, s77, 8192\n"
    "buffer_load_dwordx4 v1, s[36:39], s76 offen sc1 lds\n"
    "buffer_load_dwordx4 v1, s[36:39], s76 offen offset:1024 sc1 lds\n"
    "s_add_u32 s76, s76, 16384\n"
    "s_add_u32 m0, s77, s59\n"
    "s_add_u32 s77, s77, 8192\n"
    "buffer_load_dwordx4 v1, s[36:39], s76 offen sc1 lds\n"
    "buffer_load_dwordx4 v1, s[36:39], s76 offen offset:1024 sc1 lds\n"
    "s_add_u32 s76, s76, 16384\n"
    "s_add_u32 m0, s77, s59\n"
    "s_add_u32 s77, s77, 8192\n"
    "buffer_load_dwordx4 v1, s[36:39], s76 offen sc1 lds\n"
    "buffer_load_dwordx4 v1, s[36:39], s76 offen offset:1024 sc1 lds\n"
    "s_add_u32 s76, s76, 16384\n"
    "s_add_u32 m0, s77, s59\n"
    "s_add_u32 s77, s77, 8192\n"
    "buffer_load_dwordx4 v1, s[36:39], s76 offen sc1 lds\n"
    "buffer_load_dwordx4 v1, s[36:39], s76 offen offset:1024 sc1 lds\n"
    "s_add_u32 s76, s76, 16384\n"
    "s_waitcnt vmcnt(0)\n"
    "s_branch .LgWO_bar_%=\n"
    ".LgWO_FIN_%=:\n"
  );
}
DEVI void role_down(const Params& p, int L, int j, int i, u32 dbg, unsigned long long tag) {
  const int lane = threadIdx.x & 63, w = threadIdx.x >> 6, e0 = j * 1152 * 576 + i * 192;
  GEMM_DECL;
  g_w0 = make_srd(p.w2 + e0, 4608u * 576 * 2 - e0 * 2); g_w1 = g_w0; g_w2 = g_w0;
  g_ldw = 1152;
  g_in_base = pgb(L, B_H + 9 * j); g_in_stride = SLOT; g_in_mask = RH - 1;
  g_out_base = pgb(L, B_PF + 5 * j); g_out_kb = 12 * i; g_out_mask = RP - 1;
  g_flag = lane < 4 ? mbox(L, lane, SL_REDF, 12 * j + 4 * i + w) : ((lane < 10 && w == 0) ? mbox(L, j, SL_UP + lane - 4, 16 + i) : OOB);
  g_paddr = mbox(L, j, SL_DOWN + i, lane);
  g_pbias = lane < 24 ? 0 : (lane < 40 ? RP : 1000);
  v16u sa, sb, va;
  gemm_args(GEMM_ARGS, p, dbg, tag, sa, sb, va);
  RUN_ASM(
    "s_nop 4\n"
    "s_getpc_b64 s[84:85]\n"
    ".LgDOWN_PC_%=:\n"
    "s_and_b32 s85, s85, 0xffff\n"
    "s_mov_b32 s86, .LgDOWN_FIN_%=-.LgDOWN_PC_%=\n"
    "s_mov_b32 s87, 0x20000\n"
    "v_mbcnt_lo_u32_b32 v40, -1, 0\n"
    "v_mbcnt_hi_u32_b32 v40, -1, v40\n"
    "v_lshlrev_b32 v40, 7, v40\n"
    "buffer_load_dword v41, v40, s[84:87], 0 offen\n"
    "v_add_u32 v40, 8192, v40\n"
    "buffer_load_dword v41, v40, s[84:87], 0 offen\n"
    "v_add_u32 v40, 8192, v40\n"
    "buffer_load_dword v41, v40, s[84:87], 0 offen\n"
    "v_add_u32 v8, s59, v0\n"
    "s_mov_b32 s68, 0\n"
    "s_mov_b32 s71, 0\n"
    "s_mov_b32 s78, 81920\n"
    "v_add_u32 v9, s78, v0\n"
    "s_mov_b32 s64, 0xbfb8aa3b\n"
    "s_mov_b32 s72, 262144\n"
    "s_add_u32 m0, s59, 0\n"
    "s_mov_b32 s74, 0\n"
    "buffer_load_dwordx4 v5, s[40:43], s74 offen lds\n"
    "s_add_u32 m0, s59, 4096\n"
    "s_mov_b32 s74, 128\n"
    "buffer_load_dwordx4 v5, s[40:43], s74 offen lds\n"
    "s_add_u32 m0, s59, 8192\n"
    "s_mov_b32 s74, 256\n"
    "buffer_load_dwordx4 v5, s[40:43], s74 offen lds\n"
    "s_add_u32 m0, s59, 12288\n"
    "s_mov_b32 s74, 36864\n"
    "buffer_load_dwordx4 v5, s[40:43], s74 offen lds\n"
    "s_add_u32 m0, s59, 16384\n"
    "s_mov_b32 s74, 36992\n"
    "buffer_load_dwordx4 v5, s[40:43], s74 offen lds\n"
    "s_add_u32 m0, s59, 20480\n"
    "s_mov_b32 s74, 37120\n"
    "buffer_load_dwordx4 v5, s[40:43], s74 offen lds\n"
    "s_add_u32 m0, s59, 24576\n"
    "s_mov_b32 s74, 73728\n"
    "buffer_load_dwordx4 v5, s[40:43], s74 offen lds\n"
    "s_add_u32 m0, s59, 28672\n"
    "s_mov_b32 s74, 73856\n"
    "buffer_load_dwordx4 v5, s[40:43], s74 offen lds\n"
    "s_add_u32 m0, s59, 32768\n"
    "s_mov_b32 s74, 73984\n"
    "buffer_load_dwordx4 v5, s[40:43], s74 offen lds\n"
    "s_add_u32 m0, s59, 36864\n"
    "s_mov_b32 s74, 110592\n"
    "buffer_load_dwordx4 v5, s[40:43], s74 offen lds\n"
    "s_add_u32 m0, s59, 40960\n"
    "s_mov_b32 s74, 110720\n"
    "buffer_load_dwordx4 v5, s[40:43], s74 offen lds\n"
    "s_add_u32 m0, s59, 45056\n"
    "s_mov_b32 s74, 110848\n"
    "buffer_load_dwordx4 v5, s[40:43], s74 offen lds\n"
    "s_add_u32 m0, s59, 49152\n"
    "s_mov_b32 s74, 147456\n"
    "buffer_load_dwordx4 v5, s[40:43], s74 offen lds\n"
    "s_add_u32 m0, s59, 53248\n"
    "s_mov_b32 s74, 147584\n"
    "buffer_load_dwordx4 v5, s[40:43], s74 offen lds\n"
    "s_add_u32 m0, s59, 57344\n"
    "s_mov_b32 s74, 147712\n"
    "buffer_load_dwordx4 v5, s[40:43], s74 offen lds\n"
    "s_add_u32 m0, s59, 61440\n"
    "s_mov_b32 s74, 184320\n"
    "buffer_load_dwordx4 v5, s[40:43], s74 offen lds\n"
    "s_add_u32 m0, s59, 65536\n"
    "s_mov_b32 s74, 184448\n"
    "buffer_load_dwordx4 v5, s[40:43], s74 offen lds\n"
    "s_add_u32 m0, s59, 69632\n"
    "s_mov_b32 s74, 184576\n"
    "buffer_load_dwordx4 v5, s[40:43], s74 offen lds\n"
    "s_add_u32 m0, s59, 73728\n"
    "s_mov_b32 s74, 221184\n"
    "buffer_load_dwordx4 v5, s[40:43], s74 offen lds\n"
    "s_add_u32 m0, s59, 77824\n"
    "s_mov_b32 s74, 221312\n"
    "buffer_load_dwordx4 v5, s[40:43], s74 offen lds\n"
    "s_add_u32 m0, s59, 81920\n"
    "s_mov_b32 s74, 221440\n"
    "buffer_load_dwordx4 v5, s[40:43], s74 offen lds\n"
    "s_add_u32 m0, s59, 86016\n"
    "s_mov_b32 s74, 258048\n"
    "buffer_load_dwordx4 v5, s[40:43], s74 offen lds\n"
    "s_add_u32 m0, s59, 90112\n"
    "s_mov_b32 s74, 258176\n"
    "buffer_load_dwordx4 v5, s[40:43], s74 offen lds\n"
    "s_add_u32 m0, s59, 94208\n"
    "s_mov_b32 s74, 258304\n"
    "buffer_load_dwordx4 v5, s[40:43], s74 offen lds\n"
    "s_add_u32 m0, s59, 98304\n"
    "s_mov_b32 s74, 294912\n"
    "buffer_load_dwordx4 v5, s[40:43], s74 offen lds\n"
    "s_add_u32 m0, s59, 102400\n"
    "s_mov_b32 s74, 295040\n"
    "buffer_load_dwordx4 v5, s[40:43], s74 offen lds\n"
    "s_add_u32 m0, s59, 106496\n"
    "s_mov_b32 s74, 295168\n"
    "buffer_load_dwordx4 v5, s[40:43], s74 offen lds\n"
    "s_add_u32 m0, s59, 110592\n"
    "s_mov_b32 s74, 331776\n"
    "buffer_load_dwordx4 v5, s[40:43], s74 offen lds\n"
    "s_add_u32 m0, s59, 114688\n"
    "s_mov_b32 s74, 331904\n"
    "buffer_load_dwordx4 v5, s[40:43], s74 offen lds\n"
    "s_add_u32 m0, s59, 118784\n"
    "s_mov_b32 s74, 332032\n"
    "buffer_load_dwordx4 v5, s[40:43], s74 offen lds\n"
    "v_add_u32 v16, 49152, v6\n"
    "v_add_u32 v17, 98304, v6\n"
    "v_add_u32 v18, 49152, v7\n"
    "v_add_u32 v19, 98304, v7\n"
    "s_waitcnt vmcnt(24) lgkmcnt(0)\n"
    "s_barrier\n"
    "s_add_u32 m0, s59, 122880\n"
    "s_mov_b32 s74, 368640\n"
    "buffer_load_dwordx4 v5, s[40:43], s74 offen lds\n"
    "s_add_u32 m0, s59, 126976\n"
    "s_mov_b32 s74, 368768\n"
    "buffer_load_dwordx4 v5, s[40:43], s74 offen lds\n"
    "s_add_u32 m0, s59, 131072\n"
    "s_mov_b32 s74, 368896\n"
    "buffer_load_dwordx4 v5, s[40:43], s74 offen lds\n"
    "s_add_u32 m0, s59, 135168\n"
    "s_mov_b32 s74, 405504\n"
    "buffer_load_dwordx4 v5, s[40:43], s74 offen lds\n"
    "s_add_u32 m0, s59, 139264\n"
    "s_mov_b32 s74, 405632\n"
    "buffer_load_dwordx4 v5, s[40:43], s74 offen lds\n"
    "s_add_u32 m0, s59, 143360\n"
    "s_mov_b32 s74, 405760\n"
    "buffer_load_dwordx4 v5, s[40:43], s74 offen lds\n"
    "ds_read_b64_tr_b16 a[0:1], v6 offset:0\n"
    "ds_read_b64_tr_b16 a[2:3], v6 offset:512\n"
    "ds_read_b64_tr_b16 a[4:5], v6 offset:8\n"
    "ds_read_b64_tr_b16 a[6:7], v6 offset:520\n"
    "ds_read_b64_tr_b16 a[8:9], v7 offset:0\n"
    "ds_read_b64_tr_b16 a[10:11], v7 offset:512\n"
    "ds_read_b64_tr_b16 a[12:13], v6 offset:12288\n"
    "ds_read_b64_tr_b16 a[14:15], v6 offset:12800\n"
    "ds_read_b64_tr_b16 a[16:17], v6 offset:12296\n"
    "ds_read_b64_tr_b16 a[18:19], v6 offset:12808\n"
    "ds_read_b64_tr_b16 a[20:21], v7 offset:12288\n"
    "ds_read_b64_tr_b16 a[22:23], v7 offset:12800\n"
    "s_waitcnt vmcnt(24) lgkmcnt(0)\n"
    "s_barrier\n"
    "s_add_u32 m0, s59, 0\n"
    "s_mov_b32 s74, 442368\n"
    "buffer_load_dwordx4 v5, s[40:43], s74 offen lds\n"
    "s_add_u32 m0, s59, 4096\n"
    "s_mov_b32 s74, 442496\n"
    "buffer_load_dwordx4 v5, s[40:43], s74 offen lds\n"
    "s_add_u32 m0, s59, 8192\n"
    "s_mov_b32 s74, 442624\n"
    "buffer_load_dwordx4 v5, s[40:43], s74 offen lds\n"
    "s_add_u32 m0, s59, 12288\n"
    "s_mov_b32 s74, 479232\n"
    "buffer_load_dwordx4 v5, s[40:43], s74 offen lds\n"
    "s_add_u32 m0, s59, 16384\n"
    "s_mov_b32 s74, 479360\n"
    "buffer_load_dwordx4 v5, s[40:43], s74 offen lds\n"
    "s_add_u32 m0, s59, 20480\n"
    "s_mov_b32 s74, 479488\n"
    "buffer_load_dwordx4 v5, s[40:43], s74 offen lds\n"
    "ds_read_b64_tr_b16 a[24:25], v6 offset:24576\n"
    "ds_read_b64_tr_b16 a[26:27], v6 offset:25088\n"
    "ds_read_b64_tr_b16 a[28:29], v6 offset:24584\n"
    "ds_read_b64_tr_b16 a[30:31], v6 offset:25096\n"
    "ds_read_b64_tr_b16 a[32:33], v7 offset:24576\n"
    "ds_read_b64_tr_b16 a[34:35], v7 offset:25088\n"
    "ds_read_b64_tr_b16 a[36:37], v6 offset:36864\n"
    "ds_read_b64_tr_b16 a[38:39], v6 offset:37376\n"
    "ds_read_b64_tr_b16 a[40:41], v6 offset:36872\n"
    "ds_read_b64_tr_b16 a[42:43], v6 offset:37384\n"
    "ds_read_b64_tr_b16 a[44:45], v7 offset:36864\n"
    "ds_read_b64_tr_b16 a[46:47], v7 offset:37376\n"
    "s_waitcnt vmcnt(24) lgkmcnt(0)\n"
    "s_barrier\n"
    "s_add_u32 m0, s59, 24576\n"
    "s_mov_b32 s74, 516096\n"
    "buffer_load_dwordx4 v5, s[40:43], s74 offen lds\n"
    "s_add_u32 m0, s59, 28672\n"
    "s_mov_b32 s74, 516224\n"
    "buffer_load_dwordx4 v5, s[40:43], s74 offen lds\n"
    "s_add_u32 m0, s59, 32768\n"
    "s_mov_b32 s74, 516352\n"
    "buffer_load_dwordx4 v5, s[40:43], s74 offen lds\n"
    "s_add_u32 m0, s59, 36864\n"
    "s_mov_b32 s74, 552960\n"
    "buffer_load_dwordx4 v5, s[40:43], s74 offen lds\n"
    "s_add_u32 m0, s59, 40960\n"
    "s_mov_b32 s74, 553088\n"
    "buffer_load_dwordx4 v5, s[40:43], s74 offen lds\n"
    "s_add_u32 m0, s59, 45056\n"
    "s_mov_b32 s74, 553216\n"
    "buffer_load_dwordx4 v5, s[40:43], s74 offen lds\n"
    "ds_read_b64_tr_b16 a[48:49], v16 offset:0\n"
    "ds_read_b64_tr_b16 a[50:51], v16 offset:512\n"
    "ds_read_b64_tr_b16 a[52:53], v16 offset:8\n"
    "ds_read_b64_tr_b16 a[54:55], v16 offset:520\n"
    "ds_read_b64_tr_b16 a[56:57], v18 offset:0\n"
    "ds_read_b64_tr_b16 a[58:59], v18 offset:512\n"
    "ds_read_b64_tr_b16 a[60:61], v16 offset:12288\n"
    "ds_read_b64_tr_b16 a[62:63], v16 offset:12800\n"
    "ds_read_b64_tr_b16 a[64:65], v16 offset:12296\n"
    "ds_read_b64_tr_b16 a[66:67], v16 offset:12808\n"
    "ds_read_b64_tr_b16 a[68:69], v18 offset:12288\n"
    "ds_read_b64_tr_b16 a[70:71], v18 offset:12800\n"
    "s_waitcnt vmcnt(24) lgkmcnt(0)\n"
    "s_barrier\n"
    "s_add_u32 m0, s59, 49152\n"
    "s_mov_b32 s74, 589824\n"
    "buffer_load_dwordx4 v5, s[40:43], s74 offen lds\n"
    "s_add_u32 m0, s59, 53248\n"
    "s_mov_b32 s74, 589952\n"
    "buffer_load_dwordx4 v5, s[40:43], s74 offen lds\n"
    "s_add_u32 m0, s59, 57344\n"
    "s_mov_b32 s74, 590080\n"
    "buffer_load_dwordx4 v5, s[40:43], s74 offen lds\n"
    "s_add_u32 m0, s59, 61440\n"
    "s_mov_b32 s74, 626688\n"
    "buffer_load_dwordx4 v5, s[40:43], s74 offen lds\n"
    "s_add_u32 m0, s59, 65536\n"
    "s_mov_b32 s74, 626816\n"
    "buffer_load_dwordx4 v5, s[40:43], s74 offen lds\n"
    "s_add_u32 m0, s59, 69632\n"
    "s_mov_b32 s74, 626944\n"
    "buffer_load_dwordx4 v5, s[40:43], s74 offen lds\n"
    "ds_read_b64_tr_b16 a[72:73], v16 offset:24576\n"
    "ds_read_b64_tr_b16 a[74:75], v16 offset:25088\n"
    "ds_read_b64_tr_b16 a[76:77], v16 offset:24584\n"
    "ds_read_b64_tr_b16 a[78:79], v16 offset:25096\n"
    "ds_read_b64_tr_b16 a[80:81], v18 offset:24576\n"
    "ds_read_b64_tr_b16 a[82:83], v18 offset:25088\n"
    "ds_read_b64_tr_b16 a[84:85], v16 offset:36864\n"
    "ds_read_b64_tr_b16 a[86:87], v16 offset:37376\n"
    "ds_read_b64_tr_b16 a[88:89], v16 offset:36872\n"
    "ds_read_b64_tr_b16 a[90:91], v16 offset:37384\n"
    "ds_read_b64_tr_b16 a[92:93], v18 offset:36864\n"
    "ds_read_b64_tr_b16 a[94:95], v18 offset:37376\n"
    "s_waitcnt vmcnt(24) lgkmcnt(0)\n"
    "s_barrier\n"
    "s_add_u32 m0, s59, 73728\n"
    "s_mov_b32 s74, 663552\n"
    "buffer_load_dwordx4 v5, s[40:43], s74 offen lds\n"
    "s_add_u32 m0, s59, 77824\n"
    "s_mov_b32 s74, 663680\n"
    "buffer_load_dwordx4 v5, s[40:43], s74 offen lds\n"
    "s_add_u32 m0, s59, 81920\n"
    "s_mov_b32 s74, 663808\n"
    "buffer_load_dwordx4 v5, s[40:43], s74 offen lds\n"
    "s_add_u32 m0, s59, 86016\n"
    "s_mov_b32 s74, 700416\n"
    "buffer_load_dwordx4 v5, s[40:43], s74 offen lds\n"
    "s_add_u32 m0, s59, 90112\n"
    "s_mov_b32 s74, 700544\n"
    "buffer_load_dwordx4 v5, s[40:43], s74 offen lds\n"
    "s_add_u32 m0, s59, 94208\n"
    "s_mov_b32 s74, 700672\n"
    "buffer_load_dwordx4 v5, s[40:43], s74 offen lds\n"
    "ds_read_b64_tr_b16 a[96:97], v17 offset:0\n"
    "ds_read_b64_tr_b16 a[98:99], v17 offset:512\n"
    "ds_read_b64_tr_b16 a[100:101], v17 offset:8\n"
    "ds_read_b64_tr_b16 a[102:103], v17 offset:520\n"
    "ds_read_b64_tr_b16 a[104:105], v19 offset:0\n"
    "ds_read_b64_tr_b16 a[106:107], v19 offset:512\n"
    "ds_read_b64_tr_b16 a[108:109], v17 offset:12288\n"
    "ds_read_b64_tr_b16 a[110:111], v17 offset:12800\n"
    "ds_read_b64_tr_b16 a[112:113], v17 offset:12296\n"
    "ds_read_b64_tr_b16 a[114:115], v17 offset:12808\n"
    "ds_read_b64_tr_b16 a[116:117], v19 offset:12288\n"
    "ds_read_b64_tr_b16 a[118:119], v19 offset:12800\n"
    "s_waitcnt vmcnt(24) lgkmcnt(0)\n"
    "s_barrier\n"
    "s_add_u32 m0, s59, 98304\n"
    "s_mov_b32 s74, 737280\n"
    "buffer_load_dwordx4 v5, s[40:43], s74 offen lds\n"
    "s_add_u32 m0, s59, 102400\n"
    "s_mov_b32 s74, 737408\n"
    "buffer_load_dwordx4 v5, s[40:43], s74 offen lds\n"
    "s_add_u32 m0, s59, 106496\n"
    "s_mov_b32 s74, 737536\n"
    "buffer_load_dwordx4 v5, s[40:43], s74 offen lds\n"
    "s_add_u32 m0, s59, 110592\n"
    "s_mov_b32 s74, 774144\n"
    "buffer_load_dwordx4 v5, s[40:43], s74 offen lds\n"
    "s_add_u32 m0, s59, 114688\n"
    "s_mov_b32 s74, 774272\n"
    "buffer_load_dwordx4 v5, s[40:43], s74 offen lds\n"
    "s_add_u32 m0, s59, 118784\n"
    "s_mov_b32 s74, 774400\n"
    "buffer_load_dwordx4 v5, s[40:43], s74 offen lds\n"
    "ds_read_b64_tr_b16 a[120:121], v17 offset:24576\n"
    "ds_read_b64_tr_b16 a[122:123], v17 offset:25088\n"
    "ds_read_b64_tr_b16 a[124:125], v17 offset:24584\n"
    "ds_read_b64_tr_b16 a[126:127], v17 offset:25096\n"
    "ds_read_b64_tr_b16 a[128:129], v19 offset:24576\n"
    "ds_read_b64_tr_b16 a[130:131], v19 offset:25088\n"
    "ds_read_b64_tr_b16 a[132:133], v17 offset:36864\n"
    "ds_read_b64_tr_b16 a[134:135], v17 offset:37376\n"
    "ds_read_b64_tr_b16 a[136:137], v17 offset:36872\n"
    "ds_read_b64_tr_b16 a[138:139], v17 offset:37384\n"
    "ds_read_b64_tr_b16 a[140:141], v19 offset:36864\n"
    "ds_read_b64_tr_b16 a[142:143], v19 offset:37376\n"
    "s_waitcnt vmcnt(24) lgkmcnt(0)\n"
    "s_barrier\n"
    "s_add_u32 m0, s59, 122880\n"
    "s_mov_b32 s74, 811008\n"
    "buffer_load_dwordx4 v5, s[40:43], s74 offen lds\n"
    "s_add_u32 m0, s59, 126976\n"
    "s_mov_b32 s74, 811136\n"
    "buffer_load_dwordx4 v5, s[40:43], s74 offen lds\n"
    "s_add_u32 m0, s59, 131072\n"
    "s_mov_b32 s74, 811264\n"
    "buffer_load_dwordx4 v5, s[40:43], s74 offen lds\n"
    "s_add_u32 m0, s59, 135168\n"
    "s_mov_b32 s74, 847872\n"
    "buffer_load_dwordx4 v5, s[40:43], s74 offen lds\n"
    "s_add_u32 m0, s59, 139264\n"
    "s_mov_b32 s74, 848000\n"
    "buffer_load_dwordx4 v5, s[40:43], s74 offen lds\n"
    "s_add_u32 m0, s59, 143360\n"
    "s_mov_b32 s74, 848128\n"
    "buffer_load_dwordx4 v5, s[40:43], s74 offen lds\n"
    "ds_read_b64_tr_b16 a[144:145], v6 offset:0\n"
    "ds_read_b64_tr_b16 a[146:147], v6 offset:512\n"
    "ds_read_b64_tr_b16 a[148:149], v6 offset:8\n"
    "ds_read_b64_tr_b16 a[150:151], v6 offset:520\n"
    "ds_read_b64_tr_b16 a[152:153], v7 offset:0\n"
    "ds_read_b64_tr_b16 a[154:155], v7 offset:512\n"
    "ds_read_b64_tr_b16 a[156:157], v6 offset:12288\n"
    "ds_read_b64_tr_b16 a[158:159], v6 offset:12800\n"
    "ds_read_b64_tr_b16 a[160:161], v6 offset:12296\n"
    "ds_read_b64_tr_b16 a[162:163], v6 offset:12808\n"
    "ds_read_b64_tr_b16 a[164:165], v7 offset:12288\n"
    "ds_read_b64_tr_b16 a[166:167], v7 offset:12800\n"
    "s_waitcnt vmcnt(24) lgkmcnt(0)\n"
    "s_barrier\n"
    "s_add_u32 m0, s59, 0\n"
    "s_mov_b32 s74, 884736\n"
    "buffer_load_dwordx4 v5, s[40:43], s74 offen lds\n"
    "s_add_u32 m0, s59, 4096\n"
    "s_mov_b32 s74, 884864\n"
    "buffer_load_dwordx4 v5, s[40:43], s74 offen lds\n"
    "s_add_u32 m0, s59, 8192\n"
    "s_mov_b32 s74, 884992\n"
    "buffer_load_dwordx4 v5, s[40:43], s74 offen lds\n"
    "s_add_u32 m0, s59, 12288\n"
    "s_mov_b32 s74, 921600\n"
    "buffer_load_dwordx4 v5, s[40:43], s74 offen lds\n"
    "s_add_u32 m0, s59, 16384\n"
    "s_mov_b32 s74, 921728\n"
    "buffer_load_dwordx4 v5, s[40:43], s74 offen lds\n"
    "s_add_u32 m0, s59, 20480\n"
    "s_mov_b32 s74, 921856\n"
    "buffer_load_dwordx4 v5, s[40:43], s74 offen lds\n"
    "ds_read_b64_tr_b16 a[168:169], v6 offset:24576\n"
    "ds_read_b64_tr_b16 a[170:171], v6 offset:25088\n"
    "ds_read_b64_tr_b16 a[172:173], v6 offset:24584\n"
    "ds_read_b64_tr_b16 a[174:175], v6 offset:25096\n"
    "ds_read_b64_tr_b16 a[176:177], v7 offset:24576\n"
    "ds_read_b64_tr_b16 a[178:179], v7 offset:25088\n"
    "ds_read_b64_tr_b16 a[180:181], v6 offset:36864\n"
    "ds_read_b64_tr_b16 a[182:183], v6 offset:37376\n"
    "ds_read_b64_tr_b16 a[184:185], v6 offset:36872\n"
    "ds_read_b64_tr_b16 a[186:187], v6 offset:37384\n"
    "ds_read_b64_tr_b16 a[188:189], v7 offset:36864\n"
    "ds_read_b64_tr_b16 a[190:191], v7 offset:37376\n"
    "s_waitcnt vmcnt(24) lgkmcnt(0)\n"
    "s_barrier\n"
    "s_add_u32 m0, s59, 24576\n"
    "s_mov_b32 s74, 958464\n"
    "buffer_load_dwordx4 v5, s[40:43], s74 offen lds\n"
    "s_add_u32 m0, s59, 28672\n"
    "s_mov_b32 s74, 958592\n"
    "buffer_load_dwordx4 v5, s[40:43], s74 offen lds\n"
    "s_add_u32 m0, s59, 32768\n"
    "s_mov_b32 s74, 958720\n"
    "buffer_load_dwordx4 v5, s[40:43], s74 offen lds\n"
    "s_add_u32 m0, s59, 36864\n"
    "s_mov_b32 s74, 995328\n"
    "buffer_load_dwordx4 v5, s[40:43], s74 offen lds\n"
    "s_add_u32 m0, s59, 40960\n"
    "s_mov_b32 s74, 995456\n"
    "buffer_load_dwordx4 v5, s[40:43], s74 offen lds\n"
    "s_add_u32 m0, s59, 45056\n"
    "s_mov_b32 s74, 995584\n"
    "buffer_load_dwordx4 v5, s[40:43], s74 offen lds\n"
    "ds_read_b64_tr_b16 a[192:193], v16 offset:0\n"
    "ds_read_b64_tr_b16 a[194:195], v16 offset:512\n"
    "ds_read_b64_tr_b16 a[196:197], v16 offset:8\n"
    "ds_read_b64_tr_b16 a[198:199], v16 offset:520\n"
    "ds_read_b64_tr_b16 a[200:201], v18 offset:0\n"
    "ds_read_b64_tr_b16 a[202:203], v18 offset:512\n"
    "ds_read_b64_tr_b16 a[204:205], v16 offset:12288\n"
    "ds_read_b64_tr_b16 a[206:207], v16 offset:12800\n"
    "ds_read_b64_tr_b16 a[208:209], v16 offset:12296\n"
    "ds_read_b64_tr_b16 a[210:211], v16 offset:12808\n"
    "ds_read_b64_tr_b16 a[212:213], v18 offset:12288\n"
    "ds_read_b64_tr_b16 a[214:215], v18 offset:12800\n"
    "s_waitcnt vmcnt(24) lgkmcnt(0)\n"
    "s_barrier\n"
    "s_add_u32 m0, s59, 49152\n"
    "s_mov_b32 s74, 1032192\n"
    "buffer_load_dwordx4 v5, s[40:43], s74 offen lds\n"
    "s_add_u32 m0, s59, 53248\n"
    "s_mov_b32 s74, 1032320\n"
    "buffer_load_dwordx4 v5, s[40:43], s74 offen lds\n"
    "s_add_u32 m0, s59, 57344\n"
    "s_mov_b32 s74, 1032448\n"
    "buffer_load_dwordx4 v5, s[40:43], s74 offen lds\n"
    "s_add_u32 m0, s59, 61440\n"
    "s_mov_b32 s74, 1069056\n"
    "buffer_load_dwordx4 v5, s[40:43], s74 offen lds\n"
    "s_add_u32 m0, s59, 65536\n"
    "s_mov_b32 s74, 1069184\n"
    "buffer_load_dwordx4 v5, s[40:43], s74 offen lds\n"
    "s_add_u32 m0, s59, 69632\n"
    "s_mov_b32 s74, 1069312\n"
    "buffer_load_dwordx4 v5, s[40:43], s74 offen lds\n"
    "ds_read_b64_tr_b16 a[216:217], v16 offset:24576\n"
    "ds_read_b64_tr_b16 a[218:219], v16 offset:25088\n"
    "ds_read_b64_tr_b16 a[220:221], v16 offset:24584\n"
    "ds_read_b64_tr_b16 a[222:223], v16 offset:25096\n"
    "ds_read_b64_tr_b16 a[224:225], v18 offset:24576\n"
    "ds_read_b64_tr_b16 a[226:227], v18 offset:25088\n"
    "ds_read_b64_tr_b16 a[228:229], v16 offset:36864\n"
    "ds_read_b64_tr_b16 a[230:231], v16 offset:37376\n"
    "ds_read_b64_tr_b16 a[232:233], v16 offset:36872\n"
    "ds_read_b64_tr_b16 a[234:235], v16 offset:37384\n"
    "ds_read_b64_tr_b16 a[236:237], v18 offset:36864\n"
    "ds_read_b64_tr_b16 a[238:239], v18 offset:37376\n"
    "s_waitcnt vmcnt(24) lgkmcnt(0)\n"
    "s_barrier\n"
    "s_add_u32 m0, s59, 73728\n"
    "s_mov_b32 s74, 1105920\n"
    "buffer_load_dwordx4 v5, s[40:43], s74 offen lds\n"
    "s_add_u32 m0, s59, 77824\n"
    "s_mov_b32 s74, 1106048\n"
    "buffer_load_dwordx4 v5, s[40:43], s74 offen lds\n"
    "s_add_u32 m0, s59, 81920\n"
    "s_mov_b32 s74, 1106176\n"
    "buffer_load_dwordx4 v5, s[40:43], s74 offen lds\n"
    "s_add_u32 m0, s59, 86016\n"
    "s_mov_b32 s74, 1142784\n"
    "buffer_load_dwordx4 v5, s[40:43], s74 offen lds\n"
    "s_add_u32 m0, s59, 90112\n"
    "s_mov_b32 s74, 1142912\n"
    "buffer_load_dwordx4 v5, s[40:43], s74 offen lds\n"
    "s_add_u32 m0, s59, 94208\n"
    "s_mov_b32 s74, 1143040\n"
    "buffer_load_dwordx4 v5, s[40:43], s74 offen lds\n"
    "ds_read_b64_tr_b16 a[240:241], v17 offset:0\n"
    "ds_read_b64_tr_b16 a[242:243], v17 offset:512\n"
    "ds_read_b64_tr_b16 a[244:245], v17 offset:8\n"
    "ds_read_b64_tr_b16 a[246:247], v17 offset:520\n"
    "ds_read_b64_tr_b16 a[248:249], v19 offset:0\n"
    "ds_read_b64_tr_b16 a[250:251], v19 offset:512\n"
    "ds_read_b64_tr_b16 a[252:253], v17 offset:12288\n"
    "ds_read_b64_tr_b16 a[254:255], v17 offset:12800\n"
    "ds_read_b64_tr_b16 v[80:81], v17 offset:12296\n"
    "ds_read_b64_tr_b16 v[82:83], v17 offset:12808\n"
    "ds_read_b64_tr_b16 v[84:85], v19 offset:12288\n"
    "ds_read_b64_tr_b16 v[86:87], v19 offset:12800\n"
    "s_waitcnt vmcnt(24) lgkmcnt(0)\n"
    "s_barrier\n"
    "s_add_u32 m0, s59, 98304\n"
    "s_mov_b32 s74, 1179648\n"
    "buffer_load_dwordx4 v5, s[40:43], s74 offen lds\n"
    "s_add_u32 m0, s59, 102400\n"
    "s_mov_b32 s74, 1179776\n"
    "buffer_load_dwordx4 v5, s[40:43], s74 offen lds\n"
    "s_add_u32 m0, s59, 106496\n"
    "s_mov_b32 s74, 1179904\n"
    "buffer_load_dwordx4 v5, s[40:43], s74 offen lds\n"
    "s_add_u32 m0, s59, 110592\n"
    "s_mov_b32 s74, 1216512\n"
    "buffer_load_dwordx4 v5, s[40:43], s74 offen lds\n"
    "s_add_u32 m0, s59, 114688\n"
    "s_mov_b32 s74, 1216640\n"
    "buffer_load_dwordx4 v5, s[40:43], s74 offen lds\n"
    "s_add_u32 m0, s59, 118784\n"
    "s_mov_b32 s74, 1216768\n"
    "buffer_load_dwordx4 v5, s[40:43], s74 offen lds\n"
    "ds_read_b64_tr_b16 v[88:89], v17 offset:24576\n"
    "ds_read_b64_tr_b16 v[90:91], v17 offset:25088\n"
    "ds_read_b64_tr_b16 v[92:93], v17 offset:24584\n"
    "ds_read_b64_tr_b16 v[94:95], v17 offset:25096\n"
    "ds_read_b64_tr_b16 v[96:97], v19 offset:24576\n"
    "ds_read_b64_tr_b16 v[98:99], v19 offset:25088\n"
    "ds_read_b64_tr_b16 v[100:101], v17 offset:36864\n"
    "ds_read_b64_tr_b16 v[102:103], v17 offset:37376\n"
    "ds_read_b64_tr_b16 v[104:105], v17 offset:36872\n"
    "ds_read_b64_tr_b16 v[106:107], v17 offset:37384\n"
    "ds_read_b64_tr_b16 v[108:109], v19 offset:36864\n"
    "ds_read_b64_tr_b16 v[110:111], v19 offset:37376\n"
    "s_waitcnt vmcnt(24) lgkmcnt(0)\n"
    "s_barrier\n"
    "s_add_u32 m0, s59, 122880\n"
    "s_mov_b32 s74, 1253376\n"
    "buffer_load_dwordx4 v5, s[40:43], s74 offen lds\n"
    "s_add_u32 m0, s59, 126976\n"
    "s_mov_b32 s74, 1253504\n"
    "buffer_load_dwordx4 v5, s[40:43], s74 offen lds\n"
    "s_add_u32 m0, s59, 131072\n"
    "s_mov_b32 s74, 1253632\n"
    "buffer_load_dwordx4 v5, s[40:43], s74 offen lds\n"
    "s_add_u32 m0, s59, 135168\n"
    "s_mov_b32 s74, 1290240\n"
    "buffer_load_dwordx4 v5, s[40:43], s74 offen lds\n"
    "s_add_u32 m0, s59, 139264\n"
    "s_mov_b32 s74, 1290368\n"
    "buffer_load_dwordx4 v5, s[40:43], s74 offen lds\n"
    "s_add_u32 m0, s59, 143360\n"
    "s_mov_b32 s74, 1290496\n"
    "buffer_load_dwordx4 v5, s[40:43], s74 offen lds\n"
    "buffer_load_dwordx2 v[12:13], off, s[36:39], 0 sc1\n"
    "ds_read_b64_tr_b16 v[112:113], v6 offset:0\n"
    "ds_read_b64_tr_b16 v[114:115], v6 offset:512\n"
    "ds_read_b64_tr_b16 v[116:117], v6 offset:8\n"
    "ds_read_b64_tr_b16 v[118:119], v6 offset:520\n"
    "ds_read_b64_tr_b16 v[120:121], v7 offset:0\n"
    "ds_read_b64_tr_b16 v[122:123], v7 offset:512\n"
    "ds_read_b64_tr_b16 v[124:125], v6 offset:12288\n"
    "ds_read_b64_tr_b16 v[126:127], v6 offset:12800\n"
    "ds_read_b64_tr_b16 v[128:129], v6 offset:12296\n"
    "ds_read_b64_tr_b16 v[130:131], v6 offset:12808\n"
    "ds_read_b64_tr_b16 v[132:133], v7 offset:12288\n"
    "ds_read_b64_tr_b16 v[134:135], v7 offset:12800\n"
    "s_waitcnt vmcnt(25) lgkmcnt(0)\n"
    "s_barrier\n"
    "ds_read_b64_tr_b16 v[136:137], v6 offset:24576\n"
    "ds_read_b64_tr_b16 v[138:139], v6 offset:25088\n"
    "ds_read_b64_tr_b16 v[140:141], v6 offset:24584\n"
    "ds_read_b64_tr_b16 v[142:143], v6 offset:25096\n"
    "ds_read_b64_tr_b16 v[144:145], v7 offset:24576\n"
    "ds_read_b64_tr_b16 v[146:147], v7 offset:25088\n"
    "ds_read_b64_tr_b16 v[148:149], v6 offset:36864\n"
    "ds_read_b64_tr_b16 v[150:151], v6 offset:37376\n"
    "ds_read_b64_tr_b16 v[152:153], v6 offset:36872\n"
    "ds_read_b64_tr_b16 v[154:155], v6 offset:37384\n"
    "ds_read_b64_tr_b16 v[156:157], v7 offset:36864\n"
    "ds_read_b64_tr_b16 v[158:159], v7 offset:37376\n"
    "s_waitcnt vmcnt(19) lgkmcnt(0)\n"
    "s_barrier\n"
    "ds_read_b64_tr_b16 v[160:161], v16 offset:0\n"
    "ds_read_b64_tr_b16 v[162:163], v16 offset:512\n"
    "ds_read_b64_tr_b16 v[164:165], v16 offset:8\n"
    "ds_read_b64_tr_b16 v[166:167], v16 offset:520\n"
    "ds_read_b64_tr_b16 v[168:169], v18 offset:0\n"
    "ds_read_b64_tr_b16 v[170:171], v18 offset:512\n"
    "ds_read_b64_tr_b16 v[172:173], v16 offset:12288\n"
    "ds_read_b64_tr_b16 v[174:175], v16 offset:12800\n"
    "ds_read_b64_tr_b16 v[176:177], v16 offset:12296\n"
    "ds_read_b64_tr_b16 v[178:179], v16 offset:12808\n"
    "ds_read_b64_tr_b16 v[180:181], v18 offset:12288\n"
    "ds_read_b64_tr_b16 v[182:183], v18 offset:12800\n"
    "s_waitcnt vmcnt(13) lgkmcnt(0)\n"
    "s_barrier\n"
    "ds_read_b64_tr_b16 v[184:185], v16 offset:24576\n"
    "ds_read_b64_tr_b16 v[186:187], v16 offset:25088\n"
    "ds_read_b64_tr_b16 v[188:189], v16 offset:24584\n"
    "ds_read_b64_tr_b16 v[190:191], v16 offset:25096\n"
    "ds_read_b64_tr_b16 v[192:193], v18 offset:24576\n"
    "ds_read_b64_tr_b16 v[194:195], v18 offset:25088\n"
    "ds_read_b64_tr_b16 v[196:197], v16 offset:36864\n"
    "ds_read_b64_tr_b16 v[198:199], v16 offset:37376\n"
    "ds_read_b64_tr_b16 v[200:201], v16 offset:36872\n"
    "ds_read_b64_tr_b16 v[202:203], v16 offset:37384\n"
    "ds_read_b64_tr_b16 v[204:205], v18 offset:36864\n"
    "ds_read_b64_tr_b16 v[206:207], v18 offset:37376\n"
    "s_waitcnt vmcnt(7) lgkmcnt(0)\n"
    "s_barrier\n"
    "ds_read_b64_tr_b16 v[208:209], v17 offset:0\n"
    "ds_read_b64_tr_b16 v[210:211], v17 offset:512\n"
    "ds_read_b64_tr_b16 v[212:213], v17 offset:8\n"
    "ds_read_b64_tr_b16 v[214:215], v17 offset:520\n"
    "ds_read_b64_tr_b16 v[216:217], v19 offset:0\n"
    "ds_read_b64_tr_b16 v[218:219], v19 offset:512\n"
    "ds_read_b64_tr_b16 v[220:221], v17 offset:12288\n"
    "ds_read_b64_tr_b16 v[222:223], v17 offset:12800\n"
    "ds_read_b64_tr_b16 v[224:225], v17 offset:12296\n"
    "ds_read_b64_tr_b16 v[226:227], v17 offset:12808\n"
    "ds_read_b64_tr_b16 v[228:229], v19 offset:12288\n"
    "ds_read_b64_tr_b16 v[230:231], v19 offset:12800\n"
    "s_waitcnt vmcnt(0) lgkmcnt(0)\n"
    "s_barrier\n"
    ".LgDOWN_par_%=:\n"
    "v_readfirstlane_b32 s74, v12\n"
    "v_readfirstlane_b32 s75, v13\n"
    "s_xor_b32 s74, s74, s66\n"
    "s_xor_b32 s75, s75, s67\n"
    "s_cselect_b32 s75, 2, 0\n"
    "s_or_b32 s75, s75, s74\n"
    "s_cmp_lt_u32 s75, 2\n"
    "s_cbranch_scc1 .LgDOWN_par1_%=\n"
    "s_sleep 4\n"
    "buffer_load_dwordx2 v[12:13], off, s[36:39], 0 sc1\n"
    "s_waitcnt vmcnt(0)\n"
    "s_sub_u32 s72, s72, 1\n"
    "s_cbranch_scc0 .LgDOWN_par_%=\n"
    "s_mov_b32 s72, 0\n"
    ".LgDOWN_par1_%=:\n"
    "s_and_b32 s74, s74, 1\n"
    "s_lshl_b32 s74, s74, 13\n"
    "s_add_u32 s36, s36, s74\n"
    "s_addc_u32 s37, s37, 0\n"
    "v_add_u32 v13, v11, v3\n"
    "v_cmp_gt_i32 vcc, s71, v13\n"
    "s_nop 0\n"
    "s_or_b64 exec, vcc, 1\n"
    "buffer_atomic_smax v11, v2, s[36:39], 0 offen sc0 sc1\n"
    "s_mov_b64 exec, -1\n"
    "s_getpc_b64 s[84:85]\n"
    ".LgDOWN_PC2_%=:\n"
    "s_and_b32 s85, s85, 0xffff\n"
    "s_mov_b32 s86, .LgDOWN_FIN_%=-.LgDOWN_PC2_%=\n"
    "s_mov_b32 s87, 0x20000\n"
    "v_mbcnt_lo_u32_b32 v42, -1, 0\n"
    "v_mbcnt_hi_u32_b32 v42, -1, v42\n"
    "v_lshlrev_b32 v42, 7, v42\n"
    "buffer_load_dword v43, v42, s[84:87], 0 offen\n"
    "v_add_u32 v42, 8192, v42\n"
    "buffer_load_dword v43, v42, s[84:87], 0 offen\n"
    "v_add_u32 v42, 8192, v42\n"
    "buffer_load_dword v43, v42, s[84:87], 0 offen\n"
    "ds_read_b64_tr_b16 v[232:233], v17 offset:24576\n"
    "ds_read_b64_tr_b16 v[234:235], v17 offset:25088\n"
    "ds_read_b64_tr_b16 v[236:237], v17 offset:24584\n"
    "ds_read_b64_tr_b16 v[238:239], v17 offset:25096\n"
    "ds_read_b64_tr_b16 v[240:241], v19 offset:24576\n"
    "ds_read_b64_tr_b16 v[242:243], v19 offset:25088\n"
    "ds_read_b64_tr_b16 v[244:245], v17 offset:36864\n"
    "ds_read_b64_tr_b16 v[246:247], v17 offset:37376\n"
    "ds_read_b64_tr_b16 v[248:249], v17 offset:36872\n"
    "ds_read_b64_tr_b16 v[250:251], v17 offset:37384\n"
    "ds_read_b64_tr_b16 v[252:253], v19 offset:36864\n"
    "ds_read_b64_tr_b16 v[254:255], v19 offset:37376\n"
    "s_waitcnt lgkmcnt(0)\n"
    "s_barrier\n"
    "s_waitcnt vmcnt(0)\n"
    "v_mov_b32 v5, v11\n"
    "v_mov_b32 v12, -1\n"
    "s_branch .LgDOWN_block1_%=\n"
    ".LgDOWN_LOOPB_%=:\n"
    ".LgDOWN_head_%=:\n"
    "v_mfma_f32_16x16x32_bf16 v[64:67], a[8:11], v[16:19], 0\n"
    "s_waitcnt lgkmcnt(6)\n"
    "ds_read_b128 v[16:19], v9 offset:0\n"
    "v_mfma_f32_16x16x32_bf16 v[68:71], a[8:11], v[20:23], 0\n"
    "s_waitcnt lgkmcnt(6)\n"
    "ds_read_b128 v[20:23], v9 offset:2048\n"
    "v_mfma_f32_16x16x32_bf16 v[64:67], a[20:23], v[24:27], v[64:67]\n"
    "s_waitcnt lgkmcnt(6)\n"
    "ds_read_b128 v[24:27], v9 offset:4096\n"
    "v_mfma_f32_16x16x32_bf16 v[68:71], a[20:23], v[28:31], v[68:71]\n"
    "s_waitcnt lgkmcnt(6)\n"
    "ds_read_b128 v[28:31], v9 offset:6144\n"
    "v_mfma_f32_16x16x32_bf16 v[64:67], a[32:35], v[32:35], v[64:67]\n"
    "s_waitcnt lgkmcnt(6)\n"
    "ds_read_b128 v[32:35], v9 offset:1024\n"
    "v_add_u32 v13, v11, v3\n"
    "v_mfma_f32_16x16x32_bf16 v[68:71], a[32:35], v[36:39], v[68:71]\n"
    "s_waitcnt lgkmcnt(6)\n"
    "ds_read_b128 v[36:39], v9 offset:3072\n"
    "v_cmp_gt_i32 vcc, s71, v13\n"
    "v_mfma_f32_16x16x32_bf16 v[64:67], a[44:47], v[40:43], v[64:67]\n"
    "s_waitcnt lgkmcnt(6)\n"
    "ds_read_b128 v[40:43], v9 offset:5120\n"
    "v_mfma_f32_16x16x32_bf16 v[68:71], a[44:47], v[44:47], v[68:71]\n"
    "s_waitcnt lgkmcnt(6)\n"
    "ds_read_b128 v[44:47], v9 offset:7168\n"
    "v_mfma_f32_16x16x32_bf16 v[48:51], a[0:3], v[16:19], 0\n"
    "s_waitcnt lgkmcnt(6)\n"
    "s_or_b64 exec, vcc, 1\n"
    "buffer_atomic_smax v11, v2, s[36:39], 0 offen sc0 sc1\n"
    "s_mov_b64 exec, -1\n"
    "v_cvt_pk_bf16_f32 v56, v56, v57\n"
    "v_cvt_pk_bf16_f32 v57, v58, v59\n"
    "v_mfma_f32_16x16x32_bf16 v[52:55], a[4:7], v[16:19], 0\n"
    "ds_read_b128 v[16:19], v9 offset:8192\n"
    "v_cvt_pk_bf16_f32 v58, v60, v61\n"
    "v_mfma_f32_16x16x32_bf16 v[48:51], a[12:15], v[20:23], v[48:51]\n"
    "s_waitcnt lgkmcnt(6)\n"
    "v_cvt_pk_bf16_f32 v59, v62, v63\n"
    "v_mfma_f32_16x16x32_bf16 v[52:55], a[16:19], v[20:23], v[52:55]\n"
    "ds_read_b128 v[20:23], v9 offset:9216\n"
    "v_mfma_f32_16x16x32_bf16 v[48:51], a[24:27], v[24:27], v[48:51]\n"
    "s_waitcnt lgkmcnt(6)\n"
    "buffer_store_dwordx4 v[56:59], v15, s[36:39], s73 offen offset:1024 sc1\n"
    "v_mfma_f32_16x16x32_bf16 v[52:55], a[28:31], v[24:27], v[52:55]\n"
    "ds_read_b128 v[24:27], v9 offset:10240\n"
    "v_mfma_f32_16x16x32_bf16 v[48:51], a[36:39], v[28:31], v[48:51]\n"
    "s_waitcnt lgkmcnt(6)\n"
    "s_and_b32 s76, s71, s54\n"
    "v_mfma_f32_16x16x32_bf16 v[52:55], a[40:43], v[28:31], v[52:55]\n"
    "ds_read_b128 v[28:31], v9 offset:11264\n"
    "v_mfma_f32_16x16x32_bf16 v[56:59], a[0:3], v[32:35], 0\n"
    "s_waitcnt lgkmcnt(6)\n"
    "s_mul_i32 s76, s76, s53\n"
    "v_mfma_f32_16x16x32_bf16 v[60:63], a[4:7], v[32:35], 0\n"
    "ds_read_b128 v[32:35], v9 offset:12288\n"
    "v_mfma_f32_16x16x32_bf16 v[56:59], a[12:15], v[36:39], v[56:59]\n"
    "s_waitcnt lgkmcnt(6)\n"
    "s_add_u32 s76, s76, s52\n"
    "v_mfma_f32_16x16x32_bf16 v[60:63], a[16:19], v[36:39], v[60:63]\n"
    "ds_read_b128 v[36:39], v9 offset:13312\n"
    "v_mfma_f32_16x16x32_bf16 v[56:59], a[24:27], v[40:43], v[56:59]\n"
    "s_waitcnt lgkmcnt(6)\n"
    "s_and_b32 s77, s71, 1\n"
    "v_mfma_f32_16x16x32_bf16 v[60:63], a[28:31], v[40:43], v[60:63]\n"
    "ds_read_b128 v[40:43], v9 offset:14336\n"
    "v_mfma_f32_16x16x32_bf16 v[56:59], a[36:39], v[44:47], v[56:59]\n"
    "s_waitcnt lgkmcnt(6)\n"
    "s_mul_i32 s77, s77, 81920\n"
    "v_mfma_f32_16x16x32_bf16 v[60:63], a[40:43], v[44:47], v[60:63]\n"
    "ds_read_b128 v[44:47], v9 offset:15360\n"
    "v_mfma_f32_16x16x32_bf16 v[48:51], a[48:51], v[16:19], v[48:51]\n"
    "s_add_u32 s77, s77, s59\n"
    "v_mfma_f32_16x16x32_bf16 v[52:55], a[52:55], v[16:19], v[52:55]\n"
    "s_waitcnt lgkmcnt(6)\n"
    "s_and_b32 s73, s68, s57\n"
    "v_mfma_f32_16x16x32_bf16 v[64:67], a[56:59], v[16:19], v[64:67]\n"
    "ds_read_b128 v[16:19], v9 offset:16384\n"
    "v_mfma_f32_16x16x32_bf16 v[56:59], a[48:51], v[20:23], v[56:59]\n"
    "s_mul_i32 s73, s73, s56\n"
    "v_mfma_f32_16x16x32_bf16 v[60:63], a[52:55], v[20:23], v[60:63]\n"
    "s_waitcnt lgkmcnt(6)\n"
    "s_add_u32 s73, s73, s55\n"
    "v_mfma_f32_16x16x32_bf16 v[68:71], a[56:59], v[20:23], v[68:71]\n"
    "ds_read_b128 v[20:23], v9 offset:17408\n"
    "v_add_u32 v13, v5, v3\n"
    "v_mfma_f32_16x16x32_bf16 v[48:51], a[60:63], v[24:27], v[48:51]\n"
    "v_cmp_gt_i32 vcc, s71, v13\n"
    "v_mfma_f32_16x16x32_bf16 v[52:55], a[64:67], v[24:27], v[52:55]\n"
    "s_waitcnt lgkmcnt(6)\n"
    "v_mfma_f32_16x16x32_bf16 v[64:67], a[68:71], v[24:27], v[64:67]\n"
    "ds_read_b128 v[24:27], v9 offset:18432\n"
    "s_or_b64 exec, vcc, 1\n"
    "buffer_atomic_smax v5, v2, s[36:39], 0 offen sc0 sc1\n"
    "s_mov_b64 exec, -1\n"
    "v_mfma_f32_16x16x32_bf16 v[56:59], a[60:63], v[28:31], v[56:59]\n"
    "v_mfma_f32_16x16x32_bf16 v[60:63], a[64:67], v[28:31], v[60:63]\n"
    "s_waitcnt lgkmcnt(6)\n"
    "v_mfma_f32_16x16x32_bf16 v[68:71], a[68:71], v[28:31], v[68:71]\n"
    "ds_read_b128 v[28:31], v9 offset:19456\n"
    "v_mfma_f32_16x16x32_bf16 v[48:51], a[72:75], v[32:35], v[48:51]\n"
    "v_mfma_f32_16x16x32_bf16 v[52:55], a[76:79], v[32:35], v[52:55]\n"
    "s_waitcnt lgkmcnt(6)\n"
    "v_mfma_f32_16x16x32_bf16 v[64:67], a[80:83], v[32:35], v[64:67]\n"
    "ds_read_b128 v[32:35], v9 offset:20480\n"
    "v_mfma_f32_16x16x32_bf16 v[56:59], a[72:75], v[36:39], v[56:59]\n"
    "v_mfma_f32_16x16x32_bf16 v[60:63], a[76:79], v[36:39], v[60:63]\n"
    "s_waitcnt lgkmcnt(6)\n"
    "v_mfma_f32_16x16x32_bf16 v[68:71], a[80:83], v[36:39], v[68:71]\n"
    "ds_read_b128 v[36:39], v9 offset:21504\n"
    "v_mfma_f32_16x16x32_bf16 v[48:51], a[84:87], v[40:43], v[48:51]\n"
    "v_mfma_f32_16x16x32_bf16 v[52:55], a[88:91], v[40:43], v[52:55]\n"
    "s_waitcnt lgkmcnt(6)\n"
    "v_mfma_f32_16x16x32_bf16 v[64:67], a[92:95], v[40:43], v[64:67]\n"
    "ds_read_b128 v[40:43], v9 offset:22528\n"
    "v_mfma_f32_16x16x32_bf16 v[56:59], a[84:87], v[44:47], v[56:59]\n"
    "v_mfma_f32_16x16x32_bf16 v[60:63], a[88:91], v[44:47], v[60:63]\n"
    "s_waitcnt lgkmcnt(6)\n"
    "v_mfma_f32_16x16x32_bf16 v[68:71], a[92:95], v[44:47], v[68:71]\n"
    "ds_read_b128 v[44:47], v9 offset:23552\n"
    "s_waitcnt vmcnt(1)\n"
    "v_add_u32 v13, v11, v3\n"
    "v_mfma_f32_16x16x32_bf16 v[48:51], a[96:99], v[16:19], v[48:51]\n"
    "buffer_atomic_smax v12, v14, s[36:39], 0 offen sc1\n"
    "v_cmp_gt_i32 vcc, s71, v13\n"
    "v_mfma_f32_16x16x32_bf16 v[52:55], a[100:103], v[16:19], v[52:55]\n"
    "s_waitcnt lgkmcnt(6)\n"
    "v_add_u32 v10, 49152, v9\n"
    "v_mfma_f32_16x16x32_bf16 v[64:67], a[104:107], v[16:19], v[64:67]\n"
    "ds_read_b128 v[16:19], v9 offset:24576\n"
    "v_mfma_f32_16x16x32_bf16 v[56:59], a[96:99], v[20:23], v[56:59]\n"
    ".LgDOWN_mid_%=:\n"
    "s_cbranch_vccnz .LgDOWN_poll0_%=\n"
    "v_mfma_f32_16x16x32_bf16 v[60:63], a[100:103], v[20:23], v[60:63]\n"
    "s_waitcnt lgkmcnt(6)\n"
    "s_add_u32 m0, s77, s59\n"
    "s_add_u32 s77, s77, 8192\n"
    "buffer_load_dwordx4 v1, s[36:39], s76 offen sc1 lds\n"
    "v_mfma_f32_16x16x32_bf16 v[68:71], a[104:107], v[20:23], v[68:71]\n"
    "ds_read_b128 v[20:23], v9 offset:25600\n"
    "v_mfma_f32_16x16x32_bf16 v[48:51], a[108:111], v[24:27], v[48:51]\n"
    "v_mfma_f32_16x16x32_bf16 v[52:55], a[112:115], v[24:27], v[52:55]\n"
    "s_waitcnt lgkmcnt(6)\n"
    "buffer_load_dwordx4 v1, s[36:39], s76 offen offset:1024 sc1 lds\n"
    "s_add_u32 s76, s76, 16384\n"
    "v_mfma_f32_16x16x32_bf16 v[64:67], a[116:119], v[24:27], v[64:67]\n"
    "ds_read_b128 v[24:27], v9 offset:26624\n"
    "v_mfma_f32_16x16x32_bf16 v[56:59], a[108:111], v[28:31], v[56:59]\n"
    "v_mfma_f32_16x16x32_bf16 v[60:63], a[112:115], v[28:31], v[60:63]\n"
    "s_waitcnt lgkmcnt(6)\n"
    "v_mfma_f32_16x16x32_bf16 v[68:71], a[116:119], v[28:31], v[68:71]\n"
    "ds_read_b128 v[28:31], v9 offset:27648\n"
    "v_mfma_f32_16x16x32_bf16 v[48:51], a[120:123], v[32:35], v[48:51]\n"
    "s_add_u32 m0, s77, s59\n"
    "s_add_u32 s77, s77, 8192\n"
    "buffer_load_dwordx4 v1, s[36:39], s76 offen sc1 lds\n"
    "v_mfma_f32_16x16x32_bf16 v[52:55], a[124:127], v[32:35], v[52:55]\n"
    "s_waitcnt lgkmcnt(6)\n"
    "v_mfma_f32_16x16x32_bf16 v[64:67], a[128:131], v[32:35], v[64:67]\n"
    "ds_read_b128 v[32:35], v9 offset:28672\n"
    "v_mfma_f32_16x16x32_bf16 v[56:59], a[120:123], v[36:39], v[56:59]\n"
    "v_mfma_f32_16x16x32_bf16 v[60:63], a[124:127], v[36:39], v[60:63]\n"
    "s_waitcnt lgkmcnt(6)\n"
    "buffer_load_dwordx4 v1, s[36:39], s76 offen offset:1024 sc1 lds\n"
    "s_add_u32 s76, s76, 16384\n"
    "v_mfma_f32_16x16x32_bf16 v[68:71], a[128:131], v[36:39], v[68:71]\n"
    "ds_read_b128 v[36:39], v9 offset:29696\n"
    "v_mfma_f32_16x16x32_bf16 v[48:51], a[132:135], v[40:43], v[48:51]\n"
    "v_mfma_f32_16x16x32_bf16 v[52:55], a[136:139], v[40:43], v[52:55]\n"
    "s_waitcnt lgkmcnt(6)\n"
    "v_mfma_f32_16x16x32_bf16 v[64:67], a[140:143], v[40:43], v[64:67]\n"
    "ds_read_b128 v[40:43], v9 offset:30720\n"
    "v_mfma_f32_16x16x32_bf16 v[56:59], a[132:135], v[44:47], v[56:59]\n"
    "s_add_u32 m0, s77, s59\n"
    "s_add_u32 s77, s77, 8192\n"
    "buffer_load_dwordx4 v1, s[36:39], s76 offen sc1 lds\n"
    "v_mfma_f32_16x16x32_bf16 v[60:63], a[136:139], v[44:47], v[60:63]\n"
    "s_waitcnt lgkmcnt(6)\n"
    "v_mfma_f32_16x16x32_bf16 v[68:71], a[140:143], v[44:47], v[68:71]\n"
    "ds_read_b128 v[44:47], v9 offset:31744\n"
    "v_mfma_f32_16x16x32_bf16 v[48:51], a[144:147], v[16:19], v[48:51]\n"
    "v_mfma_f32_16x16x32_bf16 v[52:55], a[148:151], v[16:19], v[52:55]\n"
    "s_waitcnt lgkmcnt(6)\n"
    "buffer_load_dwordx4 v1, s[36:39], s76 offen offset:1024 sc1 lds\n"
    "s_add_u32 s76, s76, 16384\n"
    "v_mfma_f32_16x16x32_bf16 v[64:67], a[152:155], v[16:19], v[64:67]\n"
    "ds_read_b128 v[16:19], v9 offset:32768\n"
    "v_mfma_f32_16x16x32_bf16 v[56:59], a[144:147], v[20:23], v[56:59]\n"
    "v_mfma_f32_16x16x32_bf16 v[60:63], a[148:151], v[20:23], v[60:63]\n"
    "s_waitcnt lgkmcnt(6)\n"
    "v_mfma_f32_16x16x32_bf16 v[68:71], a[152:155], v[20:23], v[68:71]\n"
    "ds_read_b128 v[20:23], v9 offset:33792\n"
    "v_mfma_f32_16x16x32_bf16 v[48:51], a[156:159], v[24:27], v[48:51]\n"
    "s_add_u32 m0, s77, s59\n"
    "s_add_u32 s77, s77, 8192\n"
    "buffer_load_dwordx4 v1, s[36:39], s76 offen sc1 lds\n"
    "v_mfma_f32_16x16x32_bf16 v[52:55], a[160:163], v[24:27], v[52:55]\n"
    "s_waitcnt lgkmcnt(6)\n"
    "v_mfma_f32_16x16x32_bf16 v[64:67], a[164:167], v[24:27], v[64:67]\n"
    "ds_read_b128 v[24:27], v9 offset:34816\n"
    "v_mfma_f32_16x16x32_bf16 v[56:59], a[156:159], v[28:31], v[56:59]\n"
    "v_mfma_f32_16x16x32_bf16 v[60:63], a[160:163], v[28:31], v[60:63]\n"
    "s_waitcnt lgkmcnt(6)\n"
    "buffer_load_dwordx4 v1, s[36:39], s76 offen offset:1024 sc1 lds\n"
    "s_add_u32 s76, s76, 16384\n"
    "v_mfma_f32_16x16x32_bf16 v[68:71], a[164:167], v[28:31], v[68:71]\n"
    "ds_read_b128 v[28:31], v9 offset:35840\n"
    "v_mfma_f32_16x16x32_bf16 v[48:51], a[168:171], v[32:35], v[48:51]\n"
    "v_mfma_f32_16x16x32_bf16 v[52:55], a[172:175], v[32:35], v[52:55]\n"
    "s_waitcnt lgkmcnt(6)\n"
    "v_mfma_f32_16x16x32_bf16 v[64:67], a[176:179], v[32:35], v[64:67]\n"
    "ds_read_b128 v[32:35], v9 offset:36864\n"
    "v_mfma_f32_16x16x32_bf16 v[56:59], a[168:171], v[36:39], v[56:59]\n"
    "s_add_u32 m0, s77, s59\n"
    "s_add_u32 s77, s77, 8192\n"
    "buffer_load_dwordx4 v1, s[36:39], s76 offen sc1 lds\n"
    "v_mfma_f32_16x16x32_bf16 v[60:63], a[172:175], v[36:39], v[60:63]\n"
    "s_waitcnt lgkmcnt(6)\n"
    "v_mfma_f32_16x16x32_bf16 v[68:71], a[176:179], v[36:39], v[68:71]\n"
    "ds_read_b128 v[36:39], v9 offset:37888\n"
    "v_mfma_f32_16x16x32_bf16 v[48:51], a[180:183], v[40:43], v[48:51]\n"
    "v_mfma_f32_16x16x32_bf16 v[52:55], a[184:187], v[40:43], v[52:55]\n"
    "s_waitcnt lgkmcnt(6)\n"
    "buffer_load_dwordx4 v1, s[36:39], s76 offen offset:1024 sc1 lds\n"
    "s_add_u32 s76, s76, 16384\n"
    "v_mfma_f32_16x16x32_bf16 v[64:67], a[188:191], v[40:43], v[64:67]\n"
    "ds_read_b128 v[40:43], v9 offset:38912\n"
    "v_mfma_f32_16x16x32_bf16 v[56:59], a[180:183], v[44:47], v[56:59]\n"
    "v_mfma_f32_16x16x32_bf16 v[60:63], a[184:187], v[44:47], v[60:63]\n"
    "s_waitcnt lgkmcnt(6)\n"
    "v_mfma_f32_16x16x32_bf16 v[68:71], a[188:191], v[44:47], v[68:71]\n"
    "ds_read_b128 v[44:47], v9 offset:39936\n"
    "v_mfma_f32_16x16x32_bf16 v[48:51], a[192:195], v[16:19], v[48:51]\n"
    "s_add_u32 m0, s77, s59\n"
    "s_add_u32 s77, s77, 8192\n"
    "buffer_load_dwordx4 v1, s[36:39], s76 offen sc1 lds\n"
    "v_mfma_f32_16x16x32_bf16 v[52:55], a[196:199], v[16:19], v[52:55]\n"
    "s_waitcnt lgkmcnt(6)\n"
    "v_mfma_f32_16x16x32_bf16 v[64:67], a[200:203], v[16:19], v[64:67]\n"
    "ds_read_b128 v[16:19], v9 offset:40960\n"
    "v_mfma_f32_16x16x32_bf16 v[56:59], a[192:195], v[20:23], v[56:59]\n"
    "v_mfma_f32_16x16x32_bf16 v[60:63], a[196:199], v[20:23], v[60:63]\n"
    "s_waitcnt lgkmcnt(6)\n"
    "buffer_load_dwordx4 v1, s[36:39], s76 offen offset:1024 sc1 lds\n"
    "s_add_u32 s76, s76, 16384\n"
    "v_mfma_f32_16x16x32_bf16 v[68:71], a[200:203], v[20:23], v[68:71]\n"
    "ds_read_b128 v[20:23], v9 offset:41984\n"
    "v_mfma_f32_16x16x32_bf16 v[48:51], a[204:207], v[24:27], v[48:51]\n"
    "v_mfma_f32_16x16x32_bf16 v[52:55], a[208:211], v[24:27], v[52:55]\n"
    "s_waitcnt lgkmcnt(6)\n"
    "v_mfma_f32_16x16x32_bf16 v[64:67], a[212:215], v[24:27], v[64:67]\n"
    "ds_read_b128 v[24:27], v9 offset:43008\n"
    "v_mfma_f32_16x16x32_bf16 v[56:59], a[204:207], v[28:31], v[56:59]\n"
    "s_add_u32 m0, s77, s59\n"
    "s_add_u32 s77, s77, 8192\n"
    "buffer_load_dwordx4 v1, s[36:39], s76 offen sc1 lds\n"
    "v_mfma_f32_16x16x32_bf16 v[60:63], a[208:211], v[28:31], v[60:63]\n"
    "s_waitcnt lgkmcnt(6)\n"
    "v_mfma_f32_16x16x32_bf16 v[68:71], a[212:215], v[28:31], v[68:71]\n"
    "ds_read_b128 v[28:31], v9 offset:44032\n"
    "v_mfma_f32_16x16x32_bf16 v[48:51], a[216:219], v[32:35], v[48:51]\n"
    "v_mfma_f32_16x16x32_bf16 v[52:55], a[220:223], v[32:35], v[52:55]\n"
    "s_waitcnt lgkmcnt(6)\n"
    "buffer_load_dwordx4 v1, s[36:39], s76 offen offset:1024 sc1 lds\n"
    "s_add_u32 s76, s76, 16384\n"
    "v_mfma_f32_16x16x32_bf16 v[64:67], a[224:227], v[32:35], v[64:67]\n"
    "ds_read_b128 v[32:35], v9 offset:45056\n"
    "v_mfma_f32_16x16x32_bf16 v[56:59], a[216:219], v[36:39], v[56:59]\n"
    "v_mfma_f32_16x16x32_bf16 v[60:63], a[220:223], v[36:39], v[60:63]\n"
    "s_waitcnt lgkmcnt(6)\n"
    "v_mfma_f32_16x16x32_bf16 v[68:71], a[224:227], v[36:39], v[68:71]\n"
    "ds_read_b128 v[36:39], v9 offset:46080\n"
    "v_mfma_f32_16x16x32_bf16 v[48:51], a[228:231], v[40:43], v[48:51]\n"
    "s_add_u32 m0, s77, s59\n"
    "s_add_u32 s77, s77, 8192\n"
    "buffer_load_dwordx4 v1, s[36:39], s76 offen sc1 lds\n"
    "v_mfma_f32_16x16x32_bf16 v[52:55], a[232:235], v[40:43], v[52:55]\n"
    "s_waitcnt lgkmcnt(6)\n"
    "v_mfma_f32_16x16x32_bf16 v[64:67], a[236:239], v[40:43], v[64:67]\n"
    "ds_read_b128 v[40:43], v9 offset:47104\n"
    "v_mfma_f32_16x16x32_bf16 v[56:59], a[228:231], v[44:47], v[56:59]\n"
    "v_mfma_f32_16x16x32_bf16 v[60:63], a[232:235], v[44:47], v[60:63]\n"
    "s_waitcnt lgkmcnt(6)\n"
    "buffer_load_dwordx4 v1, s[36:39], s76 offen offset:1024 sc1 lds\n"
    "s_add_u32 s76, s76, 16384\n"
    "v_mfma_f32_16x16x32_bf16 v[68:71], a[236:239], v[44:47], v[68:71]\n"
    "ds_read_b128 v[44:47], v9 offset:48128\n"
    "v_mfma_f32_16x16x32_bf16 v[48:51], a[240:243], v[16:19], v[48:51]\n"
    "v_mfma_f32_16x16x32_bf16 v[52:55], a[244:247], v[16:19], v[52:55]\n"
    "s_waitcnt lgkmcnt(6)\n"
    "v_mfma_f32_16x16x32_bf16 v[64:67], a[248:251], v[16:19], v[64:67]\n"
    "ds_read_b128 v[16:19], v10 offset:0\n"
    "v_mfma_f32_16x16x32_bf16 v[56:59], a[240:243], v[20:23], v[56:59]\n"
    "s_add_u32 m0, s77, s59\n"
    "s_add_u32 s77, s77, 8192\n"
    "buffer_load_dwordx4 v1, s[36:39], s76 offen sc1 lds\n"
    "v_mfma_f32_16x16x32_bf16 v[60:63], a[244:247], v[20:23], v[60:63]\n"
    "s_waitcnt lgkmcnt(6)\n"
    "v_mfma_f32_16x16x32_bf16 v[68:71], a[248:251], v[20:23], v[68:71]\n"
    "ds_read_b128 v[20:23], v10 offset:1024\n"
    "v_mfma_f32_16x16x32_bf16 v[48:51], a[252:255], v[24:27], v[48:51]\n"
    "v_mfma_f32_16x16x32_bf16 v[52:55], v[80:83], v[24:27], v[52:55]\n"
    "s_waitcnt lgkmcnt(6)\n"
    "buffer_load_dwordx4 v1, s[36:39], s76 offen offset:1024 sc1 lds\n"
    "s_add_u32 s76, s76, 16384\n"
    "v_mfma_f32_16x16x32_bf16 v[64:67], v[84:87], v[24:27], v[64:67]\n"
    "ds_read_b128 v[24:27], v10 offset:2048\n"
    "v_mfma_f32_16x16x32_bf16 v[56:59], a[252:255], v[28:31], v[56:59]\n"
    "v_mfma_f32_16x16x32_bf16 v[60:63], v[80:83], v[28:31], v[60:63]\n"
    "s_waitcnt lgkmcnt(6)\n"
    "v_mfma_f32_16x16x32_bf16 v[68:71], v[84:87], v[28:31], v[68:71]\n"
    "ds_read_b128 v[28:31], v10 offset:3072\n"
    ".LgDOWN_q132_%=:\n"
    "v_mfma_f32_16x16x32_bf16 v[48:51], v[88:91], v[32:35], v[48:51]\n"
    "v_mfma_f32_16x16x32_bf16 v[52:55], v[92:95], v[32:35], v[52:55]\n"
    "s_waitcnt lgkmcnt(6)\n"
    "v_mfma_f32_16x16x32_bf16 v[64:67], v[96:99], v[32:35], v[64:67]\n"
    "ds_read_b128 v[32:35], v10 offset:4096\n"
    "v_mfma_f32_16x16x32_bf16 v[56:59], v[88:91], v[36:39], v[56:59]\n"
    "v_mfma_f32_16x16x32_bf16 v[60:63], v[92:95], v[36:39], v[60:63]\n"
    "s_waitcnt lgkmcnt(6)\n"
    "v_mfma_f32_16x16x32_bf16 v[68:71], v[96:99], v[36:39], v[68:71]\n"
    "ds_read_b128 v[36:39], v10 offset:5120\n"
    "v_mfma_f32_16x16x32_bf16 v[48:51], v[100:103], v[40:43], v[48:51]\n"
    "v_mfma_f32_16x16x32_bf16 v[52:55], v[104:107], v[40:43], v[52:55]\n"
    "s_waitcnt lgkmcnt(6)\n"
    "v_mfma_f32_16x16x32_bf16 v[64:67], v[108:111], v[40:43], v[64:67]\n"
    "ds_read_b128 v[40:43], v10 offset:6144\n"
    "v_mfma_f32_16x16x32_bf16 v[56:59], v[100:103], v[44:47], v[56:59]\n"
    "v_mfma_f32_16x16x32_bf16 v[60:63], v[104:107], v[44:47], v[60:63]\n"
    "s_waitcnt lgkmcnt(6)\n"
    "v_mfma_f32_16x16x32_bf16 v[68:71], v[108:111], v[44:47], v[68:71]\n"
    "ds_read_b128 v[44:47], v10 offset:7168\n"
    "v_mfma_f32_16x16x32_bf16 v[48:51], v[112:115], v[16:19], v[48:51]\n"
    "v_mfma_f32_16x16x32_bf16 v[52:55], v[116:119], v[16:19], v[52:55]\n"
    "s_waitcnt lgkmcnt(6)\n"
    "v_mfma_f32_16x16x32_bf16 v[64:67], v[120:123], v[16:19], v[64:67]\n"
    "ds_read_b128 v[16:19], v10 offset:8192\n"
    "v_mfma_f32_16x16x32_bf16 v[56:59], v[112:115], v[20:23], v[56:59]\n"
    "v_mfma_f32_16x16x32_bf16 v[60:63], v[116:119], v[20:23], v[60:63]\n"
    "s_waitcnt lgkmcnt(6)\n"
    "v_mfma_f32_16x16x32_bf16 v[68:71], v[120:123], v[20:23], v[68:71]\n"
    "ds_read_b128 v[20:23], v10 offset:9216\n"
    "v_mfma_f32_16x16x32_bf16 v[48:51], v[124:127], v[24:27], v[48:51]\n"
    "v_mfma_f32_16x16x32_bf16 v[52:55], v[128:131], v[24:27], v[52:55]\n"
    "s_waitcnt lgkmcnt(6)\n"
    ".LgDOWN_q152_%=:\n"
    "v_mfma_f32_16x16x32_bf16 v[64:67], v[132:135], v[24:27], v[64:67]\n"
    "ds_read_b128 v[24:27], v10 offset:10240\n"
    "v_mfma_f32_16x16x32_bf16 v[56:59], v[124:127], v[28:31], v[56:59]\n"
    "v_mfma_f32_16x16x32_bf16 v[60:63], v[128:131], v[28:31], v[60:63]\n"
    "s_waitcnt lgkmcnt(6)\n"
    "v_mfma_f32_16x16x32_bf16 v[68:71], v[132:135], v[28:31], v[68:71]\n"
    "ds_read_b128 v[28:31], v10 offset:11264\n"
    "v_mfma_f32_16x16x32_bf16 v[48:51], v[136:139], v[32:35], v[48:51]\n"
    "v_mfma_f32_16x16x32_bf16 v[52:55], v[140:143], v[32:35], v[52:55]\n"
    "s_waitcnt lgkmcnt(6)\n"
    "v_mfma_f32_16x16x32_bf16 v[64:67], v[144:147], v[32:35], v[64:67]\n"
    "ds_read_b128 v[32:35], v10 offset:12288\n"
    "v_mfma_f32_16x16x32_bf16 v[56:59], v[136:139], v[36:39], v[56:59]\n"
    "v_mfma_f32_16x16x32_bf16 v[60:63], v[140:143], v[36:39], v[60:63]\n"
    "s_waitcnt lgkmcnt(6)\n"
    "v_mfma_f32_16x16x32_bf16 v[68:71], v[144:147], v[36:39], v[68:71]\n"
    "ds_read_b128 v[36:39], v10 offset:13312\n"
    "v_mfma_f32_16x16x32_bf16 v[48:51], v[148:151], v[40:43], v[48:51]\n"
    "v_mfma_f32_16x16x32_bf16 v[52:55], v[152:155], v[40:43], v[52:55]\n"
    "s_waitcnt lgkmcnt(6)\n"
    "v_mfma_f32_16x16x32_bf16 v[64:67], v[156:159], v[40:43], v[64:67]\n"
    "ds_read_b128 v[40:43], v10 offset:14336\n"
    "v_mfma_f32_16x16x32_bf16 v[56:59], v[148:151], v[44:47], v[56:59]\n"
    "v_mfma_f32_16x16x32_bf16 v[60:63], v[152:155], v[44:47], v[60:63]\n"
    "s_waitcnt lgkmcnt(6)\n"
    "v_mfma_f32_16x16x32_bf16 v[68:71], v[156:159], v[44:47], v[68:71]\n"
    "ds_read_b128 v[44:47], v10 offset:15360\n"
    "v_mfma_f32_16x16x32_bf16 v[48:51], v[160:163], v[16:19], v[48:51]\n"
    "v_mfma_f32_16x16x32_bf16 v[52:55], v[164:167], v[16:19], v[52:55]\n"
    "s_waitcnt lgkmcnt(6)\n"
    "v_mfma_f32_16x16x32_bf16 v[64:67], v[168:171], v[16:19], v[64:67]\n"
    "ds_read_b128 v[16:19], v10 offset:16384\n"
    "v_mfma_f32_16x16x32_bf16 v[56:59], v[160:163], v[20:23], v[56:59]\n"
    ".LgDOWN_q172_%=:\n"
    "v_mfma_f32_16x16x32_bf16 v[60:63], v[164:167], v[20:23], v[60:63]\n"
    "s_waitcnt lgkmcnt(6)\n"
    "v_mov_b32 v12, s68\n"
    "v_mfma_f32_16x16x32_bf16 v[68:71], v[168:171], v[20:23], v[68:71]\n"
    "ds_read_b128 v[20:23], v10 offset:17408\n"
    "v_mfma_f32_16x16x32_bf16 v[48:51], v[172:175], v[24:27], v[48:51]\n"
    "s_mov_b32 s68, s71\n"
    "v_mfma_f32_16x16x32_bf16 v[52:55], v[176:179], v[24:27], v[52:55]\n"
    "s_waitcnt lgkmcnt(6)\n"
    "s_add_u32 s71, s68, 1\n"
    "v_mfma_f32_16x16x32_bf16 v[64:67], v[180:183], v[24:27], v[64:67]\n"
    "ds_read_b128 v[24:27], v10 offset:18432\n"
    "v_mfma_f32_16x16x32_bf16 v[56:59], v[172:175], v[28:31], v[56:59]\n"
    "v_mfma_f32_16x16x32_bf16 v[60:63], v[176:179], v[28:31], v[60:63]\n"
    "s_waitcnt lgkmcnt(6)\n"
    "v_mfma_f32_16x16x32_bf16 v[68:71], v[180:183], v[28:31], v[68:71]\n"
    "ds_read_b128 v[28:31], v10 offset:19456\n"
    "v_mfma_f32_16x16x32_bf16 v[48:51], v[184:187], v[32:35], v[48:51]\n"
    "v_mfma_f32_16x16x32_bf16 v[52:55], v[188:191], v[32:35], v[52:55]\n"
    "s_waitcnt lgkmcnt(6)\n"
    "v_mfma_f32_16x16x32_bf16 v[64:67], v[192:195], v[32:35], v[64:67]\n"
    "ds_read_b128 v[32:35], v10 offset:20480\n"
    "v_mfma_f32_16x16x32_bf16 v[56:59], v[184:187], v[36:39], v[56:59]\n"
    "v_mfma_f32_16x16x32_bf16 v[60:63], v[188:191], v[36:39], v[60:63]\n"
    "s_waitcnt lgkmcnt(6)\n"
    "v_mfma_f32_16x16x32_bf16 v[68:71], v[192:195], v[36:39], v[68:71]\n"
    "ds_read_b128 v[36:39], v10 offset:21504\n"
    "v_mfma_f32_16x16x32_bf16 v[48:51], v[196:199], v[40:43], v[48:51]\n"
    "v_mfma_f32_16x16x32_bf16 v[52:55], v[200:203], v[40:43], v[52:55]\n"
    "s_waitcnt lgkmcnt(6)\n"
    "v_mfma_f32_16x16x32_bf16 v[64:67], v[204:207], v[40:43], v[64:67]\n"
    "ds_read_b128 v[40:43], v10 offset:22528\n"
    "v_mfma_f32_16x16x32_bf16 v[56:59], v[196:199], v[44:47], v[56:59]\n"
    "v_mfma_f32_16x16x32_bf16 v[60:63], v[200:203], v[44:47], v[60:63]\n"
    "s_waitcnt lgkmcnt(6)\n"
    "v_mfma_f32_16x16x32_bf16 v[68:71], v[204:207], v[44:47], v[68:71]\n"
    "ds_read_b128 v[44:47], v10 offset:23552\n"
    "v_mfma_f32_16x16x32_bf16 v[64:67], v[216:219], v[16:19], v[64:67]\n"
    "s_waitcnt lgkmcnt(6)\n"
    "ds_read_b128 v[16:19], v10 offset:16384\n"
    "v_mfma_f32_16x16x32_bf16 v[68:71], v[216:219], v[20:23], v[68:71]\n"
    "s_waitcnt lgkmcnt(6)\n"
    "ds_read_b128 v[20:23], v10 offset:18432\n"
    "v_mfma_f32_16x16x32_bf16 v[64:67], v[228:231], v[24:27], v[64:67]\n"
    "s_waitcnt lgkmcnt(6)\n"
    "ds_read_b128 v[24:27], v10 offset:20480\n"
    "v_mfma_f32_16x16x32_bf16 v[68:71], v[228:231], v[28:31], v[68:71]\n"
    "s_waitcnt lgkmcnt(6)\n"
    "ds_read_b128 v[28:31], v10 offset:22528\n"
    "v_mfma_f32_16x16x32_bf16 v[64:67], v[240:243], v[32:35], v[64:67]\n"
    "s_waitcnt lgkmcnt(6)\n"
    "ds_read_b128 v[32:35], v10 offset:17408\n"
    "v_mfma_f32_16x16x32_bf16 v[68:71], v[240:243], v[36:39], v[68:71]\n"
    "s_waitcnt lgkmcnt(6)\n"
    "ds_read_b128 v[36:39], v10 offset:19456\n"
    "v_mfma_f32_16x16x32_bf16 v[64:67], v[252:255], v[40:43], v[64:67]\n"
    "s_waitcnt lgkmcnt(6)\n"
    "ds_read_b128 v[40:43], v10 offset:21504\n"
    "v_mfma_f32_16x16x32_bf16 v[68:71], v[252:255], v[44:47], v[68:71]\n"
    "s_waitcnt lgkmcnt(6)\n"
    "ds_read_b128 v[44:47], v10 offset:23552\n"
    "v_mfma_f32_16x16x32_bf16 v[48:51], v[208:211], v[16:19], v[48:51]\n"
    "v_mfma_f32_16x16x32_bf16 v[52:55], v[212:215], v[16:19], v[52:55]\n"
    "s_waitcnt lgkmcnt(6)\n"
    "v_cvt_pk_bf16_f32 v64, v64, v65\n"
    "v_cvt_pk_bf16_f32 v65, v66, v67\n"
    "v_mfma_f32_16x16x32_bf16 v[48:51], v[220:223], v[20:23], v[48:51]\n"
    "s_waitcnt lgkmcnt(5)\n"
    "v_cvt_pk_bf16_f32 v66, v68, v69\n"
    "v_cvt_pk_bf16_f32 v67, v70, v71\n"
    "v_xor_b32 v9, s78, v9\n"
    "v_mfma_f32_16x16x32_bf16 v[52:55], v[224:227], v[20:23], v[52:55]\n"
    "s_waitcnt vmcnt(0) lgkmcnt(0)\n"
    "s_barrier\n"
    "ds_read_b128 v[16:19], v9 offset:0\n"
    "s_nop 0\n"
    "v_mfma_f32_16x16x32_bf16 v[48:51], v[232:235], v[24:27], v[48:51]\n"
    "ds_read_b128 v[20:23], v9 offset:1024\n"
    "v_mfma_f32_16x16x32_bf16 v[52:55], v[236:239], v[24:27], v[52:55]\n"
    "ds_read_b128 v[24:27], v9 offset:2048\n"
    "v_mfma_f32_16x16x32_bf16 v[48:51], v[244:247], v[28:31], v[48:51]\n"
    "v_permlane16_swap_b32 v64, v66\n"
    "v_mfma_f32_16x16x32_bf16 v[52:55], v[248:251], v[28:31], v[52:55]\n"
    "ds_read_b128 v[28:31], v9 offset:3072\n"
    "v_mfma_f32_16x16x32_bf16 v[56:59], v[208:211], v[32:35], v[56:59]\n"
    "v_permlane16_swap_b32 v65, v67\n"
    "v_mfma_f32_16x16x32_bf16 v[60:63], v[212:215], v[32:35], v[60:63]\n"
    "ds_read_b128 v[32:35], v9 offset:4096\n"
    "v_cvt_pk_bf16_f32 v48, v48, v49\n"
    "v_mfma_f32_16x16x32_bf16 v[56:59], v[220:223], v[36:39], v[56:59]\n"
    "buffer_store_dwordx4 v[64:67], v4, s[36:39], s73 offen sc1\n"
    "v_cvt_pk_bf16_f32 v49, v50, v51\n"
    "v_mfma_f32_16x16x32_bf16 v[60:63], v[224:227], v[36:39], v[60:63]\n"
    "ds_read_b128 v[36:39], v9 offset:5120\n"
    "v_cvt_pk_bf16_f32 v50, v52, v53\n"
    "v_mfma_f32_16x16x32_bf16 v[56:59], v[232:235], v[40:43], v[56:59]\n"
    "v_cvt_pk_bf16_f32 v51, v54, v55\n"
    "v_mfma_f32_16x16x32_bf16 v[60:63], v[236:239], v[40:43], v[60:63]\n"
    "ds_read_b128 v[40:43], v9 offset:6144\n"
    "v_mfma_f32_16x16x32_bf16 v[56:59], v[244:247], v[44:47], v[56:59]\n"
    "s_waitcnt lgkmcnt(6)\n"
    "v_mfma_f32_16x16x32_bf16 v[60:63], v[248:251], v[44:47], v[60:63]\n"
    "ds_read_b128 v[44:47], v9 offset:7168\n"
    "buffer_store_dwordx4 v[48:51], v15, s[36:39], s73 offen sc1\n"
    "s_branch .LgDOWN_head_%=\n"
    ".LgDOWN_LOOPE_%=:\n"
    ".LgDOWN_done_%=:\n"
    "s_waitcnt vmcnt(0)\n"
    "buffer_atomic_smax v12, v14, s[36:39], 0 offen sc1\n"
    "s_waitcnt vmcnt(0)\n"
    "s_branch .LgDOWN_FIN_%=\n"
    ".LgDOWN_poll0_%=:\n"
    "v_mfma_f32_16x16x32_bf16 v[60:63], a[100:103], v[20:23], v[60:63]\n"
    "s_waitcnt lgkmcnt(6)\n"
    "s_or_b64 exec, vcc, 1\n"
    "buffer_atomic_smax v11, v2, s[36:39], 0 offen sc0 sc1\n"
    "s_mov_b64 exec, -1\n"
    "v_mfma_f32_16x16x32_bf16 v[68:71], a[104:107], v[20:23], v[68:71]\n"
    "ds_read_b128 v[20:23], v9 offset:25600\n"
    "v_mfma_f32_16x16x32_bf16 v[48:51], a[108:111], v[24:27], v[48:51]\n"
    "v_mfma_f32_16x16x32_bf16 v[52:55], a[112:115], v[24:27], v[52:55]\n"
    "s_waitcnt lgkmcnt(6)\n"
    "v_mfma_f32_16x16x32_bf16 v[64:67], a[116:119], v[24:27], v[64:67]\n"
    "ds_read_b128 v[24:27], v9 offset:26624\n"
    "v_mfma_f32_16x16x32_bf16 v[56:59], a[108:111], v[28:31], v[56:59]\n"
    "v_mfma_f32_16x16x32_bf16 v[60:63], a[112:115], v[28:31], v[60:63]\n"
    "s_waitcnt lgkmcnt(6)\n"
    "v_mfma_f32_16x16x32_bf16 v[68:71], a[116:119], v[28:31], v[68:71]\n"
    "ds_read_b128 v[28:31], v9 offset:27648\n"
    "v_mfma_f32_16x16x32_bf16 v[48:51], a[120:123], v[32:35], v[48:51]\n"
    "v_mfma_f32_16x16x32_bf16 v[52:55], a[124:127], v[32:35], v[52:55]\n"
    "s_waitcnt lgkmcnt(6)\n"
    "v_mfma_f32_16x16x32_bf16 v[64:67], a[128:131], v[32:35], v[64:67]\n"
    "ds_read_b128 v[32:35], v9 offset:28672\n"
    "v_mfma_f32_16x16x32_bf16 v[56:59], a[120:123], v[36:39], v[56:59]\n"
    "v_mfma_f32_16x16x32_bf16 v[60:63], a[124:127], v[36:39], v[60:63]\n"
    "s_waitcnt lgkmcnt(6)\n"
    "v_mfma_f32_16x16x32_bf16 v[68:71], a[128:131], v[36:39], v[68:71]\n"
    "ds_read_b128 v[36:39], v9 offset:29696\n"
    "v_mfma_f32_16x16x32_bf16 v[48:51], a[132:135], v[40:43], v[48:51]\n"
    "v_mfma_f32_16x16x32_bf16 v[52:55], a[136:139], v[40:43], v[52:55]\n"
    "s_waitcnt lgkmcnt(6)\n"
    "s_waitcnt vmcnt(2)\n"
    "v_add_u32 v13, v5, v3\n"
    "v_mfma_f32_16x16x32_bf16 v[64:67], a[140:143], v[40:43], v[64:67]\n"
    "ds_read_b128 v[40:43], v9 offset:30720\n"
    "v_cmp_gt_i32 vcc, s71, v13\n"
    "v_mfma_f32_16x16x32_bf16 v[56:59], a[132:135], v[44:47], v[56:59]\n"
    "v_mfma_f32_16x16x32_bf16 v[60:63], a[136:139], v[44:47], v[60:63]\n"
    "s_waitcnt lgkmcnt(6)\n"
    "v_mfma_f32_16x16x32_bf16 v[68:71], a[140:143], v[44:47], v[68:71]\n"
    "ds_read_b128 v[44:47], v9 offset:31744\n"
    "s_cbranch_vccz .LgDOWN_dma72_%=\n"
    "v_mfma_f32_16x16x32_bf16 v[48:51], a[144:147], v[16:19], v[48:51]\n"
    "s_or_b64 exec, vcc, 1\n"
    "buffer_atomic_smax v5, v2, s[36:39], 0 offen sc0 sc1\n"
    "s_mov_b64 exec, -1\n"
    "v_mfma_f32_16x16x32_bf16 v[52:55], a[148:151], v[16:19], v[52:55]\n"
    "s_waitcnt lgkmcnt(6)\n"
    "v_mfma_f32_16x16x32_bf16 v[64:67], a[152:155], v[16:19], v[64:67]\n"
    "ds_read_b128 v[16:19], v9 offset:32768\n"
    "v_mfma_f32_16x16x32_bf16 v[56:59], a[144:147], v[20:23], v[56:59]\n"
    "v_mfma_f32_16x16x32_bf16 v[60:63], a[148:151], v[20:23], v[60:63]\n"
    "s_waitcnt lgkmcnt(6)\n"
    "v_mfma_f32_16x16x32_bf16 v[68:71], a[152:155], v[20:23], v[68:71]\n"
    "ds_read_b128 v[20:23], v9 offset:33792\n"
    "v_mfma_f32_16x16x32_bf16 v[48:51], a[156:159], v[24:27], v[48:51]\n"
    "v_mfma_f32_16x16x32_bf16 v[52:55], a[160:163], v[24:27], v[52:55]\n"
    "s_waitcnt lgkmcnt(6)\n"
    "v_mfma_f32_16x16x32_bf16 v[64:67], a[164:167], v[24:27], v[64:67]\n"
    "ds_read_b128 v[24:27], v9 offset:34816\n"
    "v_mfma_f32_16x16x32_bf16 v[56:59], a[156:159], v[28:31], v[56:59]\n"
    "v_mfma_f32_16x16x32_bf16 v[60:63], a[160:163], v[28:31], v[60:63]\n"
    "s_waitcnt lgkmcnt(6)\n"
    "v_mfma_f32_16x16x32_bf16 v[68:71], a[164:167], v[28:31], v[68:71]\n"
    "ds_read_b128 v[28:31], v9 offset:35840\n"
    "v_mfma_f32_16x16x32_bf16 v[48:51], a[168:171], v[32:35], v[48:51]\n"
    "v_mfma_f32_16x16x32_bf16 v[52:55], a[172:175], v[32:35], v[52:55]\n"
    "s_waitcnt lgkmcnt(6)\n"
    "v_mfma_f32_16x16x32_bf16 v[64:67], a[176:179], v[32:35], v[64:67]\n"
    "ds_read_b128 v[32:35], v9 offset:36864\n"
    "v_mfma_f32_16x16x32_bf16 v[56:59], a[168:171], v[36:39], v[56:59]\n"
    "s_waitcnt vmcnt(1)\n"
    "v_add_u32 v13, v11, v3\n"
    "v_mfma_f32_16x16x32_bf16 v[60:63], a[172:175], v[36:39], v[60:63]\n"
    "s_waitcnt lgkmcnt(6)\n"
    "v_cmp_gt_i32 vcc, s71, v13\n"
    "v_mfma_f32_16x16x32_bf16 v[68:71], a[176:179], v[36:39], v[68:71]\n"
    "ds_read_b128 v[36:39], v9 offset:37888\n"
    "v_mfma_f32_16x16x32_bf16 v[48:51], a[180:183], v[40:43], v[48:51]\n"
    "v_mfma_f32_16x16x32_bf16 v[52:55], a[184:187], v[40:43], v[52:55]\n"
    "s_waitcnt lgkmcnt(6)\n"
    "s_cbranch_vccz .LgDOWN_dma92_%=\n"
    "v_mfma_f32_16x16x32_bf16 v[64:67], a[188:191], v[40:43], v[64:67]\n"
    "ds_read_b128 v[40:43], v9 offset:38912\n"
    "s_or_b64 exec, vcc, 1\n"
    "buffer_atomic_smax v11, v2, s[36:39], 0 offen sc0 sc1\n"
    "s_mov_b64 exec, -1\n"
    "v_mfma_f32_16x16x32_bf16 v[56:59], a[180:183], v[44:47], v[56:59]\n"
    "v_mfma_f32_16x16x32_bf16 v[60:63], a[184:187], v[44:47], v[60:63]\n"
    "s_waitcnt lgkmcnt(6)\n"
    "v_mfma_f32_16x16x32_bf16 v[68:71], a[188:191], v[44:47], v[68:71]\n"
    "ds_read_b128 v[44:47], v9 offset:39936\n"
    "v_mfma_f32_16x16x32_bf16 v[48:51], a[192:195], v[16:19], v[48:51]\n"
    "v_mfma_f32_16x16x32_bf16 v[52:55], a[196:199], v[16:19], v[52:55]\n"
    "s_waitcnt lgkmcnt(6)\n"
    "v_mfma_f32_16x16x32_bf16 v[64:67], a[200:203], v[16:19], v[64:67]\n"
    "ds_read_b128 v[16:19], v9 offset:40960\n"
    "v_mfma_f32_16x16x32_bf16 v[56:59], a[192:195], v[20:23], v[56:59]\n"
    "v_mfma_f32_16x16x32_bf16 v[60:63], a[196:199], v[20:23], v[60:63]\n"
    "s_waitcnt lgkmcnt(6)\n"
    "v_mfma_f32_16x16x32_bf16 v[68:71], a[200:203], v[20:23], v[68:71]\n"
    "ds_read_b128 v[20:23], v9 offset:41984\n"
    "v_mfma_f32_16x16x32_bf16 v[48:51], a[204:207], v[24:27], v[48:51]\n"
    "v_mfma_f32_16x16x32_bf16 v[52:55], a[208:211], v[24:27], v[52:55]\n"
    "s_waitcnt lgkmcnt(6)\n"
    "v_mfma_f32_16x16x32_bf16 v[64:67], a[212:215], v[24:27], v[64:67]\n"
    "ds_read_b128 v[24:27], v9 offset:43008\n"
    "v_mfma_f32_16x16x32_bf16 v[56:59], a[204:207], v[28:31], v[56:59]\n"
    "v_mfma_f32_16x16x32_bf16 v[60:63], a[208:211], v[28:31], v[60:63]\n"
    "s_waitcnt lgkmcnt(6)\n"
    "v_mfma_f32_16x16x32_bf16 v[68:71], a[212:215], v[28:31], v[68:71]\n"
    "ds_read_b128 v[28:31], v9 offset:44032\n"
    "s_waitcnt vmcnt(1)\n"
    "v_add_u32 v13, v5, v3\n"
    "v_mfma_f32_16x16x32_bf16 v[48:51], a[216:219], v[32:35], v[48:51]\n"
    "v_cmp_gt_i32 vcc, s71, v13\n"
    "v_mfma_f32_16x16x32_bf16 v[52:55], a[220:223], v[32:35], v[52:55]\n"
    "s_waitcnt lgkmcnt(6)\n"
    "v_mfma_f32_16x16x32_bf16 v[64:67], a[224:227], v[32:35], v[64:67]\n"
    "ds_read_b128 v[32:35], v9 offset:45056\n"
    "v_mfma_f32_16x16x32_bf16 v[56:59], a[216:219], v[36:39], v[56:59]\n"
    "s_cbranch_vccz .LgDOWN_dma112_%=\n"
    "v_mfma_f32_16x16x32_bf16 v[60:63], a[220:223], v[36:39], v[60:63]\n"
    "s_waitcnt lgkmcnt(6)\n"
    "s_or_b64 exec, vcc, 1\n"
    "buffer_atomic_smax v5, v2, s[36:39], 0 offen sc0 sc1\n"
    "s_mov_b64 exec, -1\n"
    "v_mfma_f32_16x16x32_bf16 v[68:71], a[224:227], v[36:39], v[68:71]\n"
    "ds_read_b128 v[36:39], v9 offset:46080\n"
    "v_mfma_f32_16x16x32_bf16 v[48:51], a[228:231], v[40:43], v[48:51]\n"
    "v_mfma_f32_16x16x32_bf16 v[52:55], a[232:235], v[40:43], v[52:55]\n"
    "s_waitcnt lgkmcnt(6)\n"
    "v_mfma_f32_16x16x32_bf16 v[64:67], a[236:239], v[40:43], v[64:67]\n"
    "ds_read_b128 v[40:43], v9 offset:47104\n"
    "v_mfma_f32_16x16x32_bf16 v[56:59], a[228:231], v[44:47], v[56:59]\n"
    "v_mfma_f32_16x16x32_bf16 v[60:63], a[232:235], v[44:47], v[60:63]\n"
    "s_waitcnt lgkmcnt(6)\n"
    "v_mfma_f32_16x16x32_bf16 v[68:71], a[236:239], v[44:47], v[68:71]\n"
    "ds_read_b128 v[44:47], v9 offset:48128\n"
    "v_mfma_f32_16x16x32_bf16 v[48:51], a[240:243], v[16:19], v[48:51]\n"
    "v_mfma_f32_16x16x32_bf16 v[52:55], a[244:247], v[16:19], v[52:55]\n"
    "s_waitcnt lgkmcnt(6)\n"
    "v_mfma_f32_16x16x32_bf16 v[64:67], a[248:251], v[16:19], v[64:67]\n"
    "ds_read_b128 v[16:19], v10 offset:0\n"
    "v_mfma_f32_16x16x32_bf16 v[56:59], a[240:243], v[20:23], v[56:59]\n"
    "v_mfma_f32_16x16x32_bf16 v[60:63], a[244:247], v[20:23], v[60:63]\n"
    "s_waitcnt lgkmcnt(6)\n"
    "v_mfma_f32_16x16x32_bf16 v[68:71], a[248:251], v[20:23], v[68:71]\n"
    "ds_read_b128 v[20:23], v10 offset:1024\n"
    "v_mfma_f32_16x16x32_bf16 v[48:51], a[252:255], v[24:27], v[48:51]\n"
    "v_mfma_f32_16x16x32_bf16 v[52:55], v[80:83], v[24:27], v[52:55]\n"
    "s_waitcnt lgkmcnt(6)\n"
    "s_waitcnt vmcnt(1)\n"
    "v_add_u32 v13, v11, v3\n"
    "v_mfma_f32_16x16x32_bf16 v[64:67], v[84:87], v[24:27], v[64:67]\n"
    "ds_read_b128 v[24:27], v10 offset:2048\n"
    "v_cmp_gt_i32 vcc, s71, v13\n"
    "v_mfma_f32_16x16x32_bf16 v[56:59], a[252:255], v[28:31], v[56:59]\n"
    "v_mfma_f32_16x16x32_bf16 v[60:63], v[80:83], v[28:31], v[60:63]\n"
    "s_waitcnt lgkmcnt(6)\n"
    "v_mfma_f32_16x16x32_bf16 v[68:71], v[84:87], v[28:31], v[68:71]\n"
    "ds_read_b128 v[28:31], v10 offset:3072\n"
    "s_cbranch_vccz .LgDOWN_dma132_%=\n"
    "v_mfma_f32_16x16x32_bf16 v[48:51], v[88:91], v[32:35], v[48:51]\n"
    "s_or_b64 exec, vcc, 1\n"
    "buffer_atomic_smax v11, v2, s[36:39], 0 offen sc0 sc1\n"
    "s_mov_b64 exec, -1\n"
    "v_mfma_f32_16x16x32_bf16 v[52:55], v[92:95], v[32:35], v[52:55]\n"
    "s_waitcnt lgkmcnt(6)\n"
    "v_mfma_f32_16x16x32_bf16 v[64:67], v[96:99], v[32:35], v[64:67]\n"
    "ds_read_b128 v[32:35], v10 offset:4096\n"
    "v_mfma_f32_16x16x32_bf16 v[56:59], v[88:91], v[36:39], v[56:59]\n"
    "v_mfma_f32_16x16x32_bf16 v[60:63], v[92:95], v[36:39], v[60:63]\n"
    "s_waitcnt lgkmcnt(6)\n"
    "v_mfma_f32_16x16x32_bf16 v[68:71], v[96:99], v[36:39], v[68:71]\n"
    "ds_read_b128 v[36:39], v10 offset:5120\n"
    "v_mfma_f32_16x16x32_bf16 v[48:51], v[100:103], v[40:43], v[48:51]\n"
    "v_mfma_f32_16x16x32_bf16 v[52:55], v[104:107], v[40:43], v[52:55]\n"
    "s_waitcnt lgkmcnt(6)\n"
    "v_mfma_f32_16x16x32_bf16 v[64:67], v[108:111], v[40:43], v[64:67]\n"
    "ds_read_b128 v[40:43], v10 offset:6144\n"
    "v_mfma_f32_16x16x32_bf16 v[56:59], v[100:103], v[44:47], v[56:59]\n"
    "v_mfma_f32_16x16x32_bf16 v[60:63], v[104:107], v[44:47], v[60:63]\n"
    "s_waitcnt lgkmcnt(6)\n"
    "v_mfma_f32_16x16x32_bf16 v[68:71], v[108:111], v[44:47], v[68:71]\n"
    "ds_read_b128 v[44:47], v10 offset:7168\n"
    "v_mfma_f32_16x16x32_bf16 v[48:51], v[112:115], v[16:19], v[48:51]\n"
    "v_mfma_f32_16x16x32_bf16 v[52:55], v[116:119], v[16:19], v[52:55]\n"
    "s_waitcnt lgkmcnt(6)\n"
    "v_mfma_f32_16x16x32_bf16 v[64:67], v[120:123], v[16:19], v[64:67]\n"
    "ds_read_b128 v[16:19], v10 offset:8192\n"
    "v_mfma_f32_16x16x32_bf16 v[56:59], v[112:115], v[20:23], v[56:59]\n"
    "s_waitcnt vmcnt(1)\n"
    "v_add_u32 v13, v5, v3\n"
    "v_mfma_f32_16x16x32_bf16 v[60:63], v[116:119], v[20:23], v[60:63]\n"
    "s_waitcnt lgkmcnt(6)\n"
    "v_cmp_gt_i32 vcc, s71, v13\n"
    "v_mfma_f32_16x16x32_bf16 v[68:71], v[120:123], v[20:23], v[68:71]\n"
    "ds_read_b128 v[20:23], v10 offset:9216\n"
    "v_mfma_f32_16x16x32_bf16 v[48:51], v[124:127], v[24:27], v[48:51]\n"
    "v_mfma_f32_16x16x32_bf16 v[52:55], v[128:131], v[24:27], v[52:55]\n"
    "s_waitcnt lgkmcnt(6)\n"
    "s_cbranch_vccz .LgDOWN_dma152_%=\n"
    "v_mfma_f32_16x16x32_bf16 v[64:67], v[132:135], v[24:27], v[64:67]\n"
    "ds_read_b128 v[24:27], v10 offset:10240\n"
    "s_or_b64 exec, vcc, 1\n"
    "buffer_atomic_smax v5, v2, s[36:39], 0 offen sc0 sc1\n"
    "s_mov_b64 exec, -1\n"
    "v_mfma_f32_16x16x32_bf16 v[56:59], v[124:127], v[28:31], v[56:59]\n"
    "v_mfma_f32_16x16x32_bf16 v[60:63], v[128:131], v[28:31], v[60:63]\n"
    "s_waitcnt lgkmcnt(6)\n"
    "v_mfma_f32_16x16x32_bf16 v[68:71], v[132:135], v[28:31], v[68:71]\n"
    "ds_read_b128 v[28:31], v10 offset:11264\n"
    "v_mfma_f32_16x16x32_bf16 v[48:51], v[136:139], v[32:35], v[48:51]\n"
    "v_mfma_f32_16x16x32_bf16 v[52:55], v[140:143], v[32:35], v[52:55]\n"
    "s_waitcnt lgkmcnt(6)\n"
    "v_mfma_f32_16x16x32_bf16 v[64:67], v[144:147], v[32:35], v[64:67]\n"
    "ds_read_b128 v[32:35], v10 offset:12288\n"
    "v_mfma_f32_16x16x32_bf16 v[56:59], v[136:139], v[36:39], v[56:59]\n"
    "v_mfma_f32_16x16x32_bf16 v[60:63], v[140:143], v[36:39], v[60:63]\n"
    "s_waitcnt lgkmcnt(6)\n"
    "v_mfma_f32_16x16x32_bf16 v[68:71], v[144:147], v[36:39], v[68:71]\n"
    "ds_read_b128 v[36:39], v10 offset:13312\n"
    "v_mfma_f32_16x16x32_bf16 v[48:51], v[148:151], v[40:43], v[48:51]\n"
    "v_mfma_f32_16x16x32_bf16 v[52:55], v[152:155], v[40:43], v[52:55]\n"
    "s_waitcnt lgkmcnt(6)\n"
    "v_mfma_f32_16x16x32_bf16 v[64:67], v[156:159], v[40:43], v[64:67]\n"
    "ds_read_b128 v[40:43], v10 offset:14336\n"
    "v_mfma_f32_16x16x32_bf16 v[56:59], v[148:151], v[44:47], v[56:59]\n"
    "v_mfma_f32_16x16x32_bf16 v[60:63], v[152:155], v[44:47], v[60:63]\n"
    "s_waitcnt lgkmcnt(6)\n"
    "v_mfma_f32_16x16x32_bf16 v[68:71], v[156:159], v[44:47], v[68:71]\n"
    "ds_read_b128 v[44:47], v10 offset:15360\n"
    "s_waitcnt vmcnt(1)\n"
    "v_add_u32 v13, v11, v3\n"
    "v_mfma_f32_16x16x32_bf16 v[48:51], v[160:163], v[16:19], v[48:51]\n"
    "v_cmp_gt_i32 vcc, s71, v13\n"
    "v_mfma_f32_16x16x32_bf16 v[52:55], v[164:167], v[16:19], v[52:55]\n"
    "s_waitcnt lgkmcnt(6)\n"
    "v_mfma_f32_16x16x32_bf16 v[64:67], v[168:171], v[16:19], v[64:67]\n"
    "ds_read_b128 v[16:19], v10 offset:16384\n"
    "v_mfma_f32_16x16x32_bf16 v[56:59], v[160:163], v[20:23], v[56:59]\n"
    "s_cbranch_vccz .LgDOWN_dma172_%=\n"
    "v_mfma_f32_16x16x32_bf16 v[60:63], v[164:167], v[20:23], v[60:63]\n"
    "s_waitcnt lgkmcnt(6)\n"
    "s_or_b64 exec, vcc, 1\n"
    "buffer_atomic_smax v11, v2, s[36:39], 0 offen sc0 sc1\n"
    "s_mov_b64 exec, -1\n"
    "v_mfma_f32_16x16x32_bf16 v[68:71], v[168:171], v[20:23], v[68:71]\n"
    "ds_read_b128 v[20:23], v10 offset:17408\n"
    "v_mfma_f32_16x16x32_bf16 v[48:51], v[172:175], v[24:27], v[48:51]\n"
    "v_mfma_f32_16x16x32_bf16 v[52:55], v[176:179], v[24:27], v[52:55]\n"
    "s_waitcnt lgkmcnt(6)\n"
    "v_mfma_f32_16x16x32_bf16 v[64:67], v[180:183], v[24:27], v[64:67]\n"
    "ds_read_b128 v[24:27], v10 offset:18432\n"
    "v_mfma_f32_16x16x32_bf16 v[56:59], v[172:175], v[28:31], v[56:59]\n"
    "v_mfma_f32_16x16x32_bf16 v[60:63], v[176:179], v[28:31], v[60:63]\n"
    "s_waitcnt lgkmcnt(6)\n"
    "v_mfma_f32_16x16x32_bf16 v[68:71], v[180:183], v[28:31], v[68:71]\n"
    "ds_read_b128 v[28:31], v10 offset:19456\n"
    "v_mfma_f32_16x16x32_bf16 v[48:51], v[184:187], v[32:35], v[48:51]\n"
    "v_mfma_f32_16x16x32_bf16 v[52:55], v[188:191], v[32:35], v[52:55]\n"
    "s_waitcnt lgkmcnt(6)\n"
    "v_mfma_f32_16x16x32_bf16 v[64:67], v[192:195], v[32:35], v[64:67]\n"
    "ds_read_b128 v[32:35], v10 offset:20480\n"
    "v_mfma_f32_16x16x32_bf16 v[56:59], v[184:187], v[36:39], v[56:59]\n"
    "v_mfma_f32_16x16x32_bf16 v[60:63], v[188:191], v[36:39], v[60:63]\n"
    "s_waitcnt lgkmcnt(6)\n"
    "v_mfma_f32_16x16x32_bf16 v[68:71], v[192:195], v[36:39], v[68:71]\n"
    "ds_read_b128 v[36:39], v10 offset:21504\n"
    "v_mfma_f32_16x16x32_bf16 v[48:51], v[196:199], v[40:43], v[48:51]\n"
    "v_mfma_f32_16x16x32_bf16 v[52:55], v[200:203], v[40:43], v[52:55]\n"
    "s_waitcnt lgkmcnt(6)\n"
    "s_waitcnt vmcnt(1)\n"
    "v_add_u32 v13, v5, v3\n"
    "v_mfma_f32_16x16x32_bf16 v[64:67], v[204:207], v[40:43], v[64:67]\n"
    "ds_read_b128 v[40:43], v10 offset:22528\n"
    "v_cmp_gt_i32 vcc, s71, v13\n"
    "v_mfma_f32_16x16x32_bf16 v[56:59], v[196:199], v[44:47], v[56:59]\n"
    "v_mfma_f32_16x16x32_bf16 v[60:63], v[200:203], v[44:47], v[60:63]\n"
    "s_waitcnt lgkmcnt(6)\n"
    "v_mfma_f32_16x16x32_bf16 v[68:71], v[204:207], v[44:47], v[68:71]\n"
    "ds_read_b128 v[44:47], v10 offset:23552\n"
    "s_cbranch_vccz .LgDOWN_dma192_%=\n"
    "v_mfma_f32_16x16x32_bf16 v[64:67], v[216:219], v[16:19], v[64:67]\n"
    "s_waitcnt lgkmcnt(6)\n"
    "ds_read_b128 v[16:19], v10 offset:16384\n"
    "s_or_b64 exec, vcc, 1\n"
    "buffer_atomic_smax v5, v2, s[36:39], 0 offen sc0 sc1\n"
    "s_mov_b64 exec, -1\n"
    "v_mfma_f32_16x16x32_bf16 v[68:71], v[216:219], v[20:23], v[68:71]\n"
    "s_waitcnt lgkmcnt(6)\n"
    "ds_read_b128 v[20:23], v10 offset:18432\n"
    "v_mfma_f32_16x16x32_bf16 v[64:67], v[228:231], v[24:27], v[64:67]\n"
    "s_waitcnt lgkmcnt(6)\n"
    "ds_read_b128 v[24:27], v10 offset:20480\n"
    "v_mfma_f32_16x16x32_bf16 v[68:71], v[228:231], v[28:31], v[68:71]\n"
    "s_waitcnt lgkmcnt(6)\n"
    "ds_read_b128 v[28:31], v10 offset:22528\n"
    "v_mfma_f32_16x16x32_bf16 v[64:67], v[240:243], v[32:35], v[64:67]\n"
    "s_waitcnt lgkmcnt(6)\n"
    "ds_read_b128 v[32:35], v10 offset:17408\n"
    "v_mfma_f32_16x16x32_bf16 v[68:71], v[240:243], v[36:39], v[68:71]\n"
    "s_waitcnt lgkmcnt(6)\n"
    "ds_read_b128 v[36:39], v10 offset:19456\n"
    "v_mfma_f32_16x16x32_bf16 v[64:67], v[252:255], v[40:43], v[64:67]\n"
    "s_waitcnt lgkmcnt(6)\n"
    "ds_read_b128 v[40:43], v10 offset:21504\n"
    "v_mfma_f32_16x16x32_bf16 v[68:71], v[252:255], v[44:47], v[68:71]\n"
    "s_waitcnt lgkmcnt(6)\n"
    "ds_read_b128 v[44:47], v10 offset:23552\n"
    "v_mfma_f32_16x16x32_bf16 v[48:51], v[208:211], v[16:19], v[48:51]\n"
    "v_mfma_f32_16x16x32_bf16 v[52:55], v[212:215], v[16:19], v[52:55]\n"
    "s_waitcnt lgkmcnt(6)\n"
    "v_cvt_pk_bf16_f32 v64, v64, v65\n"
    "v_cvt_pk_bf16_f32 v65, v66, v67\n"
    "v_mfma_f32_16x16x32_bf16 v[48:51], v[220:223], v[20:23], v[48:51]\n"
    "s_waitcnt lgkmcnt(5)\n"
    "v_cvt_pk_bf16_f32 v66, v68, v69\n"
    "v_cvt_pk_bf16_f32 v67, v70, v71\n"
    "v_mfma_f32_16x16x32_bf16 v[52:55], v[224:227], v[20:23], v[52:55]\n"
    "s_nop 0\n"
    "v_mfma_f32_16x16x32_bf16 v[48:51], v[232:235], v[24:27], v[48:51]\n"
    "v_mfma_f32_16x16x32_bf16 v[52:55], v[236:239], v[24:27], v[52:55]\n"
    "s_waitcnt lgkmcnt(4)\n"
    "v_mfma_f32_16x16x32_bf16 v[48:51], v[244:247], v[28:31], v[48:51]\n"
    "s_waitcnt lgkmcnt(3)\n"
    "v_permlane16_swap_b32 v64, v66\n"
    "v_mfma_f32_16x16x32_bf16 v[52:55], v[248:251], v[28:31], v[52:55]\n"
    "v_mfma_f32_16x16x32_bf16 v[56:59], v[208:211], v[32:35], v[56:59]\n"
    "s_waitcnt lgkmcnt(2)\n"
    "v_permlane16_swap_b32 v65, v67\n"
    "v_mfma_f32_16x16x32_bf16 v[60:63], v[212:215], v[32:35], v[60:63]\n"
    "v_cvt_pk_bf16_f32 v48, v48, v49\n"
    "v_mfma_f32_16x16x32_bf16 v[56:59], v[220:223], v[36:39], v[56:59]\n"
    "s_waitcnt lgkmcnt(1)\n"
    "buffer_store_dwordx4 v[64:67], v4, s[36:39], s73 offen sc1\n"
    "v_cvt_pk_bf16_f32 v49, v50, v51\n"
    "v_mfma_f32_16x16x32_bf16 v[60:63], v[224:227], v[36:39], v[60:63]\n"
    "v_cvt_pk_bf16_f32 v50, v52, v53\n"
    "v_mfma_f32_16x16x32_bf16 v[56:59], v[232:235], v[40:43], v[56:59]\n"
    "s_waitcnt lgkmcnt(0)\n"
    "v_cvt_pk_bf16_f32 v51, v54, v55\n"
    "v_mfma_f32_16x16x32_bf16 v[60:63], v[236:239], v[40:43], v[60:63]\n"
    "v_mfma_f32_16x16x32_bf16 v[56:59], v[244:247], v[44:47], v[56:59]\n"
    "v_mfma_f32_16x16x32_bf16 v[60:63], v[248:251], v[44:47], v[60:63]\n"
    "buffer_store_dwordx4 v[48:51], v15, s[36:39], s73 offen sc1\n"
    "s_nop 2\n"
    "v_cvt_pk_bf16_f32 v56, v56, v57\n"
    "v_cvt_pk_bf16_f32 v57, v58, v59\n"
    "s_nop 1\n"
    "v_cvt_pk_bf16_f32 v58, v60, v61\n"
    "v_cvt_pk_bf16_f32 v59, v62, v63\n"
    "buffer_store_dwordx4 v[56:59], v15, s[36:39], s73 offen offset:1024 sc1\n"
    "v_mov_b32 v12, s68\n"
    "s_mov_b32 s68, s71\n"
    "s_cmp_eq_u32 s68, 64\n"
    "s_cbranch_scc1 .LgDOWN_done_%=\n"
    "s_waitcnt vmcnt(4)\n"
    "v_add_u32 v13, v11, v3\n"
    "v_cmp_gt_i32 vcc, s71, v13\n"
    "s_cbranch_vccnz .LgDOWN_idle_%=\n"
    "s_add_u32 m0, s77, s59\n"
    "s_add_u32 s77, s77, 8192\n"
    "buffer_load_dwordx4 v1, s[36:39], s76 offen sc1 lds\n"
    "buffer_load_dwordx4 v1, s[36:39], s76 offen offset:1024 sc1 lds\n"
    "s_add_u32 s76, s76, 16384\n"
    "s_add_u32 m0, s77, s59\n"
    "s_add_u32 s77, s77, 8192\n"
    "buffer_load_dwordx4 v1, s[36:39], s76 offen sc1 lds\n"
    "buffer_load_dwordx4 v1, s[36:39], s76 offen offset:1024 sc1 lds\n"
    "s_add_u32 s76, s76, 16384\n"
    "s_add_u32 m0, s77, s59\n"
    "s_add_u32 s77, s77, 8192\n"
    "buffer_load_dwordx4 v1, s[36:39], s76 offen sc1 lds\n"
    "buffer_load_dwordx4 v1, s[36:39], s76 offen offset:1024 sc1 lds\n"
    "s_add_u32 s76, s76, 16384\n"
    "s_add_u32 m0, s77, s59\n"
    "s_add_u32 s77, s77, 8192\n"
    "buffer_load_dwordx4 v1, s[36:39], s76 offen sc1 lds\n"
    "buffer_load_dwordx4 v1, s[36:39], s76 offen offset:1024 sc1 lds\n"
    "s_add_u32 s76, s76, 16384\n"
    "s_add_u32 m0, s77, s59\n"
    "s_add_u32 s77, s77, 8192\n"
    "buffer_load_dwordx4 v1, s[36:39], s76 offen sc1 lds\n"
    "buffer_load_dwordx4 v1, s[36:39], s76 offen offset:1024 sc1 lds\n"
    "s_add_u32 s76, s76, 16384\n"
    "s_add_u32 m0, s77, s59\n"
    "s_add_u32 s77, s77, 8192\n"
    "buffer_load_dwordx4 v1, s[36:39], s76 offen sc1 lds\n"
    "buffer_load_dwordx4 v1, s[36:39], s76 offen offset:1024 sc1 lds\n"
    "s_add_u32 s76, s76, 16384\n"
    "s_add_u32 m0, s77, s59\n"
    "s_add_u32 s77, s77, 8192\n"
    "buffer_load_dwordx4 v1, s[36:39], s76 offen sc1 lds\n"
    "buffer_load_dwordx4 v1, s[36:39], s76 offen offset:1024 sc1 lds\n"
    "s_add_u32 s76, s76, 16384\n"
    "s_add_u32 m0, s77, s59\n"
    "s_add_u32 s77, s77, 8192\n"
    "buffer_load_dwordx4 v1, s[36:39], s76 offen sc1 lds\n"
    "buffer_load_dwordx4 v1, s[36:39], s76 offen offset:1024 sc1 lds\n"
    "s_add_u32 s76, s76, 16384\n"
    "s_add_u32 m0, s77, s59\n"
    "s_add_u32 s77, s77, 8192\n"
    "buffer_load_dwordx4 v1, s[36:39], s76 offen sc1 lds\n"
    "buffer_load_dwordx4 v1, s[36:39], s76 offen offset:1024 sc1 lds\n"
    "s_add_u32 s76, s76, 16384\n"
    ".LgDOWN_late_%=:\n"
    "s_waitcnt vmcnt(18)\n"
    "buffer_atomic_smax v12, v14, s[36:39], 0 offen sc1\n"
    "s_waitcnt vmcnt(1)\n"
    ".LgDOWN_bar_%=:\n"
    "v_xor_b32 v9, s78, v9\n"
    "s_add_u32 s71, s68, 1\n"
    "s_barrier\n"
    "ds_read_b128 v[16:19], v9 offset:0\n"
    "ds_read_b128 v[20:23], v9 offset:1024\n"
    "ds_read_b128 v[24:27], v9 offset:2048\n"
    "ds_read_b128 v[28:31], v9 offset:3072\n"
    "ds_read_b128 v[32:35], v9 offset:4096\n"
    "ds_read_b128 v[36:39], v9 offset:5120\n"
    "ds_read_b128 v[40:43], v9 offset:6144\n"
    "ds_read_b128 v[44:47], v9 offset:7168\n"
    "v_add_u32 v13, v11, v3\n"
    "v_cmp_gt_i32 vcc, s71, v13\n"
    "s_nop 0\n"
    "s_or_b64 exec, vcc, 1\n"
    "buffer_atomic_smax v11, v2, s[36:39], 0 offen sc0 sc1\n"
    "s_mov_b64 exec, -1\n"
    "s_waitcnt lgkmcnt(7)\n"
    "v_mfma_f32_16x16x32_bf16 v[64:67], a[8:11], v[16:19], 0\n"
    "s_waitcnt lgkmcnt(6)\n"
    "ds_read_b128 v[16:19], v9 offset:0\n"
    "v_mfma_f32_16x16x32_bf16 v[68:71], a[8:11], v[20:23], 0\n"
    "s_waitcnt lgkmcnt(6)\n"
    "ds_read_b128 v[20:23], v9 offset:2048\n"
    "v_mfma_f32_16x16x32_bf16 v[64:67], a[20:23], v[24:27], v[64:67]\n"
    "s_waitcnt lgkmcnt(6)\n"
    "ds_read_b128 v[24:27], v9 offset:4096\n"
    "v_mfma_f32_16x16x32_bf16 v[68:71], a[20:23], v[28:31], v[68:71]\n"
    "s_waitcnt lgkmcnt(6)\n"
    "ds_read_b128 v[28:31], v9 offset:6144\n"
    "v_mfma_f32_16x16x32_bf16 v[64:67], a[32:35], v[32:35], v[64:67]\n"
    "s_waitcnt lgkmcnt(6)\n"
    "ds_read_b128 v[32:35], v9 offset:1024\n"
    "v_mfma_f32_16x16x32_bf16 v[68:71], a[32:35], v[36:39], v[68:71]\n"
    "s_waitcnt lgkmcnt(6)\n"
    "ds_read_b128 v[36:39], v9 offset:3072\n"
    "v_mfma_f32_16x16x32_bf16 v[64:67], a[44:47], v[40:43], v[64:67]\n"
    "s_waitcnt lgkmcnt(6)\n"
    "ds_read_b128 v[40:43], v9 offset:5120\n"
    "v_mfma_f32_16x16x32_bf16 v[68:71], a[44:47], v[44:47], v[68:71]\n"
    "s_waitcnt lgkmcnt(6)\n"
    "ds_read_b128 v[44:47], v9 offset:7168\n"
    "v_mfma_f32_16x16x32_bf16 v[48:51], a[0:3], v[16:19], 0\n"
    "s_waitcnt lgkmcnt(6)\n"
    "v_mfma_f32_16x16x32_bf16 v[52:55], a[4:7], v[16:19], 0\n"
    "ds_read_b128 v[16:19], v9 offset:8192\n"
    "v_mfma_f32_16x16x32_bf16 v[48:51], a[12:15], v[20:23], v[48:51]\n"
    "s_waitcnt lgkmcnt(6)\n"
    "s_waitcnt vmcnt(1)\n"
    "buffer_atomic_smax v12, v14, s[36:39], 0 offen sc1\n"
    "v_mfma_f32_16x16x32_bf16 v[52:55], a[16:19], v[20:23], v[52:55]\n"
    "ds_read_b128 v[20:23], v9 offset:9216\n"
    "v_mfma_f32_16x16x32_bf16 v[48:51], a[24:27], v[24:27], v[48:51]\n"
    "s_waitcnt lgkmcnt(6)\n"
    "v_mfma_f32_16x16x32_bf16 v[52:55], a[28:31], v[24:27], v[52:55]\n"
    "ds_read_b128 v[24:27], v9 offset:10240\n"
    "v_mfma_f32_16x16x32_bf16 v[48:51], a[36:39], v[28:31], v[48:51]\n"
    "s_waitcnt lgkmcnt(6)\n"
    "s_and_b32 s76, s71, s54\n"
    "v_mfma_f32_16x16x32_bf16 v[52:55], a[40:43], v[28:31], v[52:55]\n"
    "ds_read_b128 v[28:31], v9 offset:11264\n"
    "v_mfma_f32_16x16x32_bf16 v[56:59], a[0:3], v[32:35], 0\n"
    "s_waitcnt lgkmcnt(6)\n"
    "s_mul_i32 s76, s76, s53\n"
    "v_mfma_f32_16x16x32_bf16 v[60:63], a[4:7], v[32:35], 0\n"
    "ds_read_b128 v[32:35], v9 offset:12288\n"
    "v_mfma_f32_16x16x32_bf16 v[56:59], a[12:15], v[36:39], v[56:59]\n"
    "s_waitcnt lgkmcnt(6)\n"
    "s_add_u32 s76, s76, s52\n"
    "v_mfma_f32_16x16x32_bf16 v[60:63], a[16:19], v[36:39], v[60:63]\n"
    "ds_read_b128 v[36:39], v9 offset:13312\n"
    "v_mfma_f32_16x16x32_bf16 v[56:59], a[24:27], v[40:43], v[56:59]\n"
    "s_waitcnt lgkmcnt(6)\n"
    "s_and_b32 s77, s71, 1\n"
    "v_mfma_f32_16x16x32_bf16 v[60:63], a[28:31], v[40:43], v[60:63]\n"
    "ds_read_b128 v[40:43], v9 offset:14336\n"
    "v_mfma_f32_16x16x32_bf16 v[56:59], a[36:39], v[44:47], v[56:59]\n"
    "s_waitcnt lgkmcnt(6)\n"
    "s_mul_i32 s77, s77, 81920\n"
    "v_mfma_f32_16x16x32_bf16 v[60:63], a[40:43], v[44:47], v[60:63]\n"
    "ds_read_b128 v[44:47], v9 offset:15360\n"
    "v_mfma_f32_16x16x32_bf16 v[48:51], a[48:51], v[16:19], v[48:51]\n"
    "s_add_u32 s77, s77, s59\n"
    "v_mfma_f32_16x16x32_bf16 v[52:55], a[52:55], v[16:19], v[52:55]\n"
    "s_waitcnt lgkmcnt(6)\n"
    "s_and_b32 s73, s68, s57\n"
    "v_mfma_f32_16x16x32_bf16 v[64:67], a[56:59], v[16:19], v[64:67]\n"
    "ds_read_b128 v[16:19], v9 offset:16384\n"
    "v_mfma_f32_16x16x32_bf16 v[56:59], a[48:51], v[20:23], v[56:59]\n"
    "s_mul_i32 s73, s73, s56\n"
    "v_mfma_f32_16x16x32_bf16 v[60:63], a[52:55], v[20:23], v[60:63]\n"
    "s_waitcnt lgkmcnt(6)\n"
    "s_add_u32 s73, s73, s55\n"
    "v_mfma_f32_16x16x32_bf16 v[68:71], a[56:59], v[20:23], v[68:71]\n"
    "ds_read_b128 v[20:23], v9 offset:17408\n"
    "v_add_u32 v13, v5, v3\n"
    "v_mfma_f32_16x16x32_bf16 v[48:51], a[60:63], v[24:27], v[48:51]\n"
    "v_cmp_gt_i32 vcc, s71, v13\n"
    "v_mfma_f32_16x16x32_bf16 v[52:55], a[64:67], v[24:27], v[52:55]\n"
    "s_waitcnt lgkmcnt(6)\n"
    "v_mfma_f32_16x16x32_bf16 v[64:67], a[68:71], v[24:27], v[64:67]\n"
    "ds_read_b128 v[24:27], v9 offset:18432\n"
    "s_or_b64 exec, vcc, 1\n"
    "buffer_atomic_smax v5, v2, s[36:39], 0 offen sc0 sc1\n"
    "s_mov_b64 exec, -1\n"
    "v_mfma_f32_16x16x32_bf16 v[56:59], a[60:63], v[28:31], v[56:59]\n"
    "v_mfma_f32_16x16x32_bf16 v[60:63], a[64:67], v[28:31], v[60:63]\n"
    "s_waitcnt lgkmcnt(6)\n"
    "v_mfma_f32_16x16x32_bf16 v[68:71], a[68:71], v[28:31], v[68:71]\n"
    "ds_read_b128 v[28:31], v9 offset:19456\n"
    "v_mfma_f32_16x16x32_bf16 v[48:51], a[72:75], v[32:35], v[48:51]\n"
    "v_mfma_f32_16x16x32_bf16 v[52:55], a[76:79], v[32:35], v[52:55]\n"
    "s_waitcnt lgkmcnt(6)\n"
    "v_mfma_f32_16x16x32_bf16 v[64:67], a[80:83], v[32:35], v[64:67]\n"
    "ds_read_b128 v[32:35], v9 offset:20480\n"
    "v_mfma_f32_16x16x32_bf16 v[56:59], a[72:75], v[36:39], v[56:59]\n"
    "v_mfma_f32_16x16x32_bf16 v[60:63], a[76:79], v[36:39], v[60:63]\n"
    "s_waitcnt lgkmcnt(6)\n"
    "v_mfma_f32_16x16x32_bf16 v[68:71], a[80:83], v[36:39], v[68:71]\n"
    "ds_read_b128 v[36:39], v9 offset:21504\n"
    "v_mfma_f32_16x16x32_bf16 v[48:51], a[84:87], v[40:43], v[48:51]\n"
    "v_mfma_f32_16x16x32_bf16 v[52:55], a[88:91], v[40:43], v[52:55]\n"
    "s_waitcnt lgkmcnt(6)\n"
    "v_mfma_f32_16x16x32_bf16 v[64:67], a[92:95], v[40:43], v[64:67]\n"
    "ds_read_b128 v[40:43], v9 offset:22528\n"
    "v_mfma_f32_16x16x32_bf16 v[56:59], a[84:87], v[44:47], v[56:59]\n"
    "v_mfma_f32_16x16x32_bf16 v[60:63], a[88:91], v[44:47], v[60:63]\n"
    "s_waitcnt lgkmcnt(6)\n"
    "v_mfma_f32_16x16x32_bf16 v[68:71], a[92:95], v[44:47], v[68:71]\n"
    "ds_read_b128 v[44:47], v9 offset:23552\n"
    "s_waitcnt vmcnt(1)\n"
    "v_add_u32 v13, v11, v3\n"
    "v_mfma_f32_16x16x32_bf16 v[48:51], a[96:99], v[16:19], v[48:51]\n"
    "buffer_atomic_smax v12, v14, s[36:39], 0 offen sc1\n"
    "v_cmp_gt_i32 vcc, s71, v13\n"
    "v_mfma_f32_16x16x32_bf16 v[52:55], a[100:103], v[16:19], v[52:55]\n"
    "s_waitcnt lgkmcnt(6)\n"
    "v_add_u32 v10, 49152, v9\n"
    "v_mfma_f32_16x16x32_bf16 v[64:67], a[104:107], v[16:19], v[64:67]\n"
    "ds_read_b128 v[16:19], v9 offset:24576\n"
    "v_mfma_f32_16x16x32_bf16 v[56:59], a[96:99], v[20:23], v[56:59]\n"
    "s_branch .LgDOWN_mid_%=\n"
    ".LgDOWN_dma72_%=:\n"
    "v_mfma_f32_16x16x32_bf16 v[48:51], a[144:147], v[16:19], v[48:51]\n"
    "s_add_u32 m0, s77, s59\n"
    "s_add_u32 s77, s77, 8192\n"
    "buffer_load_dwordx4 v1, s[36:39], s76 offen sc1 lds\n"
    "v_mfma_f32_16x16x32_bf16 v[52:55], a[148:151], v[16:19], v[52:55]\n"
    "s_waitcnt lgkmcnt(6)\n"
    "v_mfma_f32_16x16x32_bf16 v[64:67], a[152:155], v[16:19], v[64:67]\n"
    "ds_read_b128 v[16:19], v9 offset:32768\n"
    "v_mfma_f32_16x16x32_bf16 v[56:59], a[144:147], v[20:23], v[56:59]\n"
    "buffer_load_dwordx4 v1, s[36:39], s76 offen offset:1024 sc1 lds\n"
    "s_add_u32 s76, s76, 16384\n"
    "v_mfma_f32_16x16x32_bf16 v[60:63], a[148:151], v[20:23], v[60:63]\n"
    "s_waitcnt lgkmcnt(6)\n"
    "v_mfma_f32_16x16x32_bf16 v[68:71], a[152:155], v[20:23], v[68:71]\n"
    "ds_read_b128 v[20:23], v9 offset:33792\n"
    "v_mfma_f32_16x16x32_bf16 v[48:51], a[156:159], v[24:27], v[48:51]\n"
    "s_add_u32 m0, s77, s59\n"
    "s_add_u32 s77, s77, 8192\n"
    "buffer_load_dwordx4 v1, s[36:39], s76 offen sc1 lds\n"
    "v_mfma_f32_16x16x32_bf16 v[52:55], a[160:163], v[24:27], v[52:55]\n"
    "s_waitcnt lgkmcnt(6)\n"
    "v_mfma_f32_16x16x32_bf16 v[64:67], a[164:167], v[24:27], v[64:67]\n"
    "ds_read_b128 v[24:27], v9 offset:34816\n"
    "v_mfma_f32_16x16x32_bf16 v[56:59], a[156:159], v[28:31], v[56:59]\n"
    "buffer_load_dwordx4 v1, s[36:39], s76 offen offset:1024 sc1 lds\n"
    "s_add_u32 s76, s76, 16384\n"
    "v_mfma_f32_16x16x32_bf16 v[60:63], a[160:163], v[28:31], v[60:63]\n"
    "s_waitcnt lgkmcnt(6)\n"
    "v_mfma_f32_16x16x32_bf16 v[68:71], a[164:167], v[28:31], v[68:71]\n"
    "ds_read_b128 v[28:31], v9 offset:35840\n"
    "v_mfma_f32_16x16x32_bf16 v[48:51], a[168:171], v[32:35], v[48:51]\n"
    "s_add_u32 m0, s77, s59\n"
    "s_add_u32 s77, s77, 8192\n"
    "buffer_load_dwordx4 v1, s[36:39], s76 offen sc1 lds\n"
    "v_mfma_f32_16x16x32_bf16 v[52:55], a[172:175], v[32:35], v[52:55]\n"
    "s_waitcnt lgkmcnt(6)\n"
    "v_mfma_f32_16x16x32_bf16 v[64:67], a[176:179], v[32:35], v[64:67]\n"
    "ds_read_b128 v[32:35], v9 offset:36864\n"
    "v_mfma_f32_16x16x32_bf16 v[56:59], a[168:171], v[36:39], v[56:59]\n"
    "v_mfma_f32_16x16x32_bf16 v[60:63], a[172:175], v[36:39], v[60:63]\n"
    "s_waitcnt lgkmcnt(6)\n"
    "buffer_load_dwordx4 v1, s[36:39], s76 offen offset:1024 sc1 lds\n"
    "s_add_u32 s76, s76, 16384\n"
    "v_mfma_f32_16x16x32_bf16 v[68:71], a[176:179], v[36:39], v[68:71]\n"
    "ds_read_b128 v[36:39], v9 offset:37888\n"
    "v_mfma_f32_16x16x32_bf16 v[48:51], a[180:183], v[40:43], v[48:51]\n"
    "v_mfma_f32_16x16x32_bf16 v[52:55], a[184:187], v[40:43], v[52:55]\n"
    "s_waitcnt lgkmcnt(6)\n"
    "s_add_u32 m0, s77, s59\n"
    "s_add_u32 s77, s77, 8192\n"
    "buffer_load_dwordx4 v1, s[36:39], s76 offen sc1 lds\n"
    "v_mfma_f32_16x16x32_bf16 v[64:67], a[188:191], v[40:43], v[64:67]\n"
    "ds_read_b128 v[40:43], v9 offset:38912\n"
    "v_mfma_f32_16x16x32_bf16 v[56:59], a[180:183], v[44:47], v[56:59]\n"
    "v_mfma_f32_16x16x32_bf16 v[60:63], a[184:187], v[44:47], v[60:63]\n"
    "s_waitcnt lgkmcnt(6)\n"
    "buffer_load_dwordx4 v1, s[36:39], s76 offen offset:1024 sc1 lds\n"
    "s_add_u32 s76, s76, 16384\n"
    "v_mfma_f32_16x16x32_bf16 v[68:71], a[188:191], v[44:47], v[68:71]\n"
    "ds_read_b128 v[44:47], v9 offset:39936\n"
    "v_mfma_f32_16x16x32_bf16 v[48:51], a[192:195], v[16:19], v[48:51]\n"
    "v_mfma_f32_16x16x32_bf16 v[52:55], a[196:199], v[16:19], v[52:55]\n"
    "s_waitcnt lgkmcnt(6)\n"
    "s_add_u32 m0, s77, s59\n"
    "s_add_u32 s77, s77, 8192\n"
    "buffer_load_dwordx4 v1, s[36:39], s76 offen sc1 lds\n"
    "v_mfma_f32_16x16x32_bf16 v[64:67], a[200:203], v[16:19], v[64:67]\n"
    "ds_read_b128 v[16:19], v9 offset:40960\n"
    "v_mfma_f32_16x16x32_bf16 v[56:59], a[192:195], v[20:23], v[56:59]\n"
    "v_mfma_f32_16x16x32_bf16 v[60:63], a[196:199], v[20:23], v[60:63]\n"
    "s_waitcnt lgkmcnt(6)\n"
    "v_mfma_f32_16x16x32_bf16 v[68:71], a[200:203], v[20:23], v[68:71]\n"
    "ds_read_b128 v[20:23], v9 offset:41984\n"
    "v_mfma_f32_16x16x32_bf16 v[48:51], a[204:207], v[24:27], v[48:51]\n"
    "buffer_load_dwordx4 v1, s[36:39], s76 offen offset:1024 sc1 lds\n"
    "s_add_u32 s76, s76, 16384\n"
    "v_mfma_f32_16x16x32_bf16 v[52:55], a[208:211], v[24:27], v[52:55]\n"
    "s_waitcnt lgkmcnt(6)\n"
    "v_mfma_f32_16x16x32_bf16 v[64:67], a[212:215], v[24:27], v[64:67]\n"
    "ds_read_b128 v[24:27], v9 offset:43008\n"
    "v_mfma_f32_16x16x32_bf16 v[56:59], a[204:207], v[28:31], v[56:59]\n"
    "s_add_u32 m0, s77, s59\n"
    "s_add_u32 s77, s77, 8192\n"
    "buffer_load_dwordx4 v1, s[36:39], s76 offen sc1 lds\n"
    "v_mfma_f32_16x16x32_bf16 v[60:63], a[208:211], v[28:31], v[60:63]\n"
    "s_waitcnt lgkmcnt(6)\n"
    "v_mfma_f32_16x16x32_bf16 v[68:71], a[212:215], v[28:31], v[68:71]\n"
    "ds_read_b128 v[28:31], v9 offset:44032\n"
    "v_mfma_f32_16x16x32_bf16 v[48:51], a[216:219], v[32:35], v[48:51]\n"
    "buffer_load_dwordx4 v1, s[36:39], s76 offen offset:1024 sc1 lds\n"
    "s_add_u32 s76, s76, 16384\n"
    "v_mfma_f32_16x16x32_bf16 v[52:55], a[220:223], v[32:35], v[52:55]\n"
    "s_waitcnt lgkmcnt(6)\n"
    "v_mfma_f32_16x16x32_bf16 v[64:67], a[224:227], v[32:35], v[64:67]\n"
    "ds_read_b128 v[32:35], v9 offset:45056\n"
    "v_mfma_f32_16x16x32_bf16 v[56:59], a[216:219], v[36:39], v[56:59]\n"
    "s_add_u32 m0, s77, s59\n"
    "s_add_u32 s77, s77, 8192\n"
    "buffer_load_dwordx4 v1, s[36:39], s76 offen sc1 lds\n"
    "v_mfma_f32_16x16x32_bf16 v[60:63], a[220:223], v[36:39], v[60:63]\n"
    "s_waitcnt lgkmcnt(6)\n"
    "v_mfma_f32_16x16x32_bf16 v[68:71], a[224:227], v[36:39], v[68:71]\n"
    "ds_read_b128 v[36:39], v9 offset:46080\n"
    "v_mfma_f32_16x16x32_bf16 v[48:51], a[228:231], v[40:43], v[48:51]\n"
    "buffer_load_dwordx4 v1, s[36:39], s76 offen offset:1024 sc1 lds\n"
    "s_add_u32 s76, s76, 16384\n"
    "v_mfma_f32_16x16x32_bf16 v[52:55], a[232:235], v[40:43], v[52:55]\n"
    "s_waitcnt lgkmcnt(6)\n"
    "v_mfma_f32_16x16x32_bf16 v[64:67], a[236:239], v[40:43], v[64:67]\n"
    "ds_read_b128 v[40:43], v9 offset:47104\n"
    "v_mfma_f32_16x16x32_bf16 v[56:59], a[228:231], v[44:47], v[56:59]\n"
    "v_mfma_f32_16x16x32_bf16 v[60:63], a[232:235], v[44:47], v[60:63]\n"
    "s_waitcnt lgkmcnt(6)\n"
    "s_add_u32 m0, s77, s59\n"
    "s_add_u32 s77, s77, 8192\n"
    "buffer_load_dwordx4 v1, s[36:39], s76 offen sc1 lds\n"
    "v_mfma_f32_16x16x32_bf16 v[68:71], a[236:239], v[44:47], v[68:71]\n"
    "ds_read_b128 v[44:47], v9 offset:48128\n"
    "v_mfma_f32_16x16x32_bf16 v[48:51], a[240:243], v[16:19], v[48:51]\n"
    "v_mfma_f32_16x16x32_bf16 v[52:55], a[244:247], v[16:19], v[52:55]\n"
    "s_waitcnt lgkmcnt(6)\n"
    "buffer_load_dwordx4 v1, s[36:39], s76 offen offset:1024 sc1 lds\n"
    "s_add_u32 s76, s76, 16384\n"
    "v_mfma_f32_16x16x32_bf16 v[64:67], a[248:251], v[16:19], v[64:67]\n"
    "ds_read_b128 v[16:19], v10 offset:0\n"
    "v_mfma_f32_16x16x32_bf16 v[56:59], a[240:243], v[20:23], v[56:59]\n"
    "v_mfma_f32_16x16x32_bf16 v[60:63], a[244:247], v[20:23], v[60:63]\n"
    "s_waitcnt lgkmcnt(6)\n"
    "s_add_u32 m0, s77, s59\n"
    "s_add_u32 s77, s77, 8192\n"
    "buffer_load_dwordx4 v1, s[36:39], s76 offen sc1 lds\n"
    "v_mfma_f32_16x16x32_bf16 v[68:71], a[248:251], v[20:23], v[68:71]\n"
    "ds_read_b128 v[20:23], v10 offset:1024\n"
    "v_mfma_f32_16x16x32_bf16 v[48:51], a[252:255], v[24:27], v[48:51]\n"
    "v_mfma_f32_16x16x32_bf16 v[52:55], v[80:83], v[24:27], v[52:55]\n"
    "s_waitcnt lgkmcnt(6)\n"
    "buffer_load_dwordx4 v1, s[36:39], s76 offen offset:1024 sc1 lds\n"
    "s_add_u32 s76, s76, 16384\n"
    "v_mfma_f32_16x16x32_bf16 v[64:67], v[84:87], v[24:27], v[64:67]\n"
    "ds_read_b128 v[24:27], v10 offset:2048\n"
    "v_mfma_f32_16x16x32_bf16 v[56:59], a[252:255], v[28:31], v[56:59]\n"
    "v_mfma_f32_16x16x32_bf16 v[60:63], v[80:83], v[28:31], v[60:63]\n"
    "s_waitcnt lgkmcnt(6)\n"
    "v_mfma_f32_16x16x32_bf16 v[68:71], v[84:87], v[28:31], v[68:71]\n"
    "ds_read_b128 v[28:31], v10 offset:3072\n"
    "s_branch .LgDOWN_q132_%=\n"
    ".LgDOWN_dma92_%=:\n"
    "v_mfma_f32_16x16x32_bf16 v[64:67], a[188:191], v[40:43], v[64:67]\n"
    "ds_read_b128 v[40:43], v9 offset:38912\n"
    "v_mfma_f32_16x16x32_bf16 v[56:59], a[180:183], v[44:47], v[56:59]\n"
    "s_add_u32 m0, s77, s59\n"
    "s_add_u32 s77, s77, 8192\n"
    "buffer_load_dwordx4 v1, s[36:39], s76 offen sc1 lds\n"
    "v_mfma_f32_16x16x32_bf16 v[60:63], a[184:187], v[44:47], v[60:63]\n"
    "s_waitcnt lgkmcnt(6)\n"
    "v_mfma_f32_16x16x32_bf16 v[68:71], a[188:191], v[44:47], v[68:71]\n"
    "ds_read_b128 v[44:47], v9 offset:39936\n"
    "v_mfma_f32_16x16x32_bf16 v[48:51], a[192:195], v[16:19], v[48:51]\n"
    "buffer_load_dwordx4 v1, s[36:39], s76 offen offset:1024 sc1 lds\n"
    "s_add_u32 s76, s76, 16384\n"
    "v_mfma_f32_16x16x32_bf16 v[52:55], a[196:199], v[16:19], v[52:55]\n"
    "s_waitcnt lgkmcnt(6)\n"
    "v_mfma_f32_16x16x32_bf16 v[64:67], a[200:203], v[16:19], v[64:67]\n"
    "ds_read_b128 v[16:19], v9 offset:40960\n"
    "v_mfma_f32_16x16x32_bf16 v[56:59], a[192:195], v[20:23], v[56:59]\n"
    "s_add_u32 m0, s77, s59\n"
    "s_add_u32 s77, s77, 8192\n"
    "buffer_load_dwordx4 v1, s[36:39], s76 offen sc1 lds\n"
    "v_mfma_f32_16x16x32_bf16 v[60:63], a[196:199], v[20:23], v[60:63]\n"
    "s_waitcnt lgkmcnt(6)\n"
    "v_mfma_f32_16x16x32_bf16 v[68:71], a[200:203], v[20:23], v[68:71]\n"
    "ds_read_b128 v[20:23], v9 offset:41984\n"
    "v_mfma_f32_16x16x32_bf16 v[48:51], a[204:207], v[24:27], v[48:51]\n"
    "buffer_load_dwordx4 v1, s[36:39], s76 offen offset:1024 sc1 lds\n"
    "s_add_u32 s76, s76, 16384\n"
    "v_mfma_f32_16x16x32_bf16 v[52:55], a[208:211], v[24:27], v[52:55]\n"
    "s_waitcnt lgkmcnt(6)\n"
    "v_mfma_f32_16x16x32_bf16 v[64:67], a[212:215], v[24:27], v[64:67]\n"
    "ds_read_b128 v[24:27], v9 offset:43008\n"
    "v_mfma_f32_16x16x32_bf16 v[56:59], a[204:207], v[28:31], v[56:59]\n"
    "s_add_u32 m0, s77, s59\n"
    "s_add_u32 s77, s77, 8192\n"
    "buffer_load_dwordx4 v1, s[36:39], s76 offen sc1 lds\n"
    "v_mfma_f32_16x16x32_bf16 v[60:63], a[208:211], v[28:31], v[60:63]\n"
    "s_waitcnt lgkmcnt(6)\n"
    "v_mfma_f32_16x16x32_bf16 v[68:71], a[212:215], v[28:31], v[68:71]\n"
    "ds_read_b128 v[28:31], v9 offset:44032\n"
    "v_mfma_f32_16x16x32_bf16 v[48:51], a[216:219], v[32:35], v[48:51]\n"
    "buffer_load_dwordx4 v1, s[36:39], s76 offen offset:1024 sc1 lds\n"
    "s_add_u32 s76, s76, 16384\n"
    "v_mfma_f32_16x16x32_bf16 v[52:55], a[220:223], v[32:35], v[52:55]\n"
    "s_waitcnt lgkmcnt(6)\n"
    "v_mfma_f32_16x16x32_bf16 v[64:67], a[224:227], v[32:35], v[64:67]\n"
    "ds_read_b128 v[32:35], v9 offset:45056\n"
    "v_mfma_f32_16x16x32_bf16 v[56:59], a[216:219], v[36:39], v[56:59]\n"
    "v_mfma_f32_16x16x32_bf16 v[60:63], a[220:223], v[36:39], v[60:63]\n"
    "s_waitcnt lgkmcnt(6)\n"
    "s_add_u32 m0, s77, s59\n"
    "s_add_u32 s77, s77, 8192\n"
    "buffer_load_dwordx4 v1, s[36:39], s76 offen sc1 lds\n"
    "v_mfma_f32_16x16x32_bf16 v[68:71], a[224:227], v[36:39], v[68:71]\n"
    "ds_read_b128 v[36:39], v9 offset:46080\n"
    "v_mfma_f32_16x16x32_bf16 v[48:51], a[228:231], v[40:43], v[48:51]\n"
    "v_mfma_f32_16x16x32_bf16 v[52:55], a[232:235], v[40:43], v[52:55]\n"
    "s_waitcnt lgkmcnt(6)\n"
    "buffer_load_dwordx4 v1, s[36:39], s76 offen offset:1024 sc1 lds\n"
    "s_add_u32 s76, s76, 16384\n"
    "v_mfma_f32_16x16x32_bf16 v[64:67], a[236:239], v[40:43], v[64:67]\n"
    "ds_read_b128 v[40:43], v9 offset:47104\n"
    "v_mfma_f32_16x16x32_bf16 v[56:59], a[228:231], v[44:47], v[56:59]\n"
    "v_mfma_f32_16x16x32_bf16 v[60:63], a[232:235], v[44:47], v[60:63]\n"
    "s_waitcnt lgkmcnt(6)\n"
    "s_add_u32 m0, s77, s59\n"
    "s_add_u32 s77, s77, 8192\n"
    "buffer_load_dwordx4 v1, s[36:39], s76 offen sc1 lds\n"
    "v_mfma_f32_16x16x32_bf16 v[68:71], a[236:239], v[44:47], v[68:71]\n"
    "ds_read_b128 v[44:47], v9 offset:48128\n"
    "v_mfma_f32_16x16x32_bf16 v[48:51], a[240:243], v[16:19], v[48:51]\n"
    "v_mfma_f32_16x16x32_bf16 v[52:55], a[244:247], v[16:19], v[52:55]\n"
    "s_waitcnt lgkmcnt(6)\n"
    "buffer_load_dwordx4 v1, s[36:39], s76 offen offset:1024 sc1 lds\n"
    "s_add_u32 s76, s76, 16384\n"
    "v_mfma_f32_16x16x32_bf16 v[64:67], a[248:251], v[16:19], v[64:67]\n"
    "ds_read_b128 v[16:19], v10 offset:0\n"
    "v_mfma_f32_16x16x32_bf16 v[56:59], a[240:243], v[20:23], v[56:59]\n"
    "v_mfma_f32_16x16x32_bf16 v[60:63], a[244:247], v[20:23], v[60:63]\n"
    "s_waitcnt lgkmcnt(6)\n"
    "s_add_u32 m0, s77, s59\n"
    "s_add_u32 s77, s77, 8192\n"
    "buffer_load_dwordx4 v1, s[36:39], s76 offen sc1 lds\n"
    "v_mfma_f32_16x16x32_bf16 v[68:71], a[248:251], v[20:23], v[68:71]\n"
    "ds_read_b128 v[20:23], v10 offset:1024\n"
    "v_mfma_f32_16x16x32_bf16 v[48:51], a[252:255], v[24:27], v[48:51]\n"
    "v_mfma_f32_16x16x32_bf16 v[52:55], v[80:83], v[24:27], v[52:55]\n"
    "s_waitcnt lgkmcnt(6)\n"
    "buffer_load_dwordx4 v1, s[36:39], s76 offen offset:1024 sc1 lds\n"
    "s_add_u32 s76, s76, 16384\n"
    "v_mfma_f32_16x16x32_bf16 v[64:67], v[84:87], v[24:27], v[64:67]\n"
    "ds_read_b128 v[24:27], v10 offset:2048\n"
    "v_mfma_f32_16x16x32_bf16 v[56:59], a[252:255], v[28:31], v[56:59]\n"
    "v_mfma_f32_16x16x32_bf16 v[60:63], v[80:83], v[28:31], v[60:63]\n"
    "s_waitcnt lgkmcnt(6)\n"
    "v_mfma_f32_16x16x32_bf16 v[68:71], v[84:87], v[28:31], v[68:71]\n"
    "ds_read_b128 v[28:31], v10 offset:3072\n"
    "v_mfma_f32_16x16x32_bf16 v[48:51], v[88:91], v[32:35], v[48:51]\n"
    "s_add_u32 m0, s77, s59\n"
    "s_add_u32 s77, s77, 8192\n"
    "buffer_load_dwordx4 v1, s[36:39], s76 offen sc1 lds\n"
    "v_mfma_f32_16x16x32_bf16 v[52:55], v[92:95], v[32:35], v[52:55]\n"
    "s_waitcnt lgkmcnt(6)\n"
    "v_mfma_f32_16x16x32_bf16 v[64:67], v[96:99], v[32:35], v[64:67]\n"
    "ds_read_b128 v[32:35], v10 offset:4096\n"
    "v_mfma_f32_16x16x32_bf16 v[56:59], v[88:91], v[36:39], v[56:59]\n"
    "buffer_load_dwordx4 v1, s[36:39], s76 offen offset:1024 sc1 lds\n"
    "s_add_u32 s76, s76, 16384\n"
    "v_mfma_f32_16x16x32_bf16 v[60:63], v[92:95], v[36:39], v[60:63]\n"
    "s_waitcnt lgkmcnt(6)\n"
    "v_mfma_f32_16x16x32_bf16 v[68:71], v[96:99], v[36:39], v[68:71]\n"
    "ds_read_b128 v[36:39], v10 offset:5120\n"
    "v_mfma_f32_16x16x32_bf16 v[48:51], v[100:103], v[40:43], v[48:51]\n"
    "s_add_u32 m0, s77, s59\n"
    "s_add_u32 s77, s77, 8192\n"
    "buffer_load_dwordx4 v1, s[36:39], s76 offen sc1 lds\n"
    "v_mfma_f32_16x16x32_bf16 v[52:55], v[104:107], v[40:43], v[52:55]\n"
    "s_waitcnt lgkmcnt(6)\n"
    "v_mfma_f32_16x16x32_bf16 v[64:67], v[108:111], v[40:43], v[64:67]\n"
    "ds_read_b128 v[40:43], v10 offset:6144\n"
    "v_mfma_f32_16x16x32_bf16 v[56:59], v[100:103], v[44:47], v[56:59]\n"
    "buffer_load_dwordx4 v1, s[36:39], s76 offen offset:1024 sc1 lds\n"
    "s_add_u32 s76, s76, 16384\n"
    "v_mfma_f32_16x16x32_bf16 v[60:63], v[104:107], v[44:47], v[60:63]\n"
    "s_waitcnt lgkmcnt(6)\n"
    "v_mfma_f32_16x16x32_bf16 v[68:71], v[108:111], v[44:47], v[68:71]\n"
    "ds_read_b128 v[44:47], v10 offset:7168\n"
    "v_mfma_f32_16x16x32_bf16 v[48:51], v[112:115], v[16:19], v[48:51]\n"
    "s_add_u32 m0, s77, s59\n"
    "s_add_u32 s77, s77, 8192\n"
    "buffer_load_dwordx4 v1, s[36:39], s76 offen sc1 lds\n"
    "v_mfma_f32_16x16x32_bf16 v[52:55], v[116:119], v[16:19], v[52:55]\n"
    "s_waitcnt lgkmcnt(6)\n"
    "v_mfma_f32_16x16x32_bf16 v[64:67], v[120:123], v[16:19], v[64:67]\n"
    "ds_read_b128 v[16:19], v10 offset:8192\n"
    "v_mfma_f32_16x16x32_bf16 v[56:59], v[112:115], v[20:23], v[56:59]\n"
    "buffer_load_dwordx4 v1, s[36:39], s76 offen offset:1024 sc1 lds\n"
    "s_add_u32 s76, s76, 16384\n"
    "v_mfma_f32_16x16x32_bf16 v[60:63], v[116:119], v[20:23], v[60:63]\n"
    "s_waitcnt lgkmcnt(6)\n"
    "v_mfma_f32_16x16x32_bf16 v[68:71], v[120:123], v[20:23], v[68:71]\n"
    "ds_read_b128 v[20:23], v10 offset:9216\n"
    "v_mfma_f32_16x16x32_bf16 v[48:51], v[124:127], v[24:27], v[48:51]\n"
    "v_mfma_f32_16x16x32_bf16 v[52:55], v[128:131], v[24:27], v[52:55]\n"
    "s_waitcnt lgkmcnt(6)\n"
    "s_branch .LgDOWN_q152_%=\n"
    ".LgDOWN_dma112_%=:\n"
    "v_mfma_f32_16x16x32_bf16 v[60:63], a[220:223], v[36:39], v[60:63]\n"
    "s_waitcnt lgkmcnt(6)\n"
    "s_add_u32 m0, s77, s59\n"
    "s_add_u32 s77, s77, 8192\n"
    "buffer_load_dwordx4 v1, s[36:39], s76 offen sc1 lds\n"
    "v_mfma_f32_16x16x32_bf16 v[68:71], a[224:227], v[36:39], v[68:71]\n"
    "ds_read_b128 v[36:39], v9 offset:46080\n"
    "v_mfma_f32_16x16x32_bf16 v[48:51], a[228:231], v[40:43], v[48:51]\n"
    "v_mfma_f32_16x16x32_bf16 v[52:55], a[232:235], v[40:43], v[52:55]\n"
    "s_waitcnt lgkmcnt(6)\n"
    "buffer_load_dwordx4 v1, s[36:39], s76 offen offset:1024 sc1 lds\n"
    "s_add_u32 s76, s76, 16384\n"
    "v_mfma_f32_16x16x32_bf16 v[64:67], a[236:239], v[40:43], v[64:67]\n"
    "ds_read_b128 v[40:43], v9 offset:47104\n"
    "v_mfma_f32_16x16x32_bf16 v[56:59], a[228:231], v[44:47], v[56:59]\n"
    "v_mfma_f32_16x16x32_bf16 v[60:63], a[232:235], v[44:47], v[60:63]\n"
    "s_waitcnt lgkmcnt(6)\n"
    "s_add_u32 m0, s77, s59\n"
    "s_add_u32 s77, s77, 8192\n"
    "buffer_load_dwordx4 v1, s[36:39], s76 offen sc1 lds\n"
    "v_mfma_f32_16x16x32_bf16 v[68:71], a[236:239], v[44:47], v[68:71]\n"
    "ds_read_b128 v[44:47], v9 offset:48128\n"
    "v_mfma_f32_16x16x32_bf16 v[48:51], a[240:243], v[16:19], v[48:51]\n"
    "v_mfma_f32_16x16x32_bf16 v[52:55], a[244:247], v[16:19], v[52:55]\n"
    "s_waitcnt lgkmcnt(6)\n"
    "buffer_load_dwordx4 v1, s[36:39], s76 offen offset:1024 sc1 lds\n"
    "s_add_u32 s76, s76, 16384\n"
    "v_mfma_f32_16x16x32_bf16 v[64:67], a[248:251], v[16:19], v[64:67]\n"
    "ds_read_b128 v[16:19], v10 offset:0\n"
    "v_mfma_f32_16x16x32_bf16 v[56:59], a[240:243], v[20:23], v[56:59]\n"
    "v_mfma_f32_16x16x32_bf16 v[60:63], a[244:247], v[20:23], v[60:63]\n"
    "s_waitcnt lgkmcnt(6)\n"
    "s_add_u32 m0, s77, s59\n"
    "s_add_u32 s77, s77, 8192\n"
    "buffer_load_dwordx4 v1, s[36:39], s76 offen sc1 lds\n"
    "v_mfma_f32_16x16x32_bf16 v[68:71], a[248:251], v[20:23], v[68:71]\n"
    "ds_read_b128 v[20:23], v10 offset:1024\n"
    "v_mfma_f32_16x16x32_bf16 v[48:51], a[252:255], v[24:27], v[48:51]\n"
    "v_mfma_f32_16x16x32_bf16 v[52:55], v[80:83], v[24:27], v[52:55]\n"
    "s_waitcnt lgkmcnt(6)\n"
    "buffer_load_dwordx4 v1, s[36:39], s76 offen offset:1024 sc1 lds\n"
    "s_add_u32 s76, s76, 16384\n"
    "v_mfma_f32_16x16x32_bf16 v[64:67], v[84:87], v[24:27], v[64:67]\n"
    "ds_read_b128 v[24:27], v10 offset:2048\n"
    "v_mfma_f32_16x16x32_bf16 v[56:59], a[252:255], v[28:31], v[56:59]\n"
    "v_mfma_f32_16x16x32_bf16 v[60:63], v[80:83], v[28:31], v[60:63]\n"
    "s_waitcnt lgkmcnt(6)\n"
    "v_mfma_f32_16x16x32_bf16 v[68:71], v[84:87], v[28:31], v[68:71]\n"
    "ds_read_b128 v[28:31], v10 offset:3072\n"
    "v_mfma_f32_16x16x32_bf16 v[48:51], v[88:91], v[32:35], v[48:51]\n"
    "s_add_u32 m0, s77, s59\n"
    "s_add_u32 s77, s77, 8192\n"
    "buffer_load_dwordx4 v1, s[36:39], s76 offen sc1 lds\n"
    "v_mfma_f32_16x16x32_bf16 v[52:55], v[92:95], v[32:35], v[52:55]\n"
    "s_waitcnt lgkmcnt(6)\n"
    "v_mfma_f32_16x16x32_bf16 v[64:67], v[96:99], v[32:35], v[64:67]\n"
    "ds_read_b128 v[32:35], v10 offset:4096\n"
    "v_mfma_f32_16x16x32_bf16 v[56:59], v[88:91], v[36:39], v[56:59]\n"
    "buffer_load_dwordx4 v1, s[36:39], s76 offen offset:1024 sc1 lds\n"
    "s_add_u32 s76, s76, 16384\n"
    "v_mfma_f32_16x16x32_bf16 v[60:63], v[92:95], v[36:39], v[60:63]\n"
    "s_waitcnt lgkmcnt(6)\n"
    "v_mfma_f32_16x16x32_bf16 v[68:71], v[96:99], v[36:39], v[68:71]\n"
    "ds_read_b128 v[36:39], v10 offset:5120\n"
    "v_mfma_f32_16x16x32_bf16 v[48:51], v[100:103], v[40:43], v[48:51]\n"
    "s_add_u32 m0, s77, s59\n"
    "s_add_u32 s77, s77, 8192\n"
    "buffer_load_dwordx4 v1, s[36:39], s76 offen sc1 lds\n"
    "v_mfma_f32_16x16x32_bf16 v[52:55], v[104:107], v[40:43], v[52:55]\n"
    "s_waitcnt lgkmcnt(6)\n"
    "v_mfma_f32_16x16x32_bf16 v[64:67], v[108:111], v[40:43], v[64:67]\n"
    "ds_read_b128 v[40:43], v10 offset:6144\n"
    "v_mfma_f32_16x16x32_bf16 v[56:59], v[100:103], v[44:47], v[56:59]\n"
    "buffer_load_dwordx4 v1, s[36:39], s76 offen offset:1024 sc1 lds\n"
    "s_add_u32 s76, s76, 16384\n"
    "v_mfma_f32_16x16x32_bf16 v[60:63], v[104:107], v[44:47], v[60:63]\n"
    "s_waitcnt lgkmcnt(6)\n"
    "v_mfma_f32_16x16x32_bf16 v[68:71], v[108:111], v[44:47], v[68:71]\n"
    "ds_read_b128 v[44:47], v10 offset:7168\n"
    "v_mfma_f32_16x16x32_bf16 v[48:51], v[112:115], v[16:19], v[48:51]\n"
    "s_add_u32 m0, s77, s59\n"
    "s_add_u32 s77, s77, 8192\n"
    "buffer_load_dwordx4 v1, s[36:39], s76 offen sc1 lds\n"
    "v_mfma_f32_16x16x32_bf16 v[52:55], v[116:119], v[16:19], v[52:55]\n"
    "s_waitcnt lgkmcnt(6)\n"
    "v_mfma_f32_16x16x32_bf16 v[64:67], v[120:123], v[16:19], v[64:67]\n"
    "ds_read_b128 v[16:19], v10 offset:8192\n"
    "v_mfma_f32_16x16x32_bf16 v[56:59], v[112:115], v[20:23], v[56:59]\n"
    "buffer_load_dwordx4 v1, s[36:39], s76 offen offset:1024 sc1 lds\n"
    "s_add_u32 s76, s76, 16384\n"
    "v_mfma_f32_16x16x32_bf16 v[60:63], v[116:119], v[20:23], v[60:63]\n"
    "s_waitcnt lgkmcnt(6)\n"
    "v_mfma_f32_16x16x32_bf16 v[68:71], v[120:123], v[20:23], v[68:71]\n"
    "ds_read_b128 v[20:23], v10 offset:9216\n"
    "v_mfma_f32_16x16x32_bf16 v[48:51], v[124:127], v[24:27], v[48:51]\n"
    "v_mfma_f32_16x16x32_bf16 v[52:55], v[128:131], v[24:27], v[52:55]\n"
    "s_waitcnt lgkmcnt(6)\n"
    "s_add_u32 m0, s77, s59\n"
    "s_add_u32 s77, s77, 8192\n"
    "buffer_load_dwordx4 v1, s[36:39], s76 offen sc1 lds\n"
    "v_mfma_f32_16x16x32_bf16 v[64:67], v[132:135], v[24:27], v[64:67]\n"
    "ds_read_b128 v[24:27], v10 offset:10240\n"
    "v_mfma_f32_16x16x32_bf16 v[56:59], v[124:127], v[28:31], v[56:59]\n"
    "v_mfma_f32_16x16x32_bf16 v[60:63], v[128:131], v[28:31], v[60:63]\n"
    "s_waitcnt lgkmcnt(6)\n"
    "buffer_load_dwordx4 v1, s[36:39], s76 offen offset:1024 sc1 lds\n"
    "s_add_u32 s76, s76, 16384\n"
    "v_mfma_f32_16x16x32_bf16 v[68:71], v[132:135], v[28:31], v[68:71]\n"
    "ds_read_b128 v[28:31], v10 offset:11264\n"
    "v_mfma_f32_16x16x32_bf16 v[48:51], v[136:139], v[32:35], v[48:51]\n"
    "v_mfma_f32_16x16x32_bf16 v[52:55], v[140:143], v[32:35], v[52:55]\n"
    "s_waitcnt lgkmcnt(6)\n"
    "s_add_u32 m0, s77, s59\n"
    "s_add_u32 s77, s77, 8192\n"
    "buffer_load_dwordx4 v1, s[36:39], s76 offen sc1 lds\n"
    "v_mfma_f32_16x16x32_bf16 v[64:67], v[144:147], v[32:35], v[64:67]\n"
    "ds_read_b128 v[32:35], v10 offset:12288\n"
    "v_mfma_f32_16x16x32_bf16 v[56:59], v[136:139], v[36:39], v[56:59]\n"
    "v_mfma_f32_16x16x32_bf16 v[60:63], v[140:143], v[36:39], v[60:63]\n"
    "s_waitcnt lgkmcnt(6)\n"
    "buffer_load_dwordx4 v1, s[36:39], s76 offen offset:1024 sc1 lds\n"
    "s_add_u32 s76, s76, 16384\n"
    "v_mfma_f32_16x16x32_bf16 v[68:71], v[144:147], v[36:39], v[68:71]\n"
    "ds_read_b128 v[36:39], v10 offset:13312\n"
    "v_mfma_f32_16x16x32_bf16 v[48:51], v[148:151], v[40:43], v[48:51]\n"
    "v_mfma_f32_16x16x32_bf16 v[52:55], v[152:155], v[40:43], v[52:55]\n"
    "s_waitcnt lgkmcnt(6)\n"
    "s_add_u32 m0, s77, s59\n"
    "s_add_u32 s77, s77, 8192\n"
    "buffer_load_dwordx4 v1, s[36:39], s76 offen sc1 lds\n"
    "v_mfma_f32_16x16x32_bf16 v[64:67], v[156:159], v[40:43], v[64:67]\n"
    "ds_read_b128 v[40:43], v10 offset:14336\n"
    "v_mfma_f32_16x16x32_bf16 v[56:59], v[148:151], v[44:47], v[56:59]\n"
    "v_mfma_f32_16x16x32_bf16 v[60:63], v[152:155], v[44:47], v[60:63]\n"
    "s_waitcnt lgkmcnt(6)\n"
    "buffer_load_dwordx4 v1, s[36:39], s76 offen offset:1024 sc1 lds\n"
    "s_add_u32 s76, s76, 16384\n"
    "v_mfma_f32_16x16x32_bf16 v[68:71], v[156:159], v[44:47], v[68:71]\n"
    "ds_read_b128 v[44:47], v10 offset:15360\n"
    "v_mfma_f32_16x16x32_bf16 v[48:51], v[160:163], v[16:19], v[48:51]\n"
    "v_mfma_f32_16x16x32_bf16 v[52:55], v[164:167], v[16:19], v[52:55]\n"
    "s_waitcnt lgkmcnt(6)\n"
    "v_mfma_f32_16x16x32_bf16 v[64:67], v[168:171], v[16:19], v[64:67]\n"
    "ds_read_b128 v[16:19], v10 offset:16384\n"
    "v_mfma_f32_16x16x32_bf16 v[56:59], v[160:163], v[20:23], v[56:59]\n"
    "s_branch .LgDOWN_q172_%=\n"
    ".LgDOWN_dma132_%=:\n"
    "v_mfma_f32_16x16x32_bf16 v[48:51], v[88:91], v[32:35], v[48:51]\n"
    "s_add_u32 m0, s77, s59\n"
    "s_add_u32 s77, s77, 8192\n"
    "buffer_load_dwordx4 v1, s[36:39], s76 offen sc1 lds\n"
    "v_mfma_f32_16x16x32_bf16 v[52:55], v[92:95], v[32:35], v[52:55]\n"
    "s_waitcnt lgkmcnt(6)\n"
    "v_mfma_f32_16x16x32_bf16 v[64:67], v[96:99], v[32:35], v[64:67]\n"
    "ds_read_b128 v[32:35], v10 offset:4096\n"
    "v_mfma_f32_16x16x32_bf16 v[56:59], v[88:91], v[36:39], v[56:59]\n"
    "buffer_load_dwordx4 v1, s[36:39], s76 offen offset:1024 sc1 lds\n"
    "s_add_u32 s76, s76, 16384\n"
    "v_mfma_f32_16x16x32_bf16 v[60:63], v[92:95], v[36:39], v[60:63]\n"
    "s_waitcnt lgkmcnt(6)\n"
    "v_mfma_f32_16x16x32_bf16 v[68:71], v[96:99], v[36:39], v[68:71]\n"
    "ds_read_b128 v[36:39], v10 offset:5120\n"
    "v_mfma_f32_16x16x32_bf16 v[48:51], v[100:103], v[40:43], v[48:51]\n"
    "s_add_u32 m0, s77, s59\n"
    "s_add_u32 s77, s77, 8192\n"
    "buffer_load_dwordx4 v1, s[36:39], s76 offen sc1 lds\n"
    "v_mfma_f32_16x16x32_bf16 v[52:55], v[104:107], v[40:43], v[52:55]\n"
    "s_waitcnt lgkmcnt(6)\n"
    "v_mfma_f32_16x16x32_bf16 v[64:67], v[108:111], v[40:43], v[64:67]\n"
    "ds_read_b128 v[40:43], v10 offset:6144\n"
    "v_mfma_f32_16x16x32_bf16 v[56:59], v[100:103], v[44:47], v[56:59]\n"
    "buffer_load_dwordx4 v1, s[36:39], s76 offen offset:1024 sc1 lds\n"
    "s_add_u32 s76, s76, 16384\n"
    "v_mfma_f32_16x16x32_bf16 v[60:63], v[104:107], v[44:47], v[60:63]\n"
    "s_waitcnt lgkmcnt(6)\n"
    "v_mfma_f32_16x16x32_bf16 v[68:71], v[108:111], v[44:47], v[68:71]\n"
    "ds_read_b128 v[44:47], v10 offset:7168\n"
    "v_mfma_f32_16x16x32_bf16 v[48:51], v[112:115], v[16:19], v[48:51]\n"
    "s_add_u32 m0, s77, s59\n"
    "s_add_u32 s77, s77, 8192\n"
    "buffer_load_dwordx4 v1, s[36:39], s76 offen sc1 lds\n"
    "v_mfma_f32_16x16x32_bf16 v[52:55], v[116:119], v[16:19], v[52:55]\n"
    "s_waitcnt lgkmcnt(6)\n"
    "v_mfma_f32_16x16x32_bf16 v[64:67], v[120:123], v[16:19], v[64:67]\n"
    "ds_read_b128 v[16:19], v10 offset:8192\n"
    "v_mfma_f32_16x16x32_bf16 v[56:59], v[112:115], v[20:23], v[56:59]\n"
    "v_mfma_f32_16x16x32_bf16 v[60:63], v[116:119], v[20:23], v[60:63]\n"
    "s_waitcnt lgkmcnt(6)\n"
    "buffer_load_dwordx4 v1, s[36:39], s76 offen offset:1024 sc1 lds\n"
    "s_add_u32 s76, s76, 16384\n"
    "v_mfma_f32_16x16x32_bf16 v[68:71], v[120:123], v[20:23], v[68:71]\n"
    "ds_read_b128 v[20:23], v10 offset:9216\n"
    "v_mfma_f32_16x16x32_bf16 v[48:51], v[124:127], v[24:27], v[48:51]\n"
    "v_mfma_f32_16x16x32_bf16 v[52:55], v[128:131], v[24:27], v[52:55]\n"
    "s_waitcnt lgkmcnt(6)\n"
    "s_add_u32 m0, s77, s59\n"
    "s_add_u32 s77, s77, 8192\n"
    "buffer_load_dwordx4 v1, s[36:39], s76 offen sc1 lds\n"
    "v_mfma_f32_16x16x32_bf16 v[64:67], v[132:135], v[24:27], v[64:67]\n"
    "ds_read_b128 v[24:27], v10 offset:10240\n"
    "v_mfma_f32_16x16x32_bf16 v[56:59], v[124:127], v[28:31], v[56:59]\n"
    "v_mfma_f32_16x16x32_bf16 v[60:63], v[128:131], v[28:31], v[60:63]\n"
    "s_waitcnt lgkmcnt(6)\n"
    "buffer_load_dwordx4 v1, s[36:39], s76 offen offset:1024 sc1 lds\n"
    "s_add_u32 s76, s76, 16384\n"
    "v_mfma_f32_16x16x32_bf16 v[68:71], v[132:135], v[28:31], v[68:71]\n"
    "ds_read_b128 v[28:31], v10 offset:11264\n"
    "v_mfma_f32_16x16x32_bf16 v[48:51], v[136:139], v[32:35], v[48:51]\n"
    "v_mfma_f32_16x16x32_bf16 v[52:55], v[140:143], v[32:35], v[52:55]\n"
    "s_waitcnt lgkmcnt(6)\n"
    "s_add_u32 m0, s77, s59\n"
    "s_add_u32 s77, s77, 8192\n"
    "buffer_load_dwordx4 v1, s[36:39], s76 offen sc1 lds\n"
    "v_mfma_f32_16x16x32_bf16 v[64:67], v[144:147], v[32:35], v[64:67]\n"
    "ds_read_b128 v[32:35], v10 offset:12288\n"
    "v_mfma_f32_16x16x32_bf16 v[56:59], v[136:139], v[36:39], v[56:59]\n"
    "v_mfma_f32_16x16x32_bf16 v[60:63], v[140:143], v[36:39], v[60:63]\n"
    "s_waitcnt lgkmcnt(6)\n"
    "v_mfma_f32_16x16x32_bf16 v[68:71], v[144:147], v[36:39], v[68:71]\n"
    "ds_read_b128 v[36:39], v10 offset:13312\n"
    "v_mfma_f32_16x16x32_bf16 v[48:51], v[148:151], v[40:43], v[48:51]\n"
    "buffer_load_dwordx4 v1, s[36:39], s76 offen offset:1024 sc1 lds\n"
    "s_add_u32 s76, s76, 16384\n"
    "v_mfma_f32_16x16x32_bf16 v[52:55], v[152:155], v[40:43], v[52:55]\n"
    "s_waitcnt lgkmcnt(6)\n"
    "v_mfma_f32_16x16x32_bf16 v[64:67], v[156:159], v[40:43], v[64:67]\n"
    "ds_read_b128 v[40:43], v10 offset:14336\n"
    "v_mfma_f32_16x16x32_bf16 v[56:59], v[148:151], v[44:47], v[56:59]\n"
    "s_add_u32 m0, s77, s59\n"
    "s_add_u32 s77, s77, 8192\n"
    "buffer_load_dwordx4 v1, s[36:39], s76 offen sc1 lds\n"
    "v_mfma_f32_16x16x32_bf16 v[60:63], v[152:155], v[44:47], v[60:63]\n"
    "s_waitcnt lgkmcnt(6)\n"
    "v_mfma_f32_16x16x32_bf16 v[68:71], v[156:159], v[44:47], v[68:71]\n"
    "ds_read_b128 v[44:47], v10 offset:15360\n"
    "v_mfma_f32_16x16x32_bf16 v[48:51], v[160:163], v[16:19], v[48:51]\n"
    "buffer_load_dwordx4 v1, s[36:39], s76 offen offset:1024 sc1 lds\n"
    "s_add_u32 s76, s76, 16384\n"
    "v_mfma_f32_16x16x32_bf16 v[52:55], v[164:167], v[16:19], v[52:55]\n"
    "s_waitcnt lgkmcnt(6)\n"
    "v_mfma_f32_16x16x32_bf16 v[64:67], v[168:171], v[16:19], v[64:67]\n"
    "ds_read_b128 v[16:19], v10 offset:16384\n"
    "v_mfma_f32_16x16x32_bf16 v[56:59], v[160:163], v[20:23], v[56:59]\n"
    "s_add_u32 m0, s77, s59\n"
    "s_add_u32 s77, s77, 8192\n"
    "buffer_load_dwordx4 v1, s[36:39], s76 offen sc1 lds\n"
    "v_mfma_f32_16x16x32_bf16 v[60:63], v[164:167], v[20:23], v[60:63]\n"
    "s_waitcnt lgkmcnt(6)\n"
    "v_mfma_f32_16x16x32_bf16 v[68:71], v[168:171], v[20:23], v[68:71]\n"
    "ds_read_b128 v[20:23], v10 offset:17408\n"
    "v_mfma_f32_16x16x32_bf16 v[48:51], v[172:175], v[24:27], v[48:51]\n"
    "buffer_load_dwordx4 v1, s[36:39], s76 offen offset:1024 sc1 lds\n"
    "s_add_u32 s76, s76, 16384\n"
    "v_mfma_f32_16x16x32_bf16 v[52:55], v[176:179], v[24:27], v[52:55]\n"
    "s_waitcnt lgkmcnt(6)\n"
    "v_mfma_f32_16x16x32_bf16 v[64:67], v[180:183], v[24:27], v[64:67]\n"
    "ds_read_b128 v[24:27], v10 offset:18432\n"
    "v_mfma_f32_16x16x32_bf16 v[56:59], v[172:175], v[28:31], v[56:59]\n"
    "v_mfma_f32_16x16x32_bf16 v[60:63], v[176:179], v[28:31], v[60:63]\n"
    "s_waitcnt lgkmcnt(6)\n"
    "s_add_u32 m0, s77, s59\n"
    "s_add_u32 s77, s77, 8192\n"
    "buffer_load_dwordx4 v1, s[36:39], s76 offen sc1 lds\n"
    "v_mfma_f32_16x16x32_bf16 v[68:71], v[180:183], v[28:31], v[68:71]\n"
    "ds_read_b128 v[28:31], v10 offset:19456\n"
    "v_mfma_f32_16x16x32_bf16 v[48:51], v[184:187], v[32:35], v[48:51]\n"
    "v_mfma_f32_16x16x32_bf16 v[52:55], v[188:191], v[32:35], v[52:55]\n"
    "s_waitcnt lgkmcnt(6)\n"
    "buffer_load_dwordx4 v1, s[36:39], s76 offen offset:1024 sc1 lds\n"
    "s_add_u32 s76, s76, 16384\n"
    "v_mfma_f32_16x16x32_bf16 v[64:67], v[192:195], v[32:35], v[64:67]\n"
    "ds_read_b128 v[32:35], v10 offset:20480\n"
    "v_mfma_f32_16x16x32_bf16 v[56:59], v[184:187], v[36:39], v[56:59]\n"
    "v_mfma_f32_16x16x32_bf16 v[60:63], v[188:191], v[36:39], v[60:63]\n"
    "s_waitcnt lgkmcnt(6)\n"
    "s_add_u32 m0, s77, s59\n"
    "s_add_u32 s77, s77, 8192\n"
    "buffer_load_dwordx4 v1, s[36:39], s76 offen sc1 lds\n"
    "v_mfma_f32_16x16x32_bf16 v[68:71], v[192:195], v[36:39], v[68:71]\n"
    "ds_read_b128 v[36:39], v10 offset:21504\n"
    "v_mfma_f32_16x16x32_bf16 v[48:51], v[196:199], v[40:43], v[48:51]\n"
    "v_mfma_f32_16x16x32_bf16 v[52:55], v[200:203], v[40:43], v[52:55]\n"
    "s_waitcnt lgkmcnt(6)\n"
    "buffer_load_dwordx4 v1, s[36:39], s76 offen offset:1024 sc1 lds\n"
    "s_add_u32 s76, s76, 16384\n"
    "v_mfma_f32_16x16x32_bf16 v[64:67], v[204:207], v[40:43], v[64:67]\n"
    "ds_read_b128 v[40:43], v10 offset:22528\n"
    "v_mfma_f32_16x16x32_bf16 v[56:59], v[196:199], v[44:47], v[56:59]\n"
    "v_mfma_f32_16x16x32_bf16 v[60:63], v[200:203], v[44:47], v[60:63]\n"
    "s_waitcnt lgkmcnt(6)\n"
    "v_mfma_f32_16x16x32_bf16 v[68:71], v[204:207], v[44:47], v[68:71]\n"
    "ds_read_b128 v[44:47], v10 offset:23552\n"
    "v_mfma_f32_16x16x32_bf16 v[64:67], v[216:219], v[16:19], v[64:67]\n"
    "s_waitcnt lgkmcnt(6)\n"
    "ds_read_b128 v[16:19], v10 offset:16384\n"
    "v_mfma_f32_16x16x32_bf16 v[68:71], v[216:219], v[20:23], v[68:71]\n"
    "s_waitcnt lgkmcnt(6)\n"
    "ds_read_b128 v[20:23], v10 offset:18432\n"
    "v_mfma_f32_16x16x32_bf16 v[64:67], v[228:231], v[24:27], v[64:67]\n"
    "s_waitcnt lgkmcnt(6)\n"
    "ds_read_b128 v[24:27], v10 offset:20480\n"
    "v_mfma_f32_16x16x32_bf16 v[68:71], v[228:231], v[28:31], v[68:71]\n"
    "s_waitcnt lgkmcnt(6)\n"
    "ds_read_b128 v[28:31], v10 offset:22528\n"
    "v_mfma_f32_16x16x32_bf16 v[64:67], v[240:243], v[32:35], v[64:67]\n"
    "s_waitcnt lgkmcnt(6)\n"
    "ds_read_b128 v[32:35], v10 offset:17408\n"
    "v_mfma_f32_16x16x32_bf16 v[68:71], v[240:243], v[36:39], v[68:71]\n"
    "s_waitcnt lgkmcnt(6)\n"
    "ds_read_b128 v[36:39], v10 offset:19456\n"
    "v_mfma_f32_16x16x32_bf16 v[64:67], v[252:255], v[40:43], v[64:67]\n"
    "s_waitcnt lgkmcnt(6)\n"
    "ds_read_b128 v[40:43], v10 offset:21504\n"
    "v_mfma_f32_16x16x32_bf16 v[68:71], v[252:255], v[44:47], v[68:71]\n"
    "s_waitcnt lgkmcnt(6)\n"
    "ds_read_b128 v[44:47], v10 offset:23552\n"
    "v_mfma_f32_16x16x32_bf16 v[48:51], v[208:211], v[16:19], v[48:51]\n"
    "v_mfma_f32_16x16x32_bf16 v[52:55], v[212:215], v[16:19], v[52:55]\n"
    "s_waitcnt lgkmcnt(6)\n"
    "v_cvt_pk_bf16_f32 v64, v64, v65\n"
    "v_cvt_pk_bf16_f32 v65, v66, v67\n"
    "v_mfma_f32_16x16x32_bf16 v[48:51], v[220:223], v[20:23], v[48:51]\n"
    "s_waitcnt lgkmcnt(5)\n"
    "v_cvt_pk_bf16_f32 v66, v68, v69\n"
    "v_cvt_pk_bf16_f32 v67, v70, v71\n"
    "v_mfma_f32_16x16x32_bf16 v[52:55], v[224:227], v[20:23], v[52:55]\n"
    "s_nop 0\n"
    "v_mfma_f32_16x16x32_bf16 v[48:51], v[232:235], v[24:27], v[48:51]\n"
    "v_mfma_f32_16x16x32_bf16 v[52:55], v[236:239], v[24:27], v[52:55]\n"
    "s_waitcnt lgkmcnt(4)\n"
    "v_mfma_f32_16x16x32_bf16 v[48:51], v[244:247], v[28:31], v[48:51]\n"
    "s_waitcnt lgkmcnt(3)\n"
    "v_permlane16_swap_b32 v64, v66\n"
    "v_mfma_f32_16x16x32_bf16 v[52:55], v[248:251], v[28:31], v[52:55]\n"
    "v_mfma_f32_16x16x32_bf16 v[56:59], v[208:211], v[32:35], v[56:59]\n"
    "s_waitcnt lgkmcnt(2)\n"
    "v_permlane16_swap_b32 v65, v67\n"
    "v_mfma_f32_16x16x32_bf16 v[60:63], v[212:215], v[32:35], v[60:63]\n"
    "v_cvt_pk_bf16_f32 v48, v48, v49\n"
    "v_mfma_f32_16x16x32_bf16 v[56:59], v[220:223], v[36:39], v[56:59]\n"
    "s_waitcnt lgkmcnt(1)\n"
    "buffer_store_dwordx4 v[64:67], v4, s[36:39], s73 offen sc1\n"
    "v_cvt_pk_bf16_f32 v49, v50, v51\n"
    "v_mfma_f32_16x16x32_bf16 v[60:63], v[224:227], v[36:39], v[60:63]\n"
    "v_cvt_pk_bf16_f32 v50, v52, v53\n"
    "v_mfma_f32_16x16x32_bf16 v[56:59], v[232:235], v[40:43], v[56:59]\n"
    "s_waitcnt lgkmcnt(0)\n"
    "v_cvt_pk_bf16_f32 v51, v54, v55\n"
    "v_mfma_f32_16x16x32_bf16 v[60:63], v[236:239], v[40:43], v[60:63]\n"
    "v_mfma_f32_16x16x32_bf16 v[56:59], v[244:247], v[44:47], v[56:59]\n"
    "v_mfma_f32_16x16x32_bf16 v[60:63], v[248:251], v[44:47], v[60:63]\n"
    "buffer_store_dwordx4 v[48:51], v15, s[36:39], s73 offen sc1\n"
    "s_nop 2\n"
    "v_cvt_pk_bf16_f32 v56, v56, v57\n"
    "v_cvt_pk_bf16_f32 v57, v58, v59\n"
    "s_nop 1\n"
    "v_cvt_pk_bf16_f32 v58, v60, v61\n"
    "v_cvt_pk_bf16_f32 v59, v62, v63\n"
    "buffer_store_dwordx4 v[56:59], v15, s[36:39], s73 offen offset:1024 sc1\n"
    "v_mov_b32 v12, s68\n"
    "s_mov_b32 s68, s71\n"
    "s_waitcnt vmcnt(3)\n"
    "s_branch .LgDOWN_bar_%=\n"
    ".LgDOWN_dma152_%=:\n"
    "v_mfma_f32_16x16x32_bf16 v[64:67], v[132:135], v[24:27], v[64:67]\n"
    "ds_read_b128 v[24:27], v10 offset:10240\n"
    "v_mfma_f32_16x16x32_bf16 v[56:59], v[124:127], v[28:31], v[56:59]\n"
    "s_add_u32 m0, s77, s59\n"
    "s_add_u32 s77, s77, 8192\n"
    "buffer_load_dwordx4 v1, s[36:39], s76 offen sc1 lds\n"
    "v_mfma_f32_16x16x32_bf16 v[60:63], v[128:131], v[28:31], v[60:63]\n"
    "s_waitcnt lgkmcnt(6)\n"
    "buffer_load_dwordx4 v1, s[36:39], s76 offen offset:1024 sc1 lds\n"
    "s_add_u32 s76, s76, 16384\n"
    "v_mfma_f32_16x16x32_bf16 v[68:71], v[132:135], v[28:31], v[68:71]\n"
    "ds_read_b128 v[28:31], v10 offset:11264\n"
    "v_mfma_f32_16x16x32_bf16 v[48:51], v[136:139], v[32:35], v[48:51]\n"
    "v_mfma_f32_16x16x32_bf16 v[52:55], v[140:143], v[32:35], v[52:55]\n"
    "s_waitcnt lgkmcnt(6)\n"
    "s_add_u32 m0, s77, s59\n"
    "s_add_u32 s77, s77, 8192\n"
    "buffer_load_dwordx4 v1, s[36:39], s76 offen sc1 lds\n"
    "v_mfma_f32_16x16x32_bf16 v[64:67], v[144:147], v[32:35], v[64:67]\n"
    "ds_read_b128 v[32:35], v10 offset:12288\n"
    "v_mfma_f32_16x16x32_bf16 v[56:59], v[136:139], v[36:39], v[56:59]\n"
    "v_mfma_f32_16x16x32_bf16 v[60:63], v[140:143], v[36:39], v[60:63]\n"
    "s_waitcnt lgkmcnt(6)\n"
    "buffer_load_dwordx4 v1, s[36:39], s76 offen offset:1024 sc1 lds\n"
    "s_add_u32 s76, s76, 16384\n"
    "v_mfma_f32_16x16x32_bf16 v[68:71], v[144:147], v[36:39], v[68:71]\n"
    "ds_read_b128 v[36:39], v10 offset:13312\n"
    "v_mfma_f32_16x16x32_bf16 v[48:51], v[148:151], v[40:43], v[48:51]\n"
    "v_mfma_f32_16x16x32_bf16 v[52:55], v[152:155], v[40:43], v[52:55]\n"
    "s_waitcnt lgkmcnt(6)\n"
    "s_add_u32 m0, s77, s59\n"
    "s_add_u32 s77, s77, 8192\n"
    "buffer_load_dwordx4 v1, s[36:39], s76 offen sc1 lds\n"
    "v_mfma_f32_16x16x32_bf16 v[64:67], v[156:159], v[40:43], v[64:67]\n"
    "ds_read_b128 v[40:43], v10 offset:14336\n"
    "v_mfma_f32_16x16x32_bf16 v[56:59], v[148:151], v[44:47], v[56:59]\n"
    "buffer_load_dwordx4 v1, s[36:39], s76 offen offset:1024 sc1 lds\n"
    "s_add_u32 s76, s76, 16384\n"
    "v_mfma_f32_16x16x32_bf16 v[60:63], v[152:155], v[44:47], v[60:63]\n"
    "s_waitcnt lgkmcnt(6)\n"
    "v_mfma_f32_16x16x32_bf16 v[68:71], v[156:159], v[44:47], v[68:71]\n"
    "ds_read_b128 v[44:47], v10 offset:15360\n"
    "v_mfma_f32_16x16x32_bf16 v[48:51], v[160:163], v[16:19], v[48:51]\n"
    "s_add_u32 m0, s77, s59\n"
    "s_add_u32 s77, s77, 8192\n"
    "buffer_load_dwordx4 v1, s[36:39], s76 offen sc1 lds\n"
    "v_mfma_f32_16x16x32_bf16 v[52:55], v[164:167], v[16:19], v[52:55]\n"
    "s_waitcnt lgkmcnt(6)\n"
    "v_mfma_f32_16x16x32_bf16 v[64:67], v[168:171], v[16:19], v[64:67]\n"
    "ds_read_b128 v[16:19], v10 offset:16384\n"
    "v_mfma_f32_16x16x32_bf16 v[56:59], v[160:163], v[20:23], v[56:59]\n"
    "buffer_load_dwordx4 v1, s[36:39], s76 offen offset:1024 sc1 lds\n"
    "s_add_u32 s76, s76, 16384\n"
    "v_mfma_f32_16x16x32_bf16 v[60:63], v[164:167], v[20:23], v[60:63]\n"
    "s_waitcnt lgkmcnt(6)\n"
    "v_mfma_f32_16x16x32_bf16 v[68:71], v[168:171], v[20:23], v[68:71]\n"
    "ds_read_b128 v[20:23], v10 offset:17408\n"
    "v_mfma_f32_16x16x32_bf16 v[48:51], v[172:175], v[24:27], v[48:51]\n"
    "s_add_u32 m0, s77, s59\n"
    "s_add_u32 s77, s77, 8192\n"
    "buffer_load_dwordx4 v1, s[36:39], s76 offen sc1 lds\n"
    "v_mfma_f32_16x16x32_bf16 v[52:55], v[176:179], v[24:27], v[52:55]\n"
    "s_waitcnt lgkmcnt(6)\n"
    "v_mfma_f32_16x16x32_bf16 v[64:67], v[180:183], v[24:27], v[64:67]\n"
    "ds_read_b128 v[24:27], v10 offset:18432\n"
    "v_mfma_f32_16x16x32_bf16 v[56:59], v[172:175], v[28:31], v[56:59]\n"
    "buffer_load_dwordx4 v1, s[36:39], s76 offen offset:1024 sc1 lds\n"
    "s_add_u32 s76, s76, 16384\n"
    "v_mfma_f32_16x16x32_bf16 v[60:63], v[176:179], v[28:31], v[60:63]\n"
    "s_waitcnt lgkmcnt(6)\n"
    "s_add_u32 m0, s77, s59\n"
    "s_add_u32 s77, s77, 8192\n"
    "buffer_load_dwordx4 v1, s[36:39], s76 offen sc1 lds\n"
    "v_mfma_f32_16x16x32_bf16 v[68:71], v[180:183], v[28:31], v[68:71]\n"
    "ds_read_b128 v[28:31], v10 offset:19456\n"
    "v_mfma_f32_16x16x32_bf16 v[48:51], v[184:187], v[32:35], v[48:51]\n"
    "v_mfma_f32_16x16x32_bf16 v[52:55], v[188:191], v[32:35], v[52:55]\n"
    "s_waitcnt lgkmcnt(6)\n"
    "buffer_load_dwordx4 v1, s[36:39], s76 offen offset:1024 sc1 lds\n"
    "s_add_u32 s76, s76, 16384\n"
    "v_mfma_f32_16x16x32_bf16 v[64:67], v[192:195], v[32:35], v[64:67]\n"
    "ds_read_b128 v[32:35], v10 offset:20480\n"
    "v_mfma_f32_16x16x32_bf16 v[56:59], v[184:187], v[36:39], v[56:59]\n"
    "v_mfma_f32_16x16x32_bf16 v[60:63], v[188:191], v[36:39], v[60:63]\n"
    "s_waitcnt lgkmcnt(6)\n"
    "s_add_u32 m0, s77, s59\n"
    "s_add_u32 s77, s77, 8192\n"
    "buffer_load_dwordx4 v1, s[36:39], s76 offen sc1 lds\n"
    "v_mfma_f32_16x16x32_bf16 v[68:71], v[192:195], v[36:39], v[68:71]\n"
    "ds_read_b128 v[36:39], v10 offset:21504\n"
    "v_mfma_f32_16x16x32_bf16 v[48:51], v[196:199], v[40:43], v[48:51]\n"
    "v_mfma_f32_16x16x32_bf16 v[52:55], v[200:203], v[40:43], v[52:55]\n"
    "s_waitcnt lgkmcnt(6)\n"
    "buffer_load_dwordx4 v1, s[36:39], s76 offen offset:1024 sc1 lds\n"
    "s_add_u32 s76, s76, 16384\n"
    "v_mfma_f32_16x16x32_bf16 v[64:67], v[204:207], v[40:43], v[64:67]\n"
    "ds_read_b128 v[40:43], v10 offset:22528\n"
    "v_mfma_f32_16x16x32_bf16 v[56:59], v[196:199], v[44:47], v[56:59]\n"
    "s_add_u32 m0, s77, s59\n"
    "s_add_u32 s77, s77, 8192\n"
    "buffer_load_dwordx4 v1, s[36:39], s76 offen sc1 lds\n"
    "v_mfma_f32_16x16x32_bf16 v[60:63], v[200:203], v[44:47], v[60:63]\n"
    "s_waitcnt lgkmcnt(6)\n"
    "v_mfma_f32_16x16x32_bf16 v[68:71], v[204:207], v[44:47], v[68:71]\n"
    "ds_read_b128 v[44:47], v10 offset:23552\n"
    "v_mfma_f32_16x16x32_bf16 v[64:67], v[216:219], v[16:19], v[64:67]\n"
    "s_waitcnt lgkmcnt(6)\n"
    "ds_read_b128 v[16:19], v10 offset:16384\n"
    "v_mfma_f32_16x16x32_bf16 v[68:71], v[216:219], v[20:23], v[68:71]\n"
    "s_waitcnt lgkmcnt(6)\n"
    "ds_read_b128 v[20:23], v10 offset:18432\n"
    "v_mfma_f32_16x16x32_bf16 v[64:67], v[228:231], v[24:27], v[64:67]\n"
    "s_waitcnt lgkmcnt(6)\n"
    "ds_read_b128 v[24:27], v10 offset:20480\n"
    "v_mfma_f32_16x16x32_bf16 v[68:71], v[228:231], v[28:31], v[68:71]\n"
    "s_waitcnt lgkmcnt(6)\n"
    "ds_read_b128 v[28:31], v10 offset:22528\n"
    "v_mfma_f32_16x16x32_bf16 v[64:67], v[240:243], v[32:35], v[64:67]\n"
    "s_waitcnt lgkmcnt(6)\n"
    "ds_read_b128 v[32:35], v10 offset:17408\n"
    "v_mfma_f32_16x16x32_bf16 v[68:71], v[240:243], v[36:39], v[68:71]\n"
    "s_waitcnt lgkmcnt(6)\n"
    "ds_read_b128 v[36:39], v10 offset:19456\n"
    "v_mfma_f32_16x16x32_bf16 v[64:67], v[252:255], v[40:43], v[64:67]\n"
    "s_waitcnt lgkmcnt(6)\n"
    "ds_read_b128 v[40:43], v10 offset:21504\n"
    "v_mfma_f32_16x16x32_bf16 v[68:71], v[252:255], v[44:47], v[68:71]\n"
    "s_waitcnt lgkmcnt(6)\n"
    "ds_read_b128 v[44:47], v10 offset:23552\n"
    "v_mfma_f32_16x16x32_bf16 v[48:51], v[208:211], v[16:19], v[48:51]\n"
    "buffer_load_dwordx4 v1, s[36:39], s76 offen offset:1024 sc1 lds\n"
    "s_add_u32 s76, s76, 16384\n"
    "v_mfma_f32_16x16x32_bf16 v[52:55], v[212:215], v[16:19], v[52:55]\n"
    "s_waitcnt lgkmcnt(6)\n"
    "v_cvt_pk_bf16_f32 v64, v64, v65\n"
    "v_cvt_pk_bf16_f32 v65, v66, v67\n"
    "v_mfma_f32_16x16x32_bf16 v[48:51], v[220:223], v[20:23], v[48:51]\n"
    "s_waitcnt lgkmcnt(5)\n"
    "s_add_u32 m0, s77, s59\n"
    "s_add_u32 s77, s77, 8192\n"
    "buffer_load_dwordx4 v1, s[36:39], s76 offen sc1 lds\n"
    "v_cvt_pk_bf16_f32 v66, v68, v69\n"
    "v_cvt_pk_bf16_f32 v67, v70, v71\n"
    "v_mfma_f32_16x16x32_bf16 v[52:55], v[224:227], v[20:23], v[52:55]\n"
    "s_nop 0\n"
    "v_mfma_f32_16x16x32_bf16 v[48:51], v[232:235], v[24:27], v[48:51]\n"
    "v_mfma_f32_16x16x32_bf16 v[52:55], v[236:239], v[24:27], v[52:55]\n"
    "s_waitcnt lgkmcnt(4)\n"
    "v_mfma_f32_16x16x32_bf16 v[48:51], v[244:247], v[28:31], v[48:51]\n"
    "s_waitcnt lgkmcnt(3)\n"
    "v_permlane16_swap_b32 v64, v66\n"
    "v_mfma_f32_16x16x32_bf16 v[52:55], v[248:251], v[28:31], v[52:55]\n"
    "v_mfma_f32_16x16x32_bf16 v[56:59], v[208:211], v[32:35], v[56:59]\n"
    "s_waitcnt lgkmcnt(2)\n"
    "buffer_load_dwordx4 v1, s[36:39], s76 offen offset:1024 sc1 lds\n"
    "s_add_u32 s76, s76, 16384\n"
    "v_permlane16_swap_b32 v65, v67\n"
    "v_mfma_f32_16x16x32_bf16 v[60:63], v[212:215], v[32:35], v[60:63]\n"
    "v_cvt_pk_bf16_f32 v48, v48, v49\n"
    "v_mfma_f32_16x16x32_bf16 v[56:59], v[220:223], v[36:39], v[56:59]\n"
    "s_waitcnt lgkmcnt(1)\n"
    "buffer_store_dwordx4 v[64:67], v4, s[36:39], s73 offen sc1\n"
    "v_cvt_pk_bf16_f32 v49, v50, v51\n"
    "v_mfma_f32_16x16x32_bf16 v[60:63], v[224:227], v[36:39], v[60:63]\n"
    "v_cvt_pk_bf16_f32 v50, v52, v53\n"
    "v_mfma_f32_16x16x32_bf16 v[56:59], v[232:235], v[40:43], v[56:59]\n"
    "s_waitcnt lgkmcnt(0)\n"
    "v_cvt_pk_bf16_f32 v51, v54, v55\n"
    "v_mfma_f32_16x16x32_bf16 v[60:63], v[236:239], v[40:43], v[60:63]\n"
    "v_mfma_f32_16x16x32_bf16 v[56:59], v[244:247], v[44:47], v[56:59]\n"
    "v_mfma_f32_16x16x32_bf16 v[60:63], v[248:251], v[44:47], v[60:63]\n"
    "buffer_store_dwordx4 v[48:51], v15, s[36:39], s73 offen sc1\n"
    "s_nop 2\n"
    "v_cvt_pk_bf16_f32 v56, v56, v57\n"
    "v_cvt_pk_bf16_f32 v57, v58, v59\n"
    "s_nop 1\n"
    "v_cvt_pk_bf16_f32 v58, v60, v61\n"
    "v_cvt_pk_bf16_f32 v59, v62, v63\n"
    "buffer_store_dwordx4 v[56:59], v15, s[36:39], s73 offen offset:1024 sc1\n"
    "v_mov_b32 v12, s68\n"
    "s_mov_b32 s68, s71\n"
    "s_waitcnt vmcnt(3)\n"
    "s_branch .LgDOWN_bar_%=\n"
    ".LgDOWN_dma172_%=:\n"
    "v_mfma_f32_16x16x32_bf16 v[60:63], v[164:167], v[20:23], v[60:63]\n"
    "s_waitcnt lgkmcnt(6)\n"
    "s_add_u32 m0, s77, s59\n"
    "s_add_u32 s77, s77, 8192\n"
    "buffer_load_dwordx4 v1, s[36:39], s76 offen sc1 lds\n"
    "v_mfma_f32_16x16x32_bf16 v[68:71], v[168:171], v[20:23], v[68:71]\n"
    "ds_read_b128 v[20:23], v10 offset:17408\n"
    "v_mfma_f32_16x16x32_bf16 v[48:51], v[172:175], v[24:27], v[48:51]\n"
    "v_mfma_f32_16x16x32_bf16 v[52:55], v[176:179], v[24:27], v[52:55]\n"
    "s_waitcnt lgkmcnt(6)\n"
    "buffer_load_dwordx4 v1, s[36:39], s76 offen offset:1024 sc1 lds\n"
    "s_add_u32 s76, s76, 16384\n"
    "v_mfma_f32_16x16x32_bf16 v[64:67], v[180:183], v[24:27], v[64:67]\n"
    "ds_read_b128 v[24:27], v10 offset:18432\n"
    "v_mfma_f32_16x16x32_bf16 v[56:59], v[172:175], v[28:31], v[56:59]\n"
    "v_mfma_f32_16x16x32_bf16 v[60:63], v[176:179], v[28:31], v[60:63]\n"
    "s_waitcnt lgkmcnt(6)\n"
    "s_add_u32 m0, s77, s59\n"
    "s_add_u32 s77, s77, 8192\n"
    "buffer_load_dwordx4 v1, s[36:39], s76 offen sc1 lds\n"
    "v_mfma_f32_16x16x32_bf16 v[68:71], v[180:183], v[28:31], v[68:71]\n"
    "ds_read_b128 v[28:31], v10 offset:19456\n"
    "v_mfma_f32_16x16x32_bf16 v[48:51], v[184:187], v[32:35], v[48:51]\n"
    "v_mfma_f32_16x16x32_bf16 v[52:55], v[188:191], v[32:35], v[52:55]\n"
    "s_waitcnt lgkmcnt(6)\n"
    "buffer_load_dwordx4 v1, s[36:39], s76 offen offset:1024 sc1 lds\n"
    "s_add_u32 s76, s76, 16384\n"
    "v_mfma_f32_16x16x32_bf16 v[64:67], v[192:195], v[32:35], v[64:67]\n"
    "ds_read_b128 v[32:35], v10 offset:20480\n"
    "v_mfma_f32_16x16x32_bf16 v[56:59], v[184:187], v[36:39], v[56:59]\n"
    "v_mfma_f32_16x16x32_bf16 v[60:63], v[188:191], v[36:39], v[60:63]\n"
    "s_waitcnt lgkmcnt(6)\n"
    "s_add_u32 m0, s77, s59\n"
    "s_add_u32 s77, s77, 8192\n"
    "buffer_load_dwordx4 v1, s[36:39], s76 offen sc1 lds\n"
    "v_mfma_f32_16x16x32_bf16 v[68:71], v[192:195], v[36:39], v[68:71]\n"
    "ds_read_b128 v[36:39], v10 offset:21504\n"
    "v_mfma_f32_16x16x32_bf16 v[48:51], v[196:199], v[40:43], v[48:51]\n"
    "v_mfma_f32_16x16x32_bf16 v[52:55], v[200:203], v[40:43], v[52:55]\n"
    "s_waitcnt lgkmcnt(6)\n"
    "buffer_load_dwordx4 v1, s[36:39], s76 offen offset:1024 sc1 lds\n"
    "s_add_u32 s76, s76, 16384\n"
    "v_mfma_f32_16x16x32_bf16 v[64:67], v[204:207], v[40:43], v[64:67]\n"
    "ds_read_b128 v[40:43], v10 offset:22528\n"
    "v_mfma_f32_16x16x32_bf16 v[56:59], v[196:199], v[44:47], v[56:59]\n"
    "v_mfma_f32_16x16x32_bf16 v[60:63], v[200:203], v[44:47], v[60:63]\n"
    "s_waitcnt lgkmcnt(6)\n"
    "s_add_u32 m0, s77, s59\n"
    "s_add_u32 s77, s77, 8192\n"
    "buffer_load_dwordx4 v1, s[36:39], s76 offen sc1 lds\n"
    "v_mfma_f32_16x16x32_bf16 v[68:71], v[204:207], v[44:47], v[68:71]\n"
    "ds_read_b128 v[44:47], v10 offset:23552\n"
    "v_mfma_f32_16x16x32_bf16 v[64:67], v[216:219], v[16:19], v[64:67]\n"
    "s_waitcnt lgkmcnt(6)\n"
    "ds_read_b128 v[16:19], v10 offset:16384\n"
    "v_mfma_f32_16x16x32_bf16 v[68:71], v[216:219], v[20:23], v[68:71]\n"
    "s_waitcnt lgkmcnt(6)\n"
    "ds_read_b128 v[20:23], v10 offset:18432\n"
    "v_mfma_f32_16x16x32_bf16 v[64:67], v[228:231], v[24:27], v[64:67]\n"
    "s_waitcnt lgkmcnt(6)\n"
    "ds_read_b128 v[24:27], v10 offset:20480\n"
    "v_mfma_f32_16x16x32_bf16 v[68:71], v[228:231], v[28:31], v[68:71]\n"
    "s_waitcnt lgkmcnt(6)\n"
    "ds_read_b128 v[28:31], v10 offset:22528\n"
    "v_mfma_f32_16x16x32_bf16 v[64:67], v[240:243], v[32:35], v[64:67]\n"
    "s_waitcnt lgkmcnt(6)\n"
    "ds_read_b128 v[32:35], v10 offset:17408\n"
    "v_mfma_f32_16x16x32_bf16 v[68:71], v[240:243], v[36:39], v[68:71]\n"
    "s_waitcnt lgkmcnt(6)\n"
    "ds_read_b128 v[36:39], v10 offset:19456\n"
    "v_mfma_f32_16x16x32_bf16 v[64:67], v[252:255], v[40:43], v[64:67]\n"
    "s_waitcnt lgkmcnt(6)\n"
    "ds_read_b128 v[40:43], v10 offset:21504\n"
    "v_mfma_f32_16x16x32_bf16 v[68:71], v[252:255], v[44:47], v[68:71]\n"
    "s_waitcnt lgkmcnt(6)\n"
    "ds_read_b128 v[44:47], v10 offset:23552\n"
    "v_mfma_f32_16x16x32_bf16 v[48:51], v[208:211], v[16:19], v[48:51]\n"
    "v_mfma_f32_16x16x32_bf16 v[52:55], v[212:215], v[16:19], v[52:55]\n"
    "s_waitcnt lgkmcnt(6)\n"
    "buffer_load_dwordx4 v1, s[36:39], s76 offen offset:1024 sc1 lds\n"
    "s_add_u32 s76, s76, 16384\n"
    "v_cvt_pk_bf16_f32 v64, v64, v65\n"
    "v_cvt_pk_bf16_f32 v65, v66, v67\n"
    "v_mfma_f32_16x16x32_bf16 v[48:51], v[220:223], v[20:23], v[48:51]\n"
    "s_waitcnt lgkmcnt(5)\n"
    "v_cvt_pk_bf16_f32 v66, v68, v69\n"
    "v_cvt_pk_bf16_f32 v67, v70, v71\n"
    "v_mfma_f32_16x16x32_bf16 v[52:55], v[224:227], v[20:23], v[52:55]\n"
    "s_nop 0\n"
    "v_mfma_f32_16x16x32_bf16 v[48:51], v[232:235], v[24:27], v[48:51]\n"
    "v_mfma_f32_16x16x32_bf16 v[52:55], v[236:239], v[24:27], v[52:55]\n"
    "s_waitcnt lgkmcnt(4)\n"
    "v_mfma_f32_16x16x32_bf16 v[48:51], v[244:247], v[28:31], v[48:51]\n"
    "s_waitcnt lgkmcnt(3)\n"
    "s_add_u32 m0, s77, s59\n"
    "s_add_u32 s77, s77, 8192\n"
    "buffer_load_dwordx4 v1, s[36:39], s76 offen sc1 lds\n"
    "v_permlane16_swap_b32 v64, v66\n"
    "v_mfma_f32_16x16x32_bf16 v[52:55], v[248:251], v[28:31], v[52:55]\n"
    "v_mfma_f32_16x16x32_bf16 v[56:59], v[208:211], v[32:35], v[56:59]\n"
    "s_waitcnt lgkmcnt(2)\n"
    "v_permlane16_swap_b32 v65, v67\n"
    "v_mfma_f32_16x16x32_bf16 v[60:63], v[212:215], v[32:35], v[60:63]\n"
    "v_cvt_pk_bf16_f32 v48, v48, v49\n"
    "v_mfma_f32_16x16x32_bf16 v[56:59], v[220:223], v[36:39], v[56:59]\n"
    "s_waitcnt lgkmcnt(1)\n"
    "buffer_load_dwordx4 v1, s[36:39], s76 offen offset:1024 sc1 lds\n"
    "s_add_u32 s76, s76, 16384\n"
    "buffer_store_dwordx4 v[64:67], v4, s[36:39], s73 offen sc1\n"
    "v_cvt_pk_bf16_f32 v49, v50, v51\n"
    "v_mfma_f32_16x16x32_bf16 v[60:63], v[224:227], v[36:39], v[60:63]\n"
    "v_cvt_pk_bf16_f32 v50, v52, v53\n"
    "v_mfma_f32_16x16x32_bf16 v[56:59], v[232:235], v[40:43], v[56:59]\n"
    "s_waitcnt lgkmcnt(0)\n"
    "v_cvt_pk_bf16_f32 v51, v54, v55\n"
    "v_mfma_f32_16x16x32_bf16 v[60:63], v[236:239], v[40:43], v[60:63]\n"
    "v_mfma_f32_16x16x32_bf16 v[56:59], v[244:247], v[44:47], v[56:59]\n"
    "s_add_u32 m0, s77, s59\n"
    "s_add_u32 s77, s77, 8192\n"
    "buffer_load_dwordx4 v1, s[36:39], s76 offen sc1 lds\n"
    "v_mfma_f32_16x16x32_bf16 v[60:63], v[248:251], v[44:47], v[60:63]\n"
    "buffer_store_dwordx4 v[48:51], v15, s[36:39], s73 offen sc1\n"
    "s_nop 2\n"
    "v_cvt_pk_bf16_f32 v56, v56, v57\n"
    "v_cvt_pk_bf16_f32 v57, v58, v59\n"
    "s_nop 1\n"
    "v_cvt_pk_bf16_f32 v58, v60, v61\n"
    "v_cvt_pk_bf16_f32 v59, v62, v63\n"
    "buffer_store_dwordx4 v[56:59], v15, s[36:39], s73 offen offset:1024 sc1\n"
    "v_mov_b32 v12, s68\n"
    "s_mov_b32 s68, s71\n"
    "buffer_load_dwordx4 v1, s[36:39], s76 offen offset:1024 sc1 lds\n"
    "s_add_u32 s76, s76, 16384\n"
    "s_add_u32 m0, s77, s59\n"
    "s_add_u32 s77, s77, 8192\n"
    "buffer_load_dwordx4 v1, s[36:39], s76 offen sc1 lds\n"
    "buffer_load_dwordx4 v1, s[36:39], s76 offen offset:1024 sc1 lds\n"
    "s_add_u32 s76, s76, 16384\n"
    "s_add_u32 m0, s77, s59\n"
    "s_add_u32 s77, s77, 8192\n"
    "buffer_load_dwordx4 v1, s[36:39], s76 offen sc1 lds\n"
    "buffer_load_dwordx4 v1, s[36:39], s76 offen offset:1024 sc1 lds\n"
    "s_add_u32 s76, s76, 16384\n"
    "s_add_u32 m0, s77, s59\n"
    "s_add_u32 s77, s77, 8192\n"
    "buffer_load_dwordx4 v1, s[36:39], s76 offen sc1 lds\n"
    "buffer_load_dwordx4 v1, s[36:39], s76 offen offset:1024 sc1 lds\n"
    "s_add_u32 s76, s76, 16384\n"
    "s_waitcnt vmcnt(7)\n"
    "buffer_atomic_smax v12, v14, s[36:39], 0 offen sc1\n"
    "s_waitcnt vmcnt(1)\n"
    "s_branch .LgDOWN_bar_%=\n"
    ".LgDOWN_dma192_%=:\n"
    "v_mfma_f32_16x16x32_bf16 v[64:67], v[216:219], v[16:19], v[64:67]\n"
    "s_waitcnt lgkmcnt(6)\n"
    "ds_read_b128 v[16:19], v10 offset:16384\n"
    "v_mfma_f32_16x16x32_bf16 v[68:71], v[216:219], v[20:23], v[68:71]\n"
    "s_waitcnt lgkmcnt(6)\n"
    "ds_read_b128 v[20:23], v10 offset:18432\n"
    "v_mfma_f32_16x16x32_bf16 v[64:67], v[228:231], v[24:27], v[64:67]\n"
    "s_waitcnt lgkmcnt(6)\n"
    "ds_read_b128 v[24:27], v10 offset:20480\n"
    "v_mfma_f32_16x16x32_bf16 v[68:71], v[228:231], v[28:31], v[68:71]\n"
    "s_waitcnt lgkmcnt(6)\n"
    "ds_read_b128 v[28:31], v10 offset:22528\n"
    "v_mfma_f32_16x16x32_bf16 v[64:67], v[240:243], v[32:35], v[64:67]\n"
    "s_waitcnt lgkmcnt(6)\n"
    "ds_read_b128 v[32:35], v10 offset:17408\n"
    "v_mfma_f32_16x16x32_bf16 v[68:71], v[240:243], v[36:39], v[68:71]\n"
    "s_waitcnt lgkmcnt(6)\n"
    "ds_read_b128 v[36:39], v10 offset:19456\n"
    "v_mfma_f32_16x16x32_bf16 v[64:67], v[252:255], v[40:43], v[64:67]\n"
    "s_waitcnt lgkmcnt(6)\n"
    "ds_read_b128 v[40:43], v10 offset:21504\n"
    "v_mfma_f32_16x16x32_bf16 v[68:71], v[252:255], v[44:47], v[68:71]\n"
    "s_waitcnt lgkmcnt(6)\n"
    "ds_read_b128 v[44:47], v10 offset:23552\n"
    "v_mfma_f32_16x16x32_bf16 v[48:51], v[208:211], v[16:19], v[48:51]\n"
    "s_add_u32 m0, s77, s59\n"
    "s_add_u32 s77, s77, 8192\n"
    "buffer_load_dwordx4 v1, s[36:39], s76 offen sc1 lds\n"
    "v_mfma_f32_16x16x32_bf16 v[52:55], v[212:215], v[16:19], v[52:55]\n"
    "s_waitcnt lgkmcnt(6)\n"
    "v_cvt_pk_bf16_f32 v64, v64, v65\n"
    "v_cvt_pk_bf16_f32 v65, v66, v67\n"
    "v_mfma_f32_16x16x32_bf16 v[48:51], v[220:223], v[20:23], v[48:51]\n"
    "s_waitcnt lgkmcnt(5)\n"
    "buffer_load_dwordx4 v1, s[36:39], s76 offen offset:1024 sc1 lds\n"
    "s_add_u32 s76, s76, 16384\n"
    "v_cvt_pk_bf16_f32 v66, v68, v69\n"
    "v_cvt_pk_bf16_f32 v67, v70, v71\n"
    "v_mfma_f32_16x16x32_bf16 v[52:55], v[224:227], v[20:23], v[52:55]\n"
    "s_nop 0\n"
    "v_mfma_f32_16x16x32_bf16 v[48:51], v[232:235], v[24:27], v[48:51]\n"
    "v_mfma_f32_16x16x32_bf16 v[52:55], v[236:239], v[24:27], v[52:55]\n"
    "s_waitcnt lgkmcnt(4)\n"
    "v_mfma_f32_16x16x32_bf16 v[48:51], v[244:247], v[28:31], v[48:51]\n"
    "s_waitcnt lgkmcnt(3)\n"
    "v_permlane16_swap_b32 v64, v66\n"
    "v_mfma_f32_16x16x32_bf16 v[52:55], v[248:251], v[28:31], v[52:55]\n"
    "v_mfma_f32_16x16x32_bf16 v[56:59], v[208:211], v[32:35], v[56:59]\n"
    "s_waitcnt lgkmcnt(2)\n"
    "s_add_u32 m0, s77, s59\n"
    "s_add_u32 s77, s77, 8192\n"
    "buffer_load_dwordx4 v1, s[36:39], s76 offen sc1 lds\n"
    "v_permlane16_swap_b32 v65, v67\n"
    "v_mfma_f32_16x16x32_bf16 v[60:63], v[212:215], v[32:35], v[60:63]\n"
    "v_cvt_pk_bf16_f32 v48, v48, v49\n"
    "v_mfma_f32_16x16x32_bf16 v[56:59], v[220:223], v[36:39], v[56:59]\n"
    "s_waitcnt lgkmcnt(1)\n"
    "buffer_store_dwordx4 v[64:67], v4, s[36:39], s73 offen sc1\n"
    "v_cvt_pk_bf16_f32 v49, v50, v51\n"
    "v_mfma_f32_16x16x32_bf16 v[60:63], v[224:227], v[36:39], v[60:63]\n"
    "v_cvt_pk_bf16_f32 v50, v52, v53\n"
    "v_mfma_f32_16x16x32_bf16 v[56:59], v[232:235], v[40:43], v[56:59]\n"
    "s_waitcnt lgkmcnt(0)\n"
    "v_cvt_pk_bf16_f32 v51, v54, v55\n"
    "v_mfma_f32_16x16x32_bf16 v[60:63], v[236:239], v[40:43], v[60:63]\n"
    "v_mfma_f32_16x16x32_bf16 v[56:59], v[244:247], v[44:47], v[56:59]\n"
    "buffer_load_dwordx4 v1, s[36:39], s76 offen offset:1024 sc1 lds\n"
    "s_add_u32 s76, s76, 16384\n"
    "v_mfma_f32_16x16x32_bf16 v[60:63], v[248:251], v[44:47], v[60:63]\n"
    "buffer_store_dwordx4 v[48:51], v15, s[36:39], s73 offen sc1\n"
    "s_nop 2\n"
    "v_cvt_pk_bf16_f32 v56, v56, v57\n"
    "v_cvt_pk_bf16_f32 v57, v58, v59\n"
    "s_nop 1\n"
    "v_cvt_pk_bf16_f32 v58, v60, v61\n"
    "v_cvt_pk_bf16_f32 v59, v62, v63\n"
    "buffer_store_dwordx4 v[56:59], v15, s[36:39], s73 offen offset:1024 sc1\n"
    "v_mov_b32 v12, s68\n"
    "s_mov_b32 s68, s71\n"
    "s_add_u32 m0, s77, s59\n"
    "s_add_u32 s77, s77, 8192\n"
    "buffer_load_dwordx4 v1, s[36:39], s76 offen sc1 lds\n"
    "buffer_load_dwordx4 v1, s[36:39], s76 offen offset:1024 sc1 lds\n"
    "s_add_u32 s76, s76, 16384\n"
    "s_add_u32 m0, s77, s59\n"
    "s_add_u32 s77, s77, 8192\n"
    "buffer_load_dwordx4 v1, s[36:39], s76 offen sc1 lds\n"
    "buffer_load_dwordx4 v1, s[36:39], s76 offen offset:1024 sc1 lds\n"
    "s_add_u32 s76, s76, 16384\n"
    "s_add_u32 m0, s77, s59\n"
    "s_add_u32 s77, s77, 8192\n"
    "buffer_load_dwordx4 v1, s[36:39], s76 offen sc1 lds\n"
    "buffer_load_dwordx4 v1, s[36:39], s76 offen offset:1024 sc1 lds\n"
    "s_add_u32 s76, s76, 16384\n"
    "s_add_u32 m0, s77, s59\n"
    "s_add_u32 s77, s77, 8192\n"
    "buffer_load_dwordx4 v1, s[36:39], s76 offen sc1 lds\n"
    "buffer_load_dwordx4 v1, s[36:39], s76 offen offset:1024 sc1 lds\n"
    "s_add_u32 s76, s76, 16384\n"
    "s_add_u32 m0, s77, s59\n"
    "s_add_u32 s77, s77, 8192\n"
    "buffer_load_dwordx4 v1, s[36:39], s76 offen sc1 lds\n"
    "buffer_load_dwordx4 v1, s[36:39], s76 offen offset:1024 sc1 lds\n"
    "s_add_u32 s76, s76, 16384\n"
    "s_add_u32 m0, s77, s59\n"
    "s_add_u32 s77, s77, 8192\n"
    "buffer_load_dwordx4 v1, s[36:39], s76 offen sc1 lds\n"
    "buffer_load_dwordx4 v1, s[36:39], s76 offen offset:1024 sc1 lds\n"
    "s_add_u32 s76, s76, 16384\n"
    "s_add_u32 m0, s77, s59\n"
    "s_add_u32 s77, s77, 8192\n"
    "buffer_load_dwordx4 v1, s[36:39], s76 offen sc1 lds\n"
    "buffer_load_dwordx4 v1, s[36:39], s76 offen offset:1024 sc1 lds\n"
    "s_add_u32 s76, s76, 16384\n"
    "s_waitcnt vmcnt(14)\n"
    "buffer_atomic_smax v12, v14, s[36:39], 0 offen sc1\n"
    "s_waitcnt vmcnt(1)\n"
    "s_branch .LgDOWN_bar_%=\n"
    ".LgDOWN_idle_%=:\n"
    "s_or_b64 exec, vcc, 1\n"
    "buffer_atomic_smax v11, v2, s[36:39], 0 offen sc0 sc1\n"
    "s_mov_b64 exec, -1\n"
    "s_waitcnt vmcnt(1)\n"
    "buffer_atomic_smax v12, v14, s[36:39], 0 offen sc1\n"
    "s_branch .LgDOWN_block0_%=\n"
    ".LgDOWN_block_%=:\n"
    "v_add_u32 v13, v11, v3\n"
    "v_cmp_gt_i32 vcc, s71, v13\n"
    "s_nop 0\n"
    "s_or_b64 exec, vcc, 1\n"
    "buffer_atomic_smax v11, v2, s[36:39], 0 offen sc0 sc1\n"
    "s_mov_b64 exec, -1\n"
    "s_sleep 3\n"
    "s_waitcnt vmcnt(1)\n"
    ".LgDOWN_block0_%=:\n"
    "v_add_u32 v13, v5, v3\n"
    "v_cmp_gt_i32 vcc, s71, v13\n"
    "s_cbranch_vccz .LgDOWN_go_%=\n"
    "v_add_u32 v13, v5, v3\n"
    "v_cmp_gt_i32 vcc, s71, v13\n"
    "s_nop 0\n"
    "s_or_b64 exec, vcc, 1\n"
    "buffer_atomic_smax v5, v2, s[36:39], 0 offen sc0 sc1\n"
    "s_mov_b64 exec, -1\n"
    "s_sleep 3\n"
    "s_waitcnt vmcnt(1)\n"
    ".LgDOWN_block1_%=:\n"
    "v_add_u32 v13, v11, v3\n"
    "v_cmp_gt_i32 vcc, s71, v13\n"
    "s_cbranch_vccz .LgDOWN_go_%=\n"
    "s_sub_u32 s72, s72, 1\n"
    "s_cbranch_scc0 .LgDOWN_block_%=\n"
    "s_mov_b32 s72, 0\n"
    ".LgDOWN_go_%=:\n"
    "s_and_b32 s76, s71, s54\n"
    "s_mul_i32 s76, s76, s53\n"
    "s_add_u32 s76, s76, s52\n"
    "s_and_b32 s77, s71, 1\n"
    "s_mul_i32 s77, s77, 81920\n"
    "s_add_u32 s77, s77, s59\n"
    "s_add_u32 m0, s77, s59\n"
    "s_add_u32 s77, s77, 8192\n"
    "buffer_load_dwordx4 v1, s[36:39], s76 offen sc1 lds\n"
    "buffer_load_dwordx4 v1, s[36:39], s76 offen offset:1024 sc1 lds\n"
    "s_add_u32 s76, s76, 16384\n"
    "s_add_u32 m0, s77, s59\n"
    "s_add_u32 s77, s77, 8192\n"
    "buffer_load_dwordx4 v1, s[36:39], s76 offen sc1 lds\n"
    "buffer_load_dwordx4 v1, s[36:39], s76 offen offset:1024 sc1 lds\n"
    "s_add_u32 s76, s76, 16384\n"
    "s_add_u32 m0, s77, s59\n"
    "s_add_u32 s77, s77, 8192\n"
    "buffer_load_dwordx4 v1, s[36:39], s76 offen sc1 lds\n"
    "buffer_load_dwordx4 v1, s[36:39], s76 offen offset:1024 sc1 lds\n"
    "s_add_u32 s76, s76, 16384\n"
    "s_add_u32 m0, s77, s59\n"
    "s_add_u32 s77, s77, 8192\n"
    "buffer_load_dwordx4 v1, s[36:39], s76 offen sc1 lds\n"
    "buffer_load_dwordx4 v1, s[36:39], s76 offen offset:1024 sc1 lds\n"
    "s_add_u32 s76, s76, 16384\n"
    "s_add_u32 m0, s77, s59\n"
    "s_add_u32 s77, s77, 8192\n"
    "buffer_load_dwordx4 v1, s[36:39], s76 offen sc1 lds\n"
    "buffer_load_dwordx4 v1, s[36:39], s76 offen offset:1024 sc1 lds\n"
    "s_add_u32 s76, s76, 16384\n"
    "s_add_u32 m0, s77, s59\n"
    "s_add_u32 s77, s77, 8192\n"
    "buffer_load_dwordx4 v1, s[36:39], s76 offen sc1 lds\n"
    "buffer_load_dwordx4 v1, s[36:39], s76 offen offset:1024 sc1 lds\n"
    "s_add_u32 s76, s76, 16384\n"
    "s_add_u32 m0, s77, s59\n"
    "s_add_u32 s77, s77, 8192\n"
    "buffer_load_dwordx4 v1, s[36:39], s76 offen sc1 lds\n"
    "buffer_load_dwordx4 v1, s[36:39], s76 offen offset:1024 sc1 lds\n"
    "s_add_u32 s76, s76, 16384\n"
    "s_add_u32 m0, s77, s59\n"
    "s_add_u32 s77, s77, 8192\n"
    "buffer_load_dwordx4 v1, s[36:39], s76 offen sc1 lds\n"
    "buffer_load_dwordx4 v1, s[36:39], s76 offen offset:1024 sc1 lds\n"
    "s_add_u32 s76, s76, 16384\n"
    "s_add_u32 m0, s77, s59\n"
    "s_add_u32 s77, s77, 8192\n"
    "buffer_load_dwordx4 v1, s[36:39], s76 offen sc1 lds\n"
    "buffer_load_dwordx4 v1, s[36:39], s76 offen offset:1024 sc1 lds\n"
    "s_add_u32 s76, s76, 16384\n"
    "s_waitcnt vmcnt(0)\n"
    "s_branch .LgDOWN_bar_%=\n"
    ".LgDOWN_FIN_%=:\n"
  );
}

DEVI void role_rec(const Params& p, int L, int j, int i, u32 dbg, unsigned long long tag, unsigned char* smem) {
  const int tid = threadIdx.x, lane = tid & 63, w = (int)rfl(tid >> 6), h = 8 * j + i;
  const int c = lane & 15, g = lane >> 4, pp = lane & 3;
  {
    v4u t;
    if (w == 0 || w == 3) {
#pragma unroll
      for (int d = 0; d < 4; ++d) {
        u32 v = 0;
#pragma unroll
        for (int e = 0; e < 2; ++e) {
          const int q = 2 * d + e;
          const bool on = w == 0 ? ((q >> 2) == (c >> 3) && ((4 * g + (q & 3)) & 7) == (c & 7)) : (q == (c & 7));
          v |= on ? 0x3F80u << (16 * e) : 0u;
        }
        t[d] = v;
      }
    } else if (w == 1) {
      t.x = (u32)(((g & 1) * 16 + c) * 8);
      t.y = (u32)(g * 128 + (c & 7) * 16 + (c >> 3) * 8);
      t.z = c < 8 ? (u32)(g * 128 + c * 16) : OOB;
      t.w = (u32)(g * 32768 + c * 16);
    } else {
      t.x = (u32)(g * 16384 + c * 16);
      t.y = g == 0 ? (u32)(c * 16) : OOB;
      t.z = (u32)((g >> 1) * 128 + (c & 7) * 16 + (2 * (g & 1) + (c >> 3)) * 32768);
      t.w = lane < 32 ? (u32)lane * 64 : OOB;
    }
    *(v4u*)(smem + 12288 + tid * 16) = t;
    asm volatile("s_waitcnt lgkmcnt(0)" ::: "memory");
  }
  const u32 wb = 576u * 4096 * 2 - h * 256;
  v16u sa = cat4(make_srd(p.ws, WS_END), make_srd(p.Wq + h * 128, wb), make_srd(p.Wk + h * 128, wb), make_srd(p.Wv + h * 128, wb));
  const v4u so = make_srd(p.Wo + h * 128 * 576, 128u * 1152);
  const int mine = 4 * i + j, unit = w == 0 ? mine : ((w == 1 && i == 3) ? 32 + j : -1);
  const u32 hot = HOT + L * 8192, hot2 = HOT2 + (1 - L) * 8192, img = hot2 + j * 2 * CHUNK;
  v16u sb = {so.x, so.y, so.z, so.w,
             pga(L, B_QKV + 3 * h + 1) + w * 2048, pga(L, B_KVM + h) + w * 1536, mbox_att(L, j, i, w), mbox(L, j, SL_REC + i, w),
             (u32)w * 1024, dbg + w * 64, INITF + L * 8192, hot + 4 * w * CHUNK + h * 256,
             unit < 0 ? OOB : hot + unit * CHUNK, unit < 0 ? OOB : hot2 + (unit >> 5) * CHUNK + (unit & 31) * 256, img, hot + (32 + w) * CHUNK + h * 256};
  const int jr = lane >> 3;
  const u32 row = (u32)(16 * (w >> 1) + 8 * ((jr >> 1) & 1) + 4 * (w & 1) + 2 * (jr >> 2) + (jr & 1));
  const u32 ch = (((u32)lane & 7) ^ wswz(jr, w)) << 4;
  v16u va = {0, 0, 0, 0, 0, 0, 0, 0, 0, 0, 0, 0, 0, 0, 0, 0};
  va[0] = lane * 16;
  va[1] = 16384 + w * 2048 + lane * 16;
  va[2] = (u32)((c >> 3) * 0xAAAA) | 0x4444u;
  const int qq = (lane >> 2) & 3;
  va[3] = (u32)((g >> 1) * 4096 + (g & 1) * 2048 + (4 * (qq >> 1) + (qq & 1)) * 128 + ((2 * w + (pp >> 1)) << 4) + 8 * (pp & 1));
  va[4] = row * 8192 + ch;
  va[5] = row * 1152 + ch;
  va[6] = (u32)(w >> 1) * 4096 + tr_base(lane, 4 * (w & 1) + pp);
  va[7] = (u32)w * 4096 + tr_base(lane, 4 * (qq >> 1) + (pp >> 1)) + 8 * (pp & 1);
  va[8] = hot2 + 32 * CHUNK + ((w == 0 && i < 4) ? 0 : 1);
  va[9] = (u32)(h * 64 + 4 * w);
  va[10] = hot + mine * CHUNK + w * 2048;
  va[11] = w == 3 ? hot + (32 + j) * CHUNK + i * 1024 : (w == 2 ? img + i * 1024 : ((w == 1 && i == 1) ? img + CHUNK : OOB));
  va[12] = w == 3 ? ~0u : 0u;
  va[13] = w == 3 ? 0u : ~0u;
  va[14] = (u32)(2 * tag); va[15] = (u32)((2 * tag) >> 32);
  RUN_ASM(
    "s_nop 4\n"
    "s_getpc_b64 s[88:89]\n"
    ".Lrec_PC_%=:\n"
    "s_and_b32 s89, s89, 0xffff\n"
    "s_mov_b32 s90, .Lrec_FIN_%=-.Lrec_PC_%=\n"
    "s_mov_b32 s91, 0x20000\n"
    "v_mbcnt_lo_u32_b32 v78, -1, 0\n"
    "v_mbcnt_hi_u32_b32 v78, -1, v78\n"
    "v_lshlrev_b32 v78, 7, v78\n"
    "buffer_load_dword v79, v78, s[88:91], 0 offen\n"
    "v_add_u32 v78, 8192, v78\n"
    "buffer_load_dword v79, v78, s[88:91], 0 offen\n"
    "v_readfirstlane_b32 s76, v8\n"
    "v_readfirstlane_b32 s77, v9\n"
    "v_readfirstlane_b32 s78, v10\n"
    "v_readfirstlane_b32 s79, v11\n"
    "v_readfirstlane_b32 s80, v12\n"
    "v_readfirstlane_b32 s81, v13\n"
    "v_readfirstlane_b32 s30, v14\n"
    "v_readfirstlane_b32 s31, v15\n"
    "s_memtime s[34:35]\n"
    "s_memrealtime s[92:93]\n"
    "s_mov_b32 s69, 262144\n"
    "s_add_u32 m0, s60, 16384\n"
    "s_mov_b32 s73, 0\n"
    "buffer_load_dwordx4 v4, s[40:43], s73 offen lds\n"
    "s_add_u32 m0, s60, 20480\n"
    "s_mov_b32 s73, 128\n"
    "buffer_load_dwordx4 v4, s[40:43], s73 offen lds\n"
    "s_add_u32 m0, s60, 24576\n"
    "s_mov_b32 s73, 0\n"
    "buffer_load_dwordx4 v4, s[44:47], s73 offen lds\n"
    "s_add_u32 m0, s60, 28672\n"
    "s_mov_b32 s73, 128\n"
    "buffer_load_dwordx4 v4, s[44:47], s73 offen lds\n"
    "s_add_u32 m0, s60, 32768\n"
    "s_mov_b32 s73, 0\n"
    "buffer_load_dwordx4 v4, s[48:51], s73 offen lds\n"
    "s_add_u32 m0, s60, 36864\n"
    "s_mov_b32 s73, 128\n"
    "buffer_load_dwordx4 v4, s[48:51], s73 offen lds\n"
    "s_add_u32 m0, s60, 40960\n"
    "s_mov_b32 s73, 262144\n"
    "buffer_load_dwordx4 v4, s[40:43], s73 offen lds\n"
    "s_add_u32 m0, s60, 45056\n"
    "s_mov_b32 s73, 262272\n"
    "buffer_load_dwordx4 v4, s[40:43], s73 offen lds\n"
    "s_add_u32 m0, s60, 49152\n"
    "s_mov_b32 s73, 262144\n"
    "buffer_load_dwordx4 v4, s[44:47], s73 offen lds\n"
    "s_add_u32 m0, s60, 53248\n"
    "s_mov_b32 s73, 262272\n"
    "buffer_load_dwordx4 v4, s[44:47], s73 offen lds\n"
    "s_add_u32 m0, s60, 57344\n"
    "s_mov_b32 s73, 262144\n"
    "buffer_load_dwordx4 v4, s[48:51], s73 offen lds\n"
    "s_add_u32 m0, s60, 61440\n"
    "s_mov_b32 s73, 262272\n"
    "buffer_load_dwordx4 v4, s[48:51], s73 offen lds\n"
    "s_add_u32 m0, s60, 65536\n"
    "s_mov_b32 s73, 524288\n"
    "buffer_load_dwordx4 v4, s[40:43], s73 offen lds\n"
    "s_add_u32 m0, s60, 69632\n"
    "s_mov_b32 s73, 524416\n"
    "buffer_load_dwordx4 v4, s[40:43], s73 offen lds\n"
    "s_add_u32 m0, s60, 73728\n"
    "s_mov_b32 s73, 524288\n"
    "buffer_load_dwordx4 v4, s[44:47], s73 offen lds\n"
    "s_add_u32 m0, s60, 77824\n"
    "s_mov_b32 s73, 524416\n"
    "buffer_load_dwordx4 v4, s[44:47], s73 offen lds\n"
    "s_add_u32 m0, s60, 81920\n"
    "s_mov_b32 s73, 524288\n"
    "buffer_load_dwordx4 v4, s[48:51], s73 offen lds\n"
    "s_add_u32 m0, s60, 86016\n"
    "s_mov_b32 s73, 524416\n"
    "buffer_load_dwordx4 v4, s[48:51], s73 offen lds\n"
    "s_add_u32 m0, s60, 90112\n"
    "s_mov_b32 s73, 786432\n"
    "buffer_load_dwordx4 v4, s[40:43], s73 offen lds\n"
    "s_add_u32 m0, s60, 94208\n"
    "s_mov_b32 s73, 786560\n"
    "buffer_load_dwordx4 v4, s[40:43], s73 offen lds\n"
    "s_add_u32 m0, s60, 98304\n"
    "s_mov_b32 s73, 786432\n"
    "buffer_load_dwordx4 v4, s[44:47], s73 offen lds\n"
    "s_add_u32 m0, s60, 102400\n"
    "s_mov_b32 s73, 786560\n"
    "buffer_load_dwordx4 v4, s[44:47], s73 offen lds\n"
    "s_add_u32 m0, s60, 106496\n"
    "s_mov_b32 s73, 786432\n"
    "buffer_load_dwordx4 v4, s[48:51], s73 offen lds\n"
    "s_add_u32 m0, s60, 110592\n"
    "s_mov_b32 s73, 786560\n"
    "buffer_load_dwordx4 v4, s[48:51], s73 offen lds\n"
    "s_add_u32 m0, s60, 114688\n"
    "s_mov_b32 s73, 1048576\n"
    "buffer_load_dwordx4 v4, s[40:43], s73 offen lds\n"
    "s_add_u32 m0, s60, 118784\n"
    "s_mov_b32 s73, 1048704\n"
    "buffer_load_dwordx4 v4, s[40:43], s73 offen lds\n"
    "s_add_u32 m0, s60, 122880\n"
    "s_mov_b32 s73, 1048576\n"
    "buffer_load_dwordx4 v4, s[44:47], s73 offen lds\n"
    "s_add_u32 m0, s60, 126976\n"
    "s_mov_b32 s73, 1048704\n"
    "buffer_load_dwordx4 v4, s[44:47], s73 offen lds\n"
    "s_add_u32 m0, s60, 131072\n"
    "s_mov_b32 s73, 1048576\n"
    "buffer_load_dwordx4 v4, s[48:51], s73 offen lds\n"
    "s_add_u32 m0, s60, 135168\n"
    "s_mov_b32 s73, 1048704\n"
    "buffer_load_dwordx4 v4, s[48:51], s73 offen lds\n"
    "s_add_u32 m0, s60, 139264\n"
    "s_mov_b32 s73, 1310720\n"
    "buffer_load_dwordx4 v4, s[40:43], s73 offen lds\n"
    "s_add_u32 m0, s60, 143360\n"
    "s_mov_b32 s73, 1310848\n"
    "buffer_load_dwordx4 v4, s[40:43], s73 offen lds\n"
    "s_add_u32 m0, s60, 147456\n"
    "s_mov_b32 s73, 1310720\n"
    "buffer_load_dwordx4 v4, s[44:47], s73 offen lds\n"
    "s_add_u32 m0, s60, 151552\n"
    "s_mov_b32 s73, 1310848\n"
    "buffer_load_dwordx4 v4, s[44:47], s73 offen lds\n"
    "s_add_u32 m0, s60, 155648\n"
    "s_mov_b32 s73, 1310720\n"
    "buffer_load_dwordx4 v4, s[48:51], s73 offen lds\n"
    "s_add_u32 m0, s60, 159744\n"
    "s_mov_b32 s73, 1310848\n"
    "buffer_load_dwordx4 v4, s[48:51], s73 offen lds\n"
    "s_waitcnt vmcnt(30) lgkmcnt(0)\n"
    "s_barrier\n"
    "v_add_u32 v8, 16384, v6\n"
    "ds_read_b64_tr_b16 a[0:1], v8 offset:0\n"
    "ds_read_b64_tr_b16 a[2:3], v8 offset:512\n"
    "ds_read_b64_tr_b16 a[8:9], v8 offset:8\n"
    "ds_read_b64_tr_b16 a[10:11], v8 offset:520\n"
    "ds_read_b64_tr_b16 a[144:145], v8 offset:8192\n"
    "ds_read_b64_tr_b16 a[146:147], v8 offset:8704\n"
    "ds_read_b64_tr_b16 a[152:153], v8 offset:8200\n"
    "ds_read_b64_tr_b16 a[154:155], v8 offset:8712\n"
    "ds_read_b64_tr_b16 v[112:113], v8 offset:16384\n"
    "ds_read_b64_tr_b16 v[114:115], v8 offset:16896\n"
    "ds_read_b64_tr_b16 v[224:225], v8 offset:16392\n"
    "ds_read_b64_tr_b16 v[226:227], v8 offset:16904\n"
    "s_waitcnt vmcnt(24) lgkmcnt(0)\n"
    "s_barrier\n"
    "s_add_u32 m0, s60, 16384\n"
    "s_mov_b32 s73, 1572864\n"
    "buffer_load_dwordx4 v4, s[40:43], s73 offen lds\n"
    "s_add_u32 m0, s60, 20480\n"
    "s_mov_b32 s73, 1572992\n"
    "buffer_load_dwordx4 v4, s[40:43], s73 offen lds\n"
    "s_add_u32 m0, s60, 24576\n"
    "s_mov_b32 s73, 1572864\n"
    "buffer_load_dwordx4 v4, s[44:47], s73 offen lds\n"
    "s_add_u32 m0, s60, 28672\n"
    "s_mov_b32 s73, 1572992\n"
    "buffer_load_dwordx4 v4, s[44:47], s73 offen lds\n"
    "s_add_u32 m0, s60, 32768\n"
    "s_mov_b32 s73, 1572864\n"
    "buffer_load_dwordx4 v4, s[48:51], s73 offen lds\n"
    "s_add_u32 m0, s60, 36864\n"
    "s_mov_b32 s73, 1572992\n"
    "buffer_load_dwordx4 v4, s[48:51], s73 offen lds\n"
    "v_add_u32 v8, 40960, v6\n"
    "ds_read_b64_tr_b16 a[4:5], v8 offset:0\n"
    "ds_read_b64_tr_b16 a[6:7], v8 offset:512\n"
    "ds_read_b64_tr_b16 a[12:13], v8 offset:8\n"
    "ds_read_b64_tr_b16 a[14:15], v8 offset:520\n"
    "ds_read_b64_tr_b16 a[148:149], v8 offset:8192\n"
    "ds_read_b64_tr_b16 a[150:151], v8 offset:8704\n"
    "ds_read_b64_tr_b16 a[156:157], v8 offset:8200\n"
    "ds_read_b64_tr_b16 a[158:159], v8 offset:8712\n"
    "ds_read_b64_tr_b16 v[116:117], v8 offset:16384\n"
    "ds_read_b64_tr_b16 v[118:119], v8 offset:16896\n"
    "ds_read_b64_tr_b16 v[228:229], v8 offset:16392\n"
    "ds_read_b64_tr_b16 v[230:231], v8 offset:16904\n"
    "s_waitcnt vmcnt(24) lgkmcnt(0)\n"
    "s_barrier\n"
    "s_add_u32 m0, s60, 40960\n"
    "s_mov_b32 s73, 1835008\n"
    "buffer_load_dwordx4 v4, s[40:43], s73 offen lds\n"
    "s_add_u32 m0, s60, 45056\n"
    "s_mov_b32 s73, 1835136\n"
    "buffer_load_dwordx4 v4, s[40:43], s73 offen lds\n"
    "s_add_u32 m0, s60, 49152\n"
    "s_mov_b32 s73, 1835008\n"
    "buffer_load_dwordx4 v4, s[44:47], s73 offen lds\n"
    "s_add_u32 m0, s60, 53248\n"
    "s_mov_b32 s73, 1835136\n"
    "buffer_load_dwordx4 v4, s[44:47], s73 offen lds\n"
    "s_add_u32 m0, s60, 57344\n"
    "s_mov_b32 s73, 1835008\n"
    "buffer_load_dwordx4 v4, s[48:51], s73 offen lds\n"
    "s_add_u32 m0, s60, 61440\n"
    "s_mov_b32 s73, 1835136\n"
    "buffer_load_dwordx4 v4, s[48:51], s73 offen lds\n"
    "v_add_u32 v8, 65536, v6\n"
    "ds_read_b64_tr_b16 a[16:17], v8 offset:0\n"
    "ds_read_b64_tr_b16 a[18:19], v8 offset:512\n"
    "ds_read_b64_tr_b16 a[24:25], v8 offset:8\n"
    "ds_read_b64_tr_b16 a[26:27], v8 offset:520\n"
    "ds_read_b64_tr_b16 a[160:161], v8 offset:8192\n"
    "ds_read_b64_tr_b16 a[162:163], v8 offset:8704\n"
    "ds_read_b64_tr_b16 a[168:169], v8 offset:8200\n"
    "ds_read_b64_tr_b16 a[170:171], v8 offset:8712\n"
    "ds_read_b64_tr_b16 v[120:121], v8 offset:16384\n"
    "ds_read_b64_tr_b16 v[122:123], v8 offset:16896\n"
    "ds_read_b64_tr_b16 v[232:233], v8 offset:16392\n"
    "ds_read_b64_tr_b16 v[234:235], v8 offset:16904\n"
    "s_waitcnt vmcnt(24) lgkmcnt(0)\n"
    "s_barrier\n"
    "s_add_u32 m0, s60, 65536\n"
    "s_mov_b32 s73, 2097152\n"
    "buffer_load_dwordx4 v4, s[40:43], s73 offen lds\n"
    "s_add_u32 m0, s60, 69632\n"
    "s_mov_b32 s73, 2097280\n"
    "buffer_load_dwordx4 v4, s[40:43], s73 offen lds\n"
    "s_add_u32 m0, s60, 73728\n"
    "s_mov_b32 s73, 2097152\n"
    "buffer_load_dwordx4 v4, s[44:47], s73 offen lds\n"
    "s_add_u32 m0, s60, 77824\n"
    "s_mov_b32 s73, 2097280\n"
    "buffer_load_dwordx4 v4, s[44:47], s73 offen lds\n"
    "s_add_u32 m0, s60, 81920\n"
    "s_mov_b32 s73, 2097152\n"
    "buffer_load_dwordx4 v4, s[48:51], s73 offen lds\n"
    "s_add_u32 m0, s60, 86016\n"
    "s_mov_b32 s73, 2097280\n"
    "buffer_load_dwordx4 v4, s[48:51], s73 offen lds\n"
    "v_add_u32 v8, 90112, v6\n"
    "ds_read_b64_tr_b16 a[20:21], v8 offset:0\n"
    "ds_read_b64_tr_b16 a[22:23], v8 offset:512\n"
    "ds_read_b64_tr_b16 a[28:29], v8 offset:8\n"
    "ds_read_b64_tr_b16 a[30:31], v8 offset:520\n"
    "ds_read_b64_tr_b16 a[164:165], v8 offset:8192\n"
    "ds_read_b64_tr_b16 a[166:167], v8 offset:8704\n"
    "ds_read_b64_tr_b16 a[172:173], v8 offset:8200\n"
    "ds_read_b64_tr_b16 a[174:175], v8 offset:8712\n"
    "ds_read_b64_tr_b16 v[124:125], v8 offset:16384\n"
    "ds_read_b64_tr_b16 v[126:127], v8 offset:16896\n"
    "ds_read_b64_tr_b16 v[236:237], v8 offset:16392\n"
    "ds_read_b64_tr_b16 v[238:239], v8 offset:16904\n"
    "s_waitcnt vmcnt(24) lgkmcnt(0)\n"
    "s_barrier\n"
    "s_add_u32 m0, s60, 90112\n"
    "s_mov_b32 s73, 2359296\n"
    "buffer_load_dwordx4 v4, s[40:43], s73 offen lds\n"
    "s_add_u32 m0, s60, 94208\n"
    "s_mov_b32 s73, 2359424\n"
    "buffer_load_dwordx4 v4, s[40:43], s73 offen lds\n"
    "s_add_u32 m0, s60, 98304\n"
    "s_mov_b32 s73, 2359296\n"
    "buffer_load_dwordx4 v4, s[44:47], s73 offen lds\n"
    "s_add_u32 m0, s60, 102400\n"
    "s_mov_b32 s73, 2359424\n"
    "buffer_load_dwordx4 v4, s[44:47], s73 offen lds\n"
    "s_add_u32 m0, s60, 106496\n"
    "s_mov_b32 s73, 2359296\n"
    "buffer_load_dwordx4 v4, s[48:51], s73 offen lds\n"
    "s_add_u32 m0, s60, 110592\n"
    "s_mov_b32 s73, 2359424\n"
    "buffer_load_dwordx4 v4, s[48:51], s73 offen lds\n"
    "v_add_u32 v8, 114688, v6\n"
    "ds_read_b64_tr_b16 a[32:33], v8 offset:0\n"
    "ds_read_b64_tr_b16 a[34:35], v8 offset:512\n"
    "ds_read_b64_tr_b16 a[40:41], v8 offset:8\n"
    "ds_read_b64_tr_b16 a[42:43], v8 offset:520\n"
    "ds_read_b64_tr_b16 a[176:177], v8 offset:8192\n"
    "ds_read_b64_tr_b16 a[178:179], v8 offset:8704\n"
    "ds_read_b64_tr_b16 a[184:185], v8 offset:8200\n"
    "ds_read_b64_tr_b16 a[186:187], v8 offset:8712\n"
    "ds_read_b64_tr_b16 v[128:129], v8 offset:16384\n"
    "ds_read_b64_tr_b16 v[130:131], v8 offset:16896\n"
    "ds_read_b64_tr_b16 v[240:241], v8 offset:16392\n"
    "ds_read_b64_tr_b16 v[242:243], v8 offset:16904\n"
    "s_waitcnt vmcnt(24) lgkmcnt(0)\n"
    "s_barrier\n"
    "s_add_u32 m0, s60, 114688\n"
    "s_mov_b32 s73, 2621440\n"
    "buffer_load_dwordx4 v4, s[40:43], s73 offen lds\n"
    "s_add_u32 m0, s60, 118784\n"
    "s_mov_b32 s73, 2621568\n"
    "buffer_load_dwordx4 v4, s[40:43], s73 offen lds\n"
    "s_add_u32 m0, s60, 122880\n"
    "s_mov_b32 s73, 2621440\n"
    "buffer_load_dwordx4 v4, s[44:47], s73 offen lds\n"
    "s_add_u32 m0, s60, 126976\n"
    "s_mov_b32 s73, 2621568\n"
    "buffer_load_dwordx4 v4, s[44:47], s73 offen lds\n"
    "s_add_u32 m0, s60, 131072\n"
    "s_mov_b32 s73, 2621440\n"
    "buffer_load_dwordx4 v4, s[48:51], s73 offen lds\n"
    "s_add_u32 m0, s60, 135168\n"
    "s_mov_b32 s73, 2621568\n"
    "buffer_load_dwordx4 v4, s[48:51], s73 offen lds\n"
    "v_add_u32 v8, 139264, v6\n"
    "ds_read_b64_tr_b16 a[36:37], v8 offset:0\n"
    "ds_read_b64_tr_b16 a[38:39], v8 offset:512\n"
    "ds_read_b64_tr_b16 a[44:45], v8 offset:8\n"
    "ds_read_b64_tr_b16 a[46:47], v8 offset:520\n"
    "ds_read_b64_tr_b16 a[180:181], v8 offset:8192\n"
    "ds_read_b64_tr_b16 a[182:183], v8 offset:8704\n"
    "ds_read_b64_tr_b16 a[188:189], v8 offset:8200\n"
    "ds_read_b64_tr_b16 a[190:191], v8 offset:8712\n"
    "ds_read_b64_tr_b16 v[132:133], v8 offset:16384\n"
    "ds_read_b64_tr_b16 v[134:135], v8 offset:16896\n"
    "ds_read_b64_tr_b16 v[244:245], v8 offset:16392\n"
    "ds_read_b64_tr_b16 v[246:247], v8 offset:16904\n"
    "s_waitcnt vmcnt(24) lgkmcnt(0)\n"
    "s_barrier\n"
    "s_add_u32 m0, s60, 139264\n"
    "s_mov_b32 s73, 2883584\n"
    "buffer_load_dwordx4 v4, s[40:43], s73 offen lds\n"
    "s_add_u32 m0, s60, 143360\n"
    "s_mov_b32 s73, 2883712\n"
    "buffer_load_dwordx4 v4, s[40:43], s73 offen lds\n"
    "s_add_u32 m0, s60, 147456\n"
    "s_mov_b32 s73, 2883584\n"
    "buffer_load_dwordx4 v4, s[44:47], s73 offen lds\n"
    "s_add_u32 m0, s60, 151552\n"
    "s_mov_b32 s73, 2883712\n"
    "buffer_load_dwordx4 v4, s[44:47], s73 offen lds\n"
    "s_add_u32 m0, s60, 155648\n"
    "s_mov_b32 s73, 2883584\n"
    "buffer_load_dwordx4 v4, s[48:51], s73 offen lds\n"
    "s_add_u32 m0, s60, 159744\n"
    "s_mov_b32 s73, 2883712\n"
    "buffer_load_dwordx4 v4, s[48:51], s73 offen lds\n"
    "v_add_u32 v8, 16384, v6\n"
    "ds_read_b64_tr_b16 a[48:49], v8 offset:0\n"
    "ds_read_b64_tr_b16 a[50:51], v8 offset:512\n"
    "ds_read_b64_tr_b16 a[56:57], v8 offset:8\n"
    "ds_read_b64_tr_b16 a[58:59], v8 offset:520\n"
    "ds_read_b64_tr_b16 a[192:193], v8 offset:8192\n"
    "ds_read_b64_tr_b16 a[194:195], v8 offset:8704\n"
    "ds_read_b64_tr_b16 a[200:201], v8 offset:8200\n"
    "ds_read_b64_tr_b16 a[202:203], v8 offset:8712\n"
    "ds_read_b64_tr_b16 v[136:137], v8 offset:16384\n"
    "ds_read_b64_tr_b16 v[138:139], v8 offset:16896\n"
    "ds_read_b64_tr_b16 v[248:249], v8 offset:16392\n"
    "ds_read_b64_tr_b16 v[250:251], v8 offset:16904\n"
    "s_waitcnt vmcnt(24) lgkmcnt(0)\n"
    "s_barrier\n"
    "s_add_u32 m0, s60, 16384\n"
    "s_mov_b32 s73, 3145728\n"
    "buffer_load_dwordx4 v4, s[40:43], s73 offen lds\n"
    "s_add_u32 m0, s60, 20480\n"
    "s_mov_b32 s73, 3145856\n"
    "buffer_load_dwordx4 v4, s[40:43], s73 offen lds\n"
    "s_add_u32 m0, s60, 24576\n"
    "s_mov_b32 s73, 3145728\n"
    "buffer_load_dwordx4 v4, s[44:47], s73 offen lds\n"
    "s_add_u32 m0, s60, 28672\n"
    "s_mov_b32 s73, 3145856\n"
    "buffer_load_dwordx4 v4, s[44:47], s73 offen lds\n"
    "s_add_u32 m0, s60, 32768\n"
    "s_mov_b32 s73, 3145728\n"
    "buffer_load_dwordx4 v4, s[48:51], s73 offen lds\n"
    "s_add_u32 m0, s60, 36864\n"
    "s_mov_b32 s73, 3145856\n"
    "buffer_load_dwordx4 v4, s[48:51], s73 offen lds\n"
    "v_add_u32 v8, 40960, v6\n"
    "ds_read_b64_tr_b16 a[52:53], v8 offset:0\n"
    "ds_read_b64_tr_b16 a[54:55], v8 offset:512\n"
    "ds_read_b64_tr_b16 a[60:61], v8 offset:8\n"
    "ds_read_b64_tr_b16 a[62:63], v8 offset:520\n"
    "ds_read_b64_tr_b16 a[196:197], v8 offset:8192\n"
    "ds_read_b64_tr_b16 a[198:199], v8 offset:8704\n"
    "ds_read_b64_tr_b16 a[204:205], v8 offset:8200\n"
    "ds_read_b64_tr_b16 a[206:207], v8 offset:8712\n"
    "ds_read_b64_tr_b16 v[140:141], v8 offset:16384\n"
    "ds_read_b64_tr_b16 v[142:143], v8 offset:16896\n"
    "ds_read_b64_tr_b16 v[252:253], v8 offset:16392\n"
    "ds_read_b64_tr_b16 v[254:255], v8 offset:16904\n"
    "s_waitcnt vmcnt(24) lgkmcnt(0)\n"
    "s_barrier\n"
    "s_add_u32 m0, s60, 40960\n"
    "s_mov_b32 s73, 3407872\n"
    "buffer_load_dwordx4 v4, s[40:43], s73 offen lds\n"
    "s_add_u32 m0, s60, 45056\n"
    "s_mov_b32 s73, 3408000\n"
    "buffer_load_dwordx4 v4, s[40:43], s73 offen lds\n"
    "s_add_u32 m0, s60, 49152\n"
    "s_mov_b32 s73, 3407872\n"
    "buffer_load_dwordx4 v4, s[44:47], s73 offen lds\n"
    "s_add_u32 m0, s60, 53248\n"
    "s_mov_b32 s73, 3408000\n"
    "buffer_load_dwordx4 v4, s[44:47], s73 offen lds\n"
    "s_add_u32 m0, s60, 57344\n"
    "s_mov_b32 s73, 3407872\n"
    "buffer_load_dwordx4 v4, s[48:51], s73 offen lds\n"
    "s_add_u32 m0, s60, 61440\n"
    "s_mov_b32 s73, 3408000\n"
    "buffer_load_dwordx4 v4, s[48:51], s73 offen lds\n"
    "v_add_u32 v8, 65536, v6\n"
    "ds_read_b64_tr_b16 a[64:65], v8 offset:0\n"
    "ds_read_b64_tr_b16 a[66:67], v8 offset:512\n"
    "ds_read_b64_tr_b16 a[72:73], v8 offset:8\n"
    "ds_read_b64_tr_b16 a[74:75], v8 offset:520\n"
    "ds_read_b64_tr_b16 a[208:209], v8 offset:8192\n"
    "ds_read_b64_tr_b16 a[210:211], v8 offset:8704\n"
    "ds_read_b64_tr_b16 a[216:217], v8 offset:8200\n"
    "ds_read_b64_tr_b16 a[218:219], v8 offset:8712\n"
    "ds_read_b64_tr_b16 v[144:145], v8 offset:16384\n"
    "ds_read_b64_tr_b16 v[146:147], v8 offset:16896\n"
    "ds_read_b64_tr_b16 v[208:209], v8 offset:16392\n"
    "ds_read_b64_tr_b16 v[210:211], v8 offset:16904\n"
    "s_waitcnt vmcnt(24) lgkmcnt(0)\n"
    "s_barrier\n"
    "s_add_u32 m0, s60, 65536\n"
    "s_mov_b32 s73, 3670016\n"
    "buffer_load_dwordx4 v4, s[40:43], s73 offen lds\n"
    "s_add_u32 m0, s60, 69632\n"
    "s_mov_b32 s73, 3670144\n"
    "buffer_load_dwordx4 v4, s[40:43], s73 offen lds\n"
    "s_add_u32 m0, s60, 73728\n"
    "s_mov_b32 s73, 3670016\n"
    "buffer_load_dwordx4 v4, s[44:47], s73 offen lds\n"
    "s_add_u32 m0, s60, 77824\n"
    "s_mov_b32 s73, 3670144\n"
    "buffer_load_dwordx4 v4, s[44:47], s73 offen lds\n"
    "s_add_u32 m0, s60, 81920\n"
    "s_mov_b32 s73, 3670016\n"
    "buffer_load_dwordx4 v4, s[48:51], s73 offen lds\n"
    "s_add_u32 m0, s60, 86016\n"
    "s_mov_b32 s73, 3670144\n"
    "buffer_load_dwordx4 v4, s[48:51], s73 offen lds\n"
    "v_add_u32 v8, 90112, v6\n"
    "ds_read_b64_tr_b16 a[68:69], v8 offset:0\n"
    "ds_read_b64_tr_b16 a[70:71], v8 offset:512\n"
    "ds_read_b64_tr_b16 a[76:77], v8 offset:8\n"
    "ds_read_b64_tr_b16 a[78:79], v8 offset:520\n"
    "ds_read_b64_tr_b16 a[212:213], v8 offset:8192\n"
    "ds_read_b64_tr_b16 a[214:215], v8 offset:8704\n"
    "ds_read_b64_tr_b16 a[220:221], v8 offset:8200\n"
    "ds_read_b64_tr_b16 a[222:223], v8 offset:8712\n"
    "ds_read_b64_tr_b16 v[148:149], v8 offset:16384\n"
    "ds_read_b64_tr_b16 v[150:151], v8 offset:16896\n"
    "ds_read_b64_tr_b16 v[212:213], v8 offset:16392\n"
    "ds_read_b64_tr_b16 v[214:215], v8 offset:16904\n"
    "s_waitcnt vmcnt(24) lgkmcnt(0)\n"
    "s_barrier\n"
    "s_add_u32 m0, s60, 90112\n"
    "s_mov_b32 s73, 3932160\n"
    "buffer_load_dwordx4 v4, s[40:43], s73 offen lds\n"
    "s_add_u32 m0, s60, 94208\n"
    "s_mov_b32 s73, 3932288\n"
    "buffer_load_dwordx4 v4, s[40:43], s73 offen lds\n"
    "s_add_u32 m0, s60, 98304\n"
    "s_mov_b32 s73, 3932160\n"
    "buffer_load_dwordx4 v4, s[44:47], s73 offen lds\n"
    "s_add_u32 m0, s60, 102400\n"
    "s_mov_b32 s73, 3932288\n"
    "buffer_load_dwordx4 v4, s[44:47], s73 offen lds\n"
    "s_add_u32 m0, s60, 106496\n"
    "s_mov_b32 s73, 3932160\n"
    "buffer_load_dwordx4 v4, s[48:51], s73 offen lds\n"
    "s_add_u32 m0, s60, 110592\n"
    "s_mov_b32 s73, 3932288\n"
    "buffer_load_dwordx4 v4, s[48:51], s73 offen lds\n"
    "v_add_u32 v8, 114688, v6\n"
    "ds_read_b64_tr_b16 a[80:81], v8 offset:0\n"
    "ds_read_b64_tr_b16 a[82:83], v8 offset:512\n"
    "ds_read_b64_tr_b16 a[88:89], v8 offset:8\n"
    "ds_read_b64_tr_b16 a[90:91], v8 offset:520\n"
    "ds_read_b64_tr_b16 a[224:225], v8 offset:8192\n"
    "ds_read_b64_tr_b16 a[226:227], v8 offset:8704\n"
    "ds_read_b64_tr_b16 a[232:233], v8 offset:8200\n"
    "ds_read_b64_tr_b16 a[234:235], v8 offset:8712\n"
    "ds_read_b64_tr_b16 v[152:153], v8 offset:16384\n"
    "ds_read_b64_tr_b16 v[154:155], v8 offset:16896\n"
    "ds_read_b64_tr_b16 v[216:217], v8 offset:16392\n"
    "ds_read_b64_tr_b16 v[218:219], v8 offset:16904\n"
    "s_waitcnt vmcnt(24) lgkmcnt(0)\n"
    "s_barrier\n"
    "s_add_u32 m0, s60, 114688\n"
    "s_mov_b32 s73, 4194304\n"
    "buffer_load_dwordx4 v4, s[40:43], s73 offen lds\n"
    "s_add_u32 m0, s60, 118784\n"
    "s_mov_b32 s73, 4194432\n"
    "buffer_load_dwordx4 v4, s[40:43], s73 offen lds\n"
    "s_add_u32 m0, s60, 122880\n"
    "s_mov_b32 s73, 4194304\n"
    "buffer_load_dwordx4 v4, s[44:47], s73 offen lds\n"
    "s_add_u32 m0, s60, 126976\n"
    "s_mov_b32 s73, 4194432\n"
    "buffer_load_dwordx4 v4, s[44:47], s73 offen lds\n"
    "s_add_u32 m0, s60, 131072\n"
    "s_mov_b32 s73, 4194304\n"
    "buffer_load_dwordx4 v4, s[48:51], s73 offen lds\n"
    "s_add_u32 m0, s60, 135168\n"
    "s_mov_b32 s73, 4194432\n"
    "buffer_load_dwordx4 v4, s[48:51], s73 offen lds\n"
    "v_add_u32 v8, 139264, v6\n"
    "ds_read_b64_tr_b16 a[84:85], v8 offset:0\n"
    "ds_read_b64_tr_b16 a[86:87], v8 offset:512\n"
    "ds_read_b64_tr_b16 a[92:93], v8 offset:8\n"
    "ds_read_b64_tr_b16 a[94:95], v8 offset:520\n"
    "ds_read_b64_tr_b16 a[228:229], v8 offset:8192\n"
    "ds_read_b64_tr_b16 a[230:231], v8 offset:8704\n"
    "ds_read_b64_tr_b16 a[236:237], v8 offset:8200\n"
    "ds_read_b64_tr_b16 a[238:239], v8 offset:8712\n"
    "ds_read_b64_tr_b16 v[156:157], v8 offset:16384\n"
    "ds_read_b64_tr_b16 v[158:159], v8 offset:16896\n"
    "ds_read_b64_tr_b16 v[220:221], v8 offset:16392\n"
    "ds_read_b64_tr_b16 v[222:223], v8 offset:16904\n"
    "s_waitcnt vmcnt(24) lgkmcnt(0)\n"
    "s_barrier\n"
    "s_add_u32 m0, s60, 139264\n"
    "s_mov_b32 s73, 4456448\n"
    "buffer_load_dwordx4 v4, s[40:43], s73 offen lds\n"
    "s_add_u32 m0, s60, 143360\n"
    "s_mov_b32 s73, 4456576\n"
    "buffer_load_dwordx4 v4, s[40:43], s73 offen lds\n"
    "s_add_u32 m0, s60, 147456\n"
    "s_mov_b32 s73, 4456448\n"
    "buffer_load_dwordx4 v4, s[44:47], s73 offen lds\n"
    "s_add_u32 m0, s60, 151552\n"
    "s_mov_b32 s73, 4456576\n"
    "buffer_load_dwordx4 v4, s[44:47], s73 offen lds\n"
    "s_add_u32 m0, s60, 155648\n"
    "s_mov_b32 s73, 4456448\n"
    "buffer_load_dwordx4 v4, s[48:51], s73 offen lds\n"
    "s_add_u32 m0, s60, 159744\n"
    "s_mov_b32 s73, 4456576\n"
    "buffer_load_dwordx4 v4, s[48:51], s73 offen lds\n"
    "v_add_u32 v8, 16384, v6\n"
    "ds_read_b64_tr_b16 a[96:97], v8 offset:0\n"
    "ds_read_b64_tr_b16 a[98:99], v8 offset:512\n"
    "ds_read_b64_tr_b16 a[104:105], v8 offset:8\n"
    "ds_read_b64_tr_b16 a[106:107], v8 offset:520\n"
    "ds_read_b64_tr_b16 a[240:241], v8 offset:8192\n"
    "ds_read_b64_tr_b16 a[242:243], v8 offset:8704\n"
    "ds_read_b64_tr_b16 a[248:249], v8 offset:8200\n"
    "ds_read_b64_tr_b16 a[250:251], v8 offset:8712\n"
    "ds_read_b64_tr_b16 v[160:161], v8 offset:16384\n"
    "ds_read_b64_tr_b16 v[162:163], v8 offset:16896\n"
    "ds_read_b64_tr_b16 v[168:169], v8 offset:16392\n"
    "ds_read_b64_tr_b16 v[170:171], v8 offset:16904\n"
    "s_waitcnt vmcnt(24) lgkmcnt(0)\n"
    "s_barrier\n"
    "v_add_u32 v8, 40960, v6\n"
    "ds_read_b64_tr_b16 a[100:101], v8 offset:0\n"
    "ds_read_b64_tr_b16 a[102:103], v8 offset:512\n"
    "ds_read_b64_tr_b16 a[108:109], v8 offset:8\n"
    "ds_read_b64_tr_b16 a[110:111], v8 offset:520\n"
    "ds_read_b64_tr_b16 a[244:245], v8 offset:8192\n"
    "ds_read_b64_tr_b16 a[246:247], v8 offset:8704\n"
    "ds_read_b64_tr_b16 a[252:253], v8 offset:8200\n"
    "ds_read_b64_tr_b16 a[254:255], v8 offset:8712\n"
    "ds_read_b64_tr_b16 v[164:165], v8 offset:16384\n"
    "ds_read_b64_tr_b16 v[166:167], v8 offset:16896\n"
    "ds_read_b64_tr_b16 v[172:173], v8 offset:16392\n"
    "ds_read_b64_tr_b16 v[174:175], v8 offset:16904\n"
    "s_waitcnt vmcnt(18) lgkmcnt(0)\n"
    "s_barrier\n"
    "s_add_u32 m0, s60, 16384\n"
    "s_mov_b32 s73, 74240\n"
    "buffer_load_dwordx4 v5, s[52:55], s73 offen lds\n"
    "s_add_u32 m0, s60, 20480\n"
    "s_mov_b32 s73, 74368\n"
    "buffer_load_dwordx4 v5, s[52:55], s73 offen lds\n"
    "s_add_u32 m0, s60, 24576\n"
    "s_mov_b32 s73, 74496\n"
    "buffer_load_dwordx4 v5, s[52:55], s73 offen lds\n"
    "s_add_u32 m0, s60, 28672\n"
    "s_mov_b32 s73, 74624\n"
    "buffer_load_dwordx4 v5, s[52:55], s73 offen lds\n"
    "s_add_u32 m0, s60, 32768\n"
    "s_mov_b32 s73, 111104\n"
    "buffer_load_dwordx4 v5, s[52:55], s73 offen lds\n"
    "s_add_u32 m0, s60, 36864\n"
    "s_mov_b32 s73, 111232\n"
    "buffer_load_dwordx4 v5, s[52:55], s73 offen lds\n"
    "s_add_u32 m0, s60, 40960\n"
    "s_mov_b32 s73, 111360\n"
    "buffer_load_dwordx4 v5, s[52:55], s73 offen lds\n"
    "s_add_u32 m0, s60, 45056\n"
    "s_mov_b32 s73, 111488\n"
    "buffer_load_dwordx4 v5, s[52:55], s73 offen lds\n"
    "v_add_u32 v8, 65536, v6\n"
    "ds_read_b64_tr_b16 a[112:113], v8 offset:0\n"
    "ds_read_b64_tr_b16 a[114:115], v8 offset:512\n"
    "ds_read_b64_tr_b16 a[120:121], v8 offset:8\n"
    "ds_read_b64_tr_b16 a[122:123], v8 offset:520\n"
    "ds_read_b64_tr_b16 v[80:81], v8 offset:8192\n"
    "ds_read_b64_tr_b16 v[82:83], v8 offset:8704\n"
    "ds_read_b64_tr_b16 v[88:89], v8 offset:8200\n"
    "ds_read_b64_tr_b16 v[90:91], v8 offset:8712\n"
    "ds_read_b64_tr_b16 v[176:177], v8 offset:16384\n"
    "ds_read_b64_tr_b16 v[178:179], v8 offset:16896\n"
    "ds_read_b64_tr_b16 v[184:185], v8 offset:16392\n"
    "ds_read_b64_tr_b16 v[186:187], v8 offset:16904\n"
    "s_waitcnt vmcnt(20) lgkmcnt(0)\n"
    "s_barrier\n"
    "s_add_u32 m0, s60, 49152\n"
    "s_mov_b32 s73, 73728\n"
    "buffer_load_dwordx4 v5, s[52:55], s73 offen lds\n"
    "s_add_u32 m0, s60, 53248\n"
    "s_mov_b32 s73, 73856\n"
    "buffer_load_dwordx4 v5, s[52:55], s73 offen lds\n"
    "s_add_u32 m0, s60, 57344\n"
    "s_mov_b32 s73, 73984\n"
    "buffer_load_dwordx4 v5, s[52:55], s73 offen lds\n"
    "s_add_u32 m0, s60, 61440\n"
    "s_mov_b32 s73, 74112\n"
    "buffer_load_dwordx4 v5, s[52:55], s73 offen lds\n"
    "s_add_u32 m0, s60, 65536\n"
    "s_mov_b32 s73, 110592\n"
    "buffer_load_dwordx4 v5, s[52:55], s73 offen lds\n"
    "s_add_u32 m0, s60, 69632\n"
    "s_mov_b32 s73, 110720\n"
    "buffer_load_dwordx4 v5, s[52:55], s73 offen lds\n"
    "s_add_u32 m0, s60, 73728\n"
    "s_mov_b32 s73, 110848\n"
    "buffer_load_dwordx4 v5, s[52:55], s73 offen lds\n"
    "s_add_u32 m0, s60, 77824\n"
    "s_mov_b32 s73, 110976\n"
    "buffer_load_dwordx4 v5, s[52:55], s73 offen lds\n"
    "v_add_u32 v8, 90112, v6\n"
    "ds_read_b64_tr_b16 a[116:117], v8 offset:0\n"
    "ds_read_b64_tr_b16 a[118:119], v8 offset:512\n"
    "ds_read_b64_tr_b16 a[124:125], v8 offset:8\n"
    "ds_read_b64_tr_b16 a[126:127], v8 offset:520\n"
    "ds_read_b64_tr_b16 v[84:85], v8 offset:8192\n"
    "ds_read_b64_tr_b16 v[86:87], v8 offset:8704\n"
    "ds_read_b64_tr_b16 v[92:93], v8 offset:8200\n"
    "ds_read_b64_tr_b16 v[94:95], v8 offset:8712\n"
    "ds_read_b64_tr_b16 v[180:181], v8 offset:16384\n"
    "ds_read_b64_tr_b16 v[182:183], v8 offset:16896\n"
    "ds_read_b64_tr_b16 v[188:189], v8 offset:16392\n"
    "ds_read_b64_tr_b16 v[190:191], v8 offset:16904\n"
    "s_waitcnt vmcnt(22) lgkmcnt(0)\n"
    "s_barrier\n"
    "s_add_u32 m0, s60, 81920\n"
    "s_mov_b32 s73, 0\n"
    "buffer_load_dwordx4 v5, s[52:55], s73 offen lds\n"
    "s_add_u32 m0, s60, 86016\n"
    "s_mov_b32 s73, 128\n"
    "buffer_load_dwordx4 v5, s[52:55], s73 offen lds\n"
    "s_add_u32 m0, s60, 90112\n"
    "s_mov_b32 s73, 256\n"
    "buffer_load_dwordx4 v5, s[52:55], s73 offen lds\n"
    "s_add_u32 m0, s60, 94208\n"
    "s_mov_b32 s73, 384\n"
    "buffer_load_dwordx4 v5, s[52:55], s73 offen lds\n"
    "s_add_u32 m0, s60, 98304\n"
    "s_mov_b32 s73, 36864\n"
    "buffer_load_dwordx4 v5, s[52:55], s73 offen lds\n"
    "s_add_u32 m0, s60, 102400\n"
    "s_mov_b32 s73, 36992\n"
    "buffer_load_dwordx4 v5, s[52:55], s73 offen lds\n"
    "s_add_u32 m0, s60, 106496\n"
    "s_mov_b32 s73, 37120\n"
    "buffer_load_dwordx4 v5, s[52:55], s73 offen lds\n"
    "s_add_u32 m0, s60, 110592\n"
    "s_mov_b32 s73, 37248\n"
    "buffer_load_dwordx4 v5, s[52:55], s73 offen lds\n"
    "v_add_u32 v8, 114688, v6\n"
    "ds_read_b64_tr_b16 a[128:129], v8 offset:0\n"
    "ds_read_b64_tr_b16 a[130:131], v8 offset:512\n"
    "ds_read_b64_tr_b16 a[136:137], v8 offset:8\n"
    "ds_read_b64_tr_b16 a[138:139], v8 offset:520\n"
    "ds_read_b64_tr_b16 v[96:97], v8 offset:8192\n"
    "ds_read_b64_tr_b16 v[98:99], v8 offset:8704\n"
    "ds_read_b64_tr_b16 v[104:105], v8 offset:8200\n"
    "ds_read_b64_tr_b16 v[106:107], v8 offset:8712\n"
    "ds_read_b64_tr_b16 v[192:193], v8 offset:16384\n"
    "ds_read_b64_tr_b16 v[194:195], v8 offset:16896\n"
    "ds_read_b64_tr_b16 v[200:201], v8 offset:16392\n"
    "ds_read_b64_tr_b16 v[202:203], v8 offset:16904\n"
    "s_waitcnt vmcnt(24) lgkmcnt(0)\n"
    "s_barrier\n"
    "v_add_u32 v8, 139264, v6\n"
    "ds_read_b64_tr_b16 a[132:133], v8 offset:0\n"
    "ds_read_b64_tr_b16 a[134:135], v8 offset:512\n"
    "ds_read_b64_tr_b16 a[140:141], v8 offset:8\n"
    "ds_read_b64_tr_b16 a[142:143], v8 offset:520\n"
    "ds_read_b64_tr_b16 v[100:101], v8 offset:8192\n"
    "ds_read_b64_tr_b16 v[102:103], v8 offset:8704\n"
    "ds_read_b64_tr_b16 v[108:109], v8 offset:8200\n"
    "ds_read_b64_tr_b16 v[110:111], v8 offset:8712\n"
    "ds_read_b64_tr_b16 v[196:197], v8 offset:16384\n"
    "ds_read_b64_tr_b16 v[198:199], v8 offset:16896\n"
    "ds_read_b64_tr_b16 v[204:205], v8 offset:16392\n"
    "ds_read_b64_tr_b16 v[206:207], v8 offset:16904\n"
    "s_waitcnt vmcnt(16) lgkmcnt(0)\n"
    "s_barrier\n"
    "s_add_u32 m0, s60, 114688\n"
    "s_mov_b32 s73, 512\n"
    "buffer_load_dwordx4 v5, s[52:55], s73 offen lds\n"
    "s_add_u32 m0, s60, 118784\n"
    "s_mov_b32 s73, 640\n"
    "buffer_load_dwordx4 v5, s[52:55], s73 offen lds\n"
    "s_add_u32 m0, s60, 122880\n"
    "s_mov_b32 s73, 768\n"
    "buffer_load_dwordx4 v5, s[52:55], s73 offen lds\n"
    "s_add_u32 m0, s60, 126976\n"
    "s_mov_b32 s73, 896\n"
    "buffer_load_dwordx4 v5, s[52:55], s73 offen lds\n"
    "s_add_u32 m0, s60, 131072\n"
    "s_mov_b32 s73, 37376\n"
    "buffer_load_dwordx4 v5, s[52:55], s73 offen lds\n"
    "s_add_u32 m0, s60, 135168\n"
    "s_mov_b32 s73, 37504\n"
    "buffer_load_dwordx4 v5, s[52:55], s73 offen lds\n"
    "s_add_u32 m0, s60, 139264\n"
    "s_mov_b32 s73, 37632\n"
    "buffer_load_dwordx4 v5, s[52:55], s73 offen lds\n"
    "s_add_u32 m0, s60, 143360\n"
    "s_mov_b32 s73, 37760\n"
    "buffer_load_dwordx4 v5, s[52:55], s73 offen lds\n"
    "s_add_u32 m0, s60, 147456\n"
    "s_mov_b32 s73, 1024\n"
    "buffer_load_dwordx4 v5, s[52:55], s73 offen lds\n"
    "s_add_u32 m0, s60, 151552\n"
    "s_mov_b32 s73, 37888\n"
    "buffer_load_dwordx4 v5, s[52:55], s73 offen lds\n"
    "s_add_u32 m0, s60, 155648\n"
    "s_mov_b32 s73, 74752\n"
    "buffer_load_dwordx4 v5, s[52:55], s73 offen lds\n"
    "s_add_u32 m0, s60, 159744\n"
    "s_mov_b32 s73, 111616\n"
    "buffer_load_dwordx4 v5, s[52:55], s73 offen lds\n"
    "buffer_load_dwordx2 v[12:13], off, s[36:39], 0 sc1\n"
    "v_add_u32 v8, 16384, v7\n"
    "v_xor_b32 v9, 32, v8\n"
    "v_xor_b32 v10, 320, v8\n"
    "v_xor_b32 v11, 32, v10\n"
    "ds_read_b64_tr_b16 v[16:17], v8 offset:0\n"
    "ds_read_b64_tr_b16 v[18:19], v8 offset:512\n"
    "ds_read_b64_tr_b16 v[20:21], v8 offset:16384\n"
    "ds_read_b64_tr_b16 v[22:23], v8 offset:16896\n"
    "ds_read_b64_tr_b16 v[24:25], v9 offset:0\n"
    "ds_read_b64_tr_b16 v[26:27], v9 offset:512\n"
    "ds_read_b64_tr_b16 v[28:29], v9 offset:16384\n"
    "ds_read_b64_tr_b16 v[30:31], v9 offset:16896\n"
    "s_waitcnt lgkmcnt(4)\n"
    "ds_read_b64_tr_b16 v[32:33], v10 offset:0\n"
    "ds_read_b64_tr_b16 v[34:35], v10 offset:512\n"
    "ds_read_b64_tr_b16 v[36:37], v10 offset:16384\n"
    "ds_read_b64_tr_b16 v[38:39], v10 offset:16896\n"
    "ds_read_b64_tr_b16 v[40:41], v11 offset:0\n"
    "ds_read_b64_tr_b16 v[42:43], v11 offset:512\n"
    "ds_read_b64_tr_b16 v[44:45], v11 offset:16384\n"
    "ds_read_b64_tr_b16 v[46:47], v11 offset:16896\n"
    "s_waitcnt vmcnt(21) lgkmcnt(0)\n"
    "s_barrier\n"
    "v_add_u32 v14, 0, v1\n"
    "ds_write_b128 v14, v[224:227] offset:0\n"
    "ds_write_b128 v14, v[228:231] offset:1024\n"
    "ds_write_b128 v14, v[232:235] offset:8192\n"
    "ds_write_b128 v14, v[236:239] offset:9216\n"
    "ds_write_b128 v14, v[240:243] offset:16384\n"
    "ds_write_b128 v14, v[244:247] offset:17408\n"
    "ds_write_b128 v14, v[248:251] offset:24576\n"
    "ds_write_b128 v14, v[252:255] offset:25600\n"
    "s_waitcnt lgkmcnt(4)\n"
    "v_add_u32 v8, 49152, v7\n"
    "v_xor_b32 v9, 32, v8\n"
    "v_xor_b32 v10, 320, v8\n"
    "v_xor_b32 v11, 32, v10\n"
    "ds_read_b64_tr_b16 v[48:49], v10 offset:0\n"
    "ds_read_b64_tr_b16 v[50:51], v10 offset:512\n"
    "ds_read_b64_tr_b16 v[52:53], v10 offset:16384\n"
    "ds_read_b64_tr_b16 v[54:55], v10 offset:16896\n"
    "ds_read_b64_tr_b16 v[56:57], v11 offset:0\n"
    "ds_read_b64_tr_b16 v[58:59], v11 offset:512\n"
    "ds_read_b64_tr_b16 v[60:61], v11 offset:16384\n"
    "ds_read_b64_tr_b16 v[62:63], v11 offset:16896\n"
    "s_waitcnt lgkmcnt(4)\n"
    "ds_read_b64_tr_b16 v[64:65], v8 offset:0\n"
    "ds_read_b64_tr_b16 v[66:67], v8 offset:512\n"
    "ds_read_b64_tr_b16 v[68:69], v8 offset:16384\n"
    "ds_read_b64_tr_b16 v[70:71], v8 offset:16896\n"
    "ds_read_b64_tr_b16 v[72:73], v9 offset:0\n"
    "ds_read_b64_tr_b16 v[74:75], v9 offset:512\n"
    "ds_read_b64_tr_b16 v[76:77], v9 offset:16384\n"
    "ds_read_b64_tr_b16 v[78:79], v9 offset:16896\n"
    "s_waitcnt vmcnt(13) lgkmcnt(0)\n"
    "s_barrier\n"
    "v_mov_b64 v[224:225], v[16:17]\n"
    "v_mov_b64 v[226:227], v[18:19]\n"
    "v_mov_b64 v[228:229], v[20:21]\n"
    "v_mov_b64 v[230:231], v[22:23]\n"
    "v_mov_b64 v[232:233], v[24:25]\n"
    "v_mov_b64 v[234:235], v[26:27]\n"
    "v_mov_b64 v[236:237], v[28:29]\n"
    "v_mov_b64 v[238:239], v[30:31]\n"
    "v_mov_b64 v[240:241], v[32:33]\n"
    "v_mov_b64 v[242:243], v[34:35]\n"
    "v_mov_b64 v[244:245], v[36:37]\n"
    "v_mov_b64 v[246:247], v[38:39]\n"
    "v_mov_b64 v[248:249], v[40:41]\n"
    "v_mov_b64 v[250:251], v[42:43]\n"
    "v_mov_b64 v[252:253], v[44:45]\n"
    "v_mov_b64 v[254:255], v[46:47]\n"
    "v_add_u32 v14, 32768, v1\n"
    "ds_write_b128 v14, v[208:211] offset:0\n"
    "ds_write_b128 v14, v[212:215] offset:1024\n"
    "ds_write_b128 v14, v[216:219] offset:8192\n"
    "ds_write_b128 v14, v[220:223] offset:9216\n"
    "ds_write_b128 v14, v[64:67] offset:16384\n"
    "ds_write_b128 v14, v[68:71] offset:17408\n"
    "ds_write_b128 v14, v[72:75] offset:24576\n"
    "ds_write_b128 v14, v[76:79] offset:25600\n"
    "s_waitcnt lgkmcnt(4)\n"
    "v_add_u32 v8, 81920, v7\n"
    "v_xor_b32 v9, 32, v8\n"
    "v_xor_b32 v10, 320, v8\n"
    "v_xor_b32 v11, 32, v10\n"
    "ds_read_b64_tr_b16 v[16:17], v8 offset:0\n"
    "ds_read_b64_tr_b16 v[18:19], v8 offset:512\n"
    "ds_read_b64_tr_b16 v[20:21], v8 offset:16384\n"
    "ds_read_b64_tr_b16 v[22:23], v8 offset:16896\n"
    "ds_read_b64_tr_b16 v[24:25], v9 offset:0\n"
    "ds_read_b64_tr_b16 v[26:27], v9 offset:512\n"
    "ds_read_b64_tr_b16 v[28:29], v9 offset:16384\n"
    "ds_read_b64_tr_b16 v[30:31], v9 offset:16896\n"
    "s_waitcnt lgkmcnt(4)\n"
    "ds_read_b64_tr_b16 v[32:33], v10 offset:0\n"
    "ds_read_b64_tr_b16 v[34:35], v10 offset:512\n"
    "ds_read_b64_tr_b16 v[36:37], v10 offset:16384\n"
    "ds_read_b64_tr_b16 v[38:39], v10 offset:16896\n"
    "ds_read_b64_tr_b16 v[40:41], v11 offset:0\n"
    "ds_read_b64_tr_b16 v[42:43], v11 offset:512\n"
    "ds_read_b64_tr_b16 v[44:45], v11 offset:16384\n"
    "ds_read_b64_tr_b16 v[46:47], v11 offset:16896\n"
    "s_waitcnt vmcnt(5) lgkmcnt(0)\n"
    "s_barrier\n"
    "v_mov_b64 v[208:209], v[48:49]\n"
    "v_mov_b64 v[210:211], v[50:51]\n"
    "v_mov_b64 v[212:213], v[52:53]\n"
    "v_mov_b64 v[214:215], v[54:55]\n"
    "v_mov_b64 v[216:217], v[56:57]\n"
    "v_mov_b64 v[218:219], v[58:59]\n"
    "v_mov_b64 v[220:221], v[60:61]\n"
    "v_mov_b64 v[222:223], v[62:63]\n"
    "v_add_u32 v14, 65536, v1\n"
    "ds_write_b128 v14, v[16:19] offset:0\n"
    "ds_write_b128 v14, v[20:23] offset:1024\n"
    "ds_write_b128 v14, v[24:27] offset:8192\n"
    "ds_write_b128 v14, v[28:31] offset:9216\n"
    "ds_write_b128 v14, v[32:35] offset:16384\n"
    "ds_write_b128 v14, v[36:39] offset:17408\n"
    "ds_write_b128 v14, v[40:43] offset:24576\n"
    "ds_write_b128 v14, v[44:47] offset:25600\n"
    "s_waitcnt lgkmcnt(4)\n"
    "v_add_u32 v8, 114688, v7\n"
    "v_xor_b32 v9, 32, v8\n"
    "v_xor_b32 v10, 320, v8\n"
    "v_xor_b32 v11, 32, v10\n"
    "ds_read_b64_tr_b16 v[48:49], v8 offset:0\n"
    "ds_read_b64_tr_b16 v[50:51], v8 offset:512\n"
    "ds_read_b64_tr_b16 v[52:53], v8 offset:16384\n"
    "ds_read_b64_tr_b16 v[54:55], v8 offset:16896\n"
    "ds_read_b64_tr_b16 v[56:57], v9 offset:0\n"
    "ds_read_b64_tr_b16 v[58:59], v9 offset:512\n"
    "ds_read_b64_tr_b16 v[60:61], v9 offset:16384\n"
    "ds_read_b64_tr_b16 v[62:63], v9 offset:16896\n"
    "s_waitcnt lgkmcnt(4)\n"
    "ds_read_b64_tr_b16 v[64:65], v10 offset:0\n"
    "ds_read_b64_tr_b16 v[66:67], v10 offset:512\n"
    "ds_read_b64_tr_b16 v[68:69], v10 offset:16384\n"
    "ds_read_b64_tr_b16 v[70:71], v10 offset:16896\n"
    "ds_read_b64_tr_b16 v[72:73], v11 offset:0\n"
    "ds_read_b64_tr_b16 v[74:75], v11 offset:512\n"
    "ds_read_b64_tr_b16 v[76:77], v11 offset:16384\n"
    "ds_read_b64_tr_b16 v[78:79], v11 offset:16896\n"
    "s_waitcnt vmcnt(0) lgkmcnt(0)\n"
    "s_barrier\n"
    ".Lrec_par_%=:\n"
    "v_readfirstlane_b32 s73, v12\n"
    "v_readfirstlane_b32 s74, v13\n"
    "s_xor_b32 s73, s73, s30\n"
    "s_xor_b32 s74, s74, s31\n"
    "s_cselect_b32 s74, 2, 0\n"
    "s_or_b32 s74, s74, s73\n"
    "s_cmp_lt_u32 s74, 2\n"
    "s_cbranch_scc1 .Lrec_par1_%=\n"
    "s_sleep 4\n"
    "buffer_load_dwordx2 v[12:13], off, s[36:39], 0 sc1\n"
    "s_waitcnt vmcnt(0)\n"
    "s_sub_u32 s69, s69, 1\n"
    "s_cbranch_scc0 .Lrec_par_%=\n"
    "s_mov_b32 s69, 0\n"
    ".Lrec_par1_%=:\n"
    "s_and_b32 s73, s73, 1\n"
    "s_lshl_b32 s73, s73, 13\n"
    "s_add_u32 s36, s36, s73\n"
    "s_addc_u32 s37, s37, 0\n"
    "v_lshlrev_b32 v44, 3, v0\n"
    "v_mov_b32 v45, 0x80000000\n"
    "v_cmp_gt_u32 vcc, 192, v0\n"
    "s_nop 1\n"
    "v_cndmask_b32 v44, v45, v44, vcc\n"
    "v_mov_b32 v46, -1\n"
    "buffer_load_dword v45, v44, s[36:39], s62 offen sc1\n"
    "s_mov_b64 exec, 1\n"
    "buffer_atomic_smax v46, off, s[36:39], s59 sc0 sc1\n"
    "s_mov_b64 exec, -1\n"
    "v_add_u32 v14, 98304, v1\n"
    "ds_write_b128 v14, v[48:51] offset:0\n"
    "ds_write_b128 v14, v[52:55] offset:1024\n"
    "ds_write_b128 v14, v[56:59] offset:8192\n"
    "ds_write_b128 v14, v[60:63] offset:9216\n"
    "ds_write_b128 v14, v[64:67] offset:16384\n"
    "ds_write_b128 v14, v[68:71] offset:17408\n"
    "ds_write_b128 v14, v[72:75] offset:24576\n"
    "ds_write_b128 v14, v[76:79] offset:25600\n"
    "s_waitcnt lgkmcnt(4)\n"
    "v_add_u32 v8, 147456, v3\n"
    "v_xor_b32 v9, 32, v8\n"
    "v_xor_b32 v10, 320, v8\n"
    "v_xor_b32 v11, 32, v10\n"
    "ds_read_b64_tr_b16 v[16:17], v8 offset:0\n"
    "ds_read_b64_tr_b16 v[18:19], v9 offset:1024\n"
    "ds_read_b64_tr_b16 v[20:21], v10 offset:0\n"
    "ds_read_b64_tr_b16 v[22:23], v11 offset:1024\n"
    "ds_read_b64_tr_b16 v[24:25], v8 offset:8192\n"
    "ds_read_b64_tr_b16 v[26:27], v9 offset:9216\n"
    "ds_read_b64_tr_b16 v[28:29], v10 offset:8192\n"
    "ds_read_b64_tr_b16 v[30:31], v11 offset:9216\n"
    "s_waitcnt lgkmcnt(4)\n"
    "s_waitcnt lgkmcnt(0)\n"
    "s_barrier\n"
    "v_add_u32 v14, 131072, v1\n"
    "ds_write_b128 v14, v[16:19] offset:0\n"
    "ds_write_b128 v14, v[20:23] offset:1024\n"
    "ds_write_b128 v14, v[24:27] offset:8192\n"
    "ds_write_b128 v14, v[28:31] offset:9216\n"
    "s_memtime s[32:33]\n"
    "s_memrealtime s[28:29]\n"
    "s_mov_b32 s84, 0x3e0293ee\n"
    "s_waitcnt vmcnt(0)\n"
    ".Lrec_init_%=:\n"
    "v_cmp_gt_i32 vcc, 0, v45\n"
    "s_cbranch_vccz .Lrec_init1_%=\n"
    "s_sleep 2\n"
    "buffer_load_dword v45, v44, s[36:39], s62 offen sc1\n"
    "s_waitcnt vmcnt(0)\n"
    "s_sub_u32 s69, s69, 1\n"
    "s_cbranch_scc0 .Lrec_init_%=\n"
    "s_mov_b32 s69, 0\n"
    ".Lrec_init1_%=:\n"
    "v_mov_b32 v3, v46\n"
    "s_mov_b32 s40, s76\n"
    "s_mov_b32 s41, s77\n"
    "s_mov_b32 s42, s78\n"
    "s_mov_b32 s43, s79\n"
    "s_mov_b32 s44, s80\n"
    "s_mov_b32 s45, s81\n"
    "s_mov_b32 s50, 0\n"
    "s_waitcnt lgkmcnt(0)\n"
    "s_mov_b32 s46, s34\n"
    "s_lshl_b32 s47, s92, 2\n"
    "s_mov_b32 s33, s32\n"
    "s_lshl_b32 s28, s28, 2\n"
    "s_mov_b32 s52, 900\n"
    "s_and_b32 s35, s40, 1\n"
    "s_and_b32 s40, s40, -2\n"
    "s_mul_i32 s35, s35, 0\n"
    "s_add_u32 s34, s35, 70\n"
    "s_lshl_b32 s35, s35, 2\n"
    "s_mov_b32 s53, 0\n"
    "s_mov_b32 s54, 0x7fffffff\n"
    "s_mov_b32 s55, 0\n"
    "s_mov_b32 s68, -1\n"
    "s_add_u32 s68, s68, 1\n"
    "s_and_b32 s70, s68, 3\n"
    "s_lshl_b32 s70, s70, 17\n"
    "s_add_u32 s70, s70, s66\n"
    "s_add_u32 s71, s70, 4096\n"
    "s_add_u32 s72, s70, 16384\n"
    "s_sub_u32 s46, s33, s46\n"
    "s_sub_u32 s47, s28, s47\n"
    "v_cvt_f32_u32 v40, s46\n"
    "v_cvt_f32_u32 v41, s47\n"
    "v_rcp_f32 v41, v41\n"
    "s_mov_b32 s46, s33\n"
    "s_mov_b32 s47, s28\n"
    "v_mul_f32 v40, v40, v41\n"
    "v_mov_b32 v42, 0x40c80000\n"
    "v_med3_f32 v40, v40, 4.0, v42\n"
    "v_rcp_f32 v41, v40\n"
    "v_mul_f32 v40, 0x43800000, v40\n"
    "v_mul_f32 v41, 0x44b54000, v41\n"
    "v_cvt_u32_f32 v40, v40\n"
    "v_cvt_u32_f32 v41, v41\n"
    "v_readfirstlane_b32 s48, v40\n"
    "v_readfirstlane_b32 s49, v41\n"
    "v_mov_b64 v[40:41], 0\n"
    "v_mov_b64 v[42:43], 0\n"
    "v_mov_b64 v[44:45], 0\n"
    "v_mov_b64 v[46:47], 0\n"
    "v_mov_b64 v[48:49], 0\n"
    "v_mov_b64 v[50:51], 0\n"
    "v_mov_b64 v[52:53], 0\n"
    "v_mov_b64 v[54:55], 0\n"
    "v_mov_b64 v[56:57], 0\n"
    "v_mov_b64 v[58:59], 0\n"
    "v_mov_b64 v[60:61], 0\n"
    "v_mov_b64 v[62:63], 0\n"
    "s_waitcnt lgkmcnt(0)\n"
    "s_barrier\n"
    "s_branch .Lrec_entry_%=\n"
    ".Lrec_LOOPB_%=:\n"
    ".Lrec_top_%=:\n"
    "buffer_load_dwordx4 v[4:7], v0, s[36:39], s70 offen sc1\n"
    "s_mov_b32 s82, s50\n"
    "s_add_u32 s50, s50, 589824\n"
    "s_cmp_eq_u32 s50, 1769472\n"
    "s_cselect_b32 s50, 0, s50\n"
    "s_add_u32 s80, s68, 2\n"
    "s_and_b32 s80, s80, 3\n"
    "s_lshl_b32 s80, s80, 17\n"
    "buffer_load_dwordx4 v[8:11], v0, s[36:39], s70 offen offset:1024 sc1\n"
    "s_and_b32 s80, s80, s45\n"
    "s_and_b32 s91, s50, s44\n"
    "s_add_u32 s80, s80, s91\n"
    "s_add_u32 s80, s80, s43\n"
    "s_add_u32 s89, s42, s50\n"
    "s_lshr_b32 s90, s68, 2\n"
    "s_lshl_b32 s90, s90, 14\n"
    "buffer_load_dwordx4 v[12:15], v0, s[36:39], s70 offen offset:2048 sc1\n"
    "s_and_b32 s91, s68, 3\n"
    "s_lshl_b32 s91, s91, 11\n"
    "s_add_u32 s90, s90, s91\n"
    "s_add_u32 s90, s90, s40\n"
    "s_add_u32 s51, s90, s41\n"
    "s_and_b32 s76, s68, 7\n"
    "s_mul_i32 s76, s76, 4194304\n"
    "buffer_load_dwordx4 v[16:19], v0, s[36:39], s70 offen offset:3072 sc1\n"
    "s_add_u32 s75, s76, s56\n"
    "s_add_u32 s83, s75, 16384\n"
    "s_add_u32 s76, s76, s57\n"
    "s_add_u32 s77, s63, s82\n"
    "s_add_u32 s86, s77, 262144\n"
    "s_add_u32 s87, s67, s82\n"
    "s_add_u32 s91, s68, 1\n"
    "buffer_load_dwordx4 v[20:23], v0, s[36:39], s71 offen sc1\n"
    "s_and_b32 s79, s91, 3\n"
    "s_lshl_b32 s79, s79, 17\n"
    "s_add_u32 s79, s79, s65\n"
    "s_add_u32 s78, s64, s82\n"
    "s_add_u32 s88, s78, 4096\n"
    "s_nop 1\n"
    "buffer_load_dwordx4 v[24:27], v0, s[36:39], s71 offen offset:1024 sc1\n"
    "s_nop 6\n"
    "buffer_load_dwordx4 v[28:31], v0, s[36:39], s71 offen offset:2048 sc1\n"
    "s_nop 6\n"
    "buffer_load_dwordx4 v[32:35], v0, s[36:39], s71 offen offset:3072 sc1\n"
    "s_waitcnt vmcnt(7)\n"
    ".Lrec_start_%=:\n"
    "v_smfmac_f32_16x16x64_bf16 v[40:43], v[4:7], a[0:7], v2\n"
    "v_max3_u32 v3, v4, v5, v6\n"
    "v_smfmac_f32_16x16x64_bf16 v[44:47], v[4:7], a[8:15], v2\n"
    "v_max_u32 v3, v3, v7\n"
    "v_smfmac_f32_16x16x64_bf16 v[48:51], v[4:7], a[144:151], v2\n"
    "buffer_load_dwordx4 v[36:39], v0, s[36:39], s72 offen sc1\n"
    "v_cmp_eq_u32 vcc, -1, v3\n"
    "v_smfmac_f32_16x16x64_bf16 v[52:55], v[4:7], a[152:159], v2\n"
    "v_smfmac_f32_16x16x64_bf16 v[56:59], v[4:7], v[112:119], v2\n"
    "s_cbranch_vccnz .Lrec_retry0_%=\n"
    ".Lrec_back0_%=:\n"
    "s_waitcnt vmcnt(7)\n"
    "s_waitcnt lgkmcnt(0)\n"
    "v_smfmac_f32_16x16x64_bf16 v[60:63], v[4:7], v[72:79], v2\n"
    "ds_read_b128 v[72:75], v1 offset:8192\n"
    "v_smfmac_f32_16x16x64_bf16 v[40:43], v[8:11], a[16:23], v2\n"
    "ds_read_b128 v[76:79], v1 offset:9216\n"
    "v_max3_u32 v3, v3, v8, v9\n"
    "v_smfmac_f32_16x16x64_bf16 v[44:47], v[8:11], a[24:31], v2\n"
    "buffer_load_dwordx4 v[68:71], v0, s[36:39], s75 offen offset:1024 sc1\n"
    "v_max3_u32 v3, v3, v10, v11\n"
    "v_smfmac_f32_16x16x64_bf16 v[48:51], v[8:11], a[160:167], v2\n"
    "v_smfmac_f32_16x16x64_bf16 v[52:55], v[8:11], a[168:175], v2\n"
    "buffer_load_dwordx4 v[64:67], v0, s[36:39], s75 offen sc1\n"
    "v_smfmac_f32_16x16x64_bf16 v[56:59], v[8:11], v[120:127], v2\n"
    "s_waitcnt vmcnt(8)\n"
    "s_waitcnt lgkmcnt(0)\n"
    "v_smfmac_f32_16x16x64_bf16 v[60:63], v[8:11], v[72:79], v2\n"
    "ds_read_b128 v[72:75], v1 offset:16384\n"
    "v_smfmac_f32_16x16x64_bf16 v[40:43], v[12:15], a[32:39], v2\n"
    "ds_read_b128 v[76:79], v1 offset:17408\n"
    "v_max3_u32 v3, v3, v12, v13\n"
    "v_smfmac_f32_16x16x64_bf16 v[44:47], v[12:15], a[40:47], v2\n"
    "v_max3_u32 v3, v3, v14, v15\n"
    "v_smfmac_f32_16x16x64_bf16 v[48:51], v[12:15], a[176:183], v2\n"
    "buffer_load_dwordx4 v[4:7], v0, s[36:39], s83 offen sc1\n"
    "v_smfmac_f32_16x16x64_bf16 v[52:55], v[12:15], a[184:191], v2\n"
    "v_smfmac_f32_16x16x64_bf16 v[56:59], v[12:15], v[128:135], v2\n"
    "s_waitcnt vmcnt(8)\n"
    "s_waitcnt lgkmcnt(0)\n"
    "v_max3_u32 v3, v3, v16, v17\n"
    "v_smfmac_f32_16x16x64_bf16 v[60:63], v[12:15], v[72:79], v2\n"
    "ds_read_b128 v[72:75], v1 offset:24576\n"
    "v_max3_u32 v3, v3, v18, v19\n"
    "v_smfmac_f32_16x16x64_bf16 v[40:43], v[16:19], a[48:55], v2\n"
    "ds_read_b128 v[76:79], v1 offset:25600\n"
    "v_cmp_eq_u32 vcc, -1, v3\n"
    "v_smfmac_f32_16x16x64_bf16 v[44:47], v[16:19], a[56:63], v2\n"
    "buffer_load_dwordx4 v[8:11], v0, s[36:39], s83 offen offset:1024 sc1\n"
    "v_smfmac_f32_16x16x64_bf16 v[48:51], v[16:19], a[192:199], v2\n"
    "s_cbranch_vccnz .Lrec_retry1_%=\n"
    ".Lrec_back1_%=:\n"
    "v_smfmac_f32_16x16x64_bf16 v[52:55], v[16:19], a[200:207], v2\n"
    "s_waitcnt vmcnt(8)\n"
    "s_waitcnt lgkmcnt(0)\n"
    "v_max3_u32 v3, v3, v20, v21\n"
    "v_smfmac_f32_16x16x64_bf16 v[60:63], v[16:19], v[72:79], v2\n"
    "ds_read_b128 v[72:75], v1 offset:32768\n"
    "v_max3_u32 v3, v3, v22, v23\n"
    "v_smfmac_f32_16x16x64_bf16 v[40:43], v[20:23], a[64:71], v2\n"
    "ds_read_b128 v[76:79], v1 offset:33792\n"
    "v_mov_b64 v[12:13], -1\n"
    "v_smfmac_f32_16x16x64_bf16 v[44:47], v[20:23], a[72:79], v2\n"
    "v_mov_b64 v[14:15], -1\n"
    "v_smfmac_f32_16x16x64_bf16 v[48:51], v[20:23], a[208:215], v2\n"
    "v_smfmac_f32_16x16x64_bf16 v[52:55], v[20:23], a[216:223], v2\n"
    "s_waitcnt vmcnt(7)\n"
    "s_waitcnt lgkmcnt(0)\n"
    "buffer_store_dwordx4 v[12:15], v0, s[36:39], s89 offen sc1\n"
    "v_max3_u32 v3, v3, v24, v25\n"
    "v_smfmac_f32_16x16x64_bf16 v[60:63], v[20:23], v[72:79], v2\n"
    "ds_read_b128 v[72:75], v1 offset:40960\n"
    "v_max3_u32 v3, v3, v26, v27\n"
    "v_smfmac_f32_16x16x64_bf16 v[40:43], v[24:27], a[80:87], v2\n"
    "ds_read_b128 v[76:79], v1 offset:41984\n"
    "v_add_u32 v1, 49152, v1\n"
    "v_smfmac_f32_16x16x64_bf16 v[44:47], v[24:27], a[88:95], v2\n"
    "s_waitcnt vmcnt(7)\n"
    "buffer_store_dwordx4 v[12:15], v0, s[36:39], s89 offen offset:1024 sc1\n"
    "v_max3_u32 v3, v3, v28, v29\n"
    "v_smfmac_f32_16x16x64_bf16 v[48:51], v[24:27], a[224:231], v2\n"
    "s_waitcnt vmcnt(7)\n"
    "v_max3_u32 v3, v3, v30, v31\n"
    "v_smfmac_f32_16x16x64_bf16 v[52:55], v[24:27], a[232:239], v2\n"
    "s_waitcnt lgkmcnt(0)\n"
    "buffer_store_dwordx4 v[12:15], v0, s[36:39], s80 offen sc1\n"
    "v_max3_u32 v3, v3, v32, v33\n"
    "v_smfmac_f32_16x16x64_bf16 v[60:63], v[24:27], v[72:79], v2\n"
    "ds_read_b128 v[72:75], v0 offset:13312\n"
    "v_max3_u32 v3, v3, v34, v35\n"
    "v_smfmac_f32_16x16x64_bf16 v[40:43], v[28:31], a[96:103], v2\n"
    "s_waitcnt vmcnt(7)\n"
    "v_max3_u32 v3, v3, v36, v37\n"
    "v_smfmac_f32_16x16x64_bf16 v[44:47], v[28:31], a[104:111], v2\n"
    "ds_read_b128 v[12:15], v0 offset:12288\n"
    "v_max3_u32 v3, v3, v38, v39\n"
    "v_smfmac_f32_16x16x64_bf16 v[40:43], v[32:35], a[112:119], v2\n"
    "s_lshr_b32 s73, s60, 1\n"
    "v_cmp_eq_u32 vcc, -1, v3\n"
    "v_smfmac_f32_16x16x64_bf16 v[44:47], v[32:35], a[120:127], v2\n"
    "v_add_u32 v79, s60, v0\n"
    "v_smfmac_f32_16x16x64_bf16 v[48:51], v[28:31], a[240:247], v2\n"
    "s_cbranch_vccnz .Lrec_retry2_%=\n"
    ".Lrec_back2_%=:\n"
    "v_xor_b32 v76, 1024, v79\n"
    "v_smfmac_f32_16x16x64_bf16 v[40:43], v[36:39], a[128:135], v2\n"
    "s_waitcnt lgkmcnt(0)\n"
    "s_memrealtime s[28:29]\n"
    "v_xor_b32 v77, 2048, v79\n"
    "v_smfmac_f32_16x16x64_bf16 v[44:47], v[36:39], a[136:143], v2\n"
    "s_memrealtime s[30:31]\n"
    "v_xor_b32 v78, 3072, v79\n"
    "v_smfmac_f32_16x16x64_bf16 v[52:55], v[28:31], a[248:255], v2\n"
    "s_memtime s[32:33]\n"
    "v_cvt_pk_bf16_f32 v40, v40, v41\n"
    "v_smfmac_f32_16x16x64_bf16 v[48:51], v[32:35], v[80:87], v2\n"
    "s_memrealtime s[96:97]\n"
    "v_cvt_pk_bf16_f32 v41, v42, v43\n"
    "v_smfmac_f32_16x16x64_bf16 v[52:55], v[32:35], v[88:95], v2\n"
    "s_memrealtime s[98:99]\n"
    "v_cvt_pk_bf16_f32 v42, v44, v45\n"
    "v_smfmac_f32_16x16x64_bf16 v[48:51], v[36:39], v[96:103], v2\n"
    "s_waitcnt vmcnt(5)\n"
    "v_cvt_pk_bf16_f32 v43, v46, v47\n"
    "v_smfmac_f32_16x16x64_bf16 v[52:55], v[36:39], v[104:111], v2\n"
    "v_lshrrev_b32 v3, 1, v79\n"
    "v_mfma_f32_16x16x32_bf16 v[44:47], v[40:43], v[12:15], 0\n"
    "s_nop 0\n"
    "v_cvt_pk_bf16_f32 v48, v48, v49\n"
    "v_cvt_pk_bf16_f32 v49, v50, v51\n"
    "s_waitcnt lgkmcnt(0)\n"
    "v_cvt_pk_bf16_f32 v50, v52, v53\n"
    "v_cvt_pk_bf16_f32 v51, v54, v55\n"
    "s_add_u32 s28, s28, s30\n"
    "s_add_u32 s96, s96, s98\n"
    "v_mfma_f32_16x16x32_bf16 v[52:55], v[48:51], v[12:15], 0\n"
    "s_add_u32 s28, s28, s96\n"
    "v_cvt_pk_bf16_f32 v40, v44, v45\n"
    "v_cvt_pk_bf16_f32 v41, v46, v47\n"
    "v_mov_b32 v75, s28\n"
    "v_mov_b32_dpp v42, v40 row_ror:8 row_mask:0xf bank_mask:0xf\n"
    "v_mov_b32_dpp v43, v41 row_ror:8 row_mask:0xf bank_mask:0xf\n"
    "v_add_u32 v75, s49, v75\n"
    "buffer_store_dword v75, off, s[36:39], s51 sc1\n"
    "v_mfma_f32_16x16x32_bf16 v[44:47], v[68:71], v[40:43], 0\n"
    "v_cvt_pk_bf16_f32 v48, v52, v53\n"
    "v_cvt_pk_bf16_f32 v49, v54, v55\n"
    "v_mfma_f32_16x16x32_bf16 v[68:71], v[64:67], v[40:43], 0\n"
    "v_mov_b32_dpp v50, v48 row_ror:8 row_mask:0xf bank_mask:0xf\n"
    "v_mov_b32_dpp v51, v49 row_ror:8 row_mask:0xf bank_mask:0xf\n"
    "s_nop 1\n"
    "v_mfma_f32_16x16x32_bf16 v[52:55], v[48:51], v[40:43], 0\n"
    "s_nop 2\n"
    "v_mov_b32_dpp v68, v44 row_ror:8 row_mask:0xf bank_mask:0xc\n"
    "v_mov_b32_dpp v69, v45 row_ror:8 row_mask:0xf bank_mask:0xc\n"
    "v_mov_b32_dpp v70, v46 row_ror:8 row_mask:0xf bank_mask:0xc\n"
    "v_mov_b32_dpp v71, v47 row_ror:8 row_mask:0xf bank_mask:0xc\n"
    "ds_write_b128 v79, v[68:71] offset:0\n"
    "v_mov_b32_dpp v52, v54 row_ror:8 row_mask:0xf bank_mask:0xc\n"
    "v_mov_b32_dpp v53, v55 row_ror:8 row_mask:0xf bank_mask:0xc\n"
    "ds_write_b64 v3, v[52:53] offset:4096\n"
    "v_smfmac_f32_16x16x64_bf16 v[56:59], v[16:19], v[136:143], v2\n"
    "buffer_store_dwordx4 v[48:51], v74, s[36:39], s76 offen sc1\n"
    "v_smfmac_f32_16x16x64_bf16 v[56:59], v[20:23], v[144:151], v2\n"
    "ds_read_b128 v[16:19], v1 offset:16384\n"
    "v_smfmac_f32_16x16x64_bf16 v[56:59], v[24:27], v[152:159], v2\n"
    "ds_read_b128 v[20:23], v1 offset:17408\n"
    "v_smfmac_f32_16x16x64_bf16 v[56:59], v[28:31], v[160:167], v2\n"
    "s_waitcnt lgkmcnt(2)\n"
    "s_barrier\n"
    "ds_read_b128 v[64:67], v76 offset:0\n"
    "v_smfmac_f32_16x16x64_bf16 v[60:63], v[28:31], v[168:175], v2\n"
    "ds_read_b128 v[40:43], v77 offset:0\n"
    "v_smfmac_f32_16x16x64_bf16 v[56:59], v[32:35], v[176:183], v2\n"
    "ds_read_b128 v[44:47], v78 offset:0\n"
    "v_smfmac_f32_16x16x64_bf16 v[60:63], v[32:35], v[184:191], v2\n"
    "ds_read_b64 v[12:13], v72 offset:4096\n"
    "ds_read_b64 v[14:15], v72 offset:4608\n"
    "v_smfmac_f32_16x16x64_bf16 v[56:59], v[36:39], v[192:199], v2\n"
    "ds_read_b64 v[52:53], v72 offset:5120\n"
    "ds_read_b64 v[54:55], v72 offset:5632\n"
    "v_smfmac_f32_16x16x64_bf16 v[60:63], v[36:39], v[200:207], v2\n"
    "ds_read_b128 v[24:27], v1 offset:0\n"
    "s_cmp_eq_u32 s68, 63\n"
    "s_waitcnt lgkmcnt(5)\n"
    "v_add_f32 v68, v68, v64\n"
    "v_add_f32 v69, v69, v65\n"
    "v_add_f32 v70, v70, v66\n"
    "v_add_f32 v71, v71, v67\n"
    "v_pk_add_f32 v[40:41], v[40:41], v[44:45]\n"
    "v_pk_add_f32 v[42:43], v[42:43], v[46:47]\n"
    "s_waitcnt lgkmcnt(1)\n"
    "v_pk_add_f32 v[12:13], v[12:13], v[14:15]\n"
    "v_pk_add_f32 v[52:53], v[52:53], v[54:55]\n"
    "v_pk_add_f32 v[68:69], v[68:69], v[40:41]\n"
    "v_pk_add_f32 v[70:71], v[70:71], v[42:43]\n"
    "v_pk_add_f32 v[12:13], v[12:13], v[52:53]\n"
    "v_max3_f32 v46, v68, v69, v70\n"
    "v_max3_f32 v47, v71, v12, v13\n"
    "v_max_f32 v3, v46, v47\n"
    "v_cvt_pk_bf16_f32 v56, v56, v57\n"
    "v_cvt_pk_bf16_f32 v57, v58, v59\n"
    "v_max_f32_dpp v46, v3, v3 row_ror:8 row_mask:0xf bank_mask:0xf\n"
    "v_max_f32_dpp v47, v3, v3 row_ror:8 row_mask:0xf bank_mask:0xf\n"
    "v_cvt_pk_bf16_f32 v58, v60, v61\n"
    "v_cvt_pk_bf16_f32 v59, v62, v63\n"
    "v_permlane16_swap_b32 v46, v47\n"
    "v_max_f32 v3, v46, v47\n"
    "v_max_f32 v46, v46, v47\n"
    "v_add_u32 v14, s73, v73\n"
    "v_xor_b32 v15, 128, v0\n"
    "v_permlane32_swap_b32 v3, v46\n"
    "v_max_f32 v3, v3, v46\n"
    "v_mul_f32 v46, 0xbe0293ee, v3\n"
    "v_pk_fma_f32 v[68:69], v[68:69], s[84:85], v[46:47] op_sel_hi:[1,0,0]\n"
    "v_pk_fma_f32 v[70:71], v[70:71], s[84:85], v[46:47] op_sel_hi:[1,0,0]\n"
    "v_pk_fma_f32 v[12:13], v[12:13], s[84:85], v[46:47] op_sel_hi:[1,0,0]\n"
    "v_exp_f32 v68, v68\n"
    "v_exp_f32 v69, v69\n"
    "v_exp_f32 v70, v70\n"
    "v_exp_f32 v71, v71\n"
    "v_exp_f32 v12, v12\n"
    "v_cvt_pk_bf16_f32 v40, v68, v69\n"
    "v_cvt_pk_bf16_f32 v41, v70, v71\n"
    "v_exp_f32 v13, v13\n"
    "v_mov_b32_dpp v42, v40 row_ror:8 row_mask:0xf bank_mask:0xf\n"
    "v_mov_b32_dpp v43, v41 row_ror:8 row_mask:0xf bank_mask:0xf\n"
    "v_cvt_pk_bf16_f32 v44, v12, v13\n"
    "v_pk_add_f32 v[68:69], v[68:69], v[70:71]\n"
    "s_waitcnt vmcnt(5)\n"
    "v_mfma_f32_16x16x32_bf16 v[48:51], v[4:7], v[40:43], 0\n"
    "ds_read_b128 v[28:31], v1 offset:1024\n"
    "v_mov_b32_dpp v45, v44 row_ror:8 row_mask:0xf bank_mask:0xf\n"
    "v_add_f32 v3, v12, v13\n"
    "v_mfma_f32_16x16x32_bf16 v[52:55], v[8:11], v[40:43], 0\n"
    "s_cbranch_scc1 .Lrec_fin_%=\n"
    "v_add_f32 v46, v68, v69\n"
    "v_fma_f32 v3, v3, 0.5, v46\n"
    "v_mfma_f32_16x16x16_bf16 v[48:51], v[56:57], v[44:45], v[48:51]\n"
    "ds_read_b128 v[32:35], v1 offset:24576\n"
    "v_add_f32_dpp v46, v3, v3 row_ror:8 row_mask:0xf bank_mask:0xf\n"
    "v_add_f32_dpp v47, v3, v3 row_ror:8 row_mask:0xf bank_mask:0xf\n"
    "v_mfma_f32_16x16x16_bf16 v[52:55], v[58:59], v[44:45], v[52:55]\n"
    "ds_read_b128 v[36:39], v1 offset:25600\n"
    "v_permlane16_swap_b32 v46, v47\n"
    "v_add_f32 v3, v46, v47\n"
    "v_add_f32 v46, v46, v47\n"
    "v_mov_b64 v[64:65], 0\n"
    "v_mov_b64 v[66:67], 0\n"
    "v_permlane32_swap_b32 v3, v46\n"
    "v_add_f32 v3, v3, v46\n"
    "v_rcp_f32 v46, v3\n"
    "v_mov_b32_dpp v48, v52 row_ror:8 row_mask:0xf bank_mask:0xc\n"
    "v_mov_b32_dpp v49, v53 row_ror:8 row_mask:0xf bank_mask:0xc\n"
    "v_mov_b32_dpp v50, v54 row_ror:8 row_mask:0xf bank_mask:0xc\n"
    "v_mov_b32_dpp v51, v55 row_ror:8 row_mask:0xf bank_mask:0xc\n"
    "v_pk_mul_f32 v[48:49], v[48:49], v[46:47] op_sel_hi:[1,0]\n"
    "v_pk_mul_f32 v[50:51], v[50:51], v[46:47] op_sel_hi:[1,0]\n"
    "v_cvt_pk_bf16_f32 v48, v48, v49\n"
    "v_cvt_pk_bf16_f32 v49, v50, v51\n"
    "ds_write_b64 v14, v[48:49] offset:8192\n"
    "buffer_store_dwordx4 v[56:59], v0, s[36:39], s76 offen offset:512 sc1\n"
    "ds_read_b128 v[72:75], v1 offset:8192\n"
    "ds_read_b128 v[76:79], v1 offset:9216\n"
    "ds_read_b128 v[4:7], v1 offset:32768\n"
    "ds_read_b128 v[8:11], v1 offset:33792\n"
    "v_lshlrev_b32 v12, 2, v0\n"
    "s_waitcnt vmcnt(3) lgkmcnt(4)\n"
    "s_barrier\n"
    "ds_read_b128 v[40:43], v0 offset:8192\n"
    "ds_read_b128 v[44:47], v0 offset:9216\n"
    "ds_read_b128 v[48:51], v15 offset:8192\n"
    "ds_read_b128 v[52:55], v15 offset:9216\n"
    "v_mov_b64 v[56:57], 0\n"
    "v_mov_b64 v[58:59], 0\n"
    "v_mov_b64 v[60:61], 0\n"
    "v_mov_b64 v[62:63], 0\n"
    "s_mov_b32 exec_hi, 0\n"
    "buffer_load_dwordx4 v[12:15], v12, s[36:39], s90 offen sc1\n"
    "s_mov_b32 exec_hi, -1\n"
    "s_waitcnt lgkmcnt(3)\n"
    "v_smfmac_f32_16x16x64_bf16 v[56:59], v[40:43], v[16:23], v2\n"
    "ds_read_b128 v[16:19], v1 offset:40960\n"
    "s_waitcnt lgkmcnt(3)\n"
    "v_smfmac_f32_16x16x64_bf16 v[56:59], v[44:47], v[24:31], v2\n"
    "ds_read_b128 v[20:23], v1 offset:41984\n"
    "v_smfmac_f32_16x16x64_bf16 v[60:63], v[40:43], v[32:39], v2\n"
    "ds_read_b128 v[24:27], v1 offset:49152\n"
    "v_mov_b64 v[70:71], 0\n"
    "v_smfmac_f32_16x16x64_bf16 v[60:63], v[44:47], v[72:79], v2\n"
    "ds_read_b128 v[28:31], v1 offset:50176\n"
    "s_waitcnt lgkmcnt(5)\n"
    "v_smfmac_f32_16x16x64_bf16 v[56:59], v[48:51], v[4:11], v2\n"
    "ds_read_b128 v[32:35], v1 offset:57344\n"
    "s_waitcnt lgkmcnt(5)\n"
    "v_smfmac_f32_16x16x64_bf16 v[56:59], v[52:55], v[208:215], v2\n"
    "ds_read_b128 v[36:39], v1 offset:58368\n"
    "ds_read_b128 v[8:11], v0 offset:14336\n"
    "s_waitcnt lgkmcnt(5)\n"
    "v_smfmac_f32_16x16x64_bf16 v[60:63], v[48:51], v[16:23], v2\n"
    "v_add_u32 v1, 32768, v1\n"
    "ds_read_b128 v[72:75], v1 offset:32768\n"
    "v_mov_b64 v[68:69], 0\n"
    "v_smfmac_f32_16x16x64_bf16 v[60:63], v[52:55], v[216:223], v2\n"
    "ds_read_b128 v[76:79], v1 offset:33792\n"
    "v_cvt_pk_bf16_f32 v56, v56, v57\n"
    "s_waitcnt lgkmcnt(5)\n"
    "v_smfmac_f32_16x16x64_bf16 v[64:67], v[40:43], v[24:31], v2\n"
    "ds_read_b128 v[16:19], v1 offset:40960\n"
    "v_cvt_pk_bf16_f32 v57, v58, v59\n"
    "v_smfmac_f32_16x16x64_bf16 v[64:67], v[44:47], v[224:231], v2\n"
    "ds_read_b128 v[20:23], v1 offset:41984\n"
    "v_cvt_pk_bf16_f32 v58, v60, v61\n"
    "v_cvt_pk_bf16_f32 v59, v62, v63\n"
    "s_waitcnt lgkmcnt(5)\n"
    "v_smfmac_f32_16x16x64_bf16 v[68:71], v[40:43], v[32:39], v2\n"
    "ds_read_b128 v[24:27], v1 offset:49152\n"
    "v_permlane16_swap_b32 v56, v58\n"
    "v_smfmac_f32_16x16x64_bf16 v[68:71], v[44:47], v[232:239], v2\n"
    "ds_read_b128 v[28:31], v1 offset:50176\n"
    "v_permlane16_swap_b32 v57, v59\n"
    "s_waitcnt lgkmcnt(4)\n"
    "v_smfmac_f32_16x16x64_bf16 v[64:67], v[48:51], v[72:79], v2\n"
    "ds_read_b128 v[32:35], v1 offset:57344\n"
    "buffer_store_dwordx4 v[56:59], v8, s[36:39], s77 offen sc1\n"
    "v_smfmac_f32_16x16x64_bf16 v[64:67], v[52:55], v[240:247], v2\n"
    "ds_read_b128 v[36:39], v1 offset:58368\n"
    "s_waitcnt lgkmcnt(4)\n"
    "v_smfmac_f32_16x16x64_bf16 v[68:71], v[48:51], v[16:23], v2\n"
    "v_smfmac_f32_16x16x64_bf16 v[68:71], v[52:55], v[248:255], v2\n"
    "v_cvt_pk_bf16_f32 v64, v64, v65\n"
    "s_waitcnt lgkmcnt(2)\n"
    "v_mfma_f32_16x16x32_bf16 v[4:7], v[40:43], v[24:27], 0\n"
    "v_cvt_pk_bf16_f32 v65, v66, v67\n"
    "v_mfma_f32_16x16x32_bf16 v[4:7], v[48:51], v[28:31], v[4:7]\n"
    "v_cvt_pk_bf16_f32 v66, v68, v69\n"
    "v_cvt_pk_bf16_f32 v67, v70, v71\n"
    "s_waitcnt lgkmcnt(0)\n"
    "v_mfma_f32_16x16x32_bf16 v[4:7], v[44:47], v[32:35], v[4:7]\n"
    "v_permlane16_swap_b32 v64, v66\n"
    "v_mfma_f32_16x16x32_bf16 v[4:7], v[52:55], v[36:39], v[4:7]\n"
    "v_permlane16_swap_b32 v65, v67\n"
    "buffer_store_dwordx4 v[64:67], v8, s[36:39], s86 offen sc1\n"
    "v_subrev_u32 v1, 81920, v1\n"
    "v_mov_b32 v76, v10\n"
    "ds_read_b128 v[72:75], v0 offset:15360\n"
    "v_mov_b32 v77, s68\n"
    "s_nop 1\n"
    "v_cvt_pk_bf16_f32 v4, v4, v5\n"
    "v_cvt_pk_bf16_f32 v5, v6, v7\n"
    "s_mov_b32 s33, s32\n"
    "v_permlane16_swap_b32 v4, v6\n"
    "v_permlane16_swap_b32 v5, v7\n"
    "buffer_store_dwordx4 v[4:7], v9, s[36:39], s87 offen sc1\n"
    "s_waitcnt vmcnt(3)\n"
    "v_max3_u32 v12, v12, v13, v14\n"
    "v_max_u32 v12, v12, v15\n"
    "v_mov_b32 v3, -1\n"
    "s_nop 0\n"
    "v_max_u32_dpp v12, v12, v12 row_ror:8 row_mask:0xf bank_mask:0xf\n"
    "s_nop 0\n"
    "s_nop 0\n"
    "v_max_u32_dpp v12, v12, v12 row_ror:4 row_mask:0xf bank_mask:0xf\n"
    "s_nop 0\n"
    "s_nop 0\n"
    "v_max_u32_dpp v12, v12, v12 row_ror:2 row_mask:0xf bank_mask:0xf\n"
    "s_nop 0\n"
    "s_nop 0\n"
    "v_max_u32_dpp v12, v12, v12 row_ror:1 row_mask:0xf bank_mask:0xf\n"
    "s_mov_b64 exec, 1\n"
    "buffer_atomic_smax v77, off, s[36:39], s58 sc1\n"
    "v_readlane_b32 s95, v12, 0\n"
    "v_readlane_b32 s99, v12, 16\n"
    "s_mov_b64 exec, -1\n"
    "s_max_u32 s95, s95, s99\n"
    "s_cmp_eq_u32 s95, -1\n"
    "s_cbranch_scc1 .Lrec_tsretry_%=\n"
    ".Lrec_tsgo_%=:\n"
    "s_memtime s[96:97]\n"
    "s_mov_b32 s31, s95\n"
    "s_sub_u32 s95, s95, s28\n"
    "s_min_u32 s95, s95, 4000\n"
    "s_add_u32 s95, s95, s34\n"
    "s_mul_i32 s95, s95, s48\n"
    "s_lshr_b32 s95, s95, 8\n"
    "s_add_u32 s32, s32, s95\n"
    "s_waitcnt lgkmcnt(0)\n"
    "s_sub_u32 s95, s32, s96\n"
    "s_mul_i32 s95, s95, 43\n"
    "s_ashr_i32 s95, s95, 10\n"
    "s_max_i32 s95, s95, 0\n"
    "s_min_i32 s95, s95, 100\n"
    ".Lrec_tsloop_%=:\n"
    "s_sub_u32 s95, s95, 1\n"
    "s_cbranch_scc0 .Lrec_tsloop_%=\n"
    "s_cmp_lg_u32 s60, 0\n"
    "s_cbranch_scc1 .Lrec_nored_%=\n"
    ".Lrec_red_%=:\n"
    "buffer_load_dwordx4 v[4:7], v0, s[36:39], s78 offen sc1\n"
    "buffer_load_dwordx4 v[8:11], v0, s[36:39], s78 offen offset:1024 sc1\n"
    "buffer_load_dwordx4 v[12:15], v0, s[36:39], s78 offen offset:2048 sc1\n"
    "buffer_load_dwordx4 v[16:19], v0, s[36:39], s78 offen offset:3072 sc1\n"
    "buffer_load_dwordx4 v[20:23], v0, s[36:39], s88 offen sc1\n"
    "buffer_load_dwordx4 v[24:27], v0, s[36:39], s88 offen offset:1024 sc1\n"
    "buffer_load_dwordx4 v[28:31], v0, s[36:39], s88 offen offset:2048 sc1\n"
    "buffer_load_dwordx4 v[32:35], v0, s[36:39], s88 offen offset:3072 sc1\n"
    "s_sub_u32 s95, s52, s35\n"
    "s_mul_i32 s95, s48, s95\n"
    "s_lshr_b32 s95, s95, 10\n"
    "s_add_u32 s32, s32, s95\n"
    "s_add_u32 s68, s68, 1\n"
    "s_and_b32 s70, s68, 3\n"
    "s_lshl_b32 s70, s70, 17\n"
    "s_add_u32 s70, s70, s66\n"
    "s_add_u32 s71, s70, 4096\n"
    "s_add_u32 s72, s70, 16384\n"
    "s_sub_u32 s46, s33, s46\n"
    "s_sub_u32 s47, s28, s47\n"
    "v_cvt_f32_u32 v40, s46\n"
    "v_cvt_f32_u32 v41, s47\n"
    "v_rcp_f32 v41, v41\n"
    "s_mov_b32 s46, s33\n"
    "s_mov_b32 s47, s28\n"
    "v_mul_f32 v40, v40, v41\n"
    "v_mov_b32 v42, 0x40c80000\n"
    "v_med3_f32 v40, v40, 4.0, v42\n"
    "v_rcp_f32 v41, v40\n"
    "v_mul_f32 v40, 0x43800000, v40\n"
    "v_mul_f32 v41, 0x44b54000, v41\n"
    "v_cvt_u32_f32 v40, v40\n"
    "v_cvt_u32_f32 v41, v41\n"
    "v_readfirstlane_b32 s48, v40\n"
    "v_readfirstlane_b32 s49, v41\n"
    "v_mov_b64 v[40:41], 0\n"
    "v_mov_b64 v[42:43], 0\n"
    "v_mov_b64 v[44:45], 0\n"
    "v_mov_b64 v[46:47], 0\n"
    "v_mov_b64 v[48:49], 0\n"
    "v_mov_b64 v[50:51], 0\n"
    "v_mov_b64 v[52:53], 0\n"
    "v_mov_b64 v[54:55], 0\n"
    "v_mov_b64 v[56:57], 0\n"
    "v_mov_b64 v[58:59], 0\n"
    "v_mov_b64 v[60:61], 0\n"
    "v_mov_b64 v[62:63], 0\n"
    "s_mov_b64 exec, 1\n"
    "buffer_atomic_smax v3, off, s[36:39], s59 sc0 sc1\n"
    "s_mov_b64 exec, -1\n"
    ".Lrec_rsum_%=:\n"
    "s_waitcnt vmcnt(8)\n"
    "v_mfma_f32_16x16x32_bf16 v[36:39], v[4:7], v[72:75], 0\n"
    "v_max3_u32 v78, v4, v5, v6\n"
    "v_max_u32 v78, v78, v7\n"
    "s_waitcnt vmcnt(7)\n"
    "v_mfma_f32_16x16x32_bf16 v[36:39], v[8:11], v[72:75], v[36:39]\n"
    "v_max3_u32 v78, v78, v8, v9\n"
    "v_max3_u32 v78, v78, v10, v11\n"
    "s_waitcnt vmcnt(6)\n"
    "v_mfma_f32_16x16x32_bf16 v[36:39], v[12:15], v[72:75], v[36:39]\n"
    "v_max3_u32 v78, v78, v12, v13\n"
    "v_max3_u32 v78, v78, v14, v15\n"
    "s_waitcnt vmcnt(5)\n"
    "v_mfma_f32_16x16x32_bf16 v[36:39], v[16:19], v[72:75], v[36:39]\n"
    "v_max3_u32 v78, v78, v16, v17\n"
    "v_max3_u32 v78, v78, v18, v19\n"
    "s_waitcnt vmcnt(4)\n"
    "v_mfma_f32_16x16x32_bf16 v[36:39], v[20:23], v[72:75], v[36:39]\n"
    "v_max3_u32 v78, v78, v20, v21\n"
    "v_max3_u32 v78, v78, v22, v23\n"
    "s_waitcnt vmcnt(3)\n"
    "v_mfma_f32_16x16x32_bf16 v[36:39], v[24:27], v[72:75], v[36:39]\n"
    "v_max3_u32 v78, v78, v24, v25\n"
    "v_max3_u32 v78, v78, v26, v27\n"
    "s_waitcnt vmcnt(2)\n"
    "v_mfma_f32_16x16x32_bf16 v[36:39], v[28:31], v[72:75], v[36:39]\n"
    "v_max3_u32 v78, v78, v28, v29\n"
    "v_max3_u32 v78, v78, v30, v31\n"
    "s_waitcnt vmcnt(1)\n"
    "v_mfma_f32_16x16x32_bf16 v[36:39], v[32:35], v[72:75], v[36:39]\n"
    "v_max3_u32 v78, v78, v32, v33\n"
    "v_max3_u32 v78, v78, v34, v35\n"
    "v_cmp_eq_u32 vcc, -1, v78\n"
    "s_nop 4\n"
    "v_cvt_pk_bf16_f32 v4, v36, v37\n"
    "v_cvt_pk_bf16_f32 v6, v36, v37\n"
    "v_cvt_pk_bf16_f32 v5, v38, v39\n"
    "v_cvt_pk_bf16_f32 v7, v38, v39\n"
    "s_cbranch_vccnz .Lrec_rretry_%=\n"
    ".Lrec_rgo_%=:\n"
    "v_permlane16_swap_b32 v4, v6\n"
    "v_permlane16_swap_b32 v5, v7\n"
    "buffer_store_dwordx4 v[4:7], v76, s[36:39], s79 offen sc1\n"
    "s_waitcnt vmcnt(1)\n"
    ".Lrec_b3_%=:\n"
    ".Lrec_entry_%=:\n"
    "s_memtime s[96:97]\n"
    "ds_read_b128 v[72:75], v1 offset:0\n"
    "ds_read_b128 v[76:79], v1 offset:1024\n"
    "v_readfirstlane_b32 s81, v3\n"
    "s_nop 0\n"
    "s_cmp_lt_i32 s81, s68\n"
    "s_cbranch_scc1 .Lrec_waitp_%=\n"
    "s_waitcnt lgkmcnt(0)\n"
    "s_sub_u32 s95, s32, s96\n"
    "s_mul_i32 s95, s95, 43\n"
    "s_ashr_i32 s95, s95, 10\n"
    "s_max_i32 s95, s95, 0\n"
    "s_min_i32 s95, s95, 100\n"
    ".Lrec_gloop_%=:\n"
    "s_sub_u32 s95, s95, 1\n"
    "s_cbranch_scc0 .Lrec_gloop_%=\n"
    "s_branch .Lrec_top_%=\n"
    ".Lrec_LOOPE_%=:\n"
    ".Lrec_fin_%=:\n"
    "buffer_store_dwordx4 v[56:59], v0, s[36:39], s76 offen offset:512 sc1\n"
    "s_waitcnt vmcnt(0) lgkmcnt(0)\n"
    "v_mov_b32 v46, s68\n"
    "s_mov_b64 exec, 1\n"
    "buffer_atomic_smax v46, off, s[36:39], s58 sc1\n"
    "s_mov_b64 exec, -1\n"
    "s_waitcnt vmcnt(0)\n"
    "s_branch .Lrec_FIN_%=\n"
    ".Lrec_retry0_%=:\n"
    "s_cmp_eq_u32 s69, 0\n"
    "s_cbranch_scc1 .Lrec_back0_%=\n"
    "s_waitcnt lgkmcnt(0)\n"
    "ds_read_b128 v[72:75], v1 offset:0\n"
    "ds_read_b128 v[76:79], v1 offset:1024\n"
    ".Lrec_again0_%=:\n"
    "buffer_load_dwordx4 v[4:7], v0, s[36:39], s70 offen sc1\n"
    "s_nop 7\n"
    "v_mov_b64 v[40:41], 0\n"
    "v_mov_b64 v[42:43], 0\n"
    "v_mov_b64 v[44:45], 0\n"
    "v_mov_b64 v[46:47], 0\n"
    "v_mov_b64 v[48:49], 0\n"
    "v_mov_b64 v[50:51], 0\n"
    "v_mov_b64 v[52:53], 0\n"
    "v_mov_b64 v[54:55], 0\n"
    "v_mov_b64 v[56:57], 0\n"
    "v_mov_b64 v[58:59], 0\n"
    "v_mov_b64 v[60:61], 0\n"
    "v_mov_b64 v[62:63], 0\n"
    "s_waitcnt vmcnt(0)\n"
    "v_max3_u32 v3, v4, v5, v6\n"
    "v_max_u32 v3, v3, v7\n"
    "v_cmp_eq_u32 vcc, -1, v3\n"
    "s_cbranch_vccz .Lrec_start_%=\n"
    "s_sub_u32 s69, s69, 1\n"
    "s_cbranch_scc0 .Lrec_again0_%=\n"
    "s_mov_b32 s69, 0\n"
    "s_branch .Lrec_start_%=\n"
    ".Lrec_retry1_%=:\n"
    "s_cmp_eq_u32 s69, 0\n"
    "s_cbranch_scc1 .Lrec_back1_%=\n"
    "s_waitcnt lgkmcnt(0)\n"
    "ds_read_b128 v[72:75], v1 offset:0\n"
    "ds_read_b128 v[76:79], v1 offset:1024\n"
    ".Lrec_again1_%=:\n"
    "buffer_load_dwordx4 v[4:7], v0, s[36:39], s70 offen sc1\n"
    "buffer_load_dwordx4 v[8:11], v0, s[36:39], s70 offen offset:1024 sc1\n"
    "buffer_load_dwordx4 v[12:15], v0, s[36:39], s70 offen offset:2048 sc1\n"
    "buffer_load_dwordx4 v[16:19], v0, s[36:39], s70 offen offset:3072 sc1\n"
    "s_nop 7\n"
    "v_mov_b64 v[40:41], 0\n"
    "v_mov_b64 v[42:43], 0\n"
    "v_mov_b64 v[44:45], 0\n"
    "v_mov_b64 v[46:47], 0\n"
    "v_mov_b64 v[48:49], 0\n"
    "v_mov_b64 v[50:51], 0\n"
    "v_mov_b64 v[52:53], 0\n"
    "v_mov_b64 v[54:55], 0\n"
    "v_mov_b64 v[56:57], 0\n"
    "v_mov_b64 v[58:59], 0\n"
    "v_mov_b64 v[60:61], 0\n"
    "v_mov_b64 v[62:63], 0\n"
    "s_waitcnt vmcnt(0)\n"
    "v_max3_u32 v3, v4, v5, v6\n"
    "v_max3_u32 v3, v3, v7, v8\n"
    "v_max3_u32 v3, v3, v9, v10\n"
    "v_max3_u32 v3, v3, v11, v12\n"
    "v_max3_u32 v3, v3, v13, v14\n"
    "v_max3_u32 v3, v3, v15, v16\n"
    "v_max3_u32 v3, v3, v17, v18\n"
    "v_max_u32 v3, v3, v19\n"
    "v_cmp_eq_u32 vcc, -1, v3\n"
    "s_cbranch_vccz .Lrec_start_%=\n"
    "s_sub_u32 s69, s69, 1\n"
    "s_cbranch_scc0 .Lrec_again1_%=\n"
    "s_mov_b32 s69, 0\n"
    "s_branch .Lrec_start_%=\n"
    ".Lrec_retry2_%=:\n"
    "s_cmp_eq_u32 s69, 0\n"
    "s_cbranch_scc1 .Lrec_back2_%=\n"
    "v_subrev_u32 v1, 49152, v1\n"
    "s_waitcnt lgkmcnt(0)\n"
    "ds_read_b128 v[72:75], v1 offset:0\n"
    "ds_read_b128 v[76:79], v1 offset:1024\n"
    ".Lrec_again2_%=:\n"
    "buffer_load_dwordx4 v[4:7], v0, s[36:39], s70 offen sc1\n"
    "buffer_load_dwordx4 v[8:11], v0, s[36:39], s70 offen offset:1024 sc1\n"
    "buffer_load_dwordx4 v[12:15], v0, s[36:39], s70 offen offset:2048 sc1\n"
    "buffer_load_dwordx4 v[16:19], v0, s[36:39], s70 offen offset:3072 sc1\n"
    "buffer_load_dwordx4 v[20:23], v0, s[36:39], s71 offen sc1\n"
    "buffer_load_dwordx4 v[24:27], v0, s[36:39], s71 offen offset:1024 sc1\n"
    "buffer_load_dwordx4 v[28:31], v0, s[36:39], s71 offen offset:2048 sc1\n"
    "buffer_load_dwordx4 v[32:35], v0, s[36:39], s71 offen offset:3072 sc1\n"
    "buffer_load_dwordx4 v[36:39], v0, s[36:39], s72 offen sc1\n"
    "s_nop 7\n"
    "v_mov_b64 v[40:41], 0\n"
    "v_mov_b64 v[42:43], 0\n"
    "v_mov_b64 v[44:45], 0\n"
    "v_mov_b64 v[46:47], 0\n"
    "v_mov_b64 v[48:49], 0\n"
    "v_mov_b64 v[50:51], 0\n"
    "v_mov_b64 v[52:53], 0\n"
    "v_mov_b64 v[54:55], 0\n"
    "v_mov_b64 v[56:57], 0\n"
    "v_mov_b64 v[58:59], 0\n"
    "v_mov_b64 v[60:61], 0\n"
    "v_mov_b64 v[62:63], 0\n"
    "s_waitcnt vmcnt(0)\n"
    "v_max3_u32 v3, v4, v5, v6\n"
    "v_max3_u32 v3, v3, v7, v8\n"
    "v_max3_u32 v3, v3, v9, v10\n"
    "v_max3_u32 v3, v3, v11, v12\n"
    "v_max3_u32 v3, v3, v13, v14\n"
    "v_max3_u32 v3, v3, v15, v16\n"
    "v_max3_u32 v3, v3, v17, v18\n"
    "v_max3_u32 v3, v3, v19, v20\n"
    "v_max3_u32 v3, v3, v21, v22\n"
    "v_max3_u32 v3, v3, v23, v24\n"
    "v_max3_u32 v3, v3, v25, v26\n"
    "v_max3_u32 v3, v3, v27, v28\n"
    "v_max3_u32 v3, v3, v29, v30\n"
    "v_max3_u32 v3, v3, v31, v32\n"
    "v_max3_u32 v3, v3, v33, v34\n"
    "v_max3_u32 v3, v3, v35, v36\n"
    "v_max3_u32 v3, v3, v37, v38\n"
    "v_max_u32 v3, v3, v39\n"
    "v_cmp_eq_u32 vcc, -1, v3\n"
    "s_cbranch_vccz .Lrec_start_%=\n"
    "s_sub_u32 s69, s69, 1\n"
    "s_cbranch_scc0 .Lrec_again2_%=\n"
    "s_mov_b32 s69, 0\n"
    "s_branch .Lrec_start_%=\n"
    ".Lrec_tsretry_%=:\n"
    "s_mov_b32 s98, 24\n"
    ".Lrec_tsretry1_%=:\n"
    "s_mov_b32 s95, s28\n"
    "s_sub_u32 s98, s98, 1\n"
    "s_cbranch_scc1 .Lrec_tsgo_%=\n"
    "s_sleep 2\n"
    "buffer_load_dwordx4 v[12:15], v11, s[36:39], s90 offen sc1\n"
    "s_waitcnt vmcnt(0)\n"
    "v_max3_u32 v12, v12, v13, v14\n"
    "v_max_u32 v12, v12, v15\n"
    "s_nop 1\n"
    "v_max_u32_dpp v12, v12, v12 row_ror:8 row_mask:0xf bank_mask:0xf\n"
    "s_nop 1\n"
    "v_max_u32_dpp v12, v12, v12 row_ror:4 row_mask:0xf bank_mask:0xf\n"
    "s_nop 1\n"
    "v_max_u32_dpp v12, v12, v12 row_ror:2 row_mask:0xf bank_mask:0xf\n"
    "s_nop 1\n"
    "v_max_u32_dpp v12, v12, v12 row_ror:1 row_mask:0xf bank_mask:0xf\n"
    "s_nop 0\n"
    "v_readlane_b32 s95, v12, 0\n"
    "v_readlane_b32 s99, v12, 16\n"
    "s_nop 0\n"
    "s_max_u32 s95, s95, s99\n"
    "s_cmp_eq_u32 s95, -1\n"
    "s_cbranch_scc1 .Lrec_tsretry1_%=\n"
    "s_branch .Lrec_tsgo_%=\n"
    ".Lrec_rretry_%=:\n"
    "s_sub_u32 s69, s69, 1\n"
    "s_cbranch_scc1 .Lrec_rdead_%=\n"
    "s_nop 7\n"
    "buffer_load_dwordx4 v[4:7], v0, s[36:39], s78 offen sc1\n"
    "buffer_load_dwordx4 v[8:11], v0, s[36:39], s78 offen offset:1024 sc1\n"
    "buffer_load_dwordx4 v[12:15], v0, s[36:39], s78 offen offset:2048 sc1\n"
    "buffer_load_dwordx4 v[16:19], v0, s[36:39], s78 offen offset:3072 sc1\n"
    "buffer_load_dwordx4 v[20:23], v0, s[36:39], s88 offen sc1\n"
    "buffer_load_dwordx4 v[24:27], v0, s[36:39], s88 offen offset:1024 sc1\n"
    "buffer_load_dwordx4 v[28:31], v0, s[36:39], s88 offen offset:2048 sc1\n"
    "buffer_load_dwordx4 v[32:35], v0, s[36:39], s88 offen offset:3072 sc1\n"
    "s_waitcnt vmcnt(0)\n"
    "s_branch .Lrec_rsum_%=\n"
    ".Lrec_rdead_%=:\n"
    "s_mov_b32 s69, 0\n"
    "s_nop 7\n"
    "s_branch .Lrec_rgo_%=\n"
    ".Lrec_nored_%=:\n"
    "s_nop 8\n"
    "s_cmp_lt_i32 s64, 0\n"
    "s_cbranch_scc0 .Lrec_red_%=\n"
    "s_sub_u32 s95, s52, s35\n"
    "s_mul_i32 s95, s48, s95\n"
    "s_lshr_b32 s95, s95, 10\n"
    "s_add_u32 s32, s32, s95\n"
    "s_add_u32 s68, s68, 1\n"
    "s_and_b32 s70, s68, 3\n"
    "s_lshl_b32 s70, s70, 17\n"
    "s_add_u32 s70, s70, s66\n"
    "s_add_u32 s71, s70, 4096\n"
    "s_add_u32 s72, s70, 16384\n"
    "s_sub_u32 s46, s33, s46\n"
    "s_sub_u32 s47, s28, s47\n"
    "v_cvt_f32_u32 v40, s46\n"
    "v_cvt_f32_u32 v41, s47\n"
    "v_rcp_f32 v41, v41\n"
    "s_mov_b32 s46, s33\n"
    "s_mov_b32 s47, s28\n"
    "v_mul_f32 v40, v40, v41\n"
    "v_mov_b32 v42, 0x40c80000\n"
    "v_med3_f32 v40, v40, 4.0, v42\n"
    "v_rcp_f32 v41, v40\n"
    "v_mul_f32 v40, 0x43800000, v40\n"
    "v_mul_f32 v41, 0x44b54000, v41\n"
    "v_cvt_u32_f32 v40, v40\n"
    "v_cvt_u32_f32 v41, v41\n"
    "v_readfirstlane_b32 s48, v40\n"
    "v_readfirstlane_b32 s49, v41\n"
    "v_mov_b64 v[40:41], 0\n"
    "v_mov_b64 v[42:43], 0\n"
    "v_mov_b64 v[44:45], 0\n"
    "v_mov_b64 v[46:47], 0\n"
    "v_mov_b64 v[48:49], 0\n"
    "v_mov_b64 v[50:51], 0\n"
    "v_mov_b64 v[52:53], 0\n"
    "v_mov_b64 v[54:55], 0\n"
    "v_mov_b64 v[56:57], 0\n"
    "v_mov_b64 v[58:59], 0\n"
    "v_mov_b64 v[60:61], 0\n"
    "v_mov_b64 v[62:63], 0\n"
    "s_mov_b64 exec, 1\n"
    "buffer_atomic_smax v3, off, s[36:39], s59 sc0 sc1\n"
    "s_mov_b64 exec, -1\n"
    "s_waitcnt vmcnt(0)\n"
    "s_branch .Lrec_b3_%=\n"
    ".Lrec_waitp_%=:\n"
    "v_mov_b32 v3, -1\n"
    "s_mov_b64 exec, 1\n"
    "buffer_atomic_smax v3, off, s[36:39], s59 sc0 sc1\n"
    "s_mov_b64 exec, -1\n"
    "s_waitcnt vmcnt(0)\n"
    "v_readfirstlane_b32 s81, v3\n"
    "s_nop 0\n"
    "s_cmp_ge_i32 s81, s68\n"
    "s_cbranch_scc1 .Lrec_waitp1_%=\n"
    "s_sleep 1\n"
    "s_sub_u32 s69, s69, 1\n"
    "s_cbranch_scc0 .Lrec_waitp_%=\n"
    "s_mov_b32 s69, 0\n"
    ".Lrec_waitp1_%=:\n"
    "s_memtime s[96:97]\n"
    "s_waitcnt lgkmcnt(0)\n"
    "s_sub_u32 s95, s32, s96\n"
    "s_mul_i32 s95, s95, 43\n"
    "s_ashr_i32 s95, s95, 10\n"
    "s_max_i32 s95, s95, 0\n"
    "s_min_i32 s95, s95, 100\n"
    ".Lrec_gloop2_%=:\n"
    "s_sub_u32 s95, s95, 1\n"
    "s_cbranch_scc0 .Lrec_gloop2_%=\n"
    "s_branch .Lrec_top_%=\n"
    ".Lrec_FIN_%=:\n"
  );
}

typedef unsigned long long u64;
DEVI u64 ld8u(rsrc_t r, u32 voff) { v2u v = ld8(r, voff, 0); asm volatile("" : "+v"(v)); return (u64)v.x | ((u64)v.y << 32); }
DEVI u32 find_shift(rsrc_t rw, bool decide, u64 tag) {
  const int lane = threadIdx.x & 63;
  unsigned budget = SPIN_LIMIT;
  if (decide) {
    u32 best[2] = {~0u, ~0u};
    const u32 probe = 126 * CHUNK + 8192;
#pragma unroll 1
    for (int r = 0; r < 5; ++r) {
      const u32 a = lane == 0 ? probe + (u32)(r & 1) * 8192 : OOB;
      u32 v = 0;
      const unsigned long long t0 = __builtin_amdgcn_s_memtime();
      asm volatile("s_waitcnt lgkmcnt(0)\n\tbuffer_atomic_or %0, %1, %2, 0 offen sc0 sc1\n\ts_waitcnt vmcnt(0)" : "+v"(v) : "v"(a), "s"(rw) : "memory");
      const u32 dt = (u32)(__builtin_amdgcn_s_memtime() - t0);
      if (r & 1) best[1] = min(best[1], dt); else best[0] = min(best[0], dt);
    }
    for (;;) {
      v2u v[4];
      const u32 a = READY + (u32)lane * 8;
#pragma unroll
      for (int k = 0; k < 4; ++k) v[k] = (v2u){0u, 0u};
      asm volatile("buffer_atomic_or_x2 %0, %4, %5, 0 offen sc0 sc1\n\tbuffer_atomic_or_x2 %1, %4, %5, 0 offen offset:512 sc0 sc1\n\t"
                   "buffer_atomic_or_x2 %2, %4, %5, 0 offen offset:1024 sc0 sc1\n\tbuffer_atomic_or_x2 %3, %4, %5, 0 offen offset:1536 sc0 sc1\n\ts_waitcnt vmcnt(0)"
                   : "+v"(v[0]), "+v"(v[1]), "+v"(v[2]), "+v"(v[3]) : "v"(a), "s"(rw) : "memory");
      bool ok = true;
#pragma unroll
      for (int k = 0; k < 4; ++k) ok = ok & (v[k].x == (u32)tag) & (v[k].y == (u32)(tag >> 32));
      if (__all(ok) || budget == 0) break;
      --budget;
    }
    const u64 v = lane == 0 ? 2 * tag + (best[0] < best[1] ? 0u : 1u) : tag;
    st8(rw, (v2u){(u32)v, (u32)(v >> 32)}, lane == 0 ? PARW : (lane == 1 ? EPOCH : OOB), 0);
  }
  u64 v;
  for (;;) {
    cbar();
    v = ld8u(rw, PARW) ^ (2 * tag);
    if (__all(v < 2) || budget == 0) break;
    --budget;
    __builtin_amdgcn_s_sleep(4);
  }
  cbar();
  return (rfl((u32)v) & 1u) * 8192;
}
DEVI void hot_init(const Params& p, rsrc_t rw, int L, int n, u32 shift) {
  const int tid = threadIdx.x, w = (int)rfl(tid >> 6);
  const u32 hot = HOT + L * 8192 + shift, hot2 = HOT2 + (1 - L) * 8192 + shift;
  const v4u ones = {~0u, ~0u, ~0u, ~0u};
#pragma unroll
  for (int k = 0; k < 9; ++k) {
    st16(rw, ones, tid * 16, hot + (u32)(9 * n + k) * CHUNK);
    st16(rw, ones, tid * 16 + 4096, hot + (u32)(9 * n + k) * CHUNK);
  }
  {
    const u32 b = hot2 + (u32)(4 + n) * 2 * CHUNK;
    st16(rw, ones, tid * 16, b); st16(rw, ones, tid * 16 + 4096, b);
    st16(rw, ones, w == 0 ? (u32)tid * 16 : OOB, b + CHUNK);
  }
  if (n < 8) {
#pragma unroll
    for (int k = 0; k < 2; ++k) {
      st16(rw, ones, tid * 16, hot2 + (u32)(32 + 2 * n + k) * CHUNK);
      st16(rw, ones, tid * 16 + 4096, hot2 + (u32)(32 + 2 * n + k) * CHUNK);
    }
  }
  if (n >= 8) {
    const u32 b = hot2 + (u32)(n - 8) * 2 * CHUNK;
#pragma unroll
    for (int k = 0; k < 3; ++k) {
      const int pc = tid + 256 * k;
      if (pc < 576) {
        v4u v = *(const v4u*)(p.mem0 + (pc & 7) * 576 + (pc >> 3) * 8);
#pragma unroll
        for (int d = 0; d < 4; ++d) v[d] = min(v[d], 0xFFFFFFFEu);
        st16(rw, v, pc < 512 ? (u32)pc * 16 : CHUNK + (u32)(pc - 512) * 16, b);
      }
    }
  }
  drain();
  __syncthreads();
  if (tid == 0) st4(rw, 0u, INITF + L * 8192 + n * 128 + shift, 0);
}

DEVI void role_att(const Params& p, int L, int j, int a, u32 dbg, u32 shift) {
  const int tid = threadIdx.x, lane = tid & 63, w = (int)rfl(tid >> 6);
  const int c = lane & 15, g = lane >> 4;
  const int hl = 4 * a + w, h = 8 * j + hl;
  const v4u ws = make_srd(p.ws + shift, WS_END);
  v16u sa = cat4(ws, ws, ws, ws);
  v16u sb = {pga(L, B_QKV + 3 * h), pga(L, B_KVM + h), pgb(L, B_O + h), 0, dbg + w * 64,
             0, 0, 0, 0, 0, 0, 0, 0, 0, 0, 0};
  v16u va = {0, 0, 0, 0, 0, 0, 0, 0, 0, 0, 0, 0, 0, 0, 0, 0};
  va[0] = lane * 16;
  va[1] = (u32)(g * 128 + (c & 7) * 16);
  va[2] = lane < 3 ? mbox(L, j, SL_WO + lane, hl) : mbox(L, j, SL_P + hl, 48);
  va[4] = mbox_att(L, j, hl, lane & 15);
  va[5] = (u32)(lane < 4 ? 0 : (lane < 7 ? RO : ((lane >> 2) == 2 ? -1 : 1000)));
  va[6] = (u32)(c - 4 * g);
  va[7] = 0;
  RUN_ASM(
    "s_nop 4\n"
    "s_getpc_b64 s[84:85]\n"
    ".Latt_PC_%=:\n"
    "s_and_b32 s85, s85, 0xffff\n"
    "s_mov_b32 s86, .Latt_FIN_%=-.Latt_PC_%=\n"
    "s_mov_b32 s87, 0x20000\n"
    "v_mbcnt_lo_u32_b32 v240, -1, 0\n"
    "v_mbcnt_hi_u32_b32 v240, -1, v240\n"
    "v_lshlrev_b32 v240, 7, v240\n"
    "buffer_load_dword v241, v240, s[84:87], 0 offen\n"
    "v_add_u32 v240, 8192, v240\n"
    "buffer_load_dword v241, v240, s[84:87], 0 offen\n"
    "v_cmp_gt_i32 s[10:11], 0, v6\n"
    "v_cmp_gt_i32 s[12:13], 1, v6\n"
    "v_cmp_gt_i32 s[14:15], 2, v6\n"
    "v_cmp_gt_i32 s[16:17], 3, v6\n"
    "v_mov_b32 v10, 0xff61b1e6\n"
    "s_mov_b32 s18, 0x3e0293ee\n"
    "s_mov_b32 s68, 0\n"
    "v_mov_b32 v8, -1\n"
    "v_mov_b32 v12, -1\n"
    "s_mov_b32 s69, 262144\n"
    "s_branch .Latt_waitp_%=\n"
    ".Latt_LOOPB_%=:\n"
    ".Latt_top_%=:\n"
    "s_waitcnt vmcnt(15)\n"
    "v_mfma_f32_16x16x32_bf16 v[160:163], v[16:19], v[48:51], 0\n"
    "v_mfma_f32_16x16x32_bf16 v[164:167], v[16:19], v[52:55], 0\n"
    "v_mfma_f32_16x16x32_bf16 v[168:171], v[20:23], v[52:55], 0\n"
    "s_waitcnt vmcnt(12)\n"
    "v_mfma_f32_16x16x32_bf16 v[160:163], v[24:27], v[56:59], v[160:163]\n"
    "v_mfma_f32_16x16x32_bf16 v[164:167], v[24:27], v[60:63], v[164:167]\n"
    "v_mfma_f32_16x16x32_bf16 v[168:171], v[28:31], v[60:63], v[168:171]\n"
    "s_waitcnt vmcnt(10)\n"
    "v_mfma_f32_16x16x32_bf16 v[160:163], v[32:35], v[64:67], v[160:163]\n"
    "v_mfma_f32_16x16x32_bf16 v[164:167], v[32:35], v[68:71], v[164:167]\n"
    "v_mfma_f32_16x16x32_bf16 v[168:171], v[36:39], v[68:71], v[168:171]\n"
    "s_waitcnt vmcnt(8)\n"
    "v_mfma_f32_16x16x32_bf16 v[160:163], v[40:43], v[72:75], v[160:163]\n"
    "v_mfma_f32_16x16x32_bf16 v[164:167], v[40:43], v[76:79], v[164:167]\n"
    "v_mfma_f32_16x16x32_bf16 v[168:171], v[44:47], v[76:79], v[168:171]\n"
    "v_add_u32 v11, v8, v5\n"
    "v_cmp_gt_i32 vcc, s68, v11\n"
    "s_nop 0\n"
    "s_or_b64 exec, vcc, 1\n"
    "buffer_atomic_smax v8, v4, s[36:39], 0 offen sc0 sc1\n"
    "s_mov_b64 exec, -1\n"
    "s_add_u32 s72, s68, 1\n"
    "s_and_b32 s4, s72, 7\n"
    "s_mul_i32 s4, s4, 4194304\n"
    "s_add_u32 s4, s4, s52\n"
    "s_add_u32 s5, s4, 4096\n"
    "s_add_u32 s6, s4, 16384\n"
    "s_add_u32 s7, s4, 20480\n"
    "s_add_u32 s8, s4, 32768\n"
    "s_add_u32 s9, s4, 36864\n"
    "s_and_b32 s73, s68, 7\n"
    "s_mul_i32 s73, s73, 4194304\n"
    "s_add_u32 s73, s73, s53\n"
    "s_add_u32 s74, s73, 3072\n"
    "s_and_b32 s75, s68, 7\n"
    "s_lshl_b32 s75, s75, 22\n"
    "s_add_u32 s75, s75, s54\n"
    "s_add_u32 s76, s75, 4096\n"
    "v_cndmask_b32 v160, v160, v10, s[10:11]\n"
    "v_cndmask_b32 v161, v161, v10, s[12:13]\n"
    "v_cndmask_b32 v162, v162, v10, s[14:15]\n"
    "v_cndmask_b32 v163, v163, v10, s[16:17]\n"
    "v_cndmask_b32 v168, v168, v10, s[10:11]\n"
    "v_cndmask_b32 v169, v169, v10, s[12:13]\n"
    "v_cndmask_b32 v170, v170, v10, s[14:15]\n"
    "v_cndmask_b32 v171, v171, v10, s[16:17]\n"
    "v_max3_f32 v224, v160, v161, v162\n"
    "v_max3_f32 v226, v164, v165, v166\n"
    "v_max_f32 v224, v224, v163\n"
    "v_max3_f32 v226, v226, v167, v168\n"
    "v_max3_f32 v226, v226, v169, v170\n"
    "v_max_f32 v226, v226, v171\n"
    "v_mov_b32 v238, v224\n"
    "v_mov_b32 v239, v226\n"
    "s_nop 0\n"
    "v_permlane16_swap_b32 v224, v238\n"
    "v_permlane16_swap_b32 v226, v239\n"
    "v_max_f32 v224, v224, v238\n"
    "v_max_f32 v226, v226, v239\n"
    "v_mov_b32 v238, v224\n"
    "v_mov_b32 v239, v226\n"
    "s_nop 0\n"
    "v_permlane32_swap_b32 v224, v238\n"
    "v_permlane32_swap_b32 v226, v239\n"
    "v_max_f32 v224, v224, v238\n"
    "v_max_f32 v226, v226, v239\n"
    "v_mul_f32 v230, 0xbe0293ee, v224\n"
    "v_mul_f32 v232, 0xbe0293ee, v226\n"
    "v_pk_fma_f32 v[160:161], v[160:161], s[18:19], v[230:231] op_sel_hi:[1,0,0]\n"
    "v_pk_fma_f32 v[162:163], v[162:163], s[18:19], v[230:231] op_sel_hi:[1,0,0]\n"
    "v_pk_fma_f32 v[164:165], v[164:165], s[18:19], v[232:233] op_sel_hi:[1,0,0]\n"
    "v_pk_fma_f32 v[166:167], v[166:167], s[18:19], v[232:233] op_sel_hi:[1,0,0]\n"
    "v_pk_fma_f32 v[168:169], v[168:169], s[18:19], v[232:233] op_sel_hi:[1,0,0]\n"
    "v_pk_fma_f32 v[170:171], v[170:171], s[18:19], v[232:233] op_sel_hi:[1,0,0]\n"
    "v_exp_f32 v160, v160\n"
    "v_exp_f32 v161, v161\n"
    "v_exp_f32 v162, v162\n"
    "v_exp_f32 v163, v163\n"
    "v_exp_f32 v164, v164\n"
    "v_exp_f32 v165, v165\n"
    "v_exp_f32 v166, v166\n"
    "v_exp_f32 v167, v167\n"
    "v_exp_f32 v168, v168\n"
    "v_exp_f32 v169, v169\n"
    "v_exp_f32 v170, v170\n"
    "v_exp_f32 v171, v171\n"
    "v_pk_add_f32 v[230:231], v[160:161], v[162:163]\n"
    "v_pk_add_f32 v[232:233], v[164:165], v[166:167]\n"
    "v_pk_add_f32 v[234:235], v[168:169], v[170:171]\n"
    "v_pk_add_f32 v[232:233], v[232:233], v[234:235]\n"
    "v_add_f32 v228, v230, v231\n"
    "v_add_f32 v229, v232, v233\n"
    "v_mov_b32 v238, v228\n"
    "v_mov_b32 v239, v229\n"
    "s_nop 0\n"
    "v_permlane16_swap_b32 v228, v238\n"
    "v_permlane16_swap_b32 v229, v239\n"
    "v_add_f32 v228, v228, v238\n"
    "v_add_f32 v229, v229, v239\n"
    "v_mov_b32 v238, v228\n"
    "v_mov_b32 v239, v229\n"
    "s_nop 0\n"
    "v_permlane32_swap_b32 v228, v238\n"
    "v_permlane32_swap_b32 v229, v239\n"
    "v_add_f32 v228, v228, v238\n"
    "v_add_f32 v229, v229, v239\n"
    "v_add_u32 v11, v12, v5\n"
    "v_cmp_gt_i32 vcc, s68, v11\n"
    "s_nop 0\n"
    "s_or_b64 exec, vcc, 1\n"
    "buffer_atomic_smax v12, v4, s[36:39], 0 offen sc0 sc1\n"
    "s_mov_b64 exec, -1\n"
    "s_waitcnt vmcnt(1)\n"
    "v_add_u32 v11, v8, v5\n"
    "v_cmp_gt_i32 vcc, s68, v11\n"
    "s_nop 0\n"
    "s_and_b32 s71, vcc_lo, 0xff\n"
    "s_cbranch_scc1 .Latt_waitr_%=\n"
    ".Latt_gor_%=:\n"
    "buffer_load_dwordx4 v[128:131], v1, s[36:39], s73 offen sc1\n"
    "buffer_load_dwordx4 v[132:135], v1, s[36:39], s73 offen offset:1536 sc1\n"
    "buffer_load_dwordx4 v[136:139], v1, s[36:39], s74 offen sc1\n"
    "buffer_load_dwordx4 v[140:143], v1, s[36:39], s74 offen offset:1536 sc1\n"
    "buffer_load_dwordx4 v[144:147], v0, s[36:39], s73 offen offset:512 sc1\n"
    "buffer_load_dwordx4 v[148:151], v0, s[36:39], s73 offen offset:2048 sc1\n"
    "buffer_load_dwordx4 v[152:155], v0, s[36:39], s74 offen offset:512 sc1\n"
    "buffer_load_dwordx4 v[156:159], v0, s[36:39], s74 offen offset:2048 sc1\n"
    "s_and_b32 s71, vcc_lo, 0xf00\n"
    "s_cselect_b32 s70, 0, 1\n"
    "s_cselect_b32 s71, 0x80000000, 0\n"
    "v_or_b32 v7, s71, v0\n"
    "s_nop 15\n"
    "buffer_load_dwordx4 v[16:19], v7, s[36:39], s6 offen sc1\n"
    "s_nop 15\n"
    "buffer_load_dwordx4 v[20:23], v7, s[36:39], s6 offen offset:1024 sc1\n"
    "s_waitcnt vmcnt(9)\n"
    "v_mfma_f32_16x16x32_bf16 v[172:175], v[128:131], v[48:51], 0\n"
    "v_mfma_f32_16x16x32_bf16 v[176:179], v[128:131], v[52:55], 0\n"
    "s_waitcnt vmcnt(8)\n"
    "v_mfma_f32_16x16x32_bf16 v[172:175], v[132:135], v[56:59], v[172:175]\n"
    "v_mfma_f32_16x16x32_bf16 v[176:179], v[132:135], v[60:63], v[176:179]\n"
    "s_waitcnt vmcnt(7)\n"
    "v_mfma_f32_16x16x32_bf16 v[172:175], v[136:139], v[64:67], v[172:175]\n"
    "v_mfma_f32_16x16x32_bf16 v[176:179], v[136:139], v[68:71], v[176:179]\n"
    "s_waitcnt vmcnt(6)\n"
    "v_mfma_f32_16x16x32_bf16 v[172:175], v[140:143], v[72:75], v[172:175]\n"
    "v_mfma_f32_16x16x32_bf16 v[176:179], v[140:143], v[76:79], v[176:179]\n"
    "s_waitcnt vmcnt(2)\n"
    "v_mov_b64 v[80:81], v[144:145]\n"
    "v_mov_b64 v[86:87], v[146:147]\n"
    "v_mov_b64 v[92:93], v[148:149]\n"
    "v_mov_b64 v[98:99], v[150:151]\n"
    "v_mov_b64 v[104:105], v[152:153]\n"
    "v_mov_b64 v[110:111], v[154:155]\n"
    "v_mov_b64 v[116:117], v[156:157]\n"
    "v_mov_b64 v[122:123], v[158:159]\n"
    "v_max3_f32 v230, v172, v173, v174\n"
    "v_max3_f32 v232, v176, v177, v178\n"
    "v_max3_f32 v230, v230, v175, v224\n"
    "v_max3_f32 v232, v232, v179, v226\n"
    "v_mov_b32 v238, v230\n"
    "v_mov_b32 v239, v232\n"
    "s_nop 0\n"
    "v_permlane16_swap_b32 v230, v238\n"
    "v_permlane16_swap_b32 v232, v239\n"
    "v_max_f32 v230, v230, v238\n"
    "v_max_f32 v232, v232, v239\n"
    "v_mul_f32 v230, 0xbe0293ee, v230\n"
    "v_mul_f32 v232, 0xbe0293ee, v232\n"
    "v_pk_fma_f32 v[172:173], v[172:173], s[18:19], v[230:231] op_sel_hi:[1,0,0]\n"
    "v_pk_fma_f32 v[174:175], v[174:175], s[18:19], v[230:231] op_sel_hi:[1,0,0]\n"
    "v_pk_fma_f32 v[176:177], v[176:177], s[18:19], v[232:233] op_sel_hi:[1,0,0]\n"
    "v_pk_fma_f32 v[178:179], v[178:179], s[18:19], v[232:233] op_sel_hi:[1,0,0]\n"
    "v_fma_f32 v224, v224, s18, v230\n"
    "v_fma_f32 v226, v226, s18, v232\n"
    "v_exp_f32 v172, v172\n"
    "v_exp_f32 v173, v173\n"
    "v_exp_f32 v174, v174\n"
    "v_exp_f32 v175, v175\n"
    "buffer_load_dwordx4 v[24:27], v7, s[36:39], s6 offen offset:2048 sc1\n"
    "v_exp_f32 v176, v176\n"
    "v_exp_f32 v177, v177\n"
    "v_exp_f32 v178, v178\n"
    "v_exp_f32 v179, v179\n"
    "buffer_load_dwordx4 v[28:31], v7, s[36:39], s6 offen offset:3072 sc1\n"
    "v_exp_f32 v224, v224\n"
    "v_exp_f32 v226, v226\n"
    "v_pk_add_f32 v[230:231], v[172:173], v[174:175]\n"
    "v_pk_add_f32 v[232:233], v[176:177], v[178:179]\n"
    "v_add_f32 v230, v230, v231\n"
    "v_add_f32 v232, v232, v233\n"
    "v_mov_b32 v238, v230\n"
    "v_mov_b32 v239, v232\n"
    "s_nop 0\n"
    "v_permlane16_swap_b32 v230, v238\n"
    "v_permlane16_swap_b32 v232, v239\n"
    "v_add_f32 v230, v230, v238\n"
    "v_add_f32 v232, v232, v239\n"
    "v_fmac_f32 v230, v228, v224\n"
    "v_fmac_f32 v232, v229, v226\n"
    "v_rcp_f32 v230, v230\n"
    "v_rcp_f32 v232, v232\n"
    "buffer_load_dwordx4 v[32:35], v7, s[36:39], s7 offen sc1\n"
    "v_mul_f32 v224, v224, v230\n"
    "v_mul_f32 v226, v226, v232\n"
    "v_pk_mul_f32 v[164:165], v[164:165], v[226:227] op_sel_hi:[1,0]\n"
    "v_pk_mul_f32 v[166:167], v[166:167], v[226:227] op_sel_hi:[1,0]\n"
    "v_pk_mul_f32 v[168:169], v[168:169], v[226:227] op_sel_hi:[1,0]\n"
    "v_pk_mul_f32 v[170:171], v[170:171], v[226:227] op_sel_hi:[1,0]\n"
    "v_pk_mul_f32 v[176:177], v[176:177], v[232:233] op_sel_hi:[1,0]\n"
    "v_pk_mul_f32 v[178:179], v[178:179], v[232:233] op_sel_hi:[1,0]\n"
    "v_pk_mul_f32 v[172:173], v[172:173], v[230:231] op_sel_hi:[1,0]\n"
    "v_pk_mul_f32 v[174:175], v[174:175], v[230:231] op_sel_hi:[1,0]\n"
    "v_pk_mul_f32 v[160:161], v[160:161], v[224:225] op_sel_hi:[1,0]\n"
    "v_pk_mul_f32 v[162:163], v[162:163], v[224:225] op_sel_hi:[1,0]\n"
    "v_cvt_pk_bf16_f32 v216, v164, v165\n"
    "v_cvt_pk_bf16_f32 v217, v166, v167\n"
    "v_cvt_pk_bf16_f32 v218, v168, v169\n"
    "v_cvt_pk_bf16_f32 v219, v170, v171\n"
    "v_cvt_pk_bf16_f32 v220, v176, v177\n"
    "v_cvt_pk_bf16_f32 v221, v178, v179\n"
    "v_cvt_pk_bf16_f32 v212, v172, v173\n"
    "v_cvt_pk_bf16_f32 v213, v174, v175\n"
    "v_cvt_pk_bf16_f32 v214, v160, v161\n"
    "v_cvt_pk_bf16_f32 v215, v162, v163\n"
    "v_mfma_f32_16x16x32_bf16 v[188:191], v[82:85], v[216:219], 0\n"
    "v_mfma_f32_16x16x32_bf16 v[192:195], v[88:91], v[216:219], 0\n"
    "v_mfma_f32_16x16x32_bf16 v[180:183], v[80:83], v[212:215], 0\n"
    "v_mfma_f32_16x16x32_bf16 v[184:187], v[86:89], v[212:215], 0\n"
    "v_mfma_f32_16x16x16_bf16 v[188:191], v[80:81], v[220:221], v[188:191]\n"
    "v_mfma_f32_16x16x16_bf16 v[192:195], v[86:87], v[220:221], v[192:195]\n"
    "v_mfma_f32_16x16x32_bf16 v[204:207], v[94:97], v[216:219], 0\n"
    "v_mfma_f32_16x16x32_bf16 v[208:211], v[100:103], v[216:219], 0\n"
    "v_cvt_pk_bf16_f32 v180, v180, v181\n"
    "v_cvt_pk_bf16_f32 v181, v182, v183\n"
    "v_mfma_f32_16x16x32_bf16 v[196:199], v[92:95], v[212:215], 0\n"
    "v_cvt_pk_bf16_f32 v182, v184, v185\n"
    "v_cvt_pk_bf16_f32 v183, v186, v187\n"
    "v_mfma_f32_16x16x32_bf16 v[200:203], v[98:101], v[212:215], 0\n"
    "buffer_store_dwordx4 v[180:183], v0, s[36:39], s75 offen sc1\n"
    "v_cvt_pk_bf16_f32 v188, v188, v189\n"
    "v_mfma_f32_16x16x16_bf16 v[204:207], v[92:93], v[220:221], v[204:207]\n"
    "v_cvt_pk_bf16_f32 v189, v190, v191\n"
    "v_cvt_pk_bf16_f32 v190, v192, v193\n"
    "v_mfma_f32_16x16x16_bf16 v[208:211], v[98:99], v[220:221], v[208:211]\n"
    "v_cvt_pk_bf16_f32 v191, v194, v195\n"
    "buffer_store_dwordx4 v[188:191], v0, s[36:39], s75 offen offset:1024 sc1\n"
    "s_nop 1\n"
    "v_mfma_f32_16x16x32_bf16 v[188:191], v[106:109], v[216:219], 0\n"
    "v_mfma_f32_16x16x32_bf16 v[192:195], v[112:115], v[216:219], 0\n"
    "v_cvt_pk_bf16_f32 v196, v196, v197\n"
    "v_cvt_pk_bf16_f32 v197, v198, v199\n"
    "v_mfma_f32_16x16x32_bf16 v[180:183], v[104:107], v[212:215], 0\n"
    "v_cvt_pk_bf16_f32 v198, v200, v201\n"
    "v_cvt_pk_bf16_f32 v199, v202, v203\n"
    "v_mfma_f32_16x16x32_bf16 v[184:187], v[110:113], v[212:215], 0\n"
    "buffer_store_dwordx4 v[196:199], v0, s[36:39], s75 offen offset:2048 sc1\n"
    "v_cvt_pk_bf16_f32 v204, v204, v205\n"
    "v_mfma_f32_16x16x16_bf16 v[188:191], v[104:105], v[220:221], v[188:191]\n"
    "v_cvt_pk_bf16_f32 v205, v206, v207\n"
    "v_cvt_pk_bf16_f32 v206, v208, v209\n"
    "v_mfma_f32_16x16x16_bf16 v[192:195], v[110:111], v[220:221], v[192:195]\n"
    "v_cvt_pk_bf16_f32 v207, v210, v211\n"
    "buffer_store_dwordx4 v[204:207], v0, s[36:39], s75 offen offset:3072 sc1\n"
    "s_nop 1\n"
    "v_mfma_f32_16x16x32_bf16 v[204:207], v[118:121], v[216:219], 0\n"
    "v_mfma_f32_16x16x32_bf16 v[208:211], v[124:127], v[216:219], 0\n"
    "v_cvt_pk_bf16_f32 v180, v180, v181\n"
    "v_cvt_pk_bf16_f32 v181, v182, v183\n"
    "v_mfma_f32_16x16x32_bf16 v[196:199], v[116:119], v[212:215], 0\n"
    "v_cvt_pk_bf16_f32 v182, v184, v185\n"
    "v_cvt_pk_bf16_f32 v183, v186, v187\n"
    "v_mfma_f32_16x16x32_bf16 v[200:203], v[122:125], v[212:215], 0\n"
    "buffer_store_dwordx4 v[180:183], v0, s[36:39], s76 offen sc1\n"
    "v_cvt_pk_bf16_f32 v188, v188, v189\n"
    "v_mfma_f32_16x16x16_bf16 v[204:207], v[116:117], v[220:221], v[204:207]\n"
    "v_cvt_pk_bf16_f32 v189, v190, v191\n"
    "v_cvt_pk_bf16_f32 v190, v192, v193\n"
    "v_mfma_f32_16x16x16_bf16 v[208:211], v[122:123], v[220:221], v[208:211]\n"
    "v_cvt_pk_bf16_f32 v191, v194, v195\n"
    "buffer_store_dwordx4 v[188:191], v0, s[36:39], s76 offen offset:1024 sc1\n"
    "v_cvt_pk_bf16_f32 v196, v196, v197\n"
    "v_cvt_pk_bf16_f32 v197, v198, v199\n"
    "v_cvt_pk_bf16_f32 v198, v200, v201\n"
    "v_cvt_pk_bf16_f32 v199, v202, v203\n"
    "buffer_store_dwordx4 v[196:199], v0, s[36:39], s76 offen offset:2048 sc1\n"
    "v_cvt_pk_bf16_f32 v204, v204, v205\n"
    "v_cvt_pk_bf16_f32 v205, v206, v207\n"
    "v_cvt_pk_bf16_f32 v206, v208, v209\n"
    "v_cvt_pk_bf16_f32 v207, v210, v211\n"
    "buffer_store_dwordx4 v[204:207], v0, s[36:39], s76 offen offset:3072 sc1\n"
    "s_nop 13\n"
    "buffer_load_dwordx4 v[36:39], v7, s[36:39], s7 offen offset:1024 sc1\n"
    "s_nop 13\n"
    "buffer_load_dwordx4 v[40:43], v7, s[36:39], s7 offen offset:2048 sc1\n"
    "s_nop 13\n"
    "buffer_load_dwordx4 v[44:47], v7, s[36:39], s7 offen offset:3072 sc1\n"
    "s_nop 13\n"
    "buffer_load_dwordx4 v[48:51], v7, s[36:39], s4 offen sc1\n"
    "s_nop 13\n"
    "buffer_load_dwordx4 v[52:55], v7, s[36:39], s4 offen offset:1024 sc1\n"
    "v_mov_b32 v9, s68\n"
    "s_waitcnt vmcnt(5)\n"
    "s_mov_b64 exec, 15\n"
    "buffer_atomic_smax v9, v2, s[36:39], 0 offen sc1\n"
    "s_mov_b64 exec, -1\n"
    "buffer_load_dwordx4 v[56:59], v7, s[36:39], s4 offen offset:2048 sc1\n"
    "buffer_load_dwordx4 v[60:63], v7, s[36:39], s4 offen offset:3072 sc1\n"
    "buffer_load_dwordx4 v[64:67], v7, s[36:39], s5 offen sc1\n"
    "buffer_load_dwordx4 v[68:71], v7, s[36:39], s5 offen offset:1024 sc1\n"
    "buffer_load_dwordx4 v[72:75], v7, s[36:39], s5 offen offset:2048 sc1\n"
    "buffer_load_dwordx4 v[76:79], v7, s[36:39], s5 offen offset:3072 sc1\n"
    "buffer_load_dwordx4 v[82:85], v7, s[36:39], s8 offen sc1\n"
    "buffer_load_dwordx4 v[88:91], v7, s[36:39], s8 offen offset:1024 sc1\n"
    "buffer_load_dwordx4 v[94:97], v7, s[36:39], s8 offen offset:2048 sc1\n"
    "buffer_load_dwordx4 v[100:103], v7, s[36:39], s8 offen offset:3072 sc1\n"
    "buffer_load_dwordx4 v[106:109], v7, s[36:39], s9 offen sc1\n"
    "buffer_load_dwordx4 v[112:115], v7, s[36:39], s9 offen offset:1024 sc1\n"
    "buffer_load_dwordx4 v[118:121], v7, s[36:39], s9 offen offset:2048 sc1\n"
    "buffer_load_dwordx4 v[124:127], v7, s[36:39], s9 offen offset:3072 sc1\n"
    "s_add_u32 s68, s68, 1\n"
    "s_cmp_lg_u32 s70, 0\n"
    "s_cbranch_scc1 .Latt_top_%=\n"
    ".Latt_LOOPE_%=:\n"
    "s_cmp_lg_u32 s68, 64\n"
    "s_cbranch_scc1 .Latt_waitp_%=\n"
    "s_waitcnt vmcnt(0)\n"
    "s_branch .Latt_FIN_%=\n"
    ".Latt_waitp_%=:\n"
    "s_sub_u32 s72, s68, 1\n"
    "v_add_u32 v11, v8, v5\n"
    "v_cmp_gt_i32 vcc, s72, v11\n"
    "s_nop 0\n"
    "s_or_b64 exec, vcc, 1\n"
    "buffer_atomic_smax v8, v4, s[36:39], 0 offen sc0 sc1\n"
    "s_mov_b64 exec, -1\n"
    "s_waitcnt vmcnt(0)\n"
    "v_add_u32 v11, v8, v5\n"
    "v_cmp_gt_i32 vcc, s72, v11\n"
    "s_cbranch_vccz .Latt_gop_%=\n"
    "s_sleep 3\n"
    "s_sub_u32 s69, s69, 1\n"
    "s_cbranch_scc0 .Latt_waitp_%=\n"
    "s_mov_b32 s69, 0\n"
    ".Latt_gop_%=:\n"
    "s_and_b32 s4, s68, 7\n"
    "s_mul_i32 s4, s4, 4194304\n"
    "s_add_u32 s4, s4, s52\n"
    "s_add_u32 s5, s4, 4096\n"
    "s_add_u32 s6, s4, 16384\n"
    "s_add_u32 s7, s4, 20480\n"
    "s_add_u32 s8, s4, 32768\n"
    "s_add_u32 s9, s4, 36864\n"
    "buffer_load_dwordx4 v[16:19], v0, s[36:39], s6 offen sc1\n"
    "buffer_load_dwordx4 v[20:23], v0, s[36:39], s6 offen offset:1024 sc1\n"
    "buffer_load_dwordx4 v[24:27], v0, s[36:39], s6 offen offset:2048 sc1\n"
    "buffer_load_dwordx4 v[28:31], v0, s[36:39], s6 offen offset:3072 sc1\n"
    "buffer_load_dwordx4 v[32:35], v0, s[36:39], s7 offen sc1\n"
    "buffer_load_dwordx4 v[36:39], v0, s[36:39], s7 offen offset:1024 sc1\n"
    "buffer_load_dwordx4 v[40:43], v0, s[36:39], s7 offen offset:2048 sc1\n"
    "buffer_load_dwordx4 v[44:47], v0, s[36:39], s7 offen offset:3072 sc1\n"
    "buffer_load_dwordx4 v[48:51], v0, s[36:39], s4 offen sc1\n"
    "buffer_load_dwordx4 v[52:55], v0, s[36:39], s4 offen offset:1024 sc1\n"
    "buffer_load_dwordx4 v[56:59], v0, s[36:39], s4 offen offset:2048 sc1\n"
    "buffer_load_dwordx4 v[60:63], v0, s[36:39], s4 offen offset:3072 sc1\n"
    "buffer_load_dwordx4 v[64:67], v0, s[36:39], s5 offen sc1\n"
    "buffer_load_dwordx4 v[68:71], v0, s[36:39], s5 offen offset:1024 sc1\n"
    "buffer_load_dwordx4 v[72:75], v0, s[36:39], s5 offen offset:2048 sc1\n"
    "buffer_load_dwordx4 v[76:79], v0, s[36:39], s5 offen offset:3072 sc1\n"
    "buffer_load_dwordx4 v[82:85], v0, s[36:39], s8 offen sc1\n"
    "buffer_load_dwordx4 v[88:91], v0, s[36:39], s8 offen offset:1024 sc1\n"
    "buffer_load_dwordx4 v[94:97], v0, s[36:39], s8 offen offset:2048 sc1\n"
    "buffer_load_dwordx4 v[100:103], v0, s[36:39], s8 offen offset:3072 sc1\n"
    "buffer_load_dwordx4 v[106:109], v0, s[36:39], s9 offen sc1\n"
    "buffer_load_dwordx4 v[112:115], v0, s[36:39], s9 offen offset:1024 sc1\n"
    "buffer_load_dwordx4 v[118:121], v0, s[36:39], s9 offen offset:2048 sc1\n"
    "buffer_load_dwordx4 v[124:127], v0, s[36:39], s9 offen offset:3072 sc1\n"
    "s_waitcnt vmcnt(8)\n"
    "s_branch .Latt_top_%=\n"
    ".Latt_waitr_%=:\n"
    "s_or_b64 exec, vcc, 1\n"
    "buffer_atomic_smax v8, v4, s[36:39], 0 offen sc0 sc1\n"
    "s_mov_b64 exec, -1\n"
    "s_sleep 3\n"
    "s_waitcnt vmcnt(1)\n"
    "v_add_u32 v11, v12, v5\n"
    "v_cmp_gt_i32 vcc, s68, v11\n"
    "s_nop 0\n"
    "s_and_b32 s71, vcc_lo, 0xff\n"
    "s_cbranch_scc0 .Latt_gor_%=\n"
    "s_or_b64 exec, vcc, 1\n"
    "buffer_atomic_smax v12, v4, s[36:39], 0 offen sc0 sc1\n"
    "s_mov_b64 exec, -1\n"
    "s_sleep 3\n"
    "s_waitcnt vmcnt(1)\n"
    "v_add_u32 v11, v8, v5\n"
    "v_cmp_gt_i32 vcc, s68, v11\n"
    "s_nop 0\n"
    "s_and_b32 s71, vcc_lo, 0xff\n"
    "s_cbranch_scc0 .Latt_gor_%=\n"
    "s_sub_u32 s69, s69, 1\n"
    "s_cbranch_scc0 .Latt_waitr_%=\n"
    ".Latt_deadr_%=:\n"
    "s_mov_b32 s69, 0\n"
    "s_branch .Latt_gor_%=\n"
    ".Latt_FIN_%=:\n"
  );
}

template <int MODE>
DEVI void red_args(const Params& p, int L, int q, u32 dbg, u64 tag, v16u& sa, v16u& sb, v16u& va, v16u& vb) {
  const int tid = threadIdx.x, lane = tid & 63, w = (int)rfl(tid >> 6);
  const int c = lane & 15, g = lane >> 4;
  const bool rowres = MODE == 0 || (MODE == 1 && L == 0);
  const bool fin = MODE == 2 && L == 1;
  const v4u ws = make_srd(p.ws, WS_END), rx = make_srd(p.x, 2048 * 1152), ro = make_srd(p.out, 2048 * 1152);
  u32 ft = OOB;
  if (lane < 32) {
    if (MODE == 1) { if (lane < 24) ft = mbox(L, lane / 6, SL_UP + lane % 6, 4 * q + w); }
    else if (!fin) ft = mbox(MODE == 0 ? 0 : 1, lane >> 3, SL_P + (lane & 7), (MODE == 0 ? 16 * L : 0) + 4 * q + w);
  } else if (MODE != 0) {
    if (lane < 44) ft = mbox(L, (lane - 32) / 3, (MODE == 1 ? SL_WO : SL_DOWN) + (lane - 32) % 3, (MODE == 1 ? 8 : 24) + 4 * q + w);
    else if (lane == 44 && !rowres) ft = MODE == 1 ? mbox(0, q, SL_REDF, 48 + w) : mbox(L, q, SL_REDA, 48 + w);
  }
  const unsigned long long fm = __ballot(ft != OOB);
  sa = cat4(ws, rowres ? rx : ws, fin ? ro : ws, (v4u){(u32)(2 * tag), (u32)((2 * tag) >> 32), 0u, 0u});
  sb = (v16u){pgb(L, MODE == 1 ? B_PA : B_PF),
              rowres ? 0u : (MODE == 1 ? pgb(1, B_X) : pgb(L, B_X1)), rowres ? 36864u : SLOT, (u32)(rowres ? 63 : (MODE == 1 ? RX2 - 1 : RX - 1)), rowres ? 128u : 1024u,
              MODE == 1 ? pgb(L, B_X1) : pgb(1, B_X), (u32)((MODE == 0 || fin) ? 63 : (MODE == 1 ? RX - 1 : RX2 - 1)),
              fin ? 0u : (MODE == 0 ? OFF_XN0 : (MODE == 1 ? pgb(L, B_XN2) : pgb(1, B_XN))), 4096u,
              rowres ? 0u : ~0u, dbg + w * 64, fin ? 0u : ~0u, (u32)fm, (u32)(fm >> 32), (u32)(L == 0 ? 0 : (L == 1 ? LN_B : LN_C)), (u32)(L == 0 ? LN_B - 1 : (L == 1 ? LN_C - 1 : 63))};
  const int need = w == 0 ? 0x10F : (w == 1 ? 0x23F : (w == 2 ? 0x4F0 : 0xF00));
  const bool in = MODE != 0 && lane < 48 && ((need >> (lane % 12)) & 1);
  const bool back = MODE != 0 && lane >= 48 && lane < 52 && !fin;
  auto frag = [&](int I) { return kb_off(2 * (I >> 1)) + (u32)((I & 1) * 512 + (q >> 1) * 1024 + (c >> 3) * 256 + (8 * (q & 1) + (c & 7)) * 16); };
  auto rowm = [&](int I) { return (u32)((8 * q + (c & 7)) * 1152 + (2 * I + (c >> 3)) * 16); };
  const int I0 = 8 * w + 2 * (g & 1) + (g >> 1);
  const u32 pv = (u32)(q * CHUNK + w * 2048 + lane * 16), pv8 = (u32)(4 * CHUNK + (4 * q + w) * 256 + lane * 16);
  va[0] = mbox(L, q, MODE == 1 ? SL_REDA : SL_REDF, lane);
  va[1] = (u32)(in ? 0 : (back ? (MODE == 1 ? RX : RX2) : 1000));
  va[2] = (u32)g * 5 * CHUNK + frag(8 * w);
  va[3] = (u32)g * 5 * CHUNK + frag(32 + w);
  va[4] = fin ? (u32)(q * 9216 + tid * 16) : frag(I0);
  va[5] = fin ? (w == 0 ? (u32)(q * 9216 + 8192 + tid * 16) : OOB) : (g == 0 ? frag(32 + w) : OOB);
  va[6] = rowres ? rowm(I0) : pv;
  va[7] = g == 0 ? (rowres ? rowm(32 + w) : pv8) : OOB;
  va[8] = pv;
  va[9] = g == 0 ? pv8 : OOB;
  va[10] = fin ? rowm(I0) - q * 9216 : (u32)(((c & 7) * 4 + w) * 8);
  va[11] = fin ? (u32)tid * 16 : (u32)((c & 7) * 32);
  va[12] = fin ? (g == 0 ? rowm(32 + w) - q * 9216 : (u32)(12288 + tid * 16)) : (g < 2 ? 0x3F800000u : 0u);
  va[13] = ft; va[14] = 0; va[15] = 0;
  const u32 one = 0x3F80u << (16 * (c & 1));
#pragma unroll
  for (int d = 0; d < 4; ++d) {
    const bool on = ((c & 7) >> 1) == d;
    vb[d] = (on && c < 8) ? one : 0u;
    vb[4 + d] = (on && c >= 8) ? one : 0u;
    vb[8 + d] = (on && g == 2 * (c >> 3)) ? one : 0u;
    vb[12 + d] = (on && g == 2 * (c >> 3) + 1) ? one : 0u;
  }
#pragma unroll
  for (int i = 0; i < 16; ++i) { sa[i] = rfl(sa[i]); sb[i] = rfl(sb[i]); }
}
#define RUN_RED(TEXT) asm volatile(TEXT : "+{s[36:51]}"(sa), "+{s[52:67]}"(sb), "+{v[0:15]}"(va), "+{v[16:31]}"(vb) :: CLOB_RED)
#define CLOB_RED \
  "v32", "v33", "v34", "v35", "v36", "v37", "v38", "v39", "v40", "v41", "v42", "v43", "v44", "v45", "v46", "v47", "v48", "v49", "v50", "v51", "v52", "v53", "v54", "v55", \
  "v56", "v57", "v58", "v59", "v60", "v61", "v62", "v63", "v64", "v65", "v66", "v67", "v68", "v69", "v70", "v71", "v72", "v73", "v74", "v75", "v76", "v77", "v78", "v79", \
  "v80", "v81", "v82", "v83", "v84", "v85", "v86", "v87", "v88", "v89", "v90", "v91", "v92", "v93", "v94", "v95", "v96", "v97", "v98", "v99", "v100", "v101", "v102", "v103", \
  "v104", "v105", "v106", "v107", "v108", "v109", "v110", "v111", "v112", "v113", "v114", "v115", "v116", "v117", "v118", "v119", "v120", "v121", "v122", "v123", "v124", "v125", "v126", "v127", \
  "v128", "v129", "v130", "v131", "v132", "v133", "v134", "v135", "s68", "s69", "s70", "s71", "s72", "s73", "s74", "s75", "s76", "s77", "s78", "s79", "s80", "s81", "s82", "s83", \
  "s84", "s85", "s86", "s87", "s88", "s89", "s90", "s91", "s92", "s93", "s94", "s95", "s96", "s97", "s98", "s99", "vcc", "scc", "memory"
DEVI void role_ln0(const Params& p, int grp, int q, u32 dbg, u64 tag) {
  v16u sa, sb, va, vb;
  red_args<0>(p, grp, q, dbg, tag, sa, sb, va, vb);
  RUN_RED(
    "s_nop 4\n"
    "s_getpc_b64 s[84:85]\n"
    ".LrLN0_PC_%=:\n"
    "s_and_b32 s85, s85, 0xffff\n"
    "s_mov_b32 s86, .LrLN0_FIN_%=-.LrLN0_PC_%=\n"
    "s_mov_b32 s87, 0x20000\n"
    "v_mbcnt_lo_u32_b32 v134, -1, 0\n"
    "v_mbcnt_hi_u32_b32 v134, -1, v134\n"
    "v_lshlrev_b32 v134, 7, v134\n"
    "buffer_load_dword v135, v134, s[84:87], 0 offen\n"
    "v_add_u32 v134, 8192, v134\n"
    "buffer_load_dword v135, v134, s[84:87], 0 offen\n"
    "s_mov_b32 s69, 0\n"
    "v_mov_b32 v122, -1\n"
    "v_mov_b32 v123, -1\n"
    "s_mov_b32 s70, 262144\n"
    "s_add_u32 s69, s66, 0\n"
    "s_and_b32 s73, s69, s55\n"
    "s_mul_i32 s73, s73, s54\n"
    "s_add_u32 s73, s73, s53\n"
    "s_add_u32 s74, s73, s56\n"
    "buffer_load_dwordx4 v[40:43], v6, s[40:43], s73 offen sc1\n"
    "buffer_load_dwordx4 v[44:47], v6, s[40:43], s74 offen sc1\n"
    "buffer_load_dwordx4 v[48:51], v7, s[40:43], s73 offen sc1\n"
    "s_add_u32 s69, s66, 1\n"
    "s_and_b32 s73, s69, s55\n"
    "s_mul_i32 s73, s73, s54\n"
    "s_add_u32 s73, s73, s53\n"
    "s_add_u32 s74, s73, s56\n"
    "buffer_load_dwordx4 v[52:55], v6, s[40:43], s73 offen sc1\n"
    "buffer_load_dwordx4 v[56:59], v6, s[40:43], s74 offen sc1\n"
    "buffer_load_dwordx4 v[60:63], v7, s[40:43], s73 offen sc1\n"
    "s_add_u32 s69, s66, 2\n"
    "s_and_b32 s73, s69, s55\n"
    "s_mul_i32 s73, s73, s54\n"
    "s_add_u32 s73, s73, s53\n"
    "s_add_u32 s74, s73, s56\n"
    "buffer_load_dwordx4 v[68:71], v6, s[40:43], s73 offen sc1\n"
    "buffer_load_dwordx4 v[72:75], v6, s[40:43], s74 offen sc1\n"
    "buffer_load_dwordx4 v[76:79], v7, s[40:43], s73 offen sc1\n"
    "s_add_u32 s69, s66, 3\n"
    "s_and_b32 s73, s69, s55\n"
    "s_mul_i32 s73, s73, s54\n"
    "s_add_u32 s73, s73, s53\n"
    "s_add_u32 s74, s73, s56\n"
    "buffer_load_dwordx4 v[124:127], v6, s[40:43], s73 offen sc1\n"
    "buffer_load_dwordx4 v[128:131], v6, s[40:43], s74 offen sc1\n"
    "buffer_load_dwordx4 v[132:135], v7, s[40:43], s73 offen sc1\n"
    "s_sub_u32 s68, s66, 1\n"
    ".LrLN0_par_%=:\n"
    "buffer_load_dwordx2 v[14:15], off, s[36:39], 0 sc1\n"
    "s_waitcnt vmcnt(0)\n"
    "v_readfirstlane_b32 s75, v14\n"
    "v_readfirstlane_b32 s76, v15\n"
    "s_xor_b32 s75, s75, s48\n"
    "s_xor_b32 s76, s76, s49\n"
    "s_cselect_b32 s76, 2, 0\n"
    "s_or_b32 s76, s76, s75\n"
    "s_cmp_lt_u32 s76, 2\n"
    "s_cbranch_scc1 .LrLN0_par1_%=\n"
    "s_sleep 4\n"
    "s_sub_u32 s70, s70, 1\n"
    "s_cbranch_scc0 .LrLN0_par_%=\n"
    "s_mov_b32 s70, 0\n"
    ".LrLN0_par1_%=:\n"
    "s_and_b32 s75, s75, 1\n"
    "s_lshl_b32 s75, s75, 13\n"
    "s_add_u32 s36, s36, s75\n"
    "s_addc_u32 s37, s37, 0\n"
    "s_and_b32 s76, s75, s61\n"
    "s_add_u32 s40, s40, s76\n"
    "s_addc_u32 s41, s41, 0\n"
    "s_and_b32 s76, s75, s63\n"
    "s_add_u32 s44, s44, s76\n"
    "s_addc_u32 s45, s45, 0\n"
    ".LrLN0_LOOPB_%=:\n"
    ".LrLN0_top_%=:\n"
    "s_add_u32 s68, s68, 1\n"
    "s_add_u32 s69, s68, 4\n"
    "s_and_b32 s75, s68, s58\n"
    "s_mul_i32 s77, s75, 81920\n"
    "s_add_u32 s77, s77, s59\n"
    "s_add_u32 s78, s77, s60\n"
    "s_and_b32 s73, s69, s55\n"
    "s_mul_i32 s73, s73, s54\n"
    "s_add_u32 s73, s73, s53\n"
    "s_add_u32 s74, s73, s56\n"
    "s_waitcnt vmcnt(27)\n"
    "v_mfma_f32_16x16x32_bf16 v[80:83], v[24:27], v[40:43], 0\n"
    "v_mfma_f32_16x16x32_bf16 v[84:87], v[28:31], v[40:43], 0\n"
    "s_waitcnt vmcnt(26)\n"
    "v_mfma_f32_16x16x32_bf16 v[88:91], v[24:27], v[44:47], 0\n"
    "v_mfma_f32_16x16x32_bf16 v[92:95], v[28:31], v[44:47], 0\n"
    "s_waitcnt vmcnt(25)\n"
    "v_mfma_f32_16x16x32_bf16 v[96:99], v[24:27], v[48:51], 0\n"
    "buffer_load_dwordx4 v[40:43], v6, s[40:43], s73 offen sc1\n"
    "buffer_load_dwordx4 v[44:47], v6, s[40:43], s74 offen sc1\n"
    "buffer_load_dwordx4 v[48:51], v7, s[40:43], s73 offen sc1\n"
    "v_pk_add_f32 v[112:113], v[80:81], v[82:83]\n"
    "v_pk_add_f32 v[114:115], v[84:85], v[86:87]\n"
    "v_pk_add_f32 v[112:113], v[112:113], v[88:89]\n"
    "v_pk_add_f32 v[114:115], v[114:115], v[90:91]\n"
    "v_pk_add_f32 v[112:113], v[112:113], v[92:93]\n"
    "v_pk_add_f32 v[114:115], v[114:115], v[94:95]\n"
    "v_pk_add_f32 v[112:113], v[112:113], v[96:97]\n"
    "v_pk_add_f32 v[114:115], v[114:115], v[98:99]\n"
    "v_pk_add_f32 v[112:113], v[112:113], v[114:115]\n"
    "v_add_f32 v112, v112, v113\n"
    "s_nop 1\n"
    "v_add_f32_dpp v14, v112, v112 row_ror:8 row_mask:0xf bank_mask:0xf\n"
    "v_add_f32_dpp v15, v112, v112 row_ror:8 row_mask:0xf bank_mask:0xf\n"
    "s_nop 1\n"
    "v_permlane16_swap_b32 v14, v15\n"
    "v_add_f32 v112, v14, v15\n"
    "v_add_f32 v14, v14, v15\n"
    "s_nop 1\n"
    "v_permlane32_swap_b32 v112, v14\n"
    "v_add_f32 v112, v112, v14\n"
    "v_mul_f32 v112, 0x3be38e39, v112\n"
    "v_mul_f32 v114, v112, v12\n"
    "v_pk_add_f32 v[80:81], v[80:81], v[112:113] op_sel_hi:[1,0] neg_lo:[0,1] neg_hi:[0,1]\n"
    "v_pk_add_f32 v[82:83], v[82:83], v[112:113] op_sel_hi:[1,0] neg_lo:[0,1] neg_hi:[0,1]\n"
    "v_pk_add_f32 v[84:85], v[84:85], v[112:113] op_sel_hi:[1,0] neg_lo:[0,1] neg_hi:[0,1]\n"
    "v_pk_add_f32 v[86:87], v[86:87], v[112:113] op_sel_hi:[1,0] neg_lo:[0,1] neg_hi:[0,1]\n"
    "v_pk_add_f32 v[88:89], v[88:89], v[112:113] op_sel_hi:[1,0] neg_lo:[0,1] neg_hi:[0,1]\n"
    "v_pk_add_f32 v[90:91], v[90:91], v[112:113] op_sel_hi:[1,0] neg_lo:[0,1] neg_hi:[0,1]\n"
    "v_pk_add_f32 v[92:93], v[92:93], v[112:113] op_sel_hi:[1,0] neg_lo:[0,1] neg_hi:[0,1]\n"
    "v_pk_add_f32 v[94:95], v[94:95], v[112:113] op_sel_hi:[1,0] neg_lo:[0,1] neg_hi:[0,1]\n"
    "v_pk_add_f32 v[96:97], v[96:97], v[114:115] op_sel_hi:[1,0] neg_lo:[0,1] neg_hi:[0,1]\n"
    "v_pk_add_f32 v[98:99], v[98:99], v[114:115] op_sel_hi:[1,0] neg_lo:[0,1] neg_hi:[0,1]\n"
    "v_pk_mul_f32 v[116:117], v[80:81], v[80:81]\n"
    "v_pk_mul_f32 v[118:119], v[82:83], v[82:83]\n"
    "v_pk_fma_f32 v[116:117], v[84:85], v[84:85], v[116:117]\n"
    "v_pk_fma_f32 v[118:119], v[86:87], v[86:87], v[118:119]\n"
    "v_pk_fma_f32 v[116:117], v[88:89], v[88:89], v[116:117]\n"
    "v_pk_fma_f32 v[118:119], v[90:91], v[90:91], v[118:119]\n"
    "v_pk_fma_f32 v[116:117], v[92:93], v[92:93], v[116:117]\n"
    "v_pk_fma_f32 v[118:119], v[94:95], v[94:95], v[118:119]\n"
    "v_pk_fma_f32 v[116:117], v[96:97], v[96:97], v[116:117]\n"
    "v_pk_fma_f32 v[118:119], v[98:99], v[98:99], v[118:119]\n"
    "v_pk_add_f32 v[116:117], v[116:117], v[118:119]\n"
    "v_add_f32 v113, v116, v117\n"
    "s_nop 1\n"
    "v_add_f32_dpp v14, v113, v113 row_ror:8 row_mask:0xf bank_mask:0xf\n"
    "v_add_f32_dpp v15, v113, v113 row_ror:8 row_mask:0xf bank_mask:0xf\n"
    "s_nop 1\n"
    "v_permlane16_swap_b32 v14, v15\n"
    "v_add_f32 v113, v14, v15\n"
    "v_add_f32 v14, v14, v15\n"
    "s_nop 1\n"
    "v_permlane32_swap_b32 v113, v14\n"
    "v_add_f32 v113, v113, v14\n"
    "ds_write_b64 v10, v[112:113]\n"
    "s_waitcnt vmcnt(17)\n"
    "s_add_u32 s75, s66, 1\n"
    "s_min_i32 s75, s75, s68\n"
    "s_sub_u32 s76, s68, 2\n"
    "s_max_i32 s75, s75, s76\n"
    "s_sub_u32 s75, s75, 1\n"
    "v_mov_b32 v14, s75\n"
    "s_mov_b64 exec, s[64:65]\n"
    "buffer_atomic_smax v14, v13, s[36:39], 0 offen sc1\n"
    "s_mov_b64 exec, -1\n"
    "s_waitcnt lgkmcnt(0)\n"
    "s_barrier\n"
    "ds_read_b128 v[32:35], v11\n"
    "ds_read_b128 v[36:39], v11 offset:16\n"
    "v_xor_b32 v10, 256, v10\n"
    "v_xor_b32 v11, 256, v11\n"
    "s_waitcnt lgkmcnt(0)\n"
    "v_pk_add_f32 v[114:115], v[32:33], v[34:35]\n"
    "v_pk_add_f32 v[116:117], v[36:37], v[38:39]\n"
    "v_pk_add_f32 v[114:115], v[114:115], v[116:117]\n"
    "v_fmac_f32 v32, 0xbe800000, v114\n"
    "v_fmac_f32 v34, 0xbe800000, v114\n"
    "v_fmac_f32 v36, 0xbe800000, v114\n"
    "v_fmac_f32 v38, 0xbe800000, v114\n"
    "v_fmac_f32 v112, 0xbe800000, v114\n"
    "v_mul_f32 v116, v32, v32\n"
    "v_fmac_f32 v116, v34, v34\n"
    "v_fmac_f32 v116, v36, v36\n"
    "v_fmac_f32 v116, v38, v38\n"
    "v_fmac_f32 v115, 0x43100000, v116\n"
    "v_mov_b32 v116, 0x3727c5ac\n"
    "v_fmac_f32 v116, 0x3ae38e39, v115\n"
    "v_rsq_f32 v116, v116\n"
    "s_nop 0\n"
    "v_mul_f32 v114, v112, v116\n"
    "v_pk_fma_f32 v[80:81], v[80:81], v[116:117], v[114:115] op_sel_hi:[1,0,0]\n"
    "v_pk_fma_f32 v[82:83], v[82:83], v[116:117], v[114:115] op_sel_hi:[1,0,0]\n"
    "v_pk_fma_f32 v[84:85], v[84:85], v[116:117], v[114:115] op_sel_hi:[1,0,0]\n"
    "v_pk_fma_f32 v[86:87], v[86:87], v[116:117], v[114:115] op_sel_hi:[1,0,0]\n"
    "v_pk_fma_f32 v[88:89], v[88:89], v[116:117], v[114:115] op_sel_hi:[1,0,0]\n"
    "v_pk_fma_f32 v[90:91], v[90:91], v[116:117], v[114:115] op_sel_hi:[1,0,0]\n"
    "v_pk_fma_f32 v[92:93], v[92:93], v[116:117], v[114:115] op_sel_hi:[1,0,0]\n"
    "v_pk_fma_f32 v[94:95], v[94:95], v[116:117], v[114:115] op_sel_hi:[1,0,0]\n"
    "v_pk_fma_f32 v[96:97], v[96:97], v[116:117], v[114:115] op_sel_hi:[1,0,0]\n"
    "v_pk_fma_f32 v[98:99], v[98:99], v[116:117], v[114:115] op_sel_hi:[1,0,0]\n"
    "v_cvt_pk_bf16_f32 v100, v80, v81\n"
    "v_cvt_pk_bf16_f32 v101, v82, v83\n"
    "v_cvt_pk_bf16_f32 v102, v84, v85\n"
    "v_cvt_pk_bf16_f32 v103, v86, v87\n"
    "v_cvt_pk_bf16_f32 v104, v88, v89\n"
    "v_cvt_pk_bf16_f32 v105, v90, v91\n"
    "v_cvt_pk_bf16_f32 v106, v92, v93\n"
    "v_cvt_pk_bf16_f32 v107, v94, v95\n"
    "v_cvt_pk_bf16_f32 v108, v96, v97\n"
    "v_cvt_pk_bf16_f32 v109, v98, v99\n"
    "v_permlane16_swap_b32 v100, v102\n"
    "v_permlane16_swap_b32 v101, v103\n"
    "buffer_store_dwordx4 v[100:103], v4, s[44:47], s77 offen sc1\n"
    "v_permlane16_swap_b32 v104, v106\n"
    "v_permlane16_swap_b32 v105, v107\n"
    "buffer_store_dwordx4 v[104:107], v4, s[44:47], s78 offen sc1\n"
    "v_permlane16_swap_b32 v108, v110\n"
    "v_permlane16_swap_b32 v109, v111\n"
    "buffer_store_dwordx4 v[108:111], v5, s[44:47], s77 offen sc1\n"
    "s_cmp_eq_u32 s68, s66\n"
    "s_cbranch_scc1 .LrLN0_first_%=\n"
    ".LrLN0_first1_%=:\n"
    "s_add_u32 s68, s68, 1\n"
    "s_add_u32 s69, s68, 4\n"
    "s_and_b32 s75, s68, s58\n"
    "s_mul_i32 s77, s75, 81920\n"
    "s_add_u32 s77, s77, s59\n"
    "s_add_u32 s78, s77, s60\n"
    "s_and_b32 s73, s69, s55\n"
    "s_mul_i32 s73, s73, s54\n"
    "s_add_u32 s73, s73, s53\n"
    "s_add_u32 s74, s73, s56\n"
    "s_waitcnt vmcnt(27)\n"
    "v_mfma_f32_16x16x32_bf16 v[80:83], v[24:27], v[52:55], 0\n"
    "v_mfma_f32_16x16x32_bf16 v[84:87], v[28:31], v[52:55], 0\n"
    "s_waitcnt vmcnt(26)\n"
    "v_mfma_f32_16x16x32_bf16 v[88:91], v[24:27], v[56:59], 0\n"
    "v_mfma_f32_16x16x32_bf16 v[92:95], v[28:31], v[56:59], 0\n"
    "s_waitcnt vmcnt(25)\n"
    "v_mfma_f32_16x16x32_bf16 v[96:99], v[24:27], v[60:63], 0\n"
    "buffer_load_dwordx4 v[52:55], v6, s[40:43], s73 offen sc1\n"
    "buffer_load_dwordx4 v[56:59], v6, s[40:43], s74 offen sc1\n"
    "buffer_load_dwordx4 v[60:63], v7, s[40:43], s73 offen sc1\n"
    "v_pk_add_f32 v[112:113], v[80:81], v[82:83]\n"
    "v_pk_add_f32 v[114:115], v[84:85], v[86:87]\n"
    "v_pk_add_f32 v[112:113], v[112:113], v[88:89]\n"
    "v_pk_add_f32 v[114:115], v[114:115], v[90:91]\n"
    "v_pk_add_f32 v[112:113], v[112:113], v[92:93]\n"
    "v_pk_add_f32 v[114:115], v[114:115], v[94:95]\n"
    "v_pk_add_f32 v[112:113], v[112:113], v[96:97]\n"
    "v_pk_add_f32 v[114:115], v[114:115], v[98:99]\n"
    "v_pk_add_f32 v[112:113], v[112:113], v[114:115]\n"
    "v_add_f32 v112, v112, v113\n"
    "s_nop 1\n"
    "v_add_f32_dpp v14, v112, v112 row_ror:8 row_mask:0xf bank_mask:0xf\n"
    "v_add_f32_dpp v15, v112, v112 row_ror:8 row_mask:0xf bank_mask:0xf\n"
    "s_nop 1\n"
    "v_permlane16_swap_b32 v14, v15\n"
    "v_add_f32 v112, v14, v15\n"
    "v_add_f32 v14, v14, v15\n"
    "s_nop 1\n"
    "v_permlane32_swap_b32 v112, v14\n"
    "v_add_f32 v112, v112, v14\n"
    "v_mul_f32 v112, 0x3be38e39, v112\n"
    "v_mul_f32 v114, v112, v12\n"
    "v_pk_add_f32 v[80:81], v[80:81], v[112:113] op_sel_hi:[1,0] neg_lo:[0,1] neg_hi:[0,1]\n"
    "v_pk_add_f32 v[82:83], v[82:83], v[112:113] op_sel_hi:[1,0] neg_lo:[0,1] neg_hi:[0,1]\n"
    "v_pk_add_f32 v[84:85], v[84:85], v[112:113] op_sel_hi:[1,0] neg_lo:[0,1] neg_hi:[0,1]\n"
    "v_pk_add_f32 v[86:87], v[86:87], v[112:113] op_sel_hi:[1,0] neg_lo:[0,1] neg_hi:[0,1]\n"
    "v_pk_add_f32 v[88:89], v[88:89], v[112:113] op_sel_hi:[1,0] neg_lo:[0,1] neg_hi:[0,1]\n"
    "v_pk_add_f32 v[90:91], v[90:91], v[112:113] op_sel_hi:[1,0] neg_lo:[0,1] neg_hi:[0,1]\n"
    "v_pk_add_f32 v[92:93], v[92:93], v[112:113] op_sel_hi:[1,0] neg_lo:[0,1] neg_hi:[0,1]\n"
    "v_pk_add_f32 v[94:95], v[94:95], v[112:113] op_sel_hi:[1,0] neg_lo:[0,1] neg_hi:[0,1]\n"
    "v_pk_add_f32 v[96:97], v[96:97], v[114:115] op_sel_hi:[1,0] neg_lo:[0,1] neg_hi:[0,1]\n"
    "v_pk_add_f32 v[98:99], v[98:99], v[114:115] op_sel_hi:[1,0] neg_lo:[0,1] neg_hi:[0,1]\n"
    "v_pk_mul_f32 v[116:117], v[80:81], v[80:81]\n"
    "v_pk_mul_f32 v[118:119], v[82:83], v[82:83]\n"
    "v_pk_fma_f32 v[116:117], v[84:85], v[84:85], v[116:117]\n"
    "v_pk_fma_f32 v[118:119], v[86:87], v[86:87], v[118:119]\n"
    "v_pk_fma_f32 v[116:117], v[88:89], v[88:89], v[116:117]\n"
    "v_pk_fma_f32 v[118:119], v[90:91], v[90:91], v[118:119]\n"
    "v_pk_fma_f32 v[116:117], v[92:93], v[92:93], v[116:117]\n"
    "v_pk_fma_f32 v[118:119], v[94:95], v[94:95], v[118:119]\n"
    "v_pk_fma_f32 v[116:117], v[96:97], v[96:97], v[116:117]\n"
    "v_pk_fma_f32 v[118:119], v[98:99], v[98:99], v[118:119]\n"
    "v_pk_add_f32 v[116:117], v[116:117], v[118:119]\n"
    "v_add_f32 v113, v116, v117\n"
    "s_nop 1\n"
    "v_add_f32_dpp v14, v113, v113 row_ror:8 row_mask:0xf bank_mask:0xf\n"
    "v_add_f32_dpp v15, v113, v113 row_ror:8 row_mask:0xf bank_mask:0xf\n"
    "s_nop 1\n"
    "v_permlane16_swap_b32 v14, v15\n"
    "v_add_f32 v113, v14, v15\n"
    "v_add_f32 v14, v14, v15\n"
    "s_nop 1\n"
    "v_permlane32_swap_b32 v113, v14\n"
    "v_add_f32 v113, v113, v14\n"
    "ds_write_b64 v10, v[112:113]\n"
    "s_waitcnt vmcnt(17)\n"
    "s_add_u32 s75, s66, 1\n"
    "s_min_i32 s75, s75, s68\n"
    "s_sub_u32 s76, s68, 2\n"
    "s_max_i32 s75, s75, s76\n"
    "s_sub_u32 s75, s75, 1\n"
    "v_mov_b32 v14, s75\n"
    "s_mov_b64 exec, s[64:65]\n"
    "buffer_atomic_smax v14, v13, s[36:39], 0 offen sc1\n"
    "s_mov_b64 exec, -1\n"
    "s_waitcnt lgkmcnt(0)\n"
    "s_barrier\n"
    "ds_read_b128 v[32:35], v11\n"
    "ds_read_b128 v[36:39], v11 offset:16\n"
    "v_xor_b32 v10, 256, v10\n"
    "v_xor_b32 v11, 256, v11\n"
    "s_waitcnt lgkmcnt(0)\n"
    "v_pk_add_f32 v[114:115], v[32:33], v[34:35]\n"
    "v_pk_add_f32 v[116:117], v[36:37], v[38:39]\n"
    "v_pk_add_f32 v[114:115], v[114:115], v[116:117]\n"
    "v_fmac_f32 v32, 0xbe800000, v114\n"
    "v_fmac_f32 v34, 0xbe800000, v114\n"
    "v_fmac_f32 v36, 0xbe800000, v114\n"
    "v_fmac_f32 v38, 0xbe800000, v114\n"
    "v_fmac_f32 v112, 0xbe800000, v114\n"
    "v_mul_f32 v116, v32, v32\n"
    "v_fmac_f32 v116, v34, v34\n"
    "v_fmac_f32 v116, v36, v36\n"
    "v_fmac_f32 v116, v38, v38\n"
    "v_fmac_f32 v115, 0x43100000, v116\n"
    "v_mov_b32 v116, 0x3727c5ac\n"
    "v_fmac_f32 v116, 0x3ae38e39, v115\n"
    "v_rsq_f32 v116, v116\n"
    "s_nop 0\n"
    "v_mul_f32 v114, v112, v116\n"
    "v_pk_fma_f32 v[80:81], v[80:81], v[116:117], v[114:115] op_sel_hi:[1,0,0]\n"
    "v_pk_fma_f32 v[82:83], v[82:83], v[116:117], v[114:115] op_sel_hi:[1,0,0]\n"
    "v_pk_fma_f32 v[84:85], v[84:85], v[116:117], v[114:115] op_sel_hi:[1,0,0]\n"
    "v_pk_fma_f32 v[86:87], v[86:87], v[116:117], v[114:115] op_sel_hi:[1,0,0]\n"
    "v_pk_fma_f32 v[88:89], v[88:89], v[116:117], v[114:115] op_sel_hi:[1,0,0]\n"
    "v_pk_fma_f32 v[90:91], v[90:91], v[116:117], v[114:115] op_sel_hi:[1,0,0]\n"
    "v_pk_fma_f32 v[92:93], v[92:93], v[116:117], v[114:115] op_sel_hi:[1,0,0]\n"
    "v_pk_fma_f32 v[94:95], v[94:95], v[116:117], v[114:115] op_sel_hi:[1,0,0]\n"
    "v_pk_fma_f32 v[96:97], v[96:97], v[116:117], v[114:115] op_sel_hi:[1,0,0]\n"
    "v_pk_fma_f32 v[98:99], v[98:99], v[116:117], v[114:115] op_sel_hi:[1,0,0]\n"
    "v_cvt_pk_bf16_f32 v100, v80, v81\n"
    "v_cvt_pk_bf16_f32 v101, v82, v83\n"
    "v_cvt_pk_bf16_f32 v102, v84, v85\n"
    "v_cvt_pk_bf16_f32 v103, v86, v87\n"
    "v_cvt_pk_bf16_f32 v104, v88, v89\n"
    "v_cvt_pk_bf16_f32 v105, v90, v91\n"
    "v_cvt_pk_bf16_f32 v106, v92, v93\n"
    "v_cvt_pk_bf16_f32 v107, v94, v95\n"
    "v_cvt_pk_bf16_f32 v108, v96, v97\n"
    "v_cvt_pk_bf16_f32 v109, v98, v99\n"
    "v_permlane16_swap_b32 v100, v102\n"
    "v_permlane16_swap_b32 v101, v103\n"
    "buffer_store_dwordx4 v[100:103], v4, s[44:47], s77 offen sc1\n"
    "v_permlane16_swap_b32 v104, v106\n"
    "v_permlane16_swap_b32 v105, v107\n"
    "buffer_store_dwordx4 v[104:107], v4, s[44:47], s78 offen sc1\n"
    "v_permlane16_swap_b32 v108, v110\n"
    "v_permlane16_swap_b32 v109, v111\n"
    "buffer_store_dwordx4 v[108:111], v5, s[44:47], s77 offen sc1\n"
    "s_add_u32 s68, s68, 1\n"
    "s_add_u32 s69, s68, 4\n"
    "s_and_b32 s75, s68, s58\n"
    "s_mul_i32 s77, s75, 81920\n"
    "s_add_u32 s77, s77, s59\n"
    "s_add_u32 s78, s77, s60\n"
    "s_and_b32 s73, s69, s55\n"
    "s_mul_i32 s73, s73, s54\n"
    "s_add_u32 s73, s73, s53\n"
    "s_add_u32 s74, s73, s56\n"
    "s_waitcnt vmcnt(27)\n"
    "v_mfma_f32_16x16x32_bf16 v[80:83], v[24:27], v[68:71], 0\n"
    "v_mfma_f32_16x16x32_bf16 v[84:87], v[28:31], v[68:71], 0\n"
    "s_waitcnt vmcnt(26)\n"
    "v_mfma_f32_16x16x32_bf16 v[88:91], v[24:27], v[72:75], 0\n"
    "v_mfma_f32_16x16x32_bf16 v[92:95], v[28:31], v[72:75], 0\n"
    "s_waitcnt vmcnt(25)\n"
    "v_mfma_f32_16x16x32_bf16 v[96:99], v[24:27], v[76:79], 0\n"
    "buffer_load_dwordx4 v[68:71], v6, s[40:43], s73 offen sc1\n"
    "buffer_load_dwordx4 v[72:75], v6, s[40:43], s74 offen sc1\n"
    "buffer_load_dwordx4 v[76:79], v7, s[40:43], s73 offen sc1\n"
    "v_pk_add_f32 v[112:113], v[80:81], v[82:83]\n"
    "v_pk_add_f32 v[114:115], v[84:85], v[86:87]\n"
    "v_pk_add_f32 v[112:113], v[112:113], v[88:89]\n"
    "v_pk_add_f32 v[114:115], v[114:115], v[90:91]\n"
    "v_pk_add_f32 v[112:113], v[112:113], v[92:93]\n"
    "v_pk_add_f32 v[114:115], v[114:115], v[94:95]\n"
    "v_pk_add_f32 v[112:113], v[112:113], v[96:97]\n"
    "v_pk_add_f32 v[114:115], v[114:115], v[98:99]\n"
    "v_pk_add_f32 v[112:113], v[112:113], v[114:115]\n"
    "v_add_f32 v112, v112, v113\n"
    "s_nop 1\n"
    "v_add_f32_dpp v14, v112, v112 row_ror:8 row_mask:0xf bank_mask:0xf\n"
    "v_add_f32_dpp v15, v112, v112 row_ror:8 row_mask:0xf bank_mask:0xf\n"
    "s_nop 1\n"
    "v_permlane16_swap_b32 v14, v15\n"
    "v_add_f32 v112, v14, v15\n"
    "v_add_f32 v14, v14, v15\n"
    "s_nop 1\n"
    "v_permlane32_swap_b32 v112, v14\n"
    "v_add_f32 v112, v112, v14\n"
    "v_mul_f32 v112, 0x3be38e39, v112\n"
    "v_mul_f32 v114, v112, v12\n"
    "v_pk_add_f32 v[80:81], v[80:81], v[112:113] op_sel_hi:[1,0] neg_lo:[0,1] neg_hi:[0,1]\n"
    "v_pk_add_f32 v[82:83], v[82:83], v[112:113] op_sel_hi:[1,0] neg_lo:[0,1] neg_hi:[0,1]\n"
    "v_pk_add_f32 v[84:85], v[84:85], v[112:113] op_sel_hi:[1,0] neg_lo:[0,1] neg_hi:[0,1]\n"
    "v_pk_add_f32 v[86:87], v[86:87], v[112:113] op_sel_hi:[1,0] neg_lo:[0,1] neg_hi:[0,1]\n"
    "v_pk_add_f32 v[88:89], v[88:89], v[112:113] op_sel_hi:[1,0] neg_lo:[0,1] neg_hi:[0,1]\n"
    "v_pk_add_f32 v[90:91], v[90:91], v[112:113] op_sel_hi:[1,0] neg_lo:[0,1] neg_hi:[0,1]\n"
    "v_pk_add_f32 v[92:93], v[92:93], v[112:113] op_sel_hi:[1,0] neg_lo:[0,1] neg_hi:[0,1]\n"
    "v_pk_add_f32 v[94:95], v[94:95], v[112:113] op_sel_hi:[1,0] neg_lo:[0,1] neg_hi:[0,1]\n"
    "v_pk_add_f32 v[96:97], v[96:97], v[114:115] op_sel_hi:[1,0] neg_lo:[0,1] neg_hi:[0,1]\n"
    "v_pk_add_f32 v[98:99], v[98:99], v[114:115] op_sel_hi:[1,0] neg_lo:[0,1] neg_hi:[0,1]\n"
    "v_pk_mul_f32 v[116:117], v[80:81], v[80:81]\n"
    "v_pk_mul_f32 v[118:119], v[82:83], v[82:83]\n"
    "v_pk_fma_f32 v[116:117], v[84:85], v[84:85], v[116:117]\n"
    "v_pk_fma_f32 v[118:119], v[86:87], v[86:87], v[118:119]\n"
    "v_pk_fma_f32 v[116:117], v[88:89], v[88:89], v[116:117]\n"
    "v_pk_fma_f32 v[118:119], v[90:91], v[90:91], v[118:119]\n"
    "v_pk_fma_f32 v[116:117], v[92:93], v[92:93], v[116:117]\n"
    "v_pk_fma_f32 v[118:119], v[94:95], v[94:95], v[118:119]\n"
    "v_pk_fma_f32 v[116:117], v[96:97], v[96:97], v[116:117]\n"
    "v_pk_fma_f32 v[118:119], v[98:99], v[98:99], v[118:119]\n"
    "v_pk_add_f32 v[116:117], v[116:117], v[118:119]\n"
    "v_add_f32 v113, v116, v117\n"
    "s_nop 1\n"
    "v_add_f32_dpp v14, v113, v113 row_ror:8 row_mask:0xf bank_mask:0xf\n"
    "v_add_f32_dpp v15, v113, v113 row_ror:8 row_mask:0xf bank_mask:0xf\n"
    "s_nop 1\n"
    "v_permlane16_swap_b32 v14, v15\n"
    "v_add_f32 v113, v14, v15\n"
    "v_add_f32 v14, v14, v15\n"
    "s_nop 1\n"
    "v_permlane32_swap_b32 v113, v14\n"
    "v_add_f32 v113, v113, v14\n"
    "ds_write_b64 v10, v[112:113]\n"
    "s_waitcnt vmcnt(17)\n"
    "s_add_u32 s75, s66, 1\n"
    "s_min_i32 s75, s75, s68\n"
    "s_sub_u32 s76, s68, 2\n"
    "s_max_i32 s75, s75, s76\n"
    "s_sub_u32 s75, s75, 1\n"
    "v_mov_b32 v14, s75\n"
    "s_mov_b64 exec, s[64:65]\n"
    "buffer_atomic_smax v14, v13, s[36:39], 0 offen sc1\n"
    "s_mov_b64 exec, -1\n"
    "s_waitcnt lgkmcnt(0)\n"
    "s_barrier\n"
    "ds_read_b128 v[32:35], v11\n"
    "ds_read_b128 v[36:39], v11 offset:16\n"
    "v_xor_b32 v10, 256, v10\n"
    "v_xor_b32 v11, 256, v11\n"
    "s_waitcnt lgkmcnt(0)\n"
    "v_pk_add_f32 v[114:115], v[32:33], v[34:35]\n"
    "v_pk_add_f32 v[116:117], v[36:37], v[38:39]\n"
    "v_pk_add_f32 v[114:115], v[114:115], v[116:117]\n"
    "v_fmac_f32 v32, 0xbe800000, v114\n"
    "v_fmac_f32 v34, 0xbe800000, v114\n"
    "v_fmac_f32 v36, 0xbe800000, v114\n"
    "v_fmac_f32 v38, 0xbe800000, v114\n"
    "v_fmac_f32 v112, 0xbe800000, v114\n"
    "v_mul_f32 v116, v32, v32\n"
    "v_fmac_f32 v116, v34, v34\n"
    "v_fmac_f32 v116, v36, v36\n"
    "v_fmac_f32 v116, v38, v38\n"
    "v_fmac_f32 v115, 0x43100000, v116\n"
    "v_mov_b32 v116, 0x3727c5ac\n"
    "v_fmac_f32 v116, 0x3ae38e39, v115\n"
    "v_rsq_f32 v116, v116\n"
    "s_nop 0\n"
    "v_mul_f32 v114, v112, v116\n"
    "v_pk_fma_f32 v[80:81], v[80:81], v[116:117], v[114:115] op_sel_hi:[1,0,0]\n"
    "v_pk_fma_f32 v[82:83], v[82:83], v[116:117], v[114:115] op_sel_hi:[1,0,0]\n"
    "v_pk_fma_f32 v[84:85], v[84:85], v[116:117], v[114:115] op_sel_hi:[1,0,0]\n"
    "v_pk_fma_f32 v[86:87], v[86:87], v[116:117], v[114:115] op_sel_hi:[1,0,0]\n"
    "v_pk_fma_f32 v[88:89], v[88:89], v[116:117], v[114:115] op_sel_hi:[1,0,0]\n"
    "v_pk_fma_f32 v[90:91], v[90:91], v[116:117], v[114:115] op_sel_hi:[1,0,0]\n"
    "v_pk_fma_f32 v[92:93], v[92:93], v[116:117], v[114:115] op_sel_hi:[1,0,0]\n"
    "v_pk_fma_f32 v[94:95], v[94:95], v[116:117], v[114:115] op_sel_hi:[1,0,0]\n"
    "v_pk_fma_f32 v[96:97], v[96:97], v[116:117], v[114:115] op_sel_hi:[1,0,0]\n"
    "v_pk_fma_f32 v[98:99], v[98:99], v[116:117], v[114:115] op_sel_hi:[1,0,0]\n"
    "v_cvt_pk_bf16_f32 v100, v80, v81\n"
    "v_cvt_pk_bf16_f32 v101, v82, v83\n"
    "v_cvt_pk_bf16_f32 v102, v84, v85\n"
    "v_cvt_pk_bf16_f32 v103, v86, v87\n"
    "v_cvt_pk_bf16_f32 v104, v88, v89\n"
    "v_cvt_pk_bf16_f32 v105, v90, v91\n"
    "v_cvt_pk_bf16_f32 v106, v92, v93\n"
    "v_cvt_pk_bf16_f32 v107, v94, v95\n"
    "v_cvt_pk_bf16_f32 v108, v96, v97\n"
    "v_cvt_pk_bf16_f32 v109, v98, v99\n"
    "v_permlane16_swap_b32 v100, v102\n"
    "v_permlane16_swap_b32 v101, v103\n"
    "buffer_store_dwordx4 v[100:103], v4, s[44:47], s77 offen sc1\n"
    "v_permlane16_swap_b32 v104, v106\n"
    "v_permlane16_swap_b32 v105, v107\n"
    "buffer_store_dwordx4 v[104:107], v4, s[44:47], s78 offen sc1\n"
    "v_permlane16_swap_b32 v108, v110\n"
    "v_permlane16_swap_b32 v109, v111\n"
    "buffer_store_dwordx4 v[108:111], v5, s[44:47], s77 offen sc1\n"
    "s_add_u32 s68, s68, 1\n"
    "s_add_u32 s69, s68, 4\n"
    "s_and_b32 s75, s68, s58\n"
    "s_mul_i32 s77, s75, 81920\n"
    "s_add_u32 s77, s77, s59\n"
    "s_add_u32 s78, s77, s60\n"
    "s_and_b32 s73, s69, s55\n"
    "s_mul_i32 s73, s73, s54\n"
    "s_add_u32 s73, s73, s53\n"
    "s_add_u32 s74, s73, s56\n"
    "s_waitcnt vmcnt(27)\n"
    "v_mfma_f32_16x16x32_bf16 v[80:83], v[24:27], v[124:127], 0\n"
    "v_mfma_f32_16x16x32_bf16 v[84:87], v[28:31], v[124:127], 0\n"
    "s_waitcnt vmcnt(26)\n"
    "v_mfma_f32_16x16x32_bf16 v[88:91], v[24:27], v[128:131], 0\n"
    "v_mfma_f32_16x16x32_bf16 v[92:95], v[28:31], v[128:131], 0\n"
    "s_waitcnt vmcnt(25)\n"
    "v_mfma_f32_16x16x32_bf16 v[96:99], v[24:27], v[132:135], 0\n"
    "buffer_load_dwordx4 v[124:127], v6, s[40:43], s73 offen sc1\n"
    "buffer_load_dwordx4 v[128:131], v6, s[40:43], s74 offen sc1\n"
    "buffer_load_dwordx4 v[132:135], v7, s[40:43], s73 offen sc1\n"
    "v_pk_add_f32 v[112:113], v[80:81], v[82:83]\n"
    "v_pk_add_f32 v[114:115], v[84:85], v[86:87]\n"
    "v_pk_add_f32 v[112:113], v[112:113], v[88:89]\n"
    "v_pk_add_f32 v[114:115], v[114:115], v[90:91]\n"
    "v_pk_add_f32 v[112:113], v[112:113], v[92:93]\n"
    "v_pk_add_f32 v[114:115], v[114:115], v[94:95]\n"
    "v_pk_add_f32 v[112:113], v[112:113], v[96:97]\n"
    "v_pk_add_f32 v[114:115], v[114:115], v[98:99]\n"
    "v_pk_add_f32 v[112:113], v[112:113], v[114:115]\n"
    "v_add_f32 v112, v112, v113\n"
    "s_nop 1\n"
    "v_add_f32_dpp v14, v112, v112 row_ror:8 row_mask:0xf bank_mask:0xf\n"
    "v_add_f32_dpp v15, v112, v112 row_ror:8 row_mask:0xf bank_mask:0xf\n"
    "s_nop 1\n"
    "v_permlane16_swap_b32 v14, v15\n"
    "v_add_f32 v112, v14, v15\n"
    "v_add_f32 v14, v14, v15\n"
    "s_nop 1\n"
    "v_permlane32_swap_b32 v112, v14\n"
    "v_add_f32 v112, v112, v14\n"
    "v_mul_f32 v112, 0x3be38e39, v112\n"
    "v_mul_f32 v114, v112, v12\n"
    "v_pk_add_f32 v[80:81], v[80:81], v[112:113] op_sel_hi:[1,0] neg_lo:[0,1] neg_hi:[0,1]\n"
    "v_pk_add_f32 v[82:83], v[82:83], v[112:113] op_sel_hi:[1,0] neg_lo:[0,1] neg_hi:[0,1]\n"
    "v_pk_add_f32 v[84:85], v[84:85], v[112:113] op_sel_hi:[1,0] neg_lo:[0,1] neg_hi:[0,1]\n"
    "v_pk_add_f32 v[86:87], v[86:87], v[112:113] op_sel_hi:[1,0] neg_lo:[0,1] neg_hi:[0,1]\n"
    "v_pk_add_f32 v[88:89], v[88:89], v[112:113] op_sel_hi:[1,0] neg_lo:[0,1] neg_hi:[0,1]\n"
    "v_pk_add_f32 v[90:91], v[90:91], v[112:113] op_sel_hi:[1,0] neg_lo:[0,1] neg_hi:[0,1]\n"
    "v_pk_add_f32 v[92:93], v[92:93], v[112:113] op_sel_hi:[1,0] neg_lo:[0,1] neg_hi:[0,1]\n"
    "v_pk_add_f32 v[94:95], v[94:95], v[112:113] op_sel_hi:[1,0] neg_lo:[0,1] neg_hi:[0,1]\n"
    "v_pk_add_f32 v[96:97], v[96:97], v[114:115] op_sel_hi:[1,0] neg_lo:[0,1] neg_hi:[0,1]\n"
    "v_pk_add_f32 v[98:99], v[98:99], v[114:115] op_sel_hi:[1,0] neg_lo:[0,1] neg_hi:[0,1]\n"
    "v_pk_mul_f32 v[116:117], v[80:81], v[80:81]\n"
    "v_pk_mul_f32 v[118:119], v[82:83], v[82:83]\n"
    "v_pk_fma_f32 v[116:117], v[84:85], v[84:85], v[116:117]\n"
    "v_pk_fma_f32 v[118:119], v[86:87], v[86:87], v[118:119]\n"
    "v_pk_fma_f32 v[116:117], v[88:89], v[88:89], v[116:117]\n"
    "v_pk_fma_f32 v[118:119], v[90:91], v[90:91], v[118:119]\n"
    "v_pk_fma_f32 v[116:117], v[92:93], v[92:93], v[116:117]\n"
    "v_pk_fma_f32 v[118:119], v[94:95], v[94:95], v[118:119]\n"
    "v_pk_fma_f32 v[116:117], v[96:97], v[96:97], v[116:117]\n"
    "v_pk_fma_f32 v[118:119], v[98:99], v[98:99], v[118:119]\n"
    "v_pk_add_f32 v[116:117], v[116:117], v[118:119]\n"
    "v_add_f32 v113, v116, v117\n"
    "s_nop 1\n"
    "v_add_f32_dpp v14, v113, v113 row_ror:8 row_mask:0xf bank_mask:0xf\n"
    "v_add_f32_dpp v15, v113, v113 row_ror:8 row_mask:0xf bank_mask:0xf\n"
    "s_nop 1\n"
    "v_permlane16_swap_b32 v14, v15\n"
    "v_add_f32 v113, v14, v15\n"
    "v_add_f32 v14, v14, v15\n"
    "s_nop 1\n"
    "v_permlane32_swap_b32 v113, v14\n"
    "v_add_f32 v113, v113, v14\n"
    "ds_write_b64 v10, v[112:113]\n"
    "s_waitcnt vmcnt(17)\n"
    "s_add_u32 s75, s66, 1\n"
    "s_min_i32 s75, s75, s68\n"
    "s_sub_u32 s76, s68, 2\n"
    "s_max_i32 s75, s75, s76\n"
    "s_sub_u32 s75, s75, 1\n"
    "v_mov_b32 v14, s75\n"
    "s_mov_b64 exec, s[64:65]\n"
    "buffer_atomic_smax v14, v13, s[36:39], 0 offen sc1\n"
    "s_mov_b64 exec, -1\n"
    "s_waitcnt lgkmcnt(0)\n"
    "s_barrier\n"
    "ds_read_b128 v[32:35], v11\n"
    "ds_read_b128 v[36:39], v11 offset:16\n"
    "v_xor_b32 v10, 256, v10\n"
    "v_xor_b32 v11, 256, v11\n"
    "s_waitcnt lgkmcnt(0)\n"
    "v_pk_add_f32 v[114:115], v[32:33], v[34:35]\n"
    "v_pk_add_f32 v[116:117], v[36:37], v[38:39]\n"
    "v_pk_add_f32 v[114:115], v[114:115], v[116:117]\n"
    "v_fmac_f32 v32, 0xbe800000, v114\n"
    "v_fmac_f32 v34, 0xbe800000, v114\n"
    "v_fmac_f32 v36, 0xbe800000, v114\n"
    "v_fmac_f32 v38, 0xbe800000, v114\n"
    "v_fmac_f32 v112, 0xbe800000, v114\n"
    "v_mul_f32 v116, v32, v32\n"
    "v_fmac_f32 v116, v34, v34\n"
    "v_fmac_f32 v116, v36, v36\n"
    "v_fmac_f32 v116, v38, v38\n"
    "v_fmac_f32 v115, 0x43100000, v116\n"
    "v_mov_b32 v116, 0x3727c5ac\n"
    "v_fmac_f32 v116, 0x3ae38e39, v115\n"
    "v_rsq_f32 v116, v116\n"
    "s_nop 0\n"
    "v_mul_f32 v114, v112, v116\n"
    "v_pk_fma_f32 v[80:81], v[80:81], v[116:117], v[114:115] op_sel_hi:[1,0,0]\n"
    "v_pk_fma_f32 v[82:83], v[82:83], v[116:117], v[114:115] op_sel_hi:[1,0,0]\n"
    "v_pk_fma_f32 v[84:85], v[84:85], v[116:117], v[114:115] op_sel_hi:[1,0,0]\n"
    "v_pk_fma_f32 v[86:87], v[86:87], v[116:117], v[114:115] op_sel_hi:[1,0,0]\n"
    "v_pk_fma_f32 v[88:89], v[88:89], v[116:117], v[114:115] op_sel_hi:[1,0,0]\n"
    "v_pk_fma_f32 v[90:91], v[90:91], v[116:117], v[114:115] op_sel_hi:[1,0,0]\n"
    "v_pk_fma_f32 v[92:93], v[92:93], v[116:117], v[114:115] op_sel_hi:[1,0,0]\n"
    "v_pk_fma_f32 v[94:95], v[94:95], v[116:117], v[114:115] op_sel_hi:[1,0,0]\n"
    "v_pk_fma_f32 v[96:97], v[96:97], v[116:117], v[114:115] op_sel_hi:[1,0,0]\n"
    "v_pk_fma_f32 v[98:99], v[98:99], v[116:117], v[114:115] op_sel_hi:[1,0,0]\n"
    "v_cvt_pk_bf16_f32 v100, v80, v81\n"
    "v_cvt_pk_bf16_f32 v101, v82, v83\n"
    "v_cvt_pk_bf16_f32 v102, v84, v85\n"
    "v_cvt_pk_bf16_f32 v103, v86, v87\n"
    "v_cvt_pk_bf16_f32 v104, v88, v89\n"
    "v_cvt_pk_bf16_f32 v105, v90, v91\n"
    "v_cvt_pk_bf16_f32 v106, v92, v93\n"
    "v_cvt_pk_bf16_f32 v107, v94, v95\n"
    "v_cvt_pk_bf16_f32 v108, v96, v97\n"
    "v_cvt_pk_bf16_f32 v109, v98, v99\n"
    "v_permlane16_swap_b32 v100, v102\n"
    "v_permlane16_swap_b32 v101, v103\n"
    "buffer_store_dwordx4 v[100:103], v4, s[44:47], s77 offen sc1\n"
    "v_permlane16_swap_b32 v104, v106\n"
    "v_permlane16_swap_b32 v105, v107\n"
    "buffer_store_dwordx4 v[104:107], v4, s[44:47], s78 offen sc1\n"
    "v_permlane16_swap_b32 v108, v110\n"
    "v_permlane16_swap_b32 v109, v111\n"
    "buffer_store_dwordx4 v[108:111], v5, s[44:47], s77 offen sc1\n"
    "s_cmp_eq_u32 s68, s67\n"
    "s_cbranch_scc1 .LrLN0_fin_%=\n"
    "s_branch .LrLN0_top_%=\n"
    ".LrLN0_LOOPE_%=:\n"
    ".LrLN0_fin_%=:\n"
    "s_waitcnt vmcnt(0)\n"
    "s_mov_b32 s68, 63\n"
    "v_mov_b32 v14, s68\n"
    "s_mov_b64 exec, s[64:65]\n"
    "buffer_atomic_smax v14, v13, s[36:39], 0 offen sc1\n"
    "s_mov_b64 exec, -1\n"
    "s_waitcnt vmcnt(0)\n"
    "s_barrier\n"
    "s_branch .LrLN0_FIN_%=\n"
    ".LrLN0_first_%=:\n"
    "s_waitcnt vmcnt(0)\n"
    "v_mov_b32 v14, s68\n"
    "s_mov_b64 exec, s[64:65]\n"
    "buffer_atomic_smax v14, v13, s[36:39], 0 offen sc1\n"
    "s_mov_b64 exec, -1\n"
    "s_branch .LrLN0_first1_%=\n"
    ".LrLN0_FIN_%=:\n"
  );
}
DEVI void role_mid(v16u sa, v16u sb, v16u va, v16u vb) {
  RUN_RED(
    "s_nop 4\n"
    "s_getpc_b64 s[84:85]\n"
    ".LrMID_PC_%=:\n"
    "s_and_b32 s85, s85, 0xffff\n"
    "s_mov_b32 s86, .LrMID_FIN_%=-.LrMID_PC_%=\n"
    "s_mov_b32 s87, 0x20000\n"
    "v_mbcnt_lo_u32_b32 v134, -1, 0\n"
    "v_mbcnt_hi_u32_b32 v134, -1, v134\n"
    "v_lshlrev_b32 v134, 7, v134\n"
    "buffer_load_dword v135, v134, s[84:87], 0 offen\n"
    "v_add_u32 v134, 8192, v134\n"
    "buffer_load_dword v135, v134, s[84:87], 0 offen\n"
    "s_mov_b32 s69, 0\n"
    "v_mov_b32 v122, -1\n"
    "v_mov_b32 v123, -1\n"
    "s_mov_b32 s70, 262144\n"
    ".LrMID_par_%=:\n"
    "buffer_load_dwordx2 v[14:15], off, s[36:39], 0 sc1\n"
    "s_waitcnt vmcnt(0)\n"
    "v_readfirstlane_b32 s75, v14\n"
    "v_readfirstlane_b32 s76, v15\n"
    "s_xor_b32 s75, s75, s48\n"
    "s_xor_b32 s76, s76, s49\n"
    "s_cselect_b32 s76, 2, 0\n"
    "s_or_b32 s76, s76, s75\n"
    "s_cmp_lt_u32 s76, 2\n"
    "s_cbranch_scc1 .LrMID_par1_%=\n"
    "s_sleep 4\n"
    "s_sub_u32 s70, s70, 1\n"
    "s_cbranch_scc0 .LrMID_par_%=\n"
    "s_mov_b32 s70, 0\n"
    ".LrMID_par1_%=:\n"
    "s_and_b32 s75, s75, 1\n"
    "s_lshl_b32 s75, s75, 13\n"
    "s_add_u32 s36, s36, s75\n"
    "s_addc_u32 s37, s37, 0\n"
    "s_and_b32 s76, s75, s61\n"
    "s_add_u32 s40, s40, s76\n"
    "s_addc_u32 s41, s41, 0\n"
    "s_and_b32 s76, s75, s63\n"
    "s_add_u32 s44, s44, s76\n"
    "s_addc_u32 s45, s45, 0\n"
    "s_branch .LrMID_block_%=\n"
    ".LrMID_LOOPB_%=:\n"
    ".LrMID_top_%=:\n"
    "s_mov_b32 s68, s69\n"
    "s_add_u32 s69, s68, 1\n"
    "s_and_b32 s75, s68, s58\n"
    "s_mul_i32 s77, s75, 4194304\n"
    "s_add_u32 s77, s77, s59\n"
    "s_add_u32 s78, s77, s60\n"
    "s_and_b32 s71, s69, 7\n"
    "s_mul_i32 s71, s71, 4194304\n"
    "s_add_u32 s71, s71, s52\n"
    "s_add_u32 s72, s71, 4096\n"
    "s_and_b32 s73, s69, s55\n"
    "s_mul_i32 s73, s73, s54\n"
    "s_add_u32 s73, s73, s53\n"
    "s_add_u32 s74, s73, s56\n"
    "s_mul_i32 s75, s75, 4194304\n"
    "s_add_u32 s75, s75, s57\n"
    "s_add_u32 s76, s75, 1024\n"
    "s_waitcnt vmcnt(12)\n"
    "v_mfma_f32_16x16x32_bf16 v[80:83], v[16:19], v[32:35], 0\n"
    "s_waitcnt vmcnt(11)\n"
    "v_mfma_f32_16x16x32_bf16 v[80:83], v[20:23], v[36:39], v[80:83]\n"
    "s_waitcnt vmcnt(10)\n"
    "v_mfma_f32_16x16x32_bf16 v[84:87], v[16:19], v[40:43], 0\n"
    "s_waitcnt vmcnt(9)\n"
    "v_mfma_f32_16x16x32_bf16 v[84:87], v[20:23], v[44:47], v[84:87]\n"
    "s_waitcnt vmcnt(8)\n"
    "v_mfma_f32_16x16x32_bf16 v[88:91], v[16:19], v[48:51], 0\n"
    "s_waitcnt vmcnt(7)\n"
    "v_mfma_f32_16x16x32_bf16 v[88:91], v[20:23], v[52:55], v[88:91]\n"
    "s_waitcnt vmcnt(6)\n"
    "v_mfma_f32_16x16x32_bf16 v[92:95], v[16:19], v[56:59], 0\n"
    "s_waitcnt vmcnt(5)\n"
    "v_mfma_f32_16x16x32_bf16 v[92:95], v[20:23], v[60:63], v[92:95]\n"
    "s_waitcnt vmcnt(4)\n"
    "v_mfma_f32_16x16x32_bf16 v[96:99], v[16:19], v[64:67], 0\n"
    "s_waitcnt vmcnt(3)\n"
    "v_mfma_f32_16x16x32_bf16 v[80:83], v[24:27], v[68:71], v[80:83]\n"
    "v_mfma_f32_16x16x32_bf16 v[84:87], v[28:31], v[68:71], v[84:87]\n"
    "s_waitcnt vmcnt(2)\n"
    "v_mfma_f32_16x16x32_bf16 v[88:91], v[24:27], v[72:75], v[88:91]\n"
    "v_mfma_f32_16x16x32_bf16 v[92:95], v[28:31], v[72:75], v[92:95]\n"
    "s_waitcnt vmcnt(1)\n"
    "v_mfma_f32_16x16x32_bf16 v[96:99], v[24:27], v[76:79], v[96:99]\n"
    "v_pk_add_f32 v[112:113], v[80:81], v[82:83]\n"
    "v_pk_add_f32 v[114:115], v[84:85], v[86:87]\n"
    "v_pk_add_f32 v[112:113], v[112:113], v[88:89]\n"
    "v_pk_add_f32 v[114:115], v[114:115], v[90:91]\n"
    "v_pk_add_f32 v[112:113], v[112:113], v[92:93]\n"
    "v_pk_add_f32 v[114:115], v[114:115], v[94:95]\n"
    "s_nop 1\n"
    "v_pk_add_f32 v[112:113], v[112:113], v[96:97]\n"
    "v_pk_add_f32 v[114:115], v[114:115], v[98:99]\n"
    "v_pk_add_f32 v[112:113], v[112:113], v[114:115]\n"
    "v_add_f32 v112, v112, v113\n"
    "v_cvt_pk_bf16_f32 v100, v80, v81\n"
    "v_cvt_pk_bf16_f32 v101, v82, v83\n"
    "v_cvt_pk_bf16_f32 v102, v84, v85\n"
    "v_cvt_pk_bf16_f32 v103, v86, v87\n"
    "v_cvt_pk_bf16_f32 v104, v88, v89\n"
    "v_cvt_pk_bf16_f32 v105, v90, v91\n"
    "v_cvt_pk_bf16_f32 v106, v92, v93\n"
    "v_cvt_pk_bf16_f32 v107, v94, v95\n"
    "v_cvt_pk_bf16_f32 v108, v96, v97\n"
    "v_cvt_pk_bf16_f32 v109, v98, v99\n"
    "v_add_f32_dpp v14, v112, v112 row_ror:8 row_mask:0xf bank_mask:0xf\n"
    "v_add_f32_dpp v15, v112, v112 row_ror:8 row_mask:0xf bank_mask:0xf\n"
    "s_nop 1\n"
    "v_permlane16_swap_b32 v14, v15\n"
    "v_add_f32 v112, v14, v15\n"
    "v_add_f32 v14, v14, v15\n"
    "s_nop 1\n"
    "v_permlane32_swap_b32 v112, v14\n"
    "v_add_f32 v112, v112, v14\n"
    "v_mul_f32 v112, 0x3be38e39, v112\n"
    "v_mul_f32 v114, v112, v12\n"
    "v_pk_add_f32 v[80:81], v[80:81], v[112:113] op_sel_hi:[1,0] neg_lo:[0,1] neg_hi:[0,1]\n"
    "v_pk_add_f32 v[82:83], v[82:83], v[112:113] op_sel_hi:[1,0] neg_lo:[0,1] neg_hi:[0,1]\n"
    "v_pk_add_f32 v[84:85], v[84:85], v[112:113] op_sel_hi:[1,0] neg_lo:[0,1] neg_hi:[0,1]\n"
    "v_pk_add_f32 v[86:87], v[86:87], v[112:113] op_sel_hi:[1,0] neg_lo:[0,1] neg_hi:[0,1]\n"
    "v_pk_add_f32 v[88:89], v[88:89], v[112:113] op_sel_hi:[1,0] neg_lo:[0,1] neg_hi:[0,1]\n"
    "v_pk_add_f32 v[90:91], v[90:91], v[112:113] op_sel_hi:[1,0] neg_lo:[0,1] neg_hi:[0,1]\n"
    "v_pk_add_f32 v[92:93], v[92:93], v[112:113] op_sel_hi:[1,0] neg_lo:[0,1] neg_hi:[0,1]\n"
    "v_pk_add_f32 v[94:95], v[94:95], v[112:113] op_sel_hi:[1,0] neg_lo:[0,1] neg_hi:[0,1]\n"
    "v_pk_add_f32 v[96:97], v[96:97], v[114:115] op_sel_hi:[1,0] neg_lo:[0,1] neg_hi:[0,1]\n"
    "v_pk_add_f32 v[98:99], v[98:99], v[114:115] op_sel_hi:[1,0] neg_lo:[0,1] neg_hi:[0,1]\n"
    "v_pk_mul_f32 v[116:117], v[80:81], v[80:81]\n"
    "v_pk_mul_f32 v[118:119], v[82:83], v[82:83]\n"
    "v_pk_fma_f32 v[116:117], v[84:85], v[84:85], v[116:117]\n"
    "v_pk_fma_f32 v[118:119], v[86:87], v[86:87], v[118:119]\n"
    "v_pk_fma_f32 v[116:117], v[88:89], v[88:89], v[116:117]\n"
    "v_pk_fma_f32 v[118:119], v[90:91], v[90:91], v[118:119]\n"
    "v_pk_fma_f32 v[116:117], v[92:93], v[92:93], v[116:117]\n"
    "v_pk_fma_f32 v[118:119], v[94:95], v[94:95], v[118:119]\n"
    "v_pk_fma_f32 v[116:117], v[96:97], v[96:97], v[116:117]\n"
    "v_pk_fma_f32 v[118:119], v[98:99], v[98:99], v[118:119]\n"
    "v_pk_add_f32 v[116:117], v[116:117], v[118:119]\n"
    "v_add_f32 v113, v116, v117\n"
    "s_nop 1\n"
    "v_add_f32_dpp v14, v113, v113 row_ror:8 row_mask:0xf bank_mask:0xf\n"
    "v_add_f32_dpp v15, v113, v113 row_ror:8 row_mask:0xf bank_mask:0xf\n"
    "s_nop 1\n"
    "v_permlane16_swap_b32 v14, v15\n"
    "v_add_f32 v113, v14, v15\n"
    "v_add_f32 v14, v14, v15\n"
    "s_nop 1\n"
    "v_permlane32_swap_b32 v113, v14\n"
    "v_add_f32 v113, v113, v14\n"
    "ds_write_b64 v10, v[112:113]\n"
    "v_add_u32 v120, v122, v1\n"
    "v_cmp_gt_i32 vcc, s69, v120\n"
    "s_nop 0\n"
    "s_or_b64 exec, vcc, 1\n"
    "buffer_atomic_smax v122, v0, s[36:39], 0 offen sc0 sc1\n"
    "s_mov_b64 exec, -1\n"
    "s_nop 0\n"
    "v_permlane16_swap_b32 v100, v102\n"
    "v_permlane16_swap_b32 v101, v103\n"
    "v_permlane16_swap_b32 v104, v106\n"
    "v_permlane16_swap_b32 v105, v107\n"
    "v_permlane16_swap_b32 v108, v110\n"
    "v_permlane16_swap_b32 v109, v111\n"
    "s_waitcnt lgkmcnt(0)\n"
    "s_barrier\n"
    "ds_read_b128 v[32:35], v11\n"
    "ds_read_b128 v[36:39], v11 offset:16\n"
    "buffer_store_dwordx4 v[100:103], v8, s[36:39], s75 offen sc1\n"
    "buffer_store_dwordx4 v[104:107], v8, s[36:39], s76 offen sc1\n"
    "buffer_store_dwordx4 v[108:111], v9, s[36:39], s75 offen sc1\n"
    "v_xor_b32 v10, 256, v10\n"
    "v_xor_b32 v11, 256, v11\n"
    "s_waitcnt lgkmcnt(0)\n"
    "v_pk_add_f32 v[114:115], v[32:33], v[34:35]\n"
    "v_pk_add_f32 v[116:117], v[36:37], v[38:39]\n"
    "v_pk_add_f32 v[114:115], v[114:115], v[116:117]\n"
    "v_fmac_f32 v32, 0xbe800000, v114\n"
    "v_fmac_f32 v34, 0xbe800000, v114\n"
    "v_fmac_f32 v36, 0xbe800000, v114\n"
    "v_fmac_f32 v38, 0xbe800000, v114\n"
    "v_fmac_f32 v112, 0xbe800000, v114\n"
    "v_mul_f32 v116, v32, v32\n"
    "v_fmac_f32 v116, v34, v34\n"
    "v_fmac_f32 v116, v36, v36\n"
    "v_fmac_f32 v116, v38, v38\n"
    "v_fmac_f32 v115, 0x43100000, v116\n"
    "v_mov_b32 v116, 0x3727c5ac\n"
    "v_fmac_f32 v116, 0x3ae38e39, v115\n"
    "v_rsq_f32 v116, v116\n"
    "s_nop 0\n"
    "v_mul_f32 v114, v112, v116\n"
    "v_pk_fma_f32 v[80:81], v[80:81], v[116:117], v[114:115] op_sel_hi:[1,0,0]\n"
    "v_pk_fma_f32 v[82:83], v[82:83], v[116:117], v[114:115] op_sel_hi:[1,0,0]\n"
    "v_pk_fma_f32 v[84:85], v[84:85], v[116:117], v[114:115] op_sel_hi:[1,0,0]\n"
    "v_pk_fma_f32 v[86:87], v[86:87], v[116:117], v[114:115] op_sel_hi:[1,0,0]\n"
    "v_pk_fma_f32 v[88:89], v[88:89], v[116:117], v[114:115] op_sel_hi:[1,0,0]\n"
    "v_pk_fma_f32 v[90:91], v[90:91], v[116:117], v[114:115] op_sel_hi:[1,0,0]\n"
    "v_pk_fma_f32 v[92:93], v[92:93], v[116:117], v[114:115] op_sel_hi:[1,0,0]\n"
    "v_pk_fma_f32 v[94:95], v[94:95], v[116:117], v[114:115] op_sel_hi:[1,0,0]\n"
    "v_pk_fma_f32 v[96:97], v[96:97], v[116:117], v[114:115] op_sel_hi:[1,0,0]\n"
    "v_pk_fma_f32 v[98:99], v[98:99], v[116:117], v[114:115] op_sel_hi:[1,0,0]\n"
    "v_cvt_pk_bf16_f32 v100, v80, v81\n"
    "v_cvt_pk_bf16_f32 v101, v82, v83\n"
    "v_cvt_pk_bf16_f32 v102, v84, v85\n"
    "v_cvt_pk_bf16_f32 v103, v86, v87\n"
    "v_cvt_pk_bf16_f32 v104, v88, v89\n"
    "v_cvt_pk_bf16_f32 v105, v90, v91\n"
    "v_cvt_pk_bf16_f32 v106, v92, v93\n"
    "v_cvt_pk_bf16_f32 v107, v94, v95\n"
    "v_cvt_pk_bf16_f32 v108, v96, v97\n"
    "v_cvt_pk_bf16_f32 v109, v98, v99\n"
    "v_permlane16_swap_b32 v100, v102\n"
    "v_permlane16_swap_b32 v101, v103\n"
    "buffer_store_dwordx4 v[100:103], v4, s[44:47], s77 offen sc1\n"
    "v_permlane16_swap_b32 v104, v106\n"
    "v_permlane16_swap_b32 v105, v107\n"
    "buffer_store_dwordx4 v[104:107], v4, s[44:47], s78 offen sc1\n"
    "v_permlane16_swap_b32 v108, v110\n"
    "v_permlane16_swap_b32 v109, v111\n"
    "buffer_store_dwordx4 v[108:111], v5, s[44:47], s77 offen sc1\n"
    "s_cmp_eq_u32 s69, 64\n"
    "s_cbranch_scc1 .LrMID_fin_%=\n"
    "s_waitcnt vmcnt(6)\n"
    "v_add_u32 v120, v122, v1\n"
    "v_cmp_gt_i32 vcc, s69, v120\n"
    "s_cbranch_vccnz .LrMID_idle_%=\n"
    "buffer_load_dwordx4 v[32:35], v2, s[36:39], s71 offen sc1\n"
    "buffer_load_dwordx4 v[36:39], v2, s[36:39], s71 offen offset:512 sc1\n"
    "buffer_load_dwordx4 v[40:43], v2, s[36:39], s71 offen offset:2048 sc1\n"
    "buffer_load_dwordx4 v[44:47], v2, s[36:39], s71 offen offset:2560 sc1\n"
    "buffer_load_dwordx4 v[48:51], v2, s[36:39], s72 offen sc1\n"
    "buffer_load_dwordx4 v[52:55], v2, s[36:39], s72 offen offset:512 sc1\n"
    "buffer_load_dwordx4 v[56:59], v2, s[36:39], s72 offen offset:2048 sc1\n"
    "buffer_load_dwordx4 v[60:63], v2, s[36:39], s72 offen offset:2560 sc1\n"
    "buffer_load_dwordx4 v[64:67], v3, s[36:39], s71 offen sc1\n"
    "buffer_load_dwordx4 v[68:71], v6, s[40:43], s73 offen sc1\n"
    "buffer_load_dwordx4 v[72:75], v6, s[40:43], s74 offen sc1\n"
    "buffer_load_dwordx4 v[76:79], v7, s[40:43], s73 offen sc1\n"
    "s_waitcnt vmcnt(12)\n"
    "v_mov_b32 v14, s68\n"
    "s_mov_b64 exec, s[64:65]\n"
    "buffer_atomic_smax v14, v13, s[36:39], 0 offen sc1\n"
    "s_mov_b64 exec, -1\n"
    "s_branch .LrMID_top_%=\n"
    ".LrMID_LOOPE_%=:\n"
    ".LrMID_fin_%=:\n"
    "s_waitcnt vmcnt(0)\n"
    "s_mov_b32 s68, 63\n"
    "v_mov_b32 v14, s68\n"
    "s_mov_b64 exec, s[64:65]\n"
    "buffer_atomic_smax v14, v13, s[36:39], 0 offen sc1\n"
    "s_mov_b64 exec, -1\n"
    "s_waitcnt vmcnt(0)\n"
    "s_barrier\n"
    "s_branch .LrMID_FIN_%=\n"
    ".LrMID_idle_%=:\n"
    "v_add_u32 v120, v123, v1\n"
    "v_cmp_gt_i32 vcc, s69, v120\n"
    "s_nop 0\n"
    "s_or_b64 exec, vcc, 1\n"
    "buffer_atomic_smax v123, v0, s[36:39], 0 offen sc0 sc1\n"
    "s_mov_b64 exec, -1\n"
    "s_waitcnt vmcnt(1)\n"
    "v_mov_b32 v14, s68\n"
    "s_mov_b64 exec, s[64:65]\n"
    "buffer_atomic_smax v14, v13, s[36:39], 0 offen sc1\n"
    "s_mov_b64 exec, -1\n"
    ".LrMID_block_%=:\n"
    "v_add_u32 v120, v122, v1\n"
    "v_cmp_gt_i32 vcc, s69, v120\n"
    "s_nop 0\n"
    "s_or_b64 exec, vcc, 1\n"
    "buffer_atomic_smax v122, v0, s[36:39], 0 offen sc0 sc1\n"
    "s_mov_b64 exec, -1\n"
    "s_sleep 3\n"
    "s_waitcnt vmcnt(1)\n"
    "v_add_u32 v120, v123, v1\n"
    "v_cmp_gt_i32 vcc, s69, v120\n"
    "s_cbranch_vccz .LrMID_go_%=\n"
    "v_add_u32 v120, v123, v1\n"
    "v_cmp_gt_i32 vcc, s69, v120\n"
    "s_nop 0\n"
    "s_or_b64 exec, vcc, 1\n"
    "buffer_atomic_smax v123, v0, s[36:39], 0 offen sc0 sc1\n"
    "s_mov_b64 exec, -1\n"
    "s_sleep 3\n"
    "s_waitcnt vmcnt(1)\n"
    "v_add_u32 v120, v122, v1\n"
    "v_cmp_gt_i32 vcc, s69, v120\n"
    "s_cbranch_vccz .LrMID_go_%=\n"
    "s_sub_u32 s70, s70, 1\n"
    "s_cbranch_scc0 .LrMID_block_%=\n"
    "s_mov_b32 s70, 0\n"
    ".LrMID_go_%=:\n"
    "s_and_b32 s71, s69, 7\n"
    "s_mul_i32 s71, s71, 4194304\n"
    "s_add_u32 s71, s71, s52\n"
    "s_add_u32 s72, s71, 4096\n"
    "s_and_b32 s73, s69, s55\n"
    "s_mul_i32 s73, s73, s54\n"
    "s_add_u32 s73, s73, s53\n"
    "s_add_u32 s74, s73, s56\n"
    "buffer_load_dwordx4 v[32:35], v2, s[36:39], s71 offen sc1\n"
    "buffer_load_dwordx4 v[36:39], v2, s[36:39], s71 offen offset:512 sc1\n"
    "buffer_load_dwordx4 v[40:43], v2, s[36:39], s71 offen offset:2048 sc1\n"
    "buffer_load_dwordx4 v[44:47], v2, s[36:39], s71 offen offset:2560 sc1\n"
    "buffer_load_dwordx4 v[48:51], v2, s[36:39], s72 offen sc1\n"
    "buffer_load_dwordx4 v[52:55], v2, s[36:39], s72 offen offset:512 sc1\n"
    "buffer_load_dwordx4 v[56:59], v2, s[36:39], s72 offen offset:2048 sc1\n"
    "buffer_load_dwordx4 v[60:63], v2, s[36:39], s72 offen offset:2560 sc1\n"
    "buffer_load_dwordx4 v[64:67], v3, s[36:39], s71 offen sc1\n"
    "buffer_load_dwordx4 v[68:71], v6, s[40:43], s73 offen sc1\n"
    "buffer_load_dwordx4 v[72:75], v6, s[40:43], s74 offen sc1\n"
    "buffer_load_dwordx4 v[76:79], v7, s[40:43], s73 offen sc1\n"
    "buffer_load_dword v121, v7, s[40:43], s73 offen\n"
    "s_branch .LrMID_top_%=\n"
    ".LrMID_FIN_%=:\n"
  );
}
DEVI void role_fin(const Params& p, int q, u32 dbg, u64 tag) {
  v16u sa, sb, va, vb;
  red_args<2>(p, 1, q, dbg, tag, sa, sb, va, vb);
  RUN_RED(
    "s_nop 4\n"
    "s_getpc_b64 s[84:85]\n"
    ".LrFIN_PC_%=:\n"
    "s_and_b32 s85, s85, 0xffff\n"
    "s_mov_b32 s86, .LrFIN_FIN_%=-.LrFIN_PC_%=\n"
    "s_mov_b32 s87, 0x20000\n"
    "v_mbcnt_lo_u32_b32 v134, -1, 0\n"
    "v_mbcnt_hi_u32_b32 v134, -1, v134\n"
    "v_lshlrev_b32 v134, 7, v134\n"
    "buffer_load_dword v135, v134, s[84:87], 0 offen\n"
    "v_add_u32 v134, 8192, v134\n"
    "buffer_load_dword v135, v134, s[84:87], 0 offen\n"
    "s_mov_b32 s69, 0\n"
    "v_mov_b32 v122, -1\n"
    "v_mov_b32 v123, -1\n"
    "s_mov_b32 s70, 262144\n"
    ".LrFIN_par_%=:\n"
    "buffer_load_dwordx2 v[14:15], off, s[36:39], 0 sc1\n"
    "s_waitcnt vmcnt(0)\n"
    "v_readfirstlane_b32 s75, v14\n"
    "v_readfirstlane_b32 s76, v15\n"
    "s_xor_b32 s75, s75, s48\n"
    "s_xor_b32 s76, s76, s49\n"
    "s_cselect_b32 s76, 2, 0\n"
    "s_or_b32 s76, s76, s75\n"
    "s_cmp_lt_u32 s76, 2\n"
    "s_cbranch_scc1 .LrFIN_par1_%=\n"
    "s_sleep 4\n"
    "s_sub_u32 s70, s70, 1\n"
    "s_cbranch_scc0 .LrFIN_par_%=\n"
    "s_mov_b32 s70, 0\n"
    ".LrFIN_par1_%=:\n"
    "s_and_b32 s75, s75, 1\n"
    "s_lshl_b32 s75, s75, 13\n"
    "s_add_u32 s36, s36, s75\n"
    "s_addc_u32 s37, s37, 0\n"
    "s_and_b32 s76, s75, s61\n"
    "s_add_u32 s40, s40, s76\n"
    "s_addc_u32 s41, s41, 0\n"
    "s_and_b32 s76, s75, s63\n"
    "s_add_u32 s44, s44, s76\n"
    "s_addc_u32 s45, s45, 0\n"
    "s_branch .LrFIN_block_%=\n"
    ".LrFIN_LOOPB_%=:\n"
    ".LrFIN_top_%=:\n"
    "s_mov_b32 s68, s69\n"
    "s_add_u32 s69, s68, 1\n"
    "s_and_b32 s75, s68, s58\n"
    "s_mul_i32 s77, s75, 36864\n"
    "s_add_u32 s77, s77, s59\n"
    "s_add_u32 s78, s77, s60\n"
    "s_and_b32 s71, s69, 7\n"
    "s_mul_i32 s71, s71, 4194304\n"
    "s_add_u32 s71, s71, s52\n"
    "s_add_u32 s72, s71, 4096\n"
    "s_and_b32 s73, s69, s55\n"
    "s_mul_i32 s73, s73, s54\n"
    "s_add_u32 s73, s73, s53\n"
    "s_add_u32 s74, s73, s56\n"
    "s_waitcnt vmcnt(11)\n"
    "v_mfma_f32_16x16x32_bf16 v[80:83], v[16:19], v[32:35], 0\n"
    "s_waitcnt vmcnt(10)\n"
    "v_mfma_f32_16x16x32_bf16 v[80:83], v[20:23], v[36:39], v[80:83]\n"
    "s_waitcnt vmcnt(9)\n"
    "v_mfma_f32_16x16x32_bf16 v[84:87], v[16:19], v[40:43], 0\n"
    "s_waitcnt vmcnt(8)\n"
    "v_mfma_f32_16x16x32_bf16 v[84:87], v[20:23], v[44:47], v[84:87]\n"
    "s_waitcnt vmcnt(7)\n"
    "v_mfma_f32_16x16x32_bf16 v[88:91], v[16:19], v[48:51], 0\n"
    "s_waitcnt vmcnt(6)\n"
    "v_mfma_f32_16x16x32_bf16 v[88:91], v[20:23], v[52:55], v[88:91]\n"
    "s_waitcnt vmcnt(5)\n"
    "v_mfma_f32_16x16x32_bf16 v[92:95], v[16:19], v[56:59], 0\n"
    "s_waitcnt vmcnt(4)\n"
    "v_mfma_f32_16x16x32_bf16 v[92:95], v[20:23], v[60:63], v[92:95]\n"
    "s_waitcnt vmcnt(3)\n"
    "v_mfma_f32_16x16x32_bf16 v[96:99], v[16:19], v[64:67], 0\n"
    "s_waitcnt vmcnt(2)\n"
    "v_mfma_f32_16x16x32_bf16 v[80:83], v[24:27], v[68:71], v[80:83]\n"
    "v_mfma_f32_16x16x32_bf16 v[84:87], v[28:31], v[68:71], v[84:87]\n"
    "s_waitcnt vmcnt(1)\n"
    "v_mfma_f32_16x16x32_bf16 v[88:91], v[24:27], v[72:75], v[88:91]\n"
    "v_mfma_f32_16x16x32_bf16 v[92:95], v[28:31], v[72:75], v[92:95]\n"
    "s_waitcnt vmcnt(0)\n"
    "v_mfma_f32_16x16x32_bf16 v[96:99], v[24:27], v[76:79], v[96:99]\n"
    "v_add_u32 v120, v122, v1\n"
    "v_cmp_gt_i32 vcc, s69, v120\n"
    "s_nop 0\n"
    "s_or_b64 exec, vcc, 1\n"
    "buffer_atomic_smax v122, v0, s[36:39], 0 offen sc0 sc1\n"
    "s_mov_b64 exec, -1\n"
    "v_mov_b32 v14, s68\n"
    "s_mov_b64 exec, s[64:65]\n"
    "buffer_atomic_smax v14, v13, s[36:39], 0 offen sc1\n"
    "s_mov_b64 exec, -1\n"
    "v_cvt_pk_bf16_f32 v100, v80, v81\n"
    "v_cvt_pk_bf16_f32 v101, v82, v83\n"
    "v_cvt_pk_bf16_f32 v102, v84, v85\n"
    "v_cvt_pk_bf16_f32 v103, v86, v87\n"
    "v_cvt_pk_bf16_f32 v104, v88, v89\n"
    "v_cvt_pk_bf16_f32 v105, v90, v91\n"
    "v_cvt_pk_bf16_f32 v106, v92, v93\n"
    "v_cvt_pk_bf16_f32 v107, v94, v95\n"
    "v_cvt_pk_bf16_f32 v108, v96, v97\n"
    "v_cvt_pk_bf16_f32 v109, v98, v99\n"
    "v_permlane16_swap_b32 v100, v102\n"
    "v_permlane16_swap_b32 v101, v103\n"
    "ds_write_b128 v10, v[100:103]\n"
    "v_permlane16_swap_b32 v104, v106\n"
    "v_permlane16_swap_b32 v105, v107\n"
    "ds_write_b128 v10, v[104:107] offset:128\n"
    "v_permlane16_swap_b32 v108, v110\n"
    "v_permlane16_swap_b32 v109, v111\n"
    "ds_write_b128 v12, v[108:111]\n"
    "v_xor_b32 v10, 16384, v10\n"
    "v_xor_b32 v12, 16384, v12\n"
    "s_waitcnt lgkmcnt(0)\n"
    "s_barrier\n"
    "ds_read_b128 v[100:103], v11 offset:0\n"
    "ds_read_b128 v[104:107], v11 offset:4096\n"
    "ds_read_b128 v[108:111], v11 offset:8192\n"
    "v_xor_b32 v11, 16384, v11\n"
    "s_waitcnt lgkmcnt(2)\n"
    "buffer_store_dwordx4 v[100:103], v4, s[44:47], s77 offen sc1\n"
    "s_waitcnt lgkmcnt(1)\n"
    "buffer_store_dwordx4 v[104:107], v4, s[44:47], s78 offen sc1\n"
    "s_waitcnt lgkmcnt(0)\n"
    "buffer_store_dwordx4 v[108:111], v5, s[44:47], s77 offen sc1\n"
    "s_cmp_eq_u32 s69, 64\n"
    "s_cbranch_scc1 .LrFIN_fin_%=\n"
    "s_waitcnt vmcnt(4)\n"
    "v_add_u32 v120, v122, v1\n"
    "v_cmp_gt_i32 vcc, s69, v120\n"
    "s_cbranch_vccnz .LrFIN_idle_%=\n"
    "buffer_load_dwordx4 v[32:35], v2, s[36:39], s71 offen sc1\n"
    "buffer_load_dwordx4 v[36:39], v2, s[36:39], s71 offen offset:512 sc1\n"
    "buffer_load_dwordx4 v[40:43], v2, s[36:39], s71 offen offset:2048 sc1\n"
    "buffer_load_dwordx4 v[44:47], v2, s[36:39], s71 offen offset:2560 sc1\n"
    "buffer_load_dwordx4 v[48:51], v2, s[36:39], s72 offen sc1\n"
    "buffer_load_dwordx4 v[52:55], v2, s[36:39], s72 offen offset:512 sc1\n"
    "buffer_load_dwordx4 v[56:59], v2, s[36:39], s72 offen offset:2048 sc1\n"
    "buffer_load_dwordx4 v[60:63], v2, s[36:39], s72 offen offset:2560 sc1\n"
    "buffer_load_dwordx4 v[64:67], v3, s[36:39], s71 offen sc1\n"
    "buffer_load_dwordx4 v[68:71], v6, s[40:43], s73 offen sc1\n"
    "buffer_load_dwordx4 v[72:75], v6, s[40:43], s74 offen sc1\n"
    "buffer_load_dwordx4 v[76:79], v7, s[40:43], s73 offen sc1\n"
    "s_branch .LrFIN_top_%=\n"
    ".LrFIN_LOOPE_%=:\n"
    ".LrFIN_fin_%=:\n"
    "s_waitcnt vmcnt(0)\n"
    "s_branch .LrFIN_FIN_%=\n"
    ".LrFIN_idle_%=:\n"
    ".LrFIN_block_%=:\n"
    "v_add_u32 v120, v122, v1\n"
    "v_cmp_gt_i32 vcc, s69, v120\n"
    "s_nop 0\n"
    "s_or_b64 exec, vcc, 1\n"
    "buffer_atomic_smax v122, v0, s[36:39], 0 offen sc0 sc1\n"
    "s_mov_b64 exec, -1\n"
    "s_sleep 3\n"
    "s_waitcnt vmcnt(1)\n"
    "v_add_u32 v120, v123, v1\n"
    "v_cmp_gt_i32 vcc, s69, v120\n"
    "s_cbranch_vccz .LrFIN_go_%=\n"
    "v_add_u32 v120, v123, v1\n"
    "v_cmp_gt_i32 vcc, s69, v120\n"
    "s_nop 0\n"
    "s_or_b64 exec, vcc, 1\n"
    "buffer_atomic_smax v123, v0, s[36:39], 0 offen sc0 sc1\n"
    "s_mov_b64 exec, -1\n"
    "s_sleep 3\n"
    "s_waitcnt vmcnt(1)\n"
    "v_add_u32 v120, v122, v1\n"
    "v_cmp_gt_i32 vcc, s69, v120\n"
    "s_cbranch_vccz .LrFIN_go_%=\n"
    "s_sub_u32 s70, s70, 1\n"
    "s_cbranch_scc0 .LrFIN_block_%=\n"
    "s_mov_b32 s70, 0\n"
    ".LrFIN_go_%=:\n"
    "s_and_b32 s71, s69, 7\n"
    "s_mul_i32 s71, s71, 4194304\n"
    "s_add_u32 s71, s71, s52\n"
    "s_add_u32 s72, s71, 4096\n"
    "s_and_b32 s73, s69, s55\n"
    "s_mul_i32 s73, s73, s54\n"
    "s_add_u32 s73, s73, s53\n"
    "s_add_u32 s74, s73, s56\n"
    "buffer_load_dwordx4 v[32:35], v2, s[36:39], s71 offen sc1\n"
    "buffer_load_dwordx4 v[36:39], v2, s[36:39], s71 offen offset:512 sc1\n"
    "buffer_load_dwordx4 v[40:43], v2, s[36:39], s71 offen offset:2048 sc1\n"
    "buffer_load_dwordx4 v[44:47], v2, s[36:39], s71 offen offset:2560 sc1\n"
    "buffer_load_dwordx4 v[48:51], v2, s[36:39], s72 offen sc1\n"
    "buffer_load_dwordx4 v[52:55], v2, s[36:39], s72 offen offset:512 sc1\n"
    "buffer_load_dwordx4 v[56:59], v2, s[36:39], s72 offen offset:2048 sc1\n"
    "buffer_load_dwordx4 v[60:63], v2, s[36:39], s72 offen offset:2560 sc1\n"
    "buffer_load_dwordx4 v[64:67], v3, s[36:39], s71 offen sc1\n"
    "buffer_load_dwordx4 v[68:71], v6, s[40:43], s73 offen sc1\n"
    "buffer_load_dwordx4 v[72:75], v6, s[40:43], s74 offen sc1\n"
    "buffer_load_dwordx4 v[76:79], v7, s[40:43], s73 offen sc1\n"
    "s_branch .LrFIN_top_%=\n"
    ".LrFIN_FIN_%=:\n"
  );
}

__global__ void __launch_bounds__(256, 1) mega(Params p) {
  extern __shared__ __attribute__((aligned(16))) unsigned char smem[];
  const rsrc_t rw = __builtin_amdgcn_make_buffer_rsrc(p.ws, 0, WS_END + 8192, 0x00020000);
  const unsigned long long t00 = __builtin_amdgcn_s_memrealtime();
  if (threadIdx.x < 64) {
    const int lane = threadIdx.x, b = blockIdx.x;
    if (p.role_mask & 0x40000) st16(rw, (v4u){~0u, ~0u, ~0u, ~0u}, lane < 22 ? CHUNK + (u32)b * 352 + lane * 16 : OOB, 0);
    const u64 tg = ld8u(rw, EPOCH) + 1;
    drain();
    if (lane == 0) {
      {
        const v2u tv = {(u32)tg, (u32)(tg >> 32)};
        const u32 a = READY + (u32)b * 8;
        asm volatile("buffer_atomic_swap_x2 %0, %1, %2, 0 offen sc1" :: "v"(tv), "v"(a), "s"(rw) : "memory");
      }
      int rid = (int)(__builtin_amdgcn_s_getreg(20 | (3 << 11)) & 7u) * 32 + (b >> 3);
      for (int k = 0; k < 256; ++k) {
        if (__hip_atomic_exchange((u64*)(p.ws + CLAIM) + ((rid & 31) * 8 + (rid >> 5)), tg, __ATOMIC_RELAXED, __HIP_MEMORY_SCOPE_AGENT) != tg) break;
        rid = (rid + 1) & 255;
      }
      *(volatile v4u*)smem = (v4u){(u32)rid, (u32)tg, (u32)(tg >> 32), 0u};
    }
  }
  __syncthreads();
  const v4u boot = *(volatile v4u*)smem;
  const int rid = (int)rfl(boot.x);
  const u64 tag = (u64)rfl(boot.y) | ((u64)rfl(boot.z) << 32);
  __syncthreads();
  const int L = rid >> 7, j = (rid >> 5) & 3, sl = rid & 31;
  const u32 dbg = OFF_DBG + (u32)rid * 66 * 256;
  auto on = [&](int r) { return ((p.role_mask >> (2 + 8 * L + r)) & 1) != 0; };
  STAMP(65, 8);
  STAMP(65 + 32, 9);
  if (((PULL >> L) & 1) && sl >= SL_WO && sl < SL_REDA) pull_weights(p, j, sl - SL_WO);
  if (sl < SL_P) { delay_start(L ? DL_R1 : 0, t00); if (on(1)) role_rec(p, L, j, sl, dbg, tag, smem); }
  else if (sl < SL_ATT) { delay_start(L ? DL_R1 : 0, t00); if (on(0)) role_p(p, L, j, sl - SL_P, dbg, tag); }
  else if (sl < SL_WO) {
    const u32 shift = find_shift(rw, rid == 128 + SL_ATT && threadIdx.x < 64, tag);
    if (p.role_mask & 2) hot_init(p, rw, L, 2 * j + sl - SL_ATT, shift);
    if (on(2)) role_att(p, L, j, sl - SL_ATT, dbg, shift);
  }
  else if (sl < SL_UP) { delay_start(L ? DL_W1 : DL_W0, t00); if (on(3)) role_wo(p, L, j, sl - SL_WO, dbg, tag); }
  else if (sl < SL_DOWN) { delay_start(L ? DL_U1 : DL_U0, t00); if (on(5)) role_up(p, L, j, sl - SL_UP, dbg, tag); }
  else if (sl < SL_REDA) { delay_start(L ? DL_D1 : DL_D0, t00); if (on(6)) role_down(p, L, j, sl - SL_DOWN, dbg, tag); }
  else {
    if (sl == SL_REDA && (p.role_mask & 2)) hot_init(p, rw, L, 8 + j, find_shift(rw, false, tag));
    if ((p.role_mask & 1) && (L == 1 || sl == SL_REDF)) role_ln0(p, L == 0 ? 2 : (sl == SL_REDA ? 1 : 0), j, dbg, tag);
    if (sl == SL_REDF && L == 1) {
      if (on(7)) role_fin(p, j, dbg, tag);
    } else {
      v16u sa, sb, va, vb;
      if (sl == SL_REDA) red_args<1>(p, L, j, dbg, tag, sa, sb, va, vb);
      else red_args<2>(p, 0, j, dbg, tag, sa, sb, va, vb);
      if (on(sl == SL_REDA ? 4 : 7)) role_mid(sa, sb, va, vb);
    }
  }
}

static void fill_params(Params& p, void* const* d_in, void* d_out, void* d_ws) {
  p.x = (const u16*)d_in[0]; p.mem0 = (const u16*)d_in[1];
  p.Wq = (const u16*)d_in[2]; p.Wk = (const u16*)d_in[3]; p.Wv = (const u16*)d_in[4]; p.Wo = (const u16*)d_in[5];
  p.w1 = (const u16*)d_in[8]; p.w3 = (const u16*)d_in[9]; p.w2 = (const u16*)d_in[10];
  p.out = (u16*)d_out; p.ws = (unsigned char*)d_ws;
}

extern "C" void kernel_launch(void* const* d_in, const int* in_sizes, int n_in, void* d_out, int out_size, void* d_ws,
                              size_t ws_size, hipStream_t stream) {
  Params p;
  fill_params(p, d_in, d_out, d_ws);
  (void)hipFuncSetAttribute((const void*)mega, hipFuncAttributeMaxDynamicSharedMemorySize, LDS_BYTES);
  p.role_mask = 0x7FFFF; p.unused = 0;
  mega<<<256, 256, LDS_BYTES, stream>>>(p);
}
```

```cpp
#include <hip/hip_runtime.h>
#include <stdint.h>
#include <type_traits>
#include <utility>

typedef unsigned short u16;
typedef unsigned int u32;
typedef short bf16x8 __attribute__((ext_vector_type(8)));
typedef short bf16x4 __attribute__((ext_vector_type(4)));
typedef float f32x4 __attribute__((ext_vector_type(4)));
typedef float f32x2 __attribute__((ext_vector_type(2)));
typedef __bf16 bf2_t __attribute__((ext_vector_type(2)));
typedef unsigned v16u __attribute__((ext_vector_type(16)));
typedef unsigned v8u __attribute__((ext_vector_type(8)));
typedef unsigned v4u __attribute__((ext_vector_type(4)));
typedef unsigned v2u __attribute__((ext_vector_type(2)));
typedef __amdgpu_buffer_rsrc_t rsrc_t;
#define DEVI __device__ __forceinline__

static constexpr int NSEG = 64;
static constexpr int PSLOW = 1, PSKIP = 2, PULL = 1, RQ = 8, RO = 8, RP = 8, RH = 8, RX = 8, RX2 = 16, LN_B = 28, LN_C = 52, PS_ATT = 3, PS_RED = 3, DL_R1 = 12, DL_W0 = 5, DL_W1 = 14, DL_U0 = 8, DL_U1 = 3, DL_D0 = 11, DL_D1 = 5;

static constexpr u32 PARW = 0, EPOCH = 128, READY = 2048, CLAIM = 4096;
static constexpr u32 CHUNK = 16384;
static constexpr u32 MBOX = CHUNK;
static constexpr u32 INITF = 5 * CHUNK;
static constexpr u32 CTRL_BYTES = 6 * CHUNK + 8192;
static constexpr u32 HOT = 6 * CHUNK, HOT2 = 2u << 20;
static constexpr u32 SLOT = 4u << 20;
static constexpr u32 PGA = SLOT, PGB = SLOT + (2u << 20);
static constexpr int cmax(int a, int b) { return a > b ? a : b; }
static constexpr int NSLOT = cmax(cmax(cmax(RQ, RO), cmax(RP, RH)), cmax(RX, RX2));
DEVI u32 pga(int L, int blk) { return PGA + (u32)blk * CHUNK + (u32)L * 8192; }
DEVI u32 pgb(int L, int blk) { return PGB + (u32)blk * CHUNK + (u32)(1 - L) * 8192; }
static constexpr int B_QKV = 0, B_KVM = 96;
static constexpr int B_O = 0, B_H = 32, B_PA = 68, B_PF = 88, B_XN2 = 108, B_X1 = 113, B_XN = 118, B_X = 123;
static constexpr u32 OFF_XN0 = SLOT * (1 + NSLOT);
static constexpr u32 OFF_DBG = OFF_XN0 + 320 * CHUNK;
static constexpr u32 WS_END = OFF_DBG + 256u * 66 * 256;
DEVI u32 kb_off(int kb) { return (u32)((kb >> 3) * CHUNK + (kb & 7) * 1024); }
static constexpr u32 OOB = 0x80000000u;
static constexpr int LDS_BYTES = 163840;
static constexpr unsigned SPIN_LIMIT = 1u << 18;
static constexpr int SL_REC = 0, SL_P = 8, SL_ATT = 16, SL_WO = 18, SL_UP = 21, SL_DOWN = 27, SL_REDA = 30, SL_REDF = 31;
DEVI u32 mbox(int L, int j, int role, int slot) { return MBOX + (u32)(j * CHUNK + L * 8192 + role * 256 + 4 * slot); }
DEVI u32 mbox_att(int L, int j, int hl, int slot) { return mbox(L, j, SL_ATT + (hl >> 2), 16 * (hl & 3) + slot); }

struct Params {
  const u16* x; const u16* mem0; const u16* Wq; const u16* Wk; const u16* Wv; const u16* Wo;
  const u16* w1; const u16* w3; const u16* w2; u16* out; unsigned char* ws;
  int role_mask; int unused;
};

template <class F, int... I> DEVI void static_for_impl(F&& f, std::integer_sequence<int, I...>) { (f(std::integral_constant<int, I>{}), ...); }
template <int N, class F> DEVI void static_for(F&& f) { static_for_impl(f, std::make_integer_sequence<int, N>{}); }
DEVI float bflo(u32 d) { return __uint_as_float(d << 16); }
DEVI float bfhi(u32 d) { return __uint_as_float(d & 0xffff0000u); }
DEVI u32 pk2(float a, float b) {
  f32x2 f = {a, b};
  bf2_t r = __builtin_convertvector(f, bf2_t);
  return __builtin_bit_cast(u32, r);
}
DEVI bf16x8 as_frag(v4u v) { return __builtin_bit_cast(bf16x8, v); }
DEVI bf16x4 as_frag4(v2u v) { return __builtin_bit_cast(bf16x4, v); }
DEVI f32x4 mfma32(v4u a, v4u b, f32x4 c) { return __builtin_amdgcn_mfma_f32_16x16x32_bf16(as_frag(a), as_frag(b), c, 0, 0, 0); }
DEVI f32x4 mfma16(v2u a, v2u b, f32x4 c) { return __builtin_amdgcn_mfma_f32_16x16x16bf16_1k(as_frag4(a), as_frag4(b), c, 0, 0, 0); }
DEVI v4u ld16(rsrc_t r, u32 voff, u32 soff) { return __builtin_amdgcn_raw_buffer_load_b128(r, voff, soff, 16); }
DEVI v2u ld8(rsrc_t r, u32 voff, u32 soff) { return __builtin_amdgcn_raw_buffer_load_b64(r, voff, soff, 16); }
DEVI u32 ld4(rsrc_t r, u32 voff, u32 soff) { return __builtin_amdgcn_raw_buffer_load_b32(r, voff, soff, 16); }
DEVI void st16(rsrc_t r, v4u v, u32 voff, u32 soff) { __builtin_amdgcn_raw_buffer_store_b128(v, r, voff, soff, 16); }
DEVI void st8(rsrc_t r, v2u v, u32 voff, u32 soff) { __builtin_amdgcn_raw_buffer_store_b64(v, r, voff, soff, 16); }
DEVI void st4(rsrc_t r, u32 v, u32 voff, u32 soff) { __builtin_amdgcn_raw_buffer_store_b32(v, r, voff, soff, 16); }
DEVI void drain() { asm volatile("s_waitcnt vmcnt(0)" ::: "memory"); }
DEVI void cbar() { asm volatile("" ::: "memory"); }
DEVI void sbar() { __builtin_amdgcn_sched_barrier(0); }
DEVI u32 rfl(u32 v) { return (u32)__builtin_amdgcn_readfirstlane((int)v); }
DEVI float xmax16_32(float x) {
  u32 a = __float_as_uint(x);
  auto r = __builtin_amdgcn_permlane16_swap(a, a, false, false);
  x = fmaxf(__uint_as_float(r[0]), __uint_as_float(r[1]));
  a = __float_as_uint(x);
  auto q = __builtin_amdgcn_permlane32_swap(a, a, false, false);
  return fmaxf(__uint_as_float(q[0]), __uint_as_float(q[1]));
}
DEVI float xsum16_32(float x) {
  u32 a = __float_as_uint(x);
  auto r = __builtin_amdgcn_permlane16_swap(a, a, false, false);
  x = __uint_as_float(r[0]) + __uint_as_float(r[1]);
  a = __float_as_uint(x);
  auto q = __builtin_amdgcn_permlane32_swap(a, a, false, false);
  return __uint_as_float(q[0]) + __uint_as_float(q[1]);
}
DEVI float ror8(float x) { return __int_as_float(__builtin_amdgcn_update_dpp(0, __float_as_int(x), 0x128, 0xf, 0xf, false)); }
DEVI v4u swap16(v2u a, v2u b) {
  auto s0 = __builtin_amdgcn_permlane16_swap(a.x, b.x, false, false);
  auto s1 = __builtin_amdgcn_permlane16_swap(a.y, b.y, false, false);
  return (v4u){s0[0], s1[0], s0[1], s1[1]};
}

#define STAMP(row, slot) do { } while (0)

DEVI bool flags_ok(int v, int bias, int s) {
  asm volatile("" : "+v"(v));
  return __all(v + bias >= s);
}
template <int SLP> DEVI void wait_flags(rsrc_t r, u32 addr, int bias, int s, unsigned& budget) {
  for (;;) {
    cbar();
    const int v = (int)ld4(r, addr, 0);
    if (flags_ok(v, bias, s) || budget == 0) break;
    --budget;
    __builtin_amdgcn_s_sleep(SLP);
  }
  cbar();
}
DEVI void publish(rsrc_t r, u32 flag, int s, int lane) {
  drain();
  if (lane == 0) st4(r, (u32)s, flag, 0);
  cbar();
}
DEVI void delay_start(int us, unsigned long long t0) {
  if (us == 0) return;
  while (__builtin_amdgcn_s_memrealtime() - t0 < (unsigned long long)us * 100u) __builtin_amdgcn_s_sleep(16);
}
DEVI void pull_weights(const Params& p, int j, int n) {
  const int tid = threadIdx.x;
  u32 acc = 0;
  {
    const u16* w = n % 3 == 0 ? p.Wq : (n % 3 == 1 ? p.Wk : p.Wv);
    const rsrc_t r = __builtin_amdgcn_make_buffer_rsrc((void*)(w + j * 1024), 0, 576 * 8192 - j * 2048, 0x00020000);
    if (PSLOW) {
#pragma unroll
      for (int k = 0; k < 3; ++k) {
        const int row = (k * 4 + n / 3) * 64 + (tid >> 2);
        acc ^= __builtin_amdgcn_raw_buffer_load_b32(r, row < 576 ? (u32)(row * 8192 + 256 + (tid & 1) * 128 + (tid & 2) * 512) : OOB, 0, 0);
      }
    }
#pragma unroll
    for (int k = PSKIP; k < 9; ++k) {
      const int row = (k * 4 + n / 3) * 16 + (tid >> 4);
      acc ^= __builtin_amdgcn_raw_buffer_load_b32(r, (u32)(row * 8192 + (tid & 15) * 128), 0, 0);
    }
  }
  {
    const rsrc_t r = __builtin_amdgcn_make_buffer_rsrc((void*)(p.Wo + (size_t)j * 1024 * 576), 0, 1024 * 1152, 0x00020000);
#pragma unroll
    for (int k = 0; k < 3; ++k) acc ^= __builtin_amdgcn_raw_buffer_load_b32(r, (u32)((k * 12 + n) * 256 + tid) * 128, 0, 0);
  }
  asm volatile("" :: "v"(acc));
}
DEVI v4u make_srd(const void* p, u32 bytes) {
  const unsigned long long a = (unsigned long long)p;
  return (v4u){rfl((u32)a), rfl((u32)(a >> 32) & 0xffffu), bytes, 0x00020000u};
}
DEVI v16u cat4(v4u a, v4u b, v4u c, v4u d) {
  return (v16u){a.x, a.y, a.z, a.w, b.x, b.y, b.z, b.w, c.x, c.y, c.z, c.w, d.x, d.y, d.z, d.w};
}
DEVI u32 wswz(int j, int g) { return (u32)(4 * ((j >> 1) & 1) ^ 2 * (g & 1)); }
DEVI u32 tr_base(int lane, int chunk) {
  const int g = lane >> 4, q = (lane >> 2) & 3;
  return (u32)(g * 1024 + q * 128) + (((u32)chunk ^ wswz(q, g)) << 4);
}

#define RUN_ASM(TEXT) do { for (int i_ = 0; i_ < 16; ++i_) { sa[i_] = rfl(sa[i_]); sb[i_] = rfl(sb[i_]); } \
    asm volatile(TEXT "s_endpgm\n" :: "{s[36:51]}"(sa), "{s[52:67]}"(sb), "{v[0:15]}"(va) : "v255", "a255", "s99", "vcc", "memory"); \
    __builtin_unreachable(); } while (0)

#define GEMM_DECL v4u g_w0, g_w1, g_w2; u32 g_ldw, g_in_base, g_in_stride, g_in_mask, g_out_base, g_out_kb, g_out_mask, g_flag, g_paddr; int g_pbias, g_pinit = -1, g_n0 = 2, g_n1 = 2
#define GEMM_ARGS g_w0, g_w1, g_w2, g_ldw, g_in_base, g_in_stride, g_in_mask, g_out_base, g_out_kb, g_out_mask, g_flag, g_paddr, g_pbias, g_pinit, g_n0, g_n1
DEVI void gemm_args(v4u g_w0, v4u g_w1, v4u g_w2, u32 g_ldw, u32 g_in_base, u32 g_in_stride, u32 g_in_mask, u32 g_out_base, u32 g_out_kb,
                    u32 g_out_mask, u32 g_flag, u32 g_paddr, int g_pbias, int g_pinit, int g_n0, int g_n1, const Params& p, u32 dbg, unsigned long long tag, v16u& sa, v16u& sb, v16u& va) {
  const int tid = threadIdx.x, lane = tid & 63, w = (int)rfl(tid >> 6);
  const int c = lane & 15, gg = lane >> 4, pp = lane & 3;
  sa = cat4(make_srd(p.ws, WS_END), g_w0, g_w1, g_w2);
  sb = (v16u){g_in_base, g_in_stride, g_in_mask, g_out_base, SLOT, g_out_mask, 0, (u32)w * 1024, dbg + w * 64, 0, 0, 0x80000000u, 0, 0, (u32)(2 * tag), (u32)((2 * tag) >> 32)};
  va[0] = lane * 16;
  va[1] = lane * 16 + w * 2048;
  va[2] = lane == 63 ? OOB : g_paddr;
  va[3] = (u32)g_pbias;
  va[4] = (gg & 1) ? OOB : kb_off((int)g_out_kb + 8 + 2 * (w >> 1)) + (u32)(((2 * (w & 1) + (gg >> 1)) * 16 + c) * 16);
  va[5] = (u32)(8 * w + (lane >> 3)) * g_ldw + ((((u32)lane & 7) ^ wswz(lane >> 3, w)) << 4);
  va[6] = (u32)(w >> 1) * 4096 + tr_base(lane, 4 * (w & 1) + pp);
  va[7] = 2 * 4096 + tr_base(lane, 2 * w + (pp >> 1)) + 8 * (pp & 1);
  for (int i = 8; i < 16; ++i) va[i] = 0;
  va[8] = w < 2 ? va[1] : OOB;
  va[11] = (u32)g_pinit;
  va[14] = lane < g_n0 ? g_flag : OOB;
  va[10] = lane < g_n1 ? OOB : g_flag;
  va[15] = kb_off((int)g_out_kb + 2 * w) + lane * 16;
}

DEVI bool confirm(int& rid, unsigned long long& tag, bool& sure, unsigned char* smem) {
  if (sure) return true;
  asm volatile("s_waitcnt lgkmcnt(0)\n\ts_barrier" ::: "memory");
  const v4u boot = *(volatile v4u*)smem;
  const int r = (int)rfl(boot.x);
  tag = (unsigned long long)rfl(boot.y) | ((unsigned long long)rfl(boot.z) << 32);
  asm volatile("s_waitcnt lgkmcnt(0)\n\ts_barrier" ::: "memory");
  const bool ok = r == rid;
  rid = r; sure = true;
  return ok;
}
DEVI void role_p(const Params& p, int L, int j, int i, u32 dbg, int& rid, unsigned long long& tag, bool& sure, unsigned char* smem) {
  const int lane = threadIdx.x & 63, w = threadIdx.x >> 6, h = 8 * j + i;
  const u32 wb = 576u * 4096 * 2 - h * 256;
  GEMM_DECL;
  g_w0 = make_srd(p.Wq + h * 128, wb); g_w1 = make_srd(p.Wk + h * 128, wb); g_w2 = make_srd(p.Wv + h * 128, wb);
  g_ldw = 8192;
  g_in_base = L ? pgb(1, B_XN) : OFF_XN0; g_in_stride = L ? SLOT : 5 * CHUNK; g_in_mask = L ? RX2 - 1 : 63;
  g_out_base = pga(L, B_QKV + 3 * h); g_out_kb = 0; g_out_mask = RQ - 1;
  g_flag = lane == 0 ? mbox(L, j, SL_REC + i, w) : (lane == 1 ? mbox_att(L, j, i, 8 + w) : OOB);
  g_n0 = 0; g_n1 = 0;
  g_paddr = mbox(L, j, SL_P + i, lane);
  g_pbias = lane < (L ? 16 : 48) ? (lane >> 3) & 1 : (lane == 48 ? 2 * RQ : 1000);
  g_pinit = lane < 16 || lane >= 48 ? -1 : (lane < 32 ? 2 * LN_B - 1 : 2 * LN_C - 1);
  v16u sa, sb, va;
  gemm_args(GEMM_ARGS, p, dbg, tag, sa, sb, va);
  asm volatile("" : "+v"(va));
  if (!confirm(rid, tag, sure, smem)) return;
  sb[14] = (u32)(2 * tag); sb[15] = (u32)((2 * tag) >> 32);
  RUN_ASM(
    "s_nop 4\n"
    "s_getpc_b64 s[84:85]\n"
    ".LgP_PC_%=:\n"
    "s_and_b32 s85, s85, 0xffff\n"
    "s_mov_b32 s86, .LgP_FIN_%=-.LgP_PC_%=\n"
    "s_mov_b32 s87, 0x20000\n"
    "v_mbcnt_lo_u32_b32 v40, -1, 0\n"
    "v_mbcnt_hi_u32_b32 v40, -1, v40\n"
    "v_lshlrev_b32 v40, 7, v40\n"
    "buffer_load_dword v41, v40, s[84:87], 0 offen\n"
    "v_add_u32 v40, 8192, v40\n"
    "buffer_load_dword v41, v40, s[84:87], 0 offen\n"
    "v_add_u32 v40, 8192, v40\n"
    "buffer_load_dword v41, v40, s[84:87], 0 offen\n"
    "s_mov_b32 s68, 0\n"
    "s_mov_b32 s71, 0\n"
    "v_add_u32 v9, 20480, v0\n"
    "s_mov_b32 s64, 0xbfb8aa3b\n"
    "s_mov_b32 s72, 262144\n"
    "s_add_u32 m0, s59, 0\n"
    "s_mov_b32 s74, 0\n"
    "buffer_load_dwordx4 v5, s[40:43], s74 offen lds\n"
    "s_add_u32 m0, s59, 4096\n"
    "s_mov_b32 s74, 128\n"
    "buffer_load_dwordx4 v5, s[40:43], s74 offen lds\n"
    "s_add_u32 m0, s59, 8192\n"
    "s_mov_b32 s74, 0\n"
    "buffer_load_dwordx4 v5, s[44:47], s74 offen lds\n"
    "s_add_u32 m0, s59, 12288\n"
    "s_mov_b32 s74, 128\n"
    "buffer_load_dwordx4 v5, s[44:47], s74 offen lds\n"
    "s_add_u32 m0, s59, 16384\n"
    "s_mov_b32 s74, 0\n"
    "buffer_load_dwordx4 v5, s[48:51], s74 offen lds\n"
    "s_add_u32 m0, s59, 20480\n"
    "s_mov_b32 s74, 128\n"
    "buffer_load_dwordx4 v5, s[48:51], s74 offen lds\n"
    "s_add_u32 m0, s59, 24576\n"
    "s_mov_b32 s74, 262144\n"
    "buffer_load_dwordx4 v5, s[40:43], s74 offen lds\n"
    "s_add_u32 m0, s59, 28672\n"
    "s_mov_b32 s74, 262272\n"
    "buffer_load_dwordx4 v5, s[40:43], s74 offen lds\n"
    "s_add_u32 m0, s59, 32768\n"
    "s_mov_b32 s74, 262144\n"
    "buffer_load_dwordx4 v5, s[44:47], s74 offen lds\n"
    "s_add_u32 m0, s59, 36864\n"
    "s_mov_b32 s74, 262272\n"
    "buffer_load_dwordx4 v5, s[44:47], s74 offen lds\n"
    "s_add_u32 m0, s59, 40960\n"
    "s_mov_b32 s74, 262144\n"
    "buffer_load_dwordx4 v5, s[48:51], s74 offen lds\n"
    "s_add_u32 m0, s59, 45056\n"
    "s_mov_b32 s74, 262272\n"
    "buffer_load_dwordx4 v5, s[48:51], s74 offen lds\n"
    "s_add_u32 m0, s59, 49152\n"
    "s_mov_b32 s74, 524288\n"
    "buffer_load_dwordx4 v5, s[40:43], s74 offen lds\n"
    "s_add_u32 m0, s59, 53248\n"
    "s_mov_b32 s74, 524416\n"
    "buffer_load_dwordx4 v5, s[40:43], s74 offen lds\n"
    "s_add_u32 m0, s59, 57344\n"
    "s_mov_b32 s74, 524288\n"
    "buffer_load_dwordx4 v5, s[44:47], s74 offen lds\n"
    "s_add_u32 m0, s59, 61440\n"
    "s_mov_b32 s74, 524416\n"
    "buffer_load_dwordx4 v5, s[44:47], s74 offen lds\n"
    "s_add_u32 m0, s59, 65536\n"
    "s_mov_b32 s74, 524288\n"
    "buffer_load_dwordx4 v5, s[48:51], s74 offen lds\n"
    "s_add_u32 m0, s59, 69632\n"
    "s_mov_b32 s74, 524416\n"
    "buffer_load_dwordx4 v5, s[48:51], s74 offen lds\n"
    "s_add_u32 m0, s59, 73728\n"
    "s_mov_b32 s74, 786432\n"
    "buffer_load_dwordx4 v5, s[40:43], s74 offen lds\n"
    "s_add_u32 m0, s59, 77824\n"
    "s_mov_b32 s74, 786560\n"
    "buffer_load_dwordx4 v5, s[40:43], s74 offen lds\n"
    "s_add_u32 m0, s59, 81920\n"
    "s_mov_b32 s74, 786432\n"
    "buffer_load_dwordx4 v5, s[44:47], s74 offen lds\n"
    "s_add_u32 m0, s59, 86016\n"
    "s_mov_b32 s74, 786560\n"
    "buffer_load_dwordx4 v5, s[44:47], s74 offen lds\n"
    "s_add_u32 m0, s59, 90112\n"
    "s_mov_b32 s74, 786432\n"
    "buffer_load_dwordx4 v5, s[48:51], s74 offen lds\n"
    "s_add_u32 m0, s59, 94208\n"
    "s_mov_b32 s74, 786560\n"
    "buffer_load_dwordx4 v5, s[48:51], s74 offen lds\n"
    "s_add_u32 m0, s59, 98304\n"
    "s_mov_b32 s74, 1048576\n"
    "buffer_load_dwordx4 v5, s[40:43], s74 offen lds\n"
    "s_add_u32 m0, s59, 102400\n"
    "s_mov_b32 s74, 1048704\n"
    "buffer_load_dwordx4 v5, s[40:43], s74 offen lds\n"
    "s_add_u32 m0, s59, 106496\n"
    "s_mov_b32 s74, 1048576\n"
    "buffer_load_dwordx4 v5, s[44:47], s74 offen lds\n"
    "s_add_u32 m0, s59, 110592\n"
    "s_mov_b32 s74, 1048704\n"
    "buffer_load_dwordx4 v5, s[44:47], s74 offen lds\n"
    "s_add_u32 m0, s59, 114688\n"
    "s_mov_b32 s74, 1048576\n"
    "buffer_load_dwordx4 v5, s[48:51], s74 offen lds\n"
    "s_add_u32 m0, s59, 118784\n"
    "s_mov_b32 s74, 1048704\n"
    "buffer_load_dwordx4 v5, s[48:51], s74 offen lds\n"
    "v_add_u32 v16, 49152, v6\n"
    "v_add_u32 v17, 98304, v6\n"
    "v_add_u32 v18, 49152, v7\n"
    "v_add_u32 v19, 98304, v7\n"
    "s_waitcnt vmcnt(24) lgkmcnt(0)\n"
    "s_barrier\n"
    "s_add_u32 m0, s59, 122880\n"
    "s_mov_b32 s74, 1310720\n"
    "buffer_load_dwordx4 v5, s[40:43], s74 offen lds\n"
    "s_add_u32 m0, s59, 126976\n"
    "s_mov_b32 s74, 1310848\n"
    "buffer_load_dwordx4 v5, s[40:43], s74 offen lds\n"
    "s_add_u32 m0, s59, 131072\n"
    "s_mov_b32 s74, 1310720\n"
    "buffer_load_dwordx4 v5, s[44:47], s74 offen lds\n"
    "s_add_u32 m0, s59, 135168\n"
    "s_mov_b32 s74, 1310848\n"
    "buffer_load_dwordx4 v5, s[44:47], s74 offen lds\n"
    "s_add_u32 m0, s59, 139264\n"
    "s_mov_b32 s74, 1310720\n"
    "buffer_load_dwordx4 v5, s[48:51], s74 offen lds\n"
    "s_add_u32 m0, s59, 143360\n"
    "s_mov_b32 s74, 1310848\n"
    "buffer_load_dwordx4 v5, s[48:51], s74 offen lds\n"
    "ds_read_b64_tr_b16 a[0:1], v6 offset:8192\n"
    "ds_read_b64_tr_b16 a[2:3], v6 offset:8704\n"
    "ds_read_b64_tr_b16 a[4:5], v6 offset:8200\n"
    "ds_read_b64_tr_b16 a[6:7], v6 offset:8712\n"
    "ds_read_b64_tr_b16 a[8:9], v6 offset:16384\n"
    "ds_read_b64_tr_b16 a[10:11], v6 offset:16896\n"
    "ds_read_b64_tr_b16 a[12:13], v6 offset:16392\n"
    "ds_read_b64_tr_b16 a[14:15], v6 offset:16904\n"
    "ds_read_b64_tr_b16 a[16:17], v6 offset:0\n"
    "ds_read_b64_tr_b16 a[18:19], v6 offset:512\n"
    "ds_read_b64_tr_b16 a[20:21], v6 offset:8\n"
    "ds_read_b64_tr_b16 a[22:23], v6 offset:520\n"
    "s_waitcnt vmcnt(24) lgkmcnt(0)\n"
    "s_barrier\n"
    "s_add_u32 m0, s59, 0\n"
    "s_mov_b32 s74, 1572864\n"
    "buffer_load_dwordx4 v5, s[40:43], s74 offen lds\n"
    "s_add_u32 m0, s59, 4096\n"
    "s_mov_b32 s74, 1572992\n"
    "buffer_load_dwordx4 v5, s[40:43], s74 offen lds\n"
    "s_add_u32 m0, s59, 8192\n"
    "s_mov_b32 s74, 1572864\n"
    "buffer_load_dwordx4 v5, s[44:47], s74 offen lds\n"
    "s_add_u32 m0, s59, 12288\n"
    "s_mov_b32 s74, 1572992\n"
    "buffer_load_dwordx4 v5, s[44:47], s74 offen lds\n"
    "s_add_u32 m0, s59, 16384\n"
    "s_mov_b32 s74, 1572864\n"
    "buffer_load_dwordx4 v5, s[48:51], s74 offen lds\n"
    "s_add_u32 m0, s59, 20480\n"
    "s_mov_b32 s74, 1572992\n"
    "buffer_load_dwordx4 v5, s[48:51], s74 offen lds\n"
    "ds_read_b64_tr_b16 a[24:25], v6 offset:32768\n"
    "ds_read_b64_tr_b16 a[26:27], v6 offset:33280\n"
    "ds_read_b64_tr_b16 a[28:29], v6 offset:32776\n"
    "ds_read_b64_tr_b16 a[30:31], v6 offset:33288\n"
    "ds_read_b64_tr_b16 a[32:33], v6 offset:40960\n"
    "ds_read_b64_tr_b16 a[34:35], v6 offset:41472\n"
    "ds_read_b64_tr_b16 a[36:37], v6 offset:40968\n"
    "ds_read_b64_tr_b16 a[38:39], v6 offset:41480\n"
    "ds_read_b64_tr_b16 a[40:41], v6 offset:24576\n"
    "ds_read_b64_tr_b16 a[42:43], v6 offset:25088\n"
    "ds_read_b64_tr_b16 a[44:45], v6 offset:24584\n"
    "ds_read_b64_tr_b16 a[46:47], v6 offset:25096\n"
    "s_waitcnt vmcnt(24) lgkmcnt(0)\n"
    "s_barrier\n"
    "s_add_u32 m0, s59, 24576\n"
    "s_mov_b32 s74, 1835008\n"
    "buffer_load_dwordx4 v5, s[40:43], s74 offen lds\n"
    "s_add_u32 m0, s59, 28672\n"
    "s_mov_b32 s74, 1835136\n"
    "buffer_load_dwordx4 v5, s[40:43], s74 offen lds\n"
    "s_add_u32 m0, s59, 32768\n"
    "s_mov_b32 s74, 1835008\n"
    "buffer_load_dwordx4 v5, s[44:47], s74 offen lds\n"
    "s_add_u32 m0, s59, 36864\n"
    "s_mov_b32 s74, 1835136\n"
    "buffer_load_dwordx4 v5, s[44:47], s74 offen lds\n"
    "s_add_u32 m0, s59, 40960\n"
    "s_mov_b32 s74, 1835008\n"
    "buffer_load_dwordx4 v5, s[48:51], s74 offen lds\n"
    "s_add_u32 m0, s59, 45056\n"
    "s_mov_b32 s74, 1835136\n"
    "buffer_load_dwordx4 v5, s[48:51], s74 offen lds\n"
    "ds_read_b64_tr_b16 a[48:49], v16 offset:8192\n"
    "ds_read_b64_tr_b16 a[50:51], v16 offset:8704\n"
    "ds_read_b64_tr_b16 a[52:53], v16 offset:8200\n"
    "ds_read_b64_tr_b16 a[54:55], v16 offset:8712\n"
    "ds_read_b64_tr_b16 a[56:57], v16 offset:16384\n"
    "ds_read_b64_tr_b16 a[58:59], v16 offset:16896\n"
    "ds_read_b64_tr_b16 a[60:61], v16 offset:16392\n"
    "ds_read_b64_tr_b16 a[62:63], v16 offset:16904\n"
    "ds_read_b64_tr_b16 a[64:65], v16 offset:0\n"
    "ds_read_b64_tr_b16 a[66:67], v16 offset:512\n"
    "ds_read_b64_tr_b16 a[68:69], v16 offset:8\n"
    "ds_read_b64_tr_b16 a[70:71], v16 offset:520\n"
    "s_waitcnt vmcnt(24) lgkmcnt(0)\n"
    "s_barrier\n"
    "s_add_u32 m0, s59, 49152\n"
    "s_mov_b32 s74, 2097152\n"
    "buffer_load_dwordx4 v5, s[40:43], s74 offen lds\n"
    "s_add_u32 m0, s59, 53248\n"
    "s_mov_b32 s74, 2097280\n"
    "buffer_load_dwordx4 v5, s[40:43], s74 offen lds\n"
    "s_add_u32 m0, s59, 57344\n"
    "s_mov_b32 s74, 2097152\n"
    "buffer_load_dwordx4 v5, s[44:47], s74 offen lds\n"
    "s_add_u32 m0, s59, 61440\n"
    "s_mov_b32 s74, 2097280\n"
    "buffer_load_dwordx4 v5, s[44:47], s74 offen lds\n"
    "s_add_u32 m0, s59, 65536\n"
    "s_mov_b32 s74, 2097152\n"
    "buffer_load_dwordx4 v5, s[48:51], s74 offen lds\n"
    "s_add_u32 m0, s59, 69632\n"
    "s_mov_b32 s74, 2097280\n"
    "buffer_load_dwordx4 v5, s[48:51], s74 offen lds\n"
    "ds_read_b64_tr_b16 a[72:73], v16 offset:32768\n"
    "ds_read_b64_tr_b16 a[74:75], v16 offset:33280\n"
    "ds_read_b64_tr_b16 a[76:77], v16 offset:32776\n"
    "ds_read_b64_tr_b16 a[78:79], v16 offset:33288\n"
    "ds_read_b64_tr_b16 a[80:81], v16 offset:40960\n"
    "ds_read_b64_tr_b16 a[82:83], v16 offset:41472\n"
    "ds_read_b64_tr_b16 a[84:85], v16 offset:40968\n"
    "ds_read_b64_tr_b16 a[86:87], v16 offset:41480\n"
    "ds_read_b64_tr_b16 a[88:89], v16 offset:24576\n"
    "ds_read_b64_tr_b16 a[90:91], v16 offset:25088\n"
    "ds_read_b64_tr_b16 a[92:93], v16 offset:24584\n"
    "ds_read_b64_tr_b16 a[94:95], v16 offset:25096\n"
    "s_waitcnt vmcnt(24) lgkmcnt(0)\n"
    "s_barrier\n"
    "s_add_u32 m0, s59, 73728\n"
    "s_mov_b32 s74, 2359296\n"
    "buffer_load_dwordx4 v5, s[40:43], s74 offen lds\n"
    "s_add_u32 m0, s59, 77824\n"
    "s_mov_b32 s74, 2359424\n"
    "buffer_load_dwordx4 v5, s[40:43], s74 offen lds\n"
    "s_add_u32 m0, s59, 81920\n"
    "s_mov_b32 s74, 2359296\n"
    "buffer_load_dwordx4 v5, s[44:47], s74 offen lds\n"
    "s_add_u32 m0, s59, 86016\n"
    "s_mov_b32 s74, 2359424\n"
    "buffer_load_dwordx4 v5, s[44:47], s74 offen lds\n"
    "s_add_u32 m0, s59, 90112\n"
    "s_mov_b32 s74, 2359296\n"
    "buffer_load_dwordx4 v5, s[48:51], s74 offen lds\n"
    "s_add_u32 m0, s59, 94208\n"
    "s_mov_b32 s74, 2359424\n"
    "buffer_load_dwordx4 v5, s[48:51], s74 offen lds\n"
    "ds_read_b64_tr_b16 a[96:97], v17 offset:8192\n"
    "ds_read_b64_tr_b16 a[98:99], v17 offset:8704\n"
    "ds_read_b64_tr_b16 a[100:101], v17 offset:8200\n"
    "ds_read_b64_tr_b16 a[102:103], v17 offset:8712\n"
    "ds_read_b64_tr_b16 a[104:105], v17 offset:16384\n"
    "ds_read_b64_tr_b16 a[106:107], v17 offset:16896\n"
    "ds_read_b64_tr_b16 a[108:109], v17 offset:16392\n"
    "ds_read_b64_tr_b16 a[110:111], v17 offset:16904\n"
    "ds_read_b64_tr_b16 a[112:113], v17 offset:0\n"
    "ds_read_b64_tr_b16 a[114:115], v17 offset:512\n"
    "ds_read_b64_tr_b16 a[116:117], v17 offset:8\n"
    "ds_read_b64_tr_b16 a[118:119], v17 offset:520\n"
    "s_waitcnt vmcnt(24) lgkmcnt(0)\n"
    "s_barrier\n"
    "s_add_u32 m0, s59, 98304\n"
    "s_mov_b32 s74, 2621440\n"
    "buffer_load_dwordx4 v5, s[40:43], s74 offen lds\n"
    "s_add_u32 m0, s59, 102400\n"
    "s_mov_b32 s74, 2621568\n"
    "buffer_load_dwordx4 v5, s[40:43], s74 offen lds\n"
    "s_add_u32 m0, s59, 106496\n"
    "s_mov_b32 s74, 2621440\n"
    "buffer_load_dwordx4 v5, s[44:47], s74 offen lds\n"
    "s_add_u32 m0, s59, 110592\n"
    "s_mov_b32 s74, 2621568\n"
    "buffer_load_dwordx4 v5, s[44:47], s74 offen lds\n"
    "s_add_u32 m0, s59, 114688\n"
    "s_mov_b32 s74, 2621440\n"
    "buffer_load_dwordx4 v5, s[48:51], s74 offen lds\n"
    "s_add_u32 m0, s59, 118784\n"
    "s_mov_b32 s74, 2621568\n"
    "buffer_load_dwordx4 v5, s[48:51], s74 offen lds\n"
    "ds_read_b64_tr_b16 a[120:121], v17 offset:32768\n"
    "ds_read_b64_tr_b16 a[122:123], v17 offset:33280\n"
    "ds_read_b64_tr_b16 a[124:125], v17 offset:32776\n"
    "ds_read_b64_tr_b16 a[126:127], v17 offset:33288\n"
    "ds_read_b64_tr_b16 a[128:129], v17 offset:40960\n"
    "ds_read_b64_tr_b16 a[130:131], v17 offset:41472\n"
    "ds_read_b64_tr_b16 a[132:133], v17 offset:40968\n"
    "ds_read_b64_tr_b16 a[134:135], v17 offset:41480\n"
    "ds_read_b64_tr_b16 a[136:137], v17 offset:24576\n"
    "ds_read_b64_tr_b16 a[138:139], v17 offset:25088\n"
    "ds_read_b64_tr_b16 a[140:141], v17 offset:24584\n"
    "ds_read_b64_tr_b16 a[142:143], v17 offset:25096\n"
    "s_waitcnt vmcnt(24) lgkmcnt(0)\n"
    "s_barrier\n"
    "s_add_u32 m0, s59, 122880\n"
    "s_mov_b32 s74, 2883584\n"
    "buffer_load_dwordx4 v5, s[40:43], s74 offen lds\n"
    "s_add_u32 m0, s59, 126976\n"
    "s_mov_b32 s74, 2883712\n"
    "buffer_load_dwordx4 v5, s[40:43], s74 offen lds\n"
    "s_add_u32 m0, s59, 131072\n"
    "s_mov_b32 s74, 2883584\n"
    "buffer_load_dwordx4 v5, s[44:47], s74 offen lds\n"
    "s_add_u32 m0, s59, 135168\n"
    "s_mov_b32 s74, 2883712\n"
    "buffer_load_dwordx4 v5, s[44:47], s74 offen lds\n"
    "s_add_u32 m0, s59, 139264\n"
    "s_mov_b32 s74, 2883584\n"
    "buffer_load_dwordx4 v5, s[48:51], s74 offen lds\n"
    "s_add_u32 m0, s59, 143360\n"
    "s_mov_b32 s74, 2883712\n"
    "buffer_load_dwordx4 v5, s[48:51], s74 offen lds\n"
    "ds_read_b64_tr_b16 a[144:145], v6 offset:8192\n"
    "ds_read_b64_tr_b16 a[146:147], v6 offset:8704\n"
    "ds_read_b64_tr_b16 a[148:149], v6 offset:8200\n"
    "ds_read_b64_tr_b16 a[150:151], v6 offset:8712\n"
    "ds_read_b64_tr_b16 a[152:153], v6 offset:16384\n"
    "ds_read_b64_tr_b16 a[154:155], v6 offset:16896\n"
    "ds_read_b64_tr_b16 a[156:157], v6 offset:16392\n"
    "ds_read_b64_tr_b16 a[158:159], v6 offset:16904\n"
    "ds_read_b64_tr_b16 a[160:161], v6 offset:0\n"
    "ds_read_b64_tr_b16 a[162:163], v6 offset:512\n"
    "ds_read_b64_tr_b16 a[164:165], v6 offset:8\n"
    "ds_read_b64_tr_b16 a[166:167], v6 offset:520\n"
    "s_waitcnt vmcnt(24) lgkmcnt(0)\n"
    "s_barrier\n"
    "s_add_u32 m0, s59, 0\n"
    "s_mov_b32 s74, 3145728\n"
    "buffer_load_dwordx4 v5, s[40:43], s74 offen lds\n"
    "s_add_u32 m0, s59, 4096\n"
    "s_mov_b32 s74, 3145856\n"
    "buffer_load_dwordx4 v5, s[40:43], s74 offen lds\n"
    "s_add_u32 m0, s59, 8192\n"
    "s_mov_b32 s74, 3145728\n"
    "buffer_load_dwordx4 v5, s[44:47], s74 offen lds\n"
    "s_add_u32 m0, s59, 12288\n"
    "s_mov_b32 s74, 3145856\n"
    "buffer_load_dwordx4 v5, s[44:47], s74 offen lds\n"
    "s_add_u32 m0, s59, 16384\n"
    "s_mov_b32 s74, 3145728\n"
    "buffer_load_dwordx4 v5, s[48:51], s74 offen lds\n"
    "s_add_u32 m0, s59, 20480\n"
    "s_mov_b32 s74, 3145856\n"
    "buffer_load_dwordx4 v5, s[48:51], s74 offen lds\n"
    "ds_read_b64_tr_b16 a[168:169], v6 offset:32768\n"
    "ds_read_b64_tr_b16 a[170:171], v6 offset:33280\n"
    "ds_read_b64_tr_b16 a[172:173], v6 offset:32776\n"
    "ds_read_b64_tr_b16 a[174:175], v6 offset:33288\n"
    "ds_read_b64_tr_b16 a[176:177], v6 offset:40960\n"
    "ds_read_b64_tr_b16 a[178:179], v6 offset:41472\n"
    "ds_read_b64_tr_b16 a[180:181], v6 offset:40968\n"
    "ds_read_b64_tr_b16 a[182:183], v6 offset:41480\n"
    "ds_read_b64_tr_b16 a[184:185], v6 offset:24576\n"
    "ds_read_b64_tr_b16 a[186:187], v6 offset:25088\n"
    "ds_read_b64_tr_b16 a[188:189], v6 offset:24584\n"
    "ds_read_b64_tr_b16 a[190:191], v6 offset:25096\n"
    "s_waitcnt vmcnt(24) lgkmcnt(0)\n"
    "s_barrier\n"
    "s_add_u32 m0, s59, 24576\n"
    "s_mov_b32 s74, 3407872\n"
    "buffer_load_dwordx4 v5, s[40:43], s74 offen lds\n"
    "s_add_u32 m0, s59, 28672\n"
    "s_mov_b32 s74, 3408000\n"
    "buffer_load_dwordx4 v5, s[40:43], s74 offen lds\n"
    "s_add_u32 m0, s59, 32768\n"
    "s_mov_b32 s74, 3407872\n"
    "buffer_load_dwordx4 v5, s[44:47], s74 offen lds\n"
    "s_add_u32 m0, s59, 36864\n"
    "s_mov_b32 s74, 3408000\n"
    "buffer_load_dwordx4 v5, s[44:47], s74 offen lds\n"
    "s_add_u32 m0, s59, 40960\n"
    "s_mov_b32 s74, 3407872\n"
    "buffer_load_dwordx4 v5, s[48:51], s74 offen lds\n"
    "s_add_u32 m0, s59, 45056\n"
    "s_mov_b32 s74, 3408000\n"
    "buffer_load_dwordx4 v5, s[48:51], s74 offen lds\n"
    "ds_read_b64_tr_b16 a[192:193], v16 offset:8192\n"
    "ds_read_b64_tr_b16 a[194:195], v16 offset:8704\n"
    "ds_read_b64_tr_b16 a[196:197], v16 offset:8200\n"
    "ds_read_b64_tr_b16 a[198:199], v16 offset:8712\n"
    "ds_read_b64_tr_b16 a[200:201], v16 offset:16384\n"
    "ds_read_b64_tr_b16 a[202:203], v16 offset:16896\n"
    "ds_read_b64_tr_b16 a[204:205], v16 offset:16392\n"
    "ds_read_b64_tr_b16 a[206:207], v16 offset:16904\n"
    "ds_read_b64_tr_b16 a[208:209], v16 offset:0\n"
    "ds_read_b64_tr_b16 a[210:211], v16 offset:512\n"
    "ds_read_b64_tr_b16 a[212:213], v16 offset:8\n"
    "ds_read_b64_tr_b16 a[214:215], v16 offset:520\n"
    "s_waitcnt vmcnt(24) lgkmcnt(0)\n"
    "s_barrier\n"
    "s_add_u32 m0, s59, 49152\n"
    "s_mov_b32 s74, 3670016\n"
    "buffer_load_dwordx4 v5, s[40:43], s74 offen lds\n"
    "s_add_u32 m0, s59, 53248\n"
    "s_mov_b32 s74, 3670144\n"
    "buffer_load_dwordx4 v5, s[40:43], s74 offen lds\n"
    "s_add_u32 m0, s59, 57344\n"
    "s_mov_b32 s74, 3670016\n"
    "buffer_load_dwordx4 v5, s[44:47], s74 offen lds\n"
    "s_add_u32 m0, s59, 61440\n"
    "s_mov_b32 s74, 3670144\n"
    "buffer_load_dwordx4 v5, s[44:47], s74 offen lds\n"
    "s_add_u32 m0, s59, 65536\n"
    "s_mov_b32 s74, 3670016\n"
    "buffer_load_dwordx4 v5, s[48:51], s74 offen lds\n"
    "s_add_u32 m0, s59, 69632\n"
    "s_mov_b32 s74, 3670144\n"
    "buffer_load_dwordx4 v5, s[48:51], s74 offen lds\n"
    "ds_read_b64_tr_b16 a[216:217], v16 offset:32768\n"
    "ds_read_b64_tr_b16 a[218:219], v16 offset:33280\n"
    "ds_read_b64_tr_b16 a[220:221], v16 offset:32776\n"
    "ds_read_b64_tr_b16 a[222:223], v16 offset:33288\n"
    "ds_read_b64_tr_b16 a[224:225], v16 offset:40960\n"
    "ds_read_b64_tr_b16 a[226:227], v16 offset:41472\n"
    "ds_read_b64_tr_b16 a[228:229], v16 offset:40968\n"
    "ds_read_b64_tr_b16 a[230:231], v16 offset:41480\n"
    "ds_read_b64_tr_b16 a[232:233], v16 offset:24576\n"
    "ds_read_b64_tr_b16 a[234:235], v16 offset:25088\n"
    "ds_read_b64_tr_b16 a[236:237], v16 offset:24584\n"
    "ds_read_b64_tr_b16 a[238:239], v16 offset:25096\n"
    "s_waitcnt vmcnt(24) lgkmcnt(0)\n"
    "s_barrier\n"
    "s_add_u32 m0, s59, 73728\n"
    "s_mov_b32 s74, 3932160\n"
    "buffer_load_dwordx4 v5, s[40:43], s74 offen lds\n"
    "s_add_u32 m0, s59, 77824\n"
    "s_mov_b32 s74, 3932288\n"
    "buffer_load_dwordx4 v5, s[40:43], s74 offen lds\n"
    "s_add_u32 m0, s59, 81920\n"
    "s_mov_b32 s74, 3932160\n"
    "buffer_load_dwordx4 v5, s[44:47], s74 offen lds\n"
    "s_add_u32 m0, s59, 86016\n"
    "s_mov_b32 s74, 3932288\n"
    "buffer_load_dwordx4 v5, s[44:47], s74 offen lds\n"
    "s_add_u32 m0, s59, 90112\n"
    "s_mov_b32 s74, 3932160\n"
    "buffer_load_dwordx4 v5, s[48:51], s74 offen lds\n"
    "s_add_u32 m0, s59, 94208\n"
    "s_mov_b32 s74, 3932288\n"
    "buffer_load_dwordx4 v5, s[48:51], s74 offen lds\n"
    "ds_read_b64_tr_b16 a[240:241], v17 offset:8192\n"
    "ds_read_b64_tr_b16 a[242:243], v17 offset:8704\n"
    "ds_read_b64_tr_b16 a[244:245], v17 offset:8200\n"
    "ds_read_b64_tr_b16 a[246:247], v17 offset:8712\n"
    "ds_read_b64_tr_b16 a[248:249], v17 offset:16384\n"
    "ds_read_b64_tr_b16 a[250:251], v17 offset:16896\n"
    "ds_read_b64_tr_b16 a[252:253], v17 offset:16392\n"
    "ds_read_b64_tr_b16 a[254:255], v17 offset:16904\n"
    "ds_read_b64_tr_b16 v[80:81], v17 offset:0\n"
    "ds_read_b64_tr_b16 v[82:83], v17 offset:512\n"
    "ds_read_b64_tr_b16 v[84:85], v17 offset:8\n"
    "ds_read_b64_tr_b16 v[86:87], v17 offset:520\n"
    "s_waitcnt vmcnt(24) lgkmcnt(0)\n"
    "s_barrier\n"
    "s_add_u32 m0, s59, 98304\n"
    "s_mov_b32 s74, 4194304\n"
    "buffer_load_dwordx4 v5, s[40:43], s74 offen lds\n"
    "s_add_u32 m0, s59, 102400\n"
    "s_mov_b32 s74, 4194432\n"
    "buffer_load_dwordx4 v5, s[40:43], s74 offen lds\n"
    "s_add_u32 m0, s59, 106496\n"
    "s_mov_b32 s74, 4194304\n"
    "buffer_load_dwordx4 v5, s[44:47], s74 offen lds\n"
    "s_add_u32 m0, s59, 110592\n"
    "s_mov_b32 s74, 4194432\n"
    "buffer_load_dwordx4 v5, s[44:47], s74 offen lds\n"
    "s_add_u32 m0, s59, 114688\n"
    "s_mov_b32 s74, 4194304\n"
    "buffer_load_dwordx4 v5, s[48:51], s74 offen lds\n"
    "s_add_u32 m0, s59, 118784\n"
    "s_mov_b32 s74, 4194432\n"
    "buffer_load_dwordx4 v5, s[48:51], s74 offen lds\n"
    "ds_read_b64_tr_b16 v[88:89], v17 offset:32768\n"
    "ds_read_b64_tr_b16 v[90:91], v17 offset:33280\n"
    "ds_read_b64_tr_b16 v[92:93], v17 offset:32776\n"
    "ds_read_b64_tr_b16 v[94:95], v17 offset:33288\n"
    "ds_read_b64_tr_b16 v[96:97], v17 offset:40960\n"
    "ds_read_b64_tr_b16 v[98:99], v17 offset:41472\n"
    "ds_read_b64_tr_b16 v[100:101], v17 offset:40968\n"
    "ds_read_b64_tr_b16 v[102:103], v17 offset:41480\n"
    "ds_read_b64_tr_b16 v[104:105], v17 offset:24576\n"
    "ds_read_b64_tr_b16 v[106:107], v17 offset:25088\n"
    "ds_read_b64_tr_b16 v[108:109], v17 offset:24584\n"
    "ds_read_b64_tr_b16 v[110:111], v17 offset:25096\n"
    "s_waitcnt vmcnt(24) lgkmcnt(0)\n"
    "s_barrier\n"
    "s_add_u32 m0, s59, 122880\n"
    "s_mov_b32 s74, 4456448\n"
    "buffer_load_dwordx4 v5, s[40:43], s74 offen lds\n"
    "s_add_u32 m0, s59, 126976\n"
    "s_mov_b32 s74, 4456576\n"
    "buffer_load_dwordx4 v5, s[40:43], s74 offen lds\n"
    "s_add_u32 m0, s59, 131072\n"
    "s_mov_b32 s74, 4456448\n"
    "buffer_load_dwordx4 v5, s[44:47], s74 offen lds\n"
    "s_add_u32 m0, s59, 135168\n"
    "s_mov_b32 s74, 4456576\n"
    "buffer_load_dwordx4 v5, s[44:47], s74 offen lds\n"
    "s_add_u32 m0, s59, 139264\n"
    "s_mov_b32 s74, 4456448\n"
    "buffer_load_dwordx4 v5, s[48:51], s74 offen lds\n"
    "s_add_u32 m0, s59, 143360\n"
    "s_mov_b32 s74, 4456576\n"
    "buffer_load_dwordx4 v5, s[48:51], s74 offen lds\n"
    "buffer_load_dwordx2 v[12:13], off, s[36:39], 0 sc1\n"
    "ds_read_b64_tr_b16 v[112:113], v6 offset:8192\n"
    "ds_read_b64_tr_b16 v[114:115], v6 offset:8704\n"
    "ds_read_b64_tr_b16 v[116:117], v6 offset:8200\n"
    "ds_read_b64_tr_b16 v[118:119], v6 offset:8712\n"
    "ds_read_b64_tr_b16 v[120:121], v6 offset:16384\n"
    "ds_read_b64_tr_b16 v[122:123], v6 offset:16896\n"
    "ds_read_b64_tr_b16 v[124:125], v6 offset:16392\n"
    "ds_read_b64_tr_b16 v[126:127], v6 offset:16904\n"
    "ds_read_b64_tr_b16 v[128:129], v6 offset:0\n"
    "ds_read_b64_tr_b16 v[130:131], v6 offset:512\n"
    "ds_read_b64_tr_b16 v[132:133], v6 offset:8\n"
    "ds_read_b64_tr_b16 v[134:135], v6 offset:520\n"
    "s_waitcnt vmcnt(25) lgkmcnt(0)\n"
    "s_barrier\n"
    "ds_read_b64_tr_b16 v[136:137], v6 offset:32768\n"
    "ds_read_b64_tr_b16 v[138:139], v6 offset:33280\n"
    "ds_read_b64_tr_b16 v[140:141], v6 offset:32776\n"
    "ds_read_b64_tr_b16 v[142:143], v6 offset:33288\n"
    "ds_read_b64_tr_b16 v[144:145], v6 offset:40960\n"
    "ds_read_b64_tr_b16 v[146:147], v6 offset:41472\n"
    "ds_read_b64_tr_b16 v[148:149], v6 offset:40968\n"
    "ds_read_b64_tr_b16 v[150:151], v6 offset:41480\n"
    "ds_read_b64_tr_b16 v[152:153], v6 offset:24576\n"
    "ds_read_b64_tr_b16 v[154:155], v6 offset:25088\n"
    "ds_read_b64_tr_b16 v[156:157], v6 offset:24584\n"
    "ds_read_b64_tr_b16 v[158:159], v6 offset:25096\n"
    "s_waitcnt vmcnt(19) lgkmcnt(0)\n"
    "s_barrier\n"
    "ds_read_b64_tr_b16 v[160:161], v16 offset:8192\n"
    "ds_read_b64_tr_b16 v[162:163], v16 offset:8704\n"
    "ds_read_b64_tr_b16 v[164:165], v16 offset:8200\n"
    "ds_read_b64_tr_b16 v[166:167], v16 offset:8712\n"
    "ds_read_b64_tr_b16 v[168:169], v16 offset:16384\n"
    "ds_read_b64_tr_b16 v[170:171], v16 offset:16896\n"
    "ds_read_b64_tr_b16 v[172:173], v16 offset:16392\n"
    "ds_read_b64_tr_b16 v[174:175], v16 offset:16904\n"
    "ds_read_b64_tr_b16 v[176:177], v16 offset:0\n"
    "ds_read_b64_tr_b16 v[178:179], v16 offset:512\n"
    "ds_read_b64_tr_b16 v[180:181], v16 offset:8\n"
    "ds_read_b64_tr_b16 v[182:183], v16 offset:520\n"
    "s_waitcnt vmcnt(13) lgkmcnt(0)\n"
    "s_barrier\n"
    "ds_read_b64_tr_b16 v[184:185], v16 offset:32768\n"
    "ds_read_b64_tr_b16 v[186:187], v16 offset:33280\n"
    "ds_read_b64_tr_b16 v[188:189], v16 offset:32776\n"
    "ds_read_b64_tr_b16 v[190:191], v16 offset:33288\n"
    "ds_read_b64_tr_b16 v[192:193], v16 offset:40960\n"
    "ds_read_b64_tr_b16 v[194:195], v16 offset:41472\n"
    "ds_read_b64_tr_b16 v[196:197], v16 offset:40968\n"
    "ds_read_b64_tr_b16 v[198:199], v16 offset:41480\n"
    "ds_read_b64_tr_b16 v[200:201], v16 offset:24576\n"
    "ds_read_b64_tr_b16 v[202:203], v16 offset:25088\n"
    "ds_read_b64_tr_b16 v[204:205], v16 offset:24584\n"
    "ds_read_b64_tr_b16 v[206:207], v16 offset:25096\n"
    "s_waitcnt vmcnt(7) lgkmcnt(0)\n"
    "s_barrier\n"
    "ds_read_b64_tr_b16 v[208:209], v17 offset:8192\n"
    "ds_read_b64_tr_b16 v[210:211], v17 offset:8704\n"
    "ds_read_b64_tr_b16 v[212:213], v17 offset:8200\n"
    "ds_read_b64_tr_b16 v[214:215], v17 offset:8712\n"
    "ds_read_b64_tr_b16 v[216:217], v17 offset:16384\n"
    "ds_read_b64_tr_b16 v[218:219], v17 offset:16896\n"
    "ds_read_b64_tr_b16 v[220:221], v17 offset:16392\n"
    "ds_read_b64_tr_b16 v[222:223], v17 offset:16904\n"
    "ds_read_b64_tr_b16 v[224:225], v17 offset:0\n"
    "ds_read_b64_tr_b16 v[226:227], v17 offset:512\n"
    "ds_read_b64_tr_b16 v[228:229], v17 offset:8\n"
    "ds_read_b64_tr_b16 v[230:231], v17 offset:520\n"
    "s_waitcnt vmcnt(0) lgkmcnt(0)\n"
    "s_barrier\n"
    ".LgP_par_%=:\n"
    "v_readfirstlane_b32 s74, v12\n"
    "v_readfirstlane_b32 s75, v13\n"
    "s_xor_b32 s74, s74, s66\n"
    "s_xor_b32 s75, s75, s67\n"
    "s_cselect_b32 s75, 2, 0\n"
    "s_or_b32 s75, s75, s74\n"
    "s_cmp_lt_u32 s75, 2\n"
    "s_cbranch_scc1 .LgP_par1_%=\n"
    "s_sleep 4\n"
    "buffer_load_dwordx2 v[12:13], off, s[36:39], 0 sc1\n"
    "s_waitcnt vmcnt(0)\n"
    "s_sub_u32 s72, s72, 1\n"
    "s_cbranch_scc0 .LgP_par_%=\n"
    "s_mov_b32 s72, 0\n"
    ".LgP_par1_%=:\n"
    "s_and_b32 s74, s74, 1\n"
    "s_lshl_b32 s74, s74, 13\n"
    "s_add_u32 s36, s36, s74\n"
    "s_addc_u32 s37, s37, 0\n"
    "v_add_u32 v13, v11, v3\n"
    "v_cmp_gt_i32 vcc, s71, v13\n"
    "s_nop 0\n"
    "s_or_b64 exec, vcc, s[62:63]\n"
    "buffer_atomic_smax v11, v2, s[36:39], 0 offen sc0 sc1\n"
    "s_mov_b64 exec, -1\n"
    "s_getpc_b64 s[84:85]\n"
    ".LgP_PC2_%=:\n"
    "s_and_b32 s85, s85, 0xffff\n"
    "s_mov_b32 s86, .LgP_FIN_%=-.LgP_PC2_%=\n"
    "s_mov_b32 s87, 0x20000\n"
    "v_mbcnt_lo_u32_b32 v42, -1, 0\n"
    "v_mbcnt_hi_u32_b32 v42, -1, v42\n"
    "v_lshlrev_b32 v42, 7, v42\n"
    "buffer_load_dword v43, v42, s[84:87], 0 offen\n"
    "v_add_u32 v42, 8192, v42\n"
    "buffer_load_dword v43, v42, s[84:87], 0 offen\n"
    "v_add_u32 v42, 8192, v42\n"
    "buffer_load_dword v43, v42, s[84:87], 0 offen\n"
    "ds_read_b64_tr_b16 v[232:233], v17 offset:32768\n"
    "ds_read_b64_tr_b16 v[234:235], v17 offset:33280\n"
    "ds_read_b64_tr_b16 v[236:237], v17 offset:32776\n"
    "ds_read_b64_tr_b16 v[238:239], v17 offset:33288\n"
    "ds_read_b64_tr_b16 v[240:241], v17 offset:40960\n"
    "ds_read_b64_tr_b16 v[242:243], v17 offset:41472\n"
    "ds_read_b64_tr_b16 v[244:245], v17 offset:40968\n"
    "ds_read_b64_tr_b16 v[246:247], v17 offset:41480\n"
    "ds_read_b64_tr_b16 v[248:249], v17 offset:24576\n"
    "ds_read_b64_tr_b16 v[250:251], v17 offset:25088\n"
    "ds_read_b64_tr_b16 v[252:253], v17 offset:24584\n"
    "ds_read_b64_tr_b16 v[254:255], v17 offset:25096\n"
    "s_waitcnt lgkmcnt(0)\n"
    "s_barrier\n"
    "s_waitcnt vmcnt(0)\n"
    "v_mov_b32 v5, v11\n"
    "v_mov_b32 v12, -1\n"
    "s_mov_b32 s73, 0x80000000\n"
    "s_mov_b32 s79, 0x80000000\n"
    "s_mov_b32 s80, 0x80000000\n"
    "s_branch .LgP_block1_1_%=\n"
    ".LgP_LOOPB_%=:\n"
    ".LgP_head0_%=:\n"
    "v_mfma_f32_16x16x32_bf16 v[32:35], a[0:3], v[16:19], 0\n"
    "v_mfma_f32_16x16x32_bf16 v[36:39], a[4:7], v[16:19], 0\n"
    "v_cvt_pk_bf16_f32 v40, v40, v41\n"
    "v_cvt_pk_bf16_f32 v41, v42, v43\n"
    "v_mfma_f32_16x16x32_bf16 v[48:51], v[16:19], a[8:11], 0\n"
    "v_cvt_pk_bf16_f32 v42, v44, v45\n"
    "v_cvt_pk_bf16_f32 v43, v46, v47\n"
    "v_mfma_f32_16x16x32_bf16 v[56:59], v[16:19], a[12:15], 0\n"
    "buffer_store_dwordx4 v[40:43], v1, s[36:39], s79 offen offset:1024 sc1\n"
    "v_cvt_pk_bf16_f32 v55, v54, v55\n"
    "v_mfma_f32_16x16x32_bf16 v[64:67], a[16:19], v[16:19], 0\n"
    "s_waitcnt lgkmcnt(1)\n"
    "s_waitcnt vmcnt(2)\n"
    "v_cvt_pk_bf16_f32 v54, v52, v53\n"
    "v_mov_b64 v[52:53], v[28:29]\n"
    "v_add_u32 v13, v11, v3\n"
    "v_mfma_f32_16x16x32_bf16 v[68:71], a[20:23], v[16:19], 0\n"
    "ds_read_b128 v[16:19], v0 offset:3072\n"
    "v_cvt_pk_bf16_f32 v63, v62, v63\n"
    "v_cmp_gt_i32 vcc, s71, v13\n"
    "v_mfma_f32_16x16x32_bf16 v[32:35], a[24:27], v[20:23], v[32:35]\n"
    "buffer_store_dwordx4 v[52:55], v1, s[36:39], s80 offen sc1\n"
    "v_cvt_pk_bf16_f32 v62, v60, v61\n"
    "v_mfma_f32_16x16x32_bf16 v[36:39], a[28:31], v[20:23], v[36:39]\n"
    "v_mov_b64 v[60:61], v[30:31]\n"
    "v_mfma_f32_16x16x32_bf16 v[48:51], v[20:23], a[32:35], v[48:51]\n"
    "v_cvt_pk_bf16_f32 v72, v72, v73\n"
    "v_cvt_pk_bf16_f32 v73, v74, v75\n"
    "s_cbranch_vccnz .LgP_poll0_%=\n"
    "v_mfma_f32_16x16x32_bf16 v[56:59], v[20:23], a[36:39], v[56:59]\n"
    "s_add_u32 m0, s59, 20480\n"
    "buffer_store_dwordx4 v[60:63], v1, s[36:39], s80 offen offset:1024 sc1\n"
    "v_cvt_pk_bf16_f32 v74, v76, v77\n"
    "v_mfma_f32_16x16x32_bf16 v[64:67], a[40:43], v[20:23], v[64:67]\n"
    "s_waitcnt lgkmcnt(1)\n"
    "buffer_load_dwordx4 v1, s[36:39], s76 offen sc1 lds\n"
    "v_cvt_pk_bf16_f32 v75, v78, v79\n"
    "v_mfma_f32_16x16x32_bf16 v[68:71], a[44:47], v[20:23], v[68:71]\n"
    "ds_read_b128 v[20:23], v0 offset:4096\n"
    "v_mfma_f32_16x16x32_bf16 v[32:35], a[48:51], v[24:27], v[32:35]\n"
    "buffer_store_dwordx4 v[72:75], v1, s[36:39], s73 offen offset:1024 sc1\n"
    "v_mfma_f32_16x16x32_bf16 v[36:39], a[52:55], v[24:27], v[36:39]\n"
    "v_mfma_f32_16x16x32_bf16 v[48:51], v[24:27], a[56:59], v[48:51]\n"
    "s_lshr_b32 s73, s68, 1\n"
    "v_mfma_f32_16x16x32_bf16 v[56:59], v[24:27], a[60:63], v[56:59]\n"
    "v_mfma_f32_16x16x32_bf16 v[64:67], a[64:67], v[24:27], v[64:67]\n"
    "s_waitcnt lgkmcnt(1)\n"
    "v_mfma_f32_16x16x32_bf16 v[68:71], a[68:71], v[24:27], v[68:71]\n"
    "ds_read_b128 v[24:27], v0 offset:5120\n"
    "v_mfma_f32_16x16x32_bf16 v[32:35], a[72:75], v[16:19], v[32:35]\n"
    "s_and_b32 s73, s73, s57\n"
    "v_mfma_f32_16x16x32_bf16 v[36:39], a[76:79], v[16:19], v[36:39]\n"
    "v_mfma_f32_16x16x32_bf16 v[48:51], v[16:19], a[80:83], v[48:51]\n"
    "s_mul_i32 s73, s73, s56\n"
    "v_mfma_f32_16x16x32_bf16 v[56:59], v[16:19], a[84:87], v[56:59]\n"
    "s_add_u32 m0, s59, 24576\n"
    "s_add_u32 s76, s76, 16384\n"
    "v_mfma_f32_16x16x32_bf16 v[64:67], a[88:91], v[16:19], v[64:67]\n"
    "s_waitcnt lgkmcnt(1)\n"
    "buffer_load_dwordx4 v1, s[36:39], s76 offen sc1 lds\n"
    "v_mfma_f32_16x16x32_bf16 v[68:71], a[92:95], v[16:19], v[68:71]\n"
    "ds_read_b128 v[16:19], v0 offset:6144\n"
    "v_mfma_f32_16x16x32_bf16 v[32:35], a[96:99], v[20:23], v[32:35]\n"
    "s_add_u32 s73, s73, s55\n"
    "v_mfma_f32_16x16x32_bf16 v[36:39], a[100:103], v[20:23], v[36:39]\n"
    "v_mfma_f32_16x16x32_bf16 v[48:51], v[20:23], a[104:107], v[48:51]\n"
    "s_add_u32 s79, s73, 16384\n"
    "v_mfma_f32_16x16x32_bf16 v[56:59], v[20:23], a[108:111], v[56:59]\n"
    "v_mfma_f32_16x16x32_bf16 v[64:67], a[112:115], v[20:23], v[64:67]\n"
    "s_waitcnt lgkmcnt(1)\n"
    "v_mfma_f32_16x16x32_bf16 v[68:71], a[116:119], v[20:23], v[68:71]\n"
    "ds_read_b128 v[20:23], v0 offset:7168\n"
    "v_mfma_f32_16x16x32_bf16 v[32:35], a[120:123], v[24:27], v[32:35]\n"
    "s_add_u32 s80, s73, 32768\n"
    "s_waitcnt vmcnt(8)\n"
    "buffer_atomic_smax v12, v10, s[36:39], s69 offen sc1\n"
    "v_mfma_f32_16x16x32_bf16 v[36:39], a[124:127], v[24:27], v[36:39]\n"
    "v_mfma_f32_16x16x32_bf16 v[48:51], v[24:27], a[128:131], v[48:51]\n"
    "v_mfma_f32_16x16x32_bf16 v[56:59], v[24:27], a[132:135], v[56:59]\n"
    "s_add_u32 m0, s59, 28672\n"
    "s_add_u32 s76, s76, 16384\n"
    "v_mfma_f32_16x16x32_bf16 v[64:67], a[136:139], v[24:27], v[64:67]\n"
    "s_waitcnt lgkmcnt(1)\n"
    "buffer_load_dwordx4 v1, s[36:39], s76 offen sc1 lds\n"
    "v_mfma_f32_16x16x32_bf16 v[68:71], a[140:143], v[24:27], v[68:71]\n"
    "ds_read_b128 v[24:27], v0 offset:8192\n"
    "v_mfma_f32_16x16x32_bf16 v[32:35], a[144:147], v[16:19], v[32:35]\n"
    "v_mfma_f32_16x16x32_bf16 v[36:39], a[148:151], v[16:19], v[36:39]\n"
    "v_mfma_f32_16x16x32_bf16 v[48:51], v[16:19], a[152:155], v[48:51]\n"
    "v_mfma_f32_16x16x32_bf16 v[56:59], v[16:19], a[156:159], v[56:59]\n"
    "v_mfma_f32_16x16x32_bf16 v[64:67], a[160:163], v[16:19], v[64:67]\n"
    "s_waitcnt lgkmcnt(1)\n"
    "v_mfma_f32_16x16x32_bf16 v[68:71], a[164:167], v[16:19], v[68:71]\n"
    "ds_read_b128 v[16:19], v0 offset:9216\n"
    "v_mfma_f32_16x16x32_bf16 v[32:35], a[168:171], v[20:23], v[32:35]\n"
    "v_mfma_f32_16x16x32_bf16 v[36:39], a[172:175], v[20:23], v[36:39]\n"
    "v_mfma_f32_16x16x32_bf16 v[48:51], v[20:23], a[176:179], v[48:51]\n"
    "v_mfma_f32_16x16x32_bf16 v[56:59], v[20:23], a[180:183], v[56:59]\n"
    "s_add_u32 m0, s59, 32768\n"
    "s_add_u32 s76, s76, 16384\n"
    "v_mfma_f32_16x16x32_bf16 v[64:67], a[184:187], v[20:23], v[64:67]\n"
    "s_waitcnt lgkmcnt(1)\n"
    "buffer_load_dwordx4 v1, s[36:39], s76 offen sc1 lds\n"
    "v_mfma_f32_16x16x32_bf16 v[68:71], a[188:191], v[20:23], v[68:71]\n"
    "ds_read_b128 v[20:23], v0 offset:10240\n"
    "v_mfma_f32_16x16x32_bf16 v[32:35], a[192:195], v[24:27], v[32:35]\n"
    "v_mfma_f32_16x16x32_bf16 v[36:39], a[196:199], v[24:27], v[36:39]\n"
    "v_mfma_f32_16x16x32_bf16 v[48:51], v[24:27], a[200:203], v[48:51]\n"
    "v_mfma_f32_16x16x32_bf16 v[56:59], v[24:27], a[204:207], v[56:59]\n"
    "v_mfma_f32_16x16x32_bf16 v[64:67], a[208:211], v[24:27], v[64:67]\n"
    "s_waitcnt lgkmcnt(1)\n"
    "s_waitcnt vmcnt(4)\n"
    "buffer_atomic_smax v12, v10, s[36:39], s70 offen sc1\n"
    "v_mfma_f32_16x16x32_bf16 v[68:71], a[212:215], v[24:27], v[68:71]\n"
    "ds_read_b128 v[24:27], v0 offset:11264\n"
    "v_mfma_f32_16x16x32_bf16 v[32:35], a[216:219], v[16:19], v[32:35]\n"
    "v_mfma_f32_16x16x32_bf16 v[36:39], a[220:223], v[16:19], v[36:39]\n"
    "v_mfma_f32_16x16x32_bf16 v[48:51], v[16:19], a[224:227], v[48:51]\n"
    "v_mfma_f32_16x16x32_bf16 v[56:59], v[16:19], a[228:231], v[56:59]\n"
    "s_add_u32 m0, s59, 36864\n"
    "s_add_u32 s76, s76, 16384\n"
    "v_mfma_f32_16x16x32_bf16 v[64:67], a[232:235], v[16:19], v[64:67]\n"
    "s_waitcnt lgkmcnt(1)\n"
    "buffer_load_dwordx4 v8, s[36:39], s76 offen sc1 lds\n"
    "v_mfma_f32_16x16x32_bf16 v[68:71], a[236:239], v[16:19], v[68:71]\n"
    "ds_read_b128 v[16:19], v0 offset:12288\n"
    "v_mfma_f32_16x16x32_bf16 v[32:35], a[240:243], v[20:23], v[32:35]\n"
    "v_mfma_f32_16x16x32_bf16 v[36:39], a[244:247], v[20:23], v[36:39]\n"
    "v_mfma_f32_16x16x32_bf16 v[48:51], v[20:23], a[248:251], v[48:51]\n"
    "v_mfma_f32_16x16x32_bf16 v[56:59], v[20:23], a[252:255], v[56:59]\n"
    "v_mfma_f32_16x16x32_bf16 v[64:67], v[80:83], v[20:23], v[64:67]\n"
    "s_waitcnt lgkmcnt(1)\n"
    "v_mfma_f32_16x16x32_bf16 v[68:71], v[84:87], v[20:23], v[68:71]\n"
    "ds_read_b128 v[20:23], v0 offset:13312\n"
    "v_mfma_f32_16x16x32_bf16 v[32:35], v[88:91], v[24:27], v[32:35]\n"
    "v_mfma_f32_16x16x32_bf16 v[36:39], v[92:95], v[24:27], v[36:39]\n"
    "v_mfma_f32_16x16x32_bf16 v[48:51], v[24:27], v[96:99], v[48:51]\n"
    "v_mfma_f32_16x16x32_bf16 v[56:59], v[24:27], v[100:103], v[56:59]\n"
    "v_mfma_f32_16x16x32_bf16 v[64:67], v[104:107], v[24:27], v[64:67]\n"
    "s_waitcnt lgkmcnt(1)\n"
    "v_mfma_f32_16x16x32_bf16 v[68:71], v[108:111], v[24:27], v[68:71]\n"
    "ds_read_b128 v[24:27], v0 offset:14336\n"
    ".LgP_join0_%=:\n"
    "v_mfma_f32_16x16x32_bf16 v[32:35], v[112:115], v[16:19], v[32:35]\n"
    "s_add_u32 s71, s68, 2\n"
    "s_waitcnt vmcnt(5)\n"
    "v_mfma_f32_16x16x32_bf16 v[36:39], v[116:119], v[16:19], v[36:39]\n"
    "v_add_u32 v13, v11, v3\n"
    "v_mfma_f32_16x16x32_bf16 v[48:51], v[16:19], v[120:123], v[48:51]\n"
    "v_cmp_gt_i32 vcc, s71, v13\n"
    "v_mfma_f32_16x16x32_bf16 v[56:59], v[16:19], v[124:127], v[56:59]\n"
    "v_mfma_f32_16x16x32_bf16 v[64:67], v[128:131], v[16:19], v[64:67]\n"
    "s_waitcnt lgkmcnt(1)\n"
    "v_mfma_f32_16x16x32_bf16 v[68:71], v[132:135], v[16:19], v[68:71]\n"
    "ds_read_b128 v[16:19], v0 offset:15360\n"
    "s_or_b64 exec, vcc, s[62:63]\n"
    "buffer_atomic_smax v11, v2, s[36:39], 0 offen sc0 sc1\n"
    "s_mov_b64 exec, -1\n"
    "v_mfma_f32_16x16x32_bf16 v[32:35], v[136:139], v[20:23], v[32:35]\n"
    "v_mfma_f32_16x16x32_bf16 v[36:39], v[140:143], v[20:23], v[36:39]\n"
    "s_lshr_b32 s76, s71, 1\n"
    "v_mfma_f32_16x16x32_bf16 v[48:51], v[20:23], v[144:147], v[48:51]\n"
    "s_and_b32 s76, s76, s54\n"
    "v_mfma_f32_16x16x32_bf16 v[56:59], v[20:23], v[148:151], v[56:59]\n"
    "s_mul_i32 s76, s76, s53\n"
    "v_mfma_f32_16x16x32_bf16 v[64:67], v[152:155], v[20:23], v[64:67]\n"
    "s_waitcnt lgkmcnt(1)\n"
    "s_add_u32 s76, s76, s52\n"
    "v_mfma_f32_16x16x32_bf16 v[68:71], v[156:159], v[20:23], v[68:71]\n"
    "ds_read_b128 v[20:23], v0 offset:16384\n"
    "v_mfma_f32_16x16x32_bf16 v[32:35], v[160:163], v[24:27], v[32:35]\n"
    "v_mfma_f32_16x16x32_bf16 v[36:39], v[164:167], v[24:27], v[36:39]\n"
    "v_mfma_f32_16x16x32_bf16 v[48:51], v[24:27], v[168:171], v[48:51]\n"
    "v_mfma_f32_16x16x32_bf16 v[56:59], v[24:27], v[172:175], v[56:59]\n"
    "v_mfma_f32_16x16x32_bf16 v[64:67], v[176:179], v[24:27], v[64:67]\n"
    "s_waitcnt lgkmcnt(1)\n"
    "v_mfma_f32_16x16x32_bf16 v[68:71], v[180:183], v[24:27], v[68:71]\n"
    "ds_read_b128 v[24:27], v0 offset:17408\n"
    "v_mfma_f32_16x16x32_bf16 v[32:35], v[184:187], v[16:19], v[32:35]\n"
    "v_mfma_f32_16x16x32_bf16 v[36:39], v[188:191], v[16:19], v[36:39]\n"
    "v_mfma_f32_16x16x32_bf16 v[48:51], v[16:19], v[192:195], v[48:51]\n"
    "v_mfma_f32_16x16x32_bf16 v[56:59], v[16:19], v[196:199], v[56:59]\n"
    "s_waitcnt vmcnt(1) lgkmcnt(0)\n"
    "s_barrier\n"
    "v_add_u32 v13, v5, v3\n"
    "v_mfma_f32_16x16x32_bf16 v[64:67], v[200:203], v[16:19], v[64:67]\n"
    "v_cmp_gt_i32 vcc, s71, v13\n"
    "v_mfma_f32_16x16x32_bf16 v[68:71], v[204:207], v[16:19], v[68:71]\n"
    "ds_read_b128 v[16:19], v9 offset:0\n"
    "v_mfma_f32_16x16x32_bf16 v[32:35], v[208:211], v[20:23], v[32:35]\n"
    "v_mfma_f32_16x16x32_bf16 v[36:39], v[212:215], v[20:23], v[36:39]\n"
    "s_or_b64 exec, vcc, s[62:63]\n"
    "buffer_atomic_smax v5, v2, s[36:39], 0 offen sc0 sc1\n"
    "s_mov_b64 exec, -1\n"
    "v_mfma_f32_16x16x32_bf16 v[48:51], v[20:23], v[216:219], v[48:51]\n"
    "v_mfma_f32_16x16x32_bf16 v[56:59], v[20:23], v[220:223], v[56:59]\n"
    "v_mfma_f32_16x16x32_bf16 v[64:67], v[224:227], v[20:23], v[64:67]\n"
    "v_mfma_f32_16x16x32_bf16 v[68:71], v[228:231], v[20:23], v[68:71]\n"
    "ds_read_b128 v[20:23], v9 offset:1024\n"
    "v_mfma_f32_16x16x32_bf16 v[32:35], v[232:235], v[24:27], v[32:35]\n"
    "v_mfma_f32_16x16x32_bf16 v[36:39], v[236:239], v[24:27], v[36:39]\n"
    "v_mfma_f32_16x16x32_bf16 v[48:51], v[24:27], v[240:243], v[48:51]\n"
    "s_mov_b32 s69, 0x80000000\n"
    "v_mfma_f32_16x16x32_bf16 v[56:59], v[24:27], v[244:247], v[56:59]\n"
    "s_mov_b32 s70, 0\n"
    "v_mfma_f32_16x16x32_bf16 v[64:67], v[248:251], v[24:27], v[64:67]\n"
    "s_waitcnt lgkmcnt(1)\n"
    "v_mov_b32 v12, s68\n"
    "v_mfma_f32_16x16x32_bf16 v[68:71], v[252:255], v[24:27], v[68:71]\n"
    "ds_read_b128 v[24:27], v9 offset:2048\n"
    "s_add_u32 s68, s68, 1\n"
    ".LgP_head1_%=:\n"
    "v_mfma_f32_16x16x32_bf16 v[40:43], a[0:3], v[16:19], 0\n"
    "v_mfma_f32_16x16x32_bf16 v[44:47], a[4:7], v[16:19], 0\n"
    "v_cvt_pk_bf16_f32 v32, v32, v33\n"
    "v_cvt_pk_bf16_f32 v33, v34, v35\n"
    "v_mfma_f32_16x16x32_bf16 v[52:55], v[16:19], a[8:11], 0\n"
    "v_cvt_pk_bf16_f32 v34, v36, v37\n"
    "v_cvt_pk_bf16_f32 v35, v38, v39\n"
    "v_mfma_f32_16x16x32_bf16 v[60:63], v[16:19], a[12:15], 0\n"
    "buffer_store_dwordx4 v[32:35], v1, s[36:39], s79 offen sc1\n"
    "v_cvt_pk_bf16_f32 v28, v48, v49\n"
    "v_mfma_f32_16x16x32_bf16 v[72:75], a[16:19], v[16:19], 0\n"
    "s_waitcnt lgkmcnt(1)\n"
    "s_waitcnt vmcnt(2)\n"
    "v_cvt_pk_bf16_f32 v29, v50, v51\n"
    "v_add_u32 v13, v11, v3\n"
    "v_mfma_f32_16x16x32_bf16 v[76:79], a[20:23], v[16:19], 0\n"
    "ds_read_b128 v[16:19], v9 offset:3072\n"
    "v_cvt_pk_bf16_f32 v30, v56, v57\n"
    "v_cmp_gt_i32 vcc, s71, v13\n"
    "v_mfma_f32_16x16x32_bf16 v[40:43], a[24:27], v[20:23], v[40:43]\n"
    "v_cvt_pk_bf16_f32 v31, v58, v59\n"
    "v_mfma_f32_16x16x32_bf16 v[44:47], a[28:31], v[20:23], v[44:47]\n"
    "v_cvt_pk_bf16_f32 v64, v64, v65\n"
    "v_cvt_pk_bf16_f32 v65, v66, v67\n"
    "v_mfma_f32_16x16x32_bf16 v[52:55], v[20:23], a[32:35], v[52:55]\n"
    "v_cvt_pk_bf16_f32 v66, v68, v69\n"
    "v_cvt_pk_bf16_f32 v67, v70, v71\n"
    "s_cbranch_vccnz .LgP_poll1_%=\n"
    "v_mfma_f32_16x16x32_bf16 v[60:63], v[20:23], a[36:39], v[60:63]\n"
    "s_add_u32 m0, s59, 0\n"
    "buffer_store_dwordx4 v[64:67], v1, s[36:39], s73 offen sc1\n"
    "v_mfma_f32_16x16x32_bf16 v[72:75], a[40:43], v[20:23], v[72:75]\n"
    "s_waitcnt lgkmcnt(1)\n"
    "buffer_load_dwordx4 v1, s[36:39], s76 offen sc1 lds\n"
    "v_mfma_f32_16x16x32_bf16 v[76:79], a[44:47], v[20:23], v[76:79]\n"
    "ds_read_b128 v[20:23], v9 offset:4096\n"
    "v_mfma_f32_16x16x32_bf16 v[40:43], a[48:51], v[24:27], v[40:43]\n"
    "s_lshr_b32 s73, s68, 1\n"
    "v_mfma_f32_16x16x32_bf16 v[44:47], a[52:55], v[24:27], v[44:47]\n"
    "v_mfma_f32_16x16x32_bf16 v[52:55], v[24:27], a[56:59], v[52:55]\n"
    "s_and_b32 s73, s73, s57\n"
    "v_mfma_f32_16x16x32_bf16 v[60:63], v[24:27], a[60:63], v[60:63]\n"
    "v_mfma_f32_16x16x32_bf16 v[72:75], a[64:67], v[24:27], v[72:75]\n"
    "s_waitcnt lgkmcnt(1)\n"
    "v_mfma_f32_16x16x32_bf16 v[76:79], a[68:71], v[24:27], v[76:79]\n"
    "ds_read_b128 v[24:27], v9 offset:5120\n"
    "v_mfma_f32_16x16x32_bf16 v[40:43], a[72:75], v[16:19], v[40:43]\n"
    "s_mul_i32 s73, s73, s56\n"
    "v_mfma_f32_16x16x32_bf16 v[44:47], a[76:79], v[16:19], v[44:47]\n"
    "v_mfma_f32_16x16x32_bf16 v[52:55], v[16:19], a[80:83], v[52:55]\n"
    "s_add_u32 s73, s73, s55\n"
    "v_mfma_f32_16x16x32_bf16 v[60:63], v[16:19], a[84:87], v[60:63]\n"
    "s_add_u32 m0, s59, 4096\n"
    "s_add_u32 s76, s76, 16384\n"
    "v_mfma_f32_16x16x32_bf16 v[72:75], a[88:91], v[16:19], v[72:75]\n"
    "s_waitcnt lgkmcnt(1)\n"
    "buffer_load_dwordx4 v1, s[36:39], s76 offen sc1 lds\n"
    "v_mfma_f32_16x16x32_bf16 v[76:79], a[92:95], v[16:19], v[76:79]\n"
    "ds_read_b128 v[16:19], v9 offset:6144\n"
    "v_mfma_f32_16x16x32_bf16 v[40:43], a[96:99], v[20:23], v[40:43]\n"
    "s_add_u32 s79, s73, 16384\n"
    "v_mfma_f32_16x16x32_bf16 v[44:47], a[100:103], v[20:23], v[44:47]\n"
    "v_mfma_f32_16x16x32_bf16 v[52:55], v[20:23], a[104:107], v[52:55]\n"
    "s_add_u32 s80, s73, 32768\n"
    "v_mfma_f32_16x16x32_bf16 v[60:63], v[20:23], a[108:111], v[60:63]\n"
    "v_mfma_f32_16x16x32_bf16 v[72:75], a[112:115], v[20:23], v[72:75]\n"
    "s_waitcnt lgkmcnt(1)\n"
    "v_mfma_f32_16x16x32_bf16 v[76:79], a[116:119], v[20:23], v[76:79]\n"
    "ds_read_b128 v[20:23], v9 offset:7168\n"
    "v_mfma_f32_16x16x32_bf16 v[40:43], a[120:123], v[24:27], v[40:43]\n"
    "s_waitcnt vmcnt(6)\n"
    "buffer_atomic_smax v12, v14, s[36:39], s69 offen sc1\n"
    "v_mfma_f32_16x16x32_bf16 v[44:47], a[124:127], v[24:27], v[44:47]\n"
    "v_mfma_f32_16x16x32_bf16 v[52:55], v[24:27], a[128:131], v[52:55]\n"
    "v_mfma_f32_16x16x32_bf16 v[60:63], v[24:27], a[132:135], v[60:63]\n"
    "s_add_u32 m0, s59, 8192\n"
    "s_add_u32 s76, s76, 16384\n"
    "v_mfma_f32_16x16x32_bf16 v[72:75], a[136:139], v[24:27], v[72:75]\n"
    "s_waitcnt lgkmcnt(1)\n"
    "buffer_load_dwordx4 v1, s[36:39], s76 offen sc1 lds\n"
    "v_mfma_f32_16x16x32_bf16 v[76:79], a[140:143], v[24:27], v[76:79]\n"
    "ds_read_b128 v[24:27], v9 offset:8192\n"
    "v_mfma_f32_16x16x32_bf16 v[40:43], a[144:147], v[16:19], v[40:43]\n"
    "v_mfma_f32_16x16x32_bf16 v[44:47], a[148:151], v[16:19], v[44:47]\n"
    "v_mfma_f32_16x16x32_bf16 v[52:55], v[16:19], a[152:155], v[52:55]\n"
    "v_mfma_f32_16x16x32_bf16 v[60:63], v[16:19], a[156:159], v[60:63]\n"
    "v_mfma_f32_16x16x32_bf16 v[72:75], a[160:163], v[16:19], v[72:75]\n"
    "s_waitcnt lgkmcnt(1)\n"
    "v_mfma_f32_16x16x32_bf16 v[76:79], a[164:167], v[16:19], v[76:79]\n"
    "ds_read_b128 v[16:19], v9 offset:9216\n"
    "v_mfma_f32_16x16x32_bf16 v[40:43], a[168:171], v[20:23], v[40:43]\n"
    "v_mfma_f32_16x16x32_bf16 v[44:47], a[172:175], v[20:23], v[44:47]\n"
    "v_mfma_f32_16x16x32_bf16 v[52:55], v[20:23], a[176:179], v[52:55]\n"
    "v_mfma_f32_16x16x32_bf16 v[60:63], v[20:23], a[180:183], v[60:63]\n"
    "s_add_u32 m0, s59, 12288\n"
    "s_add_u32 s76, s76, 16384\n"
    "v_mfma_f32_16x16x32_bf16 v[72:75], a[184:187], v[20:23], v[72:75]\n"
    "s_waitcnt lgkmcnt(1)\n"
    "buffer_load_dwordx4 v1, s[36:39], s76 offen sc1 lds\n"
    "v_mfma_f32_16x16x32_bf16 v[76:79], a[188:191], v[20:23], v[76:79]\n"
    "ds_read_b128 v[20:23], v9 offset:10240\n"
    "v_mfma_f32_16x16x32_bf16 v[40:43], a[192:195], v[24:27], v[40:43]\n"
    "v_mfma_f32_16x16x32_bf16 v[44:47], a[196:199], v[24:27], v[44:47]\n"
    "s_waitcnt vmcnt(5)\n"
    "buffer_atomic_smax v12, v14, s[36:39], s70 offen sc1\n"
    "v_mfma_f32_16x16x32_bf16 v[52:55], v[24:27], a[200:203], v[52:55]\n"
    "v_mfma_f32_16x16x32_bf16 v[60:63], v[24:27], a[204:207], v[60:63]\n"
    "v_mfma_f32_16x16x32_bf16 v[72:75], a[208:211], v[24:27], v[72:75]\n"
    "s_waitcnt lgkmcnt(1)\n"
    "v_mfma_f32_16x16x32_bf16 v[76:79], a[212:215], v[24:27], v[76:79]\n"
    "ds_read_b128 v[24:27], v9 offset:11264\n"
    "v_mfma_f32_16x16x32_bf16 v[40:43], a[216:219], v[16:19], v[40:43]\n"
    "v_mfma_f32_16x16x32_bf16 v[44:47], a[220:223], v[16:19], v[44:47]\n"
    "v_mfma_f32_16x16x32_bf16 v[52:55], v[16:19], a[224:227], v[52:55]\n"
    "v_mfma_f32_16x16x32_bf16 v[60:63], v[16:19], a[228:231], v[60:63]\n"
    "s_add_u32 m0, s59, 16384\n"
    "s_add_u32 s76, s76, 16384\n"
    "v_mfma_f32_16x16x32_bf16 v[72:75], a[232:235], v[16:19], v[72:75]\n"
    "s_waitcnt lgkmcnt(1)\n"
    "buffer_load_dwordx4 v8, s[36:39], s76 offen sc1 lds\n"
    "v_mfma_f32_16x16x32_bf16 v[76:79], a[236:239], v[16:19], v[76:79]\n"
    "ds_read_b128 v[16:19], v9 offset:12288\n"
    "v_mfma_f32_16x16x32_bf16 v[40:43], a[240:243], v[20:23], v[40:43]\n"
    "v_mfma_f32_16x16x32_bf16 v[44:47], a[244:247], v[20:23], v[44:47]\n"
    "v_mfma_f32_16x16x32_bf16 v[52:55], v[20:23], a[248:251], v[52:55]\n"
    "v_mfma_f32_16x16x32_bf16 v[60:63], v[20:23], a[252:255], v[60:63]\n"
    "v_mfma_f32_16x16x32_bf16 v[72:75], v[80:83], v[20:23], v[72:75]\n"
    "s_waitcnt lgkmcnt(1)\n"
    "v_mfma_f32_16x16x32_bf16 v[76:79], v[84:87], v[20:23], v[76:79]\n"
    "ds_read_b128 v[20:23], v9 offset:13312\n"
    "v_mfma_f32_16x16x32_bf16 v[40:43], v[88:91], v[24:27], v[40:43]\n"
    "v_mfma_f32_16x16x32_bf16 v[44:47], v[92:95], v[24:27], v[44:47]\n"
    "v_mfma_f32_16x16x32_bf16 v[52:55], v[24:27], v[96:99], v[52:55]\n"
    "v_mfma_f32_16x16x32_bf16 v[60:63], v[24:27], v[100:103], v[60:63]\n"
    "v_mfma_f32_16x16x32_bf16 v[72:75], v[104:107], v[24:27], v[72:75]\n"
    "s_waitcnt lgkmcnt(1)\n"
    "v_mfma_f32_16x16x32_bf16 v[76:79], v[108:111], v[24:27], v[76:79]\n"
    "ds_read_b128 v[24:27], v9 offset:14336\n"
    ".LgP_join1_%=:\n"
    "v_mfma_f32_16x16x32_bf16 v[40:43], v[112:115], v[16:19], v[40:43]\n"
    "s_add_u32 s71, s68, 2\n"
    "s_waitcnt vmcnt(5)\n"
    "v_mfma_f32_16x16x32_bf16 v[44:47], v[116:119], v[16:19], v[44:47]\n"
    "v_add_u32 v13, v11, v3\n"
    "v_mfma_f32_16x16x32_bf16 v[52:55], v[16:19], v[120:123], v[52:55]\n"
    "v_cmp_gt_i32 vcc, s71, v13\n"
    "v_mfma_f32_16x16x32_bf16 v[60:63], v[16:19], v[124:127], v[60:63]\n"
    "v_mfma_f32_16x16x32_bf16 v[72:75], v[128:131], v[16:19], v[72:75]\n"
    "s_waitcnt lgkmcnt(1)\n"
    "v_mfma_f32_16x16x32_bf16 v[76:79], v[132:135], v[16:19], v[76:79]\n"
    "ds_read_b128 v[16:19], v9 offset:15360\n"
    "s_or_b64 exec, vcc, s[62:63]\n"
    "buffer_atomic_smax v11, v2, s[36:39], 0 offen sc0 sc1\n"
    "s_mov_b64 exec, -1\n"
    "v_mfma_f32_16x16x32_bf16 v[40:43], v[136:139], v[20:23], v[40:43]\n"
    "v_mfma_f32_16x16x32_bf16 v[44:47], v[140:143], v[20:23], v[44:47]\n"
    "s_lshr_b32 s76, s71, 1\n"
    "v_mfma_f32_16x16x32_bf16 v[52:55], v[20:23], v[144:147], v[52:55]\n"
    "s_and_b32 s76, s76, s54\n"
    "v_mfma_f32_16x16x32_bf16 v[60:63], v[20:23], v[148:151], v[60:63]\n"
    "s_mul_i32 s76, s76, s53\n"
    "v_mfma_f32_16x16x32_bf16 v[72:75], v[152:155], v[20:23], v[72:75]\n"
    "s_waitcnt lgkmcnt(1)\n"
    "s_add_u32 s76, s76, s52\n"
    "v_mfma_f32_16x16x32_bf16 v[76:79], v[156:159], v[20:23], v[76:79]\n"
    "ds_read_b128 v[20:23], v9 offset:16384\n"
    "s_add_u32 s76, s76, 1024\n"
    "v_mfma_f32_16x16x32_bf16 v[40:43], v[160:163], v[24:27], v[40:43]\n"
    "v_mfma_f32_16x16x32_bf16 v[44:47], v[164:167], v[24:27], v[44:47]\n"
    "v_mfma_f32_16x16x32_bf16 v[52:55], v[24:27], v[168:171], v[52:55]\n"
    "v_mfma_f32_16x16x32_bf16 v[60:63], v[24:27], v[172:175], v[60:63]\n"
    "v_mfma_f32_16x16x32_bf16 v[72:75], v[176:179], v[24:27], v[72:75]\n"
    "s_waitcnt lgkmcnt(1)\n"
    "v_mfma_f32_16x16x32_bf16 v[76:79], v[180:183], v[24:27], v[76:79]\n"
    "ds_read_b128 v[24:27], v9 offset:17408\n"
    "v_mfma_f32_16x16x32_bf16 v[40:43], v[184:187], v[16:19], v[40:43]\n"
    "v_mfma_f32_16x16x32_bf16 v[44:47], v[188:191], v[16:19], v[44:47]\n"
    "v_mfma_f32_16x16x32_bf16 v[52:55], v[16:19], v[192:195], v[52:55]\n"
    "v_mfma_f32_16x16x32_bf16 v[60:63], v[16:19], v[196:199], v[60:63]\n"
    "s_waitcnt vmcnt(1) lgkmcnt(0)\n"
    "s_barrier\n"
    "v_add_u32 v13, v5, v3\n"
    "v_mfma_f32_16x16x32_bf16 v[72:75], v[200:203], v[16:19], v[72:75]\n"
    "v_cmp_gt_i32 vcc, s71, v13\n"
    "v_mfma_f32_16x16x32_bf16 v[76:79], v[204:207], v[16:19], v[76:79]\n"
    "ds_read_b128 v[16:19], v0 offset:0\n"
    "v_mfma_f32_16x16x32_bf16 v[40:43], v[208:211], v[20:23], v[40:43]\n"
    "v_mfma_f32_16x16x32_bf16 v[44:47], v[212:215], v[20:23], v[44:47]\n"
    "s_or_b64 exec, vcc, s[62:63]\n"
    "buffer_atomic_smax v5, v2, s[36:39], 0 offen sc0 sc1\n"
    "s_mov_b64 exec, -1\n"
    "v_mfma_f32_16x16x32_bf16 v[52:55], v[20:23], v[216:219], v[52:55]\n"
    "v_mfma_f32_16x16x32_bf16 v[60:63], v[20:23], v[220:223], v[60:63]\n"
    "v_mfma_f32_16x16x32_bf16 v[72:75], v[224:227], v[20:23], v[72:75]\n"
    "v_mfma_f32_16x16x32_bf16 v[76:79], v[228:231], v[20:23], v[76:79]\n"
    "ds_read_b128 v[20:23], v0 offset:1024\n"
    "v_mfma_f32_16x16x32_bf16 v[40:43], v[232:235], v[24:27], v[40:43]\n"
    "v_mfma_f32_16x16x32_bf16 v[44:47], v[236:239], v[24:27], v[44:47]\n"
    "v_mfma_f32_16x16x32_bf16 v[52:55], v[24:27], v[240:243], v[52:55]\n"
    "s_mov_b32 s69, 0x80000000\n"
    "v_mfma_f32_16x16x32_bf16 v[60:63], v[24:27], v[244:247], v[60:63]\n"
    "s_mov_b32 s70, 0\n"
    "v_mfma_f32_16x16x32_bf16 v[72:75], v[248:251], v[24:27], v[72:75]\n"
    "s_waitcnt lgkmcnt(1)\n"
    "v_mov_b32 v12, s68\n"
    "v_mfma_f32_16x16x32_bf16 v[76:79], v[252:255], v[24:27], v[76:79]\n"
    "ds_read_b128 v[24:27], v0 offset:2048\n"
    "s_add_u32 s68, s68, 1\n"
    "s_branch .LgP_head0_%=\n"
    ".LgP_LOOPE_%=:\n"
    ".LgP_done_%=:\n"
    "s_waitcnt vmcnt(0)\n"
    "buffer_atomic_smax v12, v10, s[36:39], 0 offen sc1\n"
    "s_waitcnt vmcnt(0)\n"
    "s_branch .LgP_FIN_%=\n"
    ".LgP_poll0_%=:\n"
    "v_mfma_f32_16x16x32_bf16 v[56:59], v[20:23], a[36:39], v[56:59]\n"
    "s_or_b64 exec, vcc, s[62:63]\n"
    "buffer_atomic_smax v11, v2, s[36:39], 0 offen sc0 sc1\n"
    "s_mov_b64 exec, -1\n"
    "buffer_store_dwordx4 v[60:63], v1, s[36:39], s80 offen offset:1024 sc1\n"
    "v_cvt_pk_bf16_f32 v74, v76, v77\n"
    "v_mfma_f32_16x16x32_bf16 v[64:67], a[40:43], v[20:23], v[64:67]\n"
    "s_waitcnt lgkmcnt(1)\n"
    "v_cvt_pk_bf16_f32 v75, v78, v79\n"
    "v_mfma_f32_16x16x32_bf16 v[68:71], a[44:47], v[20:23], v[68:71]\n"
    "ds_read_b128 v[20:23], v0 offset:4096\n"
    "v_mfma_f32_16x16x32_bf16 v[32:35], a[48:51], v[24:27], v[32:35]\n"
    "buffer_store_dwordx4 v[72:75], v1, s[36:39], s73 offen offset:1024 sc1\n"
    "v_mfma_f32_16x16x32_bf16 v[36:39], a[52:55], v[24:27], v[36:39]\n"
    "v_mfma_f32_16x16x32_bf16 v[48:51], v[24:27], a[56:59], v[48:51]\n"
    "s_lshr_b32 s73, s68, 1\n"
    "v_mfma_f32_16x16x32_bf16 v[56:59], v[24:27], a[60:63], v[56:59]\n"
    "v_mfma_f32_16x16x32_bf16 v[64:67], a[64:67], v[24:27], v[64:67]\n"
    "s_waitcnt lgkmcnt(1)\n"
    "v_mfma_f32_16x16x32_bf16 v[68:71], a[68:71], v[24:27], v[68:71]\n"
    "ds_read_b128 v[24:27], v0 offset:5120\n"
    "v_mfma_f32_16x16x32_bf16 v[32:35], a[72:75], v[16:19], v[32:35]\n"
    "s_and_b32 s73, s73, s57\n"
    "v_mfma_f32_16x16x32_bf16 v[36:39], a[76:79], v[16:19], v[36:39]\n"
    "v_mfma_f32_16x16x32_bf16 v[48:51], v[16:19], a[80:83], v[48:51]\n"
    "s_mul_i32 s73, s73, s56\n"
    "v_mfma_f32_16x16x32_bf16 v[56:59], v[16:19], a[84:87], v[56:59]\n"
    "v_mfma_f32_16x16x32_bf16 v[64:67], a[88:91], v[16:19], v[64:67]\n"
    "s_waitcnt lgkmcnt(1)\n"
    "v_mfma_f32_16x16x32_bf16 v[68:71], a[92:95], v[16:19], v[68:71]\n"
    "ds_read_b128 v[16:19], v0 offset:6144\n"
    "v_mfma_f32_16x16x32_bf16 v[32:35], a[96:99], v[20:23], v[32:35]\n"
    "s_add_u32 s73, s73, s55\n"
    "s_waitcnt vmcnt(5)\n"
    "v_add_u32 v13, v5, v3\n"
    "v_mfma_f32_16x16x32_bf16 v[36:39], a[100:103], v[20:23], v[36:39]\n"
    "v_cmp_gt_i32 vcc, s71, v13\n"
    "v_mfma_f32_16x16x32_bf16 v[48:51], v[20:23], a[104:107], v[48:51]\n"
    "s_add_u32 s79, s73, 16384\n"
    "v_mfma_f32_16x16x32_bf16 v[56:59], v[20:23], a[108:111], v[56:59]\n"
    "v_mfma_f32_16x16x32_bf16 v[64:67], a[112:115], v[20:23], v[64:67]\n"
    "s_waitcnt lgkmcnt(1)\n"
    "s_cbranch_vccz .LgP_dma0_29_%=\n"
    "v_mfma_f32_16x16x32_bf16 v[68:71], a[116:119], v[20:23], v[68:71]\n"
    "ds_read_b128 v[20:23], v0 offset:7168\n"
    "s_or_b64 exec, vcc, s[62:63]\n"
    "buffer_atomic_smax v5, v2, s[36:39], 0 offen sc0 sc1\n"
    "s_mov_b64 exec, -1\n"
    "v_mfma_f32_16x16x32_bf16 v[32:35], a[120:123], v[24:27], v[32:35]\n"
    "s_add_u32 s80, s73, 32768\n"
    "s_waitcnt vmcnt(8)\n"
    "buffer_atomic_smax v12, v10, s[36:39], s69 offen sc1\n"
    "v_mfma_f32_16x16x32_bf16 v[36:39], a[124:127], v[24:27], v[36:39]\n"
    "v_mfma_f32_16x16x32_bf16 v[48:51], v[24:27], a[128:131], v[48:51]\n"
    "v_mfma_f32_16x16x32_bf16 v[56:59], v[24:27], a[132:135], v[56:59]\n"
    "v_mfma_f32_16x16x32_bf16 v[64:67], a[136:139], v[24:27], v[64:67]\n"
    "s_waitcnt lgkmcnt(1)\n"
    "v_mfma_f32_16x16x32_bf16 v[68:71], a[140:143], v[24:27], v[68:71]\n"
    "ds_read_b128 v[24:27], v0 offset:8192\n"
    "v_mfma_f32_16x16x32_bf16 v[32:35], a[144:147], v[16:19], v[32:35]\n"
    "v_mfma_f32_16x16x32_bf16 v[36:39], a[148:151], v[16:19], v[36:39]\n"
    "v_mfma_f32_16x16x32_bf16 v[48:51], v[16:19], a[152:155], v[48:51]\n"
    "v_mfma_f32_16x16x32_bf16 v[56:59], v[16:19], a[156:159], v[56:59]\n"
    "v_mfma_f32_16x16x32_bf16 v[64:67], a[160:163], v[16:19], v[64:67]\n"
    "s_waitcnt lgkmcnt(1)\n"
    "v_mfma_f32_16x16x32_bf16 v[68:71], a[164:167], v[16:19], v[68:71]\n"
    "ds_read_b128 v[16:19], v0 offset:9216\n"
    "v_mfma_f32_16x16x32_bf16 v[32:35], a[168:171], v[20:23], v[32:35]\n"
    "v_mfma_f32_16x16x32_bf16 v[36:39], a[172:175], v[20:23], v[36:39]\n"
    "v_mfma_f32_16x16x32_bf16 v[48:51], v[20:23], a[176:179], v[48:51]\n"
    "s_waitcnt vmcnt(4)\n"
    "v_add_u32 v13, v11, v3\n"
    "v_mfma_f32_16x16x32_bf16 v[56:59], v[20:23], a[180:183], v[56:59]\n"
    "v_cmp_gt_i32 vcc, s71, v13\n"
    "v_mfma_f32_16x16x32_bf16 v[64:67], a[184:187], v[20:23], v[64:67]\n"
    "s_waitcnt lgkmcnt(1)\n"
    "v_mfma_f32_16x16x32_bf16 v[68:71], a[188:191], v[20:23], v[68:71]\n"
    "ds_read_b128 v[20:23], v0 offset:10240\n"
    "v_mfma_f32_16x16x32_bf16 v[32:35], a[192:195], v[24:27], v[32:35]\n"
    "s_cbranch_vccz .LgP_dma0_49_%=\n"
    "v_mfma_f32_16x16x32_bf16 v[36:39], a[196:199], v[24:27], v[36:39]\n"
    "s_or_b64 exec, vcc, s[62:63]\n"
    "buffer_atomic_smax v11, v2, s[36:39], 0 offen sc0 sc1\n"
    "s_mov_b64 exec, -1\n"
    "v_mfma_f32_16x16x32_bf16 v[48:51], v[24:27], a[200:203], v[48:51]\n"
    "v_mfma_f32_16x16x32_bf16 v[56:59], v[24:27], a[204:207], v[56:59]\n"
    "v_mfma_f32_16x16x32_bf16 v[64:67], a[208:211], v[24:27], v[64:67]\n"
    "s_waitcnt lgkmcnt(1)\n"
    "s_waitcnt vmcnt(3)\n"
    "buffer_atomic_smax v12, v10, s[36:39], s70 offen sc1\n"
    "v_mfma_f32_16x16x32_bf16 v[68:71], a[212:215], v[24:27], v[68:71]\n"
    "ds_read_b128 v[24:27], v0 offset:11264\n"
    "v_mfma_f32_16x16x32_bf16 v[32:35], a[216:219], v[16:19], v[32:35]\n"
    "v_mfma_f32_16x16x32_bf16 v[36:39], a[220:223], v[16:19], v[36:39]\n"
    "v_mfma_f32_16x16x32_bf16 v[48:51], v[16:19], a[224:227], v[48:51]\n"
    "v_mfma_f32_16x16x32_bf16 v[56:59], v[16:19], a[228:231], v[56:59]\n"
    "v_mfma_f32_16x16x32_bf16 v[64:67], a[232:235], v[16:19], v[64:67]\n"
    "s_waitcnt lgkmcnt(1)\n"
    "v_mfma_f32_16x16x32_bf16 v[68:71], a[236:239], v[16:19], v[68:71]\n"
    "ds_read_b128 v[16:19], v0 offset:12288\n"
    "v_mfma_f32_16x16x32_bf16 v[32:35], a[240:243], v[20:23], v[32:35]\n"
    "v_mfma_f32_16x16x32_bf16 v[36:39], a[244:247], v[20:23], v[36:39]\n"
    "v_mfma_f32_16x16x32_bf16 v[48:51], v[20:23], a[248:251], v[48:51]\n"
    "v_mfma_f32_16x16x32_bf16 v[56:59], v[20:23], a[252:255], v[56:59]\n"
    "v_mfma_f32_16x16x32_bf16 v[64:67], v[80:83], v[20:23], v[64:67]\n"
    "s_waitcnt lgkmcnt(1)\n"
    "s_waitcnt vmcnt(3)\n"
    "v_add_u32 v13, v5, v3\n"
    "v_mfma_f32_16x16x32_bf16 v[68:71], v[84:87], v[20:23], v[68:71]\n"
    "ds_read_b128 v[20:23], v0 offset:13312\n"
    "v_cmp_gt_i32 vcc, s71, v13\n"
    "v_mfma_f32_16x16x32_bf16 v[32:35], v[88:91], v[24:27], v[32:35]\n"
    "v_mfma_f32_16x16x32_bf16 v[36:39], v[92:95], v[24:27], v[36:39]\n"
    "v_mfma_f32_16x16x32_bf16 v[48:51], v[24:27], v[96:99], v[48:51]\n"
    "s_cbranch_vccz .LgP_dma0_69_%=\n"
    "v_mfma_f32_16x16x32_bf16 v[56:59], v[24:27], v[100:103], v[56:59]\n"
    "s_or_b64 exec, vcc, s[62:63]\n"
    "buffer_atomic_smax v5, v2, s[36:39], 0 offen sc0 sc1\n"
    "s_mov_b64 exec, -1\n"
    "v_mfma_f32_16x16x32_bf16 v[64:67], v[104:107], v[24:27], v[64:67]\n"
    "s_waitcnt lgkmcnt(1)\n"
    "v_mfma_f32_16x16x32_bf16 v[68:71], v[108:111], v[24:27], v[68:71]\n"
    "ds_read_b128 v[24:27], v0 offset:14336\n"
    "v_mfma_f32_16x16x32_bf16 v[32:35], v[112:115], v[16:19], v[32:35]\n"
    "v_mfma_f32_16x16x32_bf16 v[36:39], v[116:119], v[16:19], v[36:39]\n"
    "v_mfma_f32_16x16x32_bf16 v[48:51], v[16:19], v[120:123], v[48:51]\n"
    "v_mfma_f32_16x16x32_bf16 v[56:59], v[16:19], v[124:127], v[56:59]\n"
    "v_mfma_f32_16x16x32_bf16 v[64:67], v[128:131], v[16:19], v[64:67]\n"
    "s_waitcnt lgkmcnt(1)\n"
    "v_mfma_f32_16x16x32_bf16 v[68:71], v[132:135], v[16:19], v[68:71]\n"
    "ds_read_b128 v[16:19], v0 offset:15360\n"
    "v_mfma_f32_16x16x32_bf16 v[32:35], v[136:139], v[20:23], v[32:35]\n"
    "v_mfma_f32_16x16x32_bf16 v[36:39], v[140:143], v[20:23], v[36:39]\n"
    "v_mfma_f32_16x16x32_bf16 v[48:51], v[20:23], v[144:147], v[48:51]\n"
    "v_mfma_f32_16x16x32_bf16 v[56:59], v[20:23], v[148:151], v[56:59]\n"
    "v_mfma_f32_16x16x32_bf16 v[64:67], v[152:155], v[20:23], v[64:67]\n"
    "s_waitcnt lgkmcnt(1)\n"
    "v_mfma_f32_16x16x32_bf16 v[68:71], v[156:159], v[20:23], v[68:71]\n"
    "ds_read_b128 v[20:23], v0 offset:16384\n"
    "v_mfma_f32_16x16x32_bf16 v[32:35], v[160:163], v[24:27], v[32:35]\n"
    "s_waitcnt vmcnt(2)\n"
    "v_add_u32 v13, v11, v3\n"
    "v_mfma_f32_16x16x32_bf16 v[36:39], v[164:167], v[24:27], v[36:39]\n"
    "v_cmp_gt_i32 vcc, s71, v13\n"
    "v_mfma_f32_16x16x32_bf16 v[48:51], v[24:27], v[168:171], v[48:51]\n"
    "v_mfma_f32_16x16x32_bf16 v[56:59], v[24:27], v[172:175], v[56:59]\n"
    "v_mfma_f32_16x16x32_bf16 v[64:67], v[176:179], v[24:27], v[64:67]\n"
    "s_waitcnt lgkmcnt(1)\n"
    "s_cbranch_vccz .LgP_dma0_89_%=\n"
    "v_mfma_f32_16x16x32_bf16 v[68:71], v[180:183], v[24:27], v[68:71]\n"
    "ds_read_b128 v[24:27], v0 offset:17408\n"
    "s_or_b64 exec, vcc, s[62:63]\n"
    "buffer_atomic_smax v11, v2, s[36:39], 0 offen sc0 sc1\n"
    "s_mov_b64 exec, -1\n"
    "v_mfma_f32_16x16x32_bf16 v[32:35], v[184:187], v[16:19], v[32:35]\n"
    "v_mfma_f32_16x16x32_bf16 v[36:39], v[188:191], v[16:19], v[36:39]\n"
    "v_mfma_f32_16x16x32_bf16 v[48:51], v[16:19], v[192:195], v[48:51]\n"
    "v_mfma_f32_16x16x32_bf16 v[56:59], v[16:19], v[196:199], v[56:59]\n"
    "v_mfma_f32_16x16x32_bf16 v[64:67], v[200:203], v[16:19], v[64:67]\n"
    "s_waitcnt lgkmcnt(1)\n"
    "v_mfma_f32_16x16x32_bf16 v[68:71], v[204:207], v[16:19], v[68:71]\n"
    "v_mfma_f32_16x16x32_bf16 v[32:35], v[208:211], v[20:23], v[32:35]\n"
    "v_mfma_f32_16x16x32_bf16 v[36:39], v[212:215], v[20:23], v[36:39]\n"
    "v_mfma_f32_16x16x32_bf16 v[48:51], v[20:23], v[216:219], v[48:51]\n"
    "v_mfma_f32_16x16x32_bf16 v[56:59], v[20:23], v[220:223], v[56:59]\n"
    "v_mfma_f32_16x16x32_bf16 v[64:67], v[224:227], v[20:23], v[64:67]\n"
    "s_waitcnt lgkmcnt(0)\n"
    "v_mfma_f32_16x16x32_bf16 v[68:71], v[228:231], v[20:23], v[68:71]\n"
    "v_mfma_f32_16x16x32_bf16 v[32:35], v[232:235], v[24:27], v[32:35]\n"
    "v_mfma_f32_16x16x32_bf16 v[36:39], v[236:239], v[24:27], v[36:39]\n"
    "v_mfma_f32_16x16x32_bf16 v[48:51], v[24:27], v[240:243], v[48:51]\n"
    "v_mfma_f32_16x16x32_bf16 v[56:59], v[24:27], v[244:247], v[56:59]\n"
    "v_mfma_f32_16x16x32_bf16 v[64:67], v[248:251], v[24:27], v[64:67]\n"
    "v_mfma_f32_16x16x32_bf16 v[68:71], v[252:255], v[24:27], v[68:71]\n"
    "v_cvt_pk_bf16_f32 v32, v32, v33\n"
    "v_cvt_pk_bf16_f32 v33, v34, v35\n"
    "v_cvt_pk_bf16_f32 v34, v36, v37\n"
    "v_cvt_pk_bf16_f32 v35, v38, v39\n"
    "buffer_store_dwordx4 v[32:35], v1, s[36:39], s79 offen sc1\n"
    "v_cvt_pk_bf16_f32 v28, v48, v49\n"
    "v_cvt_pk_bf16_f32 v29, v50, v51\n"
    "v_cvt_pk_bf16_f32 v30, v56, v57\n"
    "v_cvt_pk_bf16_f32 v31, v58, v59\n"
    "v_cvt_pk_bf16_f32 v64, v64, v65\n"
    "v_cvt_pk_bf16_f32 v65, v66, v67\n"
    "v_cvt_pk_bf16_f32 v66, v68, v69\n"
    "v_cvt_pk_bf16_f32 v67, v70, v71\n"
    "buffer_store_dwordx4 v[64:67], v1, s[36:39], s73 offen sc1\n"
    "v_mov_b32 v12, s68\n"
    "s_mov_b32 s73, 0x80000000\n"
    "s_mov_b32 s79, 0x80000000\n"
    "s_mov_b32 s80, 0x80000000\n"
    "s_mov_b32 s68, s71\n"
    "s_waitcnt vmcnt(3)\n"
    "v_add_u32 v13, v5, v3\n"
    "v_cmp_gt_i32 vcc, s71, v13\n"
    "s_cbranch_vccnz .LgP_idle0_%=\n"
    "s_add_u32 m0, s59, 20480\n"
    "s_nop 0\n"
    "buffer_load_dwordx4 v1, s[36:39], s76 offen sc1 lds\n"
    "s_add_u32 m0, s59, 24576\n"
    "s_add_u32 s76, s76, 16384\n"
    "buffer_load_dwordx4 v1, s[36:39], s76 offen sc1 lds\n"
    "s_add_u32 m0, s59, 28672\n"
    "s_add_u32 s76, s76, 16384\n"
    "buffer_load_dwordx4 v1, s[36:39], s76 offen sc1 lds\n"
    "s_add_u32 m0, s59, 32768\n"
    "s_add_u32 s76, s76, 16384\n"
    "buffer_load_dwordx4 v1, s[36:39], s76 offen sc1 lds\n"
    "s_add_u32 m0, s59, 36864\n"
    "s_add_u32 s76, s76, 16384\n"
    "buffer_load_dwordx4 v8, s[36:39], s76 offen sc1 lds\n"
    "s_waitcnt vmcnt(5)\n"
    "buffer_atomic_smax v12, v14, s[36:39], 0 offen sc1\n"
    "s_mov_b32 s69, 0x80000000\n"
    "s_mov_b32 s70, 0x80000000\n"
    "s_waitcnt vmcnt(8)\n"
    "s_add_u32 s71, s68, 1\n"
    "v_add_u32 v13, v11, v3\n"
    "v_cmp_gt_i32 vcc, s71, v13\n"
    "s_nop 0\n"
    "s_or_b64 exec, vcc, s[62:63]\n"
    "buffer_atomic_smax v11, v2, s[36:39], 0 offen sc0 sc1\n"
    "s_mov_b64 exec, -1\n"
    "s_waitcnt vmcnt(2)\n"
    "s_branch .LgP_bar1_%=\n"
    ".LgP_dma0_29_%=:\n"
    "v_mfma_f32_16x16x32_bf16 v[68:71], a[116:119], v[20:23], v[68:71]\n"
    "ds_read_b128 v[20:23], v0 offset:7168\n"
    "v_mfma_f32_16x16x32_bf16 v[32:35], a[120:123], v[24:27], v[32:35]\n"
    "s_add_u32 m0, s59, 20480\n"
    "s_add_u32 s80, s73, 32768\n"
    "s_waitcnt vmcnt(7)\n"
    "buffer_atomic_smax v12, v10, s[36:39], s69 offen sc1\n"
    "v_mfma_f32_16x16x32_bf16 v[36:39], a[124:127], v[24:27], v[36:39]\n"
    "buffer_load_dwordx4 v1, s[36:39], s76 offen sc1 lds\n"
    "v_mfma_f32_16x16x32_bf16 v[48:51], v[24:27], a[128:131], v[48:51]\n"
    "s_add_u32 m0, s59, 24576\n"
    "s_add_u32 s76, s76, 16384\n"
    "v_mfma_f32_16x16x32_bf16 v[56:59], v[24:27], a[132:135], v[56:59]\n"
    "buffer_load_dwordx4 v1, s[36:39], s76 offen sc1 lds\n"
    "v_mfma_f32_16x16x32_bf16 v[64:67], a[136:139], v[24:27], v[64:67]\n"
    "s_waitcnt lgkmcnt(1)\n"
    "v_mfma_f32_16x16x32_bf16 v[68:71], a[140:143], v[24:27], v[68:71]\n"
    "ds_read_b128 v[24:27], v0 offset:8192\n"
    "v_mfma_f32_16x16x32_bf16 v[32:35], a[144:147], v[16:19], v[32:35]\n"
    "s_add_u32 m0, s59, 28672\n"
    "s_add_u32 s76, s76, 16384\n"
    "v_mfma_f32_16x16x32_bf16 v[36:39], a[148:151], v[16:19], v[36:39]\n"
    "buffer_load_dwordx4 v1, s[36:39], s76 offen sc1 lds\n"
    "v_mfma_f32_16x16x32_bf16 v[48:51], v[16:19], a[152:155], v[48:51]\n"
    "v_mfma_f32_16x16x32_bf16 v[56:59], v[16:19], a[156:159], v[56:59]\n"
    "s_add_u32 m0, s59, 32768\n"
    "s_add_u32 s76, s76, 16384\n"
    "v_mfma_f32_16x16x32_bf16 v[64:67], a[160:163], v[16:19], v[64:67]\n"
    "s_waitcnt lgkmcnt(1)\n"
    "buffer_load_dwordx4 v1, s[36:39], s76 offen sc1 lds\n"
    "v_mfma_f32_16x16x32_bf16 v[68:71], a[164:167], v[16:19], v[68:71]\n"
    "ds_read_b128 v[16:19], v0 offset:9216\n"
    "v_mfma_f32_16x16x32_bf16 v[32:35], a[168:171], v[20:23], v[32:35]\n"
    "v_mfma_f32_16x16x32_bf16 v[36:39], a[172:175], v[20:23], v[36:39]\n"
    "s_add_u32 m0, s59, 36864\n"
    "s_add_u32 s76, s76, 16384\n"
    "v_mfma_f32_16x16x32_bf16 v[48:51], v[20:23], a[176:179], v[48:51]\n"
    "buffer_load_dwordx4 v8, s[36:39], s76 offen sc1 lds\n"
    "v_mfma_f32_16x16x32_bf16 v[56:59], v[20:23], a[180:183], v[56:59]\n"
    "v_mfma_f32_16x16x32_bf16 v[64:67], a[184:187], v[20:23], v[64:67]\n"
    "s_waitcnt lgkmcnt(1)\n"
    "v_mfma_f32_16x16x32_bf16 v[68:71], a[188:191], v[20:23], v[68:71]\n"
    "ds_read_b128 v[20:23], v0 offset:10240\n"
    "v_mfma_f32_16x16x32_bf16 v[32:35], a[192:195], v[24:27], v[32:35]\n"
    "v_mfma_f32_16x16x32_bf16 v[36:39], a[196:199], v[24:27], v[36:39]\n"
    "v_mfma_f32_16x16x32_bf16 v[48:51], v[24:27], a[200:203], v[48:51]\n"
    "v_mfma_f32_16x16x32_bf16 v[56:59], v[24:27], a[204:207], v[56:59]\n"
    "v_mfma_f32_16x16x32_bf16 v[64:67], a[208:211], v[24:27], v[64:67]\n"
    "s_waitcnt lgkmcnt(1)\n"
    "s_waitcnt vmcnt(6)\n"
    "buffer_atomic_smax v12, v10, s[36:39], s70 offen sc1\n"
    "v_mfma_f32_16x16x32_bf16 v[68:71], a[212:215], v[24:27], v[68:71]\n"
    "ds_read_b128 v[24:27], v0 offset:11264\n"
    "v_mfma_f32_16x16x32_bf16 v[32:35], a[216:219], v[16:19], v[32:35]\n"
    "v_mfma_f32_16x16x32_bf16 v[36:39], a[220:223], v[16:19], v[36:39]\n"
    "v_mfma_f32_16x16x32_bf16 v[48:51], v[16:19], a[224:227], v[48:51]\n"
    "v_mfma_f32_16x16x32_bf16 v[56:59], v[16:19], a[228:231], v[56:59]\n"
    "v_mfma_f32_16x16x32_bf16 v[64:67], a[232:235], v[16:19], v[64:67]\n"
    "s_waitcnt lgkmcnt(1)\n"
    "v_mfma_f32_16x16x32_bf16 v[68:71], a[236:239], v[16:19], v[68:71]\n"
    "ds_read_b128 v[16:19], v0 offset:12288\n"
    "v_mfma_f32_16x16x32_bf16 v[32:35], a[240:243], v[20:23], v[32:35]\n"
    "v_mfma_f32_16x16x32_bf16 v[36:39], a[244:247], v[20:23], v[36:39]\n"
    "v_mfma_f32_16x16x32_bf16 v[48:51], v[20:23], a[248:251], v[48:51]\n"
    "v_mfma_f32_16x16x32_bf16 v[56:59], v[20:23], a[252:255], v[56:59]\n"
    "v_mfma_f32_16x16x32_bf16 v[64:67], v[80:83], v[20:23], v[64:67]\n"
    "s_waitcnt lgkmcnt(1)\n"
    "v_mfma_f32_16x16x32_bf16 v[68:71], v[84:87], v[20:23], v[68:71]\n"
    "ds_read_b128 v[20:23], v0 offset:13312\n"
    "v_mfma_f32_16x16x32_bf16 v[32:35], v[88:91], v[24:27], v[32:35]\n"
    "v_mfma_f32_16x16x32_bf16 v[36:39], v[92:95], v[24:27], v[36:39]\n"
    "v_mfma_f32_16x16x32_bf16 v[48:51], v[24:27], v[96:99], v[48:51]\n"
    "v_mfma_f32_16x16x32_bf16 v[56:59], v[24:27], v[100:103], v[56:59]\n"
    "v_mfma_f32_16x16x32_bf16 v[64:67], v[104:107], v[24:27], v[64:67]\n"
    "s_waitcnt lgkmcnt(1)\n"
    "v_mfma_f32_16x16x32_bf16 v[68:71], v[108:111], v[24:27], v[68:71]\n"
    "ds_read_b128 v[24:27], v0 offset:14336\n"
    "s_branch .LgP_join0_%=\n"
    ".LgP_dma0_49_%=:\n"
    "v_mfma_f32_16x16x32_bf16 v[36:39], a[196:199], v[24:27], v[36:39]\n"
    "s_add_u32 m0, s59, 20480\n"
    "v_mfma_f32_16x16x32_bf16 v[48:51], v[24:27], a[200:203], v[48:51]\n"
    "buffer_load_dwordx4 v1, s[36:39], s76 offen sc1 lds\n"
    "v_mfma_f32_16x16x32_bf16 v[56:59], v[24:27], a[204:207], v[56:59]\n"
    "s_add_u32 m0, s59, 24576\n"
    "s_add_u32 s76, s76, 16384\n"
    "v_mfma_f32_16x16x32_bf16 v[64:67], a[208:211], v[24:27], v[64:67]\n"
    "s_waitcnt lgkmcnt(1)\n"
    "buffer_load_dwordx4 v1, s[36:39], s76 offen sc1 lds\n"
    "s_waitcnt vmcnt(4)\n"
    "buffer_atomic_smax v12, v10, s[36:39], s70 offen sc1\n"
    "v_mfma_f32_16x16x32_bf16 v[68:71], a[212:215], v[24:27], v[68:71]\n"
    "ds_read_b128 v[24:27], v0 offset:11264\n"
    "v_mfma_f32_16x16x32_bf16 v[32:35], a[216:219], v[16:19], v[32:35]\n"
    "v_mfma_f32_16x16x32_bf16 v[36:39], a[220:223], v[16:19], v[36:39]\n"
    "v_mfma_f32_16x16x32_bf16 v[48:51], v[16:19], a[224:227], v[48:51]\n"
    "s_add_u32 m0, s59, 28672\n"
    "s_add_u32 s76, s76, 16384\n"
    "v_mfma_f32_16x16x32_bf16 v[56:59], v[16:19], a[228:231], v[56:59]\n"
    "buffer_load_dwordx4 v1, s[36:39], s76 offen sc1 lds\n"
    "v_mfma_f32_16x16x32_bf16 v[64:67], a[232:235], v[16:19], v[64:67]\n"
    "s_waitcnt lgkmcnt(1)\n"
    "v_mfma_f32_16x16x32_bf16 v[68:71], a[236:239], v[16:19], v[68:71]\n"
    "ds_read_b128 v[16:19], v0 offset:12288\n"
    "v_mfma_f32_16x16x32_bf16 v[32:35], a[240:243], v[20:23], v[32:35]\n"
    "s_add_u32 m0, s59, 32768\n"
    "s_add_u32 s76, s76, 16384\n"
    "v_mfma_f32_16x16x32_bf16 v[36:39], a[244:247], v[20:23], v[36:39]\n"
    "buffer_load_dwordx4 v1, s[36:39], s76 offen sc1 lds\n"
    "v_mfma_f32_16x16x32_bf16 v[48:51], v[20:23], a[248:251], v[48:51]\n"
    "v_mfma_f32_16x16x32_bf16 v[56:59], v[20:23], a[252:255], v[56:59]\n"
    "s_add_u32 m0, s59, 36864\n"
    "s_add_u32 s76, s76, 16384\n"
    "v_mfma_f32_16x16x32_bf16 v[64:67], v[80:83], v[20:23], v[64:67]\n"
    "s_waitcnt lgkmcnt(1)\n"
    "buffer_load_dwordx4 v8, s[36:39], s76 offen sc1 lds\n"
    "v_mfma_f32_16x16x32_bf16 v[68:71], v[84:87], v[20:23], v[68:71]\n"
    "ds_read_b128 v[20:23], v0 offset:13312\n"
    "v_mfma_f32_16x16x32_bf16 v[32:35], v[88:91], v[24:27], v[32:35]\n"
    "v_mfma_f32_16x16x32_bf16 v[36:39], v[92:95], v[24:27], v[36:39]\n"
    "v_mfma_f32_16x16x32_bf16 v[48:51], v[24:27], v[96:99], v[48:51]\n"
    "v_mfma_f32_16x16x32_bf16 v[56:59], v[24:27], v[100:103], v[56:59]\n"
    "v_mfma_f32_16x16x32_bf16 v[64:67], v[104:107], v[24:27], v[64:67]\n"
    "s_waitcnt lgkmcnt(1)\n"
    "v_mfma_f32_16x16x32_bf16 v[68:71], v[108:111], v[24:27], v[68:71]\n"
    "ds_read_b128 v[24:27], v0 offset:14336\n"
    "s_branch .LgP_join0_%=\n"
    ".LgP_dma0_69_%=:\n"
    "v_mfma_f32_16x16x32_bf16 v[56:59], v[24:27], v[100:103], v[56:59]\n"
    "s_add_u32 m0, s59, 20480\n"
    "v_mfma_f32_16x16x32_bf16 v[64:67], v[104:107], v[24:27], v[64:67]\n"
    "s_waitcnt lgkmcnt(1)\n"
    "buffer_load_dwordx4 v1, s[36:39], s76 offen sc1 lds\n"
    "v_mfma_f32_16x16x32_bf16 v[68:71], v[108:111], v[24:27], v[68:71]\n"
    "ds_read_b128 v[24:27], v0 offset:14336\n"
    "v_mfma_f32_16x16x32_bf16 v[32:35], v[112:115], v[16:19], v[32:35]\n"
    "v_mfma_f32_16x16x32_bf16 v[36:39], v[116:119], v[16:19], v[36:39]\n"
    "s_add_u32 m0, s59, 24576\n"
    "s_add_u32 s76, s76, 16384\n"
    "v_mfma_f32_16x16x32_bf16 v[48:51], v[16:19], v[120:123], v[48:51]\n"
    "buffer_load_dwordx4 v1, s[36:39], s76 offen sc1 lds\n"
    "v_mfma_f32_16x16x32_bf16 v[56:59], v[16:19], v[124:127], v[56:59]\n"
    "v_mfma_f32_16x16x32_bf16 v[64:67], v[128:131], v[16:19], v[64:67]\n"
    "s_waitcnt lgkmcnt(1)\n"
    "v_mfma_f32_16x16x32_bf16 v[68:71], v[132:135], v[16:19], v[68:71]\n"
    "ds_read_b128 v[16:19], v0 offset:15360\n"
    "v_mfma_f32_16x16x32_bf16 v[32:35], v[136:139], v[20:23], v[32:35]\n"
    "s_add_u32 m0, s59, 28672\n"
    "s_add_u32 s76, s76, 16384\n"
    "v_mfma_f32_16x16x32_bf16 v[36:39], v[140:143], v[20:23], v[36:39]\n"
    "buffer_load_dwordx4 v1, s[36:39], s76 offen sc1 lds\n"
    "v_mfma_f32_16x16x32_bf16 v[48:51], v[20:23], v[144:147], v[48:51]\n"
    "s_add_u32 m0, s59, 32768\n"
    "s_add_u32 s76, s76, 16384\n"
    "v_mfma_f32_16x16x32_bf16 v[56:59], v[20:23], v[148:151], v[56:59]\n"
    "buffer_load_dwordx4 v1, s[36:39], s76 offen sc1 lds\n"
    "v_mfma_f32_16x16x32_bf16 v[64:67], v[152:155], v[20:23], v[64:67]\n"
    "s_waitcnt lgkmcnt(1)\n"
    "v_mfma_f32_16x16x32_bf16 v[68:71], v[156:159], v[20:23], v[68:71]\n"
    "ds_read_b128 v[20:23], v0 offset:16384\n"
    "v_mfma_f32_16x16x32_bf16 v[32:35], v[160:163], v[24:27], v[32:35]\n"
    "v_mfma_f32_16x16x32_bf16 v[36:39], v[164:167], v[24:27], v[36:39]\n"
    "s_add_u32 m0, s59, 36864\n"
    "s_add_u32 s76, s76, 16384\n"
    "v_mfma_f32_16x16x32_bf16 v[48:51], v[24:27], v[168:171], v[48:51]\n"
    "buffer_load_dwordx4 v8, s[36:39], s76 offen sc1 lds\n"
    "v_mfma_f32_16x16x32_bf16 v[56:59], v[24:27], v[172:175], v[56:59]\n"
    "v_mfma_f32_16x16x32_bf16 v[64:67], v[176:179], v[24:27], v[64:67]\n"
    "s_waitcnt lgkmcnt(1)\n"
    "v_mfma_f32_16x16x32_bf16 v[68:71], v[180:183], v[24:27], v[68:71]\n"
    "ds_read_b128 v[24:27], v0 offset:17408\n"
    "v_mfma_f32_16x16x32_bf16 v[32:35], v[184:187], v[16:19], v[32:35]\n"
    "v_mfma_f32_16x16x32_bf16 v[36:39], v[188:191], v[16:19], v[36:39]\n"
    "v_mfma_f32_16x16x32_bf16 v[48:51], v[16:19], v[192:195], v[48:51]\n"
    "v_mfma_f32_16x16x32_bf16 v[56:59], v[16:19], v[196:199], v[56:59]\n"
    "v_mfma_f32_16x16x32_bf16 v[64:67], v[200:203], v[16:19], v[64:67]\n"
    "s_waitcnt lgkmcnt(1)\n"
    "v_mfma_f32_16x16x32_bf16 v[68:71], v[204:207], v[16:19], v[68:71]\n"
    "v_mfma_f32_16x16x32_bf16 v[32:35], v[208:211], v[20:23], v[32:35]\n"
    "v_mfma_f32_16x16x32_bf16 v[36:39], v[212:215], v[20:23], v[36:39]\n"
    "v_mfma_f32_16x16x32_bf16 v[48:51], v[20:23], v[216:219], v[48:51]\n"
    "v_mfma_f32_16x16x32_bf16 v[56:59], v[20:23], v[220:223], v[56:59]\n"
    "v_mfma_f32_16x16x32_bf16 v[64:67], v[224:227], v[20:23], v[64:67]\n"
    "s_waitcnt lgkmcnt(0)\n"
    "v_mfma_f32_16x16x32_bf16 v[68:71], v[228:231], v[20:23], v[68:71]\n"
    "v_mfma_f32_16x16x32_bf16 v[32:35], v[232:235], v[24:27], v[32:35]\n"
    "v_mfma_f32_16x16x32_bf16 v[36:39], v[236:239], v[24:27], v[36:39]\n"
    "v_mfma_f32_16x16x32_bf16 v[48:51], v[24:27], v[240:243], v[48:51]\n"
    "v_mfma_f32_16x16x32_bf16 v[56:59], v[24:27], v[244:247], v[56:59]\n"
    "v_mfma_f32_16x16x32_bf16 v[64:67], v[248:251], v[24:27], v[64:67]\n"
    "v_mfma_f32_16x16x32_bf16 v[68:71], v[252:255], v[24:27], v[68:71]\n"
    "v_cvt_pk_bf16_f32 v32, v32, v33\n"
    "v_cvt_pk_bf16_f32 v33, v34, v35\n"
    "v_cvt_pk_bf16_f32 v34, v36, v37\n"
    "v_cvt_pk_bf16_f32 v35, v38, v39\n"
    "buffer_store_dwordx4 v[32:35], v1, s[36:39], s79 offen sc1\n"
    "v_cvt_pk_bf16_f32 v28, v48, v49\n"
    "v_cvt_pk_bf16_f32 v29, v50, v51\n"
    "v_cvt_pk_bf16_f32 v30, v56, v57\n"
    "v_cvt_pk_bf16_f32 v31, v58, v59\n"
    "v_cvt_pk_bf16_f32 v64, v64, v65\n"
    "v_cvt_pk_bf16_f32 v65, v66, v67\n"
    "v_cvt_pk_bf16_f32 v66, v68, v69\n"
    "v_cvt_pk_bf16_f32 v67, v70, v71\n"
    "buffer_store_dwordx4 v[64:67], v1, s[36:39], s73 offen sc1\n"
    "v_mov_b32 v12, s68\n"
    "s_mov_b32 s73, 0x80000000\n"
    "s_mov_b32 s79, 0x80000000\n"
    "s_mov_b32 s80, 0x80000000\n"
    "s_mov_b32 s68, s71\n"
    "s_mov_b32 s69, 0\n"
    "s_mov_b32 s70, 0x80000000\n"
    "s_waitcnt vmcnt(8)\n"
    "s_add_u32 s71, s68, 1\n"
    "v_add_u32 v13, v11, v3\n"
    "v_cmp_gt_i32 vcc, s71, v13\n"
    "s_nop 0\n"
    "s_or_b64 exec, vcc, s[62:63]\n"
    "buffer_atomic_smax v11, v2, s[36:39], 0 offen sc0 sc1\n"
    "s_mov_b64 exec, -1\n"
    "s_waitcnt vmcnt(3)\n"
    "s_branch .LgP_bar1_%=\n"
    ".LgP_dma0_89_%=:\n"
    "v_mfma_f32_16x16x32_bf16 v[68:71], v[180:183], v[24:27], v[68:71]\n"
    "ds_read_b128 v[24:27], v0 offset:17408\n"
    "v_mfma_f32_16x16x32_bf16 v[32:35], v[184:187], v[16:19], v[32:35]\n"
    "s_add_u32 m0, s59, 20480\n"
    "v_mfma_f32_16x16x32_bf16 v[36:39], v[188:191], v[16:19], v[36:39]\n"
    "buffer_load_dwordx4 v1, s[36:39], s76 offen sc1 lds\n"
    "v_mfma_f32_16x16x32_bf16 v[48:51], v[16:19], v[192:195], v[48:51]\n"
    "s_add_u32 m0, s59, 24576\n"
    "s_add_u32 s76, s76, 16384\n"
    "v_mfma_f32_16x16x32_bf16 v[56:59], v[16:19], v[196:199], v[56:59]\n"
    "buffer_load_dwordx4 v1, s[36:39], s76 offen sc1 lds\n"
    "v_mfma_f32_16x16x32_bf16 v[64:67], v[200:203], v[16:19], v[64:67]\n"
    "s_waitcnt lgkmcnt(1)\n"
    "v_mfma_f32_16x16x32_bf16 v[68:71], v[204:207], v[16:19], v[68:71]\n"
    "v_mfma_f32_16x16x32_bf16 v[32:35], v[208:211], v[20:23], v[32:35]\n"
    "s_add_u32 m0, s59, 28672\n"
    "s_add_u32 s76, s76, 16384\n"
    "v_mfma_f32_16x16x32_bf16 v[36:39], v[212:215], v[20:23], v[36:39]\n"
    "buffer_load_dwordx4 v1, s[36:39], s76 offen sc1 lds\n"
    "v_mfma_f32_16x16x32_bf16 v[48:51], v[20:23], v[216:219], v[48:51]\n"
    "v_mfma_f32_16x16x32_bf16 v[56:59], v[20:23], v[220:223], v[56:59]\n"
    "s_add_u32 m0, s59, 32768\n"
    "s_add_u32 s76, s76, 16384\n"
    "v_mfma_f32_16x16x32_bf16 v[64:67], v[224:227], v[20:23], v[64:67]\n"
    "s_waitcnt lgkmcnt(0)\n"
    "buffer_load_dwordx4 v1, s[36:39], s76 offen sc1 lds\n"
    "v_mfma_f32_16x16x32_bf16 v[68:71], v[228:231], v[20:23], v[68:71]\n"
    "v_mfma_f32_16x16x32_bf16 v[32:35], v[232:235], v[24:27], v[32:35]\n"
    "v_mfma_f32_16x16x32_bf16 v[36:39], v[236:239], v[24:27], v[36:39]\n"
    "s_add_u32 m0, s59, 36864\n"
    "s_add_u32 s76, s76, 16384\n"
    "v_mfma_f32_16x16x32_bf16 v[48:51], v[24:27], v[240:243], v[48:51]\n"
    "buffer_load_dwordx4 v8, s[36:39], s76 offen sc1 lds\n"
    "v_mfma_f32_16x16x32_bf16 v[56:59], v[24:27], v[244:247], v[56:59]\n"
    "v_mfma_f32_16x16x32_bf16 v[64:67], v[248:251], v[24:27], v[64:67]\n"
    "v_mfma_f32_16x16x32_bf16 v[68:71], v[252:255], v[24:27], v[68:71]\n"
    "v_cvt_pk_bf16_f32 v32, v32, v33\n"
    "v_cvt_pk_bf16_f32 v33, v34, v35\n"
    "v_cvt_pk_bf16_f32 v34, v36, v37\n"
    "v_cvt_pk_bf16_f32 v35, v38, v39\n"
    "buffer_store_dwordx4 v[32:35], v1, s[36:39], s79 offen sc1\n"
    "v_cvt_pk_bf16_f32 v28, v48, v49\n"
    "v_cvt_pk_bf16_f32 v29, v50, v51\n"
    "v_cvt_pk_bf16_f32 v30, v56, v57\n"
    "v_cvt_pk_bf16_f32 v31, v58, v59\n"
    "v_cvt_pk_bf16_f32 v64, v64, v65\n"
    "v_cvt_pk_bf16_f32 v65, v66, v67\n"
    "v_cvt_pk_bf16_f32 v66, v68, v69\n"
    "v_cvt_pk_bf16_f32 v67, v70, v71\n"
    "buffer_store_dwordx4 v[64:67], v1, s[36:39], s73 offen sc1\n"
    "v_mov_b32 v12, s68\n"
    "s_mov_b32 s73, 0x80000000\n"
    "s_mov_b32 s79, 0x80000000\n"
    "s_mov_b32 s80, 0x80000000\n"
    "s_mov_b32 s68, s71\n"
    "s_mov_b32 s69, 0\n"
    "s_mov_b32 s70, 0x80000000\n"
    "s_waitcnt vmcnt(7)\n"
    "s_add_u32 s71, s68, 1\n"
    "v_add_u32 v13, v11, v3\n"
    "v_cmp_gt_i32 vcc, s71, v13\n"
    "s_nop 0\n"
    "s_or_b64 exec, vcc, s[62:63]\n"
    "buffer_atomic_smax v11, v2, s[36:39], 0 offen sc0 sc1\n"
    "s_mov_b64 exec, -1\n"
    "s_waitcnt vmcnt(3)\n"
    "s_branch .LgP_bar1_%=\n"
    ".LgP_idle0_%=:\n"
    "s_or_b64 exec, vcc, s[62:63]\n"
    "buffer_atomic_smax v5, v2, s[36:39], 0 offen sc0 sc1\n"
    "s_mov_b64 exec, -1\n"
    "s_waitcnt vmcnt(1)\n"
    "buffer_atomic_smax v12, v14, s[36:39], 0 offen sc1\n"
    "s_branch .LgP_block0_1_%=\n"
    ".LgP_block0_%=:\n"
    "v_add_u32 v13, v11, v3\n"
    "v_cmp_gt_i32 vcc, s71, v13\n"
    "s_nop 0\n"
    "s_or_b64 exec, vcc, s[62:63]\n"
    "buffer_atomic_smax v11, v2, s[36:39], 0 offen sc0 sc1\n"
    "s_mov_b64 exec, -1\n"
    "s_sleep 3\n"
    "s_waitcnt vmcnt(1)\n"
    ".LgP_block0_0_%=:\n"
    "v_add_u32 v13, v5, v3\n"
    "v_cmp_gt_i32 vcc, s71, v13\n"
    "s_cbranch_vccz .LgP_go0_%=\n"
    "v_add_u32 v13, v5, v3\n"
    "v_cmp_gt_i32 vcc, s71, v13\n"
    "s_nop 0\n"
    "s_or_b64 exec, vcc, s[62:63]\n"
    "buffer_atomic_smax v5, v2, s[36:39], 0 offen sc0 sc1\n"
    "s_mov_b64 exec, -1\n"
    "s_sleep 3\n"
    "s_waitcnt vmcnt(1)\n"
    ".LgP_block0_1_%=:\n"
    "v_add_u32 v13, v11, v3\n"
    "v_cmp_gt_i32 vcc, s71, v13\n"
    "s_cbranch_vccz .LgP_go0_%=\n"
    "s_sub_u32 s72, s72, 1\n"
    "s_cbranch_scc0 .LgP_block0_%=\n"
    "s_mov_b32 s72, 0\n"
    ".LgP_go0_%=:\n"
    "s_lshr_b32 s76, s71, 1\n"
    "s_and_b32 s76, s76, s54\n"
    "s_mul_i32 s76, s76, s53\n"
    "s_add_u32 s76, s76, s52\n"
    "s_add_u32 s76, s76, 1024\n"
    "s_add_u32 m0, s59, 20480\n"
    "s_nop 0\n"
    "buffer_load_dwordx4 v1, s[36:39], s76 offen sc1 lds\n"
    "s_add_u32 m0, s59, 24576\n"
    "s_add_u32 s76, s76, 16384\n"
    "buffer_load_dwordx4 v1, s[36:39], s76 offen sc1 lds\n"
    "s_add_u32 m0, s59, 28672\n"
    "s_add_u32 s76, s76, 16384\n"
    "buffer_load_dwordx4 v1, s[36:39], s76 offen sc1 lds\n"
    "s_add_u32 m0, s59, 32768\n"
    "s_add_u32 s76, s76, 16384\n"
    "buffer_load_dwordx4 v1, s[36:39], s76 offen sc1 lds\n"
    "s_add_u32 m0, s59, 36864\n"
    "s_add_u32 s76, s76, 16384\n"
    "buffer_load_dwordx4 v8, s[36:39], s76 offen sc1 lds\n"
    "s_mov_b32 s69, 0x80000000\n"
    "s_mov_b32 s70, 0x80000000\n"
    "s_waitcnt vmcnt(5)\n"
    "s_add_u32 s71, s68, 1\n"
    "v_add_u32 v13, v11, v3\n"
    "v_cmp_gt_i32 vcc, s71, v13\n"
    "s_nop 0\n"
    "s_or_b64 exec, vcc, s[62:63]\n"
    "buffer_atomic_smax v11, v2, s[36:39], 0 offen sc0 sc1\n"
    "s_mov_b64 exec, -1\n"
    "s_waitcnt vmcnt(1)\n"
    "s_branch .LgP_bar1_%=\n"
    ".LgP_bar0_%=:\n"
    "s_barrier\n"
    "ds_read_b128 v[16:19], v0 offset:0\n"
    "ds_read_b128 v[20:23], v0 offset:1024\n"
    "ds_read_b128 v[24:27], v0 offset:2048\n"
    "v_add_u32 v13, v5, v3\n"
    "v_cmp_gt_i32 vcc, s71, v13\n"
    "s_nop 0\n"
    "s_or_b64 exec, vcc, s[62:63]\n"
    "buffer_atomic_smax v5, v2, s[36:39], 0 offen sc0 sc1\n"
    "s_mov_b64 exec, -1\n"
    "s_lshr_b32 s76, s71, 1\n"
    "s_and_b32 s76, s76, s54\n"
    "s_mul_i32 s76, s76, s53\n"
    "s_add_u32 s76, s76, s52\n"
    "s_add_u32 s76, s76, 1024\n"
    "s_waitcnt lgkmcnt(2)\n"
    "s_branch .LgP_head0_%=\n"
    ".LgP_poll1_%=:\n"
    "v_mfma_f32_16x16x32_bf16 v[60:63], v[20:23], a[36:39], v[60:63]\n"
    "s_or_b64 exec, vcc, s[62:63]\n"
    "buffer_atomic_smax v11, v2, s[36:39], 0 offen sc0 sc1\n"
    "s_mov_b64 exec, -1\n"
    "buffer_store_dwordx4 v[64:67], v1, s[36:39], s73 offen sc1\n"
    "v_mfma_f32_16x16x32_bf16 v[72:75], a[40:43], v[20:23], v[72:75]\n"
    "s_waitcnt lgkmcnt(1)\n"
    "v_mfma_f32_16x16x32_bf16 v[76:79], a[44:47], v[20:23], v[76:79]\n"
    "ds_read_b128 v[20:23], v9 offset:4096\n"
    "v_mfma_f32_16x16x32_bf16 v[40:43], a[48:51], v[24:27], v[40:43]\n"
    "s_lshr_b32 s73, s68, 1\n"
    "v_mfma_f32_16x16x32_bf16 v[44:47], a[52:55], v[24:27], v[44:47]\n"
    "v_mfma_f32_16x16x32_bf16 v[52:55], v[24:27], a[56:59], v[52:55]\n"
    "s_and_b32 s73, s73, s57\n"
    "v_mfma_f32_16x16x32_bf16 v[60:63], v[24:27], a[60:63], v[60:63]\n"
    "v_mfma_f32_16x16x32_bf16 v[72:75], a[64:67], v[24:27], v[72:75]\n"
    "s_waitcnt lgkmcnt(1)\n"
    "v_mfma_f32_16x16x32_bf16 v[76:79], a[68:71], v[24:27], v[76:79]\n"
    "ds_read_b128 v[24:27], v9 offset:5120\n"
    "v_mfma_f32_16x16x32_bf16 v[40:43], a[72:75], v[16:19], v[40:43]\n"
    "s_mul_i32 s73, s73, s56\n"
    "v_mfma_f32_16x16x32_bf16 v[44:47], a[76:79], v[16:19], v[44:47]\n"
    "v_mfma_f32_16x16x32_bf16 v[52:55], v[16:19], a[80:83], v[52:55]\n"
    "s_add_u32 s73, s73, s55\n"
    "v_mfma_f32_16x16x32_bf16 v[60:63], v[16:19], a[84:87], v[60:63]\n"
    "v_mfma_f32_16x16x32_bf16 v[72:75], a[88:91], v[16:19], v[72:75]\n"
    "s_waitcnt lgkmcnt(1)\n"
    "v_mfma_f32_16x16x32_bf16 v[76:79], a[92:95], v[16:19], v[76:79]\n"
    "ds_read_b128 v[16:19], v9 offset:6144\n"
    "v_mfma_f32_16x16x32_bf16 v[40:43], a[96:99], v[20:23], v[40:43]\n"
    "s_add_u32 s79, s73, 16384\n"
    "s_waitcnt vmcnt(3)\n"
    "v_add_u32 v13, v5, v3\n"
    "v_mfma_f32_16x16x32_bf16 v[44:47], a[100:103], v[20:23], v[44:47]\n"
    "v_cmp_gt_i32 vcc, s71, v13\n"
    "v_mfma_f32_16x16x32_bf16 v[52:55], v[20:23], a[104:107], v[52:55]\n"
    "s_add_u32 s80, s73, 32768\n"
    "v_mfma_f32_16x16x32_bf16 v[60:63], v[20:23], a[108:111], v[60:63]\n"
    "v_mfma_f32_16x16x32_bf16 v[72:75], a[112:115], v[20:23], v[72:75]\n"
    "s_waitcnt lgkmcnt(1)\n"
    "s_cbranch_vccz .LgP_dma1_29_%=\n"
    "v_mfma_f32_16x16x32_bf16 v[76:79], a[116:119], v[20:23], v[76:79]\n"
    "ds_read_b128 v[20:23], v9 offset:7168\n"
    "s_or_b64 exec, vcc, s[62:63]\n"
    "buffer_atomic_smax v5, v2, s[36:39], 0 offen sc0 sc1\n"
    "s_mov_b64 exec, -1\n"
    "v_mfma_f32_16x16x32_bf16 v[40:43], a[120:123], v[24:27], v[40:43]\n"
    "s_waitcnt vmcnt(6)\n"
    "buffer_atomic_smax v12, v14, s[36:39], s69 offen sc1\n"
    "v_mfma_f32_16x16x32_bf16 v[44:47], a[124:127], v[24:27], v[44:47]\n"
    "v_mfma_f32_16x16x32_bf16 v[52:55], v[24:27], a[128:131], v[52:55]\n"
    "v_mfma_f32_16x16x32_bf16 v[60:63], v[24:27], a[132:135], v[60:63]\n"
    "v_mfma_f32_16x16x32_bf16 v[72:75], a[136:139], v[24:27], v[72:75]\n"
    "s_waitcnt lgkmcnt(1)\n"
    "v_mfma_f32_16x16x32_bf16 v[76:79], a[140:143], v[24:27], v[76:79]\n"
    "ds_read_b128 v[24:27], v9 offset:8192\n"
    "v_mfma_f32_16x16x32_bf16 v[40:43], a[144:147], v[16:19], v[40:43]\n"
    "v_mfma_f32_16x16x32_bf16 v[44:47], a[148:151], v[16:19], v[44:47]\n"
    "v_mfma_f32_16x16x32_bf16 v[52:55], v[16:19], a[152:155], v[52:55]\n"
    "v_mfma_f32_16x16x32_bf16 v[60:63], v[16:19], a[156:159], v[60:63]\n"
    "v_mfma_f32_16x16x32_bf16 v[72:75], a[160:163], v[16:19], v[72:75]\n"
    "s_waitcnt lgkmcnt(1)\n"
    "v_mfma_f32_16x16x32_bf16 v[76:79], a[164:167], v[16:19], v[76:79]\n"
    "ds_read_b128 v[16:19], v9 offset:9216\n"
    "v_mfma_f32_16x16x32_bf16 v[40:43], a[168:171], v[20:23], v[40:43]\n"
    "v_mfma_f32_16x16x32_bf16 v[44:47], a[172:175], v[20:23], v[44:47]\n"
    "v_mfma_f32_16x16x32_bf16 v[52:55], v[20:23], a[176:179], v[52:55]\n"
    "s_waitcnt vmcnt(3)\n"
    "v_add_u32 v13, v11, v3\n"
    "v_mfma_f32_16x16x32_bf16 v[60:63], v[20:23], a[180:183], v[60:63]\n"
    "v_cmp_gt_i32 vcc, s71, v13\n"
    "v_mfma_f32_16x16x32_bf16 v[72:75], a[184:187], v[20:23], v[72:75]\n"
    "s_waitcnt lgkmcnt(1)\n"
    "v_mfma_f32_16x16x32_bf16 v[76:79], a[188:191], v[20:23], v[76:79]\n"
    "ds_read_b128 v[20:23], v9 offset:10240\n"
    "v_mfma_f32_16x16x32_bf16 v[40:43], a[192:195], v[24:27], v[40:43]\n"
    "s_cbranch_vccz .LgP_dma1_49_%=\n"
    "v_mfma_f32_16x16x32_bf16 v[44:47], a[196:199], v[24:27], v[44:47]\n"
    "s_or_b64 exec, vcc, s[62:63]\n"
    "buffer_atomic_smax v11, v2, s[36:39], 0 offen sc0 sc1\n"
    "s_mov_b64 exec, -1\n"
    "s_waitcnt vmcnt(3)\n"
    "buffer_atomic_smax v12, v14, s[36:39], s70 offen sc1\n"
    "v_mfma_f32_16x16x32_bf16 v[52:55], v[24:27], a[200:203], v[52:55]\n"
    "v_mfma_f32_16x16x32_bf16 v[60:63], v[24:27], a[204:207], v[60:63]\n"
    "v_mfma_f32_16x16x32_bf16 v[72:75], a[208:211], v[24:27], v[72:75]\n"
    "s_waitcnt lgkmcnt(1)\n"
    "v_mfma_f32_16x16x32_bf16 v[76:79], a[212:215], v[24:27], v[76:79]\n"
    "ds_read_b128 v[24:27], v9 offset:11264\n"
    "v_mfma_f32_16x16x32_bf16 v[40:43], a[216:219], v[16:19], v[40:43]\n"
    "v_mfma_f32_16x16x32_bf16 v[44:47], a[220:223], v[16:19], v[44:47]\n"
    "v_mfma_f32_16x16x32_bf16 v[52:55], v[16:19], a[224:227], v[52:55]\n"
    "v_mfma_f32_16x16x32_bf16 v[60:63], v[16:19], a[228:231], v[60:63]\n"
    "v_mfma_f32_16x16x32_bf16 v[72:75], a[232:235], v[16:19], v[72:75]\n"
    "s_waitcnt lgkmcnt(1)\n"
    "v_mfma_f32_16x16x32_bf16 v[76:79], a[236:239], v[16:19], v[76:79]\n"
    "ds_read_b128 v[16:19], v9 offset:12288\n"
    "v_mfma_f32_16x16x32_bf16 v[40:43], a[240:243], v[20:23], v[40:43]\n"
    "v_mfma_f32_16x16x32_bf16 v[44:47], a[244:247], v[20:23], v[44:47]\n"
    "v_mfma_f32_16x16x32_bf16 v[52:55], v[20:23], a[248:251], v[52:55]\n"
    "v_mfma_f32_16x16x32_bf16 v[60:63], v[20:23], a[252:255], v[60:63]\n"
    "v_mfma_f32_16x16x32_bf16 v[72:75], v[80:83], v[20:23], v[72:75]\n"
    "s_waitcnt lgkmcnt(1)\n"
    "s_waitcnt vmcnt(3)\n"
    "v_add_u32 v13, v5, v3\n"
    "v_mfma_f32_16x16x32_bf16 v[76:79], v[84:87], v[20:23], v[76:79]\n"
    "ds_read_b128 v[20:23], v9 offset:13312\n"
    "v_cmp_gt_i32 vcc, s71, v13\n"
    "v_mfma_f32_16x16x32_bf16 v[40:43], v[88:91], v[24:27], v[40:43]\n"
    "v_mfma_f32_16x16x32_bf16 v[44:47], v[92:95], v[24:27], v[44:47]\n"
    "v_mfma_f32_16x16x32_bf16 v[52:55], v[24:27], v[96:99], v[52:55]\n"
    "s_cbranch_vccz .LgP_dma1_69_%=\n"
    "v_mfma_f32_16x16x32_bf16 v[60:63], v[24:27], v[100:103], v[60:63]\n"
    "s_or_b64 exec, vcc, s[62:63]\n"
    "buffer_atomic_smax v5, v2, s[36:39], 0 offen sc0 sc1\n"
    "s_mov_b64 exec, -1\n"
    "v_mfma_f32_16x16x32_bf16 v[72:75], v[104:107], v[24:27], v[72:75]\n"
    "s_waitcnt lgkmcnt(1)\n"
    "v_mfma_f32_16x16x32_bf16 v[76:79], v[108:111], v[24:27], v[76:79]\n"
    "ds_read_b128 v[24:27], v9 offset:14336\n"
    "v_mfma_f32_16x16x32_bf16 v[40:43], v[112:115], v[16:19], v[40:43]\n"
    "v_mfma_f32_16x16x32_bf16 v[44:47], v[116:119], v[16:19], v[44:47]\n"
    "v_mfma_f32_16x16x32_bf16 v[52:55], v[16:19], v[120:123], v[52:55]\n"
    "v_mfma_f32_16x16x32_bf16 v[60:63], v[16:19], v[124:127], v[60:63]\n"
    "v_mfma_f32_16x16x32_bf16 v[72:75], v[128:131], v[16:19], v[72:75]\n"
    "s_waitcnt lgkmcnt(1)\n"
    "v_mfma_f32_16x16x32_bf16 v[76:79], v[132:135], v[16:19], v[76:79]\n"
    "ds_read_b128 v[16:19], v9 offset:15360\n"
    "v_mfma_f32_16x16x32_bf16 v[40:43], v[136:139], v[20:23], v[40:43]\n"
    "v_mfma_f32_16x16x32_bf16 v[44:47], v[140:143], v[20:23], v[44:47]\n"
    "v_mfma_f32_16x16x32_bf16 v[52:55], v[20:23], v[144:147], v[52:55]\n"
    "v_mfma_f32_16x16x32_bf16 v[60:63], v[20:23], v[148:151], v[60:63]\n"
    "v_mfma_f32_16x16x32_bf16 v[72:75], v[152:155], v[20:23], v[72:75]\n"
    "s_waitcnt lgkmcnt(1)\n"
    "v_mfma_f32_16x16x32_bf16 v[76:79], v[156:159], v[20:23], v[76:79]\n"
    "ds_read_b128 v[20:23], v9 offset:16384\n"
    "v_mfma_f32_16x16x32_bf16 v[40:43], v[160:163], v[24:27], v[40:43]\n"
    "s_waitcnt vmcnt(2)\n"
    "v_add_u32 v13, v11, v3\n"
    "v_mfma_f32_16x16x32_bf16 v[44:47], v[164:167], v[24:27], v[44:47]\n"
    "v_cmp_gt_i32 vcc, s71, v13\n"
    "v_mfma_f32_16x16x32_bf16 v[52:55], v[24:27], v[168:171], v[52:55]\n"
    "v_mfma_f32_16x16x32_bf16 v[60:63], v[24:27], v[172:175], v[60:63]\n"
    "v_mfma_f32_16x16x32_bf16 v[72:75], v[176:179], v[24:27], v[72:75]\n"
    "s_waitcnt lgkmcnt(1)\n"
    "s_cbranch_vccz .LgP_dma1_89_%=\n"
    "v_mfma_f32_16x16x32_bf16 v[76:79], v[180:183], v[24:27], v[76:79]\n"
    "ds_read_b128 v[24:27], v9 offset:17408\n"
    "s_or_b64 exec, vcc, s[62:63]\n"
    "buffer_atomic_smax v11, v2, s[36:39], 0 offen sc0 sc1\n"
    "s_mov_b64 exec, -1\n"
    "v_mfma_f32_16x16x32_bf16 v[40:43], v[184:187], v[16:19], v[40:43]\n"
    "v_mfma_f32_16x16x32_bf16 v[44:47], v[188:191], v[16:19], v[44:47]\n"
    "v_mfma_f32_16x16x32_bf16 v[52:55], v[16:19], v[192:195], v[52:55]\n"
    "v_mfma_f32_16x16x32_bf16 v[60:63], v[16:19], v[196:199], v[60:63]\n"
    "v_mfma_f32_16x16x32_bf16 v[72:75], v[200:203], v[16:19], v[72:75]\n"
    "s_waitcnt lgkmcnt(1)\n"
    "v_mfma_f32_16x16x32_bf16 v[76:79], v[204:207], v[16:19], v[76:79]\n"
    "v_mfma_f32_16x16x32_bf16 v[40:43], v[208:211], v[20:23], v[40:43]\n"
    "v_mfma_f32_16x16x32_bf16 v[44:47], v[212:215], v[20:23], v[44:47]\n"
    "v_mfma_f32_16x16x32_bf16 v[52:55], v[20:23], v[216:219], v[52:55]\n"
    "v_mfma_f32_16x16x32_bf16 v[60:63], v[20:23], v[220:223], v[60:63]\n"
    "v_mfma_f32_16x16x32_bf16 v[72:75], v[224:227], v[20:23], v[72:75]\n"
    "s_waitcnt lgkmcnt(0)\n"
    "v_mfma_f32_16x16x32_bf16 v[76:79], v[228:231], v[20:23], v[76:79]\n"
    "v_mfma_f32_16x16x32_bf16 v[40:43], v[232:235], v[24:27], v[40:43]\n"
    "v_mfma_f32_16x16x32_bf16 v[44:47], v[236:239], v[24:27], v[44:47]\n"
    "v_mfma_f32_16x16x32_bf16 v[52:55], v[24:27], v[240:243], v[52:55]\n"
    "v_mfma_f32_16x16x32_bf16 v[60:63], v[24:27], v[244:247], v[60:63]\n"
    "v_mfma_f32_16x16x32_bf16 v[72:75], v[248:251], v[24:27], v[72:75]\n"
    "v_mfma_f32_16x16x32_bf16 v[76:79], v[252:255], v[24:27], v[76:79]\n"
    "v_cvt_pk_bf16_f32 v40, v40, v41\n"
    "v_cvt_pk_bf16_f32 v41, v42, v43\n"
    "v_cvt_pk_bf16_f32 v42, v44, v45\n"
    "v_cvt_pk_bf16_f32 v43, v46, v47\n"
    "buffer_store_dwordx4 v[40:43], v1, s[36:39], s79 offen offset:1024 sc1\n"
    "v_cvt_pk_bf16_f32 v55, v54, v55\n"
    "v_cvt_pk_bf16_f32 v54, v52, v53\n"
    "v_mov_b64 v[52:53], v[28:29]\n"
    "buffer_store_dwordx4 v[52:55], v1, s[36:39], s80 offen sc1\n"
    "v_cvt_pk_bf16_f32 v63, v62, v63\n"
    "v_cvt_pk_bf16_f32 v62, v60, v61\n"
    "v_mov_b64 v[60:61], v[30:31]\n"
    "buffer_store_dwordx4 v[60:63], v1, s[36:39], s80 offen offset:1024 sc1\n"
    "v_cvt_pk_bf16_f32 v72, v72, v73\n"
    "v_cvt_pk_bf16_f32 v73, v74, v75\n"
    "v_cvt_pk_bf16_f32 v74, v76, v77\n"
    "v_cvt_pk_bf16_f32 v75, v78, v79\n"
    "buffer_store_dwordx4 v[72:75], v1, s[36:39], s73 offen offset:1024 sc1\n"
    "v_mov_b32 v12, s68\n"
    "s_mov_b32 s73, 0x80000000\n"
    "s_mov_b32 s79, 0x80000000\n"
    "s_mov_b32 s80, 0x80000000\n"
    "s_mov_b32 s68, s71\n"
    "s_cmp_eq_u32 s68, 128\n"
    "s_cbranch_scc1 .LgP_done_%=\n"
    "s_waitcnt vmcnt(5)\n"
    "v_add_u32 v13, v5, v3\n"
    "v_cmp_gt_i32 vcc, s71, v13\n"
    "s_cbranch_vccnz .LgP_idle1_%=\n"
    "s_add_u32 m0, s59, 0\n"
    "s_nop 0\n"
    "buffer_load_dwordx4 v1, s[36:39], s76 offen sc1 lds\n"
    "s_add_u32 m0, s59, 4096\n"
    "s_add_u32 s76, s76, 16384\n"
    "buffer_load_dwordx4 v1, s[36:39], s76 offen sc1 lds\n"
    "s_add_u32 m0, s59, 8192\n"
    "s_add_u32 s76, s76, 16384\n"
    "buffer_load_dwordx4 v1, s[36:39], s76 offen sc1 lds\n"
    "s_add_u32 m0, s59, 12288\n"
    "s_add_u32 s76, s76, 16384\n"
    "buffer_load_dwordx4 v1, s[36:39], s76 offen sc1 lds\n"
    "s_add_u32 m0, s59, 16384\n"
    "s_add_u32 s76, s76, 16384\n"
    "buffer_load_dwordx4 v8, s[36:39], s76 offen sc1 lds\n"
    "s_waitcnt vmcnt(5)\n"
    "buffer_atomic_smax v12, v10, s[36:39], 0 offen sc1\n"
    "s_mov_b32 s69, 0x80000000\n"
    "s_mov_b32 s70, 0x80000000\n"
    "s_waitcnt vmcnt(10)\n"
    "s_add_u32 s71, s68, 1\n"
    "v_add_u32 v13, v11, v3\n"
    "v_cmp_gt_i32 vcc, s71, v13\n"
    "s_nop 0\n"
    "s_or_b64 exec, vcc, s[62:63]\n"
    "buffer_atomic_smax v11, v2, s[36:39], 0 offen sc0 sc1\n"
    "s_mov_b64 exec, -1\n"
    "s_waitcnt vmcnt(2)\n"
    "s_branch .LgP_bar0_%=\n"
    ".LgP_dma1_29_%=:\n"
    "v_mfma_f32_16x16x32_bf16 v[76:79], a[116:119], v[20:23], v[76:79]\n"
    "ds_read_b128 v[20:23], v9 offset:7168\n"
    "v_mfma_f32_16x16x32_bf16 v[40:43], a[120:123], v[24:27], v[40:43]\n"
    "s_add_u32 m0, s59, 0\n"
    "s_waitcnt vmcnt(5)\n"
    "buffer_atomic_smax v12, v14, s[36:39], s69 offen sc1\n"
    "v_mfma_f32_16x16x32_bf16 v[44:47], a[124:127], v[24:27], v[44:47]\n"
    "buffer_load_dwordx4 v1, s[36:39], s76 offen sc1 lds\n"
    "v_mfma_f32_16x16x32_bf16 v[52:55], v[24:27], a[128:131], v[52:55]\n"
    "s_add_u32 m0, s59, 4096\n"
    "s_add_u32 s76, s76, 16384\n"
    "v_mfma_f32_16x16x32_bf16 v[60:63], v[24:27], a[132:135], v[60:63]\n"
    "buffer_load_dwordx4 v1, s[36:39], s76 offen sc1 lds\n"
    "v_mfma_f32_16x16x32_bf16 v[72:75], a[136:139], v[24:27], v[72:75]\n"
    "s_waitcnt lgkmcnt(1)\n"
    "v_mfma_f32_16x16x32_bf16 v[76:79], a[140:143], v[24:27], v[76:79]\n"
    "ds_read_b128 v[24:27], v9 offset:8192\n"
    "v_mfma_f32_16x16x32_bf16 v[40:43], a[144:147], v[16:19], v[40:43]\n"
    "s_add_u32 m0, s59, 8192\n"
    "s_add_u32 s76, s76, 16384\n"
    "v_mfma_f32_16x16x32_bf16 v[44:47], a[148:151], v[16:19], v[44:47]\n"
    "buffer_load_dwordx4 v1, s[36:39], s76 offen sc1 lds\n"
    "v_mfma_f32_16x16x32_bf16 v[52:55], v[16:19], a[152:155], v[52:55]\n"
    "v_mfma_f32_16x16x32_bf16 v[60:63], v[16:19], a[156:159], v[60:63]\n"
    "s_add_u32 m0, s59, 12288\n"
    "s_add_u32 s76, s76, 16384\n"
    "v_mfma_f32_16x16x32_bf16 v[72:75], a[160:163], v[16:19], v[72:75]\n"
    "s_waitcnt lgkmcnt(1)\n"
    "buffer_load_dwordx4 v1, s[36:39], s76 offen sc1 lds\n"
    "v_mfma_f32_16x16x32_bf16 v[76:79], a[164:167], v[16:19], v[76:79]\n"
    "ds_read_b128 v[16:19], v9 offset:9216\n"
    "v_mfma_f32_16x16x32_bf16 v[40:43], a[168:171], v[20:23], v[40:43]\n"
    "v_mfma_f32_16x16x32_bf16 v[44:47], a[172:175], v[20:23], v[44:47]\n"
    "s_add_u32 m0, s59, 16384\n"
    "s_add_u32 s76, s76, 16384\n"
    "v_mfma_f32_16x16x32_bf16 v[52:55], v[20:23], a[176:179], v[52:55]\n"
    "buffer_load_dwordx4 v8, s[36:39], s76 offen sc1 lds\n"
    "v_mfma_f32_16x16x32_bf16 v[60:63], v[20:23], a[180:183], v[60:63]\n"
    "v_mfma_f32_16x16x32_bf16 v[72:75], a[184:187], v[20:23], v[72:75]\n"
    "s_waitcnt lgkmcnt(1)\n"
    "v_mfma_f32_16x16x32_bf16 v[76:79], a[188:191], v[20:23], v[76:79]\n"
    "ds_read_b128 v[20:23], v9 offset:10240\n"
    "v_mfma_f32_16x16x32_bf16 v[40:43], a[192:195], v[24:27], v[40:43]\n"
    "v_mfma_f32_16x16x32_bf16 v[44:47], a[196:199], v[24:27], v[44:47]\n"
    "s_waitcnt vmcnt(6)\n"
    "buffer_atomic_smax v12, v14, s[36:39], s70 offen sc1\n"
    "v_mfma_f32_16x16x32_bf16 v[52:55], v[24:27], a[200:203], v[52:55]\n"
    "v_mfma_f32_16x16x32_bf16 v[60:63], v[24:27], a[204:207], v[60:63]\n"
    "v_mfma_f32_16x16x32_bf16 v[72:75], a[208:211], v[24:27], v[72:75]\n"
    "s_waitcnt lgkmcnt(1)\n"
    "v_mfma_f32_16x16x32_bf16 v[76:79], a[212:215], v[24:27], v[76:79]\n"
    "ds_read_b128 v[24:27], v9 offset:11264\n"
    "v_mfma_f32_16x16x32_bf16 v[40:43], a[216:219], v[16:19], v[40:43]\n"
    "v_mfma_f32_16x16x32_bf16 v[44:47], a[220:223], v[16:19], v[44:47]\n"
    "v_mfma_f32_16x16x32_bf16 v[52:55], v[16:19], a[224:227], v[52:55]\n"
    "v_mfma_f32_16x16x32_bf16 v[60:63], v[16:19], a[228:231], v[60:63]\n"
    "v_mfma_f32_16x16x32_bf16 v[72:75], a[232:235], v[16:19], v[72:75]\n"
    "s_waitcnt lgkmcnt(1)\n"
    "v_mfma_f32_16x16x32_bf16 v[76:79], a[236:239], v[16:19], v[76:79]\n"
    "ds_read_b128 v[16:19], v9 offset:12288\n"
    "v_mfma_f32_16x16x32_bf16 v[40:43], a[240:243], v[20:23], v[40:43]\n"
    "v_mfma_f32_16x16x32_bf16 v[44:47], a[244:247], v[20:23], v[44:47]\n"
    "v_mfma_f32_16x16x32_bf16 v[52:55], v[20:23], a[248:251], v[52:55]\n"
    "v_mfma_f32_16x16x32_bf16 v[60:63], v[20:23], a[252:255], v[60:63]\n"
    "v_mfma_f32_16x16x32_bf16 v[72:75], v[80:83], v[20:23], v[72:75]\n"
    "s_waitcnt lgkmcnt(1)\n"
    "v_mfma_f32_16x16x32_bf16 v[76:79], v[84:87], v[20:23], v[76:79]\n"
    "ds_read_b128 v[20:23], v9 offset:13312\n"
    "v_mfma_f32_16x16x32_bf16 v[40:43], v[88:91], v[24:27], v[40:43]\n"
    "v_mfma_f32_16x16x32_bf16 v[44:47], v[92:95], v[24:27], v[44:47]\n"
    "v_mfma_f32_16x16x32_bf16 v[52:55], v[24:27], v[96:99], v[52:55]\n"
    "v_mfma_f32_16x16x32_bf16 v[60:63], v[24:27], v[100:103], v[60:63]\n"
    "v_mfma_f32_16x16x32_bf16 v[72:75], v[104:107], v[24:27], v[72:75]\n"
    "s_waitcnt lgkmcnt(1)\n"
    "v_mfma_f32_16x16x32_bf16 v[76:79], v[108:111], v[24:27], v[76:79]\n"
    "ds_read_b128 v[24:27], v9 offset:14336\n"
    "s_branch .LgP_join1_%=\n"
    ".LgP_dma1_49_%=:\n"
    "v_mfma_f32_16x16x32_bf16 v[44:47], a[196:199], v[24:27], v[44:47]\n"
    "s_add_u32 m0, s59, 0\n"
    "s_waitcnt vmcnt(2)\n"
    "buffer_atomic_smax v12, v14, s[36:39], s70 offen sc1\n"
    "v_mfma_f32_16x16x32_bf16 v[52:55], v[24:27], a[200:203], v[52:55]\n"
    "buffer_load_dwordx4 v1, s[36:39], s76 offen sc1 lds\n"
    "v_mfma_f32_16x16x32_bf16 v[60:63], v[24:27], a[204:207], v[60:63]\n"
    "s_add_u32 m0, s59, 4096\n"
    "s_add_u32 s76, s76, 16384\n"
    "v_mfma_f32_16x16x32_bf16 v[72:75], a[208:211], v[24:27], v[72:75]\n"
    "s_waitcnt lgkmcnt(1)\n"
    "buffer_load_dwordx4 v1, s[36:39], s76 offen sc1 lds\n"
    "v_mfma_f32_16x16x32_bf16 v[76:79], a[212:215], v[24:27], v[76:79]\n"
    "ds_read_b128 v[24:27], v9 offset:11264\n"
    "v_mfma_f32_16x16x32_bf16 v[40:43], a[216:219], v[16:19], v[40:43]\n"
    "v_mfma_f32_16x16x32_bf16 v[44:47], a[220:223], v[16:19], v[44:47]\n"
    "v_mfma_f32_16x16x32_bf16 v[52:55], v[16:19], a[224:227], v[52:55]\n"
    "s_add_u32 m0, s59, 8192\n"
    "s_add_u32 s76, s76, 16384\n"
    "v_mfma_f32_16x16x32_bf16 v[60:63], v[16:19], a[228:231], v[60:63]\n"
    "buffer_load_dwordx4 v1, s[36:39], s76 offen sc1 lds\n"
    "v_mfma_f32_16x16x32_bf16 v[72:75], a[232:235], v[16:19], v[72:75]\n"
    "s_waitcnt lgkmcnt(1)\n"
    "v_mfma_f32_16x16x32_bf16 v[76:79], a[236:239], v[16:19], v[76:79]\n"
    "ds_read_b128 v[16:19], v9 offset:12288\n"
    "v_mfma_f32_16x16x32_bf16 v[40:43], a[240:243], v[20:23], v[40:43]\n"
    "s_add_u32 m0, s59, 12288\n"
    "s_add_u32 s76, s76, 16384\n"
    "v_mfma_f32_16x16x32_bf16 v[44:47], a[244:247], v[20:23], v[44:47]\n"
    "buffer_load_dwordx4 v1, s[36:39], s76 offen sc1 lds\n"
    "v_mfma_f32_16x16x32_bf16 v[52:55], v[20:23], a[248:251], v[52:55]\n"
    "v_mfma_f32_16x16x32_bf16 v[60:63], v[20:23], a[252:255], v[60:63]\n"
    "s_add_u32 m0, s59, 16384\n"
    "s_add_u32 s76, s76, 16384\n"
    "v_mfma_f32_16x16x32_bf16 v[72:75], v[80:83], v[20:23], v[72:75]\n"
    "s_waitcnt lgkmcnt(1)\n"
    "buffer_load_dwordx4 v8, s[36:39], s76 offen sc1 lds\n"
    "v_mfma_f32_16x16x32_bf16 v[76:79], v[84:87], v[20:23], v[76:79]\n"
    "ds_read_b128 v[20:23], v9 offset:13312\n"
    "v_mfma_f32_16x16x32_bf16 v[40:43], v[88:91], v[24:27], v[40:43]\n"
    "v_mfma_f32_16x16x32_bf16 v[44:47], v[92:95], v[24:27], v[44:47]\n"
    "v_mfma_f32_16x16x32_bf16 v[52:55], v[24:27], v[96:99], v[52:55]\n"
    "v_mfma_f32_16x16x32_bf16 v[60:63], v[24:27], v[100:103], v[60:63]\n"
    "v_mfma_f32_16x16x32_bf16 v[72:75], v[104:107], v[24:27], v[72:75]\n"
    "s_waitcnt lgkmcnt(1)\n"
    "v_mfma_f32_16x16x32_bf16 v[76:79], v[108:111], v[24:27], v[76:79]\n"
    "ds_read_b128 v[24:27], v9 offset:14336\n"
    "s_branch .LgP_join1_%=\n"
    ".LgP_dma1_69_%=:\n"
    "v_mfma_f32_16x16x32_bf16 v[60:63], v[24:27], v[100:103], v[60:63]\n"
    "s_add_u32 m0, s59, 0\n"
    "v_mfma_f32_16x16x32_bf16 v[72:75], v[104:107], v[24:27], v[72:75]\n"
    "s_waitcnt lgkmcnt(1)\n"
    "buffer_load_dwordx4 v1, s[36:39], s76 offen sc1 lds\n"
    "v_mfma_f32_16x16x32_bf16 v[76:79], v[108:111], v[24:27], v[76:79]\n"
    "ds_read_b128 v[24:27], v9 offset:14336\n"
    "v_mfma_f32_16x16x32_bf16 v[40:43], v[112:115], v[16:19], v[40:43]\n"
    "v_mfma_f32_16x16x32_bf16 v[44:47], v[116:119], v[16:19], v[44:47]\n"
    "s_add_u32 m0, s59, 4096\n"
    "s_add_u32 s76, s76, 16384\n"
    "v_mfma_f32_16x16x32_bf16 v[52:55], v[16:19], v[120:123], v[52:55]\n"
    "buffer_load_dwordx4 v1, s[36:39], s76 offen sc1 lds\n"
    "v_mfma_f32_16x16x32_bf16 v[60:63], v[16:19], v[124:127], v[60:63]\n"
    "v_mfma_f32_16x16x32_bf16 v[72:75], v[128:131], v[16:19], v[72:75]\n"
    "s_waitcnt lgkmcnt(1)\n"
    "v_mfma_f32_16x16x32_bf16 v[76:79], v[132:135], v[16:19], v[76:79]\n"
    "ds_read_b128 v[16:19], v9 offset:15360\n"
    "v_mfma_f32_16x16x32_bf16 v[40:43], v[136:139], v[20:23], v[40:43]\n"
    "s_add_u32 m0, s59, 8192\n"
    "s_add_u32 s76, s76, 16384\n"
    "v_mfma_f32_16x16x32_bf16 v[44:47], v[140:143], v[20:23], v[44:47]\n"
    "buffer_load_dwordx4 v1, s[36:39], s76 offen sc1 lds\n"
    "v_mfma_f32_16x16x32_bf16 v[52:55], v[20:23], v[144:147], v[52:55]\n"
    "s_add_u32 m0, s59, 12288\n"
    "s_add_u32 s76, s76, 16384\n"
    "v_mfma_f32_16x16x32_bf16 v[60:63], v[20:23], v[148:151], v[60:63]\n"
    "buffer_load_dwordx4 v1, s[36:39], s76 offen sc1 lds\n"
    "v_mfma_f32_16x16x32_bf16 v[72:75], v[152:155], v[20:23], v[72:75]\n"
    "s_waitcnt lgkmcnt(1)\n"
    "v_mfma_f32_16x16x32_bf16 v[76:79], v[156:159], v[20:23], v[76:79]\n"
    "ds_read_b128 v[20:23], v9 offset:16384\n"
    "v_mfma_f32_16x16x32_bf16 v[40:43], v[160:163], v[24:27], v[40:43]\n"
    "v_mfma_f32_16x16x32_bf16 v[44:47], v[164:167], v[24:27], v[44:47]\n"
    "s_add_u32 m0, s59, 16384\n"
    "s_add_u32 s76, s76, 16384\n"
    "v_mfma_f32_16x16x32_bf16 v[52:55], v[24:27], v[168:171], v[52:55]\n"
    "buffer_load_dwordx4 v8, s[36:39], s76 offen sc1 lds\n"
    "v_mfma_f32_16x16x32_bf16 v[60:63], v[24:27], v[172:175], v[60:63]\n"
    "v_mfma_f32_16x16x32_bf16 v[72:75], v[176:179], v[24:27], v[72:75]\n"
    "s_waitcnt lgkmcnt(1)\n"
    "v_mfma_f32_16x16x32_bf16 v[76:79], v[180:183], v[24:27], v[76:79]\n"
    "ds_read_b128 v[24:27], v9 offset:17408\n"
    "v_mfma_f32_16x16x32_bf16 v[40:43], v[184:187], v[16:19], v[40:43]\n"
    "v_mfma_f32_16x16x32_bf16 v[44:47], v[188:191], v[16:19], v[44:47]\n"
    "v_mfma_f32_16x16x32_bf16 v[52:55], v[16:19], v[192:195], v[52:55]\n"
    "v_mfma_f32_16x16x32_bf16 v[60:63], v[16:19], v[196:199], v[60:63]\n"
    "v_mfma_f32_16x16x32_bf16 v[72:75], v[200:203], v[16:19], v[72:75]\n"
    "s_waitcnt lgkmcnt(1)\n"
    "v_mfma_f32_16x16x32_bf16 v[76:79], v[204:207], v[16:19], v[76:79]\n"
    "v_mfma_f32_16x16x32_bf16 v[40:43], v[208:211], v[20:23], v[40:43]\n"
    "v_mfma_f32_16x16x32_bf16 v[44:47], v[212:215], v[20:23], v[44:47]\n"
    "v_mfma_f32_16x16x32_bf16 v[52:55], v[20:23], v[216:219], v[52:55]\n"
    "v_mfma_f32_16x16x32_bf16 v[60:63], v[20:23], v[220:223], v[60:63]\n"
    "v_mfma_f32_16x16x32_bf16 v[72:75], v[224:227], v[20:23], v[72:75]\n"
    "s_waitcnt lgkmcnt(0)\n"
    "v_mfma_f32_16x16x32_bf16 v[76:79], v[228:231], v[20:23], v[76:79]\n"
    "v_mfma_f32_16x16x32_bf16 v[40:43], v[232:235], v[24:27], v[40:43]\n"
    "v_mfma_f32_16x16x32_bf16 v[44:47], v[236:239], v[24:27], v[44:47]\n"
    "v_mfma_f32_16x16x32_bf16 v[52:55], v[24:27], v[240:243], v[52:55]\n"
    "v_mfma_f32_16x16x32_bf16 v[60:63], v[24:27], v[244:247], v[60:63]\n"
    "v_mfma_f32_16x16x32_bf16 v[72:75], v[248:251], v[24:27], v[72:75]\n"
    "v_mfma_f32_16x16x32_bf16 v[76:79], v[252:255], v[24:27], v[76:79]\n"
    "v_cvt_pk_bf16_f32 v40, v40, v41\n"
    "v_cvt_pk_bf16_f32 v41, v42, v43\n"
    "v_cvt_pk_bf16_f32 v42, v44, v45\n"
    "v_cvt_pk_bf16_f32 v43, v46, v47\n"
    "buffer_store_dwordx4 v[40:43], v1, s[36:39], s79 offen offset:1024 sc1\n"
    "v_cvt_pk_bf16_f32 v55, v54, v55\n"
    "v_cvt_pk_bf16_f32 v54, v52, v53\n"
    "v_mov_b64 v[52:53], v[28:29]\n"
    "buffer_store_dwordx4 v[52:55], v1, s[36:39], s80 offen sc1\n"
    "v_cvt_pk_bf16_f32 v63, v62, v63\n"
    "v_cvt_pk_bf16_f32 v62, v60, v61\n"
    "v_mov_b64 v[60:61], v[30:31]\n"
    "buffer_store_dwordx4 v[60:63], v1, s[36:39], s80 offen offset:1024 sc1\n"
    "v_cvt_pk_bf16_f32 v72, v72, v73\n"
    "v_cvt_pk_bf16_f32 v73, v74, v75\n"
    "v_cvt_pk_bf16_f32 v74, v76, v77\n"
    "v_cvt_pk_bf16_f32 v75, v78, v79\n"
    "buffer_store_dwordx4 v[72:75], v1, s[36:39], s73 offen offset:1024 sc1\n"
    "v_mov_b32 v12, s68\n"
    "s_mov_b32 s73, 0x80000000\n"
    "s_mov_b32 s79, 0x80000000\n"
    "s_mov_b32 s80, 0x80000000\n"
    "s_mov_b32 s68, s71\n"
    "s_mov_b32 s69, 0\n"
    "s_mov_b32 s70, 0x80000000\n"
    "s_waitcnt vmcnt(10)\n"
    "s_add_u32 s71, s68, 1\n"
    "v_add_u32 v13, v11, v3\n"
    "v_cmp_gt_i32 vcc, s71, v13\n"
    "s_nop 0\n"
    "s_or_b64 exec, vcc, s[62:63]\n"
    "buffer_atomic_smax v11, v2, s[36:39], 0 offen sc0 sc1\n"
    "s_mov_b64 exec, -1\n"
    "s_waitcnt vmcnt(5)\n"
    "s_branch .LgP_bar0_%=\n"
    ".LgP_dma1_89_%=:\n"
    "v_mfma_f32_16x16x32_bf16 v[76:79], v[180:183], v[24:27], v[76:79]\n"
    "ds_read_b128 v[24:27], v9 offset:17408\n"
    "v_mfma_f32_16x16x32_bf16 v[40:43], v[184:187], v[16:19], v[40:43]\n"
    "s_add_u32 m0, s59, 0\n"
    "v_mfma_f32_16x16x32_bf16 v[44:47], v[188:191], v[16:19], v[44:47]\n"
    "buffer_load_dwordx4 v1, s[36:39], s76 offen sc1 lds\n"
    "v_mfma_f32_16x16x32_bf16 v[52:55], v[16:19], v[192:195], v[52:55]\n"
    "s_add_u32 m0, s59, 4096\n"
    "s_add_u32 s76, s76, 16384\n"
    "v_mfma_f32_16x16x32_bf16 v[60:63], v[16:19], v[196:199], v[60:63]\n"
    "buffer_load_dwordx4 v1, s[36:39], s76 offen sc1 lds\n"
    "v_mfma_f32_16x16x32_bf16 v[72:75], v[200:203], v[16:19], v[72:75]\n"
    "s_waitcnt lgkmcnt(1)\n"
    "v_mfma_f32_16x16x32_bf16 v[76:79], v[204:207], v[16:19], v[76:79]\n"
    "v_mfma_f32_16x16x32_bf16 v[40:43], v[208:211], v[20:23], v[40:43]\n"
    "s_add_u32 m0, s59, 8192\n"
    "s_add_u32 s76, s76, 16384\n"
    "v_mfma_f32_16x16x32_bf16 v[44:47], v[212:215], v[20:23], v[44:47]\n"
    "buffer_load_dwordx4 v1, s[36:39], s76 offen sc1 lds\n"
    "v_mfma_f32_16x16x32_bf16 v[52:55], v[20:23], v[216:219], v[52:55]\n"
    "v_mfma_f32_16x16x32_bf16 v[60:63], v[20:23], v[220:223], v[60:63]\n"
    "s_add_u32 m0, s59, 12288\n"
    "s_add_u32 s76, s76, 16384\n"
    "v_mfma_f32_16x16x32_bf16 v[72:75], v[224:227], v[20:23], v[72:75]\n"
    "s_waitcnt lgkmcnt(0)\n"
    "buffer_load_dwordx4 v1, s[36:39], s76 offen sc1 lds\n"
    "v_mfma_f32_16x16x32_bf16 v[76:79], v[228:231], v[20:23], v[76:79]\n"
    "v_mfma_f32_16x16x32_bf16 v[40:43], v[232:235], v[24:27], v[40:43]\n"
    "v_mfma_f32_16x16x32_bf16 v[44:47], v[236:239], v[24:27], v[44:47]\n"
    "s_add_u32 m0, s59, 16384\n"
    "s_add_u32 s76, s76, 16384\n"
    "v_mfma_f32_16x16x32_bf16 v[52:55], v[24:27], v[240:243], v[52:55]\n"
    "buffer_load_dwordx4 v8, s[36:39], s76 offen sc1 lds\n"
    "v_mfma_f32_16x16x32_bf16 v[60:63], v[24:27], v[244:247], v[60:63]\n"
    "v_mfma_f32_16x16x32_bf16 v[72:75], v[248:251], v[24:27], v[72:75]\n"
    "v_mfma_f32_16x16x32_bf16 v[76:79], v[252:255], v[24:27], v[76:79]\n"
    "v_cvt_pk_bf16_f32 v40, v40, v41\n"
    "v_cvt_pk_bf16_f32 v41, v42, v43\n"
    "v_cvt_pk_bf16_f32 v42, v44, v45\n"
    "v_cvt_pk_bf16_f32 v43, v46, v47\n"
    "buffer_store_dwordx4 v[40:43], v1, s[36:39], s79 offen offset:1024 sc1\n"
    "v_cvt_pk_bf16_f32 v55, v54, v55\n"
    "v_cvt_pk_bf16_f32 v54, v52, v53\n"
    "v_mov_b64 v[52:53], v[28:29]\n"
    "buffer_store_dwordx4 v[52:55], v1, s[36:39], s80 offen sc1\n"
    "v_cvt_pk_bf16_f32 v63, v62, v63\n"
    "v_cvt_pk_bf16_f32 v62, v60, v61\n"
    "v_mov_b64 v[60:61], v[30:31]\n"
    "buffer_store_dwordx4 v[60:63], v1, s[36:39], s80 offen offset:1024 sc1\n"
    "v_cvt_pk_bf16_f32 v72, v72, v73\n"
    "v_cvt_pk_bf16_f32 v73, v74, v75\n"
    "v_cvt_pk_bf16_f32 v74, v76, v77\n"
    "v_cvt_pk_bf16_f32 v75, v78, v79\n"
    "buffer_store_dwordx4 v[72:75], v1, s[36:39], s73 offen offset:1024 sc1\n"
    "v_mov_b32 v12, s68\n"
    "s_mov_b32 s73, 0x80000000\n"
    "s_mov_b32 s79, 0x80000000\n"
    "s_mov_b32 s80, 0x80000000\n"
    "s_mov_b32 s68, s71\n"
    "s_mov_b32 s69, 0\n"
    "s_mov_b32 s70, 0x80000000\n"
    "s_waitcnt vmcnt(9)\n"
    "s_add_u32 s71, s68, 1\n"
    "v_add_u32 v13, v11, v3\n"
    "v_cmp_gt_i32 vcc, s71, v13\n"
    "s_nop 0\n"
    "s_or_b64 exec, vcc, s[62:63]\n"
    "buffer_atomic_smax v11, v2, s[36:39], 0 offen sc0 sc1\n"
    "s_mov_b64 exec, -1\n"
    "s_waitcnt vmcnt(5)\n"
    "s_branch .LgP_bar0_%=\n"
    ".LgP_idle1_%=:\n"
    "s_or_b64 exec, vcc, s[62:63]\n"
    "buffer_atomic_smax v5, v2, s[36:39], 0 offen sc0 sc1\n"
    "s_mov_b64 exec, -1\n"
    "s_waitcnt vmcnt(1)\n"
    "buffer_atomic_smax v12, v10, s[36:39], 0 offen sc1\n"
    "s_branch .LgP_block1_1_%=\n"
    ".LgP_block1_%=:\n"
    "v_add_u32 v13, v11, v3\n"
    "v_cmp_gt_i32 vcc, s71, v13\n"
    "s_nop 0\n"
    "s_or_b64 exec, vcc, s[62:63]\n"
    "buffer_atomic_smax v11, v2, s[36:39], 0 offen sc0 sc1\n"
    "s_mov_b64 exec, -1\n"
    "s_sleep 3\n"
    "s_waitcnt vmcnt(1)\n"
    ".LgP_block1_0_%=:\n"
    "v_add_u32 v13, v5, v3\n"
    "v_cmp_gt_i32 vcc, s71, v13\n"
    "s_cbranch_vccz .LgP_go1_%=\n"
    "v_add_u32 v13, v5, v3\n"
    "v_cmp_gt_i32 vcc, s71, v13\n"
    "s_nop 0\n"
    "s_or_b64 exec, vcc, s[62:63]\n"
    "buffer_atomic_smax v5, v2, s[36:39], 0 offen sc0 sc1\n"
    "s_mov_b64 exec, -1\n"
    "s_sleep 3\n"
    "s_waitcnt vmcnt(1)\n"
    ".LgP_block1_1_%=:\n"
    "v_add_u32 v13, v11, v3\n"
    "v_cmp_gt_i32 vcc, s71, v13\n"
    "s_cbranch_vccz .LgP_go1_%=\n"
    "s_sub_u32 s72, s72, 1\n"
    "s_cbranch_scc0 .LgP_block1_%=\n"
    "s_mov_b32 s72, 0\n"
    ".LgP_go1_%=:\n"
    "s_lshr_b32 s76, s71, 1\n"
    "s_and_b32 s76, s76, s54\n"
    "s_mul_i32 s76, s76, s53\n"
    "s_add_u32 s76, s76, s52\n"
    "s_add_u32 m0, s59, 0\n"
    "s_nop 0\n"
    "buffer_load_dwordx4 v1, s[36:39], s76 offen sc1 lds\n"
    "s_add_u32 m0, s59, 4096\n"
    "s_add_u32 s76, s76, 16384\n"
    "buffer_load_dwordx4 v1, s[36:39], s76 offen sc1 lds\n"
    "s_add_u32 m0, s59, 8192\n"
    "s_add_u32 s76, s76, 16384\n"
    "buffer_load_dwordx4 v1, s[36:39], s76 offen sc1 lds\n"
    "s_add_u32 m0, s59, 12288\n"
    "s_add_u32 s76, s76, 16384\n"
    "buffer_load_dwordx4 v1, s[36:39], s76 offen sc1 lds\n"
    "s_add_u32 m0, s59, 16384\n"
    "s_add_u32 s76, s76, 16384\n"
    "buffer_load_dwordx4 v8, s[36:39], s76 offen sc1 lds\n"
    "s_mov_b32 s69, 0x80000000\n"
    "s_mov_b32 s70, 0x80000000\n"
    "s_waitcnt vmcnt(5)\n"
    "s_add_u32 s71, s68, 1\n"
    "v_add_u32 v13, v11, v3\n"
    "v_cmp_gt_i32 vcc, s71, v13\n"
    "s_nop 0\n"
    "s_or_b64 exec, vcc, s[62:63]\n"
    "buffer_atomic_smax v11, v2, s[36:39], 0 offen sc0 sc1\n"
    "s_mov_b64 exec, -1\n"
    "s_waitcnt vmcnt(1)\n"
    "s_branch .LgP_bar0_%=\n"
    ".LgP_bar1_%=:\n"
    "s_barrier\n"
    "ds_read_b128 v[16:19], v9 offset:0\n"
    "ds_read_b128 v[20:23], v9 offset:1024\n"
    "ds_read_b128 v[24:27], v9 offset:2048\n"
    "v_add_u32 v13, v5, v3\n"
    "v_cmp_gt_i32 vcc, s71, v13\n"
    "s_nop 0\n"
    "s_or_b64 exec, vcc, s[62:63]\n"
    "buffer_atomic_smax v5, v2, s[36:39], 0 offen sc0 sc1\n"
    "s_mov_b64 exec, -1\n"
    "s_lshr_b32 s76, s71, 1\n"
    "s_and_b32 s76, s76, s54\n"
    "s_mul_i32 s76, s76, s53\n"
    "s_add_u32 s76, s76, s52\n"
    "s_waitcnt lgkmcnt(2)\n"
    "s_branch .LgP_head1_%=\n"
    ".LgP_FIN_%=:\n"
  );
}
DEVI void role_up(const Params& p, int L, int j, int i, u32 dbg, unsigned long long tag) {
  const int lane = threadIdx.x & 63, w = threadIdx.x >> 6, col = (6 * j + i) * 192;
  const u32 wb = 576u * 4608 * 2 - col * 2;
  GEMM_DECL;
  g_w0 = make_srd(p.w1 + col, wb); g_w1 = make_srd(p.w3 + col, wb); g_w2 = g_w1;
  g_ldw = 9216;
  g_in_base = pgb(L, B_XN2); g_in_stride = SLOT; g_in_mask = RX - 1;
  g_out_base = pgb(L, B_H + 9 * j); g_out_kb = 12 * i; g_out_mask = RH - 1;
  g_flag = lane < 3 ? mbox(L, j, SL_DOWN + lane, 4 * i + w) : OOB;
  g_n0 = 64; g_n1 = 0;
  g_paddr = mbox(L, j, SL_UP + i, lane);
  g_pbias = lane < 16 ? lane >> 3 : (lane < 19 ? 2 * RH : 1000);
  v16u sa, sb, va;
  gemm_args(GEMM_ARGS, p, dbg, tag, sa, sb, va);
  RUN_ASM(
    "s_nop 4\n"
    "s_getpc_b64 s[84:85]\n"
    ".LgUP_PC_%=:\n"
    "s_and_b32 s85, s85, 0xffff\n"
    "s_mov_b32 s86, .LgUP_FIN_%=-.LgUP_PC_%=\n"
    "s_mov_b32 s87, 0x20000\n"
    "v_mbcnt_lo_u32_b32 v40, -1, 0\n"
    "v_mbcnt_hi_u32_b32 v40, -1, v40\n"
    "v_lshlrev_b32 v40, 7, v40\n"
    "buffer_load_dword v41, v40, s[84:87], 0 offen\n"
    "v_add_u32 v40, 8192, v40\n"
    "buffer_load_dword v41, v40, s[84:87], 0 offen\n"
    "v_add_u32 v40, 8192, v40\n"
    "buffer_load_dword v41, v40, s[84:87], 0 offen\n"
    "s_mov_b32 s68, 0\n"
    "s_mov_b32 s71, 0\n"
    "v_add_u32 v9, 20480, v0\n"
    "s_mov_b32 s64, 0xbfb8aa3b\n"
    "s_mov_b32 s72, 262144\n"
    "s_add_u32 m0, s59, 0\n"
    "s_mov_b32 s74, 0\n"
    "buffer_load_dwordx4 v5, s[40:43], s74 offen lds\n"
    "s_add_u32 m0, s59, 4096\n"
    "s_mov_b32 s74, 128\n"
    "buffer_load_dwordx4 v5, s[40:43], s74 offen lds\n"
    "s_add_u32 m0, s59, 8192\n"
    "s_mov_b32 s74, 256\n"
    "buffer_load_dwordx4 v5, s[40:43], s74 offen lds\n"
    "s_add_u32 m0, s59, 12288\n"
    "s_mov_b32 s74, 0\n"
    "buffer_load_dwordx4 v5, s[44:47], s74 offen lds\n"
    "s_add_u32 m0, s59, 16384\n"
    "s_mov_b32 s74, 128\n"
    "buffer_load_dwordx4 v5, s[44:47], s74 offen lds\n"
    "s_add_u32 m0, s59, 20480\n"
    "s_mov_b32 s74, 256\n"
    "buffer_load_dwordx4 v5, s[44:47], s74 offen lds\n"
    "s_add_u32 m0, s59, 24576\n"
    "s_mov_b32 s74, 294912\n"
    "buffer_load_dwordx4 v5, s[40:43], s74 offen lds\n"
    "s_add_u32 m0, s59, 28672\n"
    "s_mov_b32 s74, 295040\n"
    "buffer_load_dwordx4 v5, s[40:43], s74 offen lds\n"
    "s_add_u32 m0, s59, 32768\n"
    "s_mov_b32 s74, 295168\n"
    "buffer_load_dwordx4 v5, s[40:43], s74 offen lds\n"
    "s_add_u32 m0, s59, 36864\n"
    "s_mov_b32 s74, 294912\n"
    "buffer_load_dwordx4 v5, s[44:47], s74 offen lds\n"
    "s_add_u32 m0, s59, 40960\n"
    "s_mov_b32 s74, 295040\n"
    "buffer_load_dwordx4 v5, s[44:47], s74 offen lds\n"
    "s_add_u32 m0, s59, 45056\n"
    "s_mov_b32 s74, 295168\n"
    "buffer_load_dwordx4 v5, s[44:47], s74 offen lds\n"
    "s_add_u32 m0, s59, 49152\n"
    "s_mov_b32 s74, 589824\n"
    "buffer_load_dwordx4 v5, s[40:43], s74 offen lds\n"
    "s_add_u32 m0, s59, 53248\n"
    "s_mov_b32 s74, 589952\n"
    "buffer_load_dwordx4 v5, s[40:43], s74 offen lds\n"
    "s_add_u32 m0, s59, 57344\n"
    "s_mov_b32 s74, 590080\n"
    "buffer_load_dwordx4 v5, s[40:43], s74 offen lds\n"
    "s_add_u32 m0, s59, 61440\n"
    "s_mov_b32 s74, 589824\n"
    "buffer_load_dwordx4 v5, s[44:47], s74 offen lds\n"
    "s_add_u32 m0, s59, 65536\n"
    "s_mov_b32 s74, 589952\n"
    "buffer_load_dwordx4 v5, s[44:47], s74 offen lds\n"
    "s_add_u32 m0, s59, 69632\n"
    "s_mov_b32 s74, 590080\n"
    "buffer_load_dwordx4 v5, s[44:47], s74 offen lds\n"
    "s_add_u32 m0, s59, 73728\n"
    "s_mov_b32 s74, 884736\n"
    "buffer_load_dwordx4 v5, s[40:43], s74 offen lds\n"
    "s_add_u32 m0, s59, 77824\n"
    "s_mov_b32 s74, 884864\n"
    "buffer_load_dwordx4 v5, s[40:43], s74 offen lds\n"
    "s_add_u32 m0, s59, 81920\n"
    "s_mov_b32 s74, 884992\n"
    "buffer_load_dwordx4 v5, s[40:43], s74 offen lds\n"
    "s_add_u32 m0, s59, 86016\n"
    "s_mov_b32 s74, 884736\n"
    "buffer_load_dwordx4 v5, s[44:47], s74 offen lds\n"
    "s_add_u32 m0, s59, 90112\n"
    "s_mov_b32 s74, 884864\n"
    "buffer_load_dwordx4 v5, s[44:47], s74 offen lds\n"
    "s_add_u32 m0, s59, 94208\n"
    "s_mov_b32 s74, 884992\n"
    "buffer_load_dwordx4 v5, s[44:47], s74 offen lds\n"
    "s_add_u32 m0, s59, 98304\n"
    "s_mov_b32 s74, 1179648\n"
    "buffer_load_dwordx4 v5, s[40:43], s74 offen lds\n"
    "s_add_u32 m0, s59, 102400\n"
    "s_mov_b32 s74, 1179776\n"
    "buffer_load_dwordx4 v5, s[40:43], s74 offen lds\n"
    "s_add_u32 m0, s59, 106496\n"
    "s_mov_b32 s74, 1179904\n"
    "buffer_load_dwordx4 v5, s[40:43], s74 offen lds\n"
    "s_add_u32 m0, s59, 110592\n"
    "s_mov_b32 s74, 1179648\n"
    "buffer_load_dwordx4 v5, s[44:47], s74 offen lds\n"
    "s_add_u32 m0, s59, 114688\n"
    "s_mov_b32 s74, 1179776\n"
    "buffer_load_dwordx4 v5, s[44:47], s74 offen lds\n"
    "s_add_u32 m0, s59, 118784\n"
    "s_mov_b32 s74, 1179904\n"
    "buffer_load_dwordx4 v5, s[44:47], s74 offen lds\n"
    "v_add_u32 v16, 49152, v6\n"
    "v_add_u32 v17, 98304, v6\n"
    "v_add_u32 v18, 49152, v7\n"
    "v_add_u32 v19, 98304, v7\n"
    "s_waitcnt vmcnt(24) lgkmcnt(0)\n"
    "s_barrier\n"
    "s_add_u32 m0, s59, 122880\n"
    "s_mov_b32 s74, 1474560\n"
    "buffer_load_dwordx4 v5, s[40:43], s74 offen lds\n"
    "s_add_u32 m0, s59, 126976\n"
    "s_mov_b32 s74, 1474688\n"
    "buffer_load_dwordx4 v5, s[40:43], s74 offen lds\n"
    "s_add_u32 m0, s59, 131072\n"
    "s_mov_b32 s74, 1474816\n"
    "buffer_load_dwordx4 v5, s[40:43], s74 offen lds\n"
    "s_add_u32 m0, s59, 135168\n"
    "s_mov_b32 s74, 1474560\n"
    "buffer_load_dwordx4 v5, s[44:47], s74 offen lds\n"
    "s_add_u32 m0, s59, 139264\n"
    "s_mov_b32 s74, 1474688\n"
    "buffer_load_dwordx4 v5, s[44:47], s74 offen lds\n"
    "s_add_u32 m0, s59, 143360\n"
    "s_mov_b32 s74, 1474816\n"
    "buffer_load_dwordx4 v5, s[44:47], s74 offen lds\n"
    "ds_read_b64_tr_b16 a[0:1], v6 offset:0\n"
    "ds_read_b64_tr_b16 a[2:3], v6 offset:512\n"
    "ds_read_b64_tr_b16 a[4:5], v6 offset:8\n"
    "ds_read_b64_tr_b16 a[6:7], v6 offset:520\n"
    "ds_read_b64_tr_b16 a[8:9], v7 offset:0\n"
    "ds_read_b64_tr_b16 a[10:11], v7 offset:512\n"
    "ds_read_b64_tr_b16 a[12:13], v6 offset:12288\n"
    "ds_read_b64_tr_b16 a[14:15], v6 offset:12800\n"
    "ds_read_b64_tr_b16 a[16:17], v6 offset:12296\n"
    "ds_read_b64_tr_b16 a[18:19], v6 offset:12808\n"
    "ds_read_b64_tr_b16 a[20:21], v7 offset:12288\n"
    "ds_read_b64_tr_b16 a[22:23], v7 offset:12800\n"
    "s_waitcnt vmcnt(24) lgkmcnt(0)\n"
    "s_barrier\n"
    "s_add_u32 m0, s59, 0\n"
    "s_mov_b32 s74, 1769472\n"
    "buffer_load_dwordx4 v5, s[40:43], s74 offen lds\n"
    "s_add_u32 m0, s59, 4096\n"
    "s_mov_b32 s74, 1769600\n"
    "buffer_load_dwordx4 v5, s[40:43], s74 offen lds\n"
    "s_add_u32 m0, s59, 8192\n"
    "s_mov_b32 s74, 1769728\n"
    "buffer_load_dwordx4 v5, s[40:43], s74 offen lds\n"
    "s_add_u32 m0, s59, 12288\n"
    "s_mov_b32 s74, 1769472\n"
    "buffer_load_dwordx4 v5, s[44:47], s74 offen lds\n"
    "s_add_u32 m0, s59, 16384\n"
    "s_mov_b32 s74, 1769600\n"
    "buffer_load_dwordx4 v5, s[44:47], s74 offen lds\n"
    "s_add_u32 m0, s59, 20480\n"
    "s_mov_b32 s74, 1769728\n"
    "buffer_load_dwordx4 v5, s[44:47], s74 offen lds\n"
    "ds_read_b64_tr_b16 a[24:25], v6 offset:24576\n"
    "ds_read_b64_tr_b16 a[26:27], v6 offset:25088\n"
    "ds_read_b64_tr_b16 a[28:29], v6 offset:24584\n"
    "ds_read_b64_tr_b16 a[30:31], v6 offset:25096\n"
    "ds_read_b64_tr_b16 a[32:33], v7 offset:24576\n"
    "ds_read_b64_tr_b16 a[34:35], v7 offset:25088\n"
    "ds_read_b64_tr_b16 a[36:37], v6 offset:36864\n"
    "ds_read_b64_tr_b16 a[38:39], v6 offset:37376\n"
    "ds_read_b64_tr_b16 a[40:41], v6 offset:36872\n"
    "ds_read_b64_tr_b16 a[42:43], v6 offset:37384\n"
    "ds_read_b64_tr_b16 a[44:45], v7 offset:36864\n"
    "ds_read_b64_tr_b16 a[46:47], v7 offset:37376\n"
    "s_waitcnt vmcnt(24) lgkmcnt(0)\n"
    "s_barrier\n"
    "s_add_u32 m0, s59, 24576\n"
    "s_mov_b32 s74, 2064384\n"
    "buffer_load_dwordx4 v5, s[40:43], s74 offen lds\n"
    "s_add_u32 m0, s59, 28672\n"
    "s_mov_b32 s74, 2064512\n"
    "buffer_load_dwordx4 v5, s[40:43], s74 offen lds\n"
    "s_add_u32 m0, s59, 32768\n"
    "s_mov_b32 s74, 2064640\n"
    "buffer_load_dwordx4 v5, s[40:43], s74 offen lds\n"
    "s_add_u32 m0, s59, 36864\n"
    "s_mov_b32 s74, 2064384\n"
    "buffer_load_dwordx4 v5, s[44:47], s74 offen lds\n"
    "s_add_u32 m0, s59, 40960\n"
    "s_mov_b32 s74, 2064512\n"
    "buffer_load_dwordx4 v5, s[44:47], s74 offen lds\n"
    "s_add_u32 m0, s59, 45056\n"
    "s_mov_b32 s74, 2064640\n"
    "buffer_load_dwordx4 v5, s[44:47], s74 offen lds\n"
    "ds_read_b64_tr_b16 a[48:49], v16 offset:0\n"
    "ds_read_b64_tr_b16 a[50:51], v16 offset:512\n"
    "ds_read_b64_tr_b16 a[52:53], v16 offset:8\n"
    "ds_read_b64_tr_b16 a[54:55], v16 offset:520\n"
    "ds_read_b64_tr_b16 a[56:57], v18 offset:0\n"
    "ds_read_b64_tr_b16 a[58:59], v18 offset:512\n"
    "ds_read_b64_tr_b16 a[60:61], v16 offset:12288\n"
    "ds_read_b64_tr_b16 a[62:63], v16 offset:12800\n"
    "ds_read_b64_tr_b16 a[64:65], v16 offset:12296\n"
    "ds_read_b64_tr_b16 a[66:67], v16 offset:12808\n"
    "ds_read_b64_tr_b16 a[68:69], v18 offset:12288\n"
    "ds_read_b64_tr_b16 a[70:71], v18 offset:12800\n"
    "s_waitcnt vmcnt(24) lgkmcnt(0)\n"
    "s_barrier\n"
    "s_add_u32 m0, s59, 49152\n"
    "s_mov_b32 s74, 2359296\n"
    "buffer_load_dwordx4 v5, s[40:43], s74 offen lds\n"
    "s_add_u32 m0, s59, 53248\n"
    "s_mov_b32 s74, 2359424\n"
    "buffer_load_dwordx4 v5, s[40:43], s74 offen lds\n"
    "s_add_u32 m0, s59, 57344\n"
    "s_mov_b32 s74, 2359552\n"
    "buffer_load_dwordx4 v5, s[40:43], s74 offen lds\n"
    "s_add_u32 m0, s59, 61440\n"
    "s_mov_b32 s74, 2359296\n"
    "buffer_load_dwordx4 v5, s[44:47], s74 offen lds\n"
    "s_add_u32 m0, s59, 65536\n"
    "s_mov_b32 s74, 2359424\n"
    "buffer_load_dwordx4 v5, s[44:47], s74 offen lds\n"
    "s_add_u32 m0, s59, 69632\n"
    "s_mov_b32 s74, 2359552\n"
    "buffer_load_dwordx4 v5, s[44:47], s74 offen lds\n"
    "ds_read_b64_tr_b16 a[72:73], v16 offset:24576\n"
    "ds_read_b64_tr_b16 a[74:75], v16 offset:25088\n"
    "ds_read_b64_tr_b16 a[76:77], v16 offset:24584\n"
    "ds_read_b64_tr_b16 a[78:79], v16 offset:25096\n"
    "ds_read_b64_tr_b16 a[80:81], v18 offset:24576\n"
    "ds_read_b64_tr_b16 a[82:83], v18 offset:25088\n"
    "ds_read_b64_tr_b16 a[84:85], v16 offset:36864\n"
    "ds_read_b64_tr_b16 a[86:87], v16 offset:37376\n"
    "ds_read_b64_tr_b16 a[88:89], v16 offset:36872\n"
    "ds_read_b64_tr_b16 a[90:91], v16 offset:37384\n"
    "ds_read_b64_tr_b16 a[92:93], v18 offset:36864\n"
    "ds_read_b64_tr_b16 a[94:95], v18 offset:37376\n"
    "s_waitcnt vmcnt(24) lgkmcnt(0)\n"
    "s_barrier\n"
    "s_add_u32 m0, s59, 73728\n"
    "s_mov_b32 s74, 2654208\n"
    "buffer_load_dwordx4 v5, s[40:43], s74 offen lds\n"
    "s_add_u32 m0, s59, 77824\n"
    "s_mov_b32 s74, 2654336\n"
    "buffer_load_dwordx4 v5, s[40:43], s74 offen lds\n"
    "s_add_u32 m0, s59, 81920\n"
    "s_mov_b32 s74, 2654464\n"
    "buffer_load_dwordx4 v5, s[40:43], s74 offen lds\n"
    "s_add_u32 m0, s59, 86016\n"
    "s_mov_b32 s74, 2654208\n"
    "buffer_load_dwordx4 v5, s[44:47], s74 offen lds\n"
    "s_add_u32 m0, s59, 90112\n"
    "s_mov_b32 s74, 2654336\n"
    "buffer_load_dwordx4 v5, s[44:47], s74 offen lds\n"
    "s_add_u32 m0, s59, 94208\n"
    "s_mov_b32 s74, 2654464\n"
    "buffer_load_dwordx4 v5, s[44:47], s74 offen lds\n"
    "ds_read_b64_tr_b16 a[96:97], v17 offset:0\n"
    "ds_read_b64_tr_b16 a[98:99], v17 offset:512\n"
    "ds_read_b64_tr_b16 a[100:101], v17 offset:8\n"
    "ds_read_b64_tr_b16 a[102:103], v17 offset:520\n"
    "ds_read_b64_tr_b16 a[104:105], v19 offset:0\n"
    "ds_read_b64_tr_b16 a[106:107], v19 offset:512\n"
    "ds_read_b64_tr_b16 a[108:109], v17 offset:12288\n"
    "ds_read_b64_tr_b16 a[110:111], v17 offset:12800\n"
    "ds_read_b64_tr_b16 a[112:113], v17 offset:12296\n"
    "ds_read_b64_tr_b16 a[114:115], v17 offset:12808\n"
    "ds_read_b64_tr_b16 a[116:117], v19 offset:12288\n"
    "ds_read_b64_tr_b16 a[118:119], v19 offset:12800\n"
    "s_waitcnt vmcnt(24) lgkmcnt(0)\n"
    "s_barrier\n"
    "s_add_u32 m0, s59, 98304\n"
    "s_mov_b32 s74, 2949120\n"
    "buffer_load_dwordx4 v5, s[40:43], s74 offen lds\n"
    "s_add_u32 m0, s59, 102400\n"
    "s_mov_b32 s74, 2949248\n"
    "buffer_load_dwordx4 v5, s[40:43], s74 offen lds\n"
    "s_add_u32 m0, s59, 106496\n"
    "s_mov_b32 s74, 2949376\n"
    "buffer_load_dwordx4 v5, s[40:43], s74 offen lds\n"
    "s_add_u32 m0, s59, 110592\n"
    "s_mov_b32 s74, 2949120\n"
    "buffer_load_dwordx4 v5, s[44:47], s74 offen lds\n"
    "s_add_u32 m0, s59, 114688\n"
    "s_mov_b32 s74, 2949248\n"
    "buffer_load_dwordx4 v5, s[44:47], s74 offen lds\n"
    "s_add_u32 m0, s59, 118784\n"
    "s_mov_b32 s74, 2949376\n"
    "buffer_load_dwordx4 v5, s[44:47], s74 offen lds\n"
    "ds_read_b64_tr_b16 a[120:121], v17 offset:24576\n"
    "ds_read_b64_tr_b16 a[122:123], v17 offset:25088\n"
    "ds_read_b64_tr_b16 a[124:125], v17 offset:24584\n"
    "ds_read_b64_tr_b16 a[126:127], v17 offset:25096\n"
    "ds_read_b64_tr_b16 a[128:129], v19 offset:24576\n"
    "ds_read_b64_tr_b16 a[130:131], v19 offset:25088\n"
    "ds_read_b64_tr_b16 a[132:133], v17 offset:36864\n"
    "ds_read_b64_tr_b16 a[134:135], v17 offset:37376\n"
    "ds_read_b64_tr_b16 a[136:137], v17 offset:36872\n"
    "ds_read_b64_tr_b16 a[138:139], v17 offset:37384\n"
    "ds_read_b64_tr_b16 a[140:141], v19 offset:36864\n"
    "ds_read_b64_tr_b16 a[142:143], v19 offset:37376\n"
    "s_waitcnt vmcnt(24) lgkmcnt(0)\n"
    "s_barrier\n"
    "s_add_u32 m0, s59, 122880\n"
    "s_mov_b32 s74, 3244032\n"
    "buffer_load_dwordx4 v5, s[40:43], s74 offen lds\n"
    "s_add_u32 m0, s59, 126976\n"
    "s_mov_b32 s74, 3244160\n"
    "buffer_load_dwordx4 v5, s[40:43], s74 offen lds\n"
    "s_add_u32 m0, s59, 131072\n"
    "s_mov_b32 s74, 3244288\n"
    "buffer_load_dwordx4 v5, s[40:43], s74 offen lds\n"
    "s_add_u32 m0, s59, 135168\n"
    "s_mov_b32 s74, 3244032\n"
    "buffer_load_dwordx4 v5, s[44:47], s74 offen lds\n"
    "s_add_u32 m0, s59, 139264\n"
    "s_mov_b32 s74, 3244160\n"
    "buffer_load_dwordx4 v5, s[44:47], s74 offen lds\n"
    "s_add_u32 m0, s59, 143360\n"
    "s_mov_b32 s74, 3244288\n"
    "buffer_load_dwordx4 v5, s[44:47], s74 offen lds\n"
    "ds_read_b64_tr_b16 a[144:145], v6 offset:0\n"
    "ds_read_b64_tr_b16 a[146:147], v6 offset:512\n"
    "ds_read_b64_tr_b16 a[148:149], v6 offset:8\n"
    "ds_read_b64_tr_b16 a[150:151], v6 offset:520\n"
    "ds_read_b64_tr_b16 a[152:153], v7 offset:0\n"
    "ds_read_b64_tr_b16 a[154:155], v7 offset:512\n"
    "ds_read_b64_tr_b16 a[156:157], v6 offset:12288\n"
    "ds_read_b64_tr_b16 a[158:159], v6 offset:12800\n"
    "ds_read_b64_tr_b16 a[160:161], v6 offset:12296\n"
    "ds_read_b64_tr_b16 a[162:163], v6 offset:12808\n"
    "ds_read_b64_tr_b16 a[164:165], v7 offset:12288\n"
    "ds_read_b64_tr_b16 a[166:167], v7 offset:12800\n"
    "s_waitcnt vmcnt(24) lgkmcnt(0)\n"
    "s_barrier\n"
    "s_add_u32 m0, s59, 0\n"
    "s_mov_b32 s74, 3538944\n"
    "buffer_load_dwordx4 v5, s[40:43], s74 offen lds\n"
    "s_add_u32 m0, s59, 4096\n"
    "s_mov_b32 s74, 3539072\n"
    "buffer_load_dwordx4 v5, s[40:43], s74 offen lds\n"
    "s_add_u32 m0, s59, 8192\n"
    "s_mov_b32 s74, 3539200\n"
    "buffer_load_dwordx4 v5, s[40:43], s74 offen lds\n"
    "s_add_u32 m0, s59, 12288\n"
    "s_mov_b32 s74, 3538944\n"
    "buffer_load_dwordx4 v5, s[44:47], s74 offen lds\n"
    "s_add_u32 m0, s59, 16384\n"
    "s_mov_b32 s74, 3539072\n"
    "buffer_load_dwordx4 v5, s[44:47], s74 offen lds\n"
    "s_add_u32 m0, s59, 20480\n"
    "s_mov_b32 s74, 3539200\n"
    "buffer_load_dwordx4 v5, s[44:47], s74 offen lds\n"
    "ds_read_b64_tr_b16 a[168:169], v6 offset:24576\n"
    "ds_read_b64_tr_b16 a[170:171], v6 offset:25088\n"
    "ds_read_b64_tr_b16 a[172:173], v6 offset:24584\n"
    "ds_read_b64_tr_b16 a[174:175], v6 offset:25096\n"
    "ds_read_b64_tr_b16 a[176:177], v7 offset:24576\n"
    "ds_read_b64_tr_b16 a[178:179], v7 offset:25088\n"
    "ds_read_b64_tr_b16 a[180:181], v6 offset:36864\n"
    "ds_read_b64_tr_b16 a[182:183], v6 offset:37376\n"
    "ds_read_b64_tr_b16 a[184:185], v6 offset:36872\n"
    "ds_read_b64_tr_b16 a[186:187], v6 offset:37384\n"
    "ds_read_b64_tr_b16 a[188:189], v7 offset:36864\n"
    "ds_read_b64_tr_b16 a[190:191], v7 offset:37376\n"
    "s_waitcnt vmcnt(24) lgkmcnt(0)\n"
    "s_barrier\n"
    "s_add_u32 m0, s59, 24576\n"
    "s_mov_b32 s74, 3833856\n"
    "buffer_load_dwordx4 v5, s[40:43], s74 offen lds\n"
    "s_add_u32 m0, s59, 28672\n"
    "s_mov_b32 s74, 3833984\n"
    "buffer_load_dwordx4 v5, s[40:43], s74 offen lds\n"
    "s_add_u32 m0, s59, 32768\n"
    "s_mov_b32 s74, 3834112\n"
    "buffer_load_dwordx4 v5, s[40:43], s74 offen lds\n"
    "s_add_u32 m0, s59, 36864\n"
    "s_mov_b32 s74, 3833856\n"
    "buffer_load_dwordx4 v5, s[44:47], s74 offen lds\n"
    "s_add_u32 m0, s59, 40960\n"
    "s_mov_b32 s74, 3833984\n"
    "buffer_load_dwordx4 v5, s[44:47], s74 offen lds\n"
    "s_add_u32 m0, s59, 45056\n"
    "s_mov_b32 s74, 3834112\n"
    "buffer_load_dwordx4 v5, s[44:47], s74 offen lds\n"
    "ds_read_b64_tr_b16 a[192:193], v16 offset:0\n"
    "ds_read_b64_tr_b16 a[194:195], v16 offset:512\n"
    "ds_read_b64_tr_b16 a[196:197], v16 offset:8\n"
    "ds_read_b64_tr_b16 a[198:199], v16 offset:520\n"
    "ds_read_b64_tr_b16 a[200:201], v18 offset:0\n"
    "ds_read_b64_tr_b16 a[202:203], v18 offset:512\n"
    "ds_read_b64_tr_b16 a[204:205], v16 offset:12288\n"
    "ds_read_b64_tr_b16 a[206:207], v16 offset:12800\n"
    "ds_read_b64_tr_b16 a[208:209], v16 offset:12296\n"
    "ds_read_b64_tr_b16 a[210:211], v16 offset:12808\n"
    "ds_read_b64_tr_b16 a[212:213], v18 offset:12288\n"
    "ds_read_b64_tr_b16 a[214:215], v18 offset:12800\n"
    "s_waitcnt vmcnt(24) lgkmcnt(0)\n"
    "s_barrier\n"
    "s_add_u32 m0, s59, 49152\n"
    "s_mov_b32 s74, 4128768\n"
    "buffer_load_dwordx4 v5, s[40:43], s74 offen lds\n"
    "s_add_u32 m0, s59, 53248\n"
    "s_mov_b32 s74, 4128896\n"
    "buffer_load_dwordx4 v5, s[40:43], s74 offen lds\n"
    "s_add_u32 m0, s59, 57344\n"
    "s_mov_b32 s74, 4129024\n"
    "buffer_load_dwordx4 v5, s[40:43], s74 offen lds\n"
    "s_add_u32 m0, s59, 61440\n"
    "s_mov_b32 s74, 4128768\n"
    "buffer_load_dwordx4 v5, s[44:47], s74 offen lds\n"
    "s_add_u32 m0, s59, 65536\n"
    "s_mov_b32 s74, 4128896\n"
    "buffer_load_dwordx4 v5, s[44:47], s74 offen lds\n"
    "s_add_u32 m0, s59, 69632\n"
    "s_mov_b32 s74, 4129024\n"
    "buffer_load_dwordx4 v5, s[44:47], s74 offen lds\n"
    "ds_read_b64_tr_b16 a[216:217], v16 offset:24576\n"
    "ds_read_b64_tr_b16 a[218:219], v16 offset:25088\n"
    "ds_read_b64_tr_b16 a[220:221], v16 offset:24584\n"
    "ds_read_b64_tr_b16 a[222:223], v16 offset:25096\n"
    "ds_read_b64_tr_b16 a[224:225], v18 offset:24576\n"
    "ds_read_b64_tr_b16 a[226:227], v18 offset:25088\n"
    "ds_read_b64_tr_b16 a[228:229], v16 offset:36864\n"
    "ds_read_b64_tr_b16 a[230:231], v16 offset:37376\n"
    "ds_read_b64_tr_b16 a[232:233], v16 offset:36872\n"
    "ds_read_b64_tr_b16 a[234:235], v16 offset:37384\n"
    "ds_read_b64_tr_b16 a[236:237], v18 offset:36864\n"
    "ds_read_b64_tr_b16 a[238:239], v18 offset:37376\n"
    "s_waitcnt vmcnt(24) lgkmcnt(0)\n"
    "s_barrier\n"
    "s_add_u32 m0, s59, 73728\n"
    "s_mov_b32 s74, 4423680\n"
    "buffer_load_dwordx4 v5, s[40:43], s74 offen lds\n"
    "s_add_u32 m0, s59, 77824\n"
    "s_mov_b32 s74, 4423808\n"
    "buffer_load_dwordx4 v5, s[40:43], s74 offen lds\n"
    "s_add_u32 m0, s59, 81920\n"
    "s_mov_b32 s74, 4423936\n"
    "buffer_load_dwordx4 v5, s[40:43], s74 offen lds\n"
    "s_add_u32 m0, s59, 86016\n"
    "s_mov_b32 s74, 4423680\n"
    "buffer_load_dwordx4 v5, s[44:47], s74 offen lds\n"
    "s_add_u32 m0, s59, 90112\n"
    "s_mov_b32 s74, 4423808\n"
    "buffer_load_dwordx4 v5, s[44:47], s74 offen lds\n"
    "s_add_u32 m0, s59, 94208\n"
    "s_mov_b32 s74, 4423936\n"
    "buffer_load_dwordx4 v5, s[44:47], s74 offen lds\n"
    "ds_read_b64_tr_b16 a[240:241], v17 offset:0\n"
    "ds_read_b64_tr_b16 a[242:243], v17 offset:512\n"
    "ds_read_b64_tr_b16 a[244:245], v17 offset:8\n"
    "ds_read_b64_tr_b16 a[246:247], v17 offset:520\n"
    "ds_read_b64_tr_b16 a[248:249], v19 offset:0\n"
    "ds_read_b64_tr_b16 a[250:251], v19 offset:512\n"
    "ds_read_b64_tr_b16 a[252:253], v17 offset:12288\n"
    "ds_read_b64_tr_b16 a[254:255], v17 offset:12800\n"
    "ds_read_b64_tr_b16 v[80:81], v17 offset:12296\n"
    "ds_read_b64_tr_b16 v[82:83], v17 offset:12808\n"
    "ds_read_b64_tr_b16 v[84:85], v19 offset:12288\n"
    "ds_read_b64_tr_b16 v[86:87], v19 offset:12800\n"
    "s_waitcnt vmcnt(24) lgkmcnt(0)\n"
    "s_barrier\n"
    "s_add_u32 m0, s59, 98304\n"
    "s_mov_b32 s74, 4718592\n"
    "buffer_load_dwordx4 v5, s[40:43], s74 offen lds\n"
    "s_add_u32 m0, s59, 102400\n"
    "s_mov_b32 s74, 4718720\n"
    "buffer_load_dwordx4 v5, s[40:43], s74 offen lds\n"
    "s_add_u32 m0, s59, 106496\n"
    "s_mov_b32 s74, 4718848\n"
    "buffer_load_dwordx4 v5, s[40:43], s74 offen lds\n"
    "s_add_u32 m0, s59, 110592\n"
    "s_mov_b32 s74, 4718592\n"
    "buffer_load_dwordx4 v5, s[44:47], s74 offen lds\n"
    "s_add_u32 m0, s59, 114688\n"
    "s_mov_b32 s74, 4718720\n"
    "buffer_load_dwordx4 v5, s[44:47], s74 offen lds\n"
    "s_add_u32 m0, s59, 118784\n"
    "s_mov_b32 s74, 4718848\n"
    "buffer_load_dwordx4 v5, s[44:47], s74 offen lds\n"
    "ds_read_b64_tr_b16 v[88:89], v17 offset:24576\n"
    "ds_read_b64_tr_b16 v[90:91], v17 offset:25088\n"
    "ds_read_b64_tr_b16 v[92:93], v17 offset:24584\n"
    "ds_read_b64_tr_b16 v[94:95], v17 offset:25096\n"
    "ds_read_b64_tr_b16 v[96:97], v19 offset:24576\n"
    "ds_read_b64_tr_b16 v[98:99], v19 offset:25088\n"
    "ds_read_b64_tr_b16 v[100:101], v17 offset:36864\n"
    "ds_read_b64_tr_b16 v[102:103], v17 offset:37376\n"
    "ds_read_b64_tr_b16 v[104:105], v17 offset:36872\n"
    "ds_read_b64_tr_b16 v[106:107], v17 offset:37384\n"
    "ds_read_b64_tr_b16 v[108:109], v19 offset:36864\n"
    "ds_read_b64_tr_b16 v[110:111], v19 offset:37376\n"
    "s_waitcnt vmcnt(24) lgkmcnt(0)\n"
    "s_barrier\n"
    "s_add_u32 m0, s59, 122880\n"
    "s_mov_b32 s74, 5013504\n"
    "buffer_load_dwordx4 v5, s[40:43], s74 offen lds\n"
    "s_add_u32 m0, s59, 126976\n"
    "s_mov_b32 s74, 5013632\n"
    "buffer_load_dwordx4 v5, s[40:43], s74 offen lds\n"
    "s_add_u32 m0, s59, 131072\n"
    "s_mov_b32 s74, 5013760\n"
    "buffer_load_dwordx4 v5, s[40:43], s74 offen lds\n"
    "s_add_u32 m0, s59, 135168\n"
    "s_mov_b32 s74, 5013504\n"
    "buffer_load_dwordx4 v5, s[44:47], s74 offen lds\n"
    "s_add_u32 m0, s59, 139264\n"
    "s_mov_b32 s74, 5013632\n"
    "buffer_load_dwordx4 v5, s[44:47], s74 offen lds\n"
    "s_add_u32 m0, s59, 143360\n"
    "s_mov_b32 s74, 5013760\n"
    "buffer_load_dwordx4 v5, s[44:47], s74 offen lds\n"
    "buffer_load_dwordx2 v[12:13], off, s[36:39], 0 sc1\n"
    "ds_read_b64_tr_b16 v[112:113], v6 offset:0\n"
    "ds_read_b64_tr_b16 v[114:115], v6 offset:512\n"
    "ds_read_b64_tr_b16 v[116:117], v6 offset:8\n"
    "ds_read_b64_tr_b16 v[118:119], v6 offset:520\n"
    "ds_read_b64_tr_b16 v[120:121], v7 offset:0\n"
    "ds_read_b64_tr_b16 v[122:123], v7 offset:512\n"
    "ds_read_b64_tr_b16 v[124:125], v6 offset:12288\n"
    "ds_read_b64_tr_b16 v[126:127], v6 offset:12800\n"
    "ds_read_b64_tr_b16 v[128:129], v6 offset:12296\n"
    "ds_read_b64_tr_b16 v[130:131], v6 offset:12808\n"
    "ds_read_b64_tr_b16 v[132:133], v7 offset:12288\n"
    "ds_read_b64_tr_b16 v[134:135], v7 offset:12800\n"
    "s_waitcnt vmcnt(25) lgkmcnt(0)\n"
    "s_barrier\n"
    "ds_read_b64_tr_b16 v[136:137], v6 offset:24576\n"
    "ds_read_b64_tr_b16 v[138:139], v6 offset:25088\n"
    "ds_read_b64_tr_b16 v[140:141], v6 offset:24584\n"
    "ds_read_b64_tr_b16 v[142:143], v6 offset:25096\n"
    "ds_read_b64_tr_b16 v[144:145], v7 offset:24576\n"
    "ds_read_b64_tr_b16 v[146:147], v7 offset:25088\n"
    "ds_read_b64_tr_b16 v[148:149], v6 offset:36864\n"
    "ds_read_b64_tr_b16 v[150:151], v6 offset:37376\n"
    "ds_read_b64_tr_b16 v[152:153], v6 offset:36872\n"
    "ds_read_b64_tr_b16 v[154:155], v6 offset:37384\n"
    "ds_read_b64_tr_b16 v[156:157], v7 offset:36864\n"
    "ds_read_b64_tr_b16 v[158:159], v7 offset:37376\n"
    "s_waitcnt vmcnt(19) lgkmcnt(0)\n"
    "s_barrier\n"
    "ds_read_b64_tr_b16 v[160:161], v16 offset:0\n"
    "ds_read_b64_tr_b16 v[162:163], v16 offset:512\n"
    "ds_read_b64_tr_b16 v[164:165], v16 offset:8\n"
    "ds_read_b64_tr_b16 v[166:167], v16 offset:520\n"
    "ds_read_b64_tr_b16 v[168:169], v18 offset:0\n"
    "ds_read_b64_tr_b16 v[170:171], v18 offset:512\n"
    "ds_read_b64_tr_b16 v[172:173], v16 offset:12288\n"
    "ds_read_b64_tr_b16 v[174:175], v16 offset:12800\n"
    "ds_read_b64_tr_b16 v[176:177], v16 offset:12296\n"
    "ds_read_b64_tr_b16 v[178:179], v16 offset:12808\n"
    "ds_read_b64_tr_b16 v[180:181], v18 offset:12288\n"
    "ds_read_b64_tr_b16 v[182:183], v18 offset:12800\n"
    "s_waitcnt vmcnt(13) lgkmcnt(0)\n"
    "s_barrier\n"
    "ds_read_b64_tr_b16 v[184:185], v16 offset:24576\n"
    "ds_read_b64_tr_b16 v[186:187], v16 offset:25088\n"
    "ds_read_b64_tr_b16 v[188:189], v16 offset:24584\n"
    "ds_read_b64_tr_b16 v[190:191], v16 offset:25096\n"
    "ds_read_b64_tr_b16 v[192:193], v18 offset:24576\n"
    "ds_read_b64_tr_b16 v[194:195], v18 offset:25088\n"
    "ds_read_b64_tr_b16 v[196:197], v16 offset:36864\n"
    "ds_read_b64_tr_b16 v[198:199], v16 offset:37376\n"
    "ds_read_b64_tr_b16 v[200:201], v16 offset:36872\n"
    "ds_read_b64_tr_b16 v[202:203], v16 offset:37384\n"
    "ds_read_b64_tr_b16 v[204:205], v18 offset:36864\n"
    "ds_read_b64_tr_b16 v[206:207], v18 offset:37376\n"
    "s_waitcnt vmcnt(7) lgkmcnt(0)\n"
    "s_barrier\n"
    "ds_read_b64_tr_b16 v[208:209], v17 offset:0\n"
    "ds_read_b64_tr_b16 v[210:211], v17 offset:512\n"
    "ds_read_b64_tr_b16 v[212:213], v17 offset:8\n"
    "ds_read_b64_tr_b16 v[214:215], v17 offset:520\n"
    "ds_read_b64_tr_b16 v[216:217], v19 offset:0\n"
    "ds_read_b64_tr_b16 v[218:219], v19 offset:512\n"
    "ds_read_b64_tr_b16 v[220:221], v17 offset:12288\n"
    "ds_read_b64_tr_b16 v[222:223], v17 offset:12800\n"
    "ds_read_b64_tr_b16 v[224:225], v17 offset:12296\n"
    "ds_read_b64_tr_b16 v[226:227], v17 offset:12808\n"
    "ds_read_b64_tr_b16 v[228:229], v19 offset:12288\n"
    "ds_read_b64_tr_b16 v[230:231], v19 offset:12800\n"
    "s_waitcnt vmcnt(0) lgkmcnt(0)\n"
    "s_barrier\n"
    ".LgUP_par_%=:\n"
    "v_readfirstlane_b32 s74, v12\n"
    "v_readfirstlane_b32 s75, v13\n"
    "s_xor_b32 s74, s74, s66\n"
    "s_xor_b32 s75, s75, s67\n"
    "s_cselect_b32 s75, 2, 0\n"
    "s_or_b32 s75, s75, s74\n"
    "s_cmp_lt_u32 s75, 2\n"
    "s_cbranch_scc1 .LgUP_par1_%=\n"
    "s_sleep 4\n"
    "buffer_load_dwordx2 v[12:13], off, s[36:39], 0 sc1\n"
    "s_waitcnt vmcnt(0)\n"
    "s_sub_u32 s72, s72, 1\n"
    "s_cbranch_scc0 .LgUP_par_%=\n"
    "s_mov_b32 s72, 0\n"
    ".LgUP_par1_%=:\n"
    "s_and_b32 s74, s74, 1\n"
    "s_lshl_b32 s74, s74, 13\n"
    "s_add_u32 s36, s36, s74\n"
    "s_addc_u32 s37, s37, 0\n"
    "v_add_u32 v13, v11, v3\n"
    "v_cmp_gt_i32 vcc, s71, v13\n"
    "s_nop 0\n"
    "s_or_b64 exec, vcc, s[62:63]\n"
    "buffer_atomic_smax v11, v2, s[36:39], 0 offen sc0 sc1\n"
    "s_mov_b64 exec, -1\n"
    "s_getpc_b64 s[84:85]\n"
    ".LgUP_PC2_%=:\n"
    "s_and_b32 s85, s85, 0xffff\n"
    "s_mov_b32 s86, .LgUP_FIN_%=-.LgUP_PC2_%=\n"
    "s_mov_b32 s87, 0x20000\n"
    "v_mbcnt_lo_u32_b32 v42, -1, 0\n"
    "v_mbcnt_hi_u32_b32 v42, -1, v42\n"
    "v_lshlrev_b32 v42, 7, v42\n"
    "buffer_load_dword v43, v42, s[84:87], 0 offen\n"
    "v_add_u32 v42, 8192, v42\n"
    "buffer_load_dword v43, v42, s[84:87], 0 offen\n"
    "v_add_u32 v42, 8192, v42\n"
    "buffer_load_dword v43, v42, s[84:87], 0 offen\n"
    "ds_read_b64_tr_b16 v[232:233], v17 offset:24576\n"
    "ds_read_b64_tr_b16 v[234:235], v17 offset:25088\n"
    "ds_read_b64_tr_b16 v[236:237], v17 offset:24584\n"
    "ds_read_b64_tr_b16 v[238:239], v17 offset:25096\n"
    "ds_read_b64_tr_b16 v[240:241], v19 offset:24576\n"
    "ds_read_b64_tr_b16 v[242:243], v19 offset:25088\n"
    "ds_read_b64_tr_b16 v[244:245], v17 offset:36864\n"
    "ds_read_b64_tr_b16 v[246:247], v17 offset:37376\n"
    "ds_read_b64_tr_b16 v[248:249], v17 offset:36872\n"
    "ds_read_b64_tr_b16 v[250:251], v17 offset:37384\n"
    "ds_read_b64_tr_b16 v[252:253], v19 offset:36864\n"
    "ds_read_b64_tr_b16 v[254:255], v19 offset:37376\n"
    "s_waitcnt lgkmcnt(0)\n"
    "s_barrier\n"
    "s_waitcnt vmcnt(0)\n"
    "v_mov_b32 v5, v11\n"
    "v_mov_b32 v12, -1\n"
    "s_mov_b32 s73, 0x80000000\n"
    "s_branch .LgUP_block1_1_%=\n"
    ".LgUP_LOOPB_%=:\n"
    ".LgUP_head0_%=:\n"
    "v_mfma_f32_16x16x32_bf16 v[32:35], a[0:3], v[16:19], 0\n"
    "v_mfma_f32_16x16x32_bf16 v[36:39], a[4:7], v[16:19], 0\n"
    "v_mul_f32 v64, v40, v64\n"
    "v_mul_f32 v65, v41, v65\n"
    "v_mfma_f32_16x16x32_bf16 v[48:51], a[8:11], v[16:19], 0\n"
    "v_mul_f32 v66, v42, v66\n"
    "v_mul_f32 v67, v43, v67\n"
    "v_mfma_f32_16x16x32_bf16 v[56:59], a[12:15], v[16:19], 0\n"
    "v_mul_f32 v68, v44, v68\n"
    "v_mul_f32 v69, v45, v69\n"
    "v_mfma_f32_16x16x32_bf16 v[60:63], a[16:19], v[16:19], 0\n"
    "s_waitcnt lgkmcnt(1)\n"
    "s_waitcnt vmcnt(2)\n"
    "v_mul_f32 v70, v46, v70\n"
    "v_mul_f32 v71, v47, v71\n"
    "v_add_u32 v13, v11, v3\n"
    "v_mfma_f32_16x16x32_bf16 v[72:75], a[20:23], v[16:19], 0\n"
    "ds_read_b128 v[16:19], v0 offset:3072\n"
    "v_mul_f32 v40, 0xbfb8aa3b, v40\n"
    "v_cmp_gt_i32 vcc, s71, v13\n"
    "v_mfma_f32_16x16x32_bf16 v[32:35], a[24:27], v[20:23], v[32:35]\n"
    "v_exp_f32 v40, v40\n"
    "v_mfma_f32_16x16x32_bf16 v[36:39], a[28:31], v[20:23], v[36:39]\n"
    "v_mul_f32 v41, 0xbfb8aa3b, v41\n"
    "v_mul_f32 v42, 0xbfb8aa3b, v42\n"
    "v_mfma_f32_16x16x32_bf16 v[48:51], a[32:35], v[20:23], v[48:51]\n"
    "v_exp_f32 v41, v41\n"
    "s_cbranch_vccnz .LgUP_poll0_%=\n"
    "v_mfma_f32_16x16x32_bf16 v[56:59], a[36:39], v[20:23], v[56:59]\n"
    "s_add_u32 m0, s59, 20480\n"
    "v_exp_f32 v42, v42\n"
    "v_mfma_f32_16x16x32_bf16 v[60:63], a[40:43], v[20:23], v[60:63]\n"
    "s_waitcnt lgkmcnt(1)\n"
    "buffer_load_dwordx4 v1, s[36:39], s76 offen sc1 lds\n"
    "v_mul_f32 v43, 0xbfb8aa3b, v43\n"
    "v_mul_f32 v44, 0xbfb8aa3b, v44\n"
    "v_mfma_f32_16x16x32_bf16 v[72:75], a[44:47], v[20:23], v[72:75]\n"
    "ds_read_b128 v[20:23], v0 offset:4096\n"
    "v_mul_f32 v45, 0xbfb8aa3b, v45\n"
    "v_mfma_f32_16x16x32_bf16 v[32:35], a[48:51], v[24:27], v[32:35]\n"
    "v_exp_f32 v43, v43\n"
    "v_mfma_f32_16x16x32_bf16 v[36:39], a[52:55], v[24:27], v[36:39]\n"
    "v_exp_f32 v44, v44\n"
    "v_mfma_f32_16x16x32_bf16 v[48:51], a[56:59], v[24:27], v[48:51]\n"
    "v_exp_f32 v45, v45\n"
    "v_mfma_f32_16x16x32_bf16 v[56:59], a[60:63], v[24:27], v[56:59]\n"
    "v_mul_f32 v46, 0xbfb8aa3b, v46\n"
    "v_mul_f32 v47, 0xbfb8aa3b, v47\n"
    "v_mfma_f32_16x16x32_bf16 v[60:63], a[64:67], v[24:27], v[60:63]\n"
    "s_waitcnt lgkmcnt(1)\n"
    "v_exp_f32 v46, v46\n"
    "v_mfma_f32_16x16x32_bf16 v[72:75], a[68:71], v[24:27], v[72:75]\n"
    "ds_read_b128 v[24:27], v0 offset:5120\n"
    "v_add_f32 v40, 1.0, v40\n"
    "v_mfma_f32_16x16x32_bf16 v[32:35], a[72:75], v[16:19], v[32:35]\n"
    "v_exp_f32 v47, v47\n"
    "v_mfma_f32_16x16x32_bf16 v[36:39], a[76:79], v[16:19], v[36:39]\n"
    "v_rcp_f32 v40, v40\n"
    "v_mfma_f32_16x16x32_bf16 v[48:51], a[80:83], v[16:19], v[48:51]\n"
    "s_add_u32 m0, s59, 24576\n"
    "s_add_u32 s76, s76, 16384\n"
    "v_add_f32 v41, 1.0, v41\n"
    "v_add_f32 v42, 1.0, v42\n"
    "v_mfma_f32_16x16x32_bf16 v[56:59], a[84:87], v[16:19], v[56:59]\n"
    "buffer_load_dwordx4 v1, s[36:39], s76 offen sc1 lds\n"
    "v_rcp_f32 v41, v41\n"
    "v_mfma_f32_16x16x32_bf16 v[60:63], a[88:91], v[16:19], v[60:63]\n"
    "s_waitcnt lgkmcnt(1)\n"
    "v_rcp_f32 v42, v42\n"
    "v_mfma_f32_16x16x32_bf16 v[72:75], a[92:95], v[16:19], v[72:75]\n"
    "ds_read_b128 v[16:19], v0 offset:6144\n"
    "v_add_f32 v43, 1.0, v43\n"
    "v_mfma_f32_16x16x32_bf16 v[32:35], a[96:99], v[20:23], v[32:35]\n"
    "v_rcp_f32 v43, v43\n"
    "v_mfma_f32_16x16x32_bf16 v[36:39], a[100:103], v[20:23], v[36:39]\n"
    "v_add_f32 v44, 1.0, v44\n"
    "v_add_f32 v45, 1.0, v45\n"
    "v_mfma_f32_16x16x32_bf16 v[48:51], a[104:107], v[20:23], v[48:51]\n"
    "v_rcp_f32 v44, v44\n"
    "v_mfma_f32_16x16x32_bf16 v[56:59], a[108:111], v[20:23], v[56:59]\n"
    "v_rcp_f32 v45, v45\n"
    "v_mfma_f32_16x16x32_bf16 v[60:63], a[112:115], v[20:23], v[60:63]\n"
    "s_waitcnt lgkmcnt(1)\n"
    "v_add_f32 v46, 1.0, v46\n"
    "v_add_f32 v47, 1.0, v47\n"
    "v_mfma_f32_16x16x32_bf16 v[72:75], a[116:119], v[20:23], v[72:75]\n"
    "ds_read_b128 v[20:23], v0 offset:7168\n"
    "v_mul_f32 v40, v40, v64\n"
    "v_mfma_f32_16x16x32_bf16 v[32:35], a[120:123], v[24:27], v[32:35]\n"
    "s_waitcnt vmcnt(5)\n"
    "buffer_atomic_smax v12, v10, s[36:39], s69 offen sc1\n"
    "v_rcp_f32 v46, v46\n"
    "v_mfma_f32_16x16x32_bf16 v[36:39], a[124:127], v[24:27], v[36:39]\n"
    "v_rcp_f32 v47, v47\n"
    "v_mfma_f32_16x16x32_bf16 v[48:51], a[128:131], v[24:27], v[48:51]\n"
    "s_add_u32 m0, s59, 28672\n"
    "s_add_u32 s76, s76, 16384\n"
    "v_mul_f32 v41, v41, v65\n"
    "v_mul_f32 v42, v42, v66\n"
    "v_mfma_f32_16x16x32_bf16 v[56:59], a[132:135], v[24:27], v[56:59]\n"
    "buffer_load_dwordx4 v1, s[36:39], s76 offen sc1 lds\n"
    "v_mul_f32 v43, v43, v67\n"
    "v_mul_f32 v44, v44, v68\n"
    "v_mfma_f32_16x16x32_bf16 v[60:63], a[136:139], v[24:27], v[60:63]\n"
    "s_waitcnt lgkmcnt(1)\n"
    "v_mul_f32 v45, v45, v69\n"
    "v_mul_f32 v46, v46, v70\n"
    "v_mfma_f32_16x16x32_bf16 v[72:75], a[140:143], v[24:27], v[72:75]\n"
    "ds_read_b128 v[24:27], v0 offset:8192\n"
    "v_mul_f32 v47, v47, v71\n"
    "v_mfma_f32_16x16x32_bf16 v[32:35], a[144:147], v[16:19], v[32:35]\n"
    "v_cvt_pk_bf16_f32 v40, v40, v41\n"
    "v_cvt_pk_bf16_f32 v41, v42, v43\n"
    "v_mfma_f32_16x16x32_bf16 v[36:39], a[148:151], v[16:19], v[36:39]\n"
    "v_cvt_pk_bf16_f32 v42, v44, v45\n"
    "v_cvt_pk_bf16_f32 v43, v46, v47\n"
    "v_mfma_f32_16x16x32_bf16 v[48:51], a[152:155], v[16:19], v[48:51]\n"
    "buffer_store_dwordx4 v[40:43], v15, s[36:39], s73 offen offset:1024 sc1\n"
    "v_mul_f32 v76, v52, v76\n"
    "v_mfma_f32_16x16x32_bf16 v[56:59], a[156:159], v[16:19], v[56:59]\n"
    "v_mul_f32 v77, v53, v77\n"
    "v_mul_f32 v78, v54, v78\n"
    "v_mfma_f32_16x16x32_bf16 v[60:63], a[160:163], v[16:19], v[60:63]\n"
    "s_waitcnt lgkmcnt(1)\n"
    "v_mul_f32 v79, v55, v79\n"
    "v_mul_f32 v52, 0xbfb8aa3b, v52\n"
    "v_mfma_f32_16x16x32_bf16 v[72:75], a[164:167], v[16:19], v[72:75]\n"
    "ds_read_b128 v[16:19], v0 offset:9216\n"
    "v_mul_f32 v53, 0xbfb8aa3b, v53\n"
    "v_mfma_f32_16x16x32_bf16 v[32:35], a[168:171], v[20:23], v[32:35]\n"
    "v_exp_f32 v52, v52\n"
    "v_mfma_f32_16x16x32_bf16 v[36:39], a[172:175], v[20:23], v[36:39]\n"
    "v_exp_f32 v53, v53\n"
    "v_mfma_f32_16x16x32_bf16 v[48:51], a[176:179], v[20:23], v[48:51]\n"
    "v_mul_f32 v54, 0xbfb8aa3b, v54\n"
    "v_mul_f32 v55, 0xbfb8aa3b, v55\n"
    "v_mfma_f32_16x16x32_bf16 v[56:59], a[180:183], v[20:23], v[56:59]\n"
    "s_add_u32 m0, s59, 32768\n"
    "s_add_u32 s76, s76, 16384\n"
    "v_exp_f32 v54, v54\n"
    "v_mfma_f32_16x16x32_bf16 v[60:63], a[184:187], v[20:23], v[60:63]\n"
    "s_waitcnt lgkmcnt(1)\n"
    "buffer_load_dwordx4 v1, s[36:39], s76 offen sc1 lds\n"
    "v_exp_f32 v55, v55\n"
    "v_mfma_f32_16x16x32_bf16 v[72:75], a[188:191], v[20:23], v[72:75]\n"
    "ds_read_b128 v[20:23], v0 offset:10240\n"
    "v_add_f32 v52, 1.0, v52\n"
    "v_mfma_f32_16x16x32_bf16 v[32:35], a[192:195], v[24:27], v[32:35]\n"
    "v_rcp_f32 v52, v52\n"
    "v_mfma_f32_16x16x32_bf16 v[36:39], a[196:199], v[24:27], v[36:39]\n"
    "v_add_f32 v53, 1.0, v53\n"
    "v_add_f32 v54, 1.0, v54\n"
    "v_mfma_f32_16x16x32_bf16 v[48:51], a[200:203], v[24:27], v[48:51]\n"
    "v_rcp_f32 v53, v53\n"
    "v_mfma_f32_16x16x32_bf16 v[56:59], a[204:207], v[24:27], v[56:59]\n"
    "v_rcp_f32 v54, v54\n"
    "v_mfma_f32_16x16x32_bf16 v[60:63], a[208:211], v[24:27], v[60:63]\n"
    "s_waitcnt lgkmcnt(1)\n"
    "v_add_f32 v55, 1.0, v55\n"
    "v_mul_f32 v52, v52, v76\n"
    "v_mfma_f32_16x16x32_bf16 v[72:75], a[212:215], v[24:27], v[72:75]\n"
    "ds_read_b128 v[24:27], v0 offset:11264\n"
    "v_mul_f32 v53, v53, v77\n"
    "v_mfma_f32_16x16x32_bf16 v[32:35], a[216:219], v[16:19], v[32:35]\n"
    "v_rcp_f32 v55, v55\n"
    "v_mfma_f32_16x16x32_bf16 v[36:39], a[220:223], v[16:19], v[36:39]\n"
    "v_mul_f32 v54, v54, v78\n"
    "v_mul_f32 v55, v55, v79\n"
    "v_mfma_f32_16x16x32_bf16 v[48:51], a[224:227], v[16:19], v[48:51]\n"
    "v_cvt_pk_bf16_f32 v52, v52, v53\n"
    "v_cvt_pk_bf16_f32 v53, v54, v55\n"
    "v_mfma_f32_16x16x32_bf16 v[56:59], a[228:231], v[16:19], v[56:59]\n"
    "s_add_u32 m0, s59, 36864\n"
    "s_add_u32 s76, s76, 16384\n"
    "s_nop 0\n"
    "v_mfma_f32_16x16x32_bf16 v[60:63], a[232:235], v[16:19], v[60:63]\n"
    "s_waitcnt lgkmcnt(1)\n"
    "buffer_load_dwordx4 v8, s[36:39], s76 offen sc1 lds\n"
    "v_permlane16_swap_b32 v52, v54\n"
    "v_mfma_f32_16x16x32_bf16 v[72:75], a[236:239], v[16:19], v[72:75]\n"
    "ds_read_b128 v[16:19], v0 offset:12288\n"
    "v_mfma_f32_16x16x32_bf16 v[32:35], a[240:243], v[20:23], v[32:35]\n"
    "v_permlane16_swap_b32 v53, v55\n"
    "v_mfma_f32_16x16x32_bf16 v[36:39], a[244:247], v[20:23], v[36:39]\n"
    "buffer_store_dwordx4 v[52:55], v4, s[36:39], s73 offen offset:1024 sc1\n"
    "v_mfma_f32_16x16x32_bf16 v[48:51], a[248:251], v[20:23], v[48:51]\n"
    "s_lshr_b32 s73, s68, 1\n"
    "v_mfma_f32_16x16x32_bf16 v[56:59], a[252:255], v[20:23], v[56:59]\n"
    "v_mfma_f32_16x16x32_bf16 v[60:63], v[80:83], v[20:23], v[60:63]\n"
    "s_waitcnt lgkmcnt(1)\n"
    "v_mfma_f32_16x16x32_bf16 v[72:75], v[84:87], v[20:23], v[72:75]\n"
    "ds_read_b128 v[20:23], v0 offset:13312\n"
    "v_mfma_f32_16x16x32_bf16 v[32:35], v[88:91], v[24:27], v[32:35]\n"
    "s_and_b32 s73, s73, s57\n"
    "v_mfma_f32_16x16x32_bf16 v[36:39], v[92:95], v[24:27], v[36:39]\n"
    "v_mfma_f32_16x16x32_bf16 v[48:51], v[96:99], v[24:27], v[48:51]\n"
    "s_mul_i32 s73, s73, s56\n"
    "v_mfma_f32_16x16x32_bf16 v[56:59], v[100:103], v[24:27], v[56:59]\n"
    "v_mfma_f32_16x16x32_bf16 v[60:63], v[104:107], v[24:27], v[60:63]\n"
    "s_waitcnt lgkmcnt(1)\n"
    "v_mfma_f32_16x16x32_bf16 v[72:75], v[108:111], v[24:27], v[72:75]\n"
    "ds_read_b128 v[24:27], v0 offset:14336\n"
    ".LgUP_join0_%=:\n"
    "v_mfma_f32_16x16x32_bf16 v[32:35], v[112:115], v[16:19], v[32:35]\n"
    "s_add_u32 s73, s73, s55\n"
    "s_add_u32 s71, s68, 2\n"
    "s_waitcnt vmcnt(5)\n"
    "v_mfma_f32_16x16x32_bf16 v[36:39], v[116:119], v[16:19], v[36:39]\n"
    "v_add_u32 v13, v11, v3\n"
    "v_mfma_f32_16x16x32_bf16 v[48:51], v[120:123], v[16:19], v[48:51]\n"
    "v_cmp_gt_i32 vcc, s71, v13\n"
    "v_mfma_f32_16x16x32_bf16 v[56:59], v[124:127], v[16:19], v[56:59]\n"
    "v_mfma_f32_16x16x32_bf16 v[60:63], v[128:131], v[16:19], v[60:63]\n"
    "s_waitcnt lgkmcnt(1)\n"
    "v_mfma_f32_16x16x32_bf16 v[72:75], v[132:135], v[16:19], v[72:75]\n"
    "ds_read_b128 v[16:19], v0 offset:15360\n"
    "s_or_b64 exec, vcc, s[62:63]\n"
    "buffer_atomic_smax v11, v2, s[36:39], 0 offen sc0 sc1\n"
    "s_mov_b64 exec, -1\n"
    "v_mfma_f32_16x16x32_bf16 v[32:35], v[136:139], v[20:23], v[32:35]\n"
    "v_mfma_f32_16x16x32_bf16 v[36:39], v[140:143], v[20:23], v[36:39]\n"
    "s_lshr_b32 s76, s71, 1\n"
    "v_mfma_f32_16x16x32_bf16 v[48:51], v[144:147], v[20:23], v[48:51]\n"
    "s_and_b32 s76, s76, s54\n"
    "v_mfma_f32_16x16x32_bf16 v[56:59], v[148:151], v[20:23], v[56:59]\n"
    "s_mul_i32 s76, s76, s53\n"
    "v_mfma_f32_16x16x32_bf16 v[60:63], v[152:155], v[20:23], v[60:63]\n"
    "s_waitcnt lgkmcnt(1)\n"
    "s_add_u32 s76, s76, s52\n"
    "v_mfma_f32_16x16x32_bf16 v[72:75], v[156:159], v[20:23], v[72:75]\n"
    "ds_read_b128 v[20:23], v0 offset:16384\n"
    "v_mfma_f32_16x16x32_bf16 v[32:35], v[160:163], v[24:27], v[32:35]\n"
    "v_mfma_f32_16x16x32_bf16 v[36:39], v[164:167], v[24:27], v[36:39]\n"
    "v_mfma_f32_16x16x32_bf16 v[48:51], v[168:171], v[24:27], v[48:51]\n"
    "v_mfma_f32_16x16x32_bf16 v[56:59], v[172:175], v[24:27], v[56:59]\n"
    "v_mfma_f32_16x16x32_bf16 v[60:63], v[176:179], v[24:27], v[60:63]\n"
    "s_waitcnt lgkmcnt(1)\n"
    "v_mfma_f32_16x16x32_bf16 v[72:75], v[180:183], v[24:27], v[72:75]\n"
    "ds_read_b128 v[24:27], v0 offset:17408\n"
    "v_mfma_f32_16x16x32_bf16 v[32:35], v[184:187], v[16:19], v[32:35]\n"
    "v_mfma_f32_16x16x32_bf16 v[36:39], v[188:191], v[16:19], v[36:39]\n"
    "v_mfma_f32_16x16x32_bf16 v[48:51], v[192:195], v[16:19], v[48:51]\n"
    "v_mfma_f32_16x16x32_bf16 v[56:59], v[196:199], v[16:19], v[56:59]\n"
    "s_waitcnt vmcnt(1) lgkmcnt(0)\n"
    "s_barrier\n"
    "v_add_u32 v13, v5, v3\n"
    "v_mfma_f32_16x16x32_bf16 v[60:63], v[200:203], v[16:19], v[60:63]\n"
    "v_cmp_gt_i32 vcc, s71, v13\n"
    "v_mfma_f32_16x16x32_bf16 v[72:75], v[204:207], v[16:19], v[72:75]\n"
    "ds_read_b128 v[16:19], v9 offset:0\n"
    "v_mfma_f32_16x16x32_bf16 v[32:35], v[208:211], v[20:23], v[32:35]\n"
    "s_waitcnt vmcnt(1)\n"
    "buffer_atomic_smax v12, v10, s[36:39], s70 offen sc1\n"
    "v_mfma_f32_16x16x32_bf16 v[36:39], v[212:215], v[20:23], v[36:39]\n"
    "s_or_b64 exec, vcc, s[62:63]\n"
    "buffer_atomic_smax v5, v2, s[36:39], 0 offen sc0 sc1\n"
    "s_mov_b64 exec, -1\n"
    "v_mfma_f32_16x16x32_bf16 v[48:51], v[216:219], v[20:23], v[48:51]\n"
    "v_mfma_f32_16x16x32_bf16 v[56:59], v[220:223], v[20:23], v[56:59]\n"
    "v_mfma_f32_16x16x32_bf16 v[60:63], v[224:227], v[20:23], v[60:63]\n"
    "v_mfma_f32_16x16x32_bf16 v[72:75], v[228:231], v[20:23], v[72:75]\n"
    "ds_read_b128 v[20:23], v9 offset:1024\n"
    "v_mfma_f32_16x16x32_bf16 v[32:35], v[232:235], v[24:27], v[32:35]\n"
    "v_mfma_f32_16x16x32_bf16 v[36:39], v[236:239], v[24:27], v[36:39]\n"
    "v_mfma_f32_16x16x32_bf16 v[48:51], v[240:243], v[24:27], v[48:51]\n"
    "s_mov_b32 s69, 0x80000000\n"
    "v_mfma_f32_16x16x32_bf16 v[56:59], v[244:247], v[24:27], v[56:59]\n"
    "s_mov_b32 s70, 0\n"
    "v_mfma_f32_16x16x32_bf16 v[60:63], v[248:251], v[24:27], v[60:63]\n"
    "s_waitcnt lgkmcnt(1)\n"
    "v_mov_b32 v12, s68\n"
    "v_mfma_f32_16x16x32_bf16 v[72:75], v[252:255], v[24:27], v[72:75]\n"
    "ds_read_b128 v[24:27], v9 offset:2048\n"
    "s_add_u32 s68, s68, 1\n"
    ".LgUP_head1_%=:\n"
    "v_mfma_f32_16x16x32_bf16 v[40:43], a[0:3], v[16:19], 0\n"
    "v_mfma_f32_16x16x32_bf16 v[44:47], a[4:7], v[16:19], 0\n"
    "v_mul_f32 v56, v32, v56\n"
    "v_mul_f32 v57, v33, v57\n"
    "v_mfma_f32_16x16x32_bf16 v[52:55], a[8:11], v[16:19], 0\n"
    "v_mul_f32 v58, v34, v58\n"
    "v_mul_f32 v59, v35, v59\n"
    "v_mfma_f32_16x16x32_bf16 v[64:67], a[12:15], v[16:19], 0\n"
    "v_mul_f32 v60, v36, v60\n"
    "v_mul_f32 v61, v37, v61\n"
    "v_mfma_f32_16x16x32_bf16 v[68:71], a[16:19], v[16:19], 0\n"
    "s_waitcnt lgkmcnt(1)\n"
    "s_waitcnt vmcnt(2)\n"
    "v_mul_f32 v62, v38, v62\n"
    "v_mul_f32 v63, v39, v63\n"
    "v_add_u32 v13, v11, v3\n"
    "v_mfma_f32_16x16x32_bf16 v[76:79], a[20:23], v[16:19], 0\n"
    "ds_read_b128 v[16:19], v9 offset:3072\n"
    "v_mul_f32 v32, 0xbfb8aa3b, v32\n"
    "v_cmp_gt_i32 vcc, s71, v13\n"
    "v_mfma_f32_16x16x32_bf16 v[40:43], a[24:27], v[20:23], v[40:43]\n"
    "v_exp_f32 v32, v32\n"
    "v_mfma_f32_16x16x32_bf16 v[44:47], a[28:31], v[20:23], v[44:47]\n"
    "v_mul_f32 v33, 0xbfb8aa3b, v33\n"
    "v_mul_f32 v34, 0xbfb8aa3b, v34\n"
    "v_mfma_f32_16x16x32_bf16 v[52:55], a[32:35], v[20:23], v[52:55]\n"
    "v_exp_f32 v33, v33\n"
    "s_cbranch_vccnz .LgUP_poll1_%=\n"
    "v_mfma_f32_16x16x32_bf16 v[64:67], a[36:39], v[20:23], v[64:67]\n"
    "s_add_u32 m0, s59, 0\n"
    "v_exp_f32 v34, v34\n"
    "v_mfma_f32_16x16x32_bf16 v[68:71], a[40:43], v[20:23], v[68:71]\n"
    "s_waitcnt lgkmcnt(1)\n"
    "buffer_load_dwordx4 v1, s[36:39], s76 offen sc1 lds\n"
    "v_mul_f32 v35, 0xbfb8aa3b, v35\n"
    "v_mul_f32 v36, 0xbfb8aa3b, v36\n"
    "v_mfma_f32_16x16x32_bf16 v[76:79], a[44:47], v[20:23], v[76:79]\n"
    "ds_read_b128 v[20:23], v9 offset:4096\n"
    "v_mul_f32 v37, 0xbfb8aa3b, v37\n"
    "v_mfma_f32_16x16x32_bf16 v[40:43], a[48:51], v[24:27], v[40:43]\n"
    "v_exp_f32 v35, v35\n"
    "v_mfma_f32_16x16x32_bf16 v[44:47], a[52:55], v[24:27], v[44:47]\n"
    "v_exp_f32 v36, v36\n"
    "v_mfma_f32_16x16x32_bf16 v[52:55], a[56:59], v[24:27], v[52:55]\n"
    "v_exp_f32 v37, v37\n"
    "v_mfma_f32_16x16x32_bf16 v[64:67], a[60:63], v[24:27], v[64:67]\n"
    "v_mul_f32 v38, 0xbfb8aa3b, v38\n"
    "v_mul_f32 v39, 0xbfb8aa3b, v39\n"
    "v_mfma_f32_16x16x32_bf16 v[68:71], a[64:67], v[24:27], v[68:71]\n"
    "s_waitcnt lgkmcnt(1)\n"
    "v_exp_f32 v38, v38\n"
    "v_mfma_f32_16x16x32_bf16 v[76:79], a[68:71], v[24:27], v[76:79]\n"
    "ds_read_b128 v[24:27], v9 offset:5120\n"
    "v_add_f32 v32, 1.0, v32\n"
    "v_mfma_f32_16x16x32_bf16 v[40:43], a[72:75], v[16:19], v[40:43]\n"
    "v_exp_f32 v39, v39\n"
    "v_mfma_f32_16x16x32_bf16 v[44:47], a[76:79], v[16:19], v[44:47]\n"
    "v_rcp_f32 v32, v32\n"
    "v_mfma_f32_16x16x32_bf16 v[52:55], a[80:83], v[16:19], v[52:55]\n"
    "s_add_u32 m0, s59, 4096\n"
    "s_add_u32 s76, s76, 16384\n"
    "v_add_f32 v33, 1.0, v33\n"
    "v_add_f32 v34, 1.0, v34\n"
    "v_mfma_f32_16x16x32_bf16 v[64:67], a[84:87], v[16:19], v[64:67]\n"
    "buffer_load_dwordx4 v1, s[36:39], s76 offen sc1 lds\n"
    "v_rcp_f32 v33, v33\n"
    "v_mfma_f32_16x16x32_bf16 v[68:71], a[88:91], v[16:19], v[68:71]\n"
    "s_waitcnt lgkmcnt(1)\n"
    "v_rcp_f32 v34, v34\n"
    "v_mfma_f32_16x16x32_bf16 v[76:79], a[92:95], v[16:19], v[76:79]\n"
    "ds_read_b128 v[16:19], v9 offset:6144\n"
    "v_add_f32 v35, 1.0, v35\n"
    "v_mfma_f32_16x16x32_bf16 v[40:43], a[96:99], v[20:23], v[40:43]\n"
    "v_rcp_f32 v35, v35\n"
    "v_mfma_f32_16x16x32_bf16 v[44:47], a[100:103], v[20:23], v[44:47]\n"
    "v_add_f32 v36, 1.0, v36\n"
    "v_add_f32 v37, 1.0, v37\n"
    "v_mfma_f32_16x16x32_bf16 v[52:55], a[104:107], v[20:23], v[52:55]\n"
    "v_rcp_f32 v36, v36\n"
    "v_mfma_f32_16x16x32_bf16 v[64:67], a[108:111], v[20:23], v[64:67]\n"
    "v_rcp_f32 v37, v37\n"
    "v_mfma_f32_16x16x32_bf16 v[68:71], a[112:115], v[20:23], v[68:71]\n"
    "s_waitcnt lgkmcnt(1)\n"
    "v_add_f32 v38, 1.0, v38\n"
    "v_add_f32 v39, 1.0, v39\n"
    "v_mfma_f32_16x16x32_bf16 v[76:79], a[116:119], v[20:23], v[76:79]\n"
    "ds_read_b128 v[20:23], v9 offset:7168\n"
    "v_mul_f32 v32, v32, v56\n"
    "v_mfma_f32_16x16x32_bf16 v[40:43], a[120:123], v[24:27], v[40:43]\n"
    "s_waitcnt vmcnt(5)\n"
    "buffer_atomic_smax v12, v14, s[36:39], s69 offen sc1\n"
    "v_rcp_f32 v38, v38\n"
    "v_mfma_f32_16x16x32_bf16 v[44:47], a[124:127], v[24:27], v[44:47]\n"
    "v_rcp_f32 v39, v39\n"
    "v_mfma_f32_16x16x32_bf16 v[52:55], a[128:131], v[24:27], v[52:55]\n"
    "s_add_u32 m0, s59, 8192\n"
    "s_add_u32 s76, s76, 16384\n"
    "v_mul_f32 v33, v33, v57\n"
    "v_mul_f32 v34, v34, v58\n"
    "v_mfma_f32_16x16x32_bf16 v[64:67], a[132:135], v[24:27], v[64:67]\n"
    "buffer_load_dwordx4 v1, s[36:39], s76 offen sc1 lds\n"
    "v_mul_f32 v35, v35, v59\n"
    "v_mul_f32 v36, v36, v60\n"
    "v_mfma_f32_16x16x32_bf16 v[68:71], a[136:139], v[24:27], v[68:71]\n"
    "s_waitcnt lgkmcnt(1)\n"
    "v_mul_f32 v37, v37, v61\n"
    "v_mul_f32 v38, v38, v62\n"
    "v_mfma_f32_16x16x32_bf16 v[76:79], a[140:143], v[24:27], v[76:79]\n"
    "ds_read_b128 v[24:27], v9 offset:8192\n"
    "v_mul_f32 v39, v39, v63\n"
    "v_mfma_f32_16x16x32_bf16 v[40:43], a[144:147], v[16:19], v[40:43]\n"
    "v_cvt_pk_bf16_f32 v32, v32, v33\n"
    "v_cvt_pk_bf16_f32 v33, v34, v35\n"
    "v_mfma_f32_16x16x32_bf16 v[44:47], a[148:151], v[16:19], v[44:47]\n"
    "v_cvt_pk_bf16_f32 v34, v36, v37\n"
    "v_cvt_pk_bf16_f32 v35, v38, v39\n"
    "v_mfma_f32_16x16x32_bf16 v[52:55], a[152:155], v[16:19], v[52:55]\n"
    "buffer_store_dwordx4 v[32:35], v15, s[36:39], s73 offen sc1\n"
    "v_mul_f32 v72, v48, v72\n"
    "v_mfma_f32_16x16x32_bf16 v[64:67], a[156:159], v[16:19], v[64:67]\n"
    "v_mul_f32 v73, v49, v73\n"
    "v_mul_f32 v74, v50, v74\n"
    "v_mfma_f32_16x16x32_bf16 v[68:71], a[160:163], v[16:19], v[68:71]\n"
    "s_waitcnt lgkmcnt(1)\n"
    "v_mul_f32 v75, v51, v75\n"
    "v_mul_f32 v48, 0xbfb8aa3b, v48\n"
    "v_mfma_f32_16x16x32_bf16 v[76:79], a[164:167], v[16:19], v[76:79]\n"
    "ds_read_b128 v[16:19], v9 offset:9216\n"
    "v_mul_f32 v49, 0xbfb8aa3b, v49\n"
    "v_mfma_f32_16x16x32_bf16 v[40:43], a[168:171], v[20:23], v[40:43]\n"
    "v_exp_f32 v48, v48\n"
    "v_mfma_f32_16x16x32_bf16 v[44:47], a[172:175], v[20:23], v[44:47]\n"
    "v_exp_f32 v49, v49\n"
    "v_mfma_f32_16x16x32_bf16 v[52:55], a[176:179], v[20:23], v[52:55]\n"
    "v_mul_f32 v50, 0xbfb8aa3b, v50\n"
    "v_mul_f32 v51, 0xbfb8aa3b, v51\n"
    "v_mfma_f32_16x16x32_bf16 v[64:67], a[180:183], v[20:23], v[64:67]\n"
    "s_add_u32 m0, s59, 12288\n"
    "s_add_u32 s76, s76, 16384\n"
    "v_exp_f32 v50, v50\n"
    "v_mfma_f32_16x16x32_bf16 v[68:71], a[184:187], v[20:23], v[68:71]\n"
    "s_waitcnt lgkmcnt(1)\n"
    "buffer_load_dwordx4 v1, s[36:39], s76 offen sc1 lds\n"
    "v_exp_f32 v51, v51\n"
    "v_mfma_f32_16x16x32_bf16 v[76:79], a[188:191], v[20:23], v[76:79]\n"
    "ds_read_b128 v[20:23], v9 offset:10240\n"
    "v_add_f32 v48, 1.0, v48\n"
    "v_mfma_f32_16x16x32_bf16 v[40:43], a[192:195], v[24:27], v[40:43]\n"
    "v_rcp_f32 v48, v48\n"
    "v_mfma_f32_16x16x32_bf16 v[44:47], a[196:199], v[24:27], v[44:47]\n"
    "v_add_f32 v49, 1.0, v49\n"
    "v_add_f32 v50, 1.0, v50\n"
    "v_mfma_f32_16x16x32_bf16 v[52:55], a[200:203], v[24:27], v[52:55]\n"
    "v_rcp_f32 v49, v49\n"
    "v_mfma_f32_16x16x32_bf16 v[64:67], a[204:207], v[24:27], v[64:67]\n"
    "v_rcp_f32 v50, v50\n"
    "v_mfma_f32_16x16x32_bf16 v[68:71], a[208:211], v[24:27], v[68:71]\n"
    "s_waitcnt lgkmcnt(1)\n"
    "v_add_f32 v51, 1.0, v51\n"
    "v_mul_f32 v48, v48, v72\n"
    "v_mfma_f32_16x16x32_bf16 v[76:79], a[212:215], v[24:27], v[76:79]\n"
    "ds_read_b128 v[24:27], v9 offset:11264\n"
    "v_mul_f32 v49, v49, v73\n"
    "v_mfma_f32_16x16x32_bf16 v[40:43], a[216:219], v[16:19], v[40:43]\n"
    "v_rcp_f32 v51, v51\n"
    "v_mfma_f32_16x16x32_bf16 v[44:47], a[220:223], v[16:19], v[44:47]\n"
    "v_mul_f32 v50, v50, v74\n"
    "v_mul_f32 v51, v51, v75\n"
    "v_mfma_f32_16x16x32_bf16 v[52:55], a[224:227], v[16:19], v[52:55]\n"
    "v_cvt_pk_bf16_f32 v48, v48, v49\n"
    "v_cvt_pk_bf16_f32 v49, v50, v51\n"
    "v_mfma_f32_16x16x32_bf16 v[64:67], a[228:231], v[16:19], v[64:67]\n"
    "s_add_u32 m0, s59, 16384\n"
    "s_add_u32 s76, s76, 16384\n"
    "s_nop 0\n"
    "v_mfma_f32_16x16x32_bf16 v[68:71], a[232:235], v[16:19], v[68:71]\n"
    "s_waitcnt lgkmcnt(1)\n"
    "buffer_load_dwordx4 v8, s[36:39], s76 offen sc1 lds\n"
    "v_permlane16_swap_b32 v48, v50\n"
    "v_mfma_f32_16x16x32_bf16 v[76:79], a[236:239], v[16:19], v[76:79]\n"
    "ds_read_b128 v[16:19], v9 offset:12288\n"
    "v_mfma_f32_16x16x32_bf16 v[40:43], a[240:243], v[20:23], v[40:43]\n"
    "v_permlane16_swap_b32 v49, v51\n"
    "v_mfma_f32_16x16x32_bf16 v[44:47], a[244:247], v[20:23], v[44:47]\n"
    "buffer_store_dwordx4 v[48:51], v4, s[36:39], s73 offen sc1\n"
    "v_mfma_f32_16x16x32_bf16 v[52:55], a[248:251], v[20:23], v[52:55]\n"
    "s_lshr_b32 s73, s68, 1\n"
    "v_mfma_f32_16x16x32_bf16 v[64:67], a[252:255], v[20:23], v[64:67]\n"
    "v_mfma_f32_16x16x32_bf16 v[68:71], v[80:83], v[20:23], v[68:71]\n"
    "s_waitcnt lgkmcnt(1)\n"
    "v_mfma_f32_16x16x32_bf16 v[76:79], v[84:87], v[20:23], v[76:79]\n"
    "ds_read_b128 v[20:23], v9 offset:13312\n"
    "v_mfma_f32_16x16x32_bf16 v[40:43], v[88:91], v[24:27], v[40:43]\n"
    "s_and_b32 s73, s73, s57\n"
    "v_mfma_f32_16x16x32_bf16 v[44:47], v[92:95], v[24:27], v[44:47]\n"
    "v_mfma_f32_16x16x32_bf16 v[52:55], v[96:99], v[24:27], v[52:55]\n"
    "s_mul_i32 s73, s73, s56\n"
    "v_mfma_f32_16x16x32_bf16 v[64:67], v[100:103], v[24:27], v[64:67]\n"
    "v_mfma_f32_16x16x32_bf16 v[68:71], v[104:107], v[24:27], v[68:71]\n"
    "s_waitcnt lgkmcnt(1)\n"
    "v_mfma_f32_16x16x32_bf16 v[76:79], v[108:111], v[24:27], v[76:79]\n"
    "ds_read_b128 v[24:27], v9 offset:14336\n"
    ".LgUP_join1_%=:\n"
    "v_mfma_f32_16x16x32_bf16 v[40:43], v[112:115], v[16:19], v[40:43]\n"
    "s_add_u32 s73, s73, s55\n"
    "s_add_u32 s71, s68, 2\n"
    "s_waitcnt vmcnt(5)\n"
    "v_mfma_f32_16x16x32_bf16 v[44:47], v[116:119], v[16:19], v[44:47]\n"
    "v_add_u32 v13, v11, v3\n"
    "v_mfma_f32_16x16x32_bf16 v[52:55], v[120:123], v[16:19], v[52:55]\n"
    "v_cmp_gt_i32 vcc, s71, v13\n"
    "v_mfma_f32_16x16x32_bf16 v[64:67], v[124:127], v[16:19], v[64:67]\n"
    "v_mfma_f32_16x16x32_bf16 v[68:71], v[128:131], v[16:19], v[68:71]\n"
    "s_waitcnt lgkmcnt(1)\n"
    "v_mfma_f32_16x16x32_bf16 v[76:79], v[132:135], v[16:19], v[76:79]\n"
    "ds_read_b128 v[16:19], v9 offset:15360\n"
    "s_or_b64 exec, vcc, s[62:63]\n"
    "buffer_atomic_smax v11, v2, s[36:39], 0 offen sc0 sc1\n"
    "s_mov_b64 exec, -1\n"
    "v_mfma_f32_16x16x32_bf16 v[40:43], v[136:139], v[20:23], v[40:43]\n"
    "v_mfma_f32_16x16x32_bf16 v[44:47], v[140:143], v[20:23], v[44:47]\n"
    "s_lshr_b32 s76, s71, 1\n"
    "v_mfma_f32_16x16x32_bf16 v[52:55], v[144:147], v[20:23], v[52:55]\n"
    "s_and_b32 s76, s76, s54\n"
    "v_mfma_f32_16x16x32_bf16 v[64:67], v[148:151], v[20:23], v[64:67]\n"
    "s_mul_i32 s76, s76, s53\n"
    "v_mfma_f32_16x16x32_bf16 v[68:71], v[152:155], v[20:23], v[68:71]\n"
    "s_waitcnt lgkmcnt(1)\n"
    "s_add_u32 s76, s76, s52\n"
    "v_mfma_f32_16x16x32_bf16 v[76:79], v[156:159], v[20:23], v[76:79]\n"
    "ds_read_b128 v[20:23], v9 offset:16384\n"
    "s_add_u32 s76, s76, 1024\n"
    "v_mfma_f32_16x16x32_bf16 v[40:43], v[160:163], v[24:27], v[40:43]\n"
    "v_mfma_f32_16x16x32_bf16 v[44:47], v[164:167], v[24:27], v[44:47]\n"
    "v_mfma_f32_16x16x32_bf16 v[52:55], v[168:171], v[24:27], v[52:55]\n"
    "v_mfma_f32_16x16x32_bf16 v[64:67], v[172:175], v[24:27], v[64:67]\n"
    "v_mfma_f32_16x16x32_bf16 v[68:71], v[176:179], v[24:27], v[68:71]\n"
    "s_waitcnt lgkmcnt(1)\n"
    "v_mfma_f32_16x16x32_bf16 v[76:79], v[180:183], v[24:27], v[76:79]\n"
    "ds_read_b128 v[24:27], v9 offset:17408\n"
    "v_mfma_f32_16x16x32_bf16 v[40:43], v[184:187], v[16:19], v[40:43]\n"
    "v_mfma_f32_16x16x32_bf16 v[44:47], v[188:191], v[16:19], v[44:47]\n"
    "v_mfma_f32_16x16x32_bf16 v[52:55], v[192:195], v[16:19], v[52:55]\n"
    "v_mfma_f32_16x16x32_bf16 v[64:67], v[196:199], v[16:19], v[64:67]\n"
    "s_waitcnt vmcnt(1) lgkmcnt(0)\n"
    "s_barrier\n"
    "v_add_u32 v13, v5, v3\n"
    "v_mfma_f32_16x16x32_bf16 v[68:71], v[200:203], v[16:19], v[68:71]\n"
    "v_cmp_gt_i32 vcc, s71, v13\n"
    "v_mfma_f32_16x16x32_bf16 v[76:79], v[204:207], v[16:19], v[76:79]\n"
    "ds_read_b128 v[16:19], v0 offset:0\n"
    "v_mfma_f32_16x16x32_bf16 v[40:43], v[208:211], v[20:23], v[40:43]\n"
    "s_waitcnt vmcnt(1)\n"
    "buffer_atomic_smax v12, v14, s[36:39], s70 offen sc1\n"
    "v_mfma_f32_16x16x32_bf16 v[44:47], v[212:215], v[20:23], v[44:47]\n"
    "s_or_b64 exec, vcc, s[62:63]\n"
    "buffer_atomic_smax v5, v2, s[36:39], 0 offen sc0 sc1\n"
    "s_mov_b64 exec, -1\n"
    "v_mfma_f32_16x16x32_bf16 v[52:55], v[216:219], v[20:23], v[52:55]\n"
    "v_mfma_f32_16x16x32_bf16 v[64:67], v[220:223], v[20:23], v[64:67]\n"
    "v_mfma_f32_16x16x32_bf16 v[68:71], v[224:227], v[20:23], v[68:71]\n"
    "v_mfma_f32_16x16x32_bf16 v[76:79], v[228:231], v[20:23], v[76:79]\n"
    "ds_read_b128 v[20:23], v0 offset:1024\n"
    "v_mfma_f32_16x16x32_bf16 v[40:43], v[232:235], v[24:27], v[40:43]\n"
    "v_mfma_f32_16x16x32_bf16 v[44:47], v[236:239], v[24:27], v[44:47]\n"
    "v_mfma_f32_16x16x32_bf16 v[52:55], v[240:243], v[24:27], v[52:55]\n"
    "s_mov_b32 s69, 0x80000000\n"
    "v_mfma_f32_16x16x32_bf16 v[64:67], v[244:247], v[24:27], v[64:67]\n"
    "s_mov_b32 s70, 0\n"
    "v_mfma_f32_16x16x32_bf16 v[68:71], v[248:251], v[24:27], v[68:71]\n"
    "s_waitcnt lgkmcnt(1)\n"
    "v_mov_b32 v12, s68\n"
    "v_mfma_f32_16x16x32_bf16 v[76:79], v[252:255], v[24:27], v[76:79]\n"
    "ds_read_b128 v[24:27], v0 offset:2048\n"
    "s_add_u32 s68, s68, 1\n"
    "s_branch .LgUP_head0_%=\n"
    ".LgUP_LOOPE_%=:\n"
    ".LgUP_done_%=:\n"
    "s_waitcnt vmcnt(0)\n"
    "buffer_atomic_smax v12, v10, s[36:39], 0 offen sc1\n"
    "s_waitcnt vmcnt(0)\n"
    "s_branch .LgUP_FIN_%=\n"
    ".LgUP_poll0_%=:\n"
    "v_mfma_f32_16x16x32_bf16 v[56:59], a[36:39], v[20:23], v[56:59]\n"
    "s_or_b64 exec, vcc, s[62:63]\n"
    "buffer_atomic_smax v11, v2, s[36:39], 0 offen sc0 sc1\n"
    "s_mov_b64 exec, -1\n"
    "v_exp_f32 v42, v42\n"
    "v_mfma_f32_16x16x32_bf16 v[60:63], a[40:43], v[20:23], v[60:63]\n"
    "s_waitcnt lgkmcnt(1)\n"
    "v_mul_f32 v43, 0xbfb8aa3b, v43\n"
    "v_mul_f32 v44, 0xbfb8aa3b, v44\n"
    "v_mfma_f32_16x16x32_bf16 v[72:75], a[44:47], v[20:23], v[72:75]\n"
    "ds_read_b128 v[20:23], v0 offset:4096\n"
    "v_mul_f32 v45, 0xbfb8aa3b, v45\n"
    "v_mfma_f32_16x16x32_bf16 v[32:35], a[48:51], v[24:27], v[32:35]\n"
    "v_exp_f32 v43, v43\n"
    "v_mfma_f32_16x16x32_bf16 v[36:39], a[52:55], v[24:27], v[36:39]\n"
    "v_exp_f32 v44, v44\n"
    "v_mfma_f32_16x16x32_bf16 v[48:51], a[56:59], v[24:27], v[48:51]\n"
    "v_exp_f32 v45, v45\n"
    "v_mfma_f32_16x16x32_bf16 v[56:59], a[60:63], v[24:27], v[56:59]\n"
    "v_mul_f32 v46, 0xbfb8aa3b, v46\n"
    "v_mul_f32 v47, 0xbfb8aa3b, v47\n"
    "v_mfma_f32_16x16x32_bf16 v[60:63], a[64:67], v[24:27], v[60:63]\n"
    "s_waitcnt lgkmcnt(1)\n"
    "v_exp_f32 v46, v46\n"
    "v_mfma_f32_16x16x32_bf16 v[72:75], a[68:71], v[24:27], v[72:75]\n"
    "ds_read_b128 v[24:27], v0 offset:5120\n"
    "v_add_f32 v40, 1.0, v40\n"
    "v_mfma_f32_16x16x32_bf16 v[32:35], a[72:75], v[16:19], v[32:35]\n"
    "v_exp_f32 v47, v47\n"
    "v_mfma_f32_16x16x32_bf16 v[36:39], a[76:79], v[16:19], v[36:39]\n"
    "v_rcp_f32 v40, v40\n"
    "v_mfma_f32_16x16x32_bf16 v[48:51], a[80:83], v[16:19], v[48:51]\n"
    "v_add_f32 v41, 1.0, v41\n"
    "v_add_f32 v42, 1.0, v42\n"
    "v_mfma_f32_16x16x32_bf16 v[56:59], a[84:87], v[16:19], v[56:59]\n"
    "v_rcp_f32 v41, v41\n"
    "v_mfma_f32_16x16x32_bf16 v[60:63], a[88:91], v[16:19], v[60:63]\n"
    "s_waitcnt lgkmcnt(1)\n"
    "v_rcp_f32 v42, v42\n"
    "v_mfma_f32_16x16x32_bf16 v[72:75], a[92:95], v[16:19], v[72:75]\n"
    "ds_read_b128 v[16:19], v0 offset:6144\n"
    "v_add_f32 v43, 1.0, v43\n"
    "v_mfma_f32_16x16x32_bf16 v[32:35], a[96:99], v[20:23], v[32:35]\n"
    "s_waitcnt vmcnt(1)\n"
    "v_rcp_f32 v43, v43\n"
    "v_add_u32 v13, v5, v3\n"
    "v_mfma_f32_16x16x32_bf16 v[36:39], a[100:103], v[20:23], v[36:39]\n"
    "v_add_f32 v44, 1.0, v44\n"
    "v_add_f32 v45, 1.0, v45\n"
    "v_cmp_gt_i32 vcc, s71, v13\n"
    "v_mfma_f32_16x16x32_bf16 v[48:51], a[104:107], v[20:23], v[48:51]\n"
    "v_rcp_f32 v44, v44\n"
    "v_mfma_f32_16x16x32_bf16 v[56:59], a[108:111], v[20:23], v[56:59]\n"
    "v_rcp_f32 v45, v45\n"
    "v_mfma_f32_16x16x32_bf16 v[60:63], a[112:115], v[20:23], v[60:63]\n"
    "s_waitcnt lgkmcnt(1)\n"
    "v_add_f32 v46, 1.0, v46\n"
    "v_add_f32 v47, 1.0, v47\n"
    "s_cbranch_vccz .LgUP_dma0_29_%=\n"
    "v_mfma_f32_16x16x32_bf16 v[72:75], a[116:119], v[20:23], v[72:75]\n"
    "ds_read_b128 v[20:23], v0 offset:7168\n"
    "s_or_b64 exec, vcc, s[62:63]\n"
    "buffer_atomic_smax v5, v2, s[36:39], 0 offen sc0 sc1\n"
    "s_mov_b64 exec, -1\n"
    "v_mul_f32 v40, v40, v64\n"
    "v_mfma_f32_16x16x32_bf16 v[32:35], a[120:123], v[24:27], v[32:35]\n"
    "s_waitcnt vmcnt(5)\n"
    "buffer_atomic_smax v12, v10, s[36:39], s69 offen sc1\n"
    "v_rcp_f32 v46, v46\n"
    "v_mfma_f32_16x16x32_bf16 v[36:39], a[124:127], v[24:27], v[36:39]\n"
    "v_rcp_f32 v47, v47\n"
    "v_mfma_f32_16x16x32_bf16 v[48:51], a[128:131], v[24:27], v[48:51]\n"
    "v_mul_f32 v41, v41, v65\n"
    "v_mul_f32 v42, v42, v66\n"
    "v_mfma_f32_16x16x32_bf16 v[56:59], a[132:135], v[24:27], v[56:59]\n"
    "v_mul_f32 v43, v43, v67\n"
    "v_mul_f32 v44, v44, v68\n"
    "v_mfma_f32_16x16x32_bf16 v[60:63], a[136:139], v[24:27], v[60:63]\n"
    "s_waitcnt lgkmcnt(1)\n"
    "v_mul_f32 v45, v45, v69\n"
    "v_mul_f32 v46, v46, v70\n"
    "v_mfma_f32_16x16x32_bf16 v[72:75], a[140:143], v[24:27], v[72:75]\n"
    "ds_read_b128 v[24:27], v0 offset:8192\n"
    "v_mul_f32 v47, v47, v71\n"
    "v_mfma_f32_16x16x32_bf16 v[32:35], a[144:147], v[16:19], v[32:35]\n"
    "v_cvt_pk_bf16_f32 v40, v40, v41\n"
    "v_cvt_pk_bf16_f32 v41, v42, v43\n"
    "v_mfma_f32_16x16x32_bf16 v[36:39], a[148:151], v[16:19], v[36:39]\n"
    "v_cvt_pk_bf16_f32 v42, v44, v45\n"
    "v_cvt_pk_bf16_f32 v43, v46, v47\n"
    "v_mfma_f32_16x16x32_bf16 v[48:51], a[152:155], v[16:19], v[48:51]\n"
    "buffer_store_dwordx4 v[40:43], v15, s[36:39], s73 offen offset:1024 sc1\n"
    "v_mul_f32 v76, v52, v76\n"
    "v_mfma_f32_16x16x32_bf16 v[56:59], a[156:159], v[16:19], v[56:59]\n"
    "v_mul_f32 v77, v53, v77\n"
    "v_mul_f32 v78, v54, v78\n"
    "v_mfma_f32_16x16x32_bf16 v[60:63], a[160:163], v[16:19], v[60:63]\n"
    "s_waitcnt lgkmcnt(1)\n"
    "v_mul_f32 v79, v55, v79\n"
    "v_mul_f32 v52, 0xbfb8aa3b, v52\n"
    "v_mfma_f32_16x16x32_bf16 v[72:75], a[164:167], v[16:19], v[72:75]\n"
    "ds_read_b128 v[16:19], v0 offset:9216\n"
    "v_mul_f32 v53, 0xbfb8aa3b, v53\n"
    "v_mfma_f32_16x16x32_bf16 v[32:35], a[168:171], v[20:23], v[32:35]\n"
    "v_exp_f32 v52, v52\n"
    "v_mfma_f32_16x16x32_bf16 v[36:39], a[172:175], v[20:23], v[36:39]\n"
    "v_exp_f32 v53, v53\n"
    "v_mfma_f32_16x16x32_bf16 v[48:51], a[176:179], v[20:23], v[48:51]\n"
    "s_waitcnt vmcnt(3)\n"
    "v_mul_f32 v54, 0xbfb8aa3b, v54\n"
    "v_mul_f32 v55, 0xbfb8aa3b, v55\n"
    "v_add_u32 v13, v11, v3\n"
    "v_mfma_f32_16x16x32_bf16 v[56:59], a[180:183], v[20:23], v[56:59]\n"
    "v_exp_f32 v54, v54\n"
    "v_cmp_gt_i32 vcc, s71, v13\n"
    "v_mfma_f32_16x16x32_bf16 v[60:63], a[184:187], v[20:23], v[60:63]\n"
    "s_waitcnt lgkmcnt(1)\n"
    "v_exp_f32 v55, v55\n"
    "v_mfma_f32_16x16x32_bf16 v[72:75], a[188:191], v[20:23], v[72:75]\n"
    "ds_read_b128 v[20:23], v0 offset:10240\n"
    "v_add_f32 v52, 1.0, v52\n"
    "v_mfma_f32_16x16x32_bf16 v[32:35], a[192:195], v[24:27], v[32:35]\n"
    "v_rcp_f32 v52, v52\n"
    "s_cbranch_vccz .LgUP_dma0_49_%=\n"
    "v_mfma_f32_16x16x32_bf16 v[36:39], a[196:199], v[24:27], v[36:39]\n"
    "s_or_b64 exec, vcc, s[62:63]\n"
    "buffer_atomic_smax v11, v2, s[36:39], 0 offen sc0 sc1\n"
    "s_mov_b64 exec, -1\n"
    "v_add_f32 v53, 1.0, v53\n"
    "v_add_f32 v54, 1.0, v54\n"
    "v_mfma_f32_16x16x32_bf16 v[48:51], a[200:203], v[24:27], v[48:51]\n"
    "v_rcp_f32 v53, v53\n"
    "v_mfma_f32_16x16x32_bf16 v[56:59], a[204:207], v[24:27], v[56:59]\n"
    "v_rcp_f32 v54, v54\n"
    "v_mfma_f32_16x16x32_bf16 v[60:63], a[208:211], v[24:27], v[60:63]\n"
    "s_waitcnt lgkmcnt(1)\n"
    "v_add_f32 v55, 1.0, v55\n"
    "v_mul_f32 v52, v52, v76\n"
    "v_mfma_f32_16x16x32_bf16 v[72:75], a[212:215], v[24:27], v[72:75]\n"
    "ds_read_b128 v[24:27], v0 offset:11264\n"
    "v_mul_f32 v53, v53, v77\n"
    "v_mfma_f32_16x16x32_bf16 v[32:35], a[216:219], v[16:19], v[32:35]\n"
    "v_rcp_f32 v55, v55\n"
    "v_mfma_f32_16x16x32_bf16 v[36:39], a[220:223], v[16:19], v[36:39]\n"
    "v_mul_f32 v54, v54, v78\n"
    "v_mul_f32 v55, v55, v79\n"
    "v_mfma_f32_16x16x32_bf16 v[48:51], a[224:227], v[16:19], v[48:51]\n"
    "v_cvt_pk_bf16_f32 v52, v52, v53\n"
    "v_cvt_pk_bf16_f32 v53, v54, v55\n"
    "v_mfma_f32_16x16x32_bf16 v[56:59], a[228:231], v[16:19], v[56:59]\n"
    "s_nop 0\n"
    "v_mfma_f32_16x16x32_bf16 v[60:63], a[232:235], v[16:19], v[60:63]\n"
    "s_waitcnt lgkmcnt(1)\n"
    "v_permlane16_swap_b32 v52, v54\n"
    "v_mfma_f32_16x16x32_bf16 v[72:75], a[236:239], v[16:19], v[72:75]\n"
    "ds_read_b128 v[16:19], v0 offset:12288\n"
    "v_mfma_f32_16x16x32_bf16 v[32:35], a[240:243], v[20:23], v[32:35]\n"
    "v_permlane16_swap_b32 v53, v55\n"
    "v_mfma_f32_16x16x32_bf16 v[36:39], a[244:247], v[20:23], v[36:39]\n"
    "buffer_store_dwordx4 v[52:55], v4, s[36:39], s73 offen offset:1024 sc1\n"
    "v_mfma_f32_16x16x32_bf16 v[48:51], a[248:251], v[20:23], v[48:51]\n"
    "s_lshr_b32 s73, s68, 1\n"
    "v_mfma_f32_16x16x32_bf16 v[56:59], a[252:255], v[20:23], v[56:59]\n"
    "v_mfma_f32_16x16x32_bf16 v[60:63], v[80:83], v[20:23], v[60:63]\n"
    "s_waitcnt lgkmcnt(1)\n"
    "s_waitcnt vmcnt(4)\n"
    "v_add_u32 v13, v5, v3\n"
    "v_mfma_f32_16x16x32_bf16 v[72:75], v[84:87], v[20:23], v[72:75]\n"
    "ds_read_b128 v[20:23], v0 offset:13312\n"
    "v_cmp_gt_i32 vcc, s71, v13\n"
    "v_mfma_f32_16x16x32_bf16 v[32:35], v[88:91], v[24:27], v[32:35]\n"
    "s_and_b32 s73, s73, s57\n"
    "v_mfma_f32_16x16x32_bf16 v[36:39], v[92:95], v[24:27], v[36:39]\n"
    "v_mfma_f32_16x16x32_bf16 v[48:51], v[96:99], v[24:27], v[48:51]\n"
    "s_mul_i32 s73, s73, s56\n"
    "s_cbranch_vccz .LgUP_dma0_69_%=\n"
    "v_mfma_f32_16x16x32_bf16 v[56:59], v[100:103], v[24:27], v[56:59]\n"
    "s_or_b64 exec, vcc, s[62:63]\n"
    "buffer_atomic_smax v5, v2, s[36:39], 0 offen sc0 sc1\n"
    "s_mov_b64 exec, -1\n"
    "v_mfma_f32_16x16x32_bf16 v[60:63], v[104:107], v[24:27], v[60:63]\n"
    "s_waitcnt lgkmcnt(1)\n"
    "v_mfma_f32_16x16x32_bf16 v[72:75], v[108:111], v[24:27], v[72:75]\n"
    "ds_read_b128 v[24:27], v0 offset:14336\n"
    "v_mfma_f32_16x16x32_bf16 v[32:35], v[112:115], v[16:19], v[32:35]\n"
    "s_add_u32 s73, s73, s55\n"
    "v_mfma_f32_16x16x32_bf16 v[36:39], v[116:119], v[16:19], v[36:39]\n"
    "v_mfma_f32_16x16x32_bf16 v[48:51], v[120:123], v[16:19], v[48:51]\n"
    "v_mfma_f32_16x16x32_bf16 v[56:59], v[124:127], v[16:19], v[56:59]\n"
    "v_mfma_f32_16x16x32_bf16 v[60:63], v[128:131], v[16:19], v[60:63]\n"
    "s_waitcnt lgkmcnt(1)\n"
    "v_mfma_f32_16x16x32_bf16 v[72:75], v[132:135], v[16:19], v[72:75]\n"
    "ds_read_b128 v[16:19], v0 offset:15360\n"
    "v_mfma_f32_16x16x32_bf16 v[32:35], v[136:139], v[20:23], v[32:35]\n"
    "v_mfma_f32_16x16x32_bf16 v[36:39], v[140:143], v[20:23], v[36:39]\n"
    "v_mfma_f32_16x16x32_bf16 v[48:51], v[144:147], v[20:23], v[48:51]\n"
    "v_mfma_f32_16x16x32_bf16 v[56:59], v[148:151], v[20:23], v[56:59]\n"
    "v_mfma_f32_16x16x32_bf16 v[60:63], v[152:155], v[20:23], v[60:63]\n"
    "s_waitcnt lgkmcnt(1)\n"
    "v_mfma_f32_16x16x32_bf16 v[72:75], v[156:159], v[20:23], v[72:75]\n"
    "ds_read_b128 v[20:23], v0 offset:16384\n"
    "v_mfma_f32_16x16x32_bf16 v[32:35], v[160:163], v[24:27], v[32:35]\n"
    "s_waitcnt vmcnt(2)\n"
    "v_add_u32 v13, v11, v3\n"
    "v_mfma_f32_16x16x32_bf16 v[36:39], v[164:167], v[24:27], v[36:39]\n"
    "v_cmp_gt_i32 vcc, s71, v13\n"
    "v_mfma_f32_16x16x32_bf16 v[48:51], v[168:171], v[24:27], v[48:51]\n"
    "v_mfma_f32_16x16x32_bf16 v[56:59], v[172:175], v[24:27], v[56:59]\n"
    "v_mfma_f32_16x16x32_bf16 v[60:63], v[176:179], v[24:27], v[60:63]\n"
    "s_waitcnt lgkmcnt(1)\n"
    "s_cbranch_vccz .LgUP_dma0_89_%=\n"
    "v_mfma_f32_16x16x32_bf16 v[72:75], v[180:183], v[24:27], v[72:75]\n"
    "ds_read_b128 v[24:27], v0 offset:17408\n"
    "s_or_b64 exec, vcc, s[62:63]\n"
    "buffer_atomic_smax v11, v2, s[36:39], 0 offen sc0 sc1\n"
    "s_mov_b64 exec, -1\n"
    "v_mfma_f32_16x16x32_bf16 v[32:35], v[184:187], v[16:19], v[32:35]\n"
    "v_mfma_f32_16x16x32_bf16 v[36:39], v[188:191], v[16:19], v[36:39]\n"
    "v_mfma_f32_16x16x32_bf16 v[48:51], v[192:195], v[16:19], v[48:51]\n"
    "v_mfma_f32_16x16x32_bf16 v[56:59], v[196:199], v[16:19], v[56:59]\n"
    "v_mfma_f32_16x16x32_bf16 v[60:63], v[200:203], v[16:19], v[60:63]\n"
    "s_waitcnt lgkmcnt(1)\n"
    "v_mfma_f32_16x16x32_bf16 v[72:75], v[204:207], v[16:19], v[72:75]\n"
    "v_mfma_f32_16x16x32_bf16 v[32:35], v[208:211], v[20:23], v[32:35]\n"
    "s_waitcnt vmcnt(2)\n"
    "buffer_atomic_smax v12, v10, s[36:39], s70 offen sc1\n"
    "v_mfma_f32_16x16x32_bf16 v[36:39], v[212:215], v[20:23], v[36:39]\n"
    "v_mfma_f32_16x16x32_bf16 v[48:51], v[216:219], v[20:23], v[48:51]\n"
    "v_mfma_f32_16x16x32_bf16 v[56:59], v[220:223], v[20:23], v[56:59]\n"
    "v_mfma_f32_16x16x32_bf16 v[60:63], v[224:227], v[20:23], v[60:63]\n"
    "s_waitcnt lgkmcnt(0)\n"
    "v_mfma_f32_16x16x32_bf16 v[72:75], v[228:231], v[20:23], v[72:75]\n"
    "v_mfma_f32_16x16x32_bf16 v[32:35], v[232:235], v[24:27], v[32:35]\n"
    "v_mfma_f32_16x16x32_bf16 v[36:39], v[236:239], v[24:27], v[36:39]\n"
    "v_mfma_f32_16x16x32_bf16 v[48:51], v[240:243], v[24:27], v[48:51]\n"
    "v_mfma_f32_16x16x32_bf16 v[56:59], v[244:247], v[24:27], v[56:59]\n"
    "v_mfma_f32_16x16x32_bf16 v[60:63], v[248:251], v[24:27], v[60:63]\n"
    "v_mfma_f32_16x16x32_bf16 v[72:75], v[252:255], v[24:27], v[72:75]\n"
    "v_pk_mul_f32 v[56:57], v[32:33], v[56:57]\n"
    "v_pk_mul_f32 v[58:59], v[34:35], v[58:59]\n"
    "s_nop 1\n"
    "v_pk_mul_f32 v[60:61], v[36:37], v[60:61]\n"
    "v_pk_mul_f32 v[62:63], v[38:39], v[62:63]\n"
    "v_pk_mul_f32 v[32:33], v[32:33], s[64:65] op_sel_hi:[1,0]\n"
    "v_pk_mul_f32 v[34:35], v[34:35], s[64:65] op_sel_hi:[1,0]\n"
    "v_pk_mul_f32 v[36:37], v[36:37], s[64:65] op_sel_hi:[1,0]\n"
    "v_pk_mul_f32 v[38:39], v[38:39], s[64:65] op_sel_hi:[1,0]\n"
    "v_exp_f32 v32, v32\n"
    "v_exp_f32 v33, v33\n"
    "v_exp_f32 v34, v34\n"
    "v_exp_f32 v35, v35\n"
    "v_exp_f32 v36, v36\n"
    "v_exp_f32 v37, v37\n"
    "v_exp_f32 v38, v38\n"
    "v_exp_f32 v39, v39\n"
    "v_pk_add_f32 v[32:33], v[32:33], 1.0 op_sel_hi:[1,0]\n"
    "v_pk_add_f32 v[34:35], v[34:35], 1.0 op_sel_hi:[1,0]\n"
    "v_pk_add_f32 v[36:37], v[36:37], 1.0 op_sel_hi:[1,0]\n"
    "v_pk_add_f32 v[38:39], v[38:39], 1.0 op_sel_hi:[1,0]\n"
    "v_rcp_f32 v32, v32\n"
    "v_rcp_f32 v33, v33\n"
    "v_rcp_f32 v34, v34\n"
    "v_rcp_f32 v35, v35\n"
    "v_rcp_f32 v36, v36\n"
    "v_rcp_f32 v37, v37\n"
    "v_rcp_f32 v38, v38\n"
    "v_rcp_f32 v39, v39\n"
    "v_pk_mul_f32 v[32:33], v[32:33], v[56:57]\n"
    "v_pk_mul_f32 v[34:35], v[34:35], v[58:59]\n"
    "v_pk_mul_f32 v[36:37], v[36:37], v[60:61]\n"
    "v_pk_mul_f32 v[38:39], v[38:39], v[62:63]\n"
    "v_cvt_pk_bf16_f32 v32, v32, v33\n"
    "v_cvt_pk_bf16_f32 v33, v34, v35\n"
    "v_cvt_pk_bf16_f32 v34, v36, v37\n"
    "v_cvt_pk_bf16_f32 v35, v38, v39\n"
    "buffer_store_dwordx4 v[32:35], v15, s[36:39], s73 offen sc1\n"
    "v_pk_mul_f32 v[72:73], v[48:49], v[72:73]\n"
    "v_pk_mul_f32 v[74:75], v[50:51], v[74:75]\n"
    "v_pk_mul_f32 v[48:49], v[48:49], s[64:65] op_sel_hi:[1,0]\n"
    "v_pk_mul_f32 v[50:51], v[50:51], s[64:65] op_sel_hi:[1,0]\n"
    "v_exp_f32 v48, v48\n"
    "v_exp_f32 v49, v49\n"
    "v_exp_f32 v50, v50\n"
    "v_exp_f32 v51, v51\n"
    "v_pk_add_f32 v[48:49], v[48:49], 1.0 op_sel_hi:[1,0]\n"
    "v_pk_add_f32 v[50:51], v[50:51], 1.0 op_sel_hi:[1,0]\n"
    "v_rcp_f32 v48, v48\n"
    "v_rcp_f32 v49, v49\n"
    "v_rcp_f32 v50, v50\n"
    "v_rcp_f32 v51, v51\n"
    "v_pk_mul_f32 v[48:49], v[48:49], v[72:73]\n"
    "v_pk_mul_f32 v[50:51], v[50:51], v[74:75]\n"
    "v_cvt_pk_bf16_f32 v48, v48, v49\n"
    "v_cvt_pk_bf16_f32 v49, v50, v51\n"
    "s_nop 0\n"
    "v_permlane16_swap_b32 v48, v50\n"
    "v_permlane16_swap_b32 v49, v51\n"
    "buffer_store_dwordx4 v[48:51], v4, s[36:39], s73 offen sc1\n"
    "v_mov_b32 v12, s68\n"
    "s_mov_b32 s73, 0x80000000\n"
    "s_mov_b32 s68, s71\n"
    "s_waitcnt vmcnt(4)\n"
    "v_add_u32 v13, v5, v3\n"
    "v_cmp_gt_i32 vcc, s71, v13\n"
    "s_cbranch_vccnz .LgUP_idle0_%=\n"
    "s_add_u32 m0, s59, 20480\n"
    "s_nop 0\n"
    "buffer_load_dwordx4 v1, s[36:39], s76 offen sc1 lds\n"
    "s_add_u32 m0, s59, 24576\n"
    "s_add_u32 s76, s76, 16384\n"
    "buffer_load_dwordx4 v1, s[36:39], s76 offen sc1 lds\n"
    "s_add_u32 m0, s59, 28672\n"
    "s_add_u32 s76, s76, 16384\n"
    "buffer_load_dwordx4 v1, s[36:39], s76 offen sc1 lds\n"
    "s_add_u32 m0, s59, 32768\n"
    "s_add_u32 s76, s76, 16384\n"
    "buffer_load_dwordx4 v1, s[36:39], s76 offen sc1 lds\n"
    "s_add_u32 m0, s59, 36864\n"
    "s_add_u32 s76, s76, 16384\n"
    "buffer_load_dwordx4 v8, s[36:39], s76 offen sc1 lds\n"
    "s_waitcnt vmcnt(5)\n"
    "buffer_atomic_smax v12, v14, s[36:39], 0 offen sc1\n"
    "s_mov_b32 s69, 0x80000000\n"
    "s_mov_b32 s70, 0x80000000\n"
    "s_waitcnt vmcnt(9)\n"
    "s_add_u32 s71, s68, 1\n"
    "v_add_u32 v13, v11, v3\n"
    "v_cmp_gt_i32 vcc, s71, v13\n"
    "s_nop 0\n"
    "s_or_b64 exec, vcc, s[62:63]\n"
    "buffer_atomic_smax v11, v2, s[36:39], 0 offen sc0 sc1\n"
    "s_mov_b64 exec, -1\n"
    "buffer_atomic_smax v12, v14, s[36:39], s73 offen sc1\n"
    "s_waitcnt vmcnt(3)\n"
    "s_branch .LgUP_bar1_%=\n"
    ".LgUP_dma0_29_%=:\n"
    "v_mfma_f32_16x16x32_bf16 v[72:75], a[116:119], v[20:23], v[72:75]\n"
    "ds_read_b128 v[20:23], v0 offset:7168\n"
    "v_mul_f32 v40, v40, v64\n"
    "v_mfma_f32_16x16x32_bf16 v[32:35], a[120:123], v[24:27], v[32:35]\n"
    "s_add_u32 m0, s59, 20480\n"
    "s_waitcnt vmcnt(4)\n"
    "buffer_atomic_smax v12, v10, s[36:39], s69 offen sc1\n"
    "v_rcp_f32 v46, v46\n"
    "v_mfma_f32_16x16x32_bf16 v[36:39], a[124:127], v[24:27], v[36:39]\n"
    "buffer_load_dwordx4 v1, s[36:39], s76 offen sc1 lds\n"
    "v_rcp_f32 v47, v47\n"
    "v_mfma_f32_16x16x32_bf16 v[48:51], a[128:131], v[24:27], v[48:51]\n"
    "s_add_u32 m0, s59, 24576\n"
    "s_add_u32 s76, s76, 16384\n"
    "v_mul_f32 v41, v41, v65\n"
    "v_mul_f32 v42, v42, v66\n"
    "v_mfma_f32_16x16x32_bf16 v[56:59], a[132:135], v[24:27], v[56:59]\n"
    "buffer_load_dwordx4 v1, s[36:39], s76 offen sc1 lds\n"
    "v_mul_f32 v43, v43, v67\n"
    "v_mul_f32 v44, v44, v68\n"
    "v_mfma_f32_16x16x32_bf16 v[60:63], a[136:139], v[24:27], v[60:63]\n"
    "s_waitcnt lgkmcnt(1)\n"
    "v_mul_f32 v45, v45, v69\n"
    "v_mul_f32 v46, v46, v70\n"
    "v_mfma_f32_16x16x32_bf16 v[72:75], a[140:143], v[24:27], v[72:75]\n"
    "ds_read_b128 v[24:27], v0 offset:8192\n"
    "v_mul_f32 v47, v47, v71\n"
    "v_mfma_f32_16x16x32_bf16 v[32:35], a[144:147], v[16:19], v[32:35]\n"
    "s_add_u32 m0, s59, 28672\n"
    "s_add_u32 s76, s76, 16384\n"
    "v_cvt_pk_bf16_f32 v40, v40, v41\n"
    "v_cvt_pk_bf16_f32 v41, v42, v43\n"
    "v_mfma_f32_16x16x32_bf16 v[36:39], a[148:151], v[16:19], v[36:39]\n"
    "buffer_load_dwordx4 v1, s[36:39], s76 offen sc1 lds\n"
    "v_cvt_pk_bf16_f32 v42, v44, v45\n"
    "v_cvt_pk_bf16_f32 v43, v46, v47\n"
    "v_mfma_f32_16x16x32_bf16 v[48:51], a[152:155], v[16:19], v[48:51]\n"
    "buffer_store_dwordx4 v[40:43], v15, s[36:39], s73 offen offset:1024 sc1\n"
    "v_mul_f32 v76, v52, v76\n"
    "v_mfma_f32_16x16x32_bf16 v[56:59], a[156:159], v[16:19], v[56:59]\n"
    "s_add_u32 m0, s59, 32768\n"
    "s_add_u32 s76, s76, 16384\n"
    "v_mul_f32 v77, v53, v77\n"
    "v_mul_f32 v78, v54, v78\n"
    "v_mfma_f32_16x16x32_bf16 v[60:63], a[160:163], v[16:19], v[60:63]\n"
    "s_waitcnt lgkmcnt(1)\n"
    "buffer_load_dwordx4 v1, s[36:39], s76 offen sc1 lds\n"
    "v_mul_f32 v79, v55, v79\n"
    "v_mul_f32 v52, 0xbfb8aa3b, v52\n"
    "v_mfma_f32_16x16x32_bf16 v[72:75], a[164:167], v[16:19], v[72:75]\n"
    "ds_read_b128 v[16:19], v0 offset:9216\n"
    "v_mul_f32 v53, 0xbfb8aa3b, v53\n"
    "v_mfma_f32_16x16x32_bf16 v[32:35], a[168:171], v[20:23], v[32:35]\n"
    "v_exp_f32 v52, v52\n"
    "v_mfma_f32_16x16x32_bf16 v[36:39], a[172:175], v[20:23], v[36:39]\n"
    "s_add_u32 m0, s59, 36864\n"
    "s_add_u32 s76, s76, 16384\n"
    "v_exp_f32 v53, v53\n"
    "v_mfma_f32_16x16x32_bf16 v[48:51], a[176:179], v[20:23], v[48:51]\n"
    "buffer_load_dwordx4 v8, s[36:39], s76 offen sc1 lds\n"
    "v_mul_f32 v54, 0xbfb8aa3b, v54\n"
    "v_mul_f32 v55, 0xbfb8aa3b, v55\n"
    "v_mfma_f32_16x16x32_bf16 v[56:59], a[180:183], v[20:23], v[56:59]\n"
    "v_exp_f32 v54, v54\n"
    "v_mfma_f32_16x16x32_bf16 v[60:63], a[184:187], v[20:23], v[60:63]\n"
    "s_waitcnt lgkmcnt(1)\n"
    "v_exp_f32 v55, v55\n"
    "v_mfma_f32_16x16x32_bf16 v[72:75], a[188:191], v[20:23], v[72:75]\n"
    "ds_read_b128 v[20:23], v0 offset:10240\n"
    "v_add_f32 v52, 1.0, v52\n"
    "v_mfma_f32_16x16x32_bf16 v[32:35], a[192:195], v[24:27], v[32:35]\n"
    "v_rcp_f32 v52, v52\n"
    "v_mfma_f32_16x16x32_bf16 v[36:39], a[196:199], v[24:27], v[36:39]\n"
    "v_add_f32 v53, 1.0, v53\n"
    "v_add_f32 v54, 1.0, v54\n"
    "v_mfma_f32_16x16x32_bf16 v[48:51], a[200:203], v[24:27], v[48:51]\n"
    "v_rcp_f32 v53, v53\n"
    "v_mfma_f32_16x16x32_bf16 v[56:59], a[204:207], v[24:27], v[56:59]\n"
    "v_rcp_f32 v54, v54\n"
    "v_mfma_f32_16x16x32_bf16 v[60:63], a[208:211], v[24:27], v[60:63]\n"
    "s_waitcnt lgkmcnt(1)\n"
    "v_add_f32 v55, 1.0, v55\n"
    "v_mul_f32 v52, v52, v76\n"
    "v_mfma_f32_16x16x32_bf16 v[72:75], a[212:215], v[24:27], v[72:75]\n"
    "ds_read_b128 v[24:27], v0 offset:11264\n"
    "v_mul_f32 v53, v53, v77\n"
    "v_mfma_f32_16x16x32_bf16 v[32:35], a[216:219], v[16:19], v[32:35]\n"
    "v_rcp_f32 v55, v55\n"
    "v_mfma_f32_16x16x32_bf16 v[36:39], a[220:223], v[16:19], v[36:39]\n"
    "v_mul_f32 v54, v54, v78\n"
    "v_mul_f32 v55, v55, v79\n"
    "v_mfma_f32_16x16x32_bf16 v[48:51], a[224:227], v[16:19], v[48:51]\n"
    "v_cvt_pk_bf16_f32 v52, v52, v53\n"
    "v_cvt_pk_bf16_f32 v53, v54, v55\n"
    "v_mfma_f32_16x16x32_bf16 v[56:59], a[228:231], v[16:19], v[56:59]\n"
    "s_nop 0\n"
    "v_mfma_f32_16x16x32_bf16 v[60:63], a[232:235], v[16:19], v[60:63]\n"
    "s_waitcnt lgkmcnt(1)\n"
    "v_permlane16_swap_b32 v52, v54\n"
    "v_mfma_f32_16x16x32_bf16 v[72:75], a[236:239], v[16:19], v[72:75]\n"
    "ds_read_b128 v[16:19], v0 offset:12288\n"
    "v_mfma_f32_16x16x32_bf16 v[32:35], a[240:243], v[20:23], v[32:35]\n"
    "v_permlane16_swap_b32 v53, v55\n"
    "v_mfma_f32_16x16x32_bf16 v[36:39], a[244:247], v[20:23], v[36:39]\n"
    "buffer_store_dwordx4 v[52:55], v4, s[36:39], s73 offen offset:1024 sc1\n"
    "v_mfma_f32_16x16x32_bf16 v[48:51], a[248:251], v[20:23], v[48:51]\n"
    "s_lshr_b32 s73, s68, 1\n"
    "v_mfma_f32_16x16x32_bf16 v[56:59], a[252:255], v[20:23], v[56:59]\n"
    "v_mfma_f32_16x16x32_bf16 v[60:63], v[80:83], v[20:23], v[60:63]\n"
    "s_waitcnt lgkmcnt(1)\n"
    "v_mfma_f32_16x16x32_bf16 v[72:75], v[84:87], v[20:23], v[72:75]\n"
    "ds_read_b128 v[20:23], v0 offset:13312\n"
    "v_mfma_f32_16x16x32_bf16 v[32:35], v[88:91], v[24:27], v[32:35]\n"
    "s_and_b32 s73, s73, s57\n"
    "v_mfma_f32_16x16x32_bf16 v[36:39], v[92:95], v[24:27], v[36:39]\n"
    "v_mfma_f32_16x16x32_bf16 v[48:51], v[96:99], v[24:27], v[48:51]\n"
    "s_mul_i32 s73, s73, s56\n"
    "v_mfma_f32_16x16x32_bf16 v[56:59], v[100:103], v[24:27], v[56:59]\n"
    "v_mfma_f32_16x16x32_bf16 v[60:63], v[104:107], v[24:27], v[60:63]\n"
    "s_waitcnt lgkmcnt(1)\n"
    "v_mfma_f32_16x16x32_bf16 v[72:75], v[108:111], v[24:27], v[72:75]\n"
    "ds_read_b128 v[24:27], v0 offset:14336\n"
    "s_branch .LgUP_join0_%=\n"
    ".LgUP_dma0_49_%=:\n"
    "v_mfma_f32_16x16x32_bf16 v[36:39], a[196:199], v[24:27], v[36:39]\n"
    "s_add_u32 m0, s59, 20480\n"
    "v_add_f32 v53, 1.0, v53\n"
    "v_add_f32 v54, 1.0, v54\n"
    "v_mfma_f32_16x16x32_bf16 v[48:51], a[200:203], v[24:27], v[48:51]\n"
    "buffer_load_dwordx4 v1, s[36:39], s76 offen sc1 lds\n"
    "v_rcp_f32 v53, v53\n"
    "v_mfma_f32_16x16x32_bf16 v[56:59], a[204:207], v[24:27], v[56:59]\n"
    "s_add_u32 m0, s59, 24576\n"
    "s_add_u32 s76, s76, 16384\n"
    "v_rcp_f32 v54, v54\n"
    "v_mfma_f32_16x16x32_bf16 v[60:63], a[208:211], v[24:27], v[60:63]\n"
    "s_waitcnt lgkmcnt(1)\n"
    "buffer_load_dwordx4 v1, s[36:39], s76 offen sc1 lds\n"
    "v_add_f32 v55, 1.0, v55\n"
    "v_mul_f32 v52, v52, v76\n"
    "v_mfma_f32_16x16x32_bf16 v[72:75], a[212:215], v[24:27], v[72:75]\n"
    "ds_read_b128 v[24:27], v0 offset:11264\n"
    "v_mul_f32 v53, v53, v77\n"
    "v_mfma_f32_16x16x32_bf16 v[32:35], a[216:219], v[16:19], v[32:35]\n"
    "v_rcp_f32 v55, v55\n"
    "v_mfma_f32_16x16x32_bf16 v[36:39], a[220:223], v[16:19], v[36:39]\n"
    "s_add_u32 m0, s59, 28672\n"
    "s_add_u32 s76, s76, 16384\n"
    "v_mul_f32 v54, v54, v78\n"
    "v_mul_f32 v55, v55, v79\n"
    "v_mfma_f32_16x16x32_bf16 v[48:51], a[224:227], v[16:19], v[48:51]\n"
    "buffer_load_dwordx4 v1, s[36:39], s76 offen sc1 lds\n"
    "v_cvt_pk_bf16_f32 v52, v52, v53\n"
    "v_cvt_pk_bf16_f32 v53, v54, v55\n"
    "v_mfma_f32_16x16x32_bf16 v[56:59], a[228:231], v[16:19], v[56:59]\n"
    "s_nop 0\n"
    "v_mfma_f32_16x16x32_bf16 v[60:63], a[232:235], v[16:19], v[60:63]\n"
    "s_waitcnt lgkmcnt(1)\n"
    "v_permlane16_swap_b32 v52, v54\n"
    "v_mfma_f32_16x16x32_bf16 v[72:75], a[236:239], v[16:19], v[72:75]\n"
    "ds_read_b128 v[16:19], v0 offset:12288\n"
    "v_mfma_f32_16x16x32_bf16 v[32:35], a[240:243], v[20:23], v[32:35]\n"
    "v_permlane16_swap_b32 v53, v55\n"
    "v_mfma_f32_16x16x32_bf16 v[36:39], a[244:247], v[20:23], v[36:39]\n"
    "s_add_u32 m0, s59, 32768\n"
    "s_add_u32 s76, s76, 16384\n"
    "buffer_store_dwordx4 v[52:55], v4, s[36:39], s73 offen offset:1024 sc1\n"
    "v_mfma_f32_16x16x32_bf16 v[48:51], a[248:251], v[20:23], v[48:51]\n"
    "buffer_load_dwordx4 v1, s[36:39], s76 offen sc1 lds\n"
    "s_lshr_b32 s73, s68, 1\n"
    "v_mfma_f32_16x16x32_bf16 v[56:59], a[252:255], v[20:23], v[56:59]\n"
    "s_add_u32 m0, s59, 36864\n"
    "s_add_u32 s76, s76, 16384\n"
    "v_mfma_f32_16x16x32_bf16 v[60:63], v[80:83], v[20:23], v[60:63]\n"
    "s_waitcnt lgkmcnt(1)\n"
    "buffer_load_dwordx4 v8, s[36:39], s76 offen sc1 lds\n"
    "v_mfma_f32_16x16x32_bf16 v[72:75], v[84:87], v[20:23], v[72:75]\n"
    "ds_read_b128 v[20:23], v0 offset:13312\n"
    "v_mfma_f32_16x16x32_bf16 v[32:35], v[88:91], v[24:27], v[32:35]\n"
    "s_and_b32 s73, s73, s57\n"
    "v_mfma_f32_16x16x32_bf16 v[36:39], v[92:95], v[24:27], v[36:39]\n"
    "v_mfma_f32_16x16x32_bf16 v[48:51], v[96:99], v[24:27], v[48:51]\n"
    "s_mul_i32 s73, s73, s56\n"
    "v_mfma_f32_16x16x32_bf16 v[56:59], v[100:103], v[24:27], v[56:59]\n"
    "v_mfma_f32_16x16x32_bf16 v[60:63], v[104:107], v[24:27], v[60:63]\n"
    "s_waitcnt lgkmcnt(1)\n"
    "v_mfma_f32_16x16x32_bf16 v[72:75], v[108:111], v[24:27], v[72:75]\n"
    "ds_read_b128 v[24:27], v0 offset:14336\n"
    "s_branch .LgUP_join0_%=\n"
    ".LgUP_dma0_69_%=:\n"
    "v_mfma_f32_16x16x32_bf16 v[56:59], v[100:103], v[24:27], v[56:59]\n"
    "s_add_u32 m0, s59, 20480\n"
    "v_mfma_f32_16x16x32_bf16 v[60:63], v[104:107], v[24:27], v[60:63]\n"
    "s_waitcnt lgkmcnt(1)\n"
    "buffer_load_dwordx4 v1, s[36:39], s76 offen sc1 lds\n"
    "v_mfma_f32_16x16x32_bf16 v[72:75], v[108:111], v[24:27], v[72:75]\n"
    "ds_read_b128 v[24:27], v0 offset:14336\n"
    "v_mfma_f32_16x16x32_bf16 v[32:35], v[112:115], v[16:19], v[32:35]\n"
    "s_add_u32 s73, s73, s55\n"
    "v_mfma_f32_16x16x32_bf16 v[36:39], v[116:119], v[16:19], v[36:39]\n"
    "s_add_u32 m0, s59, 24576\n"
    "s_add_u32 s76, s76, 16384\n"
    "v_mfma_f32_16x16x32_bf16 v[48:51], v[120:123], v[16:19], v[48:51]\n"
    "buffer_load_dwordx4 v1, s[36:39], s76 offen sc1 lds\n"
    "v_mfma_f32_16x16x32_bf16 v[56:59], v[124:127], v[16:19], v[56:59]\n"
    "v_mfma_f32_16x16x32_bf16 v[60:63], v[128:131], v[16:19], v[60:63]\n"
    "s_waitcnt lgkmcnt(1)\n"
    "v_mfma_f32_16x16x32_bf16 v[72:75], v[132:135], v[16:19], v[72:75]\n"
    "ds_read_b128 v[16:19], v0 offset:15360\n"
    "v_mfma_f32_16x16x32_bf16 v[32:35], v[136:139], v[20:23], v[32:35]\n"
    "s_add_u32 m0, s59, 28672\n"
    "s_add_u32 s76, s76, 16384\n"
    "v_mfma_f32_16x16x32_bf16 v[36:39], v[140:143], v[20:23], v[36:39]\n"
    "buffer_load_dwordx4 v1, s[36:39], s76 offen sc1 lds\n"
    "v_mfma_f32_16x16x32_bf16 v[48:51], v[144:147], v[20:23], v[48:51]\n"
    "s_add_u32 m0, s59, 32768\n"
    "s_add_u32 s76, s76, 16384\n"
    "v_mfma_f32_16x16x32_bf16 v[56:59], v[148:151], v[20:23], v[56:59]\n"
    "buffer_load_dwordx4 v1, s[36:39], s76 offen sc1 lds\n"
    "v_mfma_f32_16x16x32_bf16 v[60:63], v[152:155], v[20:23], v[60:63]\n"
    "s_waitcnt lgkmcnt(1)\n"
    "v_mfma_f32_16x16x32_bf16 v[72:75], v[156:159], v[20:23], v[72:75]\n"
    "ds_read_b128 v[20:23], v0 offset:16384\n"
    "v_mfma_f32_16x16x32_bf16 v[32:35], v[160:163], v[24:27], v[32:35]\n"
    "v_mfma_f32_16x16x32_bf16 v[36:39], v[164:167], v[24:27], v[36:39]\n"
    "s_add_u32 m0, s59, 36864\n"
    "s_add_u32 s76, s76, 16384\n"
    "v_mfma_f32_16x16x32_bf16 v[48:51], v[168:171], v[24:27], v[48:51]\n"
    "buffer_load_dwordx4 v8, s[36:39], s76 offen sc1 lds\n"
    "v_mfma_f32_16x16x32_bf16 v[56:59], v[172:175], v[24:27], v[56:59]\n"
    "v_mfma_f32_16x16x32_bf16 v[60:63], v[176:179], v[24:27], v[60:63]\n"
    "s_waitcnt lgkmcnt(1)\n"
    "v_mfma_f32_16x16x32_bf16 v[72:75], v[180:183], v[24:27], v[72:75]\n"
    "ds_read_b128 v[24:27], v0 offset:17408\n"
    "v_mfma_f32_16x16x32_bf16 v[32:35], v[184:187], v[16:19], v[32:35]\n"
    "v_mfma_f32_16x16x32_bf16 v[36:39], v[188:191], v[16:19], v[36:39]\n"
    "v_mfma_f32_16x16x32_bf16 v[48:51], v[192:195], v[16:19], v[48:51]\n"
    "v_mfma_f32_16x16x32_bf16 v[56:59], v[196:199], v[16:19], v[56:59]\n"
    "v_mfma_f32_16x16x32_bf16 v[60:63], v[200:203], v[16:19], v[60:63]\n"
    "s_waitcnt lgkmcnt(1)\n"
    "v_mfma_f32_16x16x32_bf16 v[72:75], v[204:207], v[16:19], v[72:75]\n"
    "v_mfma_f32_16x16x32_bf16 v[32:35], v[208:211], v[20:23], v[32:35]\n"
    "s_waitcnt vmcnt(5)\n"
    "buffer_atomic_smax v12, v10, s[36:39], s70 offen sc1\n"
    "v_mfma_f32_16x16x32_bf16 v[36:39], v[212:215], v[20:23], v[36:39]\n"
    "v_mfma_f32_16x16x32_bf16 v[48:51], v[216:219], v[20:23], v[48:51]\n"
    "v_mfma_f32_16x16x32_bf16 v[56:59], v[220:223], v[20:23], v[56:59]\n"
    "v_mfma_f32_16x16x32_bf16 v[60:63], v[224:227], v[20:23], v[60:63]\n"
    "s_waitcnt lgkmcnt(0)\n"
    "v_mfma_f32_16x16x32_bf16 v[72:75], v[228:231], v[20:23], v[72:75]\n"
    "v_mfma_f32_16x16x32_bf16 v[32:35], v[232:235], v[24:27], v[32:35]\n"
    "v_mfma_f32_16x16x32_bf16 v[36:39], v[236:239], v[24:27], v[36:39]\n"
    "v_mfma_f32_16x16x32_bf16 v[48:51], v[240:243], v[24:27], v[48:51]\n"
    "v_mfma_f32_16x16x32_bf16 v[56:59], v[244:247], v[24:27], v[56:59]\n"
    "v_mfma_f32_16x16x32_bf16 v[60:63], v[248:251], v[24:27], v[60:63]\n"
    "v_mfma_f32_16x16x32_bf16 v[72:75], v[252:255], v[24:27], v[72:75]\n"
    "v_pk_mul_f32 v[56:57], v[32:33], v[56:57]\n"
    "v_pk_mul_f32 v[58:59], v[34:35], v[58:59]\n"
    "s_nop 1\n"
    "v_pk_mul_f32 v[60:61], v[36:37], v[60:61]\n"
    "v_pk_mul_f32 v[62:63], v[38:39], v[62:63]\n"
    "v_pk_mul_f32 v[32:33], v[32:33], s[64:65] op_sel_hi:[1,0]\n"
    "v_pk_mul_f32 v[34:35], v[34:35], s[64:65] op_sel_hi:[1,0]\n"
    "v_pk_mul_f32 v[36:37], v[36:37], s[64:65] op_sel_hi:[1,0]\n"
    "v_pk_mul_f32 v[38:39], v[38:39], s[64:65] op_sel_hi:[1,0]\n"
    "v_exp_f32 v32, v32\n"
    "v_exp_f32 v33, v33\n"
    "v_exp_f32 v34, v34\n"
    "v_exp_f32 v35, v35\n"
    "v_exp_f32 v36, v36\n"
    "v_exp_f32 v37, v37\n"
    "v_exp_f32 v38, v38\n"
    "v_exp_f32 v39, v39\n"
    "v_pk_add_f32 v[32:33], v[32:33], 1.0 op_sel_hi:[1,0]\n"
    "v_pk_add_f32 v[34:35], v[34:35], 1.0 op_sel_hi:[1,0]\n"
    "v_pk_add_f32 v[36:37], v[36:37], 1.0 op_sel_hi:[1,0]\n"
    "v_pk_add_f32 v[38:39], v[38:39], 1.0 op_sel_hi:[1,0]\n"
    "v_rcp_f32 v32, v32\n"
    "v_rcp_f32 v33, v33\n"
    "v_rcp_f32 v34, v34\n"
    "v_rcp_f32 v35, v35\n"
    "v_rcp_f32 v36, v36\n"
    "v_rcp_f32 v37, v37\n"
    "v_rcp_f32 v38, v38\n"
    "v_rcp_f32 v39, v39\n"
    "v_pk_mul_f32 v[32:33], v[32:33], v[56:57]\n"
    "v_pk_mul_f32 v[34:35], v[34:35], v[58:59]\n"
    "v_pk_mul_f32 v[36:37], v[36:37], v[60:61]\n"
    "v_pk_mul_f32 v[38:39], v[38:39], v[62:63]\n"
    "v_cvt_pk_bf16_f32 v32, v32, v33\n"
    "v_cvt_pk_bf16_f32 v33, v34, v35\n"
    "v_cvt_pk_bf16_f32 v34, v36, v37\n"
    "v_cvt_pk_bf16_f32 v35, v38, v39\n"
    "buffer_store_dwordx4 v[32:35], v15, s[36:39], s73 offen sc1\n"
    "v_pk_mul_f32 v[72:73], v[48:49], v[72:73]\n"
    "v_pk_mul_f32 v[74:75], v[50:51], v[74:75]\n"
    "v_pk_mul_f32 v[48:49], v[48:49], s[64:65] op_sel_hi:[1,0]\n"
    "v_pk_mul_f32 v[50:51], v[50:51], s[64:65] op_sel_hi:[1,0]\n"
    "v_exp_f32 v48, v48\n"
    "v_exp_f32 v49, v49\n"
    "v_exp_f32 v50, v50\n"
    "v_exp_f32 v51, v51\n"
    "v_pk_add_f32 v[48:49], v[48:49], 1.0 op_sel_hi:[1,0]\n"
    "v_pk_add_f32 v[50:51], v[50:51], 1.0 op_sel_hi:[1,0]\n"
    "v_rcp_f32 v48, v48\n"
    "v_rcp_f32 v49, v49\n"
    "v_rcp_f32 v50, v50\n"
    "v_rcp_f32 v51, v51\n"
    "v_pk_mul_f32 v[48:49], v[48:49], v[72:73]\n"
    "v_pk_mul_f32 v[50:51], v[50:51], v[74:75]\n"
    "v_cvt_pk_bf16_f32 v48, v48, v49\n"
    "v_cvt_pk_bf16_f32 v49, v50, v51\n"
    "s_nop 0\n"
    "v_permlane16_swap_b32 v48, v50\n"
    "v_permlane16_swap_b32 v49, v51\n"
    "buffer_store_dwordx4 v[48:51], v4, s[36:39], s73 offen sc1\n"
    "v_mov_b32 v12, s68\n"
    "s_mov_b32 s73, 0x80000000\n"
    "s_mov_b32 s68, s71\n"
    "s_mov_b32 s69, 0\n"
    "s_mov_b32 s70, 0x80000000\n"
    "s_waitcnt vmcnt(9)\n"
    "s_add_u32 s71, s68, 1\n"
    "v_add_u32 v13, v11, v3\n"
    "v_cmp_gt_i32 vcc, s71, v13\n"
    "s_nop 0\n"
    "s_or_b64 exec, vcc, s[62:63]\n"
    "buffer_atomic_smax v11, v2, s[36:39], 0 offen sc0 sc1\n"
    "s_mov_b64 exec, -1\n"
    "buffer_atomic_smax v12, v14, s[36:39], s73 offen sc1\n"
    "s_waitcnt vmcnt(5)\n"
    "s_branch .LgUP_bar1_%=\n"
    ".LgUP_dma0_89_%=:\n"
    "v_mfma_f32_16x16x32_bf16 v[72:75], v[180:183], v[24:27], v[72:75]\n"
    "ds_read_b128 v[24:27], v0 offset:17408\n"
    "v_mfma_f32_16x16x32_bf16 v[32:35], v[184:187], v[16:19], v[32:35]\n"
    "s_add_u32 m0, s59, 20480\n"
    "v_mfma_f32_16x16x32_bf16 v[36:39], v[188:191], v[16:19], v[36:39]\n"
    "buffer_load_dwordx4 v1, s[36:39], s76 offen sc1 lds\n"
    "v_mfma_f32_16x16x32_bf16 v[48:51], v[192:195], v[16:19], v[48:51]\n"
    "s_add_u32 m0, s59, 24576\n"
    "s_add_u32 s76, s76, 16384\n"
    "v_mfma_f32_16x16x32_bf16 v[56:59], v[196:199], v[16:19], v[56:59]\n"
    "buffer_load_dwordx4 v1, s[36:39], s76 offen sc1 lds\n"
    "v_mfma_f32_16x16x32_bf16 v[60:63], v[200:203], v[16:19], v[60:63]\n"
    "s_waitcnt lgkmcnt(1)\n"
    "v_mfma_f32_16x16x32_bf16 v[72:75], v[204:207], v[16:19], v[72:75]\n"
    "v_mfma_f32_16x16x32_bf16 v[32:35], v[208:211], v[20:23], v[32:35]\n"
    "s_add_u32 m0, s59, 28672\n"
    "s_add_u32 s76, s76, 16384\n"
    "s_waitcnt vmcnt(3)\n"
    "buffer_atomic_smax v12, v10, s[36:39], s70 offen sc1\n"
    "v_mfma_f32_16x16x32_bf16 v[36:39], v[212:215], v[20:23], v[36:39]\n"
    "buffer_load_dwordx4 v1, s[36:39], s76 offen sc1 lds\n"
    "v_mfma_f32_16x16x32_bf16 v[48:51], v[216:219], v[20:23], v[48:51]\n"
    "v_mfma_f32_16x16x32_bf16 v[56:59], v[220:223], v[20:23], v[56:59]\n"
    "s_add_u32 m0, s59, 32768\n"
    "s_add_u32 s76, s76, 16384\n"
    "v_mfma_f32_16x16x32_bf16 v[60:63], v[224:227], v[20:23], v[60:63]\n"
    "s_waitcnt lgkmcnt(0)\n"
    "buffer_load_dwordx4 v1, s[36:39], s76 offen sc1 lds\n"
    "v_mfma_f32_16x16x32_bf16 v[72:75], v[228:231], v[20:23], v[72:75]\n"
    "v_mfma_f32_16x16x32_bf16 v[32:35], v[232:235], v[24:27], v[32:35]\n"
    "v_mfma_f32_16x16x32_bf16 v[36:39], v[236:239], v[24:27], v[36:39]\n"
    "s_add_u32 m0, s59, 36864\n"
    "s_add_u32 s76, s76, 16384\n"
    "v_mfma_f32_16x16x32_bf16 v[48:51], v[240:243], v[24:27], v[48:51]\n"
    "buffer_load_dwordx4 v8, s[36:39], s76 offen sc1 lds\n"
    "v_mfma_f32_16x16x32_bf16 v[56:59], v[244:247], v[24:27], v[56:59]\n"
    "v_mfma_f32_16x16x32_bf16 v[60:63], v[248:251], v[24:27], v[60:63]\n"
    "v_mfma_f32_16x16x32_bf16 v[72:75], v[252:255], v[24:27], v[72:75]\n"
    "v_pk_mul_f32 v[56:57], v[32:33], v[56:57]\n"
    "v_pk_mul_f32 v[58:59], v[34:35], v[58:59]\n"
    "s_nop 1\n"
    "v_pk_mul_f32 v[60:61], v[36:37], v[60:61]\n"
    "v_pk_mul_f32 v[62:63], v[38:39], v[62:63]\n"
    "v_pk_mul_f32 v[32:33], v[32:33], s[64:65] op_sel_hi:[1,0]\n"
    "v_pk_mul_f32 v[34:35], v[34:35], s[64:65] op_sel_hi:[1,0]\n"
    "v_pk_mul_f32 v[36:37], v[36:37], s[64:65] op_sel_hi:[1,0]\n"
    "v_pk_mul_f32 v[38:39], v[38:39], s[64:65] op_sel_hi:[1,0]\n"
    "v_exp_f32 v32, v32\n"
    "v_exp_f32 v33, v33\n"
    "v_exp_f32 v34, v34\n"
    "v_exp_f32 v35, v35\n"
    "v_exp_f32 v36, v36\n"
    "v_exp_f32 v37, v37\n"
    "v_exp_f32 v38, v38\n"
    "v_exp_f32 v39, v39\n"
    "v_pk_add_f32 v[32:33], v[32:33], 1.0 op_sel_hi:[1,0]\n"
    "v_pk_add_f32 v[34:35], v[34:35], 1.0 op_sel_hi:[1,0]\n"
    "v_pk_add_f32 v[36:37], v[36:37], 1.0 op_sel_hi:[1,0]\n"
    "v_pk_add_f32 v[38:39], v[38:39], 1.0 op_sel_hi:[1,0]\n"
    "v_rcp_f32 v32, v32\n"
    "v_rcp_f32 v33, v33\n"
    "v_rcp_f32 v34, v34\n"
    "v_rcp_f32 v35, v35\n"
    "v_rcp_f32 v36, v36\n"
    "v_rcp_f32 v37, v37\n"
    "v_rcp_f32 v38, v38\n"
    "v_rcp_f32 v39, v39\n"
    "v_pk_mul_f32 v[32:33], v[32:33], v[56:57]\n"
    "v_pk_mul_f32 v[34:35], v[34:35], v[58:59]\n"
    "v_pk_mul_f32 v[36:37], v[36:37], v[60:61]\n"
    "v_pk_mul_f32 v[38:39], v[38:39], v[62:63]\n"
    "v_cvt_pk_bf16_f32 v32, v32, v33\n"
    "v_cvt_pk_bf16_f32 v33, v34, v35\n"
    "v_cvt_pk_bf16_f32 v34, v36, v37\n"
    "v_cvt_pk_bf16_f32 v35, v38, v39\n"
    "buffer_store_dwordx4 v[32:35], v15, s[36:39], s73 offen sc1\n"
    "v_pk_mul_f32 v[72:73], v[48:49], v[72:73]\n"
    "v_pk_mul_f32 v[74:75], v[50:51], v[74:75]\n"
    "v_pk_mul_f32 v[48:49], v[48:49], s[64:65] op_sel_hi:[1,0]\n"
    "v_pk_mul_f32 v[50:51], v[50:51], s[64:65] op_sel_hi:[1,0]\n"
    "v_exp_f32 v48, v48\n"
    "v_exp_f32 v49, v49\n"
    "v_exp_f32 v50, v50\n"
    "v_exp_f32 v51, v51\n"
    "v_pk_add_f32 v[48:49], v[48:49], 1.0 op_sel_hi:[1,0]\n"
    "v_pk_add_f32 v[50:51], v[50:51], 1.0 op_sel_hi:[1,0]\n"
    "v_rcp_f32 v48, v48\n"
    "v_rcp_f32 v49, v49\n"
    "v_rcp_f32 v50, v50\n"
    "v_rcp_f32 v51, v51\n"
    "v_pk_mul_f32 v[48:49], v[48:49], v[72:73]\n"
    "v_pk_mul_f32 v[50:51], v[50:51], v[74:75]\n"
    "v_cvt_pk_bf16_f32 v48, v48, v49\n"
    "v_cvt_pk_bf16_f32 v49, v50, v51\n"
    "s_nop 0\n"
    "v_permlane16_swap_b32 v48, v50\n"
    "v_permlane16_swap_b32 v49, v51\n"
    "buffer_store_dwordx4 v[48:51], v4, s[36:39], s73 offen sc1\n"
    "v_mov_b32 v12, s68\n"
    "s_mov_b32 s73, 0x80000000\n"
    "s_mov_b32 s68, s71\n"
    "s_mov_b32 s69, 0\n"
    "s_mov_b32 s70, 0x80000000\n"
    "s_waitcnt vmcnt(8)\n"
    "s_add_u32 s71, s68, 1\n"
    "v_add_u32 v13, v11, v3\n"
    "v_cmp_gt_i32 vcc, s71, v13\n"
    "s_nop 0\n"
    "s_or_b64 exec, vcc, s[62:63]\n"
    "buffer_atomic_smax v11, v2, s[36:39], 0 offen sc0 sc1\n"
    "s_mov_b64 exec, -1\n"
    "buffer_atomic_smax v12, v14, s[36:39], s73 offen sc1\n"
    "s_waitcnt vmcnt(4)\n"
    "s_branch .LgUP_bar1_%=\n"
    ".LgUP_idle0_%=:\n"
    "s_or_b64 exec, vcc, s[62:63]\n"
    "buffer_atomic_smax v5, v2, s[36:39], 0 offen sc0 sc1\n"
    "s_mov_b64 exec, -1\n"
    "s_waitcnt vmcnt(1)\n"
    "buffer_atomic_smax v12, v14, s[36:39], 0 offen sc1\n"
    "s_branch .LgUP_block0_1_%=\n"
    ".LgUP_block0_%=:\n"
    "v_add_u32 v13, v11, v3\n"
    "v_cmp_gt_i32 vcc, s71, v13\n"
    "s_nop 0\n"
    "s_or_b64 exec, vcc, s[62:63]\n"
    "buffer_atomic_smax v11, v2, s[36:39], 0 offen sc0 sc1\n"
    "s_mov_b64 exec, -1\n"
    "s_sleep 3\n"
    "s_waitcnt vmcnt(1)\n"
    ".LgUP_block0_0_%=:\n"
    "v_add_u32 v13, v5, v3\n"
    "v_cmp_gt_i32 vcc, s71, v13\n"
    "s_cbranch_vccz .LgUP_go0_%=\n"
    "v_add_u32 v13, v5, v3\n"
    "v_cmp_gt_i32 vcc, s71, v13\n"
    "s_nop 0\n"
    "s_or_b64 exec, vcc, s[62:63]\n"
    "buffer_atomic_smax v5, v2, s[36:39], 0 offen sc0 sc1\n"
    "s_mov_b64 exec, -1\n"
    "s_sleep 3\n"
    "s_waitcnt vmcnt(1)\n"
    ".LgUP_block0_1_%=:\n"
    "v_add_u32 v13, v11, v3\n"
    "v_cmp_gt_i32 vcc, s71, v13\n"
    "s_cbranch_vccz .LgUP_go0_%=\n"
    "s_sub_u32 s72, s72, 1\n"
    "s_cbranch_scc0 .LgUP_block0_%=\n"
    "s_mov_b32 s72, 0\n"
    ".LgUP_go0_%=:\n"
    "s_lshr_b32 s76, s71, 1\n"
    "s_and_b32 s76, s76, s54\n"
    "s_mul_i32 s76, s76, s53\n"
    "s_add_u32 s76, s76, s52\n"
    "s_add_u32 s76, s76, 1024\n"
    "s_add_u32 m0, s59, 20480\n"
    "s_nop 0\n"
    "buffer_load_dwordx4 v1, s[36:39], s76 offen sc1 lds\n"
    "s_add_u32 m0, s59, 24576\n"
    "s_add_u32 s76, s76, 16384\n"
    "buffer_load_dwordx4 v1, s[36:39], s76 offen sc1 lds\n"
    "s_add_u32 m0, s59, 28672\n"
    "s_add_u32 s76, s76, 16384\n"
    "buffer_load_dwordx4 v1, s[36:39], s76 offen sc1 lds\n"
    "s_add_u32 m0, s59, 32768\n"
    "s_add_u32 s76, s76, 16384\n"
    "buffer_load_dwordx4 v1, s[36:39], s76 offen sc1 lds\n"
    "s_add_u32 m0, s59, 36864\n"
    "s_add_u32 s76, s76, 16384\n"
    "buffer_load_dwordx4 v8, s[36:39], s76 offen sc1 lds\n"
    "s_mov_b32 s69, 0x80000000\n"
    "s_mov_b32 s70, 0x80000000\n"
    "s_waitcnt vmcnt(5)\n"
    "s_add_u32 s71, s68, 1\n"
    "v_add_u32 v13, v11, v3\n"
    "v_cmp_gt_i32 vcc, s71, v13\n"
    "s_nop 0\n"
    "s_or_b64 exec, vcc, s[62:63]\n"
    "buffer_atomic_smax v11, v2, s[36:39], 0 offen sc0 sc1\n"
    "s_mov_b64 exec, -1\n"
    "buffer_atomic_smax v12, v14, s[36:39], s73 offen sc1\n"
    "s_waitcnt vmcnt(2)\n"
    "s_branch .LgUP_bar1_%=\n"
    ".LgUP_bar0_%=:\n"
    "s_barrier\n"
    "ds_read_b128 v[16:19], v0 offset:0\n"
    "ds_read_b128 v[20:23], v0 offset:1024\n"
    "ds_read_b128 v[24:27], v0 offset:2048\n"
    "v_add_u32 v13, v5, v3\n"
    "v_cmp_gt_i32 vcc, s71, v13\n"
    "s_nop 0\n"
    "s_or_b64 exec, vcc, s[62:63]\n"
    "buffer_atomic_smax v5, v2, s[36:39], 0 offen sc0 sc1\n"
    "s_mov_b64 exec, -1\n"
    "s_lshr_b32 s76, s71, 1\n"
    "s_and_b32 s76, s76, s54\n"
    "s_mul_i32 s76, s76, s53\n"
    "s_add_u32 s76, s76, s52\n"
    "s_add_u32 s76, s76, 1024\n"
    "s_waitcnt lgkmcnt(2)\n"
    "s_branch .LgUP_head0_%=\n"
    ".LgUP_poll1_%=:\n"
    "v_mfma_f32_16x16x32_bf16 v[64:67], a[36:39], v[20:23], v[64:67]\n"
    "s_or_b64 exec, vcc, s[62:63]\n"
    "buffer_atomic_smax v11, v2, s[36:39], 0 offen sc0 sc1\n"
    "s_mov_b64 exec, -1\n"
    "v_exp_f32 v34, v34\n"
    "v_mfma_f32_16x16x32_bf16 v[68:71], a[40:43], v[20:23], v[68:71]\n"
    "s_waitcnt lgkmcnt(1)\n"
    "v_mul_f32 v35, 0xbfb8aa3b, v35\n"
    "v_mul_f32 v36, 0xbfb8aa3b, v36\n"
    "v_mfma_f32_16x16x32_bf16 v[76:79], a[44:47], v[20:23], v[76:79]\n"
    "ds_read_b128 v[20:23], v9 offset:4096\n"
    "v_mul_f32 v37, 0xbfb8aa3b, v37\n"
    "v_mfma_f32_16x16x32_bf16 v[40:43], a[48:51], v[24:27], v[40:43]\n"
    "v_exp_f32 v35, v35\n"
    "v_mfma_f32_16x16x32_bf16 v[44:47], a[52:55], v[24:27], v[44:47]\n"
    "v_exp_f32 v36, v36\n"
    "v_mfma_f32_16x16x32_bf16 v[52:55], a[56:59], v[24:27], v[52:55]\n"
    "v_exp_f32 v37, v37\n"
    "v_mfma_f32_16x16x32_bf16 v[64:67], a[60:63], v[24:27], v[64:67]\n"
    "v_mul_f32 v38, 0xbfb8aa3b, v38\n"
    "v_mul_f32 v39, 0xbfb8aa3b, v39\n"
    "v_mfma_f32_16x16x32_bf16 v[68:71], a[64:67], v[24:27], v[68:71]\n"
    "s_waitcnt lgkmcnt(1)\n"
    "v_exp_f32 v38, v38\n"
    "v_mfma_f32_16x16x32_bf16 v[76:79], a[68:71], v[24:27], v[76:79]\n"
    "ds_read_b128 v[24:27], v9 offset:5120\n"
    "v_add_f32 v32, 1.0, v32\n"
    "v_mfma_f32_16x16x32_bf16 v[40:43], a[72:75], v[16:19], v[40:43]\n"
    "v_exp_f32 v39, v39\n"
    "v_mfma_f32_16x16x32_bf16 v[44:47], a[76:79], v[16:19], v[44:47]\n"
    "v_rcp_f32 v32, v32\n"
    "v_mfma_f32_16x16x32_bf16 v[52:55], a[80:83], v[16:19], v[52:55]\n"
    "v_add_f32 v33, 1.0, v33\n"
    "v_add_f32 v34, 1.0, v34\n"
    "v_mfma_f32_16x16x32_bf16 v[64:67], a[84:87], v[16:19], v[64:67]\n"
    "v_rcp_f32 v33, v33\n"
    "v_mfma_f32_16x16x32_bf16 v[68:71], a[88:91], v[16:19], v[68:71]\n"
    "s_waitcnt lgkmcnt(1)\n"
    "v_rcp_f32 v34, v34\n"
    "v_mfma_f32_16x16x32_bf16 v[76:79], a[92:95], v[16:19], v[76:79]\n"
    "ds_read_b128 v[16:19], v9 offset:6144\n"
    "v_add_f32 v35, 1.0, v35\n"
    "v_mfma_f32_16x16x32_bf16 v[40:43], a[96:99], v[20:23], v[40:43]\n"
    "s_waitcnt vmcnt(1)\n"
    "v_rcp_f32 v35, v35\n"
    "v_add_u32 v13, v5, v3\n"
    "v_mfma_f32_16x16x32_bf16 v[44:47], a[100:103], v[20:23], v[44:47]\n"
    "v_add_f32 v36, 1.0, v36\n"
    "v_add_f32 v37, 1.0, v37\n"
    "v_cmp_gt_i32 vcc, s71, v13\n"
    "v_mfma_f32_16x16x32_bf16 v[52:55], a[104:107], v[20:23], v[52:55]\n"
    "v_rcp_f32 v36, v36\n"
    "v_mfma_f32_16x16x32_bf16 v[64:67], a[108:111], v[20:23], v[64:67]\n"
    "v_rcp_f32 v37, v37\n"
    "v_mfma_f32_16x16x32_bf16 v[68:71], a[112:115], v[20:23], v[68:71]\n"
    "s_waitcnt lgkmcnt(1)\n"
    "v_add_f32 v38, 1.0, v38\n"
    "v_add_f32 v39, 1.0, v39\n"
    "s_cbranch_vccz .LgUP_dma1_29_%=\n"
    "v_mfma_f32_16x16x32_bf16 v[76:79], a[116:119], v[20:23], v[76:79]\n"
    "ds_read_b128 v[20:23], v9 offset:7168\n"
    "s_or_b64 exec, vcc, s[62:63]\n"
    "buffer_atomic_smax v5, v2, s[36:39], 0 offen sc0 sc1\n"
    "s_mov_b64 exec, -1\n"
    "v_mul_f32 v32, v32, v56\n"
    "v_mfma_f32_16x16x32_bf16 v[40:43], a[120:123], v[24:27], v[40:43]\n"
    "s_waitcnt vmcnt(5)\n"
    "buffer_atomic_smax v12, v14, s[36:39], s69 offen sc1\n"
    "v_rcp_f32 v38, v38\n"
    "v_mfma_f32_16x16x32_bf16 v[44:47], a[124:127], v[24:27], v[44:47]\n"
    "v_rcp_f32 v39, v39\n"
    "v_mfma_f32_16x16x32_bf16 v[52:55], a[128:131], v[24:27], v[52:55]\n"
    "v_mul_f32 v33, v33, v57\n"
    "v_mul_f32 v34, v34, v58\n"
    "v_mfma_f32_16x16x32_bf16 v[64:67], a[132:135], v[24:27], v[64:67]\n"
    "v_mul_f32 v35, v35, v59\n"
    "v_mul_f32 v36, v36, v60\n"
    "v_mfma_f32_16x16x32_bf16 v[68:71], a[136:139], v[24:27], v[68:71]\n"
    "s_waitcnt lgkmcnt(1)\n"
    "v_mul_f32 v37, v37, v61\n"
    "v_mul_f32 v38, v38, v62\n"
    "v_mfma_f32_16x16x32_bf16 v[76:79], a[140:143], v[24:27], v[76:79]\n"
    "ds_read_b128 v[24:27], v9 offset:8192\n"
    "v_mul_f32 v39, v39, v63\n"
    "v_mfma_f32_16x16x32_bf16 v[40:43], a[144:147], v[16:19], v[40:43]\n"
    "v_cvt_pk_bf16_f32 v32, v32, v33\n"
    "v_cvt_pk_bf16_f32 v33, v34, v35\n"
    "v_mfma_f32_16x16x32_bf16 v[44:47], a[148:151], v[16:19], v[44:47]\n"
    "v_cvt_pk_bf16_f32 v34, v36, v37\n"
    "v_cvt_pk_bf16_f32 v35, v38, v39\n"
    "v_mfma_f32_16x16x32_bf16 v[52:55], a[152:155], v[16:19], v[52:55]\n"
    "buffer_store_dwordx4 v[32:35], v15, s[36:39], s73 offen sc1\n"
    "v_mul_f32 v72, v48, v72\n"
    "v_mfma_f32_16x16x32_bf16 v[64:67], a[156:159], v[16:19], v[64:67]\n"
    "v_mul_f32 v73, v49, v73\n"
    "v_mul_f32 v74, v50, v74\n"
    "v_mfma_f32_16x16x32_bf16 v[68:71], a[160:163], v[16:19], v[68:71]\n"
    "s_waitcnt lgkmcnt(1)\n"
    "v_mul_f32 v75, v51, v75\n"
    "v_mul_f32 v48, 0xbfb8aa3b, v48\n"
    "v_mfma_f32_16x16x32_bf16 v[76:79], a[164:167], v[16:19], v[76:79]\n"
    "ds_read_b128 v[16:19], v9 offset:9216\n"
    "v_mul_f32 v49, 0xbfb8aa3b, v49\n"
    "v_mfma_f32_16x16x32_bf16 v[40:43], a[168:171], v[20:23], v[40:43]\n"
    "v_exp_f32 v48, v48\n"
    "v_mfma_f32_16x16x32_bf16 v[44:47], a[172:175], v[20:23], v[44:47]\n"
    "v_exp_f32 v49, v49\n"
    "v_mfma_f32_16x16x32_bf16 v[52:55], a[176:179], v[20:23], v[52:55]\n"
    "s_waitcnt vmcnt(3)\n"
    "v_mul_f32 v50, 0xbfb8aa3b, v50\n"
    "v_mul_f32 v51, 0xbfb8aa3b, v51\n"
    "v_add_u32 v13, v11, v3\n"
    "v_mfma_f32_16x16x32_bf16 v[64:67], a[180:183], v[20:23], v[64:67]\n"
    "v_exp_f32 v50, v50\n"
    "v_cmp_gt_i32 vcc, s71, v13\n"
    "v_mfma_f32_16x16x32_bf16 v[68:71], a[184:187], v[20:23], v[68:71]\n"
    "s_waitcnt lgkmcnt(1)\n"
    "v_exp_f32 v51, v51\n"
    "v_mfma_f32_16x16x32_bf16 v[76:79], a[188:191], v[20:23], v[76:79]\n"
    "ds_read_b128 v[20:23], v9 offset:10240\n"
    "v_add_f32 v48, 1.0, v48\n"
    "v_mfma_f32_16x16x32_bf16 v[40:43], a[192:195], v[24:27], v[40:43]\n"
    "v_rcp_f32 v48, v48\n"
    "s_cbranch_vccz .LgUP_dma1_49_%=\n"
    "v_mfma_f32_16x16x32_bf16 v[44:47], a[196:199], v[24:27], v[44:47]\n"
    "s_or_b64 exec, vcc, s[62:63]\n"
    "buffer_atomic_smax v11, v2, s[36:39], 0 offen sc0 sc1\n"
    "s_mov_b64 exec, -1\n"
    "v_add_f32 v49, 1.0, v49\n"
    "v_add_f32 v50, 1.0, v50\n"
    "v_mfma_f32_16x16x32_bf16 v[52:55], a[200:203], v[24:27], v[52:55]\n"
    "v_rcp_f32 v49, v49\n"
    "v_mfma_f32_16x16x32_bf16 v[64:67], a[204:207], v[24:27], v[64:67]\n"
    "v_rcp_f32 v50, v50\n"
    "v_mfma_f32_16x16x32_bf16 v[68:71], a[208:211], v[24:27], v[68:71]\n"
    "s_waitcnt lgkmcnt(1)\n"
    "v_add_f32 v51, 1.0, v51\n"
    "v_mul_f32 v48, v48, v72\n"
    "v_mfma_f32_16x16x32_bf16 v[76:79], a[212:215], v[24:27], v[76:79]\n"
    "ds_read_b128 v[24:27], v9 offset:11264\n"
    "v_mul_f32 v49, v49, v73\n"
    "v_mfma_f32_16x16x32_bf16 v[40:43], a[216:219], v[16:19], v[40:43]\n"
    "v_rcp_f32 v51, v51\n"
    "v_mfma_f32_16x16x32_bf16 v[44:47], a[220:223], v[16:19], v[44:47]\n"
    "v_mul_f32 v50, v50, v74\n"
    "v_mul_f32 v51, v51, v75\n"
    "v_mfma_f32_16x16x32_bf16 v[52:55], a[224:227], v[16:19], v[52:55]\n"
    "v_cvt_pk_bf16_f32 v48, v48, v49\n"
    "v_cvt_pk_bf16_f32 v49, v50, v51\n"
    "v_mfma_f32_16x16x32_bf16 v[64:67], a[228:231], v[16:19], v[64:67]\n"
    "s_nop 0\n"
    "v_mfma_f32_16x16x32_bf16 v[68:71], a[232:235], v[16:19], v[68:71]\n"
    "s_waitcnt lgkmcnt(1)\n"
    "v_permlane16_swap_b32 v48, v50\n"
    "v_mfma_f32_16x16x32_bf16 v[76:79], a[236:239], v[16:19], v[76:79]\n"
    "ds_read_b128 v[16:19], v9 offset:12288\n"
    "v_mfma_f32_16x16x32_bf16 v[40:43], a[240:243], v[20:23], v[40:43]\n"
    "v_permlane16_swap_b32 v49, v51\n"
    "v_mfma_f32_16x16x32_bf16 v[44:47], a[244:247], v[20:23], v[44:47]\n"
    "buffer_store_dwordx4 v[48:51], v4, s[36:39], s73 offen sc1\n"
    "v_mfma_f32_16x16x32_bf16 v[52:55], a[248:251], v[20:23], v[52:55]\n"
    "s_lshr_b32 s73, s68, 1\n"
    "v_mfma_f32_16x16x32_bf16 v[64:67], a[252:255], v[20:23], v[64:67]\n"
    "v_mfma_f32_16x16x32_bf16 v[68:71], v[80:83], v[20:23], v[68:71]\n"
    "s_waitcnt lgkmcnt(1)\n"
    "s_waitcnt vmcnt(4)\n"
    "v_add_u32 v13, v5, v3\n"
    "v_mfma_f32_16x16x32_bf16 v[76:79], v[84:87], v[20:23], v[76:79]\n"
    "ds_read_b128 v[20:23], v9 offset:13312\n"
    "v_cmp_gt_i32 vcc, s71, v13\n"
    "v_mfma_f32_16x16x32_bf16 v[40:43], v[88:91], v[24:27], v[40:43]\n"
    "s_and_b32 s73, s73, s57\n"
    "v_mfma_f32_16x16x32_bf16 v[44:47], v[92:95], v[24:27], v[44:47]\n"
    "v_mfma_f32_16x16x32_bf16 v[52:55], v[96:99], v[24:27], v[52:55]\n"
    "s_mul_i32 s73, s73, s56\n"
    "s_cbranch_vccz .LgUP_dma1_69_%=\n"
    "v_mfma_f32_16x16x32_bf16 v[64:67], v[100:103], v[24:27], v[64:67]\n"
    "s_or_b64 exec, vcc, s[62:63]\n"
    "buffer_atomic_smax v5, v2, s[36:39], 0 offen sc0 sc1\n"
    "s_mov_b64 exec, -1\n"
    "v_mfma_f32_16x16x32_bf16 v[68:71], v[104:107], v[24:27], v[68:71]\n"
    "s_waitcnt lgkmcnt(1)\n"
    "v_mfma_f32_16x16x32_bf16 v[76:79], v[108:111], v[24:27], v[76:79]\n"
    "ds_read_b128 v[24:27], v9 offset:14336\n"
    "v_mfma_f32_16x16x32_bf16 v[40:43], v[112:115], v[16:19], v[40:43]\n"
    "s_add_u32 s73, s73, s55\n"
    "v_mfma_f32_16x16x32_bf16 v[44:47], v[116:119], v[16:19], v[44:47]\n"
    "v_mfma_f32_16x16x32_bf16 v[52:55], v[120:123], v[16:19], v[52:55]\n"
    "v_mfma_f32_16x16x32_bf16 v[64:67], v[124:127], v[16:19], v[64:67]\n"
    "v_mfma_f32_16x16x32_bf16 v[68:71], v[128:131], v[16:19], v[68:71]\n"
    "s_waitcnt lgkmcnt(1)\n"
    "v_mfma_f32_16x16x32_bf16 v[76:79], v[132:135], v[16:19], v[76:79]\n"
    "ds_read_b128 v[16:19], v9 offset:15360\n"
    "v_mfma_f32_16x16x32_bf16 v[40:43], v[136:139], v[20:23], v[40:43]\n"
    "v_mfma_f32_16x16x32_bf16 v[44:47], v[140:143], v[20:23], v[44:47]\n"
    "v_mfma_f32_16x16x32_bf16 v[52:55], v[144:147], v[20:23], v[52:55]\n"
    "v_mfma_f32_16x16x32_bf16 v[64:67], v[148:151], v[20:23], v[64:67]\n"
    "v_mfma_f32_16x16x32_bf16 v[68:71], v[152:155], v[20:23], v[68:71]\n"
    "s_waitcnt lgkmcnt(1)\n"
    "v_mfma_f32_16x16x32_bf16 v[76:79], v[156:159], v[20:23], v[76:79]\n"
    "ds_read_b128 v[20:23], v9 offset:16384\n"
    "v_mfma_f32_16x16x32_bf16 v[40:43], v[160:163], v[24:27], v[40:43]\n"
    "s_waitcnt vmcnt(2)\n"
    "v_add_u32 v13, v11, v3\n"
    "v_mfma_f32_16x16x32_bf16 v[44:47], v[164:167], v[24:27], v[44:47]\n"
    "v_cmp_gt_i32 vcc, s71, v13\n"
    "v_mfma_f32_16x16x32_bf16 v[52:55], v[168:171], v[24:27], v[52:55]\n"
    "v_mfma_f32_16x16x32_bf16 v[64:67], v[172:175], v[24:27], v[64:67]\n"
    "v_mfma_f32_16x16x32_bf16 v[68:71], v[176:179], v[24:27], v[68:71]\n"
    "s_waitcnt lgkmcnt(1)\n"
    "s_cbranch_vccz .LgUP_dma1_89_%=\n"
    "v_mfma_f32_16x16x32_bf16 v[76:79], v[180:183], v[24:27], v[76:79]\n"
    "ds_read_b128 v[24:27], v9 offset:17408\n"
    "s_or_b64 exec, vcc, s[62:63]\n"
    "buffer_atomic_smax v11, v2, s[36:39], 0 offen sc0 sc1\n"
    "s_mov_b64 exec, -1\n"
    "v_mfma_f32_16x16x32_bf16 v[40:43], v[184:187], v[16:19], v[40:43]\n"
    "v_mfma_f32_16x16x32_bf16 v[44:47], v[188:191], v[16:19], v[44:47]\n"
    "v_mfma_f32_16x16x32_bf16 v[52:55], v[192:195], v[16:19], v[52:55]\n"
    "v_mfma_f32_16x16x32_bf16 v[64:67], v[196:199], v[16:19], v[64:67]\n"
    "v_mfma_f32_16x16x32_bf16 v[68:71], v[200:203], v[16:19], v[68:71]\n"
    "s_waitcnt lgkmcnt(1)\n"
    "v_mfma_f32_16x16x32_bf16 v[76:79], v[204:207], v[16:19], v[76:79]\n"
    "v_mfma_f32_16x16x32_bf16 v[40:43], v[208:211], v[20:23], v[40:43]\n"
    "s_waitcnt vmcnt(2)\n"
    "buffer_atomic_smax v12, v14, s[36:39], s70 offen sc1\n"
    "v_mfma_f32_16x16x32_bf16 v[44:47], v[212:215], v[20:23], v[44:47]\n"
    "v_mfma_f32_16x16x32_bf16 v[52:55], v[216:219], v[20:23], v[52:55]\n"
    "v_mfma_f32_16x16x32_bf16 v[64:67], v[220:223], v[20:23], v[64:67]\n"
    "v_mfma_f32_16x16x32_bf16 v[68:71], v[224:227], v[20:23], v[68:71]\n"
    "s_waitcnt lgkmcnt(0)\n"
    "v_mfma_f32_16x16x32_bf16 v[76:79], v[228:231], v[20:23], v[76:79]\n"
    "v_mfma_f32_16x16x32_bf16 v[40:43], v[232:235], v[24:27], v[40:43]\n"
    "v_mfma_f32_16x16x32_bf16 v[44:47], v[236:239], v[24:27], v[44:47]\n"
    "v_mfma_f32_16x16x32_bf16 v[52:55], v[240:243], v[24:27], v[52:55]\n"
    "v_mfma_f32_16x16x32_bf16 v[64:67], v[244:247], v[24:27], v[64:67]\n"
    "v_mfma_f32_16x16x32_bf16 v[68:71], v[248:251], v[24:27], v[68:71]\n"
    "v_mfma_f32_16x16x32_bf16 v[76:79], v[252:255], v[24:27], v[76:79]\n"
    "v_pk_mul_f32 v[64:65], v[40:41], v[64:65]\n"
    "v_pk_mul_f32 v[66:67], v[42:43], v[66:67]\n"
    "s_nop 1\n"
    "v_pk_mul_f32 v[68:69], v[44:45], v[68:69]\n"
    "v_pk_mul_f32 v[70:71], v[46:47], v[70:71]\n"
    "v_pk_mul_f32 v[40:41], v[40:41], s[64:65] op_sel_hi:[1,0]\n"
    "v_pk_mul_f32 v[42:43], v[42:43], s[64:65] op_sel_hi:[1,0]\n"
    "v_pk_mul_f32 v[44:45], v[44:45], s[64:65] op_sel_hi:[1,0]\n"
    "v_pk_mul_f32 v[46:47], v[46:47], s[64:65] op_sel_hi:[1,0]\n"
    "v_exp_f32 v40, v40\n"
    "v_exp_f32 v41, v41\n"
    "v_exp_f32 v42, v42\n"
    "v_exp_f32 v43, v43\n"
    "v_exp_f32 v44, v44\n"
    "v_exp_f32 v45, v45\n"
    "v_exp_f32 v46, v46\n"
    "v_exp_f32 v47, v47\n"
    "v_pk_add_f32 v[40:41], v[40:41], 1.0 op_sel_hi:[1,0]\n"
    "v_pk_add_f32 v[42:43], v[42:43], 1.0 op_sel_hi:[1,0]\n"
    "v_pk_add_f32 v[44:45], v[44:45], 1.0 op_sel_hi:[1,0]\n"
    "v_pk_add_f32 v[46:47], v[46:47], 1.0 op_sel_hi:[1,0]\n"
    "v_rcp_f32 v40, v40\n"
    "v_rcp_f32 v41, v41\n"
    "v_rcp_f32 v42, v42\n"
    "v_rcp_f32 v43, v43\n"
    "v_rcp_f32 v44, v44\n"
    "v_rcp_f32 v45, v45\n"
    "v_rcp_f32 v46, v46\n"
    "v_rcp_f32 v47, v47\n"
    "v_pk_mul_f32 v[40:41], v[40:41], v[64:65]\n"
    "v_pk_mul_f32 v[42:43], v[42:43], v[66:67]\n"
    "v_pk_mul_f32 v[44:45], v[44:45], v[68:69]\n"
    "v_pk_mul_f32 v[46:47], v[46:47], v[70:71]\n"
    "v_cvt_pk_bf16_f32 v40, v40, v41\n"
    "v_cvt_pk_bf16_f32 v41, v42, v43\n"
    "v_cvt_pk_bf16_f32 v42, v44, v45\n"
    "v_cvt_pk_bf16_f32 v43, v46, v47\n"
    "buffer_store_dwordx4 v[40:43], v15, s[36:39], s73 offen offset:1024 sc1\n"
    "v_pk_mul_f32 v[76:77], v[52:53], v[76:77]\n"
    "v_pk_mul_f32 v[78:79], v[54:55], v[78:79]\n"
    "v_pk_mul_f32 v[52:53], v[52:53], s[64:65] op_sel_hi:[1,0]\n"
    "v_pk_mul_f32 v[54:55], v[54:55], s[64:65] op_sel_hi:[1,0]\n"
    "v_exp_f32 v52, v52\n"
    "v_exp_f32 v53, v53\n"
    "v_exp_f32 v54, v54\n"
    "v_exp_f32 v55, v55\n"
    "v_pk_add_f32 v[52:53], v[52:53], 1.0 op_sel_hi:[1,0]\n"
    "v_pk_add_f32 v[54:55], v[54:55], 1.0 op_sel_hi:[1,0]\n"
    "v_rcp_f32 v52, v52\n"
    "v_rcp_f32 v53, v53\n"
    "v_rcp_f32 v54, v54\n"
    "v_rcp_f32 v55, v55\n"
    "v_pk_mul_f32 v[52:53], v[52:53], v[76:77]\n"
    "v_pk_mul_f32 v[54:55], v[54:55], v[78:79]\n"
    "v_cvt_pk_bf16_f32 v52, v52, v53\n"
    "v_cvt_pk_bf16_f32 v53, v54, v55\n"
    "s_nop 0\n"
    "v_permlane16_swap_b32 v52, v54\n"
    "v_permlane16_swap_b32 v53, v55\n"
    "buffer_store_dwordx4 v[52:55], v4, s[36:39], s73 offen offset:1024 sc1\n"
    "v_mov_b32 v12, s68\n"
    "s_mov_b32 s73, 0x80000000\n"
    "s_mov_b32 s68, s71\n"
    "s_cmp_eq_u32 s68, 128\n"
    "s_cbranch_scc1 .LgUP_done_%=\n"
    "s_waitcnt vmcnt(4)\n"
    "v_add_u32 v13, v5, v3\n"
    "v_cmp_gt_i32 vcc, s71, v13\n"
    "s_cbranch_vccnz .LgUP_idle1_%=\n"
    "s_add_u32 m0, s59, 0\n"
    "s_nop 0\n"
    "buffer_load_dwordx4 v1, s[36:39], s76 offen sc1 lds\n"
    "s_add_u32 m0, s59, 4096\n"
    "s_add_u32 s76, s76, 16384\n"
    "buffer_load_dwordx4 v1, s[36:39], s76 offen sc1 lds\n"
    "s_add_u32 m0, s59, 8192\n"
    "s_add_u32 s76, s76, 16384\n"
    "buffer_load_dwordx4 v1, s[36:39], s76 offen sc1 lds\n"
    "s_add_u32 m0, s59, 12288\n"
    "s_add_u32 s76, s76, 16384\n"
    "buffer_load_dwordx4 v1, s[36:39], s76 offen sc1 lds\n"
    "s_add_u32 m0, s59, 16384\n"
    "s_add_u32 s76, s76, 16384\n"
    "buffer_load_dwordx4 v8, s[36:39], s76 offen sc1 lds\n"
    "s_waitcnt vmcnt(5)\n"
    "buffer_atomic_smax v12, v10, s[36:39], 0 offen sc1\n"
    "s_mov_b32 s69, 0x80000000\n"
    "s_mov_b32 s70, 0x80000000\n"
    "s_waitcnt vmcnt(9)\n"
    "s_add_u32 s71, s68, 1\n"
    "v_add_u32 v13, v11, v3\n"
    "v_cmp_gt_i32 vcc, s71, v13\n"
    "s_nop 0\n"
    "s_or_b64 exec, vcc, s[62:63]\n"
    "buffer_atomic_smax v11, v2, s[36:39], 0 offen sc0 sc1\n"
    "s_mov_b64 exec, -1\n"
    "buffer_atomic_smax v12, v14, s[36:39], s73 offen sc1\n"
    "s_waitcnt vmcnt(3)\n"
    "s_branch .LgUP_bar0_%=\n"
    ".LgUP_dma1_29_%=:\n"
    "v_mfma_f32_16x16x32_bf16 v[76:79], a[116:119], v[20:23], v[76:79]\n"
    "ds_read_b128 v[20:23], v9 offset:7168\n"
    "v_mul_f32 v32, v32, v56\n"
    "v_mfma_f32_16x16x32_bf16 v[40:43], a[120:123], v[24:27], v[40:43]\n"
    "s_add_u32 m0, s59, 0\n"
    "s_waitcnt vmcnt(4)\n"
    "buffer_atomic_smax v12, v14, s[36:39], s69 offen sc1\n"
    "v_rcp_f32 v38, v38\n"
    "v_mfma_f32_16x16x32_bf16 v[44:47], a[124:127], v[24:27], v[44:47]\n"
    "buffer_load_dwordx4 v1, s[36:39], s76 offen sc1 lds\n"
    "v_rcp_f32 v39, v39\n"
    "v_mfma_f32_16x16x32_bf16 v[52:55], a[128:131], v[24:27], v[52:55]\n"
    "s_add_u32 m0, s59, 4096\n"
    "s_add_u32 s76, s76, 16384\n"
    "v_mul_f32 v33, v33, v57\n"
    "v_mul_f32 v34, v34, v58\n"
    "v_mfma_f32_16x16x32_bf16 v[64:67], a[132:135], v[24:27], v[64:67]\n"
    "buffer_load_dwordx4 v1, s[36:39], s76 offen sc1 lds\n"
    "v_mul_f32 v35, v35, v59\n"
    "v_mul_f32 v36, v36, v60\n"
    "v_mfma_f32_16x16x32_bf16 v[68:71], a[136:139], v[24:27], v[68:71]\n"
    "s_waitcnt lgkmcnt(1)\n"
    "v_mul_f32 v37, v37, v61\n"
    "v_mul_f32 v38, v38, v62\n"
    "v_mfma_f32_16x16x32_bf16 v[76:79], a[140:143], v[24:27], v[76:79]\n"
    "ds_read_b128 v[24:27], v9 offset:8192\n"
    "v_mul_f32 v39, v39, v63\n"
    "v_mfma_f32_16x16x32_bf16 v[40:43], a[144:147], v[16:19], v[40:43]\n"
    "s_add_u32 m0, s59, 8192\n"
    "s_add_u32 s76, s76, 16384\n"
    "v_cvt_pk_bf16_f32 v32, v32, v33\n"
    "v_cvt_pk_bf16_f32 v33, v34, v35\n"
    "v_mfma_f32_16x16x32_bf16 v[44:47], a[148:151], v[16:19], v[44:47]\n"
    "buffer_load_dwordx4 v1, s[36:39], s76 offen sc1 lds\n"
    "v_cvt_pk_bf16_f32 v34, v36, v37\n"
    "v_cvt_pk_bf16_f32 v35, v38, v39\n"
    "v_mfma_f32_16x16x32_bf16 v[52:55], a[152:155], v[16:19], v[52:55]\n"
    "buffer_store_dwordx4 v[32:35], v15, s[36:39], s73 offen sc1\n"
    "v_mul_f32 v72, v48, v72\n"
    "v_mfma_f32_16x16x32_bf16 v[64:67], a[156:159], v[16:19], v[64:67]\n"
    "s_add_u32 m0, s59, 12288\n"
    "s_add_u32 s76, s76, 16384\n"
    "v_mul_f32 v73, v49, v73\n"
    "v_mul_f32 v74, v50, v74\n"
    "v_mfma_f32_16x16x32_bf16 v[68:71], a[160:163], v[16:19], v[68:71]\n"
    "s_waitcnt lgkmcnt(1)\n"
    "buffer_load_dwordx4 v1, s[36:39], s76 offen sc1 lds\n"
    "v_mul_f32 v75, v51, v75\n"
    "v_mul_f32 v48, 0xbfb8aa3b, v48\n"
    "v_mfma_f32_16x16x32_bf16 v[76:79], a[164:167], v[16:19], v[76:79]\n"
    "ds_read_b128 v[16:19], v9 offset:9216\n"
    "v_mul_f32 v49, 0xbfb8aa3b, v49\n"
    "v_mfma_f32_16x16x32_bf16 v[40:43], a[168:171], v[20:23], v[40:43]\n"
    "v_exp_f32 v48, v48\n"
    "v_mfma_f32_16x16x32_bf16 v[44:47], a[172:175], v[20:23], v[44:47]\n"
    "s_add_u32 m0, s59, 16384\n"
    "s_add_u32 s76, s76, 16384\n"
    "v_exp_f32 v49, v49\n"
    "v_mfma_f32_16x16x32_bf16 v[52:55], a[176:179], v[20:23], v[52:55]\n"
    "buffer_load_dwordx4 v8, s[36:39], s76 offen sc1 lds\n"
    "v_mul_f32 v50, 0xbfb8aa3b, v50\n"
    "v_mul_f32 v51, 0xbfb8aa3b, v51\n"
    "v_mfma_f32_16x16x32_bf16 v[64:67], a[180:183], v[20:23], v[64:67]\n"
    "v_exp_f32 v50, v50\n"
    "v_mfma_f32_16x16x32_bf16 v[68:71], a[184:187], v[20:23], v[68:71]\n"
    "s_waitcnt lgkmcnt(1)\n"
    "v_exp_f32 v51, v51\n"
    "v_mfma_f32_16x16x32_bf16 v[76:79], a[188:191], v[20:23], v[76:79]\n"
    "ds_read_b128 v[20:23], v9 offset:10240\n"
    "v_add_f32 v48, 1.0, v48\n"
    "v_mfma_f32_16x16x32_bf16 v[40:43], a[192:195], v[24:27], v[40:43]\n"
    "v_rcp_f32 v48, v48\n"
    "v_mfma_f32_16x16x32_bf16 v[44:47], a[196:199], v[24:27], v[44:47]\n"
    "v_add_f32 v49, 1.0, v49\n"
    "v_add_f32 v50, 1.0, v50\n"
    "v_mfma_f32_16x16x32_bf16 v[52:55], a[200:203], v[24:27], v[52:55]\n"
    "v_rcp_f32 v49, v49\n"
    "v_mfma_f32_16x16x32_bf16 v[64:67], a[204:207], v[24:27], v[64:67]\n"
    "v_rcp_f32 v50, v50\n"
    "v_mfma_f32_16x16x32_bf16 v[68:71], a[208:211], v[24:27], v[68:71]\n"
    "s_waitcnt lgkmcnt(1)\n"
    "v_add_f32 v51, 1.0, v51\n"
    "v_mul_f32 v48, v48, v72\n"
    "v_mfma_f32_16x16x32_bf16 v[76:79], a[212:215], v[24:27], v[76:79]\n"
    "ds_read_b128 v[24:27], v9 offset:11264\n"
    "v_mul_f32 v49, v49, v73\n"
    "v_mfma_f32_16x16x32_bf16 v[40:43], a[216:219], v[16:19], v[40:43]\n"
    "v_rcp_f32 v51, v51\n"
    "v_mfma_f32_16x16x32_bf16 v[44:47], a[220:223], v[16:19], v[44:47]\n"
    "v_mul_f32 v50, v50, v74\n"
    "v_mul_f32 v51, v51, v75\n"
    "v_mfma_f32_16x16x32_bf16 v[52:55], a[224:227], v[16:19], v[52:55]\n"
    "v_cvt_pk_bf16_f32 v48, v48, v49\n"
    "v_cvt_pk_bf16_f32 v49, v50, v51\n"
    "v_mfma_f32_16x16x32_bf16 v[64:67], a[228:231], v[16:19], v[64:67]\n"
    "s_nop 0\n"
    "v_mfma_f32_16x16x32_bf16 v[68:71], a[232:235], v[16:19], v[68:71]\n"
    "s_waitcnt lgkmcnt(1)\n"
    "v_permlane16_swap_b32 v48, v50\n"
    "v_mfma_f32_16x16x32_bf16 v[76:79], a[236:239], v[16:19], v[76:79]\n"
    "ds_read_b128 v[16:19], v9 offset:12288\n"
    "v_mfma_f32_16x16x32_bf16 v[40:43], a[240:243], v[20:23], v[40:43]\n"
    "v_permlane16_swap_b32 v49, v51\n"
    "v_mfma_f32_16x16x32_bf16 v[44:47], a[244:247], v[20:23], v[44:47]\n"
    "buffer_store_dwordx4 v[48:51], v4, s[36:39], s73 offen sc1\n"
    "v_mfma_f32_16x16x32_bf16 v[52:55], a[248:251], v[20:23], v[52:55]\n"
    "s_lshr_b32 s73, s68, 1\n"
    "v_mfma_f32_16x16x32_bf16 v[64:67], a[252:255], v[20:23], v[64:67]\n"
    "v_mfma_f32_16x16x32_bf16 v[68:71], v[80:83], v[20:23], v[68:71]\n"
    "s_waitcnt lgkmcnt(1)\n"
    "v_mfma_f32_16x16x32_bf16 v[76:79], v[84:87], v[20:23], v[76:79]\n"
    "ds_read_b128 v[20:23], v9 offset:13312\n"
    "v_mfma_f32_16x16x32_bf16 v[40:43], v[88:91], v[24:27], v[40:43]\n"
    "s_and_b32 s73, s73, s57\n"
    "v_mfma_f32_16x16x32_bf16 v[44:47], v[92:95], v[24:27], v[44:47]\n"
    "v_mfma_f32_16x16x32_bf16 v[52:55], v[96:99], v[24:27], v[52:55]\n"
    "s_mul_i32 s73, s73, s56\n"
    "v_mfma_f32_16x16x32_bf16 v[64:67], v[100:103], v[24:27], v[64:67]\n"
    "v_mfma_f32_16x16x32_bf16 v[68:71], v[104:107], v[24:27], v[68:71]\n"
    "s_waitcnt lgkmcnt(1)\n"
    "v_mfma_f32_16x16x32_bf16 v[76:79], v[108:111], v[24:27], v[76:79]\n"
    "ds_read_b128 v[24:27], v9 offset:14336\n"
    "s_branch .LgUP_join1_%=\n"
    ".LgUP_dma1_49_%=:\n"
    "v_mfma_f32_16x16x32_bf16 v[44:47], a[196:199], v[24:27], v[44:47]\n"
    "s_add_u32 m0, s59, 0\n"
    "v_add_f32 v49, 1.0, v49\n"
    "v_add_f32 v50, 1.0, v50\n"
    "v_mfma_f32_16x16x32_bf16 v[52:55], a[200:203], v[24:27], v[52:55]\n"
    "buffer_load_dwordx4 v1, s[36:39], s76 offen sc1 lds\n"
    "v_rcp_f32 v49, v49\n"
    "v_mfma_f32_16x16x32_bf16 v[64:67], a[204:207], v[24:27], v[64:67]\n"
    "s_add_u32 m0, s59, 4096\n"
    "s_add_u32 s76, s76, 16384\n"
    "v_rcp_f32 v50, v50\n"
    "v_mfma_f32_16x16x32_bf16 v[68:71], a[208:211], v[24:27], v[68:71]\n"
    "s_waitcnt lgkmcnt(1)\n"
    "buffer_load_dwordx4 v1, s[36:39], s76 offen sc1 lds\n"
    "v_add_f32 v51, 1.0, v51\n"
    "v_mul_f32 v48, v48, v72\n"
    "v_mfma_f32_16x16x32_bf16 v[76:79], a[212:215], v[24:27], v[76:79]\n"
    "ds_read_b128 v[24:27], v9 offset:11264\n"
    "v_mul_f32 v49, v49, v73\n"
    "v_mfma_f32_16x16x32_bf16 v[40:43], a[216:219], v[16:19], v[40:43]\n"
    "v_rcp_f32 v51, v51\n"
    "v_mfma_f32_16x16x32_bf16 v[44:47], a[220:223], v[16:19], v[44:47]\n"
    "s_add_u32 m0, s59, 8192\n"
    "s_add_u32 s76, s76, 16384\n"
    "v_mul_f32 v50, v50, v74\n"
    "v_mul_f32 v51, v51, v75\n"
    "v_mfma_f32_16x16x32_bf16 v[52:55], a[224:227], v[16:19], v[52:55]\n"
    "buffer_load_dwordx4 v1, s[36:39], s76 offen sc1 lds\n"
    "v_cvt_pk_bf16_f32 v48, v48, v49\n"
    "v_cvt_pk_bf16_f32 v49, v50, v51\n"
    "v_mfma_f32_16x16x32_bf16 v[64:67], a[228:231], v[16:19], v[64:67]\n"
    "s_nop 0\n"
    "v_mfma_f32_16x16x32_bf16 v[68:71], a[232:235], v[16:19], v[68:71]\n"
    "s_waitcnt lgkmcnt(1)\n"
    "v_permlane16_swap_b32 v48, v50\n"
    "v_mfma_f32_16x16x32_bf16 v[76:79], a[236:239], v[16:19], v[76:79]\n"
    "ds_read_b128 v[16:19], v9 offset:12288\n"
    "v_mfma_f32_16x16x32_bf16 v[40:43], a[240:243], v[20:23], v[40:43]\n"
    "v_permlane16_swap_b32 v49, v51\n"
    "v_mfma_f32_16x16x32_bf16 v[44:47], a[244:247], v[20:23], v[44:47]\n"
    "s_add_u32 m0, s59, 12288\n"
    "s_add_u32 s76, s76, 16384\n"
    "buffer_store_dwordx4 v[48:51], v4, s[36:39], s73 offen sc1\n"
    "v_mfma_f32_16x16x32_bf16 v[52:55], a[248:251], v[20:23], v[52:55]\n"
    "buffer_load_dwordx4 v1, s[36:39], s76 offen sc1 lds\n"
    "s_lshr_b32 s73, s68, 1\n"
    "v_mfma_f32_16x16x32_bf16 v[64:67], a[252:255], v[20:23], v[64:67]\n"
    "s_add_u32 m0, s59, 16384\n"
    "s_add_u32 s76, s76, 16384\n"
    "v_mfma_f32_16x16x32_bf16 v[68:71], v[80:83], v[20:23], v[68:71]\n"
    "s_waitcnt lgkmcnt(1)\n"
    "buffer_load_dwordx4 v8, s[36:39], s76 offen sc1 lds\n"
    "v_mfma_f32_16x16x32_bf16 v[76:79], v[84:87], v[20:23], v[76:79]\n"
    "ds_read_b128 v[20:23], v9 offset:13312\n"
    "v_mfma_f32_16x16x32_bf16 v[40:43], v[88:91], v[24:27], v[40:43]\n"
    "s_and_b32 s73, s73, s57\n"
    "v_mfma_f32_16x16x32_bf16 v[44:47], v[92:95], v[24:27], v[44:47]\n"
    "v_mfma_f32_16x16x32_bf16 v[52:55], v[96:99], v[24:27], v[52:55]\n"
    "s_mul_i32 s73, s73, s56\n"
    "v_mfma_f32_16x16x32_bf16 v[64:67], v[100:103], v[24:27], v[64:67]\n"
    "v_mfma_f32_16x16x32_bf16 v[68:71], v[104:107], v[24:27], v[68:71]\n"
    "s_waitcnt lgkmcnt(1)\n"
    "v_mfma_f32_16x16x32_bf16 v[76:79], v[108:111], v[24:27], v[76:79]\n"
    "ds_read_b128 v[24:27], v9 offset:14336\n"
    "s_branch .LgUP_join1_%=\n"
    ".LgUP_dma1_69_%=:\n"
    "v_mfma_f32_16x16x32_bf16 v[64:67], v[100:103], v[24:27], v[64:67]\n"
    "s_add_u32 m0, s59, 0\n"
    "v_mfma_f32_16x16x32_bf16 v[68:71], v[104:107], v[24:27], v[68:71]\n"
    "s_waitcnt lgkmcnt(1)\n"
    "buffer_load_dwordx4 v1, s[36:39], s76 offen sc1 lds\n"
    "v_mfma_f32_16x16x32_bf16 v[76:79], v[108:111], v[24:27], v[76:79]\n"
    "ds_read_b128 v[24:27], v9 offset:14336\n"
    "v_mfma_f32_16x16x32_bf16 v[40:43], v[112:115], v[16:19], v[40:43]\n"
    "s_add_u32 s73, s73, s55\n"
    "v_mfma_f32_16x16x32_bf16 v[44:47], v[116:119], v[16:19], v[44:47]\n"
    "s_add_u32 m0, s59, 4096\n"
    "s_add_u32 s76, s76, 16384\n"
    "v_mfma_f32_16x16x32_bf16 v[52:55], v[120:123], v[16:19], v[52:55]\n"
    "buffer_load_dwordx4 v1, s[36:39], s76 offen sc1 lds\n"
    "v_mfma_f32_16x16x32_bf16 v[64:67], v[124:127], v[16:19], v[64:67]\n"
    "v_mfma_f32_16x16x32_bf16 v[68:71], v[128:131], v[16:19], v[68:71]\n"
    "s_waitcnt lgkmcnt(1)\n"
    "v_mfma_f32_16x16x32_bf16 v[76:79], v[132:135], v[16:19], v[76:79]\n"
    "ds_read_b128 v[16:19], v9 offset:15360\n"
    "v_mfma_f32_16x16x32_bf16 v[40:43], v[136:139], v[20:23], v[40:43]\n"
    "s_add_u32 m0, s59, 8192\n"
    "s_add_u32 s76, s76, 16384\n"
    "v_mfma_f32_16x16x32_bf16 v[44:47], v[140:143], v[20:23], v[44:47]\n"
    "buffer_load_dwordx4 v1, s[36:39], s76 offen sc1 lds\n"
    "v_mfma_f32_16x16x32_bf16 v[52:55], v[144:147], v[20:23], v[52:55]\n"
    "s_add_u32 m0, s59, 12288\n"
    "s_add_u32 s76, s76, 16384\n"
    "v_mfma_f32_16x16x32_bf16 v[64:67], v[148:151], v[20:23], v[64:67]\n"
    "buffer_load_dwordx4 v1, s[36:39], s76 offen sc1 lds\n"
    "v_mfma_f32_16x16x32_bf16 v[68:71], v[152:155], v[20:23], v[68:71]\n"
    "s_waitcnt lgkmcnt(1)\n"
    "v_mfma_f32_16x16x32_bf16 v[76:79], v[156:159], v[20:23], v[76:79]\n"
    "ds_read_b128 v[20:23], v9 offset:16384\n"
    "v_mfma_f32_16x16x32_bf16 v[40:43], v[160:163], v[24:27], v[40:43]\n"
    "v_mfma_f32_16x16x32_bf16 v[44:47], v[164:167], v[24:27], v[44:47]\n"
    "s_add_u32 m0, s59, 16384\n"
    "s_add_u32 s76, s76, 16384\n"
    "v_mfma_f32_16x16x32_bf16 v[52:55], v[168:171], v[24:27], v[52:55]\n"
    "buffer_load_dwordx4 v8, s[36:39], s76 offen sc1 lds\n"
    "v_mfma_f32_16x16x32_bf16 v[64:67], v[172:175], v[24:27], v[64:67]\n"
    "v_mfma_f32_16x16x32_bf16 v[68:71], v[176:179], v[24:27], v[68:71]\n"
    "s_waitcnt lgkmcnt(1)\n"
    "v_mfma_f32_16x16x32_bf16 v[76:79], v[180:183], v[24:27], v[76:79]\n"
    "ds_read_b128 v[24:27], v9 offset:17408\n"
    "v_mfma_f32_16x16x32_bf16 v[40:43], v[184:187], v[16:19], v[40:43]\n"
    "v_mfma_f32_16x16x32_bf16 v[44:47], v[188:191], v[16:19], v[44:47]\n"
    "v_mfma_f32_16x16x32_bf16 v[52:55], v[192:195], v[16:19], v[52:55]\n"
    "v_mfma_f32_16x16x32_bf16 v[64:67], v[196:199], v[16:19], v[64:67]\n"
    "v_mfma_f32_16x16x32_bf16 v[68:71], v[200:203], v[16:19], v[68:71]\n"
    "s_waitcnt lgkmcnt(1)\n"
    "v_mfma_f32_16x16x32_bf16 v[76:79], v[204:207], v[16:19], v[76:79]\n"
    "v_mfma_f32_16x16x32_bf16 v[40:43], v[208:211], v[20:23], v[40:43]\n"
    "s_waitcnt vmcnt(5)\n"
    "buffer_atomic_smax v12, v14, s[36:39], s70 offen sc1\n"
    "v_mfma_f32_16x16x32_bf16 v[44:47], v[212:215], v[20:23], v[44:47]\n"
    "v_mfma_f32_16x16x32_bf16 v[52:55], v[216:219], v[20:23], v[52:55]\n"
    "v_mfma_f32_16x16x32_bf16 v[64:67], v[220:223], v[20:23], v[64:67]\n"
    "v_mfma_f32_16x16x32_bf16 v[68:71], v[224:227], v[20:23], v[68:71]\n"
    "s_waitcnt lgkmcnt(0)\n"
    "v_mfma_f32_16x16x32_bf16 v[76:79], v[228:231], v[20:23], v[76:79]\n"
    "v_mfma_f32_16x16x32_bf16 v[40:43], v[232:235], v[24:27], v[40:43]\n"
    "v_mfma_f32_16x16x32_bf16 v[44:47], v[236:239], v[24:27], v[44:47]\n"
    "v_mfma_f32_16x16x32_bf16 v[52:55], v[240:243], v[24:27], v[52:55]\n"
    "v_mfma_f32_16x16x32_bf16 v[64:67], v[244:247], v[24:27], v[64:67]\n"
    "v_mfma_f32_16x16x32_bf16 v[68:71], v[248:251], v[24:27], v[68:71]\n"
    "v_mfma_f32_16x16x32_bf16 v[76:79], v[252:255], v[24:27], v[76:79]\n"
    "v_pk_mul_f32 v[64:65], v[40:41], v[64:65]\n"
    "v_pk_mul_f32 v[66:67], v[42:43], v[66:67]\n"
    "s_nop 1\n"
    "v_pk_mul_f32 v[68:69], v[44:45], v[68:69]\n"
    "v_pk_mul_f32 v[70:71], v[46:47], v[70:71]\n"
    "v_pk_mul_f32 v[40:41], v[40:41], s[64:65] op_sel_hi:[1,0]\n"
    "v_pk_mul_f32 v[42:43], v[42:43], s[64:65] op_sel_hi:[1,0]\n"
    "v_pk_mul_f32 v[44:45], v[44:45], s[64:65] op_sel_hi:[1,0]\n"
    "v_pk_mul_f32 v[46:47], v[46:47], s[64:65] op_sel_hi:[1,0]\n"
    "v_exp_f32 v40, v40\n"
    "v_exp_f32 v41, v41\n"
    "v_exp_f32 v42, v42\n"
    "v_exp_f32 v43, v43\n"
    "v_exp_f32 v44, v44\n"
    "v_exp_f32 v45, v45\n"
    "v_exp_f32 v46, v46\n"
    "v_exp_f32 v47, v47\n"
    "v_pk_add_f32 v[40:41], v[40:41], 1.0 op_sel_hi:[1,0]\n"
    "v_pk_add_f32 v[42:43], v[42:43], 1.0 op_sel_hi:[1,0]\n"
    "v_pk_add_f32 v[44:45], v[44:45], 1.0 op_sel_hi:[1,0]\n"
    "v_pk_add_f32 v[46:47], v[46:47], 1.0 op_sel_hi:[1,0]\n"
    "v_rcp_f32 v40, v40\n"
    "v_rcp_f32 v41, v41\n"
    "v_rcp_f32 v42, v42\n"
    "v_rcp_f32 v43, v43\n"
    "v_rcp_f32 v44, v44\n"
    "v_rcp_f32 v45, v45\n"
    "v_rcp_f32 v46, v46\n"
    "v_rcp_f32 v47, v47\n"
    "v_pk_mul_f32 v[40:41], v[40:41], v[64:65]\n"
    "v_pk_mul_f32 v[42:43], v[42:43], v[66:67]\n"
    "v_pk_mul_f32 v[44:45], v[44:45], v[68:69]\n"
    "v_pk_mul_f32 v[46:47], v[46:47], v[70:71]\n"
    "v_cvt_pk_bf16_f32 v40, v40, v41\n"
    "v_cvt_pk_bf16_f32 v41, v42, v43\n"
    "v_cvt_pk_bf16_f32 v42, v44, v45\n"
    "v_cvt_pk_bf16_f32 v43, v46, v47\n"
    "buffer_store_dwordx4 v[40:43], v15, s[36:39], s73 offen offset:1024 sc1\n"
    "v_pk_mul_f32 v[76:77], v[52:53], v[76:77]\n"
    "v_pk_mul_f32 v[78:79], v[54:55], v[78:79]\n"
    "v_pk_mul_f32 v[52:53], v[52:53], s[64:65] op_sel_hi:[1,0]\n"
    "v_pk_mul_f32 v[54:55], v[54:55], s[64:65] op_sel_hi:[1,0]\n"
    "v_exp_f32 v52, v52\n"
    "v_exp_f32 v53, v53\n"
    "v_exp_f32 v54, v54\n"
    "v_exp_f32 v55, v55\n"
    "v_pk_add_f32 v[52:53], v[52:53], 1.0 op_sel_hi:[1,0]\n"
    "v_pk_add_f32 v[54:55], v[54:55], 1.0 op_sel_hi:[1,0]\n"
    "v_rcp_f32 v52, v52\n"
    "v_rcp_f32 v53, v53\n"
    "v_rcp_f32 v54, v54\n"
    "v_rcp_f32 v55, v55\n"
    "v_pk_mul_f32 v[52:53], v[52:53], v[76:77]\n"
    "v_pk_mul_f32 v[54:55], v[54:55], v[78:79]\n"
    "v_cvt_pk_bf16_f32 v52, v52, v53\n"
    "v_cvt_pk_bf16_f32 v53, v54, v55\n"
    "s_nop 0\n"
    "v_permlane16_swap_b32 v52, v54\n"
    "v_permlane16_swap_b32 v53, v55\n"
    "buffer_store_dwordx4 v[52:55], v4, s[36:39], s73 offen offset:1024 sc1\n"
    "v_mov_b32 v12, s68\n"
    "s_mov_b32 s73, 0x80000000\n"
    "s_mov_b32 s68, s71\n"
    "s_mov_b32 s69, 0\n"
    "s_mov_b32 s70, 0x80000000\n"
    "s_waitcnt vmcnt(9)\n"
    "s_add_u32 s71, s68, 1\n"
    "v_add_u32 v13, v11, v3\n"
    "v_cmp_gt_i32 vcc, s71, v13\n"
    "s_nop 0\n"
    "s_or_b64 exec, vcc, s[62:63]\n"
    "buffer_atomic_smax v11, v2, s[36:39], 0 offen sc0 sc1\n"
    "s_mov_b64 exec, -1\n"
    "buffer_atomic_smax v12, v14, s[36:39], s73 offen sc1\n"
    "s_waitcnt vmcnt(5)\n"
    "s_branch .LgUP_bar0_%=\n"
    ".LgUP_dma1_89_%=:\n"
    "v_mfma_f32_16x16x32_bf16 v[76:79], v[180:183], v[24:27], v[76:79]\n"
    "ds_read_b128 v[24:27], v9 offset:17408\n"
    "v_mfma_f32_16x16x32_bf16 v[40:43], v[184:187], v[16:19], v[40:43]\n"
    "s_add_u32 m0, s59, 0\n"
    "v_mfma_f32_16x16x32_bf16 v[44:47], v[188:191], v[16:19], v[44:47]\n"
    "buffer_load_dwordx4 v1, s[36:39], s76 offen sc1 lds\n"
    "v_mfma_f32_16x16x32_bf16 v[52:55], v[192:195], v[16:19], v[52:55]\n"
    "s_add_u32 m0, s59, 4096\n"
    "s_add_u32 s76, s76, 16384\n"
    "v_mfma_f32_16x16x32_bf16 v[64:67], v[196:199], v[16:19], v[64:67]\n"
    "buffer_load_dwordx4 v1, s[36:39], s76 offen sc1 lds\n"
    "v_mfma_f32_16x16x32_bf16 v[68:71], v[200:203], v[16:19], v[68:71]\n"
    "s_waitcnt lgkmcnt(1)\n"
    "v_mfma_f32_16x16x32_bf16 v[76:79], v[204:207], v[16:19], v[76:79]\n"
    "v_mfma_f32_16x16x32_bf16 v[40:43], v[208:211], v[20:23], v[40:43]\n"
    "s_add_u32 m0, s59, 8192\n"
    "s_add_u32 s76, s76, 16384\n"
    "s_waitcnt vmcnt(3)\n"
    "buffer_atomic_smax v12, v14, s[36:39], s70 offen sc1\n"
    "v_mfma_f32_16x16x32_bf16 v[44:47], v[212:215], v[20:23], v[44:47]\n"
    "buffer_load_dwordx4 v1, s[36:39], s76 offen sc1 lds\n"
    "v_mfma_f32_16x16x32_bf16 v[52:55], v[216:219], v[20:23], v[52:55]\n"
    "v_mfma_f32_16x16x32_bf16 v[64:67], v[220:223], v[20:23], v[64:67]\n"
    "s_add_u32 m0, s59, 12288\n"
    "s_add_u32 s76, s76, 16384\n"
    "v_mfma_f32_16x16x32_bf16 v[68:71], v[224:227], v[20:23], v[68:71]\n"
    "s_waitcnt lgkmcnt(0)\n"
    "buffer_load_dwordx4 v1, s[36:39], s76 offen sc1 lds\n"
    "v_mfma_f32_16x16x32_bf16 v[76:79], v[228:231], v[20:23], v[76:79]\n"
    "v_mfma_f32_16x16x32_bf16 v[40:43], v[232:235], v[24:27], v[40:43]\n"
    "v_mfma_f32_16x16x32_bf16 v[44:47], v[236:239], v[24:27], v[44:47]\n"
    "s_add_u32 m0, s59, 16384\n"
    "s_add_u32 s76, s76, 16384\n"
    "v_mfma_f32_16x16x32_bf16 v[52:55], v[240:243], v[24:27], v[52:55]\n"
    "buffer_load_dwordx4 v8, s[36:39], s76 offen sc1 lds\n"
    "v_mfma_f32_16x16x32_bf16 v[64:67], v[244:247], v[24:27], v[64:67]\n"
    "v_mfma_f32_16x16x32_bf16 v[68:71], v[248:251], v[24:27], v[68:71]\n"
    "v_mfma_f32_16x16x32_bf16 v[76:79], v[252:255], v[24:27], v[76:79]\n"
    "v_pk_mul_f32 v[64:65], v[40:41], v[64:65]\n"
    "v_pk_mul_f32 v[66:67], v[42:43], v[66:67]\n"
    "s_nop 1\n"
    "v_pk_mul_f32 v[68:69], v[44:45], v[68:69]\n"
    "v_pk_mul_f32 v[70:71], v[46:47], v[70:71]\n"
    "v_pk_mul_f32 v[40:41], v[40:41], s[64:65] op_sel_hi:[1,0]\n"
    "v_pk_mul_f32 v[42:43], v[42:43], s[64:65] op_sel_hi:[1,0]\n"
    "v_pk_mul_f32 v[44:45], v[44:45], s[64:65] op_sel_hi:[1,0]\n"
    "v_pk_mul_f32 v[46:47], v[46:47], s[64:65] op_sel_hi:[1,0]\n"
    "v_exp_f32 v40, v40\n"
    "v_exp_f32 v41, v41\n"
    "v_exp_f32 v42, v42\n"
    "v_exp_f32 v43, v43\n"
    "v_exp_f32 v44, v44\n"
    "v_exp_f32 v45, v45\n"
    "v_exp_f32 v46, v46\n"
    "v_exp_f32 v47, v47\n"
    "v_pk_add_f32 v[40:41], v[40:41], 1.0 op_sel_hi:[1,0]\n"
    "v_pk_add_f32 v[42:43], v[42:43], 1.0 op_sel_hi:[1,0]\n"
    "v_pk_add_f32 v[44:45], v[44:45], 1.0 op_sel_hi:[1,0]\n"
    "v_pk_add_f32 v[46:47], v[46:47], 1.0 op_sel_hi:[1,0]\n"
    "v_rcp_f32 v40, v40\n"
    "v_rcp_f32 v41, v41\n"
    "v_rcp_f32 v42, v42\n"
    "v_rcp_f32 v43, v43\n"
    "v_rcp_f32 v44, v44\n"
    "v_rcp_f32 v45, v45\n"
    "v_rcp_f32 v46, v46\n"
    "v_rcp_f32 v47, v47\n"
    "v_pk_mul_f32 v[40:41], v[40:41], v[64:65]\n"
    "v_pk_mul_f32 v[42:43], v[42:43], v[66:67]\n"
    "v_pk_mul_f32 v[44:45], v[44:45], v[68:69]\n"
    "v_pk_mul_f32 v[46:47], v[46:47], v[70:71]\n"
    "v_cvt_pk_bf16_f32 v40, v40, v41\n"
    "v_cvt_pk_bf16_f32 v41, v42, v43\n"
    "v_cvt_pk_bf16_f32 v42, v44, v45\n"
    "v_cvt_pk_bf16_f32 v43, v46, v47\n"
    "buffer_store_dwordx4 v[40:43], v15, s[36:39], s73 offen offset:1024 sc1\n"
    "v_pk_mul_f32 v[76:77], v[52:53], v[76:77]\n"
    "v_pk_mul_f32 v[78:79], v[54:55], v[78:79]\n"
    "v_pk_mul_f32 v[52:53], v[52:53], s[64:65] op_sel_hi:[1,0]\n"
    "v_pk_mul_f32 v[54:55], v[54:55], s[64:65] op_sel_hi:[1,0]\n"
    "v_exp_f32 v52, v52\n"
    "v_exp_f32 v53, v53\n"
    "v_exp_f32 v54, v54\n"
    "v_exp_f32 v55, v55\n"
    "v_pk_add_f32 v[52:53], v[52:53], 1.0 op_sel_hi:[1,0]\n"
    "v_pk_add_f32 v[54:55], v[54:55], 1.0 op_sel_hi:[1,0]\n"
    "v_rcp_f32 v52, v52\n"
    "v_rcp_f32 v53, v53\n"
    "v_rcp_f32 v54, v54\n"
    "v_rcp_f32 v55, v55\n"
    "v_pk_mul_f32 v[52:53], v[52:53], v[76:77]\n"
    "v_pk_mul_f32 v[54:55], v[54:55], v[78:79]\n"
    "v_cvt_pk_bf16_f32 v52, v52, v53\n"
    "v_cvt_pk_bf16_f32 v53, v54, v55\n"
    "s_nop 0\n"
    "v_permlane16_swap_b32 v52, v54\n"
    "v_permlane16_swap_b32 v53, v55\n"
    "buffer_store_dwordx4 v[52:55], v4, s[36:39], s73 offen offset:1024 sc1\n"
    "v_mov_b32 v12, s68\n"
    "s_mov_b32 s73, 0x80000000\n"
    "s_mov_b32 s68, s71\n"
    "s_mov_b32 s69, 0\n"
    "s_mov_b32 s70, 0x80000000\n"
    "s_waitcnt vmcnt(8)\n"
    "s_add_u32 s71, s68, 1\n"
    "v_add_u32 v13, v11, v3\n"
    "v_cmp_gt_i32 vcc, s71, v13\n"
    "s_nop 0\n"
    "s_or_b64 exec, vcc, s[62:63]\n"
    "buffer_atomic_smax v11, v2, s[36:39], 0 offen sc0 sc1\n"
    "s_mov_b64 exec, -1\n"
    "buffer_atomic_smax v12, v14, s[36:39], s73 offen sc1\n"
    "s_waitcnt vmcnt(4)\n"
    "s_branch .LgUP_bar0_%=\n"
    ".LgUP_idle1_%=:\n"
    "s_or_b64 exec, vcc, s[62:63]\n"
    "buffer_atomic_smax v5, v2, s[36:39], 0 offen sc0 sc1\n"
    "s_mov_b64 exec, -1\n"
    "s_waitcnt vmcnt(1)\n"
    "buffer_atomic_smax v12, v10, s[36:39], 0 offen sc1\n"
    "s_branch .LgUP_block1_1_%=\n"
    ".LgUP_block1_%=:\n"
    "v_add_u32 v13, v11, v3\n"
    "v_cmp_gt_i32 vcc, s71, v13\n"
    "s_nop 0\n"
    "s_or_b64 exec, vcc, s[62:63]\n"
    "buffer_atomic_smax v11, v2, s[36:39], 0 offen sc0 sc1\n"
    "s_mov_b64 exec, -1\n"
    "s_sleep 3\n"
    "s_waitcnt vmcnt(1)\n"
    ".LgUP_block1_0_%=:\n"
    "v_add_u32 v13, v5, v3\n"
    "v_cmp_gt_i32 vcc, s71, v13\n"
    "s_cbranch_vccz .LgUP_go1_%=\n"
    "v_add_u32 v13, v5, v3\n"
    "v_cmp_gt_i32 vcc, s71, v13\n"
    "s_nop 0\n"
    "s_or_b64 exec, vcc, s[62:63]\n"
    "buffer_atomic_smax v5, v2, s[36:39], 0 offen sc0 sc1\n"
    "s_mov_b64 exec, -1\n"
    "s_sleep 3\n"
    "s_waitcnt vmcnt(1)\n"
    ".LgUP_block1_1_%=:\n"
    "v_add_u32 v13, v11, v3\n"
    "v_cmp_gt_i32 vcc, s71, v13\n"
    "s_cbranch_vccz .LgUP_go1_%=\n"
    "s_sub_u32 s72, s72, 1\n"
    "s_cbranch_scc0 .LgUP_block1_%=\n"
    "s_mov_b32 s72, 0\n"
    ".LgUP_go1_%=:\n"
    "s_lshr_b32 s76, s71, 1\n"
    "s_and_b32 s76, s76, s54\n"
    "s_mul_i32 s76, s76, s53\n"
    "s_add_u32 s76, s76, s52\n"
    "s_add_u32 m0, s59, 0\n"
    "s_nop 0\n"
    "buffer_load_dwordx4 v1, s[36:39], s76 offen sc1 lds\n"
    "s_add_u32 m0, s59, 4096\n"
    "s_add_u32 s76, s76, 16384\n"
    "buffer_load_dwordx4 v1, s[36:39], s76 offen sc1 lds\n"
    "s_add_u32 m0, s59, 8192\n"
    "s_add_u32 s76, s76, 16384\n"
    "buffer_load_dwordx4 v1, s[36:39], s76 offen sc1 lds\n"
    "s_add_u32 m0, s59, 12288\n"
    "s_add_u32 s76, s76, 16384\n"
    "buffer_load_dwordx4 v1, s[36:39], s76 offen sc1 lds\n"
    "s_add_u32 m0, s59, 16384\n"
    "s_add_u32 s76, s76, 16384\n"
    "buffer_load_dwordx4 v8, s[36:39], s76 offen sc1 lds\n"
    "s_mov_b32 s69, 0x80000000\n"
    "s_mov_b32 s70, 0x80000000\n"
    "s_waitcnt vmcnt(5)\n"
    "s_add_u32 s71, s68, 1\n"
    "v_add_u32 v13, v11, v3\n"
    "v_cmp_gt_i32 vcc, s71, v13\n"
    "s_nop 0\n"
    "s_or_b64 exec, vcc, s[62:63]\n"
    "buffer_atomic_smax v11, v2, s[36:39], 0 offen sc0 sc1\n"
    "s_mov_b64 exec, -1\n"
    "buffer_atomic_smax v12, v14, s[36:39], s73 offen sc1\n"
    "s_waitcnt vmcnt(2)\n"
    "s_branch .LgUP_bar0_%=\n"
    ".LgUP_bar1_%=:\n"
    "s_barrier\n"
    "ds_read_b128 v[16:19], v9 offset:0\n"
    "ds_read_b128 v[20:23], v9 offset:1024\n"
    "ds_read_b128 v[24:27], v9 offset:2048\n"
    "v_add_u32 v13, v5, v3\n"
    "v_cmp_gt_i32 vcc, s71, v13\n"
    "s_nop 0\n"
    "s_or_b64 exec, vcc, s[62:63]\n"
    "buffer_atomic_smax v5, v2, s[36:39], 0 offen sc0 sc1\n"
    "s_mov_b64 exec, -1\n"
    "s_lshr_b32 s76, s71, 1\n"
    "s_and_b32 s76, s76, s54\n"
    "s_mul_i32 s76, s76, s53\n"
    "s_add_u32 s76, s76, s52\n"
    "s_waitcnt lgkmcnt(2)\n"
    "s_branch .LgUP_head1_%=\n"
    ".LgUP_FIN_%=:\n"
  );
}
DEVI void role_wo(const Params& p, int L, int j, int i, u32 dbg, unsigned long long tag) {
  const int lane = threadIdx.x & 63, w = threadIdx.x >> 6, e0 = j * 1024 * 576 + i * 192;
  GEMM_DECL;
  g_w0 = make_srd(p.Wo + e0, 4096u * 576 * 2 - e0 * 2); g_w1 = g_w0; g_w2 = g_w0;
  g_ldw = 1152;
  g_in_base = pgb(L, B_O + 8 * j); g_in_stride = SLOT; g_in_mask = RO - 1;
  g_out_base = pgb(L, B_PA + 5 * j); g_out_kb = 12 * i; g_out_mask = RP - 1;
  g_flag = lane < 4 ? mbox(L, lane, SL_REDA, 12 * j + 4 * i + w) : ((lane < 12 && w == 0) ? mbox_att(L, j, lane - 4, 4 + i) : OOB);
  g_paddr = mbox(L, j, SL_WO + i, lane);
  g_pbias = lane < 8 ? 0 : (lane < 24 ? 2 * RP : 1000);
  v16u sa, sb, va;
  gemm_args(GEMM_ARGS, p, dbg, tag, sa, sb, va);
  RUN_ASM(
    "s_nop 4\n"
    "s_getpc_b64 s[84:85]\n"
    ".LgWO_PC_%=:\n"
    "s_and_b32 s85, s85, 0xffff\n"
    "s_mov_b32 s86, .LgWO_FIN_%=-.LgWO_PC_%=\n"
    "s_mov_b32 s87, 0x20000\n"
    "v_mbcnt_lo_u32_b32 v40, -1, 0\n"
    "v_mbcnt_hi_u32_b32 v40, -1, v40\n"
    "v_lshlrev_b32 v40, 7, v40\n"
    "buffer_load_dword v41, v40, s[84:87], 0 offen\n"
    "v_add_u32 v40, 8192, v40\n"
    "buffer_load_dword v41, v40, s[84:87], 0 offen\n"
    "v_add_u32 v40, 8192, v40\n"
    "buffer_load_dword v41, v40, s[84:87], 0 offen\n"
    "s_mov_b32 s68, 0\n"
    "s_mov_b32 s71, 0\n"
    "v_add_u32 v9, 32768, v0\n"
    "s_mov_b32 s64, 0xbfb8aa3b\n"
    "s_mov_b32 s72, 262144\n"
    "s_add_u32 m0, s59, 0\n"
    "s_mov_b32 s74, 0\n"
    "buffer_load_dwordx4 v5, s[40:43], s74 offen lds\n"
    "s_add_u32 m0, s59, 4096\n"
    "s_mov_b32 s74, 128\n"
    "buffer_load_dwordx4 v5, s[40:43], s74 offen lds\n"
    "s_add_u32 m0, s59, 8192\n"
    "s_mov_b32 s74, 256\n"
    "buffer_load_dwordx4 v5, s[40:43], s74 offen lds\n"
    "s_add_u32 m0, s59, 12288\n"
    "s_mov_b32 s74, 36864\n"
    "buffer_load_dwordx4 v5, s[40:43], s74 offen lds\n"
    "s_add_u32 m0, s59, 16384\n"
    "s_mov_b32 s74, 36992\n"
    "buffer_load_dwordx4 v5, s[40:43], s74 offen lds\n"
    "s_add_u32 m0, s59, 20480\n"
    "s_mov_b32 s74, 37120\n"
    "buffer_load_dwordx4 v5, s[40:43], s74 offen lds\n"
    "s_add_u32 m0, s59, 24576\n"
    "s_mov_b32 s74, 73728\n"
    "buffer_load_dwordx4 v5, s[40:43], s74 offen lds\n"
    "s_add_u32 m0, s59, 28672\n"
    "s_mov_b32 s74, 73856\n"
    "buffer_load_dwordx4 v5, s[40:43], s74 offen lds\n"
    "s_add_u32 m0, s59, 32768\n"
    "s_mov_b32 s74, 73984\n"
    "buffer_load_dwordx4 v5, s[40:43], s74 offen lds\n"
    "s_add_u32 m0, s59, 36864\n"
    "s_mov_b32 s74, 110592\n"
    "buffer_load_dwordx4 v5, s[40:43], s74 offen lds\n"
    "s_add_u32 m0, s59, 40960\n"
    "s_mov_b32 s74, 110720\n"
    "buffer_load_dwordx4 v5, s[40:43], s74 offen lds\n"
    "s_add_u32 m0, s59, 45056\n"
    "s_mov_b32 s74, 110848\n"
    "buffer_load_dwordx4 v5, s[40:43], s74 offen lds\n"
    "s_add_u32 m0, s59, 49152\n"
    "s_mov_b32 s74, 147456\n"
    "buffer_load_dwordx4 v5, s[40:43], s74 offen lds\n"
    "s_add_u32 m0, s59, 53248\n"
    "s_mov_b32 s74, 147584\n"
    "buffer_load_dwordx4 v5, s[40:43], s74 offen lds\n"
    "s_add_u32 m0, s59, 57344\n"
    "s_mov_b32 s74, 147712\n"
    "buffer_load_dwordx4 v5, s[40:43], s74 offen lds\n"
    "s_add_u32 m0, s59, 61440\n"
    "s_mov_b32 s74, 184320\n"
    "buffer_load_dwordx4 v5, s[40:43], s74 offen lds\n"
    "s_add_u32 m0, s59, 65536\n"
    "s_mov_b32 s74, 184448\n"
    "buffer_load_dwordx4 v5, s[40:43], s74 offen lds\n"
    "s_add_u32 m0, s59, 69632\n"
    "s_mov_b32 s74, 184576\n"
    "buffer_load_dwordx4 v5, s[40:43], s74 offen lds\n"
    "s_add_u32 m0, s59, 73728\n"
    "s_mov_b32 s74, 221184\n"
    "buffer_load_dwordx4 v5, s[40:43], s74 offen lds\n"
    "s_add_u32 m0, s59, 77824\n"
    "s_mov_b32 s74, 221312\n"
    "buffer_load_dwordx4 v5, s[40:43], s74 offen lds\n"
    "s_add_u32 m0, s59, 81920\n"
    "s_mov_b32 s74, 221440\n"
    "buffer_load_dwordx4 v5, s[40:43], s74 offen lds\n"
    "s_add_u32 m0, s59, 86016\n"
    "s_mov_b32 s74, 258048\n"
    "buffer_load_dwordx4 v5, s[40:43], s74 offen lds\n"
    "s_add_u32 m0, s59, 90112\n"
    "s_mov_b32 s74, 258176\n"
    "buffer_load_dwordx4 v5, s[40:43], s74 offen lds\n"
    "s_add_u32 m0, s59, 94208\n"
    "s_mov_b32 s74, 258304\n"
    "buffer_load_dwordx4 v5, s[40:43], s74 offen lds\n"
    "s_add_u32 m0, s59, 98304\n"
    "s_mov_b32 s74, 294912\n"
    "buffer_load_dwordx4 v5, s[40:43], s74 offen lds\n"
    "s_add_u32 m0, s59, 102400\n"
    "s_mov_b32 s74, 295040\n"
    "buffer_load_dwordx4 v5, s[40:43], s74 offen lds\n"
    "s_add_u32 m0, s59, 106496\n"
    "s_mov_b32 s74, 295168\n"
    "buffer_load_dwordx4 v5, s[40:43], s74 offen lds\n"
    "s_add_u32 m0, s59, 110592\n"
    "s_mov_b32 s74, 331776\n"
    "buffer_load_dwordx4 v5, s[40:43], s74 offen lds\n"
    "s_add_u32 m0, s59, 114688\n"
    "s_mov_b32 s74, 331904\n"
    "buffer_load_dwordx4 v5, s[40:43], s74 offen lds\n"
    "s_add_u32 m0, s59, 118784\n"
    "s_mov_b32 s74, 332032\n"
    "buffer_load_dwordx4 v5, s[40:43], s74 offen lds\n"
    "v_add_u32 v16, 49152, v6\n"
    "v_add_u32 v17, 98304, v6\n"
    "v_add_u32 v18, 49152, v7\n"
    "v_add_u32 v19, 98304, v7\n"
    "s_waitcnt vmcnt(24) lgkmcnt(0)\n"
    "s_barrier\n"
    "s_add_u32 m0, s59, 122880\n"
    "s_mov_b32 s74, 368640\n"
    "buffer_load_dwordx4 v5, s[40:43], s74 offen lds\n"
    "s_add_u32 m0, s59, 126976\n"
    "s_mov_b32 s74, 368768\n"
    "buffer_load_dwordx4 v5, s[40:43], s74 offen lds\n"
    "s_add_u32 m0, s59, 131072\n"
    "s_mov_b32 s74, 368896\n"
    "buffer_load_dwordx4 v5, s[40:43], s74 offen lds\n"
    "s_add_u32 m0, s59, 135168\n"
    "s_mov_b32 s74, 405504\n"
    "buffer_load_dwordx4 v5, s[40:43], s74 offen lds\n"
    "s_add_u32 m0, s59, 139264\n"
    "s_mov_b32 s74, 405632\n"
    "buffer_load_dwordx4 v5, s[40:43], s74 offen lds\n"
    "s_add_u32 m0, s59, 143360\n"
    "s_mov_b32 s74, 405760\n"
    "buffer_load_dwordx4 v5, s[40:43], s74 offen lds\n"
    "ds_read_b64_tr_b16 a[0:1], v6 offset:0\n"
    "ds_read_b64_tr_b16 a[2:3], v6 offset:512\n"
    "ds_read_b64_tr_b16 a[4:5], v6 offset:8\n"
    "ds_read_b64_tr_b16 a[6:7], v6 offset:520\n"
    "ds_read_b64_tr_b16 a[8:9], v7 offset:0\n"
    "ds_read_b64_tr_b16 a[10:11], v7 offset:512\n"
    "ds_read_b64_tr_b16 a[12:13], v6 offset:12288\n"
    "ds_read_b64_tr_b16 a[14:15], v6 offset:12800\n"
    "ds_read_b64_tr_b16 a[16:17], v6 offset:12296\n"
    "ds_read_b64_tr_b16 a[18:19], v6 offset:12808\n"
    "ds_read_b64_tr_b16 a[20:21], v7 offset:12288\n"
    "ds_read_b64_tr_b16 a[22:23], v7 offset:12800\n"
    "s_waitcnt vmcnt(24) lgkmcnt(0)\n"
    "s_barrier\n"
    "s_add_u32 m0, s59, 0\n"
    "s_mov_b32 s74, 442368\n"
    "buffer_load_dwordx4 v5, s[40:43], s74 offen lds\n"
    "s_add_u32 m0, s59, 4096\n"
    "s_mov_b32 s74, 442496\n"
    "buffer_load_dwordx4 v5, s[40:43], s74 offen lds\n"
    "s_add_u32 m0, s59, 8192\n"
    "s_mov_b32 s74, 442624\n"
    "buffer_load_dwordx4 v5, s[40:43], s74 offen lds\n"
    "s_add_u32 m0, s59, 12288\n"
    "s_mov_b32 s74, 479232\n"
    "buffer_load_dwordx4 v5, s[40:43], s74 offen lds\n"
    "s_add_u32 m0, s59, 16384\n"
    "s_mov_b32 s74, 479360\n"
    "buffer_load_dwordx4 v5, s[40:43], s74 offen lds\n"
    "s_add_u32 m0, s59, 20480\n"
    "s_mov_b32 s74, 479488\n"
    "buffer_load_dwordx4 v5, s[40:43], s74 offen lds\n"
    "ds_read_b64_tr_b16 a[24:25], v6 offset:24576\n"
    "ds_read_b64_tr_b16 a[26:27], v6 offset:25088\n"
    "ds_read_b64_tr_b16 a[28:29], v6 offset:24584\n"
    "ds_read_b64_tr_b16 a[30:31], v6 offset:25096\n"
    "ds_read_b64_tr_b16 a[32:33], v7 offset:24576\n"
    "ds_read_b64_tr_b16 a[34:35], v7 offset:25088\n"
    "ds_read_b64_tr_b16 a[36:37], v6 offset:36864\n"
    "ds_read_b64_tr_b16 a[38:39], v6 offset:37376\n"
    "ds_read_b64_tr_b16 a[40:41], v6 offset:36872\n"
    "ds_read_b64_tr_b16 a[42:43], v6 offset:37384\n"
    "ds_read_b64_tr_b16 a[44:45], v7 offset:36864\n"
    "ds_read_b64_tr_b16 a[46:47], v7 offset:37376\n"
    "s_waitcnt vmcnt(24) lgkmcnt(0)\n"
    "s_barrier\n"
    "s_add_u32 m0, s59, 24576\n"
    "s_mov_b32 s74, 516096\n"
    "buffer_load_dwordx4 v5, s[40:43], s74 offen lds\n"
    "s_add_u32 m0, s59, 28672\n"
    "s_mov_b32 s74, 516224\n"
    "buffer_load_dwordx4 v5, s[40:43], s74 offen lds\n"
    "s_add_u32 m0, s59, 32768\n"
    "s_mov_b32 s74, 516352\n"
    "buffer_load_dwordx4 v5, s[40:43], s74 offen lds\n"
    "s_add_u32 m0, s59, 36864\n"
    "s_mov_b32 s74, 552960\n"
    "buffer_load_dwordx4 v5, s[40:43], s74 offen lds\n"
    "s_add_u32 m0, s59, 40960\n"
    "s_mov_b32 s74, 553088\n"
    "buffer_load_dwordx4 v5, s[40:43], s74 offen lds\n"
    "s_add_u32 m0, s59, 45056\n"
    "s_mov_b32 s74, 553216\n"
    "buffer_load_dwordx4 v5, s[40:43], s74 offen lds\n"
    "ds_read_b64_tr_b16 a[48:49], v16 offset:0\n"
    "ds_read_b64_tr_b16 a[50:51], v16 offset:512\n"
    "ds_read_b64_tr_b16 a[52:53], v16 offset:8\n"
    "ds_read_b64_tr_b16 a[54:55], v16 offset:520\n"
    "ds_read_b64_tr_b16 a[56:57], v18 offset:0\n"
    "ds_read_b64_tr_b16 a[58:59], v18 offset:512\n"
    "ds_read_b64_tr_b16 a[60:61], v16 offset:12288\n"
    "ds_read_b64_tr_b16 a[62:63], v16 offset:12800\n"
    "ds_read_b64_tr_b16 a[64:65], v16 offset:12296\n"
    "ds_read_b64_tr_b16 a[66:67], v16 offset:12808\n"
    "ds_read_b64_tr_b16 a[68:69], v18 offset:12288\n"
    "ds_read_b64_tr_b16 a[70:71], v18 offset:12800\n"
    "s_waitcnt vmcnt(24) lgkmcnt(0)\n"
    "s_barrier\n"
    "s_add_u32 m0, s59, 49152\n"
    "s_mov_b32 s74, 589824\n"
    "buffer_load_dwordx4 v5, s[40:43], s74 offen lds\n"
    "s_add_u32 m0, s59, 53248\n"
    "s_mov_b32 s74, 589952\n"
    "buffer_load_dwordx4 v5, s[40:43], s74 offen lds\n"
    "s_add_u32 m0, s59, 57344\n"
    "s_mov_b32 s74, 590080\n"
    "buffer_load_dwordx4 v5, s[40:43], s74 offen lds\n"
    "s_add_u32 m0, s59, 61440\n"
    "s_mov_b32 s74, 626688\n"
    "buffer_load_dwordx4 v5, s[40:43], s74 offen lds\n"
    "s_add_u32 m0, s59, 65536\n"
    "s_mov_b32 s74, 626816\n"
    "buffer_load_dwordx4 v5, s[40:43], s74 offen lds\n"
    "s_add_u32 m0, s59, 69632\n"
    "s_mov_b32 s74, 626944\n"
    "buffer_load_dwordx4 v5, s[40:43], s74 offen lds\n"
    "ds_read_b64_tr_b16 a[72:73], v16 offset:24576\n"
    "ds_read_b64_tr_b16 a[74:75], v16 offset:25088\n"
    "ds_read_b64_tr_b16 a[76:77], v16 offset:24584\n"
    "ds_read_b64_tr_b16 a[78:79], v16 offset:25096\n"
    "ds_read_b64_tr_b16 a[80:81], v18 offset:24576\n"
    "ds_read_b64_tr_b16 a[82:83], v18 offset:25088\n"
    "ds_read_b64_tr_b16 a[84:85], v16 offset:36864\n"
    "ds_read_b64_tr_b16 a[86:87], v16 offset:37376\n"
    "ds_read_b64_tr_b16 a[88:89], v16 offset:36872\n"
    "ds_read_b64_tr_b16 a[90:91], v16 offset:37384\n"
    "ds_read_b64_tr_b16 a[92:93], v18 offset:36864\n"
    "ds_read_b64_tr_b16 a[94:95], v18 offset:37376\n"
    "s_waitcnt vmcnt(24) lgkmcnt(0)\n"
    "s_barrier\n"
    "s_add_u32 m0, s59, 73728\n"
    "s_mov_b32 s74, 663552\n"
    "buffer_load_dwordx4 v5, s[40:43], s74 offen lds\n"
    "s_add_u32 m0, s59, 77824\n"
    "s_mov_b32 s74, 663680\n"
    "buffer_load_dwordx4 v5, s[40:43], s74 offen lds\n"
    "s_add_u32 m0, s59, 81920\n"
    "s_mov_b32 s74, 663808\n"
    "buffer_load_dwordx4 v5, s[40:43], s74 offen lds\n"
    "s_add_u32 m0, s59, 86016\n"
    "s_mov_b32 s74, 700416\n"
    "buffer_load_dwordx4 v5, s[40:43], s74 offen lds\n"
    "s_add_u32 m0, s59, 90112\n"
    "s_mov_b32 s74, 700544\n"
    "buffer_load_dwordx4 v5, s[40:43], s74 offen lds\n"
    "s_add_u32 m0, s59, 94208\n"
    "s_mov_b32 s74, 700672\n"
    "buffer_load_dwordx4 v5, s[40:43], s74 offen lds\n"
    "ds_read_b64_tr_b16 a[96:97], v17 offset:0\n"
    "ds_read_b64_tr_b16 a[98:99], v17 offset:512\n"
    "ds_read_b64_tr_b16 a[100:101], v17 offset:8\n"
    "ds_read_b64_tr_b16 a[102:103], v17 offset:520\n"
    "ds_read_b64_tr_b16 a[104:105], v19 offset:0\n"
    "ds_read_b64_tr_b16 a[106:107], v19 offset:512\n"
    "ds_read_b64_tr_b16 a[108:109], v17 offset:12288\n"
    "ds_read_b64_tr_b16 a[110:111], v17 offset:12800\n"
    "ds_read_b64_tr_b16 a[112:113], v17 offset:12296\n"
    "ds_read_b64_tr_b16 a[114:115], v17 offset:12808\n"
    "ds_read_b64_tr_b16 a[116:117], v19 offset:12288\n"
    "ds_read_b64_tr_b16 a[118:119], v19 offset:12800\n"
    "s_waitcnt vmcnt(24) lgkmcnt(0)\n"
    "s_barrier\n"
    "s_add_u32 m0, s59, 98304\n"
    "s_mov_b32 s74, 737280\n"
    "buffer_load_dwordx4 v5, s[40:43], s74 offen lds\n"
    "s_add_u32 m0, s59, 102400\n"
    "s_mov_b32 s74, 737408\n"
    "buffer_load_dwordx4 v5, s[40:43], s74 offen lds\n"
    "s_add_u32 m0, s59, 106496\n"
    "s_mov_b32 s74, 737536\n"
    "buffer_load_dwordx4 v5, s[40:43], s74 offen lds\n"
    "s_add_u32 m0, s59, 110592\n"
    "s_mov_b32 s74, 774144\n"
    "buffer_load_dwordx4 v5, s[40:43], s74 offen lds\n"
    "s_add_u32 m0, s59, 114688\n"
    "s_mov_b32 s74, 774272\n"
    "buffer_load_dwordx4 v5, s[40:43], s74 offen lds\n"
    "s_add_u32 m0, s59, 118784\n"
    "s_mov_b32 s74, 774400\n"
    "buffer_load_dwordx4 v5, s[40:43], s74 offen lds\n"
    "ds_read_b64_tr_b16 a[120:121], v17 offset:24576\n"
    "ds_read_b64_tr_b16 a[122:123], v17 offset:25088\n"
    "ds_read_b64_tr_b16 a[124:125], v17 offset:24584\n"
    "ds_read_b64_tr_b16 a[126:127], v17 offset:25096\n"
    "ds_read_b64_tr_b16 a[128:129], v19 offset:24576\n"
    "ds_read_b64_tr_b16 a[130:131], v19 offset:25088\n"
    "ds_read_b64_tr_b16 a[132:133], v17 offset:36864\n"
    "ds_read_b64_tr_b16 a[134:135], v17 offset:37376\n"
    "ds_read_b64_tr_b16 a[136:137], v17 offset:36872\n"
    "ds_read_b64_tr_b16 a[138:139], v17 offset:37384\n"
    "ds_read_b64_tr_b16 a[140:141], v19 offset:36864\n"
    "ds_read_b64_tr_b16 a[142:143], v19 offset:37376\n"
    "s_waitcnt vmcnt(24) lgkmcnt(0)\n"
    "s_barrier\n"
    "s_add_u32 m0, s59, 122880\n"
    "s_mov_b32 s74, 811008\n"
    "buffer_load_dwordx4 v5, s[40:43], s74 offen lds\n"
    "s_add_u32 m0, s59, 126976\n"
    "s_mov_b32 s74, 811136\n"
    "buffer_load_dwordx4 v5, s[40:43], s74 offen lds\n"
    "s_add_u32 m0, s59, 131072\n"
    "s_mov_b32 s74, 811264\n"
    "buffer_load_dwordx4 v5, s[40:43], s74 offen lds\n"
    "s_add_u32 m0, s59, 135168\n"
    "s_mov_b32 s74, 847872\n"
    "buffer_load_dwordx4 v5, s[40:43], s74 offen lds\n"
    "s_add_u32 m0, s59, 139264\n"
    "s_mov_b32 s74, 848000\n"
    "buffer_load_dwordx4 v5, s[40:43], s74 offen lds\n"
    "s_add_u32 m0, s59, 143360\n"
    "s_mov_b32 s74, 848128\n"
    "buffer_load_dwordx4 v5, s[40:43], s74 offen lds\n"
    "ds_read_b64_tr_b16 a[144:145], v6 offset:0\n"
    "ds_read_b64_tr_b16 a[146:147], v6 offset:512\n"
    "ds_read_b64_tr_b16 a[148:149], v6 offset:8\n"
    "ds_read_b64_tr_b16 a[150:151], v6 offset:520\n"
    "ds_read_b64_tr_b16 a[152:153], v7 offset:0\n"
    "ds_read_b64_tr_b16 a[154:155], v7 offset:512\n"
    "ds_read_b64_tr_b16 a[156:157], v6 offset:12288\n"
    "ds_read_b64_tr_b16 a[158:159], v6 offset:12800\n"
    "ds_read_b64_tr_b16 a[160:161], v6 offset:12296\n"
    "ds_read_b64_tr_b16 a[162:163], v6 offset:12808\n"
    "ds_read_b64_tr_b16 a[164:165], v7 offset:12288\n"
    "ds_read_b64_tr_b16 a[166:167], v7 offset:12800\n"
    "s_waitcnt vmcnt(24) lgkmcnt(0)\n"
    "s_barrier\n"
    "s_add_u32 m0, s59, 0\n"
    "s_mov_b32 s74, 884736\n"
    "buffer_load_dwordx4 v5, s[40:43], s74 offen lds\n"
    "s_add_u32 m0, s59, 4096\n"
    "s_mov_b32 s74, 884864\n"
    "buffer_load_dwordx4 v5, s[40:43], s74 offen lds\n"
    "s_add_u32 m0, s59, 8192\n"
    "s_mov_b32 s74, 884992\n"
    "buffer_load_dwordx4 v5, s[40:43], s74 offen lds\n"
    "s_add_u32 m0, s59, 12288\n"
    "s_mov_b32 s74, 921600\n"
    "buffer_load_dwordx4 v5, s[40:43], s74 offen lds\n"
    "s_add_u32 m0, s59, 16384\n"
    "s_mov_b32 s74, 921728\n"
    "buffer_load_dwordx4 v5, s[40:43], s74 offen lds\n"
    "s_add_u32 m0, s59, 20480\n"
    "s_mov_b32 s74, 921856\n"
    "buffer_load_dwordx4 v5, s[40:43], s74 offen lds\n"
    "ds_read_b64_tr_b16 a[168:169], v6 offset:24576\n"
    "ds_read_b64_tr_b16 a[170:171], v6 offset:25088\n"
    "ds_read_b64_tr_b16 a[172:173], v6 offset:24584\n"
    "ds_read_b64_tr_b16 a[174:175], v6 offset:25096\n"
    "ds_read_b64_tr_b16 a[176:177], v7 offset:24576\n"
    "ds_read_b64_tr_b16 a[178:179], v7 offset:25088\n"
    "ds_read_b64_tr_b16 a[180:181], v6 offset:36864\n"
    "ds_read_b64_tr_b16 a[182:183], v6 offset:37376\n"
    "ds_read_b64_tr_b16 a[184:185], v6 offset:36872\n"
    "ds_read_b64_tr_b16 a[186:187], v6 offset:37384\n"
    "ds_read_b64_tr_b16 a[188:189], v7 offset:36864\n"
    "ds_read_b64_tr_b16 a[190:191], v7 offset:37376\n"
    "s_waitcnt vmcnt(24) lgkmcnt(0)\n"
    "s_barrier\n"
    "s_add_u32 m0, s59, 24576\n"
    "s_mov_b32 s74, 958464\n"
    "buffer_load_dwordx4 v5, s[40:43], s74 offen lds\n"
    "s_add_u32 m0, s59, 28672\n"
    "s_mov_b32 s74, 958592\n"
    "buffer_load_dwordx4 v5, s[40:43], s74 offen lds\n"
    "s_add_u32 m0, s59, 32768\n"
    "s_mov_b32 s74, 958720\n"
    "buffer_load_dwordx4 v5, s[40:43], s74 offen lds\n"
    "s_add_u32 m0, s59, 36864\n"
    "s_mov_b32 s74, 995328\n"
    "buffer_load_dwordx4 v5, s[40:43], s74 offen lds\n"
    "s_add_u32 m0, s59, 40960\n"
    "s_mov_b32 s74, 995456\n"
    "buffer_load_dwordx4 v5, s[40:43], s74 offen lds\n"
    "s_add_u32 m0, s59, 45056\n"
    "s_mov_b32 s74, 995584\n"
    "buffer_load_dwordx4 v5, s[40:43], s74 offen lds\n"
    "ds_read_b64_tr_b16 a[192:193], v16 offset:0\n"
    "ds_read_b64_tr_b16 a[194:195], v16 offset:512\n"
    "ds_read_b64_tr_b16 a[196:197], v16 offset:8\n"
    "ds_read_b64_tr_b16 a[198:199], v16 offset:520\n"
    "ds_read_b64_tr_b16 a[200:201], v18 offset:0\n"
    "ds_read_b64_tr_b16 a[202:203], v18 offset:512\n"
    "ds_read_b64_tr_b16 a[204:205], v16 offset:12288\n"
    "ds_read_b64_tr_b16 a[206:207], v16 offset:12800\n"
    "ds_read_b64_tr_b16 a[208:209], v16 offset:12296\n"
    "ds_read_b64_tr_b16 a[210:211], v16 offset:12808\n"
    "ds_read_b64_tr_b16 a[212:213], v18 offset:12288\n"
    "ds_read_b64_tr_b16 a[214:215], v18 offset:12800\n"
    "s_waitcnt vmcnt(24) lgkmcnt(0)\n"
    "s_barrier\n"
    "s_add_u32 m0, s59, 49152\n"
    "s_mov_b32 s74, 1032192\n"
    "buffer_load_dwordx4 v5, s[40:43], s74 offen lds\n"
    "s_add_u32 m0, s59, 53248\n"
    "s_mov_b32 s74, 1032320\n"
    "buffer_load_dwordx4 v5, s[40:43], s74 offen lds\n"
    "s_add_u32 m0, s59, 57344\n"
    "s_mov_b32 s74, 1032448\n"
    "buffer_load_dwordx4 v5, s[40:43], s74 offen lds\n"
    "s_add_u32 m0, s59, 61440\n"
    "s_mov_b32 s74, 1069056\n"
    "buffer_load_dwordx4 v5, s[40:43], s74 offen lds\n"
    "s_add_u32 m0, s59, 65536\n"
    "s_mov_b32 s74, 1069184\n"
    "buffer_load_dwordx4 v5, s[40:43], s74 offen lds\n"
    "s_add_u32 m0, s59, 69632\n"
    "s_mov_b32 s74, 1069312\n"
    "buffer_load_dwordx4 v5, s[40:43], s74 offen lds\n"
    "ds_read_b64_tr_b16 a[216:217], v16 offset:24576\n"
    "ds_read_b64_tr_b16 a[218:219], v16 offset:25088\n"
    "ds_read_b64_tr_b16 a[220:221], v16 offset:24584\n"
    "ds_read_b64_tr_b16 a[222:223], v16 offset:25096\n"
    "ds_read_b64_tr_b16 a[224:225], v18 offset:24576\n"
    "ds_read_b64_tr_b16 a[226:227], v18 offset:25088\n"
    "ds_read_b64_tr_b16 a[228:229], v16 offset:36864\n"
    "ds_read_b64_tr_b16 a[230:231], v16 offset:37376\n"
    "ds_read_b64_tr_b16 a[232:233], v16 offset:36872\n"
    "ds_read_b64_tr_b16 a[234:235], v16 offset:37384\n"
    "ds_read_b64_tr_b16 a[236:237], v18 offset:36864\n"
    "ds_read_b64_tr_b16 a[238:239], v18 offset:37376\n"
    "s_waitcnt vmcnt(24) lgkmcnt(0)\n"
    "s_barrier\n"
    "s_add_u32 m0, s59, 73728\n"
    "s_mov_b32 s74, 1105920\n"
    "buffer_load_dwordx4 v5, s[40:43], s74 offen lds\n"
    "s_add_u32 m0, s59, 77824\n"
    "s_mov_b32 s74, 1106048\n"
    "buffer_load_dwordx4 v5, s[40:43], s74 offen lds\n"
    "s_add_u32 m0, s59, 81920\n"
    "s_mov_b32 s74, 1106176\n"
    "buffer_load_dwordx4 v5, s[40:43], s74 offen lds\n"
    "s_add_u32 m0, s59, 86016\n"
    "s_mov_b32 s74, 1142784\n"
    "buffer_load_dwordx4 v5, s[40:43], s74 offen lds\n"
    "s_add_u32 m0, s59, 90112\n"
    "s_mov_b32 s74, 1142912\n"
    "buffer_load_dwordx4 v5, s[40:43], s74 offen lds\n"
    "s_add_u32 m0, s59, 94208\n"
    "s_mov_b32 s74, 1143040\n"
    "buffer_load_dwordx4 v5, s[40:43], s74 offen lds\n"
    "buffer_load_dwordx2 v[12:13], off, s[36:39], 0 sc1\n"
    "ds_read_b64_tr_b16 a[240:241], v17 offset:0\n"
    "ds_read_b64_tr_b16 a[242:243], v17 offset:512\n"
    "ds_read_b64_tr_b16 a[244:245], v17 offset:8\n"
    "ds_read_b64_tr_b16 a[246:247], v17 offset:520\n"
    "ds_read_b64_tr_b16 a[248:249], v19 offset:0\n"
    "ds_read_b64_tr_b16 a[250:251], v19 offset:512\n"
    "ds_read_b64_tr_b16 a[252:253], v17 offset:12288\n"
    "ds_read_b64_tr_b16 a[254:255], v17 offset:12800\n"
    "ds_read_b64_tr_b16 v[128:129], v17 offset:12296\n"
    "ds_read_b64_tr_b16 v[130:131], v17 offset:12808\n"
    "ds_read_b64_tr_b16 v[132:133], v19 offset:12288\n"
    "ds_read_b64_tr_b16 v[134:135], v19 offset:12800\n"
    "s_waitcnt vmcnt(25) lgkmcnt(0)\n"
    "s_barrier\n"
    "ds_read_b64_tr_b16 v[136:137], v17 offset:24576\n"
    "ds_read_b64_tr_b16 v[138:139], v17 offset:25088\n"
    "ds_read_b64_tr_b16 v[140:141], v17 offset:24584\n"
    "ds_read_b64_tr_b16 v[142:143], v17 offset:25096\n"
    "ds_read_b64_tr_b16 v[144:145], v19 offset:24576\n"
    "ds_read_b64_tr_b16 v[146:147], v19 offset:25088\n"
    "ds_read_b64_tr_b16 v[148:149], v17 offset:36864\n"
    "ds_read_b64_tr_b16 v[150:151], v17 offset:37376\n"
    "ds_read_b64_tr_b16 v[152:153], v17 offset:36872\n"
    "ds_read_b64_tr_b16 v[154:155], v17 offset:37384\n"
    "ds_read_b64_tr_b16 v[156:157], v19 offset:36864\n"
    "ds_read_b64_tr_b16 v[158:159], v19 offset:37376\n"
    "s_waitcnt vmcnt(19) lgkmcnt(0)\n"
    "s_barrier\n"
    "ds_read_b64_tr_b16 v[160:161], v6 offset:0\n"
    "ds_read_b64_tr_b16 v[162:163], v6 offset:512\n"
    "ds_read_b64_tr_b16 v[164:165], v6 offset:8\n"
    "ds_read_b64_tr_b16 v[166:167], v6 offset:520\n"
    "ds_read_b64_tr_b16 v[168:169], v7 offset:0\n"
    "ds_read_b64_tr_b16 v[170:171], v7 offset:512\n"
    "ds_read_b64_tr_b16 v[172:173], v6 offset:12288\n"
    "ds_read_b64_tr_b16 v[174:175], v6 offset:12800\n"
    "ds_read_b64_tr_b16 v[176:177], v6 offset:12296\n"
    "ds_read_b64_tr_b16 v[178:179], v6 offset:12808\n"
    "ds_read_b64_tr_b16 v[180:181], v7 offset:12288\n"
    "ds_read_b64_tr_b16 v[182:183], v7 offset:12800\n"
    "s_waitcnt vmcnt(13) lgkmcnt(0)\n"
    "s_barrier\n"
    "ds_read_b64_tr_b16 v[184:185], v6 offset:24576\n"
    "ds_read_b64_tr_b16 v[186:187], v6 offset:25088\n"
    "ds_read_b64_tr_b16 v[188:189], v6 offset:24584\n"
    "ds_read_b64_tr_b16 v[190:191], v6 offset:25096\n"
    "ds_read_b64_tr_b16 v[192:193], v7 offset:24576\n"
    "ds_read_b64_tr_b16 v[194:195], v7 offset:25088\n"
    "ds_read_b64_tr_b16 v[196:197], v6 offset:36864\n"
    "ds_read_b64_tr_b16 v[198:199], v6 offset:37376\n"
    "ds_read_b64_tr_b16 v[200:201], v6 offset:36872\n"
    "ds_read_b64_tr_b16 v[202:203], v6 offset:37384\n"
    "ds_read_b64_tr_b16 v[204:205], v7 offset:36864\n"
    "ds_read_b64_tr_b16 v[206:207], v7 offset:37376\n"
    "s_waitcnt vmcnt(7) lgkmcnt(0)\n"
    "s_barrier\n"
    "ds_read_b64_tr_b16 v[208:209], v16 offset:0\n"
    "ds_read_b64_tr_b16 v[210:211], v16 offset:512\n"
    "ds_read_b64_tr_b16 v[212:213], v16 offset:8\n"
    "ds_read_b64_tr_b16 v[214:215], v16 offset:520\n"
    "ds_read_b64_tr_b16 v[216:217], v18 offset:0\n"
    "ds_read_b64_tr_b16 v[218:219], v18 offset:512\n"
    "ds_read_b64_tr_b16 v[220:221], v16 offset:12288\n"
    "ds_read_b64_tr_b16 v[222:223], v16 offset:12800\n"
    "ds_read_b64_tr_b16 v[224:225], v16 offset:12296\n"
    "ds_read_b64_tr_b16 v[226:227], v16 offset:12808\n"
    "ds_read_b64_tr_b16 v[228:229], v18 offset:12288\n"
    "ds_read_b64_tr_b16 v[230:231], v18 offset:12800\n"
    "s_waitcnt vmcnt(0) lgkmcnt(0)\n"
    "s_barrier\n"
    ".LgWO_par_%=:\n"
    "v_readfirstlane_b32 s74, v12\n"
    "v_readfirstlane_b32 s75, v13\n"
    "s_xor_b32 s74, s74, s66\n"
    "s_xor_b32 s75, s75, s67\n"
    "s_cselect_b32 s75, 2, 0\n"
    "s_or_b32 s75, s75, s74\n"
    "s_cmp_lt_u32 s75, 2\n"
    "s_cbranch_scc1 .LgWO_par1_%=\n"
    "s_sleep 4\n"
    "buffer_load_dwordx2 v[12:13], off, s[36:39], 0 sc1\n"
    "s_waitcnt vmcnt(0)\n"
    "s_sub_u32 s72, s72, 1\n"
    "s_cbranch_scc0 .LgWO_par_%=\n"
    "s_mov_b32 s72, 0\n"
    ".LgWO_par1_%=:\n"
    "s_and_b32 s74, s74, 1\n"
    "s_lshl_b32 s74, s74, 13\n"
    "s_add_u32 s36, s36, s74\n"
    "s_addc_u32 s37, s37, 0\n"
    "v_add_u32 v13, v11, v3\n"
    "v_cmp_gt_i32 vcc, s71, v13\n"
    "s_nop 0\n"
    "s_or_b64 exec, vcc, s[62:63]\n"
    "buffer_atomic_smax v11, v2, s[36:39], 0 offen sc0 sc1\n"
    "s_mov_b64 exec, -1\n"
    "s_getpc_b64 s[84:85]\n"
    ".LgWO_PC2_%=:\n"
    "s_and_b32 s85, s85, 0xffff\n"
    "s_mov_b32 s86, .LgWO_FIN_%=-.LgWO_PC2_%=\n"
    "s_mov_b32 s87, 0x20000\n"
    "v_mbcnt_lo_u32_b32 v42, -1, 0\n"
    "v_mbcnt_hi_u32_b32 v42, -1, v42\n"
    "v_lshlrev_b32 v42, 7, v42\n"
    "buffer_load_dword v43, v42, s[84:87], 0 offen\n"
    "v_add_u32 v42, 8192, v42\n"
    "buffer_load_dword v43, v42, s[84:87], 0 offen\n"
    "v_add_u32 v42, 8192, v42\n"
    "buffer_load_dword v43, v42, s[84:87], 0 offen\n"
    "ds_read_b64_tr_b16 v[232:233], v16 offset:24576\n"
    "ds_read_b64_tr_b16 v[234:235], v16 offset:25088\n"
    "ds_read_b64_tr_b16 v[236:237], v16 offset:24584\n"
    "ds_read_b64_tr_b16 v[238:239], v16 offset:25096\n"
    "ds_read_b64_tr_b16 v[240:241], v18 offset:24576\n"
    "ds_read_b64_tr_b16 v[242:243], v18 offset:25088\n"
    "ds_read_b64_tr_b16 v[244:245], v16 offset:36864\n"
    "ds_read_b64_tr_b16 v[246:247], v16 offset:37376\n"
    "ds_read_b64_tr_b16 v[248:249], v16 offset:36872\n"
    "ds_read_b64_tr_b16 v[250:251], v16 offset:37384\n"
    "ds_read_b64_tr_b16 v[252:253], v18 offset:36864\n"
    "ds_read_b64_tr_b16 v[254:255], v18 offset:37376\n"
    "s_waitcnt lgkmcnt(0)\n"
    "s_barrier\n"
    "s_waitcnt vmcnt(0)\n"
    "v_mov_b32 v5, v11\n"
    "v_mov_b32 v12, -1\n"
    "s_mov_b32 s73, 0x80000000\n"
    "s_branch .LgWO_block1_1_%=\n"
    ".LgWO_LOOPB_%=:\n"
    ".LgWO_head0_%=:\n"
    "v_mfma_f32_16x16x32_bf16 v[48:51], a[0:3], v[16:19], 0\n"
    "v_mfma_f32_16x16x32_bf16 v[52:55], a[4:7], v[16:19], 0\n"
    "s_waitcnt lgkmcnt(2)\n"
    "v_cvt_pk_bf16_f32 v56, v56, v57\n"
    "v_cvt_pk_bf16_f32 v57, v58, v59\n"
    "v_mfma_f32_16x16x32_bf16 v[64:67], a[8:11], v[16:19], 0\n"
    "ds_read_b128 v[16:19], v0 offset:4096\n"
    "v_cvt_pk_bf16_f32 v58, v60, v61\n"
    "v_mfma_f32_16x16x32_bf16 v[48:51], a[12:15], v[20:23], v[48:51]\n"
    "v_cvt_pk_bf16_f32 v59, v62, v63\n"
    "v_mfma_f32_16x16x32_bf16 v[52:55], a[16:19], v[20:23], v[52:55]\n"
    "s_waitcnt lgkmcnt(2)\n"
    "s_waitcnt vmcnt(1)\n"
    "buffer_store_dwordx4 v[56:59], v15, s[36:39], s73 offen offset:1024 sc1\n"
    "v_cvt_pk_bf16_f32 v68, v68, v69\n"
    "v_add_u32 v13, v11, v3\n"
    "v_mfma_f32_16x16x32_bf16 v[64:67], a[20:23], v[20:23], v[64:67]\n"
    "ds_read_b128 v[20:23], v0 offset:5120\n"
    "v_cvt_pk_bf16_f32 v69, v70, v71\n"
    "v_cmp_gt_i32 vcc, s71, v13\n"
    "v_mfma_f32_16x16x32_bf16 v[48:51], a[24:27], v[24:27], v[48:51]\n"
    "s_nop 0\n"
    "v_mfma_f32_16x16x32_bf16 v[52:55], a[28:31], v[24:27], v[52:55]\n"
    "s_waitcnt lgkmcnt(2)\n"
    "v_permlane16_swap_b32 v68, v70\n"
    "v_mfma_f32_16x16x32_bf16 v[64:67], a[32:35], v[24:27], v[64:67]\n"
    "ds_read_b128 v[24:27], v0 offset:6144\n"
    "s_cbranch_vccnz .LgWO_poll0_%=\n"
    "v_mfma_f32_16x16x32_bf16 v[48:51], a[36:39], v[28:31], v[48:51]\n"
    "v_permlane16_swap_b32 v69, v71\n"
    "v_mfma_f32_16x16x32_bf16 v[52:55], a[40:43], v[28:31], v[52:55]\n"
    "s_waitcnt lgkmcnt(2)\n"
    "buffer_store_dwordx4 v[68:71], v4, s[36:39], s73 offen offset:1024 sc1\n"
    "v_mfma_f32_16x16x32_bf16 v[64:67], a[44:47], v[28:31], v[64:67]\n"
    "ds_read_b128 v[28:31], v0 offset:7168\n"
    "v_mfma_f32_16x16x32_bf16 v[48:51], a[48:51], v[16:19], v[48:51]\n"
    "s_add_u32 m0, s59, 32768\n"
    "s_lshr_b32 s73, s68, 1\n"
    "v_mfma_f32_16x16x32_bf16 v[52:55], a[52:55], v[16:19], v[52:55]\n"
    "s_waitcnt lgkmcnt(2)\n"
    "buffer_load_dwordx4 v1, s[36:39], s76 offen sc1 lds\n"
    "v_mfma_f32_16x16x32_bf16 v[64:67], a[56:59], v[16:19], v[64:67]\n"
    "ds_read_b128 v[16:19], v0 offset:8192\n"
    "v_mfma_f32_16x16x32_bf16 v[48:51], a[60:63], v[20:23], v[48:51]\n"
    "v_mfma_f32_16x16x32_bf16 v[52:55], a[64:67], v[20:23], v[52:55]\n"
    "s_waitcnt lgkmcnt(2)\n"
    "v_mfma_f32_16x16x32_bf16 v[64:67], a[68:71], v[20:23], v[64:67]\n"
    "ds_read_b128 v[20:23], v0 offset:9216\n"
    "v_mfma_f32_16x16x32_bf16 v[48:51], a[72:75], v[24:27], v[48:51]\n"
    "s_add_u32 m0, s59, 36864\n"
    "s_add_u32 s76, s76, 16384\n"
    "s_and_b32 s73, s73, s57\n"
    "v_mfma_f32_16x16x32_bf16 v[52:55], a[76:79], v[24:27], v[52:55]\n"
    "s_waitcnt lgkmcnt(2)\n"
    "buffer_load_dwordx4 v1, s[36:39], s76 offen sc1 lds\n"
    "v_mfma_f32_16x16x32_bf16 v[64:67], a[80:83], v[24:27], v[64:67]\n"
    "ds_read_b128 v[24:27], v0 offset:10240\n"
    "v_mfma_f32_16x16x32_bf16 v[48:51], a[84:87], v[28:31], v[48:51]\n"
    "v_mfma_f32_16x16x32_bf16 v[52:55], a[88:91], v[28:31], v[52:55]\n"
    "s_waitcnt lgkmcnt(2)\n"
    "v_mfma_f32_16x16x32_bf16 v[64:67], a[92:95], v[28:31], v[64:67]\n"
    "ds_read_b128 v[28:31], v0 offset:11264\n"
    "v_mfma_f32_16x16x32_bf16 v[48:51], a[96:99], v[16:19], v[48:51]\n"
    "s_add_u32 m0, s59, 40960\n"
    "s_add_u32 s76, s76, 16384\n"
    "s_mul_i32 s73, s73, s56\n"
    "v_mfma_f32_16x16x32_bf16 v[52:55], a[100:103], v[16:19], v[52:55]\n"
    "s_waitcnt lgkmcnt(2)\n"
    "buffer_load_dwordx4 v1, s[36:39], s76 offen sc1 lds\n"
    "v_mfma_f32_16x16x32_bf16 v[64:67], a[104:107], v[16:19], v[64:67]\n"
    "ds_read_b128 v[16:19], v0 offset:12288\n"
    "v_mfma_f32_16x16x32_bf16 v[48:51], a[108:111], v[20:23], v[48:51]\n"
    "v_mfma_f32_16x16x32_bf16 v[52:55], a[112:115], v[20:23], v[52:55]\n"
    "s_waitcnt lgkmcnt(2)\n"
    "v_mfma_f32_16x16x32_bf16 v[64:67], a[116:119], v[20:23], v[64:67]\n"
    "ds_read_b128 v[20:23], v0 offset:13312\n"
    "v_mfma_f32_16x16x32_bf16 v[48:51], a[120:123], v[24:27], v[48:51]\n"
    "s_add_u32 m0, s59, 45056\n"
    "s_add_u32 s76, s76, 16384\n"
    "s_add_u32 s73, s73, s55\n"
    "s_waitcnt vmcnt(7)\n"
    "buffer_atomic_smax v12, v10, s[36:39], s69 offen sc1\n"
    "v_mfma_f32_16x16x32_bf16 v[52:55], a[124:127], v[24:27], v[52:55]\n"
    "s_waitcnt lgkmcnt(2)\n"
    "buffer_load_dwordx4 v1, s[36:39], s76 offen sc1 lds\n"
    "v_mfma_f32_16x16x32_bf16 v[64:67], a[128:131], v[24:27], v[64:67]\n"
    "ds_read_b128 v[24:27], v0 offset:14336\n"
    "v_mfma_f32_16x16x32_bf16 v[48:51], a[132:135], v[28:31], v[48:51]\n"
    "v_mfma_f32_16x16x32_bf16 v[52:55], a[136:139], v[28:31], v[52:55]\n"
    "s_waitcnt lgkmcnt(2)\n"
    "v_mfma_f32_16x16x32_bf16 v[64:67], a[140:143], v[28:31], v[64:67]\n"
    "ds_read_b128 v[28:31], v0 offset:15360\n"
    "v_mfma_f32_16x16x32_bf16 v[48:51], a[144:147], v[16:19], v[48:51]\n"
    "s_add_u32 m0, s59, 49152\n"
    "s_add_u32 s76, s76, 16384\n"
    "v_mfma_f32_16x16x32_bf16 v[52:55], a[148:151], v[16:19], v[52:55]\n"
    "s_waitcnt lgkmcnt(2)\n"
    "buffer_load_dwordx4 v1, s[36:39], s76 offen sc1 lds\n"
    "v_mfma_f32_16x16x32_bf16 v[64:67], a[152:155], v[16:19], v[64:67]\n"
    "ds_read_b128 v[16:19], v0 offset:16384\n"
    "v_mfma_f32_16x16x32_bf16 v[48:51], a[156:159], v[20:23], v[48:51]\n"
    "v_mfma_f32_16x16x32_bf16 v[52:55], a[160:163], v[20:23], v[52:55]\n"
    "s_waitcnt lgkmcnt(2)\n"
    "v_mfma_f32_16x16x32_bf16 v[64:67], a[164:167], v[20:23], v[64:67]\n"
    "ds_read_b128 v[20:23], v0 offset:17408\n"
    "v_mfma_f32_16x16x32_bf16 v[48:51], a[168:171], v[24:27], v[48:51]\n"
    "s_add_u32 m0, s59, 53248\n"
    "s_add_u32 s76, s76, 16384\n"
    "v_mfma_f32_16x16x32_bf16 v[52:55], a[172:175], v[24:27], v[52:55]\n"
    "s_waitcnt lgkmcnt(2)\n"
    "buffer_load_dwordx4 v1, s[36:39], s76 offen sc1 lds\n"
    "v_mfma_f32_16x16x32_bf16 v[64:67], a[176:179], v[24:27], v[64:67]\n"
    "ds_read_b128 v[24:27], v0 offset:18432\n"
    "v_mfma_f32_16x16x32_bf16 v[48:51], a[180:183], v[28:31], v[48:51]\n"
    "v_mfma_f32_16x16x32_bf16 v[52:55], a[184:187], v[28:31], v[52:55]\n"
    "s_waitcnt lgkmcnt(2)\n"
    "v_mfma_f32_16x16x32_bf16 v[64:67], a[188:191], v[28:31], v[64:67]\n"
    "ds_read_b128 v[28:31], v0 offset:19456\n"
    "v_mfma_f32_16x16x32_bf16 v[48:51], a[192:195], v[16:19], v[48:51]\n"
    "s_add_u32 m0, s59, 57344\n"
    "s_add_u32 s76, s76, 16384\n"
    "v_mfma_f32_16x16x32_bf16 v[52:55], a[196:199], v[16:19], v[52:55]\n"
    "s_waitcnt lgkmcnt(2)\n"
    "buffer_load_dwordx4 v1, s[36:39], s76 offen sc1 lds\n"
    "v_mfma_f32_16x16x32_bf16 v[64:67], a[200:203], v[16:19], v[64:67]\n"
    "ds_read_b128 v[16:19], v0 offset:20480\n"
    "s_waitcnt vmcnt(8)\n"
    "buffer_atomic_smax v12, v10, s[36:39], s70 offen sc1\n"
    "v_mfma_f32_16x16x32_bf16 v[48:51], a[204:207], v[20:23], v[48:51]\n"
    "v_mfma_f32_16x16x32_bf16 v[52:55], a[208:211], v[20:23], v[52:55]\n"
    "s_waitcnt lgkmcnt(2)\n"
    "v_mfma_f32_16x16x32_bf16 v[64:67], a[212:215], v[20:23], v[64:67]\n"
    "ds_read_b128 v[20:23], v0 offset:21504\n"
    "v_mfma_f32_16x16x32_bf16 v[48:51], a[216:219], v[24:27], v[48:51]\n"
    "s_add_u32 m0, s59, 61440\n"
    "s_add_u32 s76, s76, 16384\n"
    "v_mfma_f32_16x16x32_bf16 v[52:55], a[220:223], v[24:27], v[52:55]\n"
    "s_waitcnt lgkmcnt(2)\n"
    "buffer_load_dwordx4 v1, s[36:39], s76 offen sc1 lds\n"
    "v_mfma_f32_16x16x32_bf16 v[64:67], a[224:227], v[24:27], v[64:67]\n"
    "ds_read_b128 v[24:27], v0 offset:22528\n"
    "v_mfma_f32_16x16x32_bf16 v[48:51], a[228:231], v[28:31], v[48:51]\n"
    "v_mfma_f32_16x16x32_bf16 v[52:55], a[232:235], v[28:31], v[52:55]\n"
    "s_waitcnt lgkmcnt(2)\n"
    "v_mfma_f32_16x16x32_bf16 v[64:67], a[236:239], v[28:31], v[64:67]\n"
    "ds_read_b128 v[28:31], v0 offset:23552\n"
    ".LgWO_join0_%=:\n"
    "v_mfma_f32_16x16x32_bf16 v[48:51], a[240:243], v[16:19], v[48:51]\n"
    "s_add_u32 s71, s68, 2\n"
    "s_waitcnt vmcnt(8)\n"
    "v_mfma_f32_16x16x32_bf16 v[52:55], a[244:247], v[16:19], v[52:55]\n"
    "s_waitcnt lgkmcnt(2)\n"
    "v_add_u32 v13, v11, v3\n"
    "v_mfma_f32_16x16x32_bf16 v[64:67], a[248:251], v[16:19], v[64:67]\n"
    "ds_read_b128 v[16:19], v0 offset:24576\n"
    "v_cmp_gt_i32 vcc, s71, v13\n"
    "v_mfma_f32_16x16x32_bf16 v[48:51], a[252:255], v[20:23], v[48:51]\n"
    "v_mfma_f32_16x16x32_bf16 v[52:55], v[128:131], v[20:23], v[52:55]\n"
    "s_waitcnt lgkmcnt(2)\n"
    "v_mfma_f32_16x16x32_bf16 v[64:67], v[132:135], v[20:23], v[64:67]\n"
    "ds_read_b128 v[20:23], v0 offset:25600\n"
    "s_or_b64 exec, vcc, s[62:63]\n"
    "buffer_atomic_smax v11, v2, s[36:39], 0 offen sc0 sc1\n"
    "s_mov_b64 exec, -1\n"
    "v_mfma_f32_16x16x32_bf16 v[48:51], v[136:139], v[24:27], v[48:51]\n"
    "v_mfma_f32_16x16x32_bf16 v[52:55], v[140:143], v[24:27], v[52:55]\n"
    "s_waitcnt lgkmcnt(2)\n"
    "s_lshr_b32 s76, s71, 1\n"
    "v_mfma_f32_16x16x32_bf16 v[64:67], v[144:147], v[24:27], v[64:67]\n"
    "ds_read_b128 v[24:27], v0 offset:26624\n"
    "s_and_b32 s76, s76, s54\n"
    "v_mfma_f32_16x16x32_bf16 v[48:51], v[148:151], v[28:31], v[48:51]\n"
    "s_mul_i32 s76, s76, s53\n"
    "v_mfma_f32_16x16x32_bf16 v[52:55], v[152:155], v[28:31], v[52:55]\n"
    "s_waitcnt lgkmcnt(2)\n"
    "s_add_u32 s76, s76, s52\n"
    "v_mfma_f32_16x16x32_bf16 v[64:67], v[156:159], v[28:31], v[64:67]\n"
    "ds_read_b128 v[28:31], v0 offset:27648\n"
    "v_mfma_f32_16x16x32_bf16 v[48:51], v[160:163], v[16:19], v[48:51]\n"
    "v_mfma_f32_16x16x32_bf16 v[52:55], v[164:167], v[16:19], v[52:55]\n"
    "s_waitcnt lgkmcnt(2)\n"
    "v_mfma_f32_16x16x32_bf16 v[64:67], v[168:171], v[16:19], v[64:67]\n"
    "ds_read_b128 v[16:19], v0 offset:28672\n"
    "v_mfma_f32_16x16x32_bf16 v[48:51], v[172:175], v[20:23], v[48:51]\n"
    "v_mfma_f32_16x16x32_bf16 v[52:55], v[176:179], v[20:23], v[52:55]\n"
    "s_waitcnt lgkmcnt(2)\n"
    "v_mfma_f32_16x16x32_bf16 v[64:67], v[180:183], v[20:23], v[64:67]\n"
    "ds_read_b128 v[20:23], v0 offset:29696\n"
    "v_mfma_f32_16x16x32_bf16 v[48:51], v[184:187], v[24:27], v[48:51]\n"
    "v_mfma_f32_16x16x32_bf16 v[52:55], v[188:191], v[24:27], v[52:55]\n"
    "s_waitcnt lgkmcnt(2)\n"
    "v_mfma_f32_16x16x32_bf16 v[64:67], v[192:195], v[24:27], v[64:67]\n"
    "ds_read_b128 v[24:27], v0 offset:30720\n"
    "v_mfma_f32_16x16x32_bf16 v[48:51], v[196:199], v[28:31], v[48:51]\n"
    "v_add_u32 v13, v5, v3\n"
    "v_mfma_f32_16x16x32_bf16 v[52:55], v[200:203], v[28:31], v[52:55]\n"
    "s_waitcnt lgkmcnt(2)\n"
    "v_cmp_gt_i32 vcc, s71, v13\n"
    "v_mfma_f32_16x16x32_bf16 v[64:67], v[204:207], v[28:31], v[64:67]\n"
    "ds_read_b128 v[28:31], v0 offset:31744\n"
    "v_mfma_f32_16x16x32_bf16 v[48:51], v[208:211], v[16:19], v[48:51]\n"
    "v_mfma_f32_16x16x32_bf16 v[52:55], v[212:215], v[16:19], v[52:55]\n"
    "s_waitcnt lgkmcnt(2)\n"
    "s_or_b64 exec, vcc, s[62:63]\n"
    "buffer_atomic_smax v5, v2, s[36:39], 0 offen sc0 sc1\n"
    "s_mov_b64 exec, -1\n"
    "v_mfma_f32_16x16x32_bf16 v[64:67], v[216:219], v[16:19], v[64:67]\n"
    "v_mfma_f32_16x16x32_bf16 v[48:51], v[220:223], v[20:23], v[48:51]\n"
    "s_waitcnt vmcnt(2) lgkmcnt(0)\n"
    "s_barrier\n"
    "ds_read_b128 v[16:19], v9 offset:0\n"
    "v_mfma_f32_16x16x32_bf16 v[52:55], v[224:227], v[20:23], v[52:55]\n"
    "v_mfma_f32_16x16x32_bf16 v[64:67], v[228:231], v[20:23], v[64:67]\n"
    "ds_read_b128 v[20:23], v9 offset:1024\n"
    "v_mfma_f32_16x16x32_bf16 v[48:51], v[232:235], v[24:27], v[48:51]\n"
    "v_mfma_f32_16x16x32_bf16 v[52:55], v[236:239], v[24:27], v[52:55]\n"
    "v_mfma_f32_16x16x32_bf16 v[64:67], v[240:243], v[24:27], v[64:67]\n"
    "ds_read_b128 v[24:27], v9 offset:2048\n"
    "s_mov_b32 s69, 0x80000000\n"
    "v_mfma_f32_16x16x32_bf16 v[48:51], v[244:247], v[28:31], v[48:51]\n"
    "s_mov_b32 s70, 0\n"
    "v_mfma_f32_16x16x32_bf16 v[52:55], v[248:251], v[28:31], v[52:55]\n"
    "s_waitcnt lgkmcnt(2)\n"
    "v_mov_b32 v12, s68\n"
    "v_mfma_f32_16x16x32_bf16 v[64:67], v[252:255], v[28:31], v[64:67]\n"
    "ds_read_b128 v[28:31], v9 offset:3072\n"
    "s_add_u32 s68, s68, 1\n"
    ".LgWO_head1_%=:\n"
    "v_mfma_f32_16x16x32_bf16 v[56:59], a[0:3], v[16:19], 0\n"
    "v_mfma_f32_16x16x32_bf16 v[60:63], a[4:7], v[16:19], 0\n"
    "s_waitcnt lgkmcnt(2)\n"
    "v_cvt_pk_bf16_f32 v48, v48, v49\n"
    "v_cvt_pk_bf16_f32 v49, v50, v51\n"
    "v_mfma_f32_16x16x32_bf16 v[68:71], a[8:11], v[16:19], 0\n"
    "ds_read_b128 v[16:19], v9 offset:4096\n"
    "v_cvt_pk_bf16_f32 v50, v52, v53\n"
    "v_mfma_f32_16x16x32_bf16 v[56:59], a[12:15], v[20:23], v[56:59]\n"
    "v_cvt_pk_bf16_f32 v51, v54, v55\n"
    "v_mfma_f32_16x16x32_bf16 v[60:63], a[16:19], v[20:23], v[60:63]\n"
    "s_waitcnt lgkmcnt(2)\n"
    "s_waitcnt vmcnt(1)\n"
    "buffer_store_dwordx4 v[48:51], v15, s[36:39], s73 offen sc1\n"
    "v_cvt_pk_bf16_f32 v64, v64, v65\n"
    "v_add_u32 v13, v11, v3\n"
    "v_mfma_f32_16x16x32_bf16 v[68:71], a[20:23], v[20:23], v[68:71]\n"
    "ds_read_b128 v[20:23], v9 offset:5120\n"
    "v_cvt_pk_bf16_f32 v65, v66, v67\n"
    "v_cmp_gt_i32 vcc, s71, v13\n"
    "v_mfma_f32_16x16x32_bf16 v[56:59], a[24:27], v[24:27], v[56:59]\n"
    "s_nop 0\n"
    "v_mfma_f32_16x16x32_bf16 v[60:63], a[28:31], v[24:27], v[60:63]\n"
    "s_waitcnt lgkmcnt(2)\n"
    "v_permlane16_swap_b32 v64, v66\n"
    "v_mfma_f32_16x16x32_bf16 v[68:71], a[32:35], v[24:27], v[68:71]\n"
    "ds_read_b128 v[24:27], v9 offset:6144\n"
    "s_cbranch_vccnz .LgWO_poll1_%=\n"
    "v_mfma_f32_16x16x32_bf16 v[56:59], a[36:39], v[28:31], v[56:59]\n"
    "v_permlane16_swap_b32 v65, v67\n"
    "v_mfma_f32_16x16x32_bf16 v[60:63], a[40:43], v[28:31], v[60:63]\n"
    "s_waitcnt lgkmcnt(2)\n"
    "buffer_store_dwordx4 v[64:67], v4, s[36:39], s73 offen sc1\n"
    "v_mfma_f32_16x16x32_bf16 v[68:71], a[44:47], v[28:31], v[68:71]\n"
    "ds_read_b128 v[28:31], v9 offset:7168\n"
    "v_mfma_f32_16x16x32_bf16 v[56:59], a[48:51], v[16:19], v[56:59]\n"
    "s_add_u32 m0, s59, 0\n"
    "s_lshr_b32 s73, s68, 1\n"
    "v_mfma_f32_16x16x32_bf16 v[60:63], a[52:55], v[16:19], v[60:63]\n"
    "s_waitcnt lgkmcnt(2)\n"
    "buffer_load_dwordx4 v1, s[36:39], s76 offen sc1 lds\n"
    "v_mfma_f32_16x16x32_bf16 v[68:71], a[56:59], v[16:19], v[68:71]\n"
    "ds_read_b128 v[16:19], v9 offset:8192\n"
    "v_mfma_f32_16x16x32_bf16 v[56:59], a[60:63], v[20:23], v[56:59]\n"
    "v_mfma_f32_16x16x32_bf16 v[60:63], a[64:67], v[20:23], v[60:63]\n"
    "s_waitcnt lgkmcnt(2)\n"
    "v_mfma_f32_16x16x32_bf16 v[68:71], a[68:71], v[20:23], v[68:71]\n"
    "ds_read_b128 v[20:23], v9 offset:9216\n"
    "v_mfma_f32_16x16x32_bf16 v[56:59], a[72:75], v[24:27], v[56:59]\n"
    "s_add_u32 m0, s59, 4096\n"
    "s_add_u32 s76, s76, 16384\n"
    "s_and_b32 s73, s73, s57\n"
    "v_mfma_f32_16x16x32_bf16 v[60:63], a[76:79], v[24:27], v[60:63]\n"
    "s_waitcnt lgkmcnt(2)\n"
    "buffer_load_dwordx4 v1, s[36:39], s76 offen sc1 lds\n"
    "v_mfma_f32_16x16x32_bf16 v[68:71], a[80:83], v[24:27], v[68:71]\n"
    "ds_read_b128 v[24:27], v9 offset:10240\n"
    "v_mfma_f32_16x16x32_bf16 v[56:59], a[84:87], v[28:31], v[56:59]\n"
    "v_mfma_f32_16x16x32_bf16 v[60:63], a[88:91], v[28:31], v[60:63]\n"
    "s_waitcnt lgkmcnt(2)\n"
    "v_mfma_f32_16x16x32_bf16 v[68:71], a[92:95], v[28:31], v[68:71]\n"
    "ds_read_b128 v[28:31], v9 offset:11264\n"
    "v_mfma_f32_16x16x32_bf16 v[56:59], a[96:99], v[16:19], v[56:59]\n"
    "s_add_u32 m0, s59, 8192\n"
    "s_add_u32 s76, s76, 16384\n"
    "s_mul_i32 s73, s73, s56\n"
    "v_mfma_f32_16x16x32_bf16 v[60:63], a[100:103], v[16:19], v[60:63]\n"
    "s_waitcnt lgkmcnt(2)\n"
    "buffer_load_dwordx4 v1, s[36:39], s76 offen sc1 lds\n"
    "v_mfma_f32_16x16x32_bf16 v[68:71], a[104:107], v[16:19], v[68:71]\n"
    "ds_read_b128 v[16:19], v9 offset:12288\n"
    "v_mfma_f32_16x16x32_bf16 v[56:59], a[108:111], v[20:23], v[56:59]\n"
    "v_mfma_f32_16x16x32_bf16 v[60:63], a[112:115], v[20:23], v[60:63]\n"
    "s_waitcnt lgkmcnt(2)\n"
    "v_mfma_f32_16x16x32_bf16 v[68:71], a[116:119], v[20:23], v[68:71]\n"
    "ds_read_b128 v[20:23], v9 offset:13312\n"
    "v_mfma_f32_16x16x32_bf16 v[56:59], a[120:123], v[24:27], v[56:59]\n"
    "s_add_u32 m0, s59, 12288\n"
    "s_add_u32 s76, s76, 16384\n"
    "s_add_u32 s73, s73, s55\n"
    "s_waitcnt vmcnt(7)\n"
    "buffer_atomic_smax v12, v14, s[36:39], s69 offen sc1\n"
    "v_mfma_f32_16x16x32_bf16 v[60:63], a[124:127], v[24:27], v[60:63]\n"
    "s_waitcnt lgkmcnt(2)\n"
    "buffer_load_dwordx4 v1, s[36:39], s76 offen sc1 lds\n"
    "v_mfma_f32_16x16x32_bf16 v[68:71], a[128:131], v[24:27], v[68:71]\n"
    "ds_read_b128 v[24:27], v9 offset:14336\n"
    "v_mfma_f32_16x16x32_bf16 v[56:59], a[132:135], v[28:31], v[56:59]\n"
    "v_mfma_f32_16x16x32_bf16 v[60:63], a[136:139], v[28:31], v[60:63]\n"
    "s_waitcnt lgkmcnt(2)\n"
    "v_mfma_f32_16x16x32_bf16 v[68:71], a[140:143], v[28:31], v[68:71]\n"
    "ds_read_b128 v[28:31], v9 offset:15360\n"
    "v_mfma_f32_16x16x32_bf16 v[56:59], a[144:147], v[16:19], v[56:59]\n"
    "s_add_u32 m0, s59, 16384\n"
    "s_add_u32 s76, s76, 16384\n"
    "v_mfma_f32_16x16x32_bf16 v[60:63], a[148:151], v[16:19], v[60:63]\n"
    "s_waitcnt lgkmcnt(2)\n"
    "buffer_load_dwordx4 v1, s[36:39], s76 offen sc1 lds\n"
    "v_mfma_f32_16x16x32_bf16 v[68:71], a[152:155], v[16:19], v[68:71]\n"
    "ds_read_b128 v[16:19], v9 offset:16384\n"
    "v_mfma_f32_16x16x32_bf16 v[56:59], a[156:159], v[20:23], v[56:59]\n"
    "v_mfma_f32_16x16x32_bf16 v[60:63], a[160:163], v[20:23], v[60:63]\n"
    "s_waitcnt lgkmcnt(2)\n"
    "v_mfma_f32_16x16x32_bf16 v[68:71], a[164:167], v[20:23], v[68:71]\n"
    "ds_read_b128 v[20:23], v9 offset:17408\n"
    "v_mfma_f32_16x16x32_bf16 v[56:59], a[168:171], v[24:27], v[56:59]\n"
    "s_add_u32 m0, s59, 20480\n"
    "s_add_u32 s76, s76, 16384\n"
    "v_mfma_f32_16x16x32_bf16 v[60:63], a[172:175], v[24:27], v[60:63]\n"
    "s_waitcnt lgkmcnt(2)\n"
    "buffer_load_dwordx4 v1, s[36:39], s76 offen sc1 lds\n"
    "v_mfma_f32_16x16x32_bf16 v[68:71], a[176:179], v[24:27], v[68:71]\n"
    "ds_read_b128 v[24:27], v9 offset:18432\n"
    "v_mfma_f32_16x16x32_bf16 v[56:59], a[180:183], v[28:31], v[56:59]\n"
    "v_mfma_f32_16x16x32_bf16 v[60:63], a[184:187], v[28:31], v[60:63]\n"
    "s_waitcnt lgkmcnt(2)\n"
    "v_mfma_f32_16x16x32_bf16 v[68:71], a[188:191], v[28:31], v[68:71]\n"
    "ds_read_b128 v[28:31], v9 offset:19456\n"
    "v_mfma_f32_16x16x32_bf16 v[56:59], a[192:195], v[16:19], v[56:59]\n"
    "s_add_u32 m0, s59, 24576\n"
    "s_add_u32 s76, s76, 16384\n"
    "v_mfma_f32_16x16x32_bf16 v[60:63], a[196:199], v[16:19], v[60:63]\n"
    "s_waitcnt lgkmcnt(2)\n"
    "buffer_load_dwordx4 v1, s[36:39], s76 offen sc1 lds\n"
    "v_mfma_f32_16x16x32_bf16 v[68:71], a[200:203], v[16:19], v[68:71]\n"
    "ds_read_b128 v[16:19], v9 offset:20480\n"
    "s_waitcnt vmcnt(8)\n"
    "buffer_atomic_smax v12, v14, s[36:39], s70 offen sc1\n"
    "v_mfma_f32_16x16x32_bf16 v[56:59], a[204:207], v[20:23], v[56:59]\n"
    "v_mfma_f32_16x16x32_bf16 v[60:63], a[208:211], v[20:23], v[60:63]\n"
    "s_waitcnt lgkmcnt(2)\n"
    "v_mfma_f32_16x16x32_bf16 v[68:71], a[212:215], v[20:23], v[68:71]\n"
    "ds_read_b128 v[20:23], v9 offset:21504\n"
    "v_mfma_f32_16x16x32_bf16 v[56:59], a[216:219], v[24:27], v[56:59]\n"
    "s_add_u32 m0, s59, 28672\n"
    "s_add_u32 s76, s76, 16384\n"
    "v_mfma_f32_16x16x32_bf16 v[60:63], a[220:223], v[24:27], v[60:63]\n"
    "s_waitcnt lgkmcnt(2)\n"
    "buffer_load_dwordx4 v1, s[36:39], s76 offen sc1 lds\n"
    "v_mfma_f32_16x16x32_bf16 v[68:71], a[224:227], v[24:27], v[68:71]\n"
    "ds_read_b128 v[24:27], v9 offset:22528\n"
    "v_mfma_f32_16x16x32_bf16 v[56:59], a[228:231], v[28:31], v[56:59]\n"
    "v_mfma_f32_16x16x32_bf16 v[60:63], a[232:235], v[28:31], v[60:63]\n"
    "s_waitcnt lgkmcnt(2)\n"
    "v_mfma_f32_16x16x32_bf16 v[68:71], a[236:239], v[28:31], v[68:71]\n"
    "ds_read_b128 v[28:31], v9 offset:23552\n"
    ".LgWO_join1_%=:\n"
    "v_mfma_f32_16x16x32_bf16 v[56:59], a[240:243], v[16:19], v[56:59]\n"
    "s_add_u32 s71, s68, 2\n"
    "s_waitcnt vmcnt(8)\n"
    "v_mfma_f32_16x16x32_bf16 v[60:63], a[244:247], v[16:19], v[60:63]\n"
    "s_waitcnt lgkmcnt(2)\n"
    "v_add_u32 v13, v11, v3\n"
    "v_mfma_f32_16x16x32_bf16 v[68:71], a[248:251], v[16:19], v[68:71]\n"
    "ds_read_b128 v[16:19], v9 offset:24576\n"
    "v_cmp_gt_i32 vcc, s71, v13\n"
    "v_mfma_f32_16x16x32_bf16 v[56:59], a[252:255], v[20:23], v[56:59]\n"
    "v_mfma_f32_16x16x32_bf16 v[60:63], v[128:131], v[20:23], v[60:63]\n"
    "s_waitcnt lgkmcnt(2)\n"
    "v_mfma_f32_16x16x32_bf16 v[68:71], v[132:135], v[20:23], v[68:71]\n"
    "ds_read_b128 v[20:23], v9 offset:25600\n"
    "s_or_b64 exec, vcc, s[62:63]\n"
    "buffer_atomic_smax v11, v2, s[36:39], 0 offen sc0 sc1\n"
    "s_mov_b64 exec, -1\n"
    "v_mfma_f32_16x16x32_bf16 v[56:59], v[136:139], v[24:27], v[56:59]\n"
    "v_mfma_f32_16x16x32_bf16 v[60:63], v[140:143], v[24:27], v[60:63]\n"
    "s_waitcnt lgkmcnt(2)\n"
    "s_lshr_b32 s76, s71, 1\n"
    "v_mfma_f32_16x16x32_bf16 v[68:71], v[144:147], v[24:27], v[68:71]\n"
    "ds_read_b128 v[24:27], v9 offset:26624\n"
    "s_and_b32 s76, s76, s54\n"
    "v_mfma_f32_16x16x32_bf16 v[56:59], v[148:151], v[28:31], v[56:59]\n"
    "s_mul_i32 s76, s76, s53\n"
    "v_mfma_f32_16x16x32_bf16 v[60:63], v[152:155], v[28:31], v[60:63]\n"
    "s_waitcnt lgkmcnt(2)\n"
    "s_add_u32 s76, s76, s52\n"
    "v_mfma_f32_16x16x32_bf16 v[68:71], v[156:159], v[28:31], v[68:71]\n"
    "ds_read_b128 v[28:31], v9 offset:27648\n"
    "s_add_u32 s76, s76, 1024\n"
    "v_mfma_f32_16x16x32_bf16 v[56:59], v[160:163], v[16:19], v[56:59]\n"
    "v_mfma_f32_16x16x32_bf16 v[60:63], v[164:167], v[16:19], v[60:63]\n"
    "s_waitcnt lgkmcnt(2)\n"
    "v_mfma_f32_16x16x32_bf16 v[68:71], v[168:171], v[16:19], v[68:71]\n"
    "ds_read_b128 v[16:19], v9 offset:28672\n"
    "v_mfma_f32_16x16x32_bf16 v[56:59], v[172:175], v[20:23], v[56:59]\n"
    "v_mfma_f32_16x16x32_bf16 v[60:63], v[176:179], v[20:23], v[60:63]\n"
    "s_waitcnt lgkmcnt(2)\n"
    "v_mfma_f32_16x16x32_bf16 v[68:71], v[180:183], v[20:23], v[68:71]\n"
    "ds_read_b128 v[20:23], v9 offset:29696\n"
    "v_mfma_f32_16x16x32_bf16 v[56:59], v[184:187], v[24:27], v[56:59]\n"
    "v_mfma_f32_16x16x32_bf16 v[60:63], v[188:191], v[24:27], v[60:63]\n"
    "s_waitcnt lgkmcnt(2)\n"
    "v_mfma_f32_16x16x32_bf16 v[68:71], v[192:195], v[24:27], v[68:71]\n"
    "ds_read_b128 v[24:27], v9 offset:30720\n"
    "v_mfma_f32_16x16x32_bf16 v[56:59], v[196:199], v[28:31], v[56:59]\n"
    "v_add_u32 v13, v5, v3\n"
    "v_mfma_f32_16x16x32_bf16 v[60:63], v[200:203], v[28:31], v[60:63]\n"
    "s_waitcnt lgkmcnt(2)\n"
    "v_cmp_gt_i32 vcc, s71, v13\n"
    "v_mfma_f32_16x16x32_bf16 v[68:71], v[204:207], v[28:31], v[68:71]\n"
    "ds_read_b128 v[28:31], v9 offset:31744\n"
    "v_mfma_f32_16x16x32_bf16 v[56:59], v[208:211], v[16:19], v[56:59]\n"
    "v_mfma_f32_16x16x32_bf16 v[60:63], v[212:215], v[16:19], v[60:63]\n"
    "s_waitcnt lgkmcnt(2)\n"
    "s_or_b64 exec, vcc, s[62:63]\n"
    "buffer_atomic_smax v5, v2, s[36:39], 0 offen sc0 sc1\n"
    "s_mov_b64 exec, -1\n"
    "v_mfma_f32_16x16x32_bf16 v[68:71], v[216:219], v[16:19], v[68:71]\n"
    "v_mfma_f32_16x16x32_bf16 v[56:59], v[220:223], v[20:23], v[56:59]\n"
    "s_waitcnt vmcnt(2) lgkmcnt(0)\n"
    "s_barrier\n"
    "ds_read_b128 v[16:19], v0 offset:0\n"
    "v_mfma_f32_16x16x32_bf16 v[60:63], v[224:227], v[20:23], v[60:63]\n"
    "v_mfma_f32_16x16x32_bf16 v[68:71], v[228:231], v[20:23], v[68:71]\n"
    "ds_read_b128 v[20:23], v0 offset:1024\n"
    "v_mfma_f32_16x16x32_bf16 v[56:59], v[232:235], v[24:27], v[56:59]\n"
    "v_mfma_f32_16x16x32_bf16 v[60:63], v[236:239], v[24:27], v[60:63]\n"
    "v_mfma_f32_16x16x32_bf16 v[68:71], v[240:243], v[24:27], v[68:71]\n"
    "ds_read_b128 v[24:27], v0 offset:2048\n"
    "s_mov_b32 s69, 0x80000000\n"
    "v_mfma_f32_16x16x32_bf16 v[56:59], v[244:247], v[28:31], v[56:59]\n"
    "s_mov_b32 s70, 0\n"
    "v_mfma_f32_16x16x32_bf16 v[60:63], v[248:251], v[28:31], v[60:63]\n"
    "s_waitcnt lgkmcnt(2)\n"
    "v_mov_b32 v12, s68\n"
    "v_mfma_f32_16x16x32_bf16 v[68:71], v[252:255], v[28:31], v[68:71]\n"
    "ds_read_b128 v[28:31], v0 offset:3072\n"
    "s_add_u32 s68, s68, 1\n"
    "s_branch .LgWO_head0_%=\n"
    ".LgWO_LOOPE_%=:\n"
    ".LgWO_done_%=:\n"
    "s_waitcnt vmcnt(0)\n"
    "buffer_atomic_smax v12, v10, s[36:39], 0 offen sc1\n"
    "s_waitcnt vmcnt(0)\n"
    "s_branch .LgWO_FIN_%=\n"
    ".LgWO_poll0_%=:\n"
    "v_mfma_f32_16x16x32_bf16 v[48:51], a[36:39], v[28:31], v[48:51]\n"
    "s_or_b64 exec, vcc, s[62:63]\n"
    "buffer_atomic_smax v11, v2, s[36:39], 0 offen sc0 sc1\n"
    "s_mov_b64 exec, -1\n"
    "s_nop 0\n"
    "v_permlane16_swap_b32 v69, v71\n"
    "v_mfma_f32_16x16x32_bf16 v[52:55], a[40:43], v[28:31], v[52:55]\n"
    "s_waitcnt lgkmcnt(2)\n"
    "buffer_store_dwordx4 v[68:71], v4, s[36:39], s73 offen offset:1024 sc1\n"
    "v_mfma_f32_16x16x32_bf16 v[64:67], a[44:47], v[28:31], v[64:67]\n"
    "ds_read_b128 v[28:31], v0 offset:7168\n"
    "v_mfma_f32_16x16x32_bf16 v[48:51], a[48:51], v[16:19], v[48:51]\n"
    "s_lshr_b32 s73, s68, 1\n"
    "v_mfma_f32_16x16x32_bf16 v[52:55], a[52:55], v[16:19], v[52:55]\n"
    "s_waitcnt lgkmcnt(2)\n"
    "v_mfma_f32_16x16x32_bf16 v[64:67], a[56:59], v[16:19], v[64:67]\n"
    "ds_read_b128 v[16:19], v0 offset:8192\n"
    "v_mfma_f32_16x16x32_bf16 v[48:51], a[60:63], v[20:23], v[48:51]\n"
    "v_mfma_f32_16x16x32_bf16 v[52:55], a[64:67], v[20:23], v[52:55]\n"
    "s_waitcnt lgkmcnt(2)\n"
    "v_mfma_f32_16x16x32_bf16 v[64:67], a[68:71], v[20:23], v[64:67]\n"
    "ds_read_b128 v[20:23], v0 offset:9216\n"
    "v_mfma_f32_16x16x32_bf16 v[48:51], a[72:75], v[24:27], v[48:51]\n"
    "s_and_b32 s73, s73, s57\n"
    "v_mfma_f32_16x16x32_bf16 v[52:55], a[76:79], v[24:27], v[52:55]\n"
    "s_waitcnt lgkmcnt(2)\n"
    "v_mfma_f32_16x16x32_bf16 v[64:67], a[80:83], v[24:27], v[64:67]\n"
    "ds_read_b128 v[24:27], v0 offset:10240\n"
    "v_mfma_f32_16x16x32_bf16 v[48:51], a[84:87], v[28:31], v[48:51]\n"
    "v_mfma_f32_16x16x32_bf16 v[52:55], a[88:91], v[28:31], v[52:55]\n"
    "s_waitcnt lgkmcnt(2)\n"
    "v_mfma_f32_16x16x32_bf16 v[64:67], a[92:95], v[28:31], v[64:67]\n"
    "ds_read_b128 v[28:31], v0 offset:11264\n"
    "v_mfma_f32_16x16x32_bf16 v[48:51], a[96:99], v[16:19], v[48:51]\n"
    "s_mul_i32 s73, s73, s56\n"
    "s_waitcnt vmcnt(3)\n"
    "v_add_u32 v13, v5, v3\n"
    "v_mfma_f32_16x16x32_bf16 v[52:55], a[100:103], v[16:19], v[52:55]\n"
    "s_waitcnt lgkmcnt(2)\n"
    "v_cmp_gt_i32 vcc, s71, v13\n"
    "v_mfma_f32_16x16x32_bf16 v[64:67], a[104:107], v[16:19], v[64:67]\n"
    "ds_read_b128 v[16:19], v0 offset:12288\n"
    "v_mfma_f32_16x16x32_bf16 v[48:51], a[108:111], v[20:23], v[48:51]\n"
    "v_mfma_f32_16x16x32_bf16 v[52:55], a[112:115], v[20:23], v[52:55]\n"
    "s_waitcnt lgkmcnt(2)\n"
    "s_cbranch_vccz .LgWO_dma0_29_%=\n"
    "v_mfma_f32_16x16x32_bf16 v[64:67], a[116:119], v[20:23], v[64:67]\n"
    "ds_read_b128 v[20:23], v0 offset:13312\n"
    "s_or_b64 exec, vcc, s[62:63]\n"
    "buffer_atomic_smax v5, v2, s[36:39], 0 offen sc0 sc1\n"
    "s_mov_b64 exec, -1\n"
    "v_mfma_f32_16x16x32_bf16 v[48:51], a[120:123], v[24:27], v[48:51]\n"
    "s_add_u32 s73, s73, s55\n"
    "s_waitcnt vmcnt(6)\n"
    "buffer_atomic_smax v12, v10, s[36:39], s69 offen sc1\n"
    "v_mfma_f32_16x16x32_bf16 v[52:55], a[124:127], v[24:27], v[52:55]\n"
    "s_waitcnt lgkmcnt(2)\n"
    "v_mfma_f32_16x16x32_bf16 v[64:67], a[128:131], v[24:27], v[64:67]\n"
    "ds_read_b128 v[24:27], v0 offset:14336\n"
    "v_mfma_f32_16x16x32_bf16 v[48:51], a[132:135], v[28:31], v[48:51]\n"
    "v_mfma_f32_16x16x32_bf16 v[52:55], a[136:139], v[28:31], v[52:55]\n"
    "s_waitcnt lgkmcnt(2)\n"
    "v_mfma_f32_16x16x32_bf16 v[64:67], a[140:143], v[28:31], v[64:67]\n"
    "ds_read_b128 v[28:31], v0 offset:15360\n"
    "v_mfma_f32_16x16x32_bf16 v[48:51], a[144:147], v[16:19], v[48:51]\n"
    "v_mfma_f32_16x16x32_bf16 v[52:55], a[148:151], v[16:19], v[52:55]\n"
    "s_waitcnt lgkmcnt(2)\n"
    "v_mfma_f32_16x16x32_bf16 v[64:67], a[152:155], v[16:19], v[64:67]\n"
    "ds_read_b128 v[16:19], v0 offset:16384\n"
    "v_mfma_f32_16x16x32_bf16 v[48:51], a[156:159], v[20:23], v[48:51]\n"
    "v_mfma_f32_16x16x32_bf16 v[52:55], a[160:163], v[20:23], v[52:55]\n"
    "s_waitcnt lgkmcnt(2)\n"
    "v_mfma_f32_16x16x32_bf16 v[64:67], a[164:167], v[20:23], v[64:67]\n"
    "ds_read_b128 v[20:23], v0 offset:17408\n"
    "v_mfma_f32_16x16x32_bf16 v[48:51], a[168:171], v[24:27], v[48:51]\n"
    "v_mfma_f32_16x16x32_bf16 v[52:55], a[172:175], v[24:27], v[52:55]\n"
    "s_waitcnt lgkmcnt(2)\n"
    "v_mfma_f32_16x16x32_bf16 v[64:67], a[176:179], v[24:27], v[64:67]\n"
    "ds_read_b128 v[24:27], v0 offset:18432\n"
    "s_waitcnt vmcnt(3)\n"
    "v_add_u32 v13, v11, v3\n"
    "v_mfma_f32_16x16x32_bf16 v[48:51], a[180:183], v[28:31], v[48:51]\n"
    "v_cmp_gt_i32 vcc, s71, v13\n"
    "v_mfma_f32_16x16x32_bf16 v[52:55], a[184:187], v[28:31], v[52:55]\n"
    "s_waitcnt lgkmcnt(2)\n"
    "v_mfma_f32_16x16x32_bf16 v[64:67], a[188:191], v[28:31], v[64:67]\n"
    "ds_read_b128 v[28:31], v0 offset:19456\n"
    "v_mfma_f32_16x16x32_bf16 v[48:51], a[192:195], v[16:19], v[48:51]\n"
    "s_cbranch_vccz .LgWO_dma0_49_%=\n"
    "v_mfma_f32_16x16x32_bf16 v[52:55], a[196:199], v[16:19], v[52:55]\n"
    "s_waitcnt lgkmcnt(2)\n"
    "s_or_b64 exec, vcc, s[62:63]\n"
    "buffer_atomic_smax v11, v2, s[36:39], 0 offen sc0 sc1\n"
    "s_mov_b64 exec, -1\n"
    "v_mfma_f32_16x16x32_bf16 v[64:67], a[200:203], v[16:19], v[64:67]\n"
    "ds_read_b128 v[16:19], v0 offset:20480\n"
    "s_waitcnt vmcnt(3)\n"
    "buffer_atomic_smax v12, v10, s[36:39], s70 offen sc1\n"
    "v_mfma_f32_16x16x32_bf16 v[48:51], a[204:207], v[20:23], v[48:51]\n"
    "v_mfma_f32_16x16x32_bf16 v[52:55], a[208:211], v[20:23], v[52:55]\n"
    "s_waitcnt lgkmcnt(2)\n"
    "v_mfma_f32_16x16x32_bf16 v[64:67], a[212:215], v[20:23], v[64:67]\n"
    "ds_read_b128 v[20:23], v0 offset:21504\n"
    "v_mfma_f32_16x16x32_bf16 v[48:51], a[216:219], v[24:27], v[48:51]\n"
    "v_mfma_f32_16x16x32_bf16 v[52:55], a[220:223], v[24:27], v[52:55]\n"
    "s_waitcnt lgkmcnt(2)\n"
    "v_mfma_f32_16x16x32_bf16 v[64:67], a[224:227], v[24:27], v[64:67]\n"
    "ds_read_b128 v[24:27], v0 offset:22528\n"
    "v_mfma_f32_16x16x32_bf16 v[48:51], a[228:231], v[28:31], v[48:51]\n"
    "v_mfma_f32_16x16x32_bf16 v[52:55], a[232:235], v[28:31], v[52:55]\n"
    "s_waitcnt lgkmcnt(2)\n"
    "v_mfma_f32_16x16x32_bf16 v[64:67], a[236:239], v[28:31], v[64:67]\n"
    "ds_read_b128 v[28:31], v0 offset:23552\n"
    "v_mfma_f32_16x16x32_bf16 v[48:51], a[240:243], v[16:19], v[48:51]\n"
    "v_mfma_f32_16x16x32_bf16 v[52:55], a[244:247], v[16:19], v[52:55]\n"
    "s_waitcnt lgkmcnt(2)\n"
    "v_mfma_f32_16x16x32_bf16 v[64:67], a[248:251], v[16:19], v[64:67]\n"
    "ds_read_b128 v[16:19], v0 offset:24576\n"
    "v_mfma_f32_16x16x32_bf16 v[48:51], a[252:255], v[20:23], v[48:51]\n"
    "v_mfma_f32_16x16x32_bf16 v[52:55], v[128:131], v[20:23], v[52:55]\n"
    "s_waitcnt lgkmcnt(2)\n"
    "s_waitcnt vmcnt(3)\n"
    "v_add_u32 v13, v5, v3\n"
    "v_mfma_f32_16x16x32_bf16 v[64:67], v[132:135], v[20:23], v[64:67]\n"
    "ds_read_b128 v[20:23], v0 offset:25600\n"
    "v_cmp_gt_i32 vcc, s71, v13\n"
    "v_mfma_f32_16x16x32_bf16 v[48:51], v[136:139], v[24:27], v[48:51]\n"
    "v_mfma_f32_16x16x32_bf16 v[52:55], v[140:143], v[24:27], v[52:55]\n"
    "s_waitcnt lgkmcnt(2)\n"
    "v_mfma_f32_16x16x32_bf16 v[64:67], v[144:147], v[24:27], v[64:67]\n"
    "ds_read_b128 v[24:27], v0 offset:26624\n"
    "s_cbranch_vccz .LgWO_dma0_69_%=\n"
    "v_mfma_f32_16x16x32_bf16 v[48:51], v[148:151], v[28:31], v[48:51]\n"
    "s_or_b64 exec, vcc, s[62:63]\n"
    "buffer_atomic_smax v5, v2, s[36:39], 0 offen sc0 sc1\n"
    "s_mov_b64 exec, -1\n"
    "v_mfma_f32_16x16x32_bf16 v[52:55], v[152:155], v[28:31], v[52:55]\n"
    "s_waitcnt lgkmcnt(2)\n"
    "v_mfma_f32_16x16x32_bf16 v[64:67], v[156:159], v[28:31], v[64:67]\n"
    "ds_read_b128 v[28:31], v0 offset:27648\n"
    "v_mfma_f32_16x16x32_bf16 v[48:51], v[160:163], v[16:19], v[48:51]\n"
    "v_mfma_f32_16x16x32_bf16 v[52:55], v[164:167], v[16:19], v[52:55]\n"
    "s_waitcnt lgkmcnt(2)\n"
    "v_mfma_f32_16x16x32_bf16 v[64:67], v[168:171], v[16:19], v[64:67]\n"
    "ds_read_b128 v[16:19], v0 offset:28672\n"
    "v_mfma_f32_16x16x32_bf16 v[48:51], v[172:175], v[20:23], v[48:51]\n"
    "v_mfma_f32_16x16x32_bf16 v[52:55], v[176:179], v[20:23], v[52:55]\n"
    "s_waitcnt lgkmcnt(2)\n"
    "v_mfma_f32_16x16x32_bf16 v[64:67], v[180:183], v[20:23], v[64:67]\n"
    "ds_read_b128 v[20:23], v0 offset:29696\n"
    "v_mfma_f32_16x16x32_bf16 v[48:51], v[184:187], v[24:27], v[48:51]\n"
    "v_mfma_f32_16x16x32_bf16 v[52:55], v[188:191], v[24:27], v[52:55]\n"
    "s_waitcnt lgkmcnt(2)\n"
    "v_mfma_f32_16x16x32_bf16 v[64:67], v[192:195], v[24:27], v[64:67]\n"
    "ds_read_b128 v[24:27], v0 offset:30720\n"
    "v_mfma_f32_16x16x32_bf16 v[48:51], v[196:199], v[28:31], v[48:51]\n"
    "v_mfma_f32_16x16x32_bf16 v[52:55], v[200:203], v[28:31], v[52:55]\n"
    "s_waitcnt lgkmcnt(2)\n"
    "v_mfma_f32_16x16x32_bf16 v[64:67], v[204:207], v[28:31], v[64:67]\n"
    "ds_read_b128 v[28:31], v0 offset:31744\n"
    "v_mfma_f32_16x16x32_bf16 v[48:51], v[208:211], v[16:19], v[48:51]\n"
    "s_waitcnt vmcnt(2)\n"
    "v_add_u32 v13, v11, v3\n"
    "v_mfma_f32_16x16x32_bf16 v[52:55], v[212:215], v[16:19], v[52:55]\n"
    "s_waitcnt lgkmcnt(2)\n"
    "v_cmp_gt_i32 vcc, s71, v13\n"
    "v_mfma_f32_16x16x32_bf16 v[64:67], v[216:219], v[16:19], v[64:67]\n"
    "v_mfma_f32_16x16x32_bf16 v[48:51], v[220:223], v[20:23], v[48:51]\n"
    "v_mfma_f32_16x16x32_bf16 v[52:55], v[224:227], v[20:23], v[52:55]\n"
    "s_waitcnt lgkmcnt(1)\n"
    "s_cbranch_vccz .LgWO_dma0_89_%=\n"
    "v_mfma_f32_16x16x32_bf16 v[64:67], v[228:231], v[20:23], v[64:67]\n"
    "s_or_b64 exec, vcc, s[62:63]\n"
    "buffer_atomic_smax v11, v2, s[36:39], 0 offen sc0 sc1\n"
    "s_mov_b64 exec, -1\n"
    "v_mfma_f32_16x16x32_bf16 v[48:51], v[232:235], v[24:27], v[48:51]\n"
    "v_mfma_f32_16x16x32_bf16 v[52:55], v[236:239], v[24:27], v[52:55]\n"
    "s_waitcnt lgkmcnt(0)\n"
    "v_mfma_f32_16x16x32_bf16 v[64:67], v[240:243], v[24:27], v[64:67]\n"
    "v_mfma_f32_16x16x32_bf16 v[48:51], v[244:247], v[28:31], v[48:51]\n"
    "v_mfma_f32_16x16x32_bf16 v[52:55], v[248:251], v[28:31], v[52:55]\n"
    "v_mfma_f32_16x16x32_bf16 v[64:67], v[252:255], v[28:31], v[64:67]\n"
    "v_cvt_pk_bf16_f32 v48, v48, v49\n"
    "v_cvt_pk_bf16_f32 v49, v50, v51\n"
    "s_nop 1\n"
    "v_cvt_pk_bf16_f32 v50, v52, v53\n"
    "v_cvt_pk_bf16_f32 v51, v54, v55\n"
    "buffer_store_dwordx4 v[48:51], v15, s[36:39], s73 offen sc1\n"
    "s_nop 0\n"
    "v_cvt_pk_bf16_f32 v64, v64, v65\n"
    "v_cvt_pk_bf16_f32 v65, v66, v67\n"
    "s_nop 0\n"
    "v_permlane16_swap_b32 v64, v66\n"
    "v_permlane16_swap_b32 v65, v67\n"
    "buffer_store_dwordx4 v[64:67], v4, s[36:39], s73 offen sc1\n"
    "v_mov_b32 v12, s68\n"
    "s_mov_b32 s73, 0x80000000\n"
    "s_mov_b32 s68, s71\n"
    "s_waitcnt vmcnt(3)\n"
    "v_add_u32 v13, v5, v3\n"
    "v_cmp_gt_i32 vcc, s71, v13\n"
    "s_cbranch_vccnz .LgWO_idle0_%=\n"
    "s_add_u32 m0, s59, 32768\n"
    "s_nop 0\n"
    "buffer_load_dwordx4 v1, s[36:39], s76 offen sc1 lds\n"
    "s_add_u32 m0, s59, 36864\n"
    "s_add_u32 s76, s76, 16384\n"
    "buffer_load_dwordx4 v1, s[36:39], s76 offen sc1 lds\n"
    "s_add_u32 m0, s59, 40960\n"
    "s_add_u32 s76, s76, 16384\n"
    "buffer_load_dwordx4 v1, s[36:39], s76 offen sc1 lds\n"
    "s_add_u32 m0, s59, 45056\n"
    "s_add_u32 s76, s76, 16384\n"
    "buffer_load_dwordx4 v1, s[36:39], s76 offen sc1 lds\n"
    "s_add_u32 m0, s59, 49152\n"
    "s_add_u32 s76, s76, 16384\n"
    "buffer_load_dwordx4 v1, s[36:39], s76 offen sc1 lds\n"
    "s_add_u32 m0, s59, 53248\n"
    "s_add_u32 s76, s76, 16384\n"
    "buffer_load_dwordx4 v1, s[36:39], s76 offen sc1 lds\n"
    "s_add_u32 m0, s59, 57344\n"
    "s_add_u32 s76, s76, 16384\n"
    "buffer_load_dwordx4 v1, s[36:39], s76 offen sc1 lds\n"
    "s_add_u32 m0, s59, 61440\n"
    "s_add_u32 s76, s76, 16384\n"
    "buffer_load_dwordx4 v1, s[36:39], s76 offen sc1 lds\n"
    "s_waitcnt vmcnt(8)\n"
    "buffer_atomic_smax v12, v14, s[36:39], 0 offen sc1\n"
    "s_mov_b32 s69, 0x80000000\n"
    "s_mov_b32 s70, 0x80000000\n"
    "s_waitcnt vmcnt(11)\n"
    "s_add_u32 s71, s68, 1\n"
    "v_add_u32 v13, v11, v3\n"
    "v_cmp_gt_i32 vcc, s71, v13\n"
    "s_nop 0\n"
    "s_or_b64 exec, vcc, s[62:63]\n"
    "buffer_atomic_smax v11, v2, s[36:39], 0 offen sc0 sc1\n"
    "s_mov_b64 exec, -1\n"
    "s_waitcnt vmcnt(2)\n"
    "s_branch .LgWO_bar1_%=\n"
    ".LgWO_dma0_29_%=:\n"
    "v_mfma_f32_16x16x32_bf16 v[64:67], a[116:119], v[20:23], v[64:67]\n"
    "ds_read_b128 v[20:23], v0 offset:13312\n"
    "v_mfma_f32_16x16x32_bf16 v[48:51], a[120:123], v[24:27], v[48:51]\n"
    "s_add_u32 m0, s59, 32768\n"
    "s_add_u32 s73, s73, s55\n"
    "s_waitcnt vmcnt(5)\n"
    "buffer_atomic_smax v12, v10, s[36:39], s69 offen sc1\n"
    "v_mfma_f32_16x16x32_bf16 v[52:55], a[124:127], v[24:27], v[52:55]\n"
    "s_waitcnt lgkmcnt(2)\n"
    "buffer_load_dwordx4 v1, s[36:39], s76 offen sc1 lds\n"
    "v_mfma_f32_16x16x32_bf16 v[64:67], a[128:131], v[24:27], v[64:67]\n"
    "ds_read_b128 v[24:27], v0 offset:14336\n"
    "v_mfma_f32_16x16x32_bf16 v[48:51], a[132:135], v[28:31], v[48:51]\n"
    "s_add_u32 m0, s59, 36864\n"
    "s_add_u32 s76, s76, 16384\n"
    "v_mfma_f32_16x16x32_bf16 v[52:55], a[136:139], v[28:31], v[52:55]\n"
    "s_waitcnt lgkmcnt(2)\n"
    "buffer_load_dwordx4 v1, s[36:39], s76 offen sc1 lds\n"
    "v_mfma_f32_16x16x32_bf16 v[64:67], a[140:143], v[28:31], v[64:67]\n"
    "ds_read_b128 v[28:31], v0 offset:15360\n"
    "v_mfma_f32_16x16x32_bf16 v[48:51], a[144:147], v[16:19], v[48:51]\n"
    "s_add_u32 m0, s59, 40960\n"
    "s_add_u32 s76, s76, 16384\n"
    "v_mfma_f32_16x16x32_bf16 v[52:55], a[148:151], v[16:19], v[52:55]\n"
    "s_waitcnt lgkmcnt(2)\n"
    "buffer_load_dwordx4 v1, s[36:39], s76 offen sc1 lds\n"
    "v_mfma_f32_16x16x32_bf16 v[64:67], a[152:155], v[16:19], v[64:67]\n"
    "ds_read_b128 v[16:19], v0 offset:16384\n"
    "v_mfma_f32_16x16x32_bf16 v[48:51], a[156:159], v[20:23], v[48:51]\n"
    "s_add_u32 m0, s59, 45056\n"
    "s_add_u32 s76, s76, 16384\n"
    "v_mfma_f32_16x16x32_bf16 v[52:55], a[160:163], v[20:23], v[52:55]\n"
    "s_waitcnt lgkmcnt(2)\n"
    "buffer_load_dwordx4 v1, s[36:39], s76 offen sc1 lds\n"
    "v_mfma_f32_16x16x32_bf16 v[64:67], a[164:167], v[20:23], v[64:67]\n"
    "ds_read_b128 v[20:23], v0 offset:17408\n"
    "v_mfma_f32_16x16x32_bf16 v[48:51], a[168:171], v[24:27], v[48:51]\n"
    "v_mfma_f32_16x16x32_bf16 v[52:55], a[172:175], v[24:27], v[52:55]\n"
    "s_waitcnt lgkmcnt(2)\n"
    "v_mfma_f32_16x16x32_bf16 v[64:67], a[176:179], v[24:27], v[64:67]\n"
    "ds_read_b128 v[24:27], v0 offset:18432\n"
    "v_mfma_f32_16x16x32_bf16 v[48:51], a[180:183], v[28:31], v[48:51]\n"
    "s_add_u32 m0, s59, 49152\n"
    "s_add_u32 s76, s76, 16384\n"
    "v_mfma_f32_16x16x32_bf16 v[52:55], a[184:187], v[28:31], v[52:55]\n"
    "s_waitcnt lgkmcnt(2)\n"
    "buffer_load_dwordx4 v1, s[36:39], s76 offen sc1 lds\n"
    "v_mfma_f32_16x16x32_bf16 v[64:67], a[188:191], v[28:31], v[64:67]\n"
    "ds_read_b128 v[28:31], v0 offset:19456\n"
    "v_mfma_f32_16x16x32_bf16 v[48:51], a[192:195], v[16:19], v[48:51]\n"
    "s_add_u32 m0, s59, 53248\n"
    "s_add_u32 s76, s76, 16384\n"
    "v_mfma_f32_16x16x32_bf16 v[52:55], a[196:199], v[16:19], v[52:55]\n"
    "s_waitcnt lgkmcnt(2)\n"
    "buffer_load_dwordx4 v1, s[36:39], s76 offen sc1 lds\n"
    "v_mfma_f32_16x16x32_bf16 v[64:67], a[200:203], v[16:19], v[64:67]\n"
    "ds_read_b128 v[16:19], v0 offset:20480\n"
    "s_waitcnt vmcnt(7)\n"
    "buffer_atomic_smax v12, v10, s[36:39], s70 offen sc1\n"
    "v_mfma_f32_16x16x32_bf16 v[48:51], a[204:207], v[20:23], v[48:51]\n"
    "s_add_u32 m0, s59, 57344\n"
    "s_add_u32 s76, s76, 16384\n"
    "v_mfma_f32_16x16x32_bf16 v[52:55], a[208:211], v[20:23], v[52:55]\n"
    "s_waitcnt lgkmcnt(2)\n"
    "buffer_load_dwordx4 v1, s[36:39], s76 offen sc1 lds\n"
    "v_mfma_f32_16x16x32_bf16 v[64:67], a[212:215], v[20:23], v[64:67]\n"
    "ds_read_b128 v[20:23], v0 offset:21504\n"
    "v_mfma_f32_16x16x32_bf16 v[48:51], a[216:219], v[24:27], v[48:51]\n"
    "s_add_u32 m0, s59, 61440\n"
    "s_add_u32 s76, s76, 16384\n"
    "v_mfma_f32_16x16x32_bf16 v[52:55], a[220:223], v[24:27], v[52:55]\n"
    "s_waitcnt lgkmcnt(2)\n"
    "buffer_load_dwordx4 v1, s[36:39], s76 offen sc1 lds\n"
    "v_mfma_f32_16x16x32_bf16 v[64:67], a[224:227], v[24:27], v[64:67]\n"
    "ds_read_b128 v[24:27], v0 offset:22528\n"
    "v_mfma_f32_16x16x32_bf16 v[48:51], a[228:231], v[28:31], v[48:51]\n"
    "v_mfma_f32_16x16x32_bf16 v[52:55], a[232:235], v[28:31], v[52:55]\n"
    "s_waitcnt lgkmcnt(2)\n"
    "v_mfma_f32_16x16x32_bf16 v[64:67], a[236:239], v[28:31], v[64:67]\n"
    "ds_read_b128 v[28:31], v0 offset:23552\n"
    "v_mfma_f32_16x16x32_bf16 v[48:51], a[240:243], v[16:19], v[48:51]\n"
    "v_mfma_f32_16x16x32_bf16 v[52:55], a[244:247], v[16:19], v[52:55]\n"
    "s_waitcnt lgkmcnt(2)\n"
    "v_mfma_f32_16x16x32_bf16 v[64:67], a[248:251], v[16:19], v[64:67]\n"
    "ds_read_b128 v[16:19], v0 offset:24576\n"
    "v_mfma_f32_16x16x32_bf16 v[48:51], a[252:255], v[20:23], v[48:51]\n"
    "v_mfma_f32_16x16x32_bf16 v[52:55], v[128:131], v[20:23], v[52:55]\n"
    "s_waitcnt lgkmcnt(2)\n"
    "v_mfma_f32_16x16x32_bf16 v[64:67], v[132:135], v[20:23], v[64:67]\n"
    "ds_read_b128 v[20:23], v0 offset:25600\n"
    "v_mfma_f32_16x16x32_bf16 v[48:51], v[136:139], v[24:27], v[48:51]\n"
    "v_mfma_f32_16x16x32_bf16 v[52:55], v[140:143], v[24:27], v[52:55]\n"
    "s_waitcnt lgkmcnt(2)\n"
    "v_mfma_f32_16x16x32_bf16 v[64:67], v[144:147], v[24:27], v[64:67]\n"
    "ds_read_b128 v[24:27], v0 offset:26624\n"
    "v_mfma_f32_16x16x32_bf16 v[48:51], v[148:151], v[28:31], v[48:51]\n"
    "v_mfma_f32_16x16x32_bf16 v[52:55], v[152:155], v[28:31], v[52:55]\n"
    "s_waitcnt lgkmcnt(2)\n"
    "v_mfma_f32_16x16x32_bf16 v[64:67], v[156:159], v[28:31], v[64:67]\n"
    "ds_read_b128 v[28:31], v0 offset:27648\n"
    "v_mfma_f32_16x16x32_bf16 v[48:51], v[160:163], v[16:19], v[48:51]\n"
    "v_mfma_f32_16x16x32_bf16 v[52:55], v[164:167], v[16:19], v[52:55]\n"
    "s_waitcnt lgkmcnt(2)\n"
    "v_mfma_f32_16x16x32_bf16 v[64:67], v[168:171], v[16:19], v[64:67]\n"
    "ds_read_b128 v[16:19], v0 offset:28672\n"
    "v_mfma_f32_16x16x32_bf16 v[48:51], v[172:175], v[20:23], v[48:51]\n"
    "v_mfma_f32_16x16x32_bf16 v[52:55], v[176:179], v[20:23], v[52:55]\n"
    "s_waitcnt lgkmcnt(2)\n"
    "v_mfma_f32_16x16x32_bf16 v[64:67], v[180:183], v[20:23], v[64:67]\n"
    "ds_read_b128 v[20:23], v0 offset:29696\n"
    "v_mfma_f32_16x16x32_bf16 v[48:51], v[184:187], v[24:27], v[48:51]\n"
    "v_mfma_f32_16x16x32_bf16 v[52:55], v[188:191], v[24:27], v[52:55]\n"
    "s_waitcnt lgkmcnt(2)\n"
    "v_mfma_f32_16x16x32_bf16 v[64:67], v[192:195], v[24:27], v[64:67]\n"
    "ds_read_b128 v[24:27], v0 offset:30720\n"
    "v_mfma_f32_16x16x32_bf16 v[48:51], v[196:199], v[28:31], v[48:51]\n"
    "v_mfma_f32_16x16x32_bf16 v[52:55], v[200:203], v[28:31], v[52:55]\n"
    "s_waitcnt lgkmcnt(2)\n"
    "v_mfma_f32_16x16x32_bf16 v[64:67], v[204:207], v[28:31], v[64:67]\n"
    "ds_read_b128 v[28:31], v0 offset:31744\n"
    "v_mfma_f32_16x16x32_bf16 v[48:51], v[208:211], v[16:19], v[48:51]\n"
    "v_mfma_f32_16x16x32_bf16 v[52:55], v[212:215], v[16:19], v[52:55]\n"
    "s_waitcnt lgkmcnt(2)\n"
    "v_mfma_f32_16x16x32_bf16 v[64:67], v[216:219], v[16:19], v[64:67]\n"
    "v_mfma_f32_16x16x32_bf16 v[48:51], v[220:223], v[20:23], v[48:51]\n"
    "v_mfma_f32_16x16x32_bf16 v[52:55], v[224:227], v[20:23], v[52:55]\n"
    "s_waitcnt lgkmcnt(1)\n"
    "v_mfma_f32_16x16x32_bf16 v[64:67], v[228:231], v[20:23], v[64:67]\n"
    "v_mfma_f32_16x16x32_bf16 v[48:51], v[232:235], v[24:27], v[48:51]\n"
    "v_mfma_f32_16x16x32_bf16 v[52:55], v[236:239], v[24:27], v[52:55]\n"
    "s_waitcnt lgkmcnt(0)\n"
    "v_mfma_f32_16x16x32_bf16 v[64:67], v[240:243], v[24:27], v[64:67]\n"
    "v_mfma_f32_16x16x32_bf16 v[48:51], v[244:247], v[28:31], v[48:51]\n"
    "v_mfma_f32_16x16x32_bf16 v[52:55], v[248:251], v[28:31], v[52:55]\n"
    "v_mfma_f32_16x16x32_bf16 v[64:67], v[252:255], v[28:31], v[64:67]\n"
    "v_cvt_pk_bf16_f32 v48, v48, v49\n"
    "v_cvt_pk_bf16_f32 v49, v50, v51\n"
    "s_nop 1\n"
    "v_cvt_pk_bf16_f32 v50, v52, v53\n"
    "v_cvt_pk_bf16_f32 v51, v54, v55\n"
    "buffer_store_dwordx4 v[48:51], v15, s[36:39], s73 offen sc1\n"
    "s_nop 0\n"
    "v_cvt_pk_bf16_f32 v64, v64, v65\n"
    "v_cvt_pk_bf16_f32 v65, v66, v67\n"
    "s_nop 0\n"
    "v_permlane16_swap_b32 v64, v66\n"
    "v_permlane16_swap_b32 v65, v67\n"
    "buffer_store_dwordx4 v[64:67], v4, s[36:39], s73 offen sc1\n"
    "v_mov_b32 v12, s68\n"
    "s_mov_b32 s73, 0x80000000\n"
    "s_mov_b32 s68, s71\n"
    "s_mov_b32 s69, 0\n"
    "s_mov_b32 s70, 0x80000000\n"
    "s_waitcnt vmcnt(13)\n"
    "s_add_u32 s71, s68, 1\n"
    "v_add_u32 v13, v11, v3\n"
    "v_cmp_gt_i32 vcc, s71, v13\n"
    "s_nop 0\n"
    "s_or_b64 exec, vcc, s[62:63]\n"
    "buffer_atomic_smax v11, v2, s[36:39], 0 offen sc0 sc1\n"
    "s_mov_b64 exec, -1\n"
    "s_waitcnt vmcnt(3)\n"
    "s_branch .LgWO_bar1_%=\n"
    ".LgWO_dma0_49_%=:\n"
    "v_mfma_f32_16x16x32_bf16 v[52:55], a[196:199], v[16:19], v[52:55]\n"
    "s_waitcnt lgkmcnt(2)\n"
    "v_mfma_f32_16x16x32_bf16 v[64:67], a[200:203], v[16:19], v[64:67]\n"
    "ds_read_b128 v[16:19], v0 offset:20480\n"
    "s_waitcnt vmcnt(2)\n"
    "buffer_atomic_smax v12, v10, s[36:39], s70 offen sc1\n"
    "v_mfma_f32_16x16x32_bf16 v[48:51], a[204:207], v[20:23], v[48:51]\n"
    "s_add_u32 m0, s59, 32768\n"
    "v_mfma_f32_16x16x32_bf16 v[52:55], a[208:211], v[20:23], v[52:55]\n"
    "s_waitcnt lgkmcnt(2)\n"
    "buffer_load_dwordx4 v1, s[36:39], s76 offen sc1 lds\n"
    "v_mfma_f32_16x16x32_bf16 v[64:67], a[212:215], v[20:23], v[64:67]\n"
    "ds_read_b128 v[20:23], v0 offset:21504\n"
    "v_mfma_f32_16x16x32_bf16 v[48:51], a[216:219], v[24:27], v[48:51]\n"
    "s_add_u32 m0, s59, 36864\n"
    "s_add_u32 s76, s76, 16384\n"
    "v_mfma_f32_16x16x32_bf16 v[52:55], a[220:223], v[24:27], v[52:55]\n"
    "s_waitcnt lgkmcnt(2)\n"
    "buffer_load_dwordx4 v1, s[36:39], s76 offen sc1 lds\n"
    "v_mfma_f32_16x16x32_bf16 v[64:67], a[224:227], v[24:27], v[64:67]\n"
    "ds_read_b128 v[24:27], v0 offset:22528\n"
    "v_mfma_f32_16x16x32_bf16 v[48:51], a[228:231], v[28:31], v[48:51]\n"
    "s_add_u32 m0, s59, 40960\n"
    "s_add_u32 s76, s76, 16384\n"
    "v_mfma_f32_16x16x32_bf16 v[52:55], a[232:235], v[28:31], v[52:55]\n"
    "s_waitcnt lgkmcnt(2)\n"
    "buffer_load_dwordx4 v1, s[36:39], s76 offen sc1 lds\n"
    "v_mfma_f32_16x16x32_bf16 v[64:67], a[236:239], v[28:31], v[64:67]\n"
    "ds_read_b128 v[28:31], v0 offset:23552\n"
    "v_mfma_f32_16x16x32_bf16 v[48:51], a[240:243], v[16:19], v[48:51]\n"
    "s_add_u32 m0, s59, 45056\n"
    "s_add_u32 s76, s76, 16384\n"
    "v_mfma_f32_16x16x32_bf16 v[52:55], a[244:247], v[16:19], v[52:55]\n"
    "s_waitcnt lgkmcnt(2)\n"
    "buffer_load_dwordx4 v1, s[36:39], s76 offen sc1 lds\n"
    "v_mfma_f32_16x16x32_bf16 v[64:67], a[248:251], v[16:19], v[64:67]\n"
    "ds_read_b128 v[16:19], v0 offset:24576\n"
    "v_mfma_f32_16x16x32_bf16 v[48:51], a[252:255], v[20:23], v[48:51]\n"
    "v_mfma_f32_16x16x32_bf16 v[52:55], v[128:131], v[20:23], v[52:55]\n"
    "s_waitcnt lgkmcnt(2)\n"
    "v_mfma_f32_16x16x32_bf16 v[64:67], v[132:135], v[20:23], v[64:67]\n"
    "ds_read_b128 v[20:23], v0 offset:25600\n"
    "v_mfma_f32_16x16x32_bf16 v[48:51], v[136:139], v[24:27], v[48:51]\n"
    "s_add_u32 m0, s59, 49152\n"
    "s_add_u32 s76, s76, 16384\n"
    "v_mfma_f32_16x16x32_bf16 v[52:55], v[140:143], v[24:27], v[52:55]\n"
    "s_waitcnt lgkmcnt(2)\n"
    "buffer_load_dwordx4 v1, s[36:39], s76 offen sc1 lds\n"
    "v_mfma_f32_16x16x32_bf16 v[64:67], v[144:147], v[24:27], v[64:67]\n"
    "ds_read_b128 v[24:27], v0 offset:26624\n"
    "v_mfma_f32_16x16x32_bf16 v[48:51], v[148:151], v[28:31], v[48:51]\n"
    "s_add_u32 m0, s59, 53248\n"
    "s_add_u32 s76, s76, 16384\n"
    "v_mfma_f32_16x16x32_bf16 v[52:55], v[152:155], v[28:31], v[52:55]\n"
    "s_waitcnt lgkmcnt(2)\n"
    "buffer_load_dwordx4 v1, s[36:39], s76 offen sc1 lds\n"
    "v_mfma_f32_16x16x32_bf16 v[64:67], v[156:159], v[28:31], v[64:67]\n"
    "ds_read_b128 v[28:31], v0 offset:27648\n"
    "v_mfma_f32_16x16x32_bf16 v[48:51], v[160:163], v[16:19], v[48:51]\n"
    "s_add_u32 m0, s59, 57344\n"
    "s_add_u32 s76, s76, 16384\n"
    "v_mfma_f32_16x16x32_bf16 v[52:55], v[164:167], v[16:19], v[52:55]\n"
    "s_waitcnt lgkmcnt(2)\n"
    "buffer_load_dwordx4 v1, s[36:39], s76 offen sc1 lds\n"
    "v_mfma_f32_16x16x32_bf16 v[64:67], v[168:171], v[16:19], v[64:67]\n"
    "ds_read_b128 v[16:19], v0 offset:28672\n"
    "v_mfma_f32_16x16x32_bf16 v[48:51], v[172:175], v[20:23], v[48:51]\n"
    "s_add_u32 m0, s59, 61440\n"
    "s_add_u32 s76, s76, 16384\n"
    "v_mfma_f32_16x16x32_bf16 v[52:55], v[176:179], v[20:23], v[52:55]\n"
    "s_waitcnt lgkmcnt(2)\n"
    "buffer_load_dwordx4 v1, s[36:39], s76 offen sc1 lds\n"
    "v_mfma_f32_16x16x32_bf16 v[64:67], v[180:183], v[20:23], v[64:67]\n"
    "ds_read_b128 v[20:23], v0 offset:29696\n"
    "v_mfma_f32_16x16x32_bf16 v[48:51], v[184:187], v[24:27], v[48:51]\n"
    "v_mfma_f32_16x16x32_bf16 v[52:55], v[188:191], v[24:27], v[52:55]\n"
    "s_waitcnt lgkmcnt(2)\n"
    "v_mfma_f32_16x16x32_bf16 v[64:67], v[192:195], v[24:27], v[64:67]\n"
    "ds_read_b128 v[24:27], v0 offset:30720\n"
    "v_mfma_f32_16x16x32_bf16 v[48:51], v[196:199], v[28:31], v[48:51]\n"
    "v_mfma_f32_16x16x32_bf16 v[52:55], v[200:203], v[28:31], v[52:55]\n"
    "s_waitcnt lgkmcnt(2)\n"
    "v_mfma_f32_16x16x32_bf16 v[64:67], v[204:207], v[28:31], v[64:67]\n"
    "ds_read_b128 v[28:31], v0 offset:31744\n"
    "v_mfma_f32_16x16x32_bf16 v[48:51], v[208:211], v[16:19], v[48:51]\n"
    "v_mfma_f32_16x16x32_bf16 v[52:55], v[212:215], v[16:19], v[52:55]\n"
    "s_waitcnt lgkmcnt(2)\n"
    "v_mfma_f32_16x16x32_bf16 v[64:67], v[216:219], v[16:19], v[64:67]\n"
    "v_mfma_f32_16x16x32_bf16 v[48:51], v[220:223], v[20:23], v[48:51]\n"
    "v_mfma_f32_16x16x32_bf16 v[52:55], v[224:227], v[20:23], v[52:55]\n"
    "s_waitcnt lgkmcnt(1)\n"
    "v_mfma_f32_16x16x32_bf16 v[64:67], v[228:231], v[20:23], v[64:67]\n"
    "v_mfma_f32_16x16x32_bf16 v[48:51], v[232:235], v[24:27], v[48:51]\n"
    "v_mfma_f32_16x16x32_bf16 v[52:55], v[236:239], v[24:27], v[52:55]\n"
    "s_waitcnt lgkmcnt(0)\n"
    "v_mfma_f32_16x16x32_bf16 v[64:67], v[240:243], v[24:27], v[64:67]\n"
    "v_mfma_f32_16x16x32_bf16 v[48:51], v[244:247], v[28:31], v[48:51]\n"
    "v_mfma_f32_16x16x32_bf16 v[52:55], v[248:251], v[28:31], v[52:55]\n"
    "v_mfma_f32_16x16x32_bf16 v[64:67], v[252:255], v[28:31], v[64:67]\n"
    "v_cvt_pk_bf16_f32 v48, v48, v49\n"
    "v_cvt_pk_bf16_f32 v49, v50, v51\n"
    "s_nop 1\n"
    "v_cvt_pk_bf16_f32 v50, v52, v53\n"
    "v_cvt_pk_bf16_f32 v51, v54, v55\n"
    "buffer_store_dwordx4 v[48:51], v15, s[36:39], s73 offen sc1\n"
    "s_nop 0\n"
    "v_cvt_pk_bf16_f32 v64, v64, v65\n"
    "v_cvt_pk_bf16_f32 v65, v66, v67\n"
    "s_nop 0\n"
    "v_permlane16_swap_b32 v64, v66\n"
    "v_permlane16_swap_b32 v65, v67\n"
    "buffer_store_dwordx4 v[64:67], v4, s[36:39], s73 offen sc1\n"
    "v_mov_b32 v12, s68\n"
    "s_mov_b32 s73, 0x80000000\n"
    "s_mov_b32 s68, s71\n"
    "s_mov_b32 s69, 0\n"
    "s_mov_b32 s70, 0x80000000\n"
    "s_waitcnt vmcnt(12)\n"
    "s_add_u32 s71, s68, 1\n"
    "v_add_u32 v13, v11, v3\n"
    "v_cmp_gt_i32 vcc, s71, v13\n"
    "s_nop 0\n"
    "s_or_b64 exec, vcc, s[62:63]\n"
    "buffer_atomic_smax v11, v2, s[36:39], 0 offen sc0 sc1\n"
    "s_mov_b64 exec, -1\n"
    "s_waitcnt vmcnt(3)\n"
    "s_branch .LgWO_bar1_%=\n"
    ".LgWO_dma0_69_%=:\n"
    "v_mfma_f32_16x16x32_bf16 v[48:51], v[148:151], v[28:31], v[48:51]\n"
    "s_add_u32 m0, s59, 32768\n"
    "v_mfma_f32_16x16x32_bf16 v[52:55], v[152:155], v[28:31], v[52:55]\n"
    "s_waitcnt lgkmcnt(2)\n"
    "buffer_load_dwordx4 v1, s[36:39], s76 offen sc1 lds\n"
    "v_mfma_f32_16x16x32_bf16 v[64:67], v[156:159], v[28:31], v[64:67]\n"
    "ds_read_b128 v[28:31], v0 offset:27648\n"
    "v_mfma_f32_16x16x32_bf16 v[48:51], v[160:163], v[16:19], v[48:51]\n"
    "s_add_u32 m0, s59, 36864\n"
    "s_add_u32 s76, s76, 16384\n"
    "v_mfma_f32_16x16x32_bf16 v[52:55], v[164:167], v[16:19], v[52:55]\n"
    "s_waitcnt lgkmcnt(2)\n"
    "buffer_load_dwordx4 v1, s[36:39], s76 offen sc1 lds\n"
    "v_mfma_f32_16x16x32_bf16 v[64:67], v[168:171], v[16:19], v[64:67]\n"
    "ds_read_b128 v[16:19], v0 offset:28672\n"
    "v_mfma_f32_16x16x32_bf16 v[48:51], v[172:175], v[20:23], v[48:51]\n"
    "v_mfma_f32_16x16x32_bf16 v[52:55], v[176:179], v[20:23], v[52:55]\n"
    "s_waitcnt lgkmcnt(2)\n"
    "v_mfma_f32_16x16x32_bf16 v[64:67], v[180:183], v[20:23], v[64:67]\n"
    "ds_read_b128 v[20:23], v0 offset:29696\n"
    "v_mfma_f32_16x16x32_bf16 v[48:51], v[184:187], v[24:27], v[48:51]\n"
    "s_add_u32 m0, s59, 40960\n"
    "s_add_u32 s76, s76, 16384\n"
    "v_mfma_f32_16x16x32_bf16 v[52:55], v[188:191], v[24:27], v[52:55]\n"
    "s_waitcnt lgkmcnt(2)\n"
    "buffer_load_dwordx4 v1, s[36:39], s76 offen sc1 lds\n"
    "v_mfma_f32_16x16x32_bf16 v[64:67], v[192:195], v[24:27], v[64:67]\n"
    "ds_read_b128 v[24:27], v0 offset:30720\n"
    "v_mfma_f32_16x16x32_bf16 v[48:51], v[196:199], v[28:31], v[48:51]\n"
    "s_add_u32 m0, s59, 45056\n"
    "s_add_u32 s76, s76, 16384\n"
    "v_mfma_f32_16x16x32_bf16 v[52:55], v[200:203], v[28:31], v[52:55]\n"
    "s_waitcnt lgkmcnt(2)\n"
    "buffer_load_dwordx4 v1, s[36:39], s76 offen sc1 lds\n"
    "v_mfma_f32_16x16x32_bf16 v[64:67], v[204:207], v[28:31], v[64:67]\n"
    "ds_read_b128 v[28:31], v0 offset:31744\n"
    "v_mfma_f32_16x16x32_bf16 v[48:51], v[208:211], v[16:19], v[48:51]\n"
    "v_mfma_f32_16x16x32_bf16 v[52:55], v[212:215], v[16:19], v[52:55]\n"
    "s_waitcnt lgkmcnt(2)\n"
    "s_add_u32 m0, s59, 49152\n"
    "s_add_u32 s76, s76, 16384\n"
    "v_mfma_f32_16x16x32_bf16 v[64:67], v[216:219], v[16:19], v[64:67]\n"
    "buffer_load_dwordx4 v1, s[36:39], s76 offen sc1 lds\n"
    "v_mfma_f32_16x16x32_bf16 v[48:51], v[220:223], v[20:23], v[48:51]\n"
    "v_mfma_f32_16x16x32_bf16 v[52:55], v[224:227], v[20:23], v[52:55]\n"
    "s_waitcnt lgkmcnt(1)\n"
    "v_mfma_f32_16x16x32_bf16 v[64:67], v[228:231], v[20:23], v[64:67]\n"
    "v_mfma_f32_16x16x32_bf16 v[48:51], v[232:235], v[24:27], v[48:51]\n"
    "v_mfma_f32_16x16x32_bf16 v[52:55], v[236:239], v[24:27], v[52:55]\n"
    "s_waitcnt lgkmcnt(0)\n"
    "v_mfma_f32_16x16x32_bf16 v[64:67], v[240:243], v[24:27], v[64:67]\n"
    "v_mfma_f32_16x16x32_bf16 v[48:51], v[244:247], v[28:31], v[48:51]\n"
    "s_add_u32 m0, s59, 53248\n"
    "s_add_u32 s76, s76, 16384\n"
    "v_mfma_f32_16x16x32_bf16 v[52:55], v[248:251], v[28:31], v[52:55]\n"
    "buffer_load_dwordx4 v1, s[36:39], s76 offen sc1 lds\n"
    "v_mfma_f32_16x16x32_bf16 v[64:67], v[252:255], v[28:31], v[64:67]\n"
    "v_cvt_pk_bf16_f32 v48, v48, v49\n"
    "v_cvt_pk_bf16_f32 v49, v50, v51\n"
    "s_nop 1\n"
    "v_cvt_pk_bf16_f32 v50, v52, v53\n"
    "v_cvt_pk_bf16_f32 v51, v54, v55\n"
    "buffer_store_dwordx4 v[48:51], v15, s[36:39], s73 offen sc1\n"
    "s_nop 0\n"
    "v_cvt_pk_bf16_f32 v64, v64, v65\n"
    "v_cvt_pk_bf16_f32 v65, v66, v67\n"
    "s_nop 0\n"
    "v_permlane16_swap_b32 v64, v66\n"
    "v_permlane16_swap_b32 v65, v67\n"
    "buffer_store_dwordx4 v[64:67], v4, s[36:39], s73 offen sc1\n"
    "v_mov_b32 v12, s68\n"
    "s_mov_b32 s73, 0x80000000\n"
    "s_mov_b32 s68, s71\n"
    "s_add_u32 m0, s59, 57344\n"
    "s_add_u32 s76, s76, 16384\n"
    "buffer_load_dwordx4 v1, s[36:39], s76 offen sc1 lds\n"
    "s_add_u32 m0, s59, 61440\n"
    "s_add_u32 s76, s76, 16384\n"
    "buffer_load_dwordx4 v1, s[36:39], s76 offen sc1 lds\n"
    "s_waitcnt vmcnt(2)\n"
    "buffer_atomic_smax v12, v14, s[36:39], 0 offen sc1\n"
    "s_mov_b32 s69, 0x80000000\n"
    "s_mov_b32 s70, 0x80000000\n"
    "s_waitcnt vmcnt(12)\n"
    "s_add_u32 s71, s68, 1\n"
    "v_add_u32 v13, v11, v3\n"
    "v_cmp_gt_i32 vcc, s71, v13\n"
    "s_nop 0\n"
    "s_or_b64 exec, vcc, s[62:63]\n"
    "buffer_atomic_smax v11, v2, s[36:39], 0 offen sc0 sc1\n"
    "s_mov_b64 exec, -1\n"
    "s_waitcnt vmcnt(2)\n"
    "s_branch .LgWO_bar1_%=\n"
    ".LgWO_dma0_89_%=:\n"
    "v_mfma_f32_16x16x32_bf16 v[64:67], v[228:231], v[20:23], v[64:67]\n"
    "v_mfma_f32_16x16x32_bf16 v[48:51], v[232:235], v[24:27], v[48:51]\n"
    "s_add_u32 m0, s59, 32768\n"
    "v_mfma_f32_16x16x32_bf16 v[52:55], v[236:239], v[24:27], v[52:55]\n"
    "s_waitcnt lgkmcnt(0)\n"
    "buffer_load_dwordx4 v1, s[36:39], s76 offen sc1 lds\n"
    "v_mfma_f32_16x16x32_bf16 v[64:67], v[240:243], v[24:27], v[64:67]\n"
    "v_mfma_f32_16x16x32_bf16 v[48:51], v[244:247], v[28:31], v[48:51]\n"
    "v_mfma_f32_16x16x32_bf16 v[52:55], v[248:251], v[28:31], v[52:55]\n"
    "v_mfma_f32_16x16x32_bf16 v[64:67], v[252:255], v[28:31], v[64:67]\n"
    "v_cvt_pk_bf16_f32 v48, v48, v49\n"
    "v_cvt_pk_bf16_f32 v49, v50, v51\n"
    "s_nop 1\n"
    "v_cvt_pk_bf16_f32 v50, v52, v53\n"
    "v_cvt_pk_bf16_f32 v51, v54, v55\n"
    "buffer_store_dwordx4 v[48:51], v15, s[36:39], s73 offen sc1\n"
    "s_nop 0\n"
    "v_cvt_pk_bf16_f32 v64, v64, v65\n"
    "v_cvt_pk_bf16_f32 v65, v66, v67\n"
    "s_nop 0\n"
    "v_permlane16_swap_b32 v64, v66\n"
    "v_permlane16_swap_b32 v65, v67\n"
    "buffer_store_dwordx4 v[64:67], v4, s[36:39], s73 offen sc1\n"
    "v_mov_b32 v12, s68\n"
    "s_mov_b32 s73, 0x80000000\n"
    "s_mov_b32 s68, s71\n"
    "s_add_u32 m0, s59, 36864\n"
    "s_add_u32 s76, s76, 16384\n"
    "buffer_load_dwordx4 v1, s[36:39], s76 offen sc1 lds\n"
    "s_add_u32 m0, s59, 40960\n"
    "s_add_u32 s76, s76, 16384\n"
    "buffer_load_dwordx4 v1, s[36:39], s76 offen sc1 lds\n"
    "s_add_u32 m0, s59, 45056\n"
    "s_add_u32 s76, s76, 16384\n"
    "buffer_load_dwordx4 v1, s[36:39], s76 offen sc1 lds\n"
    "s_add_u32 m0, s59, 49152\n"
    "s_add_u32 s76, s76, 16384\n"
    "buffer_load_dwordx4 v1, s[36:39], s76 offen sc1 lds\n"
    "s_add_u32 m0, s59, 53248\n"
    "s_add_u32 s76, s76, 16384\n"
    "buffer_load_dwordx4 v1, s[36:39], s76 offen sc1 lds\n"
    "s_add_u32 m0, s59, 57344\n"
    "s_add_u32 s76, s76, 16384\n"
    "buffer_load_dwordx4 v1, s[36:39], s76 offen sc1 lds\n"
    "s_add_u32 m0, s59, 61440\n"
    "s_add_u32 s76, s76, 16384\n"
    "buffer_load_dwordx4 v1, s[36:39], s76 offen sc1 lds\n"
    "s_waitcnt vmcnt(7)\n"
    "buffer_atomic_smax v12, v14, s[36:39], 0 offen sc1\n"
    "s_mov_b32 s69, 0x80000000\n"
    "s_mov_b32 s70, 0x80000000\n"
    "s_waitcnt vmcnt(11)\n"
    "s_add_u32 s71, s68, 1\n"
    "v_add_u32 v13, v11, v3\n"
    "v_cmp_gt_i32 vcc, s71, v13\n"
    "s_nop 0\n"
    "s_or_b64 exec, vcc, s[62:63]\n"
    "buffer_atomic_smax v11, v2, s[36:39], 0 offen sc0 sc1\n"
    "s_mov_b64 exec, -1\n"
    "s_waitcnt vmcnt(2)\n"
    "s_branch .LgWO_bar1_%=\n"
    ".LgWO_idle0_%=:\n"
    "s_or_b64 exec, vcc, s[62:63]\n"
    "buffer_atomic_smax v5, v2, s[36:39], 0 offen sc0 sc1\n"
    "s_mov_b64 exec, -1\n"
    "s_waitcnt vmcnt(1)\n"
    "buffer_atomic_smax v12, v14, s[36:39], 0 offen sc1\n"
    "s_branch .LgWO_block0_1_%=\n"
    ".LgWO_block0_%=:\n"
    "v_add_u32 v13, v11, v3\n"
    "v_cmp_gt_i32 vcc, s71, v13\n"
    "s_nop 0\n"
    "s_or_b64 exec, vcc, s[62:63]\n"
    "buffer_atomic_smax v11, v2, s[36:39], 0 offen sc0 sc1\n"
    "s_mov_b64 exec, -1\n"
    "s_sleep 3\n"
    "s_waitcnt vmcnt(1)\n"
    ".LgWO_block0_0_%=:\n"
    "v_add_u32 v13, v5, v3\n"
    "v_cmp_gt_i32 vcc, s71, v13\n"
    "s_cbranch_vccz .LgWO_go0_%=\n"
    "v_add_u32 v13, v5, v3\n"
    "v_cmp_gt_i32 vcc, s71, v13\n"
    "s_nop 0\n"
    "s_or_b64 exec, vcc, s[62:63]\n"
    "buffer_atomic_smax v5, v2, s[36:39], 0 offen sc0 sc1\n"
    "s_mov_b64 exec, -1\n"
    "s_sleep 3\n"
    "s_waitcnt vmcnt(1)\n"
    ".LgWO_block0_1_%=:\n"
    "v_add_u32 v13, v11, v3\n"
    "v_cmp_gt_i32 vcc, s71, v13\n"
    "s_cbranch_vccz .LgWO_go0_%=\n"
    "s_sub_u32 s72, s72, 1\n"
    "s_cbranch_scc0 .LgWO_block0_%=\n"
    "s_mov_b32 s72, 0\n"
    ".LgWO_go0_%=:\n"
    "s_lshr_b32 s76, s71, 1\n"
    "s_and_b32 s76, s76, s54\n"
    "s_mul_i32 s76, s76, s53\n"
    "s_add_u32 s76, s76, s52\n"
    "s_add_u32 s76, s76, 1024\n"
    "s_add_u32 m0, s59, 32768\n"
    "s_nop 0\n"
    "buffer_load_dwordx4 v1, s[36:39], s76 offen sc1 lds\n"
    "s_add_u32 m0, s59, 36864\n"
    "s_add_u32 s76, s76, 16384\n"
    "buffer_load_dwordx4 v1, s[36:39], s76 offen sc1 lds\n"
    "s_add_u32 m0, s59, 40960\n"
    "s_add_u32 s76, s76, 16384\n"
    "buffer_load_dwordx4 v1, s[36:39], s76 offen sc1 lds\n"
    "s_add_u32 m0, s59, 45056\n"
    "s_add_u32 s76, s76, 16384\n"
    "buffer_load_dwordx4 v1, s[36:39], s76 offen sc1 lds\n"
    "s_add_u32 m0, s59, 49152\n"
    "s_add_u32 s76, s76, 16384\n"
    "buffer_load_dwordx4 v1, s[36:39], s76 offen sc1 lds\n"
    "s_add_u32 m0, s59, 53248\n"
    "s_add_u32 s76, s76, 16384\n"
    "buffer_load_dwordx4 v1, s[36:39], s76 offen sc1 lds\n"
    "s_add_u32 m0, s59, 57344\n"
    "s_add_u32 s76, s76, 16384\n"
    "buffer_load_dwordx4 v1, s[36:39], s76 offen sc1 lds\n"
    "s_add_u32 m0, s59, 61440\n"
    "s_add_u32 s76, s76, 16384\n"
    "buffer_load_dwordx4 v1, s[36:39], s76 offen sc1 lds\n"
    "s_mov_b32 s69, 0x80000000\n"
    "s_mov_b32 s70, 0x80000000\n"
    "s_waitcnt vmcnt(8)\n"
    "s_add_u32 s71, s68, 1\n"
    "v_add_u32 v13, v11, v3\n"
    "v_cmp_gt_i32 vcc, s71, v13\n"
    "s_nop 0\n"
    "s_or_b64 exec, vcc, s[62:63]\n"
    "buffer_atomic_smax v11, v2, s[36:39], 0 offen sc0 sc1\n"
    "s_mov_b64 exec, -1\n"
    "s_waitcnt vmcnt(1)\n"
    "s_branch .LgWO_bar1_%=\n"
    ".LgWO_bar0_%=:\n"
    "s_barrier\n"
    "ds_read_b128 v[16:19], v0 offset:0\n"
    "ds_read_b128 v[20:23], v0 offset:1024\n"
    "ds_read_b128 v[24:27], v0 offset:2048\n"
    "ds_read_b128 v[28:31], v0 offset:3072\n"
    "v_add_u32 v13, v5, v3\n"
    "v_cmp_gt_i32 vcc, s71, v13\n"
    "s_nop 0\n"
    "s_or_b64 exec, vcc, s[62:63]\n"
    "buffer_atomic_smax v5, v2, s[36:39], 0 offen sc0 sc1\n"
    "s_mov_b64 exec, -1\n"
    "s_lshr_b32 s76, s71, 1\n"
    "s_and_b32 s76, s76, s54\n"
    "s_mul_i32 s76, s76, s53\n"
    "s_add_u32 s76, s76, s52\n"
    "s_add_u32 s76, s76, 1024\n"
    "s_waitcnt lgkmcnt(3)\n"
    "s_branch .LgWO_head0_%=\n"
    ".LgWO_poll1_%=:\n"
    "v_mfma_f32_16x16x32_bf16 v[56:59], a[36:39], v[28:31], v[56:59]\n"
    "s_or_b64 exec, vcc, s[62:63]\n"
    "buffer_atomic_smax v11, v2, s[36:39], 0 offen sc0 sc1\n"
    "s_mov_b64 exec, -1\n"
    "s_nop 0\n"
    "v_permlane16_swap_b32 v65, v67\n"
    "v_mfma_f32_16x16x32_bf16 v[60:63], a[40:43], v[28:31], v[60:63]\n"
    "s_waitcnt lgkmcnt(2)\n"
    "buffer_store_dwordx4 v[64:67], v4, s[36:39], s73 offen sc1\n"
    "v_mfma_f32_16x16x32_bf16 v[68:71], a[44:47], v[28:31], v[68:71]\n"
    "ds_read_b128 v[28:31], v9 offset:7168\n"
    "v_mfma_f32_16x16x32_bf16 v[56:59], a[48:51], v[16:19], v[56:59]\n"
    "s_lshr_b32 s73, s68, 1\n"
    "v_mfma_f32_16x16x32_bf16 v[60:63], a[52:55], v[16:19], v[60:63]\n"
    "s_waitcnt lgkmcnt(2)\n"
    "v_mfma_f32_16x16x32_bf16 v[68:71], a[56:59], v[16:19], v[68:71]\n"
    "ds_read_b128 v[16:19], v9 offset:8192\n"
    "v_mfma_f32_16x16x32_bf16 v[56:59], a[60:63], v[20:23], v[56:59]\n"
    "v_mfma_f32_16x16x32_bf16 v[60:63], a[64:67], v[20:23], v[60:63]\n"
    "s_waitcnt lgkmcnt(2)\n"
    "v_mfma_f32_16x16x32_bf16 v[68:71], a[68:71], v[20:23], v[68:71]\n"
    "ds_read_b128 v[20:23], v9 offset:9216\n"
    "v_mfma_f32_16x16x32_bf16 v[56:59], a[72:75], v[24:27], v[56:59]\n"
    "s_and_b32 s73, s73, s57\n"
    "v_mfma_f32_16x16x32_bf16 v[60:63], a[76:79], v[24:27], v[60:63]\n"
    "s_waitcnt lgkmcnt(2)\n"
    "v_mfma_f32_16x16x32_bf16 v[68:71], a[80:83], v[24:27], v[68:71]\n"
    "ds_read_b128 v[24:27], v9 offset:10240\n"
    "v_mfma_f32_16x16x32_bf16 v[56:59], a[84:87], v[28:31], v[56:59]\n"
    "v_mfma_f32_16x16x32_bf16 v[60:63], a[88:91], v[28:31], v[60:63]\n"
    "s_waitcnt lgkmcnt(2)\n"
    "v_mfma_f32_16x16x32_bf16 v[68:71], a[92:95], v[28:31], v[68:71]\n"
    "ds_read_b128 v[28:31], v9 offset:11264\n"
    "v_mfma_f32_16x16x32_bf16 v[56:59], a[96:99], v[16:19], v[56:59]\n"
    "s_mul_i32 s73, s73, s56\n"
    "s_waitcnt vmcnt(3)\n"
    "v_add_u32 v13, v5, v3\n"
    "v_mfma_f32_16x16x32_bf16 v[60:63], a[100:103], v[16:19], v[60:63]\n"
    "s_waitcnt lgkmcnt(2)\n"
    "v_cmp_gt_i32 vcc, s71, v13\n"
    "v_mfma_f32_16x16x32_bf16 v[68:71], a[104:107], v[16:19], v[68:71]\n"
    "ds_read_b128 v[16:19], v9 offset:12288\n"
    "v_mfma_f32_16x16x32_bf16 v[56:59], a[108:111], v[20:23], v[56:59]\n"
    "v_mfma_f32_16x16x32_bf16 v[60:63], a[112:115], v[20:23], v[60:63]\n"
    "s_waitcnt lgkmcnt(2)\n"
    "s_cbranch_vccz .LgWO_dma1_29_%=\n"
    "v_mfma_f32_16x16x32_bf16 v[68:71], a[116:119], v[20:23], v[68:71]\n"
    "ds_read_b128 v[20:23], v9 offset:13312\n"
    "s_or_b64 exec, vcc, s[62:63]\n"
    "buffer_atomic_smax v5, v2, s[36:39], 0 offen sc0 sc1\n"
    "s_mov_b64 exec, -1\n"
    "v_mfma_f32_16x16x32_bf16 v[56:59], a[120:123], v[24:27], v[56:59]\n"
    "s_add_u32 s73, s73, s55\n"
    "s_waitcnt vmcnt(6)\n"
    "buffer_atomic_smax v12, v14, s[36:39], s69 offen sc1\n"
    "v_mfma_f32_16x16x32_bf16 v[60:63], a[124:127], v[24:27], v[60:63]\n"
    "s_waitcnt lgkmcnt(2)\n"
    "v_mfma_f32_16x16x32_bf16 v[68:71], a[128:131], v[24:27], v[68:71]\n"
    "ds_read_b128 v[24:27], v9 offset:14336\n"
    "v_mfma_f32_16x16x32_bf16 v[56:59], a[132:135], v[28:31], v[56:59]\n"
    "v_mfma_f32_16x16x32_bf16 v[60:63], a[136:139], v[28:31], v[60:63]\n"
    "s_waitcnt lgkmcnt(2)\n"
    "v_mfma_f32_16x16x32_bf16 v[68:71], a[140:143], v[28:31], v[68:71]\n"
    "ds_read_b128 v[28:31], v9 offset:15360\n"
    "v_mfma_f32_16x16x32_bf16 v[56:59], a[144:147], v[16:19], v[56:59]\n"
    "v_mfma_f32_16x16x32_bf16 v[60:63], a[148:151], v[16:19], v[60:63]\n"
    "s_waitcnt lgkmcnt(2)\n"
    "v_mfma_f32_16x16x32_bf16 v[68:71], a[152:155], v[16:19], v[68:71]\n"
    "ds_read_b128 v[16:19], v9 offset:16384\n"
    "v_mfma_f32_16x16x32_bf16 v[56:59], a[156:159], v[20:23], v[56:59]\n"
    "v_mfma_f32_16x16x32_bf16 v[60:63], a[160:163], v[20:23], v[60:63]\n"
    "s_waitcnt lgkmcnt(2)\n"
    "v_mfma_f32_16x16x32_bf16 v[68:71], a[164:167], v[20:23], v[68:71]\n"
    "ds_read_b128 v[20:23], v9 offset:17408\n"
    "v_mfma_f32_16x16x32_bf16 v[56:59], a[168:171], v[24:27], v[56:59]\n"
    "v_mfma_f32_16x16x32_bf16 v[60:63], a[172:175], v[24:27], v[60:63]\n"
    "s_waitcnt lgkmcnt(2)\n"
    "v_mfma_f32_16x16x32_bf16 v[68:71], a[176:179], v[24:27], v[68:71]\n"
    "ds_read_b128 v[24:27], v9 offset:18432\n"
    "s_waitcnt vmcnt(3)\n"
    "v_add_u32 v13, v11, v3\n"
    "v_mfma_f32_16x16x32_bf16 v[56:59], a[180:183], v[28:31], v[56:59]\n"
    "v_cmp_gt_i32 vcc, s71, v13\n"
    "v_mfma_f32_16x16x32_bf16 v[60:63], a[184:187], v[28:31], v[60:63]\n"
    "s_waitcnt lgkmcnt(2)\n"
    "v_mfma_f32_16x16x32_bf16 v[68:71], a[188:191], v[28:31], v[68:71]\n"
    "ds_read_b128 v[28:31], v9 offset:19456\n"
    "v_mfma_f32_16x16x32_bf16 v[56:59], a[192:195], v[16:19], v[56:59]\n"
    "s_cbranch_vccz .LgWO_dma1_49_%=\n"
    "v_mfma_f32_16x16x32_bf16 v[60:63], a[196:199], v[16:19], v[60:63]\n"
    "s_waitcnt lgkmcnt(2)\n"
    "s_or_b64 exec, vcc, s[62:63]\n"
    "buffer_atomic_smax v11, v2, s[36:39], 0 offen sc0 sc1\n"
    "s_mov_b64 exec, -1\n"
    "v_mfma_f32_16x16x32_bf16 v[68:71], a[200:203], v[16:19], v[68:71]\n"
    "ds_read_b128 v[16:19], v9 offset:20480\n"
    "s_waitcnt vmcnt(3)\n"
    "buffer_atomic_smax v12, v14, s[36:39], s70 offen sc1\n"
    "v_mfma_f32_16x16x32_bf16 v[56:59], a[204:207], v[20:23], v[56:59]\n"
    "v_mfma_f32_16x16x32_bf16 v[60:63], a[208:211], v[20:23], v[60:63]\n"
    "s_waitcnt lgkmcnt(2)\n"
    "v_mfma_f32_16x16x32_bf16 v[68:71], a[212:215], v[20:23], v[68:71]\n"
    "ds_read_b128 v[20:23], v9 offset:21504\n"
    "v_mfma_f32_16x16x32_bf16 v[56:59], a[216:219], v[24:27], v[56:59]\n"
    "v_mfma_f32_16x16x32_bf16 v[60:63], a[220:223], v[24:27], v[60:63]\n"
    "s_waitcnt lgkmcnt(2)\n"
    "v_mfma_f32_16x16x32_bf16 v[68:71], a[224:227], v[24:27], v[68:71]\n"
    "ds_read_b128 v[24:27], v9 offset:22528\n"
    "v_mfma_f32_16x16x32_bf16 v[56:59], a[228:231], v[28:31], v[56:59]\n"
    "v_mfma_f32_16x16x32_bf16 v[60:63], a[232:235], v[28:31], v[60:63]\n"
    "s_waitcnt lgkmcnt(2)\n"
    "v_mfma_f32_16x16x32_bf16 v[68:71], a[236:239], v[28:31], v[68:71]\n"
    "ds_read_b128 v[28:31], v9 offset:23552\n"
    "v_mfma_f32_16x16x32_bf16 v[56:59], a[240:243], v[16:19], v[56:59]\n"
    "v_mfma_f32_16x16x32_bf16 v[60:63], a[244:247], v[16:19], v[60:63]\n"
    "s_waitcnt lgkmcnt(2)\n"
    "v_mfma_f32_16x16x32_bf16 v[68:71], a[248:251], v[16:19], v[68:71]\n"
    "ds_read_b128 v[16:19], v9 offset:24576\n"
    "v_mfma_f32_16x16x32_bf16 v[56:59], a[252:255], v[20:23], v[56:59]\n"
    "v_mfma_f32_16x16x32_bf16 v[60:63], v[128:131], v[20:23], v[60:63]\n"
    "s_waitcnt lgkmcnt(2)\n"
    "s_waitcnt vmcnt(3)\n"
    "v_add_u32 v13, v5, v3\n"
    "v_mfma_f32_16x16x32_bf16 v[68:71], v[132:135], v[20:23], v[68:71]\n"
    "ds_read_b128 v[20:23], v9 offset:25600\n"
    "v_cmp_gt_i32 vcc, s71, v13\n"
    "v_mfma_f32_16x16x32_bf16 v[56:59], v[136:139], v[24:27], v[56:59]\n"
    "v_mfma_f32_16x16x32_bf16 v[60:63], v[140:143], v[24:27], v[60:63]\n"
    "s_waitcnt lgkmcnt(2)\n"
    "v_mfma_f32_16x16x32_bf16 v[68:71], v[144:147], v[24:27], v[68:71]\n"
    "ds_read_b128 v[24:27], v9 offset:26624\n"
    "s_cbranch_vccz .LgWO_dma1_69_%=\n"
    "v_mfma_f32_16x16x32_bf16 v[56:59], v[148:151], v[28:31], v[56:59]\n"
    "s_or_b64 exec, vcc, s[62:63]\n"
    "buffer_atomic_smax v5, v2, s[36:39], 0 offen sc0 sc1\n"
    "s_mov_b64 exec, -1\n"
    "v_mfma_f32_16x16x32_bf16 v[60:63], v[152:155], v[28:31], v[60:63]\n"
    "s_waitcnt lgkmcnt(2)\n"
    "v_mfma_f32_16x16x32_bf16 v[68:71], v[156:159], v[28:31], v[68:71]\n"
    "ds_read_b128 v[28:31], v9 offset:27648\n"
    "v_mfma_f32_16x16x32_bf16 v[56:59], v[160:163], v[16:19], v[56:59]\n"
    "v_mfma_f32_16x16x32_bf16 v[60:63], v[164:167], v[16:19], v[60:63]\n"
    "s_waitcnt lgkmcnt(2)\n"
    "v_mfma_f32_16x16x32_bf16 v[68:71], v[168:171], v[16:19], v[68:71]\n"
    "ds_read_b128 v[16:19], v9 offset:28672\n"
    "v_mfma_f32_16x16x32_bf16 v[56:59], v[172:175], v[20:23], v[56:59]\n"
    "v_mfma_f32_16x16x32_bf16 v[60:63], v[176:179], v[20:23], v[60:63]\n"
    "s_waitcnt lgkmcnt(2)\n"
    "v_mfma_f32_16x16x32_bf16 v[68:71], v[180:183], v[20:23], v[68:71]\n"
    "ds_read_b128 v[20:23], v9 offset:29696\n"
    "v_mfma_f32_16x16x32_bf16 v[56:59], v[184:187], v[24:27], v[56:59]\n"
    "v_mfma_f32_16x16x32_bf16 v[60:63], v[188:191], v[24:27], v[60:63]\n"
    "s_waitcnt lgkmcnt(2)\n"
    "v_mfma_f32_16x16x32_bf16 v[68:71], v[192:195], v[24:27], v[68:71]\n"
    "ds_read_b128 v[24:27], v9 offset:30720\n"
    "v_mfma_f32_16x16x32_bf16 v[56:59], v[196:199], v[28:31], v[56:59]\n"
    "v_mfma_f32_16x16x32_bf16 v[60:63], v[200:203], v[28:31], v[60:63]\n"
    "s_waitcnt lgkmcnt(2)\n"
    "v_mfma_f32_16x16x32_bf16 v[68:71], v[204:207], v[28:31], v[68:71]\n"
    "ds_read_b128 v[28:31], v9 offset:31744\n"
    "v_mfma_f32_16x16x32_bf16 v[56:59], v[208:211], v[16:19], v[56:59]\n"
    "s_waitcnt vmcnt(2)\n"
    "v_add_u32 v13, v11, v3\n"
    "v_mfma_f32_16x16x32_bf16 v[60:63], v[212:215], v[16:19], v[60:63]\n"
    "s_waitcnt lgkmcnt(2)\n"
    "v_cmp_gt_i32 vcc, s71, v13\n"
    "v_mfma_f32_16x16x32_bf16 v[68:71], v[216:219], v[16:19], v[68:71]\n"
    "v_mfma_f32_16x16x32_bf16 v[56:59], v[220:223], v[20:23], v[56:59]\n"
    "v_mfma_f32_16x16x32_bf16 v[60:63], v[224:227], v[20:23], v[60:63]\n"
    "s_waitcnt lgkmcnt(1)\n"
    "s_cbranch_vccz .LgWO_dma1_89_%=\n"
    "v_mfma_f32_16x16x32_bf16 v[68:71], v[228:231], v[20:23], v[68:71]\n"
    "s_or_b64 exec, vcc, s[62:63]\n"
    "buffer_atomic_smax v11, v2, s[36:39], 0 offen sc0 sc1\n"
    "s_mov_b64 exec, -1\n"
    "v_mfma_f32_16x16x32_bf16 v[56:59], v[232:235], v[24:27], v[56:59]\n"
    "v_mfma_f32_16x16x32_bf16 v[60:63], v[236:239], v[24:27], v[60:63]\n"
    "s_waitcnt lgkmcnt(0)\n"
    "v_mfma_f32_16x16x32_bf16 v[68:71], v[240:243], v[24:27], v[68:71]\n"
    "v_mfma_f32_16x16x32_bf16 v[56:59], v[244:247], v[28:31], v[56:59]\n"
    "v_mfma_f32_16x16x32_bf16 v[60:63], v[248:251], v[28:31], v[60:63]\n"
    "v_mfma_f32_16x16x32_bf16 v[68:71], v[252:255], v[28:31], v[68:71]\n"
    "v_cvt_pk_bf16_f32 v56, v56, v57\n"
    "v_cvt_pk_bf16_f32 v57, v58, v59\n"
    "s_nop 1\n"
    "v_cvt_pk_bf16_f32 v58, v60, v61\n"
    "v_cvt_pk_bf16_f32 v59, v62, v63\n"
    "buffer_store_dwordx4 v[56:59], v15, s[36:39], s73 offen offset:1024 sc1\n"
    "s_nop 0\n"
    "v_cvt_pk_bf16_f32 v68, v68, v69\n"
    "v_cvt_pk_bf16_f32 v69, v70, v71\n"
    "s_nop 0\n"
    "v_permlane16_swap_b32 v68, v70\n"
    "v_permlane16_swap_b32 v69, v71\n"
    "buffer_store_dwordx4 v[68:71], v4, s[36:39], s73 offen offset:1024 sc1\n"
    "v_mov_b32 v12, s68\n"
    "s_mov_b32 s73, 0x80000000\n"
    "s_mov_b32 s68, s71\n"
    "s_cmp_eq_u32 s68, 128\n"
    "s_cbranch_scc1 .LgWO_done_%=\n"
    "s_waitcnt vmcnt(3)\n"
    "v_add_u32 v13, v5, v3\n"
    "v_cmp_gt_i32 vcc, s71, v13\n"
    "s_cbranch_vccnz .LgWO_idle1_%=\n"
    "s_add_u32 m0, s59, 0\n"
    "s_nop 0\n"
    "buffer_load_dwordx4 v1, s[36:39], s76 offen sc1 lds\n"
    "s_add_u32 m0, s59, 4096\n"
    "s_add_u32 s76, s76, 16384\n"
    "buffer_load_dwordx4 v1, s[36:39], s76 offen sc1 lds\n"
    "s_add_u32 m0, s59, 8192\n"
    "s_add_u32 s76, s76, 16384\n"
    "buffer_load_dwordx4 v1, s[36:39], s76 offen sc1 lds\n"
    "s_add_u32 m0, s59, 12288\n"
    "s_add_u32 s76, s76, 16384\n"
    "buffer_load_dwordx4 v1, s[36:39], s76 offen sc1 lds\n"
    "s_add_u32 m0, s59, 16384\n"
    "s_add_u32 s76, s76, 16384\n"
    "buffer_load_dwordx4 v1, s[36:39], s76 offen sc1 lds\n"
    "s_add_u32 m0, s59, 20480\n"
    "s_add_u32 s76, s76, 16384\n"
    "buffer_load_dwordx4 v1, s[36:39], s76 offen sc1 lds\n"
    "s_add_u32 m0, s59, 24576\n"
    "s_add_u32 s76, s76, 16384\n"
    "buffer_load_dwordx4 v1, s[36:39], s76 offen sc1 lds\n"
    "s_add_u32 m0, s59, 28672\n"
    "s_add_u32 s76, s76, 16384\n"
    "buffer_load_dwordx4 v1, s[36:39], s76 offen sc1 lds\n"
    "s_waitcnt vmcnt(8)\n"
    "buffer_atomic_smax v12, v10, s[36:39], 0 offen sc1\n"
    "s_mov_b32 s69, 0x80000000\n"
    "s_mov_b32 s70, 0x80000000\n"
    "s_waitcnt vmcnt(11)\n"
    "s_add_u32 s71, s68, 1\n"
    "v_add_u32 v13, v11, v3\n"
    "v_cmp_gt_i32 vcc, s71, v13\n"
    "s_nop 0\n"
    "s_or_b64 exec, vcc, s[62:63]\n"
    "buffer_atomic_smax v11, v2, s[36:39], 0 offen sc0 sc1\n"
    "s_mov_b64 exec, -1\n"
    "s_waitcnt vmcnt(2)\n"
    "s_branch .LgWO_bar0_%=\n"
    ".LgWO_dma1_29_%=:\n"
    "v_mfma_f32_16x16x32_bf16 v[68:71], a[116:119], v[20:23], v[68:71]\n"
    "ds_read_b128 v[20:23], v9 offset:13312\n"
    "v_mfma_f32_16x16x32_bf16 v[56:59], a[120:123], v[24:27], v[56:59]\n"
    "s_add_u32 m0, s59, 0\n"
    "s_add_u32 s73, s73, s55\n"
    "s_waitcnt vmcnt(5)\n"
    "buffer_atomic_smax v12, v14, s[36:39], s69 offen sc1\n"
    "v_mfma_f32_16x16x32_bf16 v[60:63], a[124:127], v[24:27], v[60:63]\n"
    "s_waitcnt lgkmcnt(2)\n"
    "buffer_load_dwordx4 v1, s[36:39], s76 offen sc1 lds\n"
    "v_mfma_f32_16x16x32_bf16 v[68:71], a[128:131], v[24:27], v[68:71]\n"
    "ds_read_b128 v[24:27], v9 offset:14336\n"
    "v_mfma_f32_16x16x32_bf16 v[56:59], a[132:135], v[28:31], v[56:59]\n"
    "s_add_u32 m0, s59, 4096\n"
    "s_add_u32 s76, s76, 16384\n"
    "v_mfma_f32_16x16x32_bf16 v[60:63], a[136:139], v[28:31], v[60:63]\n"
    "s_waitcnt lgkmcnt(2)\n"
    "buffer_load_dwordx4 v1, s[36:39], s76 offen sc1 lds\n"
    "v_mfma_f32_16x16x32_bf16 v[68:71], a[140:143], v[28:31], v[68:71]\n"
    "ds_read_b128 v[28:31], v9 offset:15360\n"
    "v_mfma_f32_16x16x32_bf16 v[56:59], a[144:147], v[16:19], v[56:59]\n"
    "s_add_u32 m0, s59, 8192\n"
    "s_add_u32 s76, s76, 16384\n"
    "v_mfma_f32_16x16x32_bf16 v[60:63], a[148:151], v[16:19], v[60:63]\n"
    "s_waitcnt lgkmcnt(2)\n"
    "buffer_load_dwordx4 v1, s[36:39], s76 offen sc1 lds\n"
    "v_mfma_f32_16x16x32_bf16 v[68:71], a[152:155], v[16:19], v[68:71]\n"
    "ds_read_b128 v[16:19], v9 offset:16384\n"
    "v_mfma_f32_16x16x32_bf16 v[56:59], a[156:159], v[20:23], v[56:59]\n"
    "s_add_u32 m0, s59, 12288\n"
    "s_add_u32 s76, s76, 16384\n"
    "v_mfma_f32_16x16x32_bf16 v[60:63], a[160:163], v[20:23], v[60:63]\n"
    "s_waitcnt lgkmcnt(2)\n"
    "buffer_load_dwordx4 v1, s[36:39], s76 offen sc1 lds\n"
    "v_mfma_f32_16x16x32_bf16 v[68:71], a[164:167], v[20:23], v[68:71]\n"
    "ds_read_b128 v[20:23], v9 offset:17408\n"
    "v_mfma_f32_16x16x32_bf16 v[56:59], a[168:171], v[24:27], v[56:59]\n"
    "v_mfma_f32_16x16x32_bf16 v[60:63], a[172:175], v[24:27], v[60:63]\n"
    "s_waitcnt lgkmcnt(2)\n"
    "v_mfma_f32_16x16x32_bf16 v[68:71], a[176:179], v[24:27], v[68:71]\n"
    "ds_read_b128 v[24:27], v9 offset:18432\n"
    "v_mfma_f32_16x16x32_bf16 v[56:59], a[180:183], v[28:31], v[56:59]\n"
    "s_add_u32 m0, s59, 16384\n"
    "s_add_u32 s76, s76, 16384\n"
    "v_mfma_f32_16x16x32_bf16 v[60:63], a[184:187], v[28:31], v[60:63]\n"
    "s_waitcnt lgkmcnt(2)\n"
    "buffer_load_dwordx4 v1, s[36:39], s76 offen sc1 lds\n"
    "v_mfma_f32_16x16x32_bf16 v[68:71], a[188:191], v[28:31], v[68:71]\n"
    "ds_read_b128 v[28:31], v9 offset:19456\n"
    "v_mfma_f32_16x16x32_bf16 v[56:59], a[192:195], v[16:19], v[56:59]\n"
    "s_add_u32 m0, s59, 20480\n"
    "s_add_u32 s76, s76, 16384\n"
    "v_mfma_f32_16x16x32_bf16 v[60:63], a[196:199], v[16:19], v[60:63]\n"
    "s_waitcnt lgkmcnt(2)\n"
    "buffer_load_dwordx4 v1, s[36:39], s76 offen sc1 lds\n"
    "v_mfma_f32_16x16x32_bf16 v[68:71], a[200:203], v[16:19], v[68:71]\n"
    "ds_read_b128 v[16:19], v9 offset:20480\n"
    "s_waitcnt vmcnt(7)\n"
    "buffer_atomic_smax v12, v14, s[36:39], s70 offen sc1\n"
    "v_mfma_f32_16x16x32_bf16 v[56:59], a[204:207], v[20:23], v[56:59]\n"
    "s_add_u32 m0, s59, 24576\n"
    "s_add_u32 s76, s76, 16384\n"
    "v_mfma_f32_16x16x32_bf16 v[60:63], a[208:211], v[20:23], v[60:63]\n"
    "s_waitcnt lgkmcnt(2)\n"
    "buffer_load_dwordx4 v1, s[36:39], s76 offen sc1 lds\n"
    "v_mfma_f32_16x16x32_bf16 v[68:71], a[212:215], v[20:23], v[68:71]\n"
    "ds_read_b128 v[20:23], v9 offset:21504\n"
    "v_mfma_f32_16x16x32_bf16 v[56:59], a[216:219], v[24:27], v[56:59]\n"
    "s_add_u32 m0, s59, 28672\n"
    "s_add_u32 s76, s76, 16384\n"
    "v_mfma_f32_16x16x32_bf16 v[60:63], a[220:223], v[24:27], v[60:63]\n"
    "s_waitcnt lgkmcnt(2)\n"
    "buffer_load_dwordx4 v1, s[36:39], s76 offen sc1 lds\n"
    "v_mfma_f32_16x16x32_bf16 v[68:71], a[224:227], v[24:27], v[68:71]\n"
    "ds_read_b128 v[24:27], v9 offset:22528\n"
    "v_mfma_f32_16x16x32_bf16 v[56:59], a[228:231], v[28:31], v[56:59]\n"
    "v_mfma_f32_16x16x32_bf16 v[60:63], a[232:235], v[28:31], v[60:63]\n"
    "s_waitcnt lgkmcnt(2)\n"
    "v_mfma_f32_16x16x32_bf16 v[68:71], a[236:239], v[28:31], v[68:71]\n"
    "ds_read_b128 v[28:31], v9 offset:23552\n"
    "v_mfma_f32_16x16x32_bf16 v[56:59], a[240:243], v[16:19], v[56:59]\n"
    "v_mfma_f32_16x16x32_bf16 v[60:63], a[244:247], v[16:19], v[60:63]\n"
    "s_waitcnt lgkmcnt(2)\n"
    "v_mfma_f32_16x16x32_bf16 v[68:71], a[248:251], v[16:19], v[68:71]\n"
    "ds_read_b128 v[16:19], v9 offset:24576\n"
    "v_mfma_f32_16x16x32_bf16 v[56:59], a[252:255], v[20:23], v[56:59]\n"
    "v_mfma_f32_16x16x32_bf16 v[60:63], v[128:131], v[20:23], v[60:63]\n"
    "s_waitcnt lgkmcnt(2)\n"
    "v_mfma_f32_16x16x32_bf16 v[68:71], v[132:135], v[20:23], v[68:71]\n"
    "ds_read_b128 v[20:23], v9 offset:25600\n"
    "v_mfma_f32_16x16x32_bf16 v[56:59], v[136:139], v[24:27], v[56:59]\n"
    "v_mfma_f32_16x16x32_bf16 v[60:63], v[140:143], v[24:27], v[60:63]\n"
    "s_waitcnt lgkmcnt(2)\n"
    "v_mfma_f32_16x16x32_bf16 v[68:71], v[144:147], v[24:27], v[68:71]\n"
    "ds_read_b128 v[24:27], v9 offset:26624\n"
    "v_mfma_f32_16x16x32_bf16 v[56:59], v[148:151], v[28:31], v[56:59]\n"
    "v_mfma_f32_16x16x32_bf16 v[60:63], v[152:155], v[28:31], v[60:63]\n"
    "s_waitcnt lgkmcnt(2)\n"
    "v_mfma_f32_16x16x32_bf16 v[68:71], v[156:159], v[28:31], v[68:71]\n"
    "ds_read_b128 v[28:31], v9 offset:27648\n"
    "v_mfma_f32_16x16x32_bf16 v[56:59], v[160:163], v[16:19], v[56:59]\n"
    "v_mfma_f32_16x16x32_bf16 v[60:63], v[164:167], v[16:19], v[60:63]\n"
    "s_waitcnt lgkmcnt(2)\n"
    "v_mfma_f32_16x16x32_bf16 v[68:71], v[168:171], v[16:19], v[68:71]\n"
    "ds_read_b128 v[16:19], v9 offset:28672\n"
    "v_mfma_f32_16x16x32_bf16 v[56:59], v[172:175], v[20:23], v[56:59]\n"
    "v_mfma_f32_16x16x32_bf16 v[60:63], v[176:179], v[20:23], v[60:63]\n"
    "s_waitcnt lgkmcnt(2)\n"
    "v_mfma_f32_16x16x32_bf16 v[68:71], v[180:183], v[20:23], v[68:71]\n"
    "ds_read_b128 v[20:23], v9 offset:29696\n"
    "v_mfma_f32_16x16x32_bf16 v[56:59], v[184:187], v[24:27], v[56:59]\n"
    "v_mfma_f32_16x16x32_bf16 v[60:63], v[188:191], v[24:27], v[60:63]\n"
    "s_waitcnt lgkmcnt(2)\n"
    "v_mfma_f32_16x16x32_bf16 v[68:71], v[192:195], v[24:27], v[68:71]\n"
    "ds_read_b128 v[24:27], v9 offset:30720\n"
    "v_mfma_f32_16x16x32_bf16 v[56:59], v[196:199], v[28:31], v[56:59]\n"
    "v_mfma_f32_16x16x32_bf16 v[60:63], v[200:203], v[28:31], v[60:63]\n"
    "s_waitcnt lgkmcnt(2)\n"
    "v_mfma_f32_16x16x32_bf16 v[68:71], v[204:207], v[28:31], v[68:71]\n"
    "ds_read_b128 v[28:31], v9 offset:31744\n"
    "v_mfma_f32_16x16x32_bf16 v[56:59], v[208:211], v[16:19], v[56:59]\n"
    "v_mfma_f32_16x16x32_bf16 v[60:63], v[212:215], v[16:19], v[60:63]\n"
    "s_waitcnt lgkmcnt(2)\n"
    "v_mfma_f32_16x16x32_bf16 v[68:71], v[216:219], v[16:19], v[68:71]\n"
    "v_mfma_f32_16x16x32_bf16 v[56:59], v[220:223], v[20:23], v[56:59]\n"
    "v_mfma_f32_16x16x32_bf16 v[60:63], v[224:227], v[20:23], v[60:63]\n"
    "s_waitcnt lgkmcnt(1)\n"
    "v_mfma_f32_16x16x32_bf16 v[68:71], v[228:231], v[20:23], v[68:71]\n"
    "v_mfma_f32_16x16x32_bf16 v[56:59], v[232:235], v[24:27], v[56:59]\n"
    "v_mfma_f32_16x16x32_bf16 v[60:63], v[236:239], v[24:27], v[60:63]\n"
    "s_waitcnt lgkmcnt(0)\n"
    "v_mfma_f32_16x16x32_bf16 v[68:71], v[240:243], v[24:27], v[68:71]\n"
    "v_mfma_f32_16x16x32_bf16 v[56:59], v[244:247], v[28:31], v[56:59]\n"
    "v_mfma_f32_16x16x32_bf16 v[60:63], v[248:251], v[28:31], v[60:63]\n"
    "v_mfma_f32_16x16x32_bf16 v[68:71], v[252:255], v[28:31], v[68:71]\n"
    "v_cvt_pk_bf16_f32 v56, v56, v57\n"
    "v_cvt_pk_bf16_f32 v57, v58, v59\n"
    "s_nop 1\n"
    "v_cvt_pk_bf16_f32 v58, v60, v61\n"
    "v_cvt_pk_bf16_f32 v59, v62, v63\n"
    "buffer_store_dwordx4 v[56:59], v15, s[36:39], s73 offen offset:1024 sc1\n"
    "s_nop 0\n"
    "v_cvt_pk_bf16_f32 v68, v68, v69\n"
    "v_cvt_pk_bf16_f32 v69, v70, v71\n"
    "s_nop 0\n"
    "v_permlane16_swap_b32 v68, v70\n"
    "v_permlane16_swap_b32 v69, v71\n"
    "buffer_store_dwordx4 v[68:71], v4, s[36:39], s73 offen offset:1024 sc1\n"
    "v_mov_b32 v12, s68\n"
    "s_mov_b32 s73, 0x80000000\n"
    "s_mov_b32 s68, s71\n"
    "s_mov_b32 s69, 0\n"
    "s_mov_b32 s70, 0x80000000\n"
    "s_waitcnt vmcnt(13)\n"
    "s_add_u32 s71, s68, 1\n"
    "v_add_u32 v13, v11, v3\n"
    "v_cmp_gt_i32 vcc, s71, v13\n"
    "s_nop 0\n"
    "s_or_b64 exec, vcc, s[62:63]\n"
    "buffer_atomic_smax v11, v2, s[36:39], 0 offen sc0 sc1\n"
    "s_mov_b64 exec, -1\n"
    "s_waitcnt vmcnt(3)\n"
    "s_branch .LgWO_bar0_%=\n"
    ".LgWO_dma1_49_%=:\n"
    "v_mfma_f32_16x16x32_bf16 v[60:63], a[196:199], v[16:19], v[60:63]\n"
    "s_waitcnt lgkmcnt(2)\n"
    "v_mfma_f32_16x16x32_bf16 v[68:71], a[200:203], v[16:19], v[68:71]\n"
    "ds_read_b128 v[16:19], v9 offset:20480\n"
    "s_waitcnt vmcnt(2)\n"
    "buffer_atomic_smax v12, v14, s[36:39], s70 offen sc1\n"
    "v_mfma_f32_16x16x32_bf16 v[56:59], a[204:207], v[20:23], v[56:59]\n"
    "s_add_u32 m0, s59, 0\n"
    "v_mfma_f32_16x16x32_bf16 v[60:63], a[208:211], v[20:23], v[60:63]\n"
    "s_waitcnt lgkmcnt(2)\n"
    "buffer_load_dwordx4 v1, s[36:39], s76 offen sc1 lds\n"
    "v_mfma_f32_16x16x32_bf16 v[68:71], a[212:215], v[20:23], v[68:71]\n"
    "ds_read_b128 v[20:23], v9 offset:21504\n"
    "v_mfma_f32_16x16x32_bf16 v[56:59], a[216:219], v[24:27], v[56:59]\n"
    "s_add_u32 m0, s59, 4096\n"
    "s_add_u32 s76, s76, 16384\n"
    "v_mfma_f32_16x16x32_bf16 v[60:63], a[220:223], v[24:27], v[60:63]\n"
    "s_waitcnt lgkmcnt(2)\n"
    "buffer_load_dwordx4 v1, s[36:39], s76 offen sc1 lds\n"
    "v_mfma_f32_16x16x32_bf16 v[68:71], a[224:227], v[24:27], v[68:71]\n"
    "ds_read_b128 v[24:27], v9 offset:22528\n"
    "v_mfma_f32_16x16x32_bf16 v[56:59], a[228:231], v[28:31], v[56:59]\n"
    "s_add_u32 m0, s59, 8192\n"
    "s_add_u32 s76, s76, 16384\n"
    "v_mfma_f32_16x16x32_bf16 v[60:63], a[232:235], v[28:31], v[60:63]\n"
    "s_waitcnt lgkmcnt(2)\n"
    "buffer_load_dwordx4 v1, s[36:39], s76 offen sc1 lds\n"
    "v_mfma_f32_16x16x32_bf16 v[68:71], a[236:239], v[28:31], v[68:71]\n"
    "ds_read_b128 v[28:31], v9 offset:23552\n"
    "v_mfma_f32_16x16x32_bf16 v[56:59], a[240:243], v[16:19], v[56:59]\n"
    "s_add_u32 m0, s59, 12288\n"
    "s_add_u32 s76, s76, 16384\n"
    "v_mfma_f32_16x16x32_bf16 v[60:63], a[244:247], v[16:19], v[60:63]\n"
    "s_waitcnt lgkmcnt(2)\n"
    "buffer_load_dwordx4 v1, s[36:39], s76 offen sc1 lds\n"
    "v_mfma_f32_16x16x32_bf16 v[68:71], a[248:251], v[16:19], v[68:71]\n"
    "ds_read_b128 v[16:19], v9 offset:24576\n"
    "v_mfma_f32_16x16x32_bf16 v[56:59], a[252:255], v[20:23], v[56:59]\n"
    "v_mfma_f32_16x16x32_bf16 v[60:63], v[128:131], v[20:23], v[60:63]\n"
    "s_waitcnt lgkmcnt(2)\n"
    "v_mfma_f32_16x16x32_bf16 v[68:71], v[132:135], v[20:23], v[68:71]\n"
    "ds_read_b128 v[20:23], v9 offset:25600\n"
    "v_mfma_f32_16x16x32_bf16 v[56:59], v[136:139], v[24:27], v[56:59]\n"
    "s_add_u32 m0, s59, 16384\n"
    "s_add_u32 s76, s76, 16384\n"
    "v_mfma_f32_16x16x32_bf16 v[60:63], v[140:143], v[24:27], v[60:63]\n"
    "s_waitcnt lgkmcnt(2)\n"
    "buffer_load_dwordx4 v1, s[36:39], s76 offen sc1 lds\n"
    "v_mfma_f32_16x16x32_bf16 v[68:71], v[144:147], v[24:27], v[68:71]\n"
    "ds_read_b128 v[24:27], v9 offset:26624\n"
    "v_mfma_f32_16x16x32_bf16 v[56:59], v[148:151], v[28:31], v[56:59]\n"
    "s_add_u32 m0, s59, 20480\n"
    "s_add_u32 s76, s76, 16384\n"
    "v_mfma_f32_16x16x32_bf16 v[60:63], v[152:155], v[28:31], v[60:63]\n"
    "s_waitcnt lgkmcnt(2)\n"
    "buffer_load_dwordx4 v1, s[36:39], s76 offen sc1 lds\n"
    "v_mfma_f32_16x16x32_bf16 v[68:71], v[156:159], v[28:31], v[68:71]\n"
    "ds_read_b128 v[28:31], v9 offset:27648\n"
    "v_mfma_f32_16x16x32_bf16 v[56:59], v[160:163], v[16:19], v[56:59]\n"
    "s_add_u32 m0, s59, 24576\n"
    "s_add_u32 s76, s76, 16384\n"
    "v_mfma_f32_16x16x32_bf16 v[60:63], v[164:167], v[16:19], v[60:63]\n"
    "s_waitcnt lgkmcnt(2)\n"
    "buffer_load_dwordx4 v1, s[36:39], s76 offen sc1 lds\n"
    "v_mfma_f32_16x16x32_bf16 v[68:71], v[168:171], v[16:19], v[68:71]\n"
    "ds_read_b128 v[16:19], v9 offset:28672\n"
    "v_mfma_f32_16x16x32_bf16 v[56:59], v[172:175], v[20:23], v[56:59]\n"
    "s_add_u32 m0, s59, 28672\n"
    "s_add_u32 s76, s76, 16384\n"
    "v_mfma_f32_16x16x32_bf16 v[60:63], v[176:179], v[20:23], v[60:63]\n"
    "s_waitcnt lgkmcnt(2)\n"
    "buffer_load_dwordx4 v1, s[36:39], s76 offen sc1 lds\n"
    "v_mfma_f32_16x16x32_bf16 v[68:71], v[180:183], v[20:23], v[68:71]\n"
    "ds_read_b128 v[20:23], v9 offset:29696\n"
    "v_mfma_f32_16x16x32_bf16 v[56:59], v[184:187], v[24:27], v[56:59]\n"
    "v_mfma_f32_16x16x32_bf16 v[60:63], v[188:191], v[24:27], v[60:63]\n"
    "s_waitcnt lgkmcnt(2)\n"
    "v_mfma_f32_16x16x32_bf16 v[68:71], v[192:195], v[24:27], v[68:71]\n"
    "ds_read_b128 v[24:27], v9 offset:30720\n"
    "v_mfma_f32_16x16x32_bf16 v[56:59], v[196:199], v[28:31], v[56:59]\n"
    "v_mfma_f32_16x16x32_bf16 v[60:63], v[200:203], v[28:31], v[60:63]\n"
    "s_waitcnt lgkmcnt(2)\n"
    "v_mfma_f32_16x16x32_bf16 v[68:71], v[204:207], v[28:31], v[68:71]\n"
    "ds_read_b128 v[28:31], v9 offset:31744\n"
    "v_mfma_f32_16x16x32_bf16 v[56:59], v[208:211], v[16:19], v[56:59]\n"
    "v_mfma_f32_16x16x32_bf16 v[60:63], v[212:215], v[16:19], v[60:63]\n"
    "s_waitcnt lgkmcnt(2)\n"
    "v_mfma_f32_16x16x32_bf16 v[68:71], v[216:219], v[16:19], v[68:71]\n"
    "v_mfma_f32_16x16x32_bf16 v[56:59], v[220:223], v[20:23], v[56:59]\n"
    "v_mfma_f32_16x16x32_bf16 v[60:63], v[224:227], v[20:23], v[60:63]\n"
    "s_waitcnt lgkmcnt(1)\n"
    "v_mfma_f32_16x16x32_bf16 v[68:71], v[228:231], v[20:23], v[68:71]\n"
    "v_mfma_f32_16x16x32_bf16 v[56:59], v[232:235], v[24:27], v[56:59]\n"
    "v_mfma_f32_16x16x32_bf16 v[60:63], v[236:239], v[24:27], v[60:63]\n"
    "s_waitcnt lgkmcnt(0)\n"
    "v_mfma_f32_16x16x32_bf16 v[68:71], v[240:243], v[24:27], v[68:71]\n"
    "v_mfma_f32_16x16x32_bf16 v[56:59], v[244:247], v[28:31], v[56:59]\n"
    "v_mfma_f32_16x16x32_bf16 v[60:63], v[248:251], v[28:31], v[60:63]\n"
    "v_mfma_f32_16x16x32_bf16 v[68:71], v[252:255], v[28:31], v[68:71]\n"
    "v_cvt_pk_bf16_f32 v56, v56, v57\n"
    "v_cvt_pk_bf16_f32 v57, v58, v59\n"
    "s_nop 1\n"
    "v_cvt_pk_bf16_f32 v58, v60, v61\n"
    "v_cvt_pk_bf16_f32 v59, v62, v63\n"
    "buffer_store_dwordx4 v[56:59], v15, s[36:39], s73 offen offset:1024 sc1\n"
    "s_nop 0\n"
    "v_cvt_pk_bf16_f32 v68, v68, v69\n"
    "v_cvt_pk_bf16_f32 v69, v70, v71\n"
    "s_nop 0\n"
    "v_permlane16_swap_b32 v68, v70\n"
    "v_permlane16_swap_b32 v69, v71\n"
    "buffer_store_dwordx4 v[68:71], v4, s[36:39], s73 offen offset:1024 sc1\n"
    "v_mov_b32 v12, s68\n"
    "s_mov_b32 s73, 0x80000000\n"
    "s_mov_b32 s68, s71\n"
    "s_mov_b32 s69, 0\n"
    "s_mov_b32 s70, 0x80000000\n"
    "s_waitcnt vmcnt(12)\n"
    "s_add_u32 s71, s68, 1\n"
    "v_add_u32 v13, v11, v3\n"
    "v_cmp_gt_i32 vcc, s71, v13\n"
    "s_nop 0\n"
    "s_or_b64 exec, vcc, s[62:63]\n"
    "buffer_atomic_smax v11, v2, s[36:39], 0 offen sc0 sc1\n"
    "s_mov_b64 exec, -1\n"
    "s_waitcnt vmcnt(3)\n"
    "s_branch .LgWO_bar0_%=\n"
    ".LgWO_dma1_69_%=:\n"
    "v_mfma_f32_16x16x32_bf16 v[56:59], v[148:151], v[28:31], v[56:59]\n"
    "s_add_u32 m0, s59, 0\n"
    "v_mfma_f32_16x16x32_bf16 v[60:63], v[152:155], v[28:31], v[60:63]\n"
    "s_waitcnt lgkmcnt(2)\n"
    "buffer_load_dwordx4 v1, s[36:39], s76 offen sc1 lds\n"
    "v_mfma_f32_16x16x32_bf16 v[68:71], v[156:159], v[28:31], v[68:71]\n"
    "ds_read_b128 v[28:31], v9 offset:27648\n"
    "v_mfma_f32_16x16x32_bf16 v[56:59], v[160:163], v[16:19], v[56:59]\n"
    "s_add_u32 m0, s59, 4096\n"
    "s_add_u32 s76, s76, 16384\n"
    "v_mfma_f32_16x16x32_bf16 v[60:63], v[164:167], v[16:19], v[60:63]\n"
    "s_waitcnt lgkmcnt(2)\n"
    "buffer_load_dwordx4 v1, s[36:39], s76 offen sc1 lds\n"
    "v_mfma_f32_16x16x32_bf16 v[68:71], v[168:171], v[16:19], v[68:71]\n"
    "ds_read_b128 v[16:19], v9 offset:28672\n"
    "v_mfma_f32_16x16x32_bf16 v[56:59], v[172:175], v[20:23], v[56:59]\n"
    "v_mfma_f32_16x16x32_bf16 v[60:63], v[176:179], v[20:23], v[60:63]\n"
    "s_waitcnt lgkmcnt(2)\n"
    "v_mfma_f32_16x16x32_bf16 v[68:71], v[180:183], v[20:23], v[68:71]\n"
    "ds_read_b128 v[20:23], v9 offset:29696\n"
    "v_mfma_f32_16x16x32_bf16 v[56:59], v[184:187], v[24:27], v[56:59]\n"
    "s_add_u32 m0, s59, 8192\n"
    "s_add_u32 s76, s76, 16384\n"
    "v_mfma_f32_16x16x32_bf16 v[60:63], v[188:191], v[24:27], v[60:63]\n"
    "s_waitcnt lgkmcnt(2)\n"
    "buffer_load_dwordx4 v1, s[36:39], s76 offen sc1 lds\n"
    "v_mfma_f32_16x16x32_bf16 v[68:71], v[192:195], v[24:27], v[68:71]\n"
    "ds_read_b128 v[24:27], v9 offset:30720\n"
    "v_mfma_f32_16x16x32_bf16 v[56:59], v[196:199], v[28:31], v[56:59]\n"
    "s_add_u32 m0, s59, 12288\n"
    "s_add_u32 s76, s76, 16384\n"
    "v_mfma_f32_16x16x32_bf16 v[60:63], v[200:203], v[28:31], v[60:63]\n"
    "s_waitcnt lgkmcnt(2)\n"
    "buffer_load_dwordx4 v1, s[36:39], s76 offen sc1 lds\n"
    "v_mfma_f32_16x16x32_bf16 v[68:71], v[204:207], v[28:31], v[68:71]\n"
    "ds_read_b128 v[28:31], v9 offset:31744\n"
    "v_mfma_f32_16x16x32_bf16 v[56:59], v[208:211], v[16:19], v[56:59]\n"
    "v_mfma_f32_16x16x32_bf16 v[60:63], v[212:215], v[16:19], v[60:63]\n"
    "s_waitcnt lgkmcnt(2)\n"
    "s_add_u32 m0, s59, 16384\n"
    "s_add_u32 s76, s76, 16384\n"
    "v_mfma_f32_16x16x32_bf16 v[68:71], v[216:219], v[16:19], v[68:71]\n"
    "buffer_load_dwordx4 v1, s[36:39], s76 offen sc1 lds\n"
    "v_mfma_f32_16x16x32_bf16 v[56:59], v[220:223], v[20:23], v[56:59]\n"
    "v_mfma_f32_16x16x32_bf16 v[60:63], v[224:227], v[20:23], v[60:63]\n"
    "s_waitcnt lgkmcnt(1)\n"
    "v_mfma_f32_16x16x32_bf16 v[68:71], v[228:231], v[20:23], v[68:71]\n"
    "v_mfma_f32_16x16x32_bf16 v[56:59], v[232:235], v[24:27], v[56:59]\n"
    "v_mfma_f32_16x16x32_bf16 v[60:63], v[236:239], v[24:27], v[60:63]\n"
    "s_waitcnt lgkmcnt(0)\n"
    "v_mfma_f32_16x16x32_bf16 v[68:71], v[240:243], v[24:27], v[68:71]\n"
    "v_mfma_f32_16x16x32_bf16 v[56:59], v[244:247], v[28:31], v[56:59]\n"
    "s_add_u32 m0, s59, 20480\n"
    "s_add_u32 s76, s76, 16384\n"
    "v_mfma_f32_16x16x32_bf16 v[60:63], v[248:251], v[28:31], v[60:63]\n"
    "buffer_load_dwordx4 v1, s[36:39], s76 offen sc1 lds\n"
    "v_mfma_f32_16x16x32_bf16 v[68:71], v[252:255], v[28:31], v[68:71]\n"
    "v_cvt_pk_bf16_f32 v56, v56, v57\n"
    "v_cvt_pk_bf16_f32 v57, v58, v59\n"
    "s_nop 1\n"
    "v_cvt_pk_bf16_f32 v58, v60, v61\n"
    "v_cvt_pk_bf16_f32 v59, v62, v63\n"
    "buffer_store_dwordx4 v[56:59], v15, s[36:39], s73 offen offset:1024 sc1\n"
    "s_nop 0\n"
    "v_cvt_pk_bf16_f32 v68, v68, v69\n"
    "v_cvt_pk_bf16_f32 v69, v70, v71\n"
    "s_nop 0\n"
    "v_permlane16_swap_b32 v68, v70\n"
    "v_permlane16_swap_b32 v69, v71\n"
    "buffer_store_dwordx4 v[68:71], v4, s[36:39], s73 offen offset:1024 sc1\n"
    "v_mov_b32 v12, s68\n"
    "s_mov_b32 s73, 0x80000000\n"
    "s_mov_b32 s68, s71\n"
    "s_add_u32 m0, s59, 24576\n"
    "s_add_u32 s76, s76, 16384\n"
    "buffer_load_dwordx4 v1, s[36:39], s76 offen sc1 lds\n"
    "s_add_u32 m0, s59, 28672\n"
    "s_add_u32 s76, s76, 16384\n"
    "buffer_load_dwordx4 v1, s[36:39], s76 offen sc1 lds\n"
    "s_waitcnt vmcnt(2)\n"
    "buffer_atomic_smax v12, v10, s[36:39], 0 offen sc1\n"
    "s_mov_b32 s69, 0x80000000\n"
    "s_mov_b32 s70, 0x80000000\n"
    "s_waitcnt vmcnt(12)\n"
    "s_add_u32 s71, s68, 1\n"
    "v_add_u32 v13, v11, v3\n"
    "v_cmp_gt_i32 vcc, s71, v13\n"
    "s_nop 0\n"
    "s_or_b64 exec, vcc, s[62:63]\n"
    "buffer_atomic_smax v11, v2, s[36:39], 0 offen sc0 sc1\n"
    "s_mov_b64 exec, -1\n"
    "s_waitcnt vmcnt(2)\n"
    "s_branch .LgWO_bar0_%=\n"
    ".LgWO_dma1_89_%=:\n"
    "v_mfma_f32_16x16x32_bf16 v[68:71], v[228:231], v[20:23], v[68:71]\n"
    "v_mfma_f32_16x16x32_bf16 v[56:59], v[232:235], v[24:27], v[56:59]\n"
    "s_add_u32 m0, s59, 0\n"
    "v_mfma_f32_16x16x32_bf16 v[60:63], v[236:239], v[24:27], v[60:63]\n"
    "s_waitcnt lgkmcnt(0)\n"
    "buffer_load_dwordx4 v1, s[36:39], s76 offen sc1 lds\n"
    "v_mfma_f32_16x16x32_bf16 v[68:71], v[240:243], v[24:27], v[68:71]\n"
    "v_mfma_f32_16x16x32_bf16 v[56:59], v[244:247], v[28:31], v[56:59]\n"
    "v_mfma_f32_16x16x32_bf16 v[60:63], v[248:251], v[28:31], v[60:63]\n"
    "v_mfma_f32_16x16x32_bf16 v[68:71], v[252:255], v[28:31], v[68:71]\n"
    "v_cvt_pk_bf16_f32 v56, v56, v57\n"
    "v_cvt_pk_bf16_f32 v57, v58, v59\n"
    "s_nop 1\n"
    "v_cvt_pk_bf16_f32 v58, v60, v61\n"
    "v_cvt_pk_bf16_f32 v59, v62, v63\n"
    "buffer_store_dwordx4 v[56:59], v15, s[36:39], s73 offen offset:1024 sc1\n"
    "s_nop 0\n"
    "v_cvt_pk_bf16_f32 v68, v68, v69\n"
    "v_cvt_pk_bf16_f32 v69, v70, v71\n"
    "s_nop 0\n"
    "v_permlane16_swap_b32 v68, v70\n"
    "v_permlane16_swap_b32 v69, v71\n"
    "buffer_store_dwordx4 v[68:71], v4, s[36:39], s73 offen offset:1024 sc1\n"
    "v_mov_b32 v12, s68\n"
    "s_mov_b32 s73, 0x80000000\n"
    "s_mov_b32 s68, s71\n"
    "s_add_u32 m0, s59, 4096\n"
    "s_add_u32 s76, s76, 16384\n"
    "buffer_load_dwordx4 v1, s[36:39], s76 offen sc1 lds\n"
    "s_add_u32 m0, s59, 8192\n"
    "s_add_u32 s76, s76, 16384\n"
    "buffer_load_dwordx4 v1, s[36:39], s76 offen sc1 lds\n"
    "s_add_u32 m0, s59, 12288\n"
    "s_add_u32 s76, s76, 16384\n"
    "buffer_load_dwordx4 v1, s[36:39], s76 offen sc1 lds\n"
    "s_add_u32 m0, s59, 16384\n"
    "s_add_u32 s76, s76, 16384\n"
    "buffer_load_dwordx4 v1, s[36:39], s76 offen sc1 lds\n"
    "s_add_u32 m0, s59, 20480\n"
    "s_add_u32 s76, s76, 16384\n"
    "buffer_load_dwordx4 v1, s[36:39], s76 offen sc1 lds\n"
    "s_add_u32 m0, s59, 24576\n"
    "s_add_u32 s76, s76, 16384\n"
    "buffer_load_dwordx4 v1, s[36:39], s76 offen sc1 lds\n"
    "s_add_u32 m0, s59, 28672\n"
    "s_add_u32 s76, s76, 16384\n"
    "buffer_load_dwordx4 v1, s[36:39], s76 offen sc1 lds\n"
    "s_waitcnt vmcnt(7)\n"
    "buffer_atomic_smax v12, v10, s[36:39], 0 offen sc1\n"
    "s_mov_b32 s69, 0x80000000\n"
    "s_mov_b32 s70, 0x80000000\n"
    "s_waitcnt vmcnt(11)\n"
    "s_add_u32 s71, s68, 1\n"
    "v_add_u32 v13, v11, v3\n"
    "v_cmp_gt_i32 vcc, s71, v13\n"
    "s_nop 0\n"
    "s_or_b64 exec, vcc, s[62:63]\n"
    "buffer_atomic_smax v11, v2, s[36:39], 0 offen sc0 sc1\n"
    "s_mov_b64 exec, -1\n"
    "s_waitcnt vmcnt(2)\n"
    "s_branch .LgWO_bar0_%=\n"
    ".LgWO_idle1_%=:\n"
    "s_or_b64 exec, vcc, s[62:63]\n"
    "buffer_atomic_smax v5, v2, s[36:39], 0 offen sc0 sc1\n"
    "s_mov_b64 exec, -1\n"
    "s_waitcnt vmcnt(1)\n"
    "buffer_atomic_smax v12, v10, s[36:39], 0 offen sc1\n"
    "s_branch .LgWO_block1_1_%=\n"
    ".LgWO_block1_%=:\n"
    "v_add_u32 v13, v11, v3\n"
    "v_cmp_gt_i32 vcc, s71, v13\n"
    "s_nop 0\n"
    "s_or_b64 exec, vcc, s[62:63]\n"
    "buffer_atomic_smax v11, v2, s[36:39], 0 offen sc0 sc1\n"
    "s_mov_b64 exec, -1\n"
    "s_sleep 3\n"
    "s_waitcnt vmcnt(1)\n"
    ".LgWO_block1_0_%=:\n"
    "v_add_u32 v13, v5, v3\n"
    "v_cmp_gt_i32 vcc, s71, v13\n"
    "s_cbranch_vccz .LgWO_go1_%=\n"
    "v_add_u32 v13, v5, v3\n"
    "v_cmp_gt_i32 vcc, s71, v13\n"
    "s_nop 0\n"
    "s_or_b64 exec, vcc, s[62:63]\n"
    "buffer_atomic_smax v5, v2, s[36:39], 0 offen sc0 sc1\n"
    "s_mov_b64 exec, -1\n"
    "s_sleep 3\n"
    "s_waitcnt vmcnt(1)\n"
    ".LgWO_block1_1_%=:\n"
    "v_add_u32 v13, v11, v3\n"
    "v_cmp_gt_i32 vcc, s71, v13\n"
    "s_cbranch_vccz .LgWO_go1_%=\n"
    "s_sub_u32 s72, s72, 1\n"
    "s_cbranch_scc0 .LgWO_block1_%=\n"
    "s_mov_b32 s72, 0\n"
    ".LgWO_go1_%=:\n"
    "s_lshr_b32 s76, s71, 1\n"
    "s_and_b32 s76, s76, s54\n"
    "s_mul_i32 s76, s76, s53\n"
    "s_add_u32 s76, s76, s52\n"
    "s_add_u32 m0, s59, 0\n"
    "s_nop 0\n"
    "buffer_load_dwordx4 v1, s[36:39], s76 offen sc1 lds\n"
    "s_add_u32 m0, s59, 4096\n"
    "s_add_u32 s76, s76, 16384\n"
    "buffer_load_dwordx4 v1, s[36:39], s76 offen sc1 lds\n"
    "s_add_u32 m0, s59, 8192\n"
    "s_add_u32 s76, s76, 16384\n"
    "buffer_load_dwordx4 v1, s[36:39], s76 offen sc1 lds\n"
    "s_add_u32 m0, s59, 12288\n"
    "s_add_u32 s76, s76, 16384\n"
    "buffer_load_dwordx4 v1, s[36:39], s76 offen sc1 lds\n"
    "s_add_u32 m0, s59, 16384\n"
    "s_add_u32 s76, s76, 16384\n"
    "buffer_load_dwordx4 v1, s[36:39], s76 offen sc1 lds\n"
    "s_add_u32 m0, s59, 20480\n"
    "s_add_u32 s76, s76, 16384\n"
    "buffer_load_dwordx4 v1, s[36:39], s76 offen sc1 lds\n"
    "s_add_u32 m0, s59, 24576\n"
    "s_add_u32 s76, s76, 16384\n"
    "buffer_load_dwordx4 v1, s[36:39], s76 offen sc1 lds\n"
    "s_add_u32 m0, s59, 28672\n"
    "s_add_u32 s76, s76, 16384\n"
    "buffer_load_dwordx4 v1, s[36:39], s76 offen sc1 lds\n"
    "s_mov_b32 s69, 0x80000000\n"
    "s_mov_b32 s70, 0x80000000\n"
    "s_waitcnt vmcnt(8)\n"
    "s_add_u32 s71, s68, 1\n"
    "v_add_u32 v13, v11, v3\n"
    "v_cmp_gt_i32 vcc, s71, v13\n"
    "s_nop 0\n"
    "s_or_b64 exec, vcc, s[62:63]\n"
    "buffer_atomic_smax v11, v2, s[36:39], 0 offen sc0 sc1\n"
    "s_mov_b64 exec, -1\n"
    "s_waitcnt vmcnt(1)\n"
    "s_branch .LgWO_bar0_%=\n"
    ".LgWO_bar1_%=:\n"
    "s_barrier\n"
    "ds_read_b128 v[16:19], v9 offset:0\n"
    "ds_read_b128 v[20:23], v9 offset:1024\n"
    "ds_read_b128 v[24:27], v9 offset:2048\n"
    "ds_read_b128 v[28:31], v9 offset:3072\n"
    "v_add_u32 v13, v5, v3\n"
    "v_cmp_gt_i32 vcc, s71, v13\n"
    "s_nop 0\n"
    "s_or_b64 exec, vcc, s[62:63]\n"
    "buffer_atomic_smax v5, v2, s[36:39], 0 offen sc0 sc1\n"
    "s_mov_b64 exec, -1\n"
    "s_lshr_b32 s76, s71, 1\n"
    "s_and_b32 s76, s76, s54\n"
    "s_mul_i32 s76, s76, s53\n"
    "s_add_u32 s76, s76, s52\n"
    "s_waitcnt lgkmcnt(3)\n"
    "s_branch .LgWO_head1_%=\n"
    ".LgWO_FIN_%=:\n"
  );
}
DEVI void role_down(const Params& p, int L, int j, int i, u32 dbg, unsigned long long tag) {
  const int lane = threadIdx.x & 63, w = threadIdx.x >> 6, e0 = j * 1152 * 576 + i * 192;
  GEMM_DECL;
  g_w0 = make_srd(p.w2 + e0, 4608u * 576 * 2 - e0 * 2); g_w1 = g_w0; g_w2 = g_w0;
  g_ldw = 1152;
  g_in_base = pgb(L, B_H + 9 * j); g_in_stride = SLOT; g_in_mask = RH - 1;
  g_out_base = pgb(L, B_PF + 5 * j); g_out_kb = 12 * i; g_out_mask = RP - 1;
  g_flag = lane < 4 ? mbox(L, lane, SL_REDF, 12 * j + 4 * i + w) : ((lane < 10 && w == 0) ? mbox(L, j, SL_UP + lane - 4, 16 + i) : OOB);
  g_paddr = mbox(L, j, SL_DOWN + i, lane);
  g_pbias = lane < 24 ? 0 : (lane < 40 ? 2 * RP : 1000);
  v16u sa, sb, va;
  gemm_args(GEMM_ARGS, p, dbg, tag, sa, sb, va);
  RUN_ASM(
    "s_nop 4\n"
    "s_getpc_b64 s[84:85]\n"
    ".LgDOWN_PC_%=:\n"
    "s_and_b32 s85, s85, 0xffff\n"
    "s_mov_b32 s86, .LgDOWN_FIN_%=-.LgDOWN_PC_%=\n"
    "s_mov_b32 s87, 0x20000\n"
    "v_mbcnt_lo_u32_b32 v40, -1, 0\n"
    "v_mbcnt_hi_u32_b32 v40, -1, v40\n"
    "v_lshlrev_b32 v40, 7, v40\n"
    "buffer_load_dword v41, v40, s[84:87], 0 offen\n"
    "v_add_u32 v40, 8192, v40\n"
    "buffer_load_dword v41, v40, s[84:87], 0 offen\n"
    "v_add_u32 v40, 8192, v40\n"
    "buffer_load_dword v41, v40, s[84:87], 0 offen\n"
    "s_mov_b32 s68, 0\n"
    "s_mov_b32 s71, 0\n"
    "v_add_u32 v9, 36864, v0\n"
    "s_mov_b32 s64, 0xbfb8aa3b\n"
    "s_mov_b32 s72, 262144\n"
    "s_add_u32 m0, s59, 0\n"
    "s_mov_b32 s74, 0\n"
    "buffer_load_dwordx4 v5, s[40:43], s74 offen lds\n"
    "s_add_u32 m0, s59, 4096\n"
    "s_mov_b32 s74, 128\n"
    "buffer_load_dwordx4 v5, s[40:43], s74 offen lds\n"
    "s_add_u32 m0, s59, 8192\n"
    "s_mov_b32 s74, 256\n"
    "buffer_load_dwordx4 v5, s[40:43], s74 offen lds\n"
    "s_add_u32 m0, s59, 12288\n"
    "s_mov_b32 s74, 36864\n"
    "buffer_load_dwordx4 v5, s[40:43], s74 offen lds\n"
    "s_add_u32 m0, s59, 16384\n"
    "s_mov_b32 s74, 36992\n"
    "buffer_load_dwordx4 v5, s[40:43], s74 offen lds\n"
    "s_add_u32 m0, s59, 20480\n"
    "s_mov_b32 s74, 37120\n"
    "buffer_load_dwordx4 v5, s[40:43], s74 offen lds\n"
    "s_add_u32 m0, s59, 24576\n"
    "s_mov_b32 s74, 73728\n"
    "buffer_load_dwordx4 v5, s[40:43], s74 offen lds\n"
    "s_add_u32 m0, s59, 28672\n"
    "s_mov_b32 s74, 73856\n"
    "buffer_load_dwordx4 v5, s[40:43], s74 offen lds\n"
    "s_add_u32 m0, s59, 32768\n"
    "s_mov_b32 s74, 73984\n"
    "buffer_load_dwordx4 v5, s[40:43], s74 offen lds\n"
    "s_add_u32 m0, s59, 36864\n"
    "s_mov_b32 s74, 110592\n"
    "buffer_load_dwordx4 v5, s[40:43], s74 offen lds\n"
    "s_add_u32 m0, s59, 40960\n"
    "s_mov_b32 s74, 110720\n"
    "buffer_load_dwordx4 v5, s[40:43], s74 offen lds\n"
    "s_add_u32 m0, s59, 45056\n"
    "s_mov_b32 s74, 110848\n"
    "buffer_load_dwordx4 v5, s[40:43], s74 offen lds\n"
    "s_add_u32 m0, s59, 49152\n"
    "s_mov_b32 s74, 147456\n"
    "buffer_load_dwordx4 v5, s[40:43], s74 offen lds\n"
    "s_add_u32 m0, s59, 53248\n"
    "s_mov_b32 s74, 147584\n"
    "buffer_load_dwordx4 v5, s[40:43], s74 offen lds\n"
    "s_add_u32 m0, s59, 57344\n"
    "s_mov_b32 s74, 147712\n"
    "buffer_load_dwordx4 v5, s[40:43], s74 offen lds\n"
    "s_add_u32 m0, s59, 61440\n"
    "s_mov_b32 s74, 184320\n"
    "buffer_load_dwordx4 v5, s[40:43], s74 offen lds\n"
    "s_add_u32 m0, s59, 65536\n"
    "s_mov_b32 s74, 184448\n"
    "buffer_load_dwordx4 v5, s[40:43], s74 offen lds\n"
    "s_add_u32 m0, s59, 69632\n"
    "s_mov_b32 s74, 184576\n"
    "buffer_load_dwordx4 v5, s[40:43], s74 offen lds\n"
    "s_add_u32 m0, s59, 73728\n"
    "s_mov_b32 s74, 221184\n"
    "buffer_load_dwordx4 v5, s[40:43], s74 offen lds\n"
    "s_add_u32 m0, s59, 77824\n"
    "s_mov_b32 s74, 221312\n"
    "buffer_load_dwordx4 v5, s[40:43], s74 offen lds\n"
    "s_add_u32 m0, s59, 81920\n"
    "s_mov_b32 s74, 221440\n"
    "buffer_load_dwordx4 v5, s[40:43], s74 offen lds\n"
    "s_add_u32 m0, s59, 86016\n"
    "s_mov_b32 s74, 258048\n"
    "buffer_load_dwordx4 v5, s[40:43], s74 offen lds\n"
    "s_add_u32 m0, s59, 90112\n"
    "s_mov_b32 s74, 258176\n"
    "buffer_load_dwordx4 v5, s[40:43], s74 offen lds\n"
    "s_add_u32 m0, s59, 94208\n"
    "s_mov_b32 s74, 258304\n"
    "buffer_load_dwordx4 v5, s[40:43], s74 offen lds\n"
    "s_add_u32 m0, s59, 98304\n"
    "s_mov_b32 s74, 294912\n"
    "buffer_load_dwordx4 v5, s[40:43], s74 offen lds\n"
    "s_add_u32 m0, s59, 102400\n"
    "s_mov_b32 s74, 295040\n"
    "buffer_load_dwordx4 v5, s[40:43], s74 offen lds\n"
    "s_add_u32 m0, s59, 106496\n"
    "s_mov_b32 s74, 295168\n"
    "buffer_load_dwordx4 v5, s[40:43], s74 offen lds\n"
    "s_add_u32 m0, s59, 110592\n"
    "s_mov_b32 s74, 331776\n"
    "buffer_load_dwordx4 v5, s[40:43], s74 offen lds\n"
    "s_add_u32 m0, s59, 114688\n"
    "s_mov_b32 s74, 331904\n"
    "buffer_load_dwordx4 v5, s[40:43], s74 offen lds\n"
    "s_add_u32 m0, s59, 118784\n"
    "s_mov_b32 s74, 332032\n"
    "buffer_load_dwordx4 v5, s[40:43], s74 offen lds\n"
    "v_add_u32 v16, 49152, v6\n"
    "v_add_u32 v17, 98304, v6\n"
    "v_add_u32 v18, 49152, v7\n"
    "v_add_u32 v19, 98304, v7\n"
    "s_waitcnt vmcnt(24) lgkmcnt(0)\n"
    "s_barrier\n"
    "s_add_u32 m0, s59, 122880\n"
    "s_mov_b32 s74, 368640\n"
    "buffer_load_dwordx4 v5, s[40:43], s74 offen lds\n"
    "s_add_u32 m0, s59, 126976\n"
    "s_mov_b32 s74, 368768\n"
    "buffer_load_dwordx4 v5, s[40:43], s74 offen lds\n"
    "s_add_u32 m0, s59, 131072\n"
    "s_mov_b32 s74, 368896\n"
    "buffer_load_dwordx4 v5, s[40:43], s74 offen lds\n"
    "s_add_u32 m0, s59, 135168\n"
    "s_mov_b32 s74, 405504\n"
    "buffer_load_dwordx4 v5, s[40:43], s74 offen lds\n"
    "s_add_u32 m0, s59, 139264\n"
    "s_mov_b32 s74, 405632\n"
    "buffer_load_dwordx4 v5, s[40:43], s74 offen lds\n"
    "s_add_u32 m0, s59, 143360\n"
    "s_mov_b32 s74, 405760\n"
    "buffer_load_dwordx4 v5, s[40:43], s74 offen lds\n"
    "ds_read_b64_tr_b16 a[0:1], v6 offset:0\n"
    "ds_read_b64_tr_b16 a[2:3], v6 offset:512\n"
    "ds_read_b64_tr_b16 a[4:5], v6 offset:8\n"
    "ds_read_b64_tr_b16 a[6:7], v6 offset:520\n"
    "ds_read_b64_tr_b16 a[8:9], v7 offset:0\n"
    "ds_read_b64_tr_b16 a[10:11], v7 offset:512\n"
    "ds_read_b64_tr_b16 a[12:13], v6 offset:12288\n"
    "ds_read_b64_tr_b16 a[14:15], v6 offset:12800\n"
    "ds_read_b64_tr_b16 a[16:17], v6 offset:12296\n"
    "ds_read_b64_tr_b16 a[18:19], v6 offset:12808\n"
    "ds_read_b64_tr_b16 a[20:21], v7 offset:12288\n"
    "ds_read_b64_tr_b16 a[22:23], v7 offset:12800\n"
    "s_waitcnt vmcnt(24) lgkmcnt(0)\n"
    "s_barrier\n"
    "s_add_u32 m0, s59, 0\n"
    "s_mov_b32 s74, 442368\n"
    "buffer_load_dwordx4 v5, s[40:43], s74 offen lds\n"
    "s_add_u32 m0, s59, 4096\n"
    "s_mov_b32 s74, 442496\n"
    "buffer_load_dwordx4 v5, s[40:43], s74 offen lds\n"
    "s_add_u32 m0, s59, 8192\n"
    "s_mov_b32 s74, 442624\n"
    "buffer_load_dwordx4 v5, s[40:43], s74 offen lds\n"
    "s_add_u32 m0, s59, 12288\n"
    "s_mov_b32 s74, 479232\n"
    "buffer_load_dwordx4 v5, s[40:43], s74 offen lds\n"
    "s_add_u32 m0, s59, 16384\n"
    "s_mov_b32 s74, 479360\n"
    "buffer_load_dwordx4 v5, s[40:43], s74 offen lds\n"
    "s_add_u32 m0, s59, 20480\n"
    "s_mov_b32 s74, 479488\n"
    "buffer_load_dwordx4 v5, s[40:43], s74 offen lds\n"
    "ds_read_b64_tr_b16 a[24:25], v6 offset:24576\n"
    "ds_read_b64_tr_b16 a[26:27], v6 offset:25088\n"
    "ds_read_b64_tr_b16 a[28:29], v6 offset:24584\n"
    "ds_read_b64_tr_b16 a[30:31], v6 offset:25096\n"
    "ds_read_b64_tr_b16 a[32:33], v7 offset:24576\n"
    "ds_read_b64_tr_b16 a[34:35], v7 offset:25088\n"
    "ds_read_b64_tr_b16 a[36:37], v6 offset:36864\n"
    "ds_read_b64_tr_b16 a[38:39], v6 offset:37376\n"
    "ds_read_b64_tr_b16 a[40:41], v6 offset:36872\n"
    "ds_read_b64_tr_b16 a[42:43], v6 offset:37384\n"
    "ds_read_b64_tr_b16 a[44:45], v7 offset:36864\n"
    "ds_read_b64_tr_b16 a[46:47], v7 offset:37376\n"
    "s_waitcnt vmcnt(24) lgkmcnt(0)\n"
    "s_barrier\n"
    "s_add_u32 m0, s59, 24576\n"
    "s_mov_b32 s74, 516096\n"
    "buffer_load_dwordx4 v5, s[40:43], s74 offen lds\n"
    "s_add_u32 m0, s59, 28672\n"
    "s_mov_b32 s74, 516224\n"
    "buffer_load_dwordx4 v5, s[40:43], s74 offen lds\n"
    "s_add_u32 m0, s59, 32768\n"
    "s_mov_b32 s74, 516352\n"
    "buffer_load_dwordx4 v5, s[40:43], s74 offen lds\n"
    "s_add_u32 m0, s59, 36864\n"
    "s_mov_b32 s74, 552960\n"
    "buffer_load_dwordx4 v5, s[40:43], s74 offen lds\n"
    "s_add_u32 m0, s59, 40960\n"
    "s_mov_b32 s74, 553088\n"
    "buffer_load_dwordx4 v5, s[40:43], s74 offen lds\n"
    "s_add_u32 m0, s59, 45056\n"
    "s_mov_b32 s74, 553216\n"
    "buffer_load_dwordx4 v5, s[40:43], s74 offen lds\n"
    "ds_read_b64_tr_b16 a[48:49], v16 offset:0\n"
    "ds_read_b64_tr_b16 a[50:51], v16 offset:512\n"
    "ds_read_b64_tr_b16 a[52:53], v16 offset:8\n"
    "ds_read_b64_tr_b16 a[54:55], v16 offset:520\n"
    "ds_read_b64_tr_b16 a[56:57], v18 offset:0\n"
    "ds_read_b64_tr_b16 a[58:59], v18 offset:512\n"
    "ds_read_b64_tr_b16 a[60:61], v16 offset:12288\n"
    "ds_read_b64_tr_b16 a[62:63], v16 offset:12800\n"
    "ds_read_b64_tr_b16 a[64:65], v16 offset:12296\n"
    "ds_read_b64_tr_b16 a[66:67], v16 offset:12808\n"
    "ds_read_b64_tr_b16 a[68:69], v18 offset:12288\n"
    "ds_read_b64_tr_b16 a[70:71], v18 offset:12800\n"
    "s_waitcnt vmcnt(24) lgkmcnt(0)\n"
    "s_barrier\n"
    "s_add_u32 m0, s59, 49152\n"
    "s_mov_b32 s74, 589824\n"
    "buffer_load_dwordx4 v5, s[40:43], s74 offen lds\n"
    "s_add_u32 m0, s59, 53248\n"
    "s_mov_b32 s74, 589952\n"
    "buffer_load_dwordx4 v5, s[40:43], s74 offen lds\n"
    "s_add_u32 m0, s59, 57344\n"
    "s_mov_b32 s74, 590080\n"
    "buffer_load_dwordx4 v5, s[40:43], s74 offen lds\n"
    "s_add_u32 m0, s59, 61440\n"
    "s_mov_b32 s74, 626688\n"
    "buffer_load_dwordx4 v5, s[40:43], s74 offen lds\n"
    "s_add_u32 m0, s59, 65536\n"
    "s_mov_b32 s74, 626816\n"
    "buffer_load_dwordx4 v5, s[40:43], s74 offen lds\n"
    "s_add_u32 m0, s59, 69632\n"
    "s_mov_b32 s74, 626944\n"
    "buffer_load_dwordx4 v5, s[40:43], s74 offen lds\n"
    "ds_read_b64_tr_b16 a[72:73], v16 offset:24576\n"
    "ds_read_b64_tr_b16 a[74:75], v16 offset:25088\n"
    "ds_read_b64_tr_b16 a[76:77], v16 offset:24584\n"
    "ds_read_b64_tr_b16 a[78:79], v16 offset:25096\n"
    "ds_read_b64_tr_b16 a[80:81], v18 offset:24576\n"
    "ds_read_b64_tr_b16 a[82:83], v18 offset:25088\n"
    "ds_read_b64_tr_b16 a[84:85], v16 offset:36864\n"
    "ds_read_b64_tr_b16 a[86:87], v16 offset:37376\n"
    "ds_read_b64_tr_b16 a[88:89], v16 offset:36872\n"
    "ds_read_b64_tr_b16 a[90:91], v16 offset:37384\n"
    "ds_read_b64_tr_b16 a[92:93], v18 offset:36864\n"
    "ds_read_b64_tr_b16 a[94:95], v18 offset:37376\n"
    "s_waitcnt vmcnt(24) lgkmcnt(0)\n"
    "s_barrier\n"
    "s_add_u32 m0, s59, 73728\n"
    "s_mov_b32 s74, 663552\n"
    "buffer_load_dwordx4 v5, s[40:43], s74 offen lds\n"
    "s_add_u32 m0, s59, 77824\n"
    "s_mov_b32 s74, 663680\n"
    "buffer_load_dwordx4 v5, s[40:43], s74 offen lds\n"
    "s_add_u32 m0, s59, 81920\n"
    "s_mov_b32 s74, 663808\n"
    "buffer_load_dwordx4 v5, s[40:43], s74 offen lds\n"
    "s_add_u32 m0, s59, 86016\n"
    "s_mov_b32 s74, 700416\n"
    "buffer_load_dwordx4 v5, s[40:43], s74 offen lds\n"
    "s_add_u32 m0, s59, 90112\n"
    "s_mov_b32 s74, 700544\n"
    "buffer_load_dwordx4 v5, s[40:43], s74 offen lds\n"
    "s_add_u32 m0, s59, 94208\n"
    "s_mov_b32 s74, 700672\n"
    "buffer_load_dwordx4 v5, s[40:43], s74 offen lds\n"
    "ds_read_b64_tr_b16 a[96:97], v17 offset:0\n"
    "ds_read_b64_tr_b16 a[98:99], v17 offset:512\n"
    "ds_read_b64_tr_b16 a[100:101], v17 offset:8\n"
    "ds_read_b64_tr_b16 a[102:103], v17 offset:520\n"
    "ds_read_b64_tr_b16 a[104:105], v19 offset:0\n"
    "ds_read_b64_tr_b16 a[106:107], v19 offset:512\n"
    "ds_read_b64_tr_b16 a[108:109], v17 offset:12288\n"
    "ds_read_b64_tr_b16 a[110:111], v17 offset:12800\n"
    "ds_read_b64_tr_b16 a[112:113], v17 offset:12296\n"
    "ds_read_b64_tr_b16 a[114:115], v17 offset:12808\n"
    "ds_read_b64_tr_b16 a[116:117], v19 offset:12288\n"
    "ds_read_b64_tr_b16 a[118:119], v19 offset:12800\n"
    "s_waitcnt vmcnt(24) lgkmcnt(0)\n"
    "s_barrier\n"
    "s_add_u32 m0, s59, 98304\n"
    "s_mov_b32 s74, 737280\n"
    "buffer_load_dwordx4 v5, s[40:43], s74 offen lds\n"
    "s_add_u32 m0, s59, 102400\n"
    "s_mov_b32 s74, 737408\n"
    "buffer_load_dwordx4 v5, s[40:43], s74 offen lds\n"
    "s_add_u32 m0, s59, 106496\n"
    "s_mov_b32 s74, 737536\n"
    "buffer_load_dwordx4 v5, s[40:43], s74 offen lds\n"
    "s_add_u32 m0, s59, 110592\n"
    "s_mov_b32 s74, 774144\n"
    "buffer_load_dwordx4 v5, s[40:43], s74 offen lds\n"
    "s_add_u32 m0, s59, 114688\n"
    "s_mov_b32 s74, 774272\n"
    "buffer_load_dwordx4 v5, s[40:43], s74 offen lds\n"
    "s_add_u32 m0, s59, 118784\n"
    "s_mov_b32 s74, 774400\n"
    "buffer_load_dwordx4 v5, s[40:43], s74 offen lds\n"
    "ds_read_b64_tr_b16 a[120:121], v17 offset:24576\n"
    "ds_read_b64_tr_b16 a[122:123], v17 offset:25088\n"
    "ds_read_b64_tr_b16 a[124:125], v17 offset:24584\n"
    "ds_read_b64_tr_b16 a[126:127], v17 offset:25096\n"
    "ds_read_b64_tr_b16 a[128:129], v19 offset:24576\n"
    "ds_read_b64_tr_b16 a[130:131], v19 offset:25088\n"
    "ds_read_b64_tr_b16 a[132:133], v17 offset:36864\n"
    "ds_read_b64_tr_b16 a[134:135], v17 offset:37376\n"
    "ds_read_b64_tr_b16 a[136:137], v17 offset:36872\n"
    "ds_read_b64_tr_b16 a[138:139], v17 offset:37384\n"
    "ds_read_b64_tr_b16 a[140:141], v19 offset:36864\n"
    "ds_read_b64_tr_b16 a[142:143], v19 offset:37376\n"
    "s_waitcnt vmcnt(24) lgkmcnt(0)\n"
    "s_barrier\n"
    "s_add_u32 m0, s59, 122880\n"
    "s_mov_b32 s74, 811008\n"
    "buffer_load_dwordx4 v5, s[40:43], s74 offen lds\n"
    "s_add_u32 m0, s59, 126976\n"
    "s_mov_b32 s74, 811136\n"
    "buffer_load_dwordx4 v5, s[40:43], s74 offen lds\n"
    "s_add_u32 m0, s59, 131072\n"
    "s_mov_b32 s74, 811264\n"
    "buffer_load_dwordx4 v5, s[40:43], s74 offen lds\n"
    "s_add_u32 m0, s59, 135168\n"
    "s_mov_b32 s74, 847872\n"
    "buffer_load_dwordx4 v5, s[40:43], s74 offen lds\n"
    "s_add_u32 m0, s59, 139264\n"
    "s_mov_b32 s74, 848000\n"
    "buffer_load_dwordx4 v5, s[40:43], s74 offen lds\n"
    "s_add_u32 m0, s59, 143360\n"
    "s_mov_b32 s74, 848128\n"
    "buffer_load_dwordx4 v5, s[40:43], s74 offen lds\n"
    "ds_read_b64_tr_b16 a[144:145], v6 offset:0\n"
    "ds_read_b64_tr_b16 a[146:147], v6 offset:512\n"
    "ds_read_b64_tr_b16 a[148:149], v6 offset:8\n"
    "ds_read_b64_tr_b16 a[150:151], v6 offset:520\n"
    "ds_read_b64_tr_b16 a[152:153], v7 offset:0\n"
    "ds_read_b64_tr_b16 a[154:155], v7 offset:512\n"
    "ds_read_b64_tr_b16 a[156:157], v6 offset:12288\n"
    "ds_read_b64_tr_b16 a[158:159], v6 offset:12800\n"
    "ds_read_b64_tr_b16 a[160:161], v6 offset:12296\n"
    "ds_read_b64_tr_b16 a[162:163], v6 offset:12808\n"
    "ds_read_b64_tr_b16 a[164:165], v7 offset:12288\n"
    "ds_read_b64_tr_b16 a[166:167], v7 offset:12800\n"
    "s_waitcnt vmcnt(24) lgkmcnt(0)\n"
    "s_barrier\n"
    "s_add_u32 m0, s59, 0\n"
    "s_mov_b32 s74, 884736\n"
    "buffer_load_dwordx4 v5, s[40:43], s74 offen lds\n"
    "s_add_u32 m0, s59, 4096\n"
    "s_mov_b32 s74, 884864\n"
    "buffer_load_dwordx4 v5, s[40:43], s74 offen lds\n"
    "s_add_u32 m0, s59, 8192\n"
    "s_mov_b32 s74, 884992\n"
    "buffer_load_dwordx4 v5, s[40:43], s74 offen lds\n"
    "s_add_u32 m0, s59, 12288\n"
    "s_mov_b32 s74, 921600\n"
    "buffer_load_dwordx4 v5, s[40:43], s74 offen lds\n"
    "s_add_u32 m0, s59, 16384\n"
    "s_mov_b32 s74, 921728\n"
    "buffer_load_dwordx4 v5, s[40:43], s74 offen lds\n"
    "s_add_u32 m0, s59, 20480\n"
    "s_mov_b32 s74, 921856\n"
    "buffer_load_dwordx4 v5, s[40:43], s74 offen lds\n"
    "ds_read_b64_tr_b16 a[168:169], v6 offset:24576\n"
    "ds_read_b64_tr_b16 a[170:171], v6 offset:25088\n"
    "ds_read_b64_tr_b16 a[172:173], v6 offset:24584\n"
    "ds_read_b64_tr_b16 a[174:175], v6 offset:25096\n"
    "ds_read_b64_tr_b16 a[176:177], v7 offset:24576\n"
    "ds_read_b64_tr_b16 a[178:179], v7 offset:25088\n"
    "ds_read_b64_tr_b16 a[180:181], v6 offset:36864\n"
    "ds_read_b64_tr_b16 a[182:183], v6 offset:37376\n"
    "ds_read_b64_tr_b16 a[184:185], v6 offset:36872\n"
    "ds_read_b64_tr_b16 a[186:187], v6 offset:37384\n"
    "ds_read_b64_tr_b16 a[188:189], v7 offset:36864\n"
    "ds_read_b64_tr_b16 a[190:191], v7 offset:37376\n"
    "s_waitcnt vmcnt(24) lgkmcnt(0)\n"
    "s_barrier\n"
    "s_add_u32 m0, s59, 24576\n"
    "s_mov_b32 s74, 958464\n"
    "buffer_load_dwordx4 v5, s[40:43], s74 offen lds\n"
    "s_add_u32 m0, s59, 28672\n"
    "s_mov_b32 s74, 958592\n"
    "buffer_load_dwordx4 v5, s[40:43], s74 offen lds\n"
    "s_add_u32 m0, s59, 32768\n"
    "s_mov_b32 s74, 958720\n"
    "buffer_load_dwordx4 v5, s[40:43], s74 offen lds\n"
    "s_add_u32 m0, s59, 36864\n"
    "s_mov_b32 s74, 995328\n"
    "buffer_load_dwordx4 v5, s[40:43], s74 offen lds\n"
    "s_add_u32 m0, s59, 40960\n"
    "s_mov_b32 s74, 995456\n"
    "buffer_load_dwordx4 v5, s[40:43], s74 offen lds\n"
    "s_add_u32 m0, s59, 45056\n"
    "s_mov_b32 s74, 995584\n"
    "buffer_load_dwordx4 v5, s[40:43], s74 offen lds\n"
    "ds_read_b64_tr_b16 a[192:193], v16 offset:0\n"
    "ds_read_b64_tr_b16 a[194:195], v16 offset:512\n"
    "ds_read_b64_tr_b16 a[196:197], v16 offset:8\n"
    "ds_read_b64_tr_b16 a[198:199], v16 offset:520\n"
    "ds_read_b64_tr_b16 a[200:201], v18 offset:0\n"
    "ds_read_b64_tr_b16 a[202:203], v18 offset:512\n"
    "ds_read_b64_tr_b16 a[204:205], v16 offset:12288\n"
    "ds_read_b64_tr_b16 a[206:207], v16 offset:12800\n"
    "ds_read_b64_tr_b16 a[208:209], v16 offset:12296\n"
    "ds_read_b64_tr_b16 a[210:211], v16 offset:12808\n"
    "ds_read_b64_tr_b16 a[212:213], v18 offset:12288\n"
    "ds_read_b64_tr_b16 a[214:215], v18 offset:12800\n"
    "s_waitcnt vmcnt(24) lgkmcnt(0)\n"
    "s_barrier\n"
    "s_add_u32 m0, s59, 49152\n"
    "s_mov_b32 s74, 1032192\n"
    "buffer_load_dwordx4 v5, s[40:43], s74 offen lds\n"
    "s_add_u32 m0, s59, 53248\n"
    "s_mov_b32 s74, 1032320\n"
    "buffer_load_dwordx4 v5, s[40:43], s74 offen lds\n"
    "s_add_u32 m0, s59, 57344\n"
    "s_mov_b32 s74, 1032448\n"
    "buffer_load_dwordx4 v5, s[40:43], s74 offen lds\n"
    "s_add_u32 m0, s59, 61440\n"
    "s_mov_b32 s74, 1069056\n"
    "buffer_load_dwordx4 v5, s[40:43], s74 offen lds\n"
    "s_add_u32 m0, s59, 65536\n"
    "s_mov_b32 s74, 1069184\n"
    "buffer_load_dwordx4 v5, s[40:43], s74 offen lds\n"
    "s_add_u32 m0, s59, 69632\n"
    "s_mov_b32 s74, 1069312\n"
    "buffer_load_dwordx4 v5, s[40:43], s74 offen lds\n"
    "ds_read_b64_tr_b16 a[216:217], v16 offset:24576\n"
    "ds_read_b64_tr_b16 a[218:219], v16 offset:25088\n"
    "ds_read_b64_tr_b16 a[220:221], v16 offset:24584\n"
    "ds_read_b64_tr_b16 a[222:223], v16 offset:25096\n"
    "ds_read_b64_tr_b16 a[224:225], v18 offset:24576\n"
    "ds_read_b64_tr_b16 a[226:227], v18 offset:25088\n"
    "ds_read_b64_tr_b16 a[228:229], v16 offset:36864\n"
    "ds_read_b64_tr_b16 a[230:231], v16 offset:37376\n"
    "ds_read_b64_tr_b16 a[232:233], v16 offset:36872\n"
    "ds_read_b64_tr_b16 a[234:235], v16 offset:37384\n"
    "ds_read_b64_tr_b16 a[236:237], v18 offset:36864\n"
    "ds_read_b64_tr_b16 a[238:239], v18 offset:37376\n"
    "s_waitcnt vmcnt(24) lgkmcnt(0)\n"
    "s_barrier\n"
    "s_add_u32 m0, s59, 73728\n"
    "s_mov_b32 s74, 1105920\n"
    "buffer_load_dwordx4 v5, s[40:43], s74 offen lds\n"
    "s_add_u32 m0, s59, 77824\n"
    "s_mov_b32 s74, 1106048\n"
    "buffer_load_dwordx4 v5, s[40:43], s74 offen lds\n"
    "s_add_u32 m0, s59, 81920\n"
    "s_mov_b32 s74, 1106176\n"
    "buffer_load_dwordx4 v5, s[40:43], s74 offen lds\n"
    "s_add_u32 m0, s59, 86016\n"
    "s_mov_b32 s74, 1142784\n"
    "buffer_load_dwordx4 v5, s[40:43], s74 offen lds\n"
    "s_add_u32 m0, s59, 90112\n"
    "s_mov_b32 s74, 1142912\n"
    "buffer_load_dwordx4 v5, s[40:43], s74 offen lds\n"
    "s_add_u32 m0, s59, 94208\n"
    "s_mov_b32 s74, 1143040\n"
    "buffer_load_dwordx4 v5, s[40:43], s74 offen lds\n"
    "ds_read_b64_tr_b16 a[240:241], v17 offset:0\n"
    "ds_read_b64_tr_b16 a[242:243], v17 offset:512\n"
    "ds_read_b64_tr_b16 a[244:245], v17 offset:8\n"
    "ds_read_b64_tr_b16 a[246:247], v17 offset:520\n"
    "ds_read_b64_tr_b16 a[248:249], v19 offset:0\n"
    "ds_read_b64_tr_b16 a[250:251], v19 offset:512\n"
    "ds_read_b64_tr_b16 a[252:253], v17 offset:12288\n"
    "ds_read_b64_tr_b16 a[254:255], v17 offset:12800\n"
    "ds_read_b64_tr_b16 v[80:81], v17 offset:12296\n"
    "ds_read_b64_tr_b16 v[82:83], v17 offset:12808\n"
    "ds_read_b64_tr_b16 v[84:85], v19 offset:12288\n"
    "ds_read_b64_tr_b16 v[86:87], v19 offset:12800\n"
    "s_waitcnt vmcnt(24) lgkmcnt(0)\n"
    "s_barrier\n"
    "s_add_u32 m0, s59, 98304\n"
    "s_mov_b32 s74, 1179648\n"
    "buffer_load_dwordx4 v5, s[40:43], s74 offen lds\n"
    "s_add_u32 m0, s59, 102400\n"
    "s_mov_b32 s74, 1179776\n"
    "buffer_load_dwordx4 v5, s[40:43], s74 offen lds\n"
    "s_add_u32 m0, s59, 106496\n"
    "s_mov_b32 s74, 1179904\n"
    "buffer_load_dwordx4 v5, s[40:43], s74 offen lds\n"
    "s_add_u32 m0, s59, 110592\n"
    "s_mov_b32 s74, 1216512\n"
    "buffer_load_dwordx4 v5, s[40:43], s74 offen lds\n"
    "s_add_u32 m0, s59, 114688\n"
    "s_mov_b32 s74, 1216640\n"
    "buffer_load_dwordx4 v5, s[40:43], s74 offen lds\n"
    "s_add_u32 m0, s59, 118784\n"
    "s_mov_b32 s74, 1216768\n"
    "buffer_load_dwordx4 v5, s[40:43], s74 offen lds\n"
    "ds_read_b64_tr_b16 v[88:89], v17 offset:24576\n"
    "ds_read_b64_tr_b16 v[90:91], v17 offset:25088\n"
    "ds_read_b64_tr_b16 v[92:93], v17 offset:24584\n"
    "ds_read_b64_tr_b16 v[94:95], v17 offset:25096\n"
    "ds_read_b64_tr_b16 v[96:97], v19 offset:24576\n"
    "ds_read_b64_tr_b16 v[98:99], v19 offset:25088\n"
    "ds_read_b64_tr_b16 v[100:101], v17 offset:36864\n"
    "ds_read_b64_tr_b16 v[102:103], v17 offset:37376\n"
    "ds_read_b64_tr_b16 v[104:105], v17 offset:36872\n"
    "ds_read_b64_tr_b16 v[106:107], v17 offset:37384\n"
    "ds_read_b64_tr_b16 v[108:109], v19 offset:36864\n"
    "ds_read_b64_tr_b16 v[110:111], v19 offset:37376\n"
    "s_waitcnt vmcnt(24) lgkmcnt(0)\n"
    "s_barrier\n"
    "s_add_u32 m0, s59, 122880\n"
    "s_mov_b32 s74, 1253376\n"
    "buffer_load_dwordx4 v5, s[40:43], s74 offen lds\n"
    "s_add_u32 m0, s59, 126976\n"
    "s_mov_b32 s74, 1253504\n"
    "buffer_load_dwordx4 v5, s[40:43], s74 offen lds\n"
    "s_add_u32 m0, s59, 131072\n"
    "s_mov_b32 s74, 1253632\n"
    "buffer_load_dwordx4 v5, s[40:43], s74 offen lds\n"
    "s_add_u32 m0, s59, 135168\n"
    "s_mov_b32 s74, 1290240\n"
    "buffer_load_dwordx4 v5, s[40:43], s74 offen lds\n"
    "s_add_u32 m0, s59, 139264\n"
    "s_mov_b32 s74, 1290368\n"
    "buffer_load_dwordx4 v5, s[40:43], s74 offen lds\n"
    "s_add_u32 m0, s59, 143360\n"
    "s_mov_b32 s74, 1290496\n"
    "buffer_load_dwordx4 v5, s[40:43], s74 offen lds\n"
    "buffer_load_dwordx2 v[12:13], off, s[36:39], 0 sc1\n"
    "ds_read_b64_tr_b16 v[112:113], v6 offset:0\n"
    "ds_read_b64_tr_b16 v[114:115], v6 offset:512\n"
    "ds_read_b64_tr_b16 v[116:117], v6 offset:8\n"
    "ds_read_b64_tr_b16 v[118:119], v6 offset:520\n"
    "ds_read_b64_tr_b16 v[120:121], v7 offset:0\n"
    "ds_read_b64_tr_b16 v[122:123], v7 offset:512\n"
    "ds_read_b64_tr_b16 v[124:125], v6 offset:12288\n"
    "ds_read_b64_tr_b16 v[126:127], v6 offset:12800\n"
    "ds_read_b64_tr_b16 v[128:129], v6 offset:12296\n"
    "ds_read_b64_tr_b16 v[130:131], v6 offset:12808\n"
    "ds_read_b64_tr_b16 v[132:133], v7 offset:12288\n"
    "ds_read_b64_tr_b16 v[134:135], v7 offset:12800\n"
    "s_waitcnt vmcnt(25) lgkmcnt(0)\n"
    "s_barrier\n"
    "ds_read_b64_tr_b16 v[136:137], v6 offset:24576\n"
    "ds_read_b64_tr_b16 v[138:139], v6 offset:25088\n"
    "ds_read_b64_tr_b16 v[140:141], v6 offset:24584\n"
    "ds_read_b64_tr_b16 v[142:143], v6 offset:25096\n"
    "ds_read_b64_tr_b16 v[144:145], v7 offset:24576\n"
    "ds_read_b64_tr_b16 v[146:147], v7 offset:25088\n"
    "ds_read_b64_tr_b16 v[148:149], v6 offset:36864\n"
    "ds_read_b64_tr_b16 v[150:151], v6 offset:37376\n"
    "ds_read_b64_tr_b16 v[152:153], v6 offset:36872\n"
    "ds_read_b64_tr_b16 v[154:155], v6 offset:37384\n"
    "ds_read_b64_tr_b16 v[156:157], v7 offset:36864\n"
    "ds_read_b64_tr_b16 v[158:159], v7 offset:37376\n"
    "s_waitcnt vmcnt(19) lgkmcnt(0)\n"
    "s_barrier\n"
    "ds_read_b64_tr_b16 v[160:161], v16 offset:0\n"
    "ds_read_b64_tr_b16 v[162:163], v16 offset:512\n"
    "ds_read_b64_tr_b16 v[164:165], v16 offset:8\n"
    "ds_read_b64_tr_b16 v[166:167], v16 offset:520\n"
    "ds_read_b64_tr_b16 v[168:169], v18 offset:0\n"
    "ds_read_b64_tr_b16 v[170:171], v18 offset:512\n"
    "ds_read_b64_tr_b16 v[172:173], v16 offset:12288\n"
    "ds_read_b64_tr_b16 v[174:175], v16 offset:12800\n"
    "ds_read_b64_tr_b16 v[176:177], v16 offset:12296\n"
    "ds_read_b64_tr_b16 v[178:179], v16 offset:12808\n"
    "ds_read_b64_tr_b16 v[180:181], v18 offset:12288\n"
    "ds_read_b64_tr_b16 v[182:183], v18 offset:12800\n"
    "s_waitcnt vmcnt(13) lgkmcnt(0)\n"
    "s_barrier\n"
    "ds_read_b64_tr_b16 v[184:185], v16 offset:24576\n"
    "ds_read_b64_tr_b16 v[186:187], v16 offset:25088\n"
    "ds_read_b64_tr_b16 v[188:189], v16 offset:24584\n"
    "ds_read_b64_tr_b16 v[190:191], v16 offset:25096\n"
    "ds_read_b64_tr_b16 v[192:193], v18 offset:24576\n"
    "ds_read_b64_tr_b16 v[194:195], v18 offset:25088\n"
    "ds_read_b64_tr_b16 v[196:197], v16 offset:36864\n"
    "ds_read_b64_tr_b16 v[198:199], v16 offset:37376\n"
    "ds_read_b64_tr_b16 v[200:201], v16 offset:36872\n"
    "ds_read_b64_tr_b16 v[202:203], v16 offset:37384\n"
    "ds_read_b64_tr_b16 v[204:205], v18 offset:36864\n"
    "ds_read_b64_tr_b16 v[206:207], v18 offset:37376\n"
    "s_waitcnt vmcnt(7) lgkmcnt(0)\n"
    "s_barrier\n"
    "ds_read_b64_tr_b16 v[208:209], v17 offset:0\n"
    "ds_read_b64_tr_b16 v[210:211], v17 offset:512\n"
    "ds_read_b64_tr_b16 v[212:213], v17 offset:8\n"
    "ds_read_b64_tr_b16 v[214:215], v17 offset:520\n"
    "ds_read_b64_tr_b16 v[216:217], v19 offset:0\n"
    "ds_read_b64_tr_b16 v[218:219], v19 offset:512\n"
    "ds_read_b64_tr_b16 v[220:221], v17 offset:12288\n"
    "ds_read_b64_tr_b16 v[222:223], v17 offset:12800\n"
    "ds_read_b64_tr_b16 v[224:225], v17 offset:12296\n"
    "ds_read_b64_tr_b16 v[226:227], v17 offset:12808\n"
    "ds_read_b64_tr_b16 v[228:229], v19 offset:12288\n"
    "ds_read_b64_tr_b16 v[230:231], v19 offset:12800\n"
    "s_waitcnt vmcnt(0) lgkmcnt(0)\n"
    "s_barrier\n"
    ".LgDOWN_par_%=:\n"
    "v_readfirstlane_b32 s74, v12\n"
    "v_readfirstlane_b32 s75, v13\n"
    "s_xor_b32 s74, s74, s66\n"
    "s_xor_b32 s75, s75, s67\n"
    "s_cselect_b32 s75, 2, 0\n"
    "s_or_b32 s75, s75, s74\n"
    "s_cmp_lt_u32 s75, 2\n"
    "s_cbranch_scc1 .LgDOWN_par1_%=\n"
    "s_sleep 4\n"
    "buffer_load_dwordx2 v[12:13], off, s[36:39], 0 sc1\n"
    "s_waitcnt vmcnt(0)\n"
    "s_sub_u32 s72, s72, 1\n"
    "s_cbranch_scc0 .LgDOWN_par_%=\n"
    "s_mov_b32 s72, 0\n"
    ".LgDOWN_par1_%=:\n"
    "s_and_b32 s74, s74, 1\n"
    "s_lshl_b32 s74, s74, 13\n"
    "s_add_u32 s36, s36, s74\n"
    "s_addc_u32 s37, s37, 0\n"
    "v_add_u32 v13, v11, v3\n"
    "v_cmp_gt_i32 vcc, s71, v13\n"
    "s_nop 0\n"
    "s_or_b64 exec, vcc, s[62:63]\n"
    "buffer_atomic_smax v11, v2, s[36:39], 0 offen sc0 sc1\n"
    "s_mov_b64 exec, -1\n"
    "s_getpc_b64 s[84:85]\n"
    ".LgDOWN_PC2_%=:\n"
    "s_and_b32 s85, s85, 0xffff\n"
    "s_mov_b32 s86, .LgDOWN_FIN_%=-.LgDOWN_PC2_%=\n"
    "s_mov_b32 s87, 0x20000\n"
    "v_mbcnt_lo_u32_b32 v42, -1, 0\n"
    "v_mbcnt_hi_u32_b32 v42, -1, v42\n"
    "v_lshlrev_b32 v42, 7, v42\n"
    "buffer_load_dword v43, v42, s[84:87], 0 offen\n"
    "v_add_u32 v42, 8192, v42\n"
    "buffer_load_dword v43, v42, s[84:87], 0 offen\n"
    "v_add_u32 v42, 8192, v42\n"
    "buffer_load_dword v43, v42, s[84:87], 0 offen\n"
    "ds_read_b64_tr_b16 v[232:233], v17 offset:24576\n"
    "ds_read_b64_tr_b16 v[234:235], v17 offset:25088\n"
    "ds_read_b64_tr_b16 v[236:237], v17 offset:24584\n"
    "ds_read_b64_tr_b16 v[238:239], v17 offset:25096\n"
    "ds_read_b64_tr_b16 v[240:241], v19 offset:24576\n"
    "ds_read_b64_tr_b16 v[242:243], v19 offset:25088\n"
    "ds_read_b64_tr_b16 v[244:245], v17 offset:36864\n"
    "ds_read_b64_tr_b16 v[246:247], v17 offset:37376\n"
    "ds_read_b64_tr_b16 v[248:249], v17 offset:36872\n"
    "ds_read_b64_tr_b16 v[250:251], v17 offset:37384\n"
    "ds_read_b64_tr_b16 v[252:253], v19 offset:36864\n"
    "ds_read_b64_tr_b16 v[254:255], v19 offset:37376\n"
    "s_waitcnt lgkmcnt(0)\n"
    "s_barrier\n"
    "s_waitcnt vmcnt(0)\n"
    "v_mov_b32 v5, v11\n"
    "v_mov_b32 v12, -1\n"
    "s_mov_b32 s73, 0x80000000\n"
    "s_branch .LgDOWN_block1_1_%=\n"
    ".LgDOWN_LOOPB_%=:\n"
    ".LgDOWN_head0_%=:\n"
    "v_mfma_f32_16x16x32_bf16 v[48:51], a[0:3], v[16:19], 0\n"
    "v_mfma_f32_16x16x32_bf16 v[52:55], a[4:7], v[16:19], 0\n"
    "s_waitcnt lgkmcnt(2)\n"
    "v_cvt_pk_bf16_f32 v56, v56, v57\n"
    "v_cvt_pk_bf16_f32 v57, v58, v59\n"
    "v_mfma_f32_16x16x32_bf16 v[64:67], a[8:11], v[16:19], 0\n"
    "ds_read_b128 v[16:19], v0 offset:4096\n"
    "v_cvt_pk_bf16_f32 v58, v60, v61\n"
    "v_mfma_f32_16x16x32_bf16 v[48:51], a[12:15], v[20:23], v[48:51]\n"
    "v_cvt_pk_bf16_f32 v59, v62, v63\n"
    "v_mfma_f32_16x16x32_bf16 v[52:55], a[16:19], v[20:23], v[52:55]\n"
    "s_waitcnt lgkmcnt(2)\n"
    "s_waitcnt vmcnt(1)\n"
    "buffer_store_dwordx4 v[56:59], v15, s[36:39], s73 offen offset:1024 sc1\n"
    "v_cvt_pk_bf16_f32 v68, v68, v69\n"
    "v_add_u32 v13, v11, v3\n"
    "v_mfma_f32_16x16x32_bf16 v[64:67], a[20:23], v[20:23], v[64:67]\n"
    "ds_read_b128 v[20:23], v0 offset:5120\n"
    "v_cvt_pk_bf16_f32 v69, v70, v71\n"
    "v_cmp_gt_i32 vcc, s71, v13\n"
    "v_mfma_f32_16x16x32_bf16 v[48:51], a[24:27], v[24:27], v[48:51]\n"
    "s_nop 0\n"
    "v_mfma_f32_16x16x32_bf16 v[52:55], a[28:31], v[24:27], v[52:55]\n"
    "s_waitcnt lgkmcnt(2)\n"
    "v_permlane16_swap_b32 v68, v70\n"
    "v_mfma_f32_16x16x32_bf16 v[64:67], a[32:35], v[24:27], v[64:67]\n"
    "ds_read_b128 v[24:27], v0 offset:6144\n"
    "s_cbranch_vccnz .LgDOWN_poll0_%=\n"
    "v_mfma_f32_16x16x32_bf16 v[48:51], a[36:39], v[28:31], v[48:51]\n"
    "v_permlane16_swap_b32 v69, v71\n"
    "v_mfma_f32_16x16x32_bf16 v[52:55], a[40:43], v[28:31], v[52:55]\n"
    "s_waitcnt lgkmcnt(2)\n"
    "buffer_store_dwordx4 v[68:71], v4, s[36:39], s73 offen offset:1024 sc1\n"
    "v_mfma_f32_16x16x32_bf16 v[64:67], a[44:47], v[28:31], v[64:67]\n"
    "ds_read_b128 v[28:31], v0 offset:7168\n"
    "v_mfma_f32_16x16x32_bf16 v[48:51], a[48:51], v[16:19], v[48:51]\n"
    "s_add_u32 m0, s59, 36864\n"
    "s_lshr_b32 s73, s68, 1\n"
    "v_mfma_f32_16x16x32_bf16 v[52:55], a[52:55], v[16:19], v[52:55]\n"
    "s_waitcnt lgkmcnt(2)\n"
    "buffer_load_dwordx4 v1, s[36:39], s76 offen sc1 lds\n"
    "v_mfma_f32_16x16x32_bf16 v[64:67], a[56:59], v[16:19], v[64:67]\n"
    "ds_read_b128 v[16:19], v0 offset:8192\n"
    "v_mfma_f32_16x16x32_bf16 v[48:51], a[60:63], v[20:23], v[48:51]\n"
    "v_mfma_f32_16x16x32_bf16 v[52:55], a[64:67], v[20:23], v[52:55]\n"
    "s_waitcnt lgkmcnt(2)\n"
    "v_mfma_f32_16x16x32_bf16 v[64:67], a[68:71], v[20:23], v[64:67]\n"
    "ds_read_b128 v[20:23], v0 offset:9216\n"
    "v_mfma_f32_16x16x32_bf16 v[48:51], a[72:75], v[24:27], v[48:51]\n"
    "s_add_u32 m0, s59, 40960\n"
    "s_add_u32 s76, s76, 16384\n"
    "s_and_b32 s73, s73, s57\n"
    "v_mfma_f32_16x16x32_bf16 v[52:55], a[76:79], v[24:27], v[52:55]\n"
    "s_waitcnt lgkmcnt(2)\n"
    "buffer_load_dwordx4 v1, s[36:39], s76 offen sc1 lds\n"
    "v_mfma_f32_16x16x32_bf16 v[64:67], a[80:83], v[24:27], v[64:67]\n"
    "ds_read_b128 v[24:27], v0 offset:10240\n"
    "v_mfma_f32_16x16x32_bf16 v[48:51], a[84:87], v[28:31], v[48:51]\n"
    "v_mfma_f32_16x16x32_bf16 v[52:55], a[88:91], v[28:31], v[52:55]\n"
    "s_waitcnt lgkmcnt(2)\n"
    "v_mfma_f32_16x16x32_bf16 v[64:67], a[92:95], v[28:31], v[64:67]\n"
    "ds_read_b128 v[28:31], v0 offset:11264\n"
    "v_mfma_f32_16x16x32_bf16 v[48:51], a[96:99], v[16:19], v[48:51]\n"
    "s_add_u32 m0, s59, 45056\n"
    "s_add_u32 s76, s76, 16384\n"
    "s_mul_i32 s73, s73, s56\n"
    "v_mfma_f32_16x16x32_bf16 v[52:55], a[100:103], v[16:19], v[52:55]\n"
    "s_waitcnt lgkmcnt(2)\n"
    "buffer_load_dwordx4 v1, s[36:39], s76 offen sc1 lds\n"
    "v_mfma_f32_16x16x32_bf16 v[64:67], a[104:107], v[16:19], v[64:67]\n"
    "ds_read_b128 v[16:19], v0 offset:12288\n"
    "v_mfma_f32_16x16x32_bf16 v[48:51], a[108:111], v[20:23], v[48:51]\n"
    "v_mfma_f32_16x16x32_bf16 v[52:55], a[112:115], v[20:23], v[52:55]\n"
    "s_waitcnt lgkmcnt(2)\n"
    "v_mfma_f32_16x16x32_bf16 v[64:67], a[116:119], v[20:23], v[64:67]\n"
    "ds_read_b128 v[20:23], v0 offset:13312\n"
    "v_mfma_f32_16x16x32_bf16 v[48:51], a[120:123], v[24:27], v[48:51]\n"
    "s_add_u32 m0, s59, 49152\n"
    "s_add_u32 s76, s76, 16384\n"
    "s_add_u32 s73, s73, s55\n"
    "s_waitcnt vmcnt(7)\n"
    "buffer_atomic_smax v12, v10, s[36:39], s69 offen sc1\n"
    "v_mfma_f32_16x16x32_bf16 v[52:55], a[124:127], v[24:27], v[52:55]\n"
    "s_waitcnt lgkmcnt(2)\n"
    "buffer_load_dwordx4 v1, s[36:39], s76 offen sc1 lds\n"
    "v_mfma_f32_16x16x32_bf16 v[64:67], a[128:131], v[24:27], v[64:67]\n"
    "ds_read_b128 v[24:27], v0 offset:14336\n"
    "v_mfma_f32_16x16x32_bf16 v[48:51], a[132:135], v[28:31], v[48:51]\n"
    "v_mfma_f32_16x16x32_bf16 v[52:55], a[136:139], v[28:31], v[52:55]\n"
    "s_waitcnt lgkmcnt(2)\n"
    "v_mfma_f32_16x16x32_bf16 v[64:67], a[140:143], v[28:31], v[64:67]\n"
    "ds_read_b128 v[28:31], v0 offset:15360\n"
    "v_mfma_f32_16x16x32_bf16 v[48:51], a[144:147], v[16:19], v[48:51]\n"
    "s_add_u32 m0, s59, 53248\n"
    "s_add_u32 s76, s76, 16384\n"
    "v_mfma_f32_16x16x32_bf16 v[52:55], a[148:151], v[16:19], v[52:55]\n"
    "s_waitcnt lgkmcnt(2)\n"
    "buffer_load_dwordx4 v1, s[36:39], s76 offen sc1 lds\n"
    "v_mfma_f32_16x16x32_bf16 v[64:67], a[152:155], v[16:19], v[64:67]\n"
    "ds_read_b128 v[16:19], v0 offset:16384\n"
    "v_mfma_f32_16x16x32_bf16 v[48:51], a[156:159], v[20:23], v[48:51]\n"
    "v_mfma_f32_16x16x32_bf16 v[52:55], a[160:163], v[20:23], v[52:55]\n"
    "s_waitcnt lgkmcnt(2)\n"
    "v_mfma_f32_16x16x32_bf16 v[64:67], a[164:167], v[20:23], v[64:67]\n"
    "ds_read_b128 v[20:23], v0 offset:17408\n"
    "v_mfma_f32_16x16x32_bf16 v[48:51], a[168:171], v[24:27], v[48:51]\n"
    "v_mfma_f32_16x16x32_bf16 v[52:55], a[172:175], v[24:27], v[52:55]\n"
    "s_waitcnt lgkmcnt(2)\n"
    "v_mfma_f32_16x16x32_bf16 v[64:67], a[176:179], v[24:27], v[64:67]\n"
    "ds_read_b128 v[24:27], v0 offset:18432\n"
    "v_mfma_f32_16x16x32_bf16 v[48:51], a[180:183], v[28:31], v[48:51]\n"
    "s_add_u32 m0, s59, 57344\n"
    "s_add_u32 s76, s76, 16384\n"
    "v_mfma_f32_16x16x32_bf16 v[52:55], a[184:187], v[28:31], v[52:55]\n"
    "s_waitcnt lgkmcnt(2)\n"
    "buffer_load_dwordx4 v1, s[36:39], s76 offen sc1 lds\n"
    "v_mfma_f32_16x16x32_bf16 v[64:67], a[188:191], v[28:31], v[64:67]\n"
    "ds_read_b128 v[28:31], v0 offset:19456\n"
    "v_mfma_f32_16x16x32_bf16 v[48:51], a[192:195], v[16:19], v[48:51]\n"
    "v_mfma_f32_16x16x32_bf16 v[52:55], a[196:199], v[16:19], v[52:55]\n"
    "s_waitcnt lgkmcnt(2)\n"
    "v_mfma_f32_16x16x32_bf16 v[64:67], a[200:203], v[16:19], v[64:67]\n"
    "ds_read_b128 v[16:19], v0 offset:20480\n"
    "s_waitcnt vmcnt(7)\n"
    "buffer_atomic_smax v12, v10, s[36:39], s70 offen sc1\n"
    "v_mfma_f32_16x16x32_bf16 v[48:51], a[204:207], v[20:23], v[48:51]\n"
    "s_add_u32 m0, s59, 61440\n"
    "s_add_u32 s76, s76, 16384\n"
    "v_mfma_f32_16x16x32_bf16 v[52:55], a[208:211], v[20:23], v[52:55]\n"
    "s_waitcnt lgkmcnt(2)\n"
    "buffer_load_dwordx4 v1, s[36:39], s76 offen sc1 lds\n"
    "v_mfma_f32_16x16x32_bf16 v[64:67], a[212:215], v[20:23], v[64:67]\n"
    "ds_read_b128 v[20:23], v0 offset:21504\n"
    "v_mfma_f32_16x16x32_bf16 v[48:51], a[216:219], v[24:27], v[48:51]\n"
    "v_mfma_f32_16x16x32_bf16 v[52:55], a[220:223], v[24:27], v[52:55]\n"
    "s_waitcnt lgkmcnt(2)\n"
    "v_mfma_f32_16x16x32_bf16 v[64:67], a[224:227], v[24:27], v[64:67]\n"
    "ds_read_b128 v[24:27], v0 offset:22528\n"
    "v_mfma_f32_16x16x32_bf16 v[48:51], a[228:231], v[28:31], v[48:51]\n"
    "s_add_u32 m0, s59, 65536\n"
    "s_add_u32 s76, s76, 16384\n"
    "v_mfma_f32_16x16x32_bf16 v[52:55], a[232:235], v[28:31], v[52:55]\n"
    "s_waitcnt lgkmcnt(2)\n"
    "buffer_load_dwordx4 v1, s[36:39], s76 offen sc1 lds\n"
    "v_mfma_f32_16x16x32_bf16 v[64:67], a[236:239], v[28:31], v[64:67]\n"
    "ds_read_b128 v[28:31], v0 offset:23552\n"
    "v_mfma_f32_16x16x32_bf16 v[48:51], a[240:243], v[16:19], v[48:51]\n"
    "v_mfma_f32_16x16x32_bf16 v[52:55], a[244:247], v[16:19], v[52:55]\n"
    "s_waitcnt lgkmcnt(2)\n"
    "v_mfma_f32_16x16x32_bf16 v[64:67], a[248:251], v[16:19], v[64:67]\n"
    "ds_read_b128 v[16:19], v0 offset:24576\n"
    "v_mfma_f32_16x16x32_bf16 v[48:51], a[252:255], v[20:23], v[48:51]\n"
    "s_add_u32 m0, s59, 69632\n"
    "s_add_u32 s76, s76, 16384\n"
    "v_mfma_f32_16x16x32_bf16 v[52:55], v[80:83], v[20:23], v[52:55]\n"
    "s_waitcnt lgkmcnt(2)\n"
    "buffer_load_dwordx4 v1, s[36:39], s76 offen sc1 lds\n"
    "v_mfma_f32_16x16x32_bf16 v[64:67], v[84:87], v[20:23], v[64:67]\n"
    "ds_read_b128 v[20:23], v0 offset:25600\n"
    "v_mfma_f32_16x16x32_bf16 v[48:51], v[88:91], v[24:27], v[48:51]\n"
    "v_mfma_f32_16x16x32_bf16 v[52:55], v[92:95], v[24:27], v[52:55]\n"
    "s_waitcnt lgkmcnt(2)\n"
    "v_mfma_f32_16x16x32_bf16 v[64:67], v[96:99], v[24:27], v[64:67]\n"
    "ds_read_b128 v[24:27], v0 offset:26624\n"
    "v_mfma_f32_16x16x32_bf16 v[48:51], v[100:103], v[28:31], v[48:51]\n"
    "v_mfma_f32_16x16x32_bf16 v[52:55], v[104:107], v[28:31], v[52:55]\n"
    "s_waitcnt lgkmcnt(2)\n"
    "v_mfma_f32_16x16x32_bf16 v[64:67], v[108:111], v[28:31], v[64:67]\n"
    "ds_read_b128 v[28:31], v0 offset:27648\n"
    ".LgDOWN_join0_%=:\n"
    "v_mfma_f32_16x16x32_bf16 v[48:51], v[112:115], v[16:19], v[48:51]\n"
    "s_add_u32 s71, s68, 2\n"
    "s_waitcnt vmcnt(9)\n"
    "v_mfma_f32_16x16x32_bf16 v[52:55], v[116:119], v[16:19], v[52:55]\n"
    "s_waitcnt lgkmcnt(2)\n"
    "v_add_u32 v13, v11, v3\n"
    "v_mfma_f32_16x16x32_bf16 v[64:67], v[120:123], v[16:19], v[64:67]\n"
    "ds_read_b128 v[16:19], v0 offset:28672\n"
    "v_cmp_gt_i32 vcc, s71, v13\n"
    "v_mfma_f32_16x16x32_bf16 v[48:51], v[124:127], v[20:23], v[48:51]\n"
    "v_mfma_f32_16x16x32_bf16 v[52:55], v[128:131], v[20:23], v[52:55]\n"
    "s_waitcnt lgkmcnt(2)\n"
    "v_mfma_f32_16x16x32_bf16 v[64:67], v[132:135], v[20:23], v[64:67]\n"
    "ds_read_b128 v[20:23], v0 offset:29696\n"
    "s_or_b64 exec, vcc, s[62:63]\n"
    "buffer_atomic_smax v11, v2, s[36:39], 0 offen sc0 sc1\n"
    "s_mov_b64 exec, -1\n"
    "v_mfma_f32_16x16x32_bf16 v[48:51], v[136:139], v[24:27], v[48:51]\n"
    "v_mfma_f32_16x16x32_bf16 v[52:55], v[140:143], v[24:27], v[52:55]\n"
    "s_waitcnt lgkmcnt(2)\n"
    "s_lshr_b32 s76, s71, 1\n"
    "v_mfma_f32_16x16x32_bf16 v[64:67], v[144:147], v[24:27], v[64:67]\n"
    "ds_read_b128 v[24:27], v0 offset:30720\n"
    "s_and_b32 s76, s76, s54\n"
    "v_mfma_f32_16x16x32_bf16 v[48:51], v[148:151], v[28:31], v[48:51]\n"
    "s_mul_i32 s76, s76, s53\n"
    "v_mfma_f32_16x16x32_bf16 v[52:55], v[152:155], v[28:31], v[52:55]\n"
    "s_waitcnt lgkmcnt(2)\n"
    "s_add_u32 s76, s76, s52\n"
    "v_mfma_f32_16x16x32_bf16 v[64:67], v[156:159], v[28:31], v[64:67]\n"
    "ds_read_b128 v[28:31], v0 offset:31744\n"
    "v_mfma_f32_16x16x32_bf16 v[48:51], v[160:163], v[16:19], v[48:51]\n"
    "v_mfma_f32_16x16x32_bf16 v[52:55], v[164:167], v[16:19], v[52:55]\n"
    "s_waitcnt lgkmcnt(2)\n"
    "v_mfma_f32_16x16x32_bf16 v[64:67], v[168:171], v[16:19], v[64:67]\n"
    "ds_read_b128 v[16:19], v0 offset:32768\n"
    "v_mfma_f32_16x16x32_bf16 v[48:51], v[172:175], v[20:23], v[48:51]\n"
    "v_mfma_f32_16x16x32_bf16 v[52:55], v[176:179], v[20:23], v[52:55]\n"
    "s_waitcnt lgkmcnt(2)\n"
    "v_mfma_f32_16x16x32_bf16 v[64:67], v[180:183], v[20:23], v[64:67]\n"
    "ds_read_b128 v[20:23], v0 offset:33792\n"
    "v_mfma_f32_16x16x32_bf16 v[48:51], v[184:187], v[24:27], v[48:51]\n"
    "v_mfma_f32_16x16x32_bf16 v[52:55], v[188:191], v[24:27], v[52:55]\n"
    "s_waitcnt lgkmcnt(2)\n"
    "v_mfma_f32_16x16x32_bf16 v[64:67], v[192:195], v[24:27], v[64:67]\n"
    "ds_read_b128 v[24:27], v0 offset:34816\n"
    "v_mfma_f32_16x16x32_bf16 v[48:51], v[196:199], v[28:31], v[48:51]\n"
    "v_add_u32 v13, v5, v3\n"
    "v_mfma_f32_16x16x32_bf16 v[52:55], v[200:203], v[28:31], v[52:55]\n"
    "s_waitcnt lgkmcnt(2)\n"
    "v_cmp_gt_i32 vcc, s71, v13\n"
    "v_mfma_f32_16x16x32_bf16 v[64:67], v[204:207], v[28:31], v[64:67]\n"
    "ds_read_b128 v[28:31], v0 offset:35840\n"
    "v_mfma_f32_16x16x32_bf16 v[48:51], v[208:211], v[16:19], v[48:51]\n"
    "v_mfma_f32_16x16x32_bf16 v[52:55], v[212:215], v[16:19], v[52:55]\n"
    "s_waitcnt lgkmcnt(2)\n"
    "s_or_b64 exec, vcc, s[62:63]\n"
    "buffer_atomic_smax v5, v2, s[36:39], 0 offen sc0 sc1\n"
    "s_mov_b64 exec, -1\n"
    "v_mfma_f32_16x16x32_bf16 v[64:67], v[216:219], v[16:19], v[64:67]\n"
    "v_mfma_f32_16x16x32_bf16 v[48:51], v[220:223], v[20:23], v[48:51]\n"
    "s_waitcnt vmcnt(2) lgkmcnt(0)\n"
    "s_barrier\n"
    "ds_read_b128 v[16:19], v9 offset:0\n"
    "v_mfma_f32_16x16x32_bf16 v[52:55], v[224:227], v[20:23], v[52:55]\n"
    "v_mfma_f32_16x16x32_bf16 v[64:67], v[228:231], v[20:23], v[64:67]\n"
    "ds_read_b128 v[20:23], v9 offset:1024\n"
    "v_mfma_f32_16x16x32_bf16 v[48:51], v[232:235], v[24:27], v[48:51]\n"
    "v_mfma_f32_16x16x32_bf16 v[52:55], v[236:239], v[24:27], v[52:55]\n"
    "v_mfma_f32_16x16x32_bf16 v[64:67], v[240:243], v[24:27], v[64:67]\n"
    "ds_read_b128 v[24:27], v9 offset:2048\n"
    "s_mov_b32 s69, 0x80000000\n"
    "v_mfma_f32_16x16x32_bf16 v[48:51], v[244:247], v[28:31], v[48:51]\n"
    "s_mov_b32 s70, 0\n"
    "v_mfma_f32_16x16x32_bf16 v[52:55], v[248:251], v[28:31], v[52:55]\n"
    "s_waitcnt lgkmcnt(2)\n"
    "v_mov_b32 v12, s68\n"
    "v_mfma_f32_16x16x32_bf16 v[64:67], v[252:255], v[28:31], v[64:67]\n"
    "ds_read_b128 v[28:31], v9 offset:3072\n"
    "s_add_u32 s68, s68, 1\n"
    ".LgDOWN_head1_%=:\n"
    "v_mfma_f32_16x16x32_bf16 v[56:59], a[0:3], v[16:19], 0\n"
    "v_mfma_f32_16x16x32_bf16 v[60:63], a[4:7], v[16:19], 0\n"
    "s_waitcnt lgkmcnt(2)\n"
    "v_cvt_pk_bf16_f32 v48, v48, v49\n"
    "v_cvt_pk_bf16_f32 v49, v50, v51\n"
    "v_mfma_f32_16x16x32_bf16 v[68:71], a[8:11], v[16:19], 0\n"
    "ds_read_b128 v[16:19], v9 offset:4096\n"
    "v_cvt_pk_bf16_f32 v50, v52, v53\n"
    "v_mfma_f32_16x16x32_bf16 v[56:59], a[12:15], v[20:23], v[56:59]\n"
    "v_cvt_pk_bf16_f32 v51, v54, v55\n"
    "v_mfma_f32_16x16x32_bf16 v[60:63], a[16:19], v[20:23], v[60:63]\n"
    "s_waitcnt lgkmcnt(2)\n"
    "s_waitcnt vmcnt(1)\n"
    "buffer_store_dwordx4 v[48:51], v15, s[36:39], s73 offen sc1\n"
    "v_cvt_pk_bf16_f32 v64, v64, v65\n"
    "v_add_u32 v13, v11, v3\n"
    "v_mfma_f32_16x16x32_bf16 v[68:71], a[20:23], v[20:23], v[68:71]\n"
    "ds_read_b128 v[20:23], v9 offset:5120\n"
    "v_cvt_pk_bf16_f32 v65, v66, v67\n"
    "v_cmp_gt_i32 vcc, s71, v13\n"
    "v_mfma_f32_16x16x32_bf16 v[56:59], a[24:27], v[24:27], v[56:59]\n"
    "s_nop 0\n"
    "v_mfma_f32_16x16x32_bf16 v[60:63], a[28:31], v[24:27], v[60:63]\n"
    "s_waitcnt lgkmcnt(2)\n"
    "v_permlane16_swap_b32 v64, v66\n"
    "v_mfma_f32_16x16x32_bf16 v[68:71], a[32:35], v[24:27], v[68:71]\n"
    "ds_read_b128 v[24:27], v9 offset:6144\n"
    "s_cbranch_vccnz .LgDOWN_poll1_%=\n"
    "v_mfma_f32_16x16x32_bf16 v[56:59], a[36:39], v[28:31], v[56:59]\n"
    "v_permlane16_swap_b32 v65, v67\n"
    "v_mfma_f32_16x16x32_bf16 v[60:63], a[40:43], v[28:31], v[60:63]\n"
    "s_waitcnt lgkmcnt(2)\n"
    "buffer_store_dwordx4 v[64:67], v4, s[36:39], s73 offen sc1\n"
    "v_mfma_f32_16x16x32_bf16 v[68:71], a[44:47], v[28:31], v[68:71]\n"
    "ds_read_b128 v[28:31], v9 offset:7168\n"
    "v_mfma_f32_16x16x32_bf16 v[56:59], a[48:51], v[16:19], v[56:59]\n"
    "s_add_u32 m0, s59, 0\n"
    "s_lshr_b32 s73, s68, 1\n"
    "v_mfma_f32_16x16x32_bf16 v[60:63], a[52:55], v[16:19], v[60:63]\n"
    "s_waitcnt lgkmcnt(2)\n"
    "buffer_load_dwordx4 v1, s[36:39], s76 offen sc1 lds\n"
    "v_mfma_f32_16x16x32_bf16 v[68:71], a[56:59], v[16:19], v[68:71]\n"
    "ds_read_b128 v[16:19], v9 offset:8192\n"
    "v_mfma_f32_16x16x32_bf16 v[56:59], a[60:63], v[20:23], v[56:59]\n"
    "v_mfma_f32_16x16x32_bf16 v[60:63], a[64:67], v[20:23], v[60:63]\n"
    "s_waitcnt lgkmcnt(2)\n"
    "v_mfma_f32_16x16x32_bf16 v[68:71], a[68:71], v[20:23], v[68:71]\n"
    "ds_read_b128 v[20:23], v9 offset:9216\n"
    "v_mfma_f32_16x16x32_bf16 v[56:59], a[72:75], v[24:27], v[56:59]\n"
    "s_add_u32 m0, s59, 4096\n"
    "s_add_u32 s76, s76, 16384\n"
    "s_and_b32 s73, s73, s57\n"
    "v_mfma_f32_16x16x32_bf16 v[60:63], a[76:79], v[24:27], v[60:63]\n"
    "s_waitcnt lgkmcnt(2)\n"
    "buffer_load_dwordx4 v1, s[36:39], s76 offen sc1 lds\n"
    "v_mfma_f32_16x16x32_bf16 v[68:71], a[80:83], v[24:27], v[68:71]\n"
    "ds_read_b128 v[24:27], v9 offset:10240\n"
    "v_mfma_f32_16x16x32_bf16 v[56:59], a[84:87], v[28:31], v[56:59]\n"
    "v_mfma_f32_16x16x32_bf16 v[60:63], a[88:91], v[28:31], v[60:63]\n"
    "s_waitcnt lgkmcnt(2)\n"
    "v_mfma_f32_16x16x32_bf16 v[68:71], a[92:95], v[28:31], v[68:71]\n"
    "ds_read_b128 v[28:31], v9 offset:11264\n"
    "v_mfma_f32_16x16x32_bf16 v[56:59], a[96:99], v[16:19], v[56:59]\n"
    "s_add_u32 m0, s59, 8192\n"
    "s_add_u32 s76, s76, 16384\n"
    "s_mul_i32 s73, s73, s56\n"
    "v_mfma_f32_16x16x32_bf16 v[60:63], a[100:103], v[16:19], v[60:63]\n"
    "s_waitcnt lgkmcnt(2)\n"
    "buffer_load_dwordx4 v1, s[36:39], s76 offen sc1 lds\n"
    "v_mfma_f32_16x16x32_bf16 v[68:71], a[104:107], v[16:19], v[68:71]\n"
    "ds_read_b128 v[16:19], v9 offset:12288\n"
    "v_mfma_f32_16x16x32_bf16 v[56:59], a[108:111], v[20:23], v[56:59]\n"
    "v_mfma_f32_16x16x32_bf16 v[60:63], a[112:115], v[20:23], v[60:63]\n"
    "s_waitcnt lgkmcnt(2)\n"
    "v_mfma_f32_16x16x32_bf16 v[68:71], a[116:119], v[20:23], v[68:71]\n"
    "ds_read_b128 v[20:23], v9 offset:13312\n"
    "v_mfma_f32_16x16x32_bf16 v[56:59], a[120:123], v[24:27], v[56:59]\n"
    "s_add_u32 m0, s59, 12288\n"
    "s_add_u32 s76, s76, 16384\n"
    "s_add_u32 s73, s73, s55\n"
    "s_waitcnt vmcnt(7)\n"
    "buffer_atomic_smax v12, v14, s[36:39], s69 offen sc1\n"
    "v_mfma_f32_16x16x32_bf16 v[60:63], a[124:127], v[24:27], v[60:63]\n"
    "s_waitcnt lgkmcnt(2)\n"
    "buffer_load_dwordx4 v1, s[36:39], s76 offen sc1 lds\n"
    "v_mfma_f32_16x16x32_bf16 v[68:71], a[128:131], v[24:27], v[68:71]\n"
    "ds_read_b128 v[24:27], v9 offset:14336\n"
    "v_mfma_f32_16x16x32_bf16 v[56:59], a[132:135], v[28:31], v[56:59]\n"
    "v_mfma_f32_16x16x32_bf16 v[60:63], a[136:139], v[28:31], v[60:63]\n"
    "s_waitcnt lgkmcnt(2)\n"
    "v_mfma_f32_16x16x32_bf16 v[68:71], a[140:143], v[28:31], v[68:71]\n"
    "ds_read_b128 v[28:31], v9 offset:15360\n"
    "v_mfma_f32_16x16x32_bf16 v[56:59], a[144:147], v[16:19], v[56:59]\n"
    "s_add_u32 m0, s59, 16384\n"
    "s_add_u32 s76, s76, 16384\n"
    "v_mfma_f32_16x16x32_bf16 v[60:63], a[148:151], v[16:19], v[60:63]\n"
    "s_waitcnt lgkmcnt(2)\n"
    "buffer_load_dwordx4 v1, s[36:39], s76 offen sc1 lds\n"
    "v_mfma_f32_16x16x32_bf16 v[68:71], a[152:155], v[16:19], v[68:71]\n"
    "ds_read_b128 v[16:19], v9 offset:16384\n"
    "v_mfma_f32_16x16x32_bf16 v[56:59], a[156:159], v[20:23], v[56:59]\n"
    "v_mfma_f32_16x16x32_bf16 v[60:63], a[160:163], v[20:23], v[60:63]\n"
    "s_waitcnt lgkmcnt(2)\n"
    "v_mfma_f32_16x16x32_bf16 v[68:71], a[164:167], v[20:23], v[68:71]\n"
    "ds_read_b128 v[20:23], v9 offset:17408\n"
    "v_mfma_f32_16x16x32_bf16 v[56:59], a[168:171], v[24:27], v[56:59]\n"
    "v_mfma_f32_16x16x32_bf16 v[60:63], a[172:175], v[24:27], v[60:63]\n"
    "s_waitcnt lgkmcnt(2)\n"
    "v_mfma_f32_16x16x32_bf16 v[68:71], a[176:179], v[24:27], v[68:71]\n"
    "ds_read_b128 v[24:27], v9 offset:18432\n"
    "v_mfma_f32_16x16x32_bf16 v[56:59], a[180:183], v[28:31], v[56:59]\n"
    "s_add_u32 m0, s59, 20480\n"
    "s_add_u32 s76, s76, 16384\n"
    "v_mfma_f32_16x16x32_bf16 v[60:63], a[184:187], v[28:31], v[60:63]\n"
    "s_waitcnt lgkmcnt(2)\n"
    "buffer_load_dwordx4 v1, s[36:39], s76 offen sc1 lds\n"
    "v_mfma_f32_16x16x32_bf16 v[68:71], a[188:191], v[28:31], v[68:71]\n"
    "ds_read_b128 v[28:31], v9 offset:19456\n"
    "v_mfma_f32_16x16x32_bf16 v[56:59], a[192:195], v[16:19], v[56:59]\n"
    "v_mfma_f32_16x16x32_bf16 v[60:63], a[196:199], v[16:19], v[60:63]\n"
    "s_waitcnt lgkmcnt(2)\n"
    "v_mfma_f32_16x16x32_bf16 v[68:71], a[200:203], v[16:19], v[68:71]\n"
    "ds_read_b128 v[16:19], v9 offset:20480\n"
    "s_waitcnt vmcnt(7)\n"
    "buffer_atomic_smax v12, v14, s[36:39], s70 offen sc1\n"
    "v_mfma_f32_16x16x32_bf16 v[56:59], a[204:207], v[20:23], v[56:59]\n"
    "s_add_u32 m0, s59, 24576\n"
    "s_add_u32 s76, s76, 16384\n"
    "v_mfma_f32_16x16x32_bf16 v[60:63], a[208:211], v[20:23], v[60:63]\n"
    "s_waitcnt lgkmcnt(2)\n"
    "buffer_load_dwordx4 v1, s[36:39], s76 offen sc1 lds\n"
    "v_mfma_f32_16x16x32_bf16 v[68:71], a[212:215], v[20:23], v[68:71]\n"
    "ds_read_b128 v[20:23], v9 offset:21504\n"
    "v_mfma_f32_16x16x32_bf16 v[56:59], a[216:219], v[24:27], v[56:59]\n"
    "v_mfma_f32_16x16x32_bf16 v[60:63], a[220:223], v[24:27], v[60:63]\n"
    "s_waitcnt lgkmcnt(2)\n"
    "v_mfma_f32_16x16x32_bf16 v[68:71], a[224:227], v[24:27], v[68:71]\n"
    "ds_read_b128 v[24:27], v9 offset:22528\n"
    "v_mfma_f32_16x16x32_bf16 v[56:59], a[228:231], v[28:31], v[56:59]\n"
    "s_add_u32 m0, s59, 28672\n"
    "s_add_u32 s76, s76, 16384\n"
    "v_mfma_f32_16x16x32_bf16 v[60:63], a[232:235], v[28:31], v[60:63]\n"
    "s_waitcnt lgkmcnt(2)\n"
    "buffer_load_dwordx4 v1, s[36:39], s76 offen sc1 lds\n"
    "v_mfma_f32_16x16x32_bf16 v[68:71], a[236:239], v[28:31], v[68:71]\n"
    "ds_read_b128 v[28:31], v9 offset:23552\n"
    "v_mfma_f32_16x16x32_bf16 v[56:59], a[240:243], v[16:19], v[56:59]\n"
    "v_mfma_f32_16x16x32_bf16 v[60:63], a[244:247], v[16:19], v[60:63]\n"
    "s_waitcnt lgkmcnt(2)\n"
    "v_mfma_f32_16x16x32_bf16 v[68:71], a[248:251], v[16:19], v[68:71]\n"
    "ds_read_b128 v[16:19], v9 offset:24576\n"
    "v_mfma_f32_16x16x32_bf16 v[56:59], a[252:255], v[20:23], v[56:59]\n"
    "s_add_u32 m0, s59, 32768\n"
    "s_add_u32 s76, s76, 16384\n"
    "v_mfma_f32_16x16x32_bf16 v[60:63], v[80:83], v[20:23], v[60:63]\n"
    "s_waitcnt lgkmcnt(2)\n"
    "buffer_load_dwordx4 v1, s[36:39], s76 offen sc1 lds\n"
    "v_mfma_f32_16x16x32_bf16 v[68:71], v[84:87], v[20:23], v[68:71]\n"
    "ds_read_b128 v[20:23], v9 offset:25600\n"
    "v_mfma_f32_16x16x32_bf16 v[56:59], v[88:91], v[24:27], v[56:59]\n"
    "v_mfma_f32_16x16x32_bf16 v[60:63], v[92:95], v[24:27], v[60:63]\n"
    "s_waitcnt lgkmcnt(2)\n"
    "v_mfma_f32_16x16x32_bf16 v[68:71], v[96:99], v[24:27], v[68:71]\n"
    "ds_read_b128 v[24:27], v9 offset:26624\n"
    "v_mfma_f32_16x16x32_bf16 v[56:59], v[100:103], v[28:31], v[56:59]\n"
    "v_mfma_f32_16x16x32_bf16 v[60:63], v[104:107], v[28:31], v[60:63]\n"
    "s_waitcnt lgkmcnt(2)\n"
    "v_mfma_f32_16x16x32_bf16 v[68:71], v[108:111], v[28:31], v[68:71]\n"
    "ds_read_b128 v[28:31], v9 offset:27648\n"
    ".LgDOWN_join1_%=:\n"
    "v_mfma_f32_16x16x32_bf16 v[56:59], v[112:115], v[16:19], v[56:59]\n"
    "s_add_u32 s71, s68, 2\n"
    "s_waitcnt vmcnt(9)\n"
    "v_mfma_f32_16x16x32_bf16 v[60:63], v[116:119], v[16:19], v[60:63]\n"
    "s_waitcnt lgkmcnt(2)\n"
    "v_add_u32 v13, v11, v3\n"
    "v_mfma_f32_16x16x32_bf16 v[68:71], v[120:123], v[16:19], v[68:71]\n"
    "ds_read_b128 v[16:19], v9 offset:28672\n"
    "v_cmp_gt_i32 vcc, s71, v13\n"
    "v_mfma_f32_16x16x32_bf16 v[56:59], v[124:127], v[20:23], v[56:59]\n"
    "v_mfma_f32_16x16x32_bf16 v[60:63], v[128:131], v[20:23], v[60:63]\n"
    "s_waitcnt lgkmcnt(2)\n"
    "v_mfma_f32_16x16x32_bf16 v[68:71], v[132:135], v[20:23], v[68:71]\n"
    "ds_read_b128 v[20:23], v9 offset:29696\n"
    "s_or_b64 exec, vcc, s[62:63]\n"
    "buffer_atomic_smax v11, v2, s[36:39], 0 offen sc0 sc1\n"
    "s_mov_b64 exec, -1\n"
    "v_mfma_f32_16x16x32_bf16 v[56:59], v[136:139], v[24:27], v[56:59]\n"
    "v_mfma_f32_16x16x32_bf16 v[60:63], v[140:143], v[24:27], v[60:63]\n"
    "s_waitcnt lgkmcnt(2)\n"
    "s_lshr_b32 s76, s71, 1\n"
    "v_mfma_f32_16x16x32_bf16 v[68:71], v[144:147], v[24:27], v[68:71]\n"
    "ds_read_b128 v[24:27], v9 offset:30720\n"
    "s_and_b32 s76, s76, s54\n"
    "v_mfma_f32_16x16x32_bf16 v[56:59], v[148:151], v[28:31], v[56:59]\n"
    "s_mul_i32 s76, s76, s53\n"
    "v_mfma_f32_16x16x32_bf16 v[60:63], v[152:155], v[28:31], v[60:63]\n"
    "s_waitcnt lgkmcnt(2)\n"
    "s_add_u32 s76, s76, s52\n"
    "v_mfma_f32_16x16x32_bf16 v[68:71], v[156:159], v[28:31], v[68:71]\n"
    "ds_read_b128 v[28:31], v9 offset:31744\n"
    "s_add_u32 s76, s76, 1024\n"
    "v_mfma_f32_16x16x32_bf16 v[56:59], v[160:163], v[16:19], v[56:59]\n"
    "v_mfma_f32_16x16x32_bf16 v[60:63], v[164:167], v[16:19], v[60:63]\n"
    "s_waitcnt lgkmcnt(2)\n"
    "v_mfma_f32_16x16x32_bf16 v[68:71], v[168:171], v[16:19], v[68:71]\n"
    "ds_read_b128 v[16:19], v9 offset:32768\n"
    "v_mfma_f32_16x16x32_bf16 v[56:59], v[172:175], v[20:23], v[56:59]\n"
    "v_mfma_f32_16x16x32_bf16 v[60:63], v[176:179], v[20:23], v[60:63]\n"
    "s_waitcnt lgkmcnt(2)\n"
    "v_mfma_f32_16x16x32_bf16 v[68:71], v[180:183], v[20:23], v[68:71]\n"
    "ds_read_b128 v[20:23], v9 offset:33792\n"
    "v_mfma_f32_16x16x32_bf16 v[56:59], v[184:187], v[24:27], v[56:59]\n"
    "v_mfma_f32_16x16x32_bf16 v[60:63], v[188:191], v[24:27], v[60:63]\n"
    "s_waitcnt lgkmcnt(2)\n"
    "v_mfma_f32_16x16x32_bf16 v[68:71], v[192:195], v[24:27], v[68:71]\n"
    "ds_read_b128 v[24:27], v9 offset:34816\n"
    "v_mfma_f32_16x16x32_bf16 v[56:59], v[196:199], v[28:31], v[56:59]\n"
    "v_add_u32 v13, v5, v3\n"
    "v_mfma_f32_16x16x32_bf16 v[60:63], v[200:203], v[28:31], v[60:63]\n"
    "s_waitcnt lgkmcnt(2)\n"
    "v_cmp_gt_i32 vcc, s71, v13\n"
    "v_mfma_f32_16x16x32_bf16 v[68:71], v[204:207], v[28:31], v[68:71]\n"
    "ds_read_b128 v[28:31], v9 offset:35840\n"
    "v_mfma_f32_16x16x32_bf16 v[56:59], v[208:211], v[16:19], v[56:59]\n"
    "v_mfma_f32_16x16x32_bf16 v[60:63], v[212:215], v[16:19], v[60:63]\n"
    "s_waitcnt lgkmcnt(2)\n"
    "s_or_b64 exec, vcc, s[62:63]\n"
    "buffer_atomic_smax v5, v2, s[36:39], 0 offen sc0 sc1\n"
    "s_mov_b64 exec, -1\n"
    "v_mfma_f32_16x16x32_bf16 v[68:71], v[216:219], v[16:19], v[68:71]\n"
    "v_mfma_f32_16x16x32_bf16 v[56:59], v[220:223], v[20:23], v[56:59]\n"
    "s_waitcnt vmcnt(2) lgkmcnt(0)\n"
    "s_barrier\n"
    "ds_read_b128 v[16:19], v0 offset:0\n"
    "v_mfma_f32_16x16x32_bf16 v[60:63], v[224:227], v[20:23], v[60:63]\n"
    "v_mfma_f32_16x16x32_bf16 v[68:71], v[228:231], v[20:23], v[68:71]\n"
    "ds_read_b128 v[20:23], v0 offset:1024\n"
    "v_mfma_f32_16x16x32_bf16 v[56:59], v[232:235], v[24:27], v[56:59]\n"
    "v_mfma_f32_16x16x32_bf16 v[60:63], v[236:239], v[24:27], v[60:63]\n"
    "v_mfma_f32_16x16x32_bf16 v[68:71], v[240:243], v[24:27], v[68:71]\n"
    "ds_read_b128 v[24:27], v0 offset:2048\n"
    "s_mov_b32 s69, 0x80000000\n"
    "v_mfma_f32_16x16x32_bf16 v[56:59], v[244:247], v[28:31], v[56:59]\n"
    "s_mov_b32 s70, 0\n"
    "v_mfma_f32_16x16x32_bf16 v[60:63], v[248:251], v[28:31], v[60:63]\n"
    "s_waitcnt lgkmcnt(2)\n"
    "v_mov_b32 v12, s68\n"
    "v_mfma_f32_16x16x32_bf16 v[68:71], v[252:255], v[28:31], v[68:71]\n"
    "ds_read_b128 v[28:31], v0 offset:3072\n"
    "s_add_u32 s68, s68, 1\n"
    "s_branch .LgDOWN_head0_%=\n"
    ".LgDOWN_LOOPE_%=:\n"
    ".LgDOWN_done_%=:\n"
    "s_waitcnt vmcnt(0)\n"
    "buffer_atomic_smax v12, v10, s[36:39], 0 offen sc1\n"
    "s_waitcnt vmcnt(0)\n"
    "s_branch .LgDOWN_FIN_%=\n"
    ".LgDOWN_poll0_%=:\n"
    "v_mfma_f32_16x16x32_bf16 v[48:51], a[36:39], v[28:31], v[48:51]\n"
    "s_or_b64 exec, vcc, s[62:63]\n"
    "buffer_atomic_smax v11, v2, s[36:39], 0 offen sc0 sc1\n"
    "s_mov_b64 exec, -1\n"
    "s_nop 0\n"
    "v_permlane16_swap_b32 v69, v71\n"
    "v_mfma_f32_16x16x32_bf16 v[52:55], a[40:43], v[28:31], v[52:55]\n"
    "s_waitcnt lgkmcnt(2)\n"
    "buffer_store_dwordx4 v[68:71], v4, s[36:39], s73 offen offset:1024 sc1\n"
    "v_mfma_f32_16x16x32_bf16 v[64:67], a[44:47], v[28:31], v[64:67]\n"
    "ds_read_b128 v[28:31], v0 offset:7168\n"
    "v_mfma_f32_16x16x32_bf16 v[48:51], a[48:51], v[16:19], v[48:51]\n"
    "s_lshr_b32 s73, s68, 1\n"
    "v_mfma_f32_16x16x32_bf16 v[52:55], a[52:55], v[16:19], v[52:55]\n"
    "s_waitcnt lgkmcnt(2)\n"
    "v_mfma_f32_16x16x32_bf16 v[64:67], a[56:59], v[16:19], v[64:67]\n"
    "ds_read_b128 v[16:19], v0 offset:8192\n"
    "v_mfma_f32_16x16x32_bf16 v[48:51], a[60:63], v[20:23], v[48:51]\n"
    "v_mfma_f32_16x16x32_bf16 v[52:55], a[64:67], v[20:23], v[52:55]\n"
    "s_waitcnt lgkmcnt(2)\n"
    "v_mfma_f32_16x16x32_bf16 v[64:67], a[68:71], v[20:23], v[64:67]\n"
    "ds_read_b128 v[20:23], v0 offset:9216\n"
    "v_mfma_f32_16x16x32_bf16 v[48:51], a[72:75], v[24:27], v[48:51]\n"
    "s_and_b32 s73, s73, s57\n"
    "v_mfma_f32_16x16x32_bf16 v[52:55], a[76:79], v[24:27], v[52:55]\n"
    "s_waitcnt lgkmcnt(2)\n"
    "v_mfma_f32_16x16x32_bf16 v[64:67], a[80:83], v[24:27], v[64:67]\n"
    "ds_read_b128 v[24:27], v0 offset:10240\n"
    "v_mfma_f32_16x16x32_bf16 v[48:51], a[84:87], v[28:31], v[48:51]\n"
    "v_mfma_f32_16x16x32_bf16 v[52:55], a[88:91], v[28:31], v[52:55]\n"
    "s_waitcnt lgkmcnt(2)\n"
    "v_mfma_f32_16x16x32_bf16 v[64:67], a[92:95], v[28:31], v[64:67]\n"
    "ds_read_b128 v[28:31], v0 offset:11264\n"
    "v_mfma_f32_16x16x32_bf16 v[48:51], a[96:99], v[16:19], v[48:51]\n"
    "s_mul_i32 s73, s73, s56\n"
    "s_waitcnt vmcnt(3)\n"
    "v_add_u32 v13, v5, v3\n"
    "v_mfma_f32_16x16x32_bf16 v[52:55], a[100:103], v[16:19], v[52:55]\n"
    "s_waitcnt lgkmcnt(2)\n"
    "v_cmp_gt_i32 vcc, s71, v13\n"
    "v_mfma_f32_16x16x32_bf16 v[64:67], a[104:107], v[16:19], v[64:67]\n"
    "ds_read_b128 v[16:19], v0 offset:12288\n"
    "v_mfma_f32_16x16x32_bf16 v[48:51], a[108:111], v[20:23], v[48:51]\n"
    "v_mfma_f32_16x16x32_bf16 v[52:55], a[112:115], v[20:23], v[52:55]\n"
    "s_waitcnt lgkmcnt(2)\n"
    "s_cbranch_vccz .LgDOWN_dma0_29_%=\n"
    "v_mfma_f32_16x16x32_bf16 v[64:67], a[116:119], v[20:23], v[64:67]\n"
    "ds_read_b128 v[20:23], v0 offset:13312\n"
    "s_or_b64 exec, vcc, s[62:63]\n"
    "buffer_atomic_smax v5, v2, s[36:39], 0 offen sc0 sc1\n"
    "s_mov_b64 exec, -1\n"
    "v_mfma_f32_16x16x32_bf16 v[48:51], a[120:123], v[24:27], v[48:51]\n"
    "s_add_u32 s73, s73, s55\n"
    "s_waitcnt vmcnt(6)\n"
    "buffer_atomic_smax v12, v10, s[36:39], s69 offen sc1\n"
    "v_mfma_f32_16x16x32_bf16 v[52:55], a[124:127], v[24:27], v[52:55]\n"
    "s_waitcnt lgkmcnt(2)\n"
    "v_mfma_f32_16x16x32_bf16 v[64:67], a[128:131], v[24:27], v[64:67]\n"
    "ds_read_b128 v[24:27], v0 offset:14336\n"
    "v_mfma_f32_16x16x32_bf16 v[48:51], a[132:135], v[28:31], v[48:51]\n"
    "v_mfma_f32_16x16x32_bf16 v[52:55], a[136:139], v[28:31], v[52:55]\n"
    "s_waitcnt lgkmcnt(2)\n"
    "v_mfma_f32_16x16x32_bf16 v[64:67], a[140:143], v[28:31], v[64:67]\n"
    "ds_read_b128 v[28:31], v0 offset:15360\n"
    "v_mfma_f32_16x16x32_bf16 v[48:51], a[144:147], v[16:19], v[48:51]\n"
    "v_mfma_f32_16x16x32_bf16 v[52:55], a[148:151], v[16:19], v[52:55]\n"
    "s_waitcnt lgkmcnt(2)\n"
    "v_mfma_f32_16x16x32_bf16 v[64:67], a[152:155], v[16:19], v[64:67]\n"
    "ds_read_b128 v[16:19], v0 offset:16384\n"
    "v_mfma_f32_16x16x32_bf16 v[48:51], a[156:159], v[20:23], v[48:51]\n"
    "v_mfma_f32_16x16x32_bf16 v[52:55], a[160:163], v[20:23], v[52:55]\n"
    "s_waitcnt lgkmcnt(2)\n"
    "v_mfma_f32_16x16x32_bf16 v[64:67], a[164:167], v[20:23], v[64:67]\n"
    "ds_read_b128 v[20:23], v0 offset:17408\n"
    "v_mfma_f32_16x16x32_bf16 v[48:51], a[168:171], v[24:27], v[48:51]\n"
    "v_mfma_f32_16x16x32_bf16 v[52:55], a[172:175], v[24:27], v[52:55]\n"
    "s_waitcnt lgkmcnt(2)\n"
    "v_mfma_f32_16x16x32_bf16 v[64:67], a[176:179], v[24:27], v[64:67]\n"
    "ds_read_b128 v[24:27], v0 offset:18432\n"
    "s_waitcnt vmcnt(3)\n"
    "v_add_u32 v13, v11, v3\n"
    "v_mfma_f32_16x16x32_bf16 v[48:51], a[180:183], v[28:31], v[48:51]\n"
    "v_cmp_gt_i32 vcc, s71, v13\n"
    "v_mfma_f32_16x16x32_bf16 v[52:55], a[184:187], v[28:31], v[52:55]\n"
    "s_waitcnt lgkmcnt(2)\n"
    "v_mfma_f32_16x16x32_bf16 v[64:67], a[188:191], v[28:31], v[64:67]\n"
    "ds_read_b128 v[28:31], v0 offset:19456\n"
    "v_mfma_f32_16x16x32_bf16 v[48:51], a[192:195], v[16:19], v[48:51]\n"
    "s_cbranch_vccz .LgDOWN_dma0_49_%=\n"
    "v_mfma_f32_16x16x32_bf16 v[52:55], a[196:199], v[16:19], v[52:55]\n"
    "s_waitcnt lgkmcnt(2)\n"
    "s_or_b64 exec, vcc, s[62:63]\n"
    "buffer_atomic_smax v11, v2, s[36:39], 0 offen sc0 sc1\n"
    "s_mov_b64 exec, -1\n"
    "v_mfma_f32_16x16x32_bf16 v[64:67], a[200:203], v[16:19], v[64:67]\n"
    "ds_read_b128 v[16:19], v0 offset:20480\n"
    "s_waitcnt vmcnt(3)\n"
    "buffer_atomic_smax v12, v10, s[36:39], s70 offen sc1\n"
    "v_mfma_f32_16x16x32_bf16 v[48:51], a[204:207], v[20:23], v[48:51]\n"
    "v_mfma_f32_16x16x32_bf16 v[52:55], a[208:211], v[20:23], v[52:55]\n"
    "s_waitcnt lgkmcnt(2)\n"
    "v_mfma_f32_16x16x32_bf16 v[64:67], a[212:215], v[20:23], v[64:67]\n"
    "ds_read_b128 v[20:23], v0 offset:21504\n"
    "v_mfma_f32_16x16x32_bf16 v[48:51], a[216:219], v[24:27], v[48:51]\n"
    "v_mfma_f32_16x16x32_bf16 v[52:55], a[220:223], v[24:27], v[52:55]\n"
    "s_waitcnt lgkmcnt(2)\n"
    "v_mfma_f32_16x16x32_bf16 v[64:67], a[224:227], v[24:27], v[64:67]\n"
    "ds_read_b128 v[24:27], v0 offset:22528\n"
    "v_mfma_f32_16x16x32_bf16 v[48:51], a[228:231], v[28:31], v[48:51]\n"
    "v_mfma_f32_16x16x32_bf16 v[52:55], a[232:235], v[28:31], v[52:55]\n"
    "s_waitcnt lgkmcnt(2)\n"
    "v_mfma_f32_16x16x32_bf16 v[64:67], a[236:239], v[28:31], v[64:67]\n"
    "ds_read_b128 v[28:31], v0 offset:23552\n"
    "v_mfma_f32_16x16x32_bf16 v[48:51], a[240:243], v[16:19], v[48:51]\n"
    "v_mfma_f32_16x16x32_bf16 v[52:55], a[244:247], v[16:19], v[52:55]\n"
    "s_waitcnt lgkmcnt(2)\n"
    "v_mfma_f32_16x16x32_bf16 v[64:67], a[248:251], v[16:19], v[64:67]\n"
    "ds_read_b128 v[16:19], v0 offset:24576\n"
    "v_mfma_f32_16x16x32_bf16 v[48:51], a[252:255], v[20:23], v[48:51]\n"
    "v_mfma_f32_16x16x32_bf16 v[52:55], v[80:83], v[20:23], v[52:55]\n"
    "s_waitcnt lgkmcnt(2)\n"
    "s_waitcnt vmcnt(3)\n"
    "v_add_u32 v13, v5, v3\n"
    "v_mfma_f32_16x16x32_bf16 v[64:67], v[84:87], v[20:23], v[64:67]\n"
    "ds_read_b128 v[20:23], v0 offset:25600\n"
    "v_cmp_gt_i32 vcc, s71, v13\n"
    "v_mfma_f32_16x16x32_bf16 v[48:51], v[88:91], v[24:27], v[48:51]\n"
    "v_mfma_f32_16x16x32_bf16 v[52:55], v[92:95], v[24:27], v[52:55]\n"
    "s_waitcnt lgkmcnt(2)\n"
    "v_mfma_f32_16x16x32_bf16 v[64:67], v[96:99], v[24:27], v[64:67]\n"
    "ds_read_b128 v[24:27], v0 offset:26624\n"
    "s_cbranch_vccz .LgDOWN_dma0_69_%=\n"
    "v_mfma_f32_16x16x32_bf16 v[48:51], v[100:103], v[28:31], v[48:51]\n"
    "s_or_b64 exec, vcc, s[62:63]\n"
    "buffer_atomic_smax v5, v2, s[36:39], 0 offen sc0 sc1\n"
    "s_mov_b64 exec, -1\n"
    "v_mfma_f32_16x16x32_bf16 v[52:55], v[104:107], v[28:31], v[52:55]\n"
    "s_waitcnt lgkmcnt(2)\n"
    "v_mfma_f32_16x16x32_bf16 v[64:67], v[108:111], v[28:31], v[64:67]\n"
    "ds_read_b128 v[28:31], v0 offset:27648\n"
    "v_mfma_f32_16x16x32_bf16 v[48:51], v[112:115], v[16:19], v[48:51]\n"
    "v_mfma_f32_16x16x32_bf16 v[52:55], v[116:119], v[16:19], v[52:55]\n"
    "s_waitcnt lgkmcnt(2)\n"
    "v_mfma_f32_16x16x32_bf16 v[64:67], v[120:123], v[16:19], v[64:67]\n"
    "ds_read_b128 v[16:19], v0 offset:28672\n"
    "v_mfma_f32_16x16x32_bf16 v[48:51], v[124:127], v[20:23], v[48:51]\n"
    "v_mfma_f32_16x16x32_bf16 v[52:55], v[128:131], v[20:23], v[52:55]\n"
    "s_waitcnt lgkmcnt(2)\n"
    "v_mfma_f32_16x16x32_bf16 v[64:67], v[132:135], v[20:23], v[64:67]\n"
    "ds_read_b128 v[20:23], v0 offset:29696\n"
    "v_mfma_f32_16x16x32_bf16 v[48:51], v[136:139], v[24:27], v[48:51]\n"
    "v_mfma_f32_16x16x32_bf16 v[52:55], v[140:143], v[24:27], v[52:55]\n"
    "s_waitcnt lgkmcnt(2)\n"
    "v_mfma_f32_16x16x32_bf16 v[64:67], v[144:147], v[24:27], v[64:67]\n"
    "ds_read_b128 v[24:27], v0 offset:30720\n"
    "v_mfma_f32_16x16x32_bf16 v[48:51], v[148:151], v[28:31], v[48:51]\n"
    "v_mfma_f32_16x16x32_bf16 v[52:55], v[152:155], v[28:31], v[52:55]\n"
    "s_waitcnt lgkmcnt(2)\n"
    "v_mfma_f32_16x16x32_bf16 v[64:67], v[156:159], v[28:31], v[64:67]\n"
    "ds_read_b128 v[28:31], v0 offset:31744\n"
    "v_mfma_f32_16x16x32_bf16 v[48:51], v[160:163], v[16:19], v[48:51]\n"
    "s_waitcnt vmcnt(2)\n"
    "v_add_u32 v13, v11, v3\n"
    "v_mfma_f32_16x16x32_bf16 v[52:55], v[164:167], v[16:19], v[52:55]\n"
    "s_waitcnt lgkmcnt(2)\n"
    "v_cmp_gt_i32 vcc, s71, v13\n"
    "v_mfma_f32_16x16x32_bf16 v[64:67], v[168:171], v[16:19], v[64:67]\n"
    "ds_read_b128 v[16:19], v0 offset:32768\n"
    "v_mfma_f32_16x16x32_bf16 v[48:51], v[172:175], v[20:23], v[48:51]\n"
    "v_mfma_f32_16x16x32_bf16 v[52:55], v[176:179], v[20:23], v[52:55]\n"
    "s_waitcnt lgkmcnt(2)\n"
    "s_cbranch_vccz .LgDOWN_dma0_89_%=\n"
    "v_mfma_f32_16x16x32_bf16 v[64:67], v[180:183], v[20:23], v[64:67]\n"
    "ds_read_b128 v[20:23], v0 offset:33792\n"
    "s_or_b64 exec, vcc, s[62:63]\n"
    "buffer_atomic_smax v11, v2, s[36:39], 0 offen sc0 sc1\n"
    "s_mov_b64 exec, -1\n"
    "v_mfma_f32_16x16x32_bf16 v[48:51], v[184:187], v[24:27], v[48:51]\n"
    "v_mfma_f32_16x16x32_bf16 v[52:55], v[188:191], v[24:27], v[52:55]\n"
    "s_waitcnt lgkmcnt(2)\n"
    "v_mfma_f32_16x16x32_bf16 v[64:67], v[192:195], v[24:27], v[64:67]\n"
    "ds_read_b128 v[24:27], v0 offset:34816\n"
    "v_mfma_f32_16x16x32_bf16 v[48:51], v[196:199], v[28:31], v[48:51]\n"
    "v_mfma_f32_16x16x32_bf16 v[52:55], v[200:203], v[28:31], v[52:55]\n"
    "s_waitcnt lgkmcnt(2)\n"
    "v_mfma_f32_16x16x32_bf16 v[64:67], v[204:207], v[28:31], v[64:67]\n"
    "ds_read_b128 v[28:31], v0 offset:35840\n"
    "v_mfma_f32_16x16x32_bf16 v[48:51], v[208:211], v[16:19], v[48:51]\n"
    "v_mfma_f32_16x16x32_bf16 v[52:55], v[212:215], v[16:19], v[52:55]\n"
    "s_waitcnt lgkmcnt(2)\n"
    "v_mfma_f32_16x16x32_bf16 v[64:67], v[216:219], v[16:19], v[64:67]\n"
    "v_mfma_f32_16x16x32_bf16 v[48:51], v[220:223], v[20:23], v[48:51]\n"
    "v_mfma_f32_16x16x32_bf16 v[52:55], v[224:227], v[20:23], v[52:55]\n"
    "s_waitcnt lgkmcnt(1)\n"
    "v_mfma_f32_16x16x32_bf16 v[64:67], v[228:231], v[20:23], v[64:67]\n"
    "v_mfma_f32_16x16x32_bf16 v[48:51], v[232:235], v[24:27], v[48:51]\n"
    "v_mfma_f32_16x16x32_bf16 v[52:55], v[236:239], v[24:27], v[52:55]\n"
    "s_waitcnt lgkmcnt(0)\n"
    "v_mfma_f32_16x16x32_bf16 v[64:67], v[240:243], v[24:27], v[64:67]\n"
    "v_mfma_f32_16x16x32_bf16 v[48:51], v[244:247], v[28:31], v[48:51]\n"
    "v_mfma_f32_16x16x32_bf16 v[52:55], v[248:251], v[28:31], v[52:55]\n"
    "v_mfma_f32_16x16x32_bf16 v[64:67], v[252:255], v[28:31], v[64:67]\n"
    "v_cvt_pk_bf16_f32 v48, v48, v49\n"
    "v_cvt_pk_bf16_f32 v49, v50, v51\n"
    "s_nop 1\n"
    "v_cvt_pk_bf16_f32 v50, v52, v53\n"
    "v_cvt_pk_bf16_f32 v51, v54, v55\n"
    "buffer_store_dwordx4 v[48:51], v15, s[36:39], s73 offen sc1\n"
    "s_nop 0\n"
    "v_cvt_pk_bf16_f32 v64, v64, v65\n"
    "v_cvt_pk_bf16_f32 v65, v66, v67\n"
    "s_nop 0\n"
    "v_permlane16_swap_b32 v64, v66\n"
    "v_permlane16_swap_b32 v65, v67\n"
    "buffer_store_dwordx4 v[64:67], v4, s[36:39], s73 offen sc1\n"
    "v_mov_b32 v12, s68\n"
    "s_mov_b32 s73, 0x80000000\n"
    "s_mov_b32 s68, s71\n"
    "s_waitcnt vmcnt(3)\n"
    "v_add_u32 v13, v5, v3\n"
    "v_cmp_gt_i32 vcc, s71, v13\n"
    "s_cbranch_vccnz .LgDOWN_idle0_%=\n"
    "s_add_u32 m0, s59, 36864\n"
    "s_nop 0\n"
    "buffer_load_dwordx4 v1, s[36:39], s76 offen sc1 lds\n"
    "s_add_u32 m0, s59, 40960\n"
    "s_add_u32 s76, s76, 16384\n"
    "buffer_load_dwordx4 v1, s[36:39], s76 offen sc1 lds\n"
    "s_add_u32 m0, s59, 45056\n"
    "s_add_u32 s76, s76, 16384\n"
    "buffer_load_dwordx4 v1, s[36:39], s76 offen sc1 lds\n"
    "s_add_u32 m0, s59, 49152\n"
    "s_add_u32 s76, s76, 16384\n"
    "buffer_load_dwordx4 v1, s[36:39], s76 offen sc1 lds\n"
    "s_add_u32 m0, s59, 53248\n"
    "s_add_u32 s76, s76, 16384\n"
    "buffer_load_dwordx4 v1, s[36:39], s76 offen sc1 lds\n"
    "s_add_u32 m0, s59, 57344\n"
    "s_add_u32 s76, s76, 16384\n"
    "buffer_load_dwordx4 v1, s[36:39], s76 offen sc1 lds\n"
    "s_add_u32 m0, s59, 61440\n"
    "s_add_u32 s76, s76, 16384\n"
    "buffer_load_dwordx4 v1, s[36:39], s76 offen sc1 lds\n"
    "s_add_u32 m0, s59, 65536\n"
    "s_add_u32 s76, s76, 16384\n"
    "buffer_load_dwordx4 v1, s[36:39], s76 offen sc1 lds\n"
    "s_add_u32 m0, s59, 69632\n"
    "s_add_u32 s76, s76, 16384\n"
    "buffer_load_dwordx4 v1, s[36:39], s76 offen sc1 lds\n"
    "s_waitcnt vmcnt(9)\n"
    "buffer_atomic_smax v12, v14, s[36:39], 0 offen sc1\n"
    "s_mov_b32 s69, 0x80000000\n"
    "s_mov_b32 s70, 0x80000000\n"
    "s_waitcnt vmcnt(12)\n"
    "s_add_u32 s71, s68, 1\n"
    "v_add_u32 v13, v11, v3\n"
    "v_cmp_gt_i32 vcc, s71, v13\n"
    "s_nop 0\n"
    "s_or_b64 exec, vcc, s[62:63]\n"
    "buffer_atomic_smax v11, v2, s[36:39], 0 offen sc0 sc1\n"
    "s_mov_b64 exec, -1\n"
    "s_waitcnt vmcnt(2)\n"
    "s_branch .LgDOWN_bar1_%=\n"
    ".LgDOWN_dma0_29_%=:\n"
    "v_mfma_f32_16x16x32_bf16 v[64:67], a[116:119], v[20:23], v[64:67]\n"
    "ds_read_b128 v[20:23], v0 offset:13312\n"
    "v_mfma_f32_16x16x32_bf16 v[48:51], a[120:123], v[24:27], v[48:51]\n"
    "s_add_u32 m0, s59, 36864\n"
    "s_add_u32 s73, s73, s55\n"
    "s_waitcnt vmcnt(5)\n"
    "buffer_atomic_smax v12, v10, s[36:39], s69 offen sc1\n"
    "v_mfma_f32_16x16x32_bf16 v[52:55], a[124:127], v[24:27], v[52:55]\n"
    "s_waitcnt lgkmcnt(2)\n"
    "buffer_load_dwordx4 v1, s[36:39], s76 offen sc1 lds\n"
    "v_mfma_f32_16x16x32_bf16 v[64:67], a[128:131], v[24:27], v[64:67]\n"
    "ds_read_b128 v[24:27], v0 offset:14336\n"
    "v_mfma_f32_16x16x32_bf16 v[48:51], a[132:135], v[28:31], v[48:51]\n"
    "s_add_u32 m0, s59, 40960\n"
    "s_add_u32 s76, s76, 16384\n"
    "v_mfma_f32_16x16x32_bf16 v[52:55], a[136:139], v[28:31], v[52:55]\n"
    "s_waitcnt lgkmcnt(2)\n"
    "buffer_load_dwordx4 v1, s[36:39], s76 offen sc1 lds\n"
    "v_mfma_f32_16x16x32_bf16 v[64:67], a[140:143], v[28:31], v[64:67]\n"
    "ds_read_b128 v[28:31], v0 offset:15360\n"
    "v_mfma_f32_16x16x32_bf16 v[48:51], a[144:147], v[16:19], v[48:51]\n"
    "s_add_u32 m0, s59, 45056\n"
    "s_add_u32 s76, s76, 16384\n"
    "v_mfma_f32_16x16x32_bf16 v[52:55], a[148:151], v[16:19], v[52:55]\n"
    "s_waitcnt lgkmcnt(2)\n"
    "buffer_load_dwordx4 v1, s[36:39], s76 offen sc1 lds\n"
    "v_mfma_f32_16x16x32_bf16 v[64:67], a[152:155], v[16:19], v[64:67]\n"
    "ds_read_b128 v[16:19], v0 offset:16384\n"
    "v_mfma_f32_16x16x32_bf16 v[48:51], a[156:159], v[20:23], v[48:51]\n"
    "v_mfma_f32_16x16x32_bf16 v[52:55], a[160:163], v[20:23], v[52:55]\n"
    "s_waitcnt lgkmcnt(2)\n"
    "v_mfma_f32_16x16x32_bf16 v[64:67], a[164:167], v[20:23], v[64:67]\n"
    "ds_read_b128 v[20:23], v0 offset:17408\n"
    "v_mfma_f32_16x16x32_bf16 v[48:51], a[168:171], v[24:27], v[48:51]\n"
    "s_add_u32 m0, s59, 49152\n"
    "s_add_u32 s76, s76, 16384\n"
    "v_mfma_f32_16x16x32_bf16 v[52:55], a[172:175], v[24:27], v[52:55]\n"
    "s_waitcnt lgkmcnt(2)\n"
    "buffer_load_dwordx4 v1, s[36:39], s76 offen sc1 lds\n"
    "v_mfma_f32_16x16x32_bf16 v[64:67], a[176:179], v[24:27], v[64:67]\n"
    "ds_read_b128 v[24:27], v0 offset:18432\n"
    "v_mfma_f32_16x16x32_bf16 v[48:51], a[180:183], v[28:31], v[48:51]\n"
    "s_add_u32 m0, s59, 53248\n"
    "s_add_u32 s76, s76, 16384\n"
    "v_mfma_f32_16x16x32_bf16 v[52:55], a[184:187], v[28:31], v[52:55]\n"
    "s_waitcnt lgkmcnt(2)\n"
    "buffer_load_dwordx4 v1, s[36:39], s76 offen sc1 lds\n"
    "v_mfma_f32_16x16x32_bf16 v[64:67], a[188:191], v[28:31], v[64:67]\n"
    "ds_read_b128 v[28:31], v0 offset:19456\n"
    "v_mfma_f32_16x16x32_bf16 v[48:51], a[192:195], v[16:19], v[48:51]\n"
    "s_add_u32 m0, s59, 57344\n"
    "s_add_u32 s76, s76, 16384\n"
    "v_mfma_f32_16x16x32_bf16 v[52:55], a[196:199], v[16:19], v[52:55]\n"
    "s_waitcnt lgkmcnt(2)\n"
    "buffer_load_dwordx4 v1, s[36:39], s76 offen sc1 lds\n"
    "v_mfma_f32_16x16x32_bf16 v[64:67], a[200:203], v[16:19], v[64:67]\n"
    "ds_read_b128 v[16:19], v0 offset:20480\n"
    "s_waitcnt vmcnt(7)\n"
    "buffer_atomic_smax v12, v10, s[36:39], s70 offen sc1\n"
    "v_mfma_f32_16x16x32_bf16 v[48:51], a[204:207], v[20:23], v[48:51]\n"
    "v_mfma_f32_16x16x32_bf16 v[52:55], a[208:211], v[20:23], v[52:55]\n"
    "s_waitcnt lgkmcnt(2)\n"
    "v_mfma_f32_16x16x32_bf16 v[64:67], a[212:215], v[20:23], v[64:67]\n"
    "ds_read_b128 v[20:23], v0 offset:21504\n"
    "v_mfma_f32_16x16x32_bf16 v[48:51], a[216:219], v[24:27], v[48:51]\n"
    "s_add_u32 m0, s59, 61440\n"
    "s_add_u32 s76, s76, 16384\n"
    "v_mfma_f32_16x16x32_bf16 v[52:55], a[220:223], v[24:27], v[52:55]\n"
    "s_waitcnt lgkmcnt(2)\n"
    "buffer_load_dwordx4 v1, s[36:39], s76 offen sc1 lds\n"
    "v_mfma_f32_16x16x32_bf16 v[64:67], a[224:227], v[24:27], v[64:67]\n"
    "ds_read_b128 v[24:27], v0 offset:22528\n"
    "v_mfma_f32_16x16x32_bf16 v[48:51], a[228:231], v[28:31], v[48:51]\n"
    "s_add_u32 m0, s59, 65536\n"
    "s_add_u32 s76, s76, 16384\n"
    "v_mfma_f32_16x16x32_bf16 v[52:55], a[232:235], v[28:31], v[52:55]\n"
    "s_waitcnt lgkmcnt(2)\n"
    "buffer_load_dwordx4 v1, s[36:39], s76 offen sc1 lds\n"
    "v_mfma_f32_16x16x32_bf16 v[64:67], a[236:239], v[28:31], v[64:67]\n"
    "ds_read_b128 v[28:31], v0 offset:23552\n"
    "v_mfma_f32_16x16x32_bf16 v[48:51], a[240:243], v[16:19], v[48:51]\n"
    "s_add_u32 m0, s59, 69632\n"
    "s_add_u32 s76, s76, 16384\n"
    "v_mfma_f32_16x16x32_bf16 v[52:55], a[244:247], v[16:19], v[52:55]\n"
    "s_waitcnt lgkmcnt(2)\n"
    "buffer_load_dwordx4 v1, s[36:39], s76 offen sc1 lds\n"
    "v_mfma_f32_16x16x32_bf16 v[64:67], a[248:251], v[16:19], v[64:67]\n"
    "ds_read_b128 v[16:19], v0 offset:24576\n"
    "v_mfma_f32_16x16x32_bf16 v[48:51], a[252:255], v[20:23], v[48:51]\n"
    "v_mfma_f32_16x16x32_bf16 v[52:55], v[80:83], v[20:23], v[52:55]\n"
    "s_waitcnt lgkmcnt(2)\n"
    "v_mfma_f32_16x16x32_bf16 v[64:67], v[84:87], v[20:23], v[64:67]\n"
    "ds_read_b128 v[20:23], v0 offset:25600\n"
    "v_mfma_f32_16x16x32_bf16 v[48:51], v[88:91], v[24:27], v[48:51]\n"
    "v_mfma_f32_16x16x32_bf16 v[52:55], v[92:95], v[24:27], v[52:55]\n"
    "s_waitcnt lgkmcnt(2)\n"
    "v_mfma_f32_16x16x32_bf16 v[64:67], v[96:99], v[24:27], v[64:67]\n"
    "ds_read_b128 v[24:27], v0 offset:26624\n"
    "v_mfma_f32_16x16x32_bf16 v[48:51], v[100:103], v[28:31], v[48:51]\n"
    "v_mfma_f32_16x16x32_bf16 v[52:55], v[104:107], v[28:31], v[52:55]\n"
    "s_waitcnt lgkmcnt(2)\n"
    "v_mfma_f32_16x16x32_bf16 v[64:67], v[108:111], v[28:31], v[64:67]\n"
    "ds_read_b128 v[28:31], v0 offset:27648\n"
    "s_branch .LgDOWN_join0_%=\n"
    ".LgDOWN_dma0_49_%=:\n"
    "v_mfma_f32_16x16x32_bf16 v[52:55], a[196:199], v[16:19], v[52:55]\n"
    "s_waitcnt lgkmcnt(2)\n"
    "v_mfma_f32_16x16x32_bf16 v[64:67], a[200:203], v[16:19], v[64:67]\n"
    "ds_read_b128 v[16:19], v0 offset:20480\n"
    "s_waitcnt vmcnt(2)\n"
    "buffer_atomic_smax v12, v10, s[36:39], s70 offen sc1\n"
    "v_mfma_f32_16x16x32_bf16 v[48:51], a[204:207], v[20:23], v[48:51]\n"
    "s_add_u32 m0, s59, 36864\n"
    "v_mfma_f32_16x16x32_bf16 v[52:55], a[208:211], v[20:23], v[52:55]\n"
    "s_waitcnt lgkmcnt(2)\n"
    "buffer_load_dwordx4 v1, s[36:39], s76 offen sc1 lds\n"
    "v_mfma_f32_16x16x32_bf16 v[64:67], a[212:215], v[20:23], v[64:67]\n"
    "ds_read_b128 v[20:23], v0 offset:21504\n"
    "v_mfma_f32_16x16x32_bf16 v[48:51], a[216:219], v[24:27], v[48:51]\n"
    "s_add_u32 m0, s59, 40960\n"
    "s_add_u32 s76, s76, 16384\n"
    "v_mfma_f32_16x16x32_bf16 v[52:55], a[220:223], v[24:27], v[52:55]\n"
    "s_waitcnt lgkmcnt(2)\n"
    "buffer_load_dwordx4 v1, s[36:39], s76 offen sc1 lds\n"
    "v_mfma_f32_16x16x32_bf16 v[64:67], a[224:227], v[24:27], v[64:67]\n"
    "ds_read_b128 v[24:27], v0 offset:22528\n"
    "v_mfma_f32_16x16x32_bf16 v[48:51], a[228:231], v[28:31], v[48:51]\n"
    "s_add_u32 m0, s59, 45056\n"
    "s_add_u32 s76, s76, 16384\n"
    "v_mfma_f32_16x16x32_bf16 v[52:55], a[232:235], v[28:31], v[52:55]\n"
    "s_waitcnt lgkmcnt(2)\n"
    "buffer_load_dwordx4 v1, s[36:39], s76 offen sc1 lds\n"
    "v_mfma_f32_16x16x32_bf16 v[64:67], a[236:239], v[28:31], v[64:67]\n"
    "ds_read_b128 v[28:31], v0 offset:23552\n"
    "v_mfma_f32_16x16x32_bf16 v[48:51], a[240:243], v[16:19], v[48:51]\n"
    "s_add_u32 m0, s59, 49152\n"
    "s_add_u32 s76, s76, 16384\n"
    "v_mfma_f32_16x16x32_bf16 v[52:55], a[244:247], v[16:19], v[52:55]\n"
    "s_waitcnt lgkmcnt(2)\n"
    "buffer_load_dwordx4 v1, s[36:39], s76 offen sc1 lds\n"
    "v_mfma_f32_16x16x32_bf16 v[64:67], a[248:251], v[16:19], v[64:67]\n"
    "ds_read_b128 v[16:19], v0 offset:24576\n"
    "v_mfma_f32_16x16x32_bf16 v[48:51], a[252:255], v[20:23], v[48:51]\n"
    "s_add_u32 m0, s59, 53248\n"
    "s_add_u32 s76, s76, 16384\n"
    "v_mfma_f32_16x16x32_bf16 v[52:55], v[80:83], v[20:23], v[52:55]\n"
    "s_waitcnt lgkmcnt(2)\n"
    "buffer_load_dwordx4 v1, s[36:39], s76 offen sc1 lds\n"
    "v_mfma_f32_16x16x32_bf16 v[64:67], v[84:87], v[20:23], v[64:67]\n"
    "ds_read_b128 v[20:23], v0 offset:25600\n"
    "v_mfma_f32_16x16x32_bf16 v[48:51], v[88:91], v[24:27], v[48:51]\n"
    "v_mfma_f32_16x16x32_bf16 v[52:55], v[92:95], v[24:27], v[52:55]\n"
    "s_waitcnt lgkmcnt(2)\n"
    "v_mfma_f32_16x16x32_bf16 v[64:67], v[96:99], v[24:27], v[64:67]\n"
    "ds_read_b128 v[24:27], v0 offset:26624\n"
    "v_mfma_f32_16x16x32_bf16 v[48:51], v[100:103], v[28:31], v[48:51]\n"
    "s_add_u32 m0, s59, 57344\n"
    "s_add_u32 s76, s76, 16384\n"
    "v_mfma_f32_16x16x32_bf16 v[52:55], v[104:107], v[28:31], v[52:55]\n"
    "s_waitcnt lgkmcnt(2)\n"
    "buffer_load_dwordx4 v1, s[36:39], s76 offen sc1 lds\n"
    "v_mfma_f32_16x16x32_bf16 v[64:67], v[108:111], v[28:31], v[64:67]\n"
    "ds_read_b128 v[28:31], v0 offset:27648\n"
    "v_mfma_f32_16x16x32_bf16 v[48:51], v[112:115], v[16:19], v[48:51]\n"
    "s_add_u32 m0, s59, 61440\n"
    "s_add_u32 s76, s76, 16384\n"
    "v_mfma_f32_16x16x32_bf16 v[52:55], v[116:119], v[16:19], v[52:55]\n"
    "s_waitcnt lgkmcnt(2)\n"
    "buffer_load_dwordx4 v1, s[36:39], s76 offen sc1 lds\n"
    "v_mfma_f32_16x16x32_bf16 v[64:67], v[120:123], v[16:19], v[64:67]\n"
    "ds_read_b128 v[16:19], v0 offset:28672\n"
    "v_mfma_f32_16x16x32_bf16 v[48:51], v[124:127], v[20:23], v[48:51]\n"
    "s_add_u32 m0, s59, 65536\n"
    "s_add_u32 s76, s76, 16384\n"
    "v_mfma_f32_16x16x32_bf16 v[52:55], v[128:131], v[20:23], v[52:55]\n"
    "s_waitcnt lgkmcnt(2)\n"
    "buffer_load_dwordx4 v1, s[36:39], s76 offen sc1 lds\n"
    "v_mfma_f32_16x16x32_bf16 v[64:67], v[132:135], v[20:23], v[64:67]\n"
    "ds_read_b128 v[20:23], v0 offset:29696\n"
    "v_mfma_f32_16x16x32_bf16 v[48:51], v[136:139], v[24:27], v[48:51]\n"
    "s_add_u32 m0, s59, 69632\n"
    "s_add_u32 s76, s76, 16384\n"
    "v_mfma_f32_16x16x32_bf16 v[52:55], v[140:143], v[24:27], v[52:55]\n"
    "s_waitcnt lgkmcnt(2)\n"
    "buffer_load_dwordx4 v1, s[36:39], s76 offen sc1 lds\n"
    "v_mfma_f32_16x16x32_bf16 v[64:67], v[144:147], v[24:27], v[64:67]\n"
    "ds_read_b128 v[24:27], v0 offset:30720\n"
    "v_mfma_f32_16x16x32_bf16 v[48:51], v[148:151], v[28:31], v[48:51]\n"
    "v_mfma_f32_16x16x32_bf16 v[52:55], v[152:155], v[28:31], v[52:55]\n"
    "s_waitcnt lgkmcnt(2)\n"
    "v_mfma_f32_16x16x32_bf16 v[64:67], v[156:159], v[28:31], v[64:67]\n"
    "ds_read_b128 v[28:31], v0 offset:31744\n"
    "v_mfma_f32_16x16x32_bf16 v[48:51], v[160:163], v[16:19], v[48:51]\n"
    "v_mfma_f32_16x16x32_bf16 v[52:55], v[164:167], v[16:19], v[52:55]\n"
    "s_waitcnt lgkmcnt(2)\n"
    "v_mfma_f32_16x16x32_bf16 v[64:67], v[168:171], v[16:19], v[64:67]\n"
    "ds_read_b128 v[16:19], v0 offset:32768\n"
    "v_mfma_f32_16x16x32_bf16 v[48:51], v[172:175], v[20:23], v[48:51]\n"
    "v_mfma_f32_16x16x32_bf16 v[52:55], v[176:179], v[20:23], v[52:55]\n"
    "s_waitcnt lgkmcnt(2)\n"
    "v_mfma_f32_16x16x32_bf16 v[64:67], v[180:183], v[20:23], v[64:67]\n"
    "ds_read_b128 v[20:23], v0 offset:33792\n"
    "v_mfma_f32_16x16x32_bf16 v[48:51], v[184:187], v[24:27], v[48:51]\n"
    "v_mfma_f32_16x16x32_bf16 v[52:55], v[188:191], v[24:27], v[52:55]\n"
    "s_waitcnt lgkmcnt(2)\n"
    "v_mfma_f32_16x16x32_bf16 v[64:67], v[192:195], v[24:27], v[64:67]\n"
    "ds_read_b128 v[24:27], v0 offset:34816\n"
    "v_mfma_f32_16x16x32_bf16 v[48:51], v[196:199], v[28:31], v[48:51]\n"
    "v_mfma_f32_16x16x32_bf16 v[52:55], v[200:203], v[28:31], v[52:55]\n"
    "s_waitcnt lgkmcnt(2)\n"
    "v_mfma_f32_16x16x32_bf16 v[64:67], v[204:207], v[28:31], v[64:67]\n"
    "ds_read_b128 v[28:31], v0 offset:35840\n"
    "v_mfma_f32_16x16x32_bf16 v[48:51], v[208:211], v[16:19], v[48:51]\n"
    "v_mfma_f32_16x16x32_bf16 v[52:55], v[212:215], v[16:19], v[52:55]\n"
    "s_waitcnt lgkmcnt(2)\n"
    "v_mfma_f32_16x16x32_bf16 v[64:67], v[216:219], v[16:19], v[64:67]\n"
    "v_mfma_f32_16x16x32_bf16 v[48:51], v[220:223], v[20:23], v[48:51]\n"
    "v_mfma_f32_16x16x32_bf16 v[52:55], v[224:227], v[20:23], v[52:55]\n"
    "s_waitcnt lgkmcnt(1)\n"
    "v_mfma_f32_16x16x32_bf16 v[64:67], v[228:231], v[20:23], v[64:67]\n"
    "v_mfma_f32_16x16x32_bf16 v[48:51], v[232:235], v[24:27], v[48:51]\n"
    "v_mfma_f32_16x16x32_bf16 v[52:55], v[236:239], v[24:27], v[52:55]\n"
    "s_waitcnt lgkmcnt(0)\n"
    "v_mfma_f32_16x16x32_bf16 v[64:67], v[240:243], v[24:27], v[64:67]\n"
    "v_mfma_f32_16x16x32_bf16 v[48:51], v[244:247], v[28:31], v[48:51]\n"
    "v_mfma_f32_16x16x32_bf16 v[52:55], v[248:251], v[28:31], v[52:55]\n"
    "v_mfma_f32_16x16x32_bf16 v[64:67], v[252:255], v[28:31], v[64:67]\n"
    "v_cvt_pk_bf16_f32 v48, v48, v49\n"
    "v_cvt_pk_bf16_f32 v49, v50, v51\n"
    "s_nop 1\n"
    "v_cvt_pk_bf16_f32 v50, v52, v53\n"
    "v_cvt_pk_bf16_f32 v51, v54, v55\n"
    "buffer_store_dwordx4 v[48:51], v15, s[36:39], s73 offen sc1\n"
    "s_nop 0\n"
    "v_cvt_pk_bf16_f32 v64, v64, v65\n"
    "v_cvt_pk_bf16_f32 v65, v66, v67\n"
    "s_nop 0\n"
    "v_permlane16_swap_b32 v64, v66\n"
    "v_permlane16_swap_b32 v65, v67\n"
    "buffer_store_dwordx4 v[64:67], v4, s[36:39], s73 offen sc1\n"
    "v_mov_b32 v12, s68\n"
    "s_mov_b32 s73, 0x80000000\n"
    "s_mov_b32 s68, s71\n"
    "s_mov_b32 s69, 0\n"
    "s_mov_b32 s70, 0x80000000\n"
    "s_waitcnt vmcnt(13)\n"
    "s_add_u32 s71, s68, 1\n"
    "v_add_u32 v13, v11, v3\n"
    "v_cmp_gt_i32 vcc, s71, v13\n"
    "s_nop 0\n"
    "s_or_b64 exec, vcc, s[62:63]\n"
    "buffer_atomic_smax v11, v2, s[36:39], 0 offen sc0 sc1\n"
    "s_mov_b64 exec, -1\n"
    "s_waitcnt vmcnt(3)\n"
    "s_branch .LgDOWN_bar1_%=\n"
    ".LgDOWN_dma0_69_%=:\n"
    "v_mfma_f32_16x16x32_bf16 v[48:51], v[100:103], v[28:31], v[48:51]\n"
    "s_add_u32 m0, s59, 36864\n"
    "v_mfma_f32_16x16x32_bf16 v[52:55], v[104:107], v[28:31], v[52:55]\n"
    "s_waitcnt lgkmcnt(2)\n"
    "buffer_load_dwordx4 v1, s[36:39], s76 offen sc1 lds\n"
    "v_mfma_f32_16x16x32_bf16 v[64:67], v[108:111], v[28:31], v[64:67]\n"
    "ds_read_b128 v[28:31], v0 offset:27648\n"
    "v_mfma_f32_16x16x32_bf16 v[48:51], v[112:115], v[16:19], v[48:51]\n"
    "s_add_u32 m0, s59, 40960\n"
    "s_add_u32 s76, s76, 16384\n"
    "v_mfma_f32_16x16x32_bf16 v[52:55], v[116:119], v[16:19], v[52:55]\n"
    "s_waitcnt lgkmcnt(2)\n"
    "buffer_load_dwordx4 v1, s[36:39], s76 offen sc1 lds\n"
    "v_mfma_f32_16x16x32_bf16 v[64:67], v[120:123], v[16:19], v[64:67]\n"
    "ds_read_b128 v[16:19], v0 offset:28672\n"
    "v_mfma_f32_16x16x32_bf16 v[48:51], v[124:127], v[20:23], v[48:51]\n"
    "s_add_u32 m0, s59, 45056\n"
    "s_add_u32 s76, s76, 16384\n"
    "v_mfma_f32_16x16x32_bf16 v[52:55], v[128:131], v[20:23], v[52:55]\n"
    "s_waitcnt lgkmcnt(2)\n"
    "buffer_load_dwordx4 v1, s[36:39], s76 offen sc1 lds\n"
    "v_mfma_f32_16x16x32_bf16 v[64:67], v[132:135], v[20:23], v[64:67]\n"
    "ds_read_b128 v[20:23], v0 offset:29696\n"
    "v_mfma_f32_16x16x32_bf16 v[48:51], v[136:139], v[24:27], v[48:51]\n"
    "v_mfma_f32_16x16x32_bf16 v[52:55], v[140:143], v[24:27], v[52:55]\n"
    "s_waitcnt lgkmcnt(2)\n"
    "v_mfma_f32_16x16x32_bf16 v[64:67], v[144:147], v[24:27], v[64:67]\n"
    "ds_read_b128 v[24:27], v0 offset:30720\n"
    "v_mfma_f32_16x16x32_bf16 v[48:51], v[148:151], v[28:31], v[48:51]\n"
    "s_add_u32 m0, s59, 49152\n"
    "s_add_u32 s76, s76, 16384\n"
    "v_mfma_f32_16x16x32_bf16 v[52:55], v[152:155], v[28:31], v[52:55]\n"
    "s_waitcnt lgkmcnt(2)\n"
    "buffer_load_dwordx4 v1, s[36:39], s76 offen sc1 lds\n"
    "v_mfma_f32_16x16x32_bf16 v[64:67], v[156:159], v[28:31], v[64:67]\n"
    "ds_read_b128 v[28:31], v0 offset:31744\n"
    "v_mfma_f32_16x16x32_bf16 v[48:51], v[160:163], v[16:19], v[48:51]\n"
    "s_add_u32 m0, s59, 53248\n"
    "s_add_u32 s76, s76, 16384\n"
    "v_mfma_f32_16x16x32_bf16 v[52:55], v[164:167], v[16:19], v[52:55]\n"
    "s_waitcnt lgkmcnt(2)\n"
    "buffer_load_dwordx4 v1, s[36:39], s76 offen sc1 lds\n"
    "v_mfma_f32_16x16x32_bf16 v[64:67], v[168:171], v[16:19], v[64:67]\n"
    "ds_read_b128 v[16:19], v0 offset:32768\n"
    "v_mfma_f32_16x16x32_bf16 v[48:51], v[172:175], v[20:23], v[48:51]\n"
    "s_add_u32 m0, s59, 57344\n"
    "s_add_u32 s76, s76, 16384\n"
    "v_mfma_f32_16x16x32_bf16 v[52:55], v[176:179], v[20:23], v[52:55]\n"
    "s_waitcnt lgkmcnt(2)\n"
    "buffer_load_dwordx4 v1, s[36:39], s76 offen sc1 lds\n"
    "v_mfma_f32_16x16x32_bf16 v[64:67], v[180:183], v[20:23], v[64:67]\n"
    "ds_read_b128 v[20:23], v0 offset:33792\n"
    "v_mfma_f32_16x16x32_bf16 v[48:51], v[184:187], v[24:27], v[48:51]\n"
    "v_mfma_f32_16x16x32_bf16 v[52:55], v[188:191], v[24:27], v[52:55]\n"
    "s_waitcnt lgkmcnt(2)\n"
    "v_mfma_f32_16x16x32_bf16 v[64:67], v[192:195], v[24:27], v[64:67]\n"
    "ds_read_b128 v[24:27], v0 offset:34816\n"
    "v_mfma_f32_16x16x32_bf16 v[48:51], v[196:199], v[28:31], v[48:51]\n"
    "s_add_u32 m0, s59, 61440\n"
    "s_add_u32 s76, s76, 16384\n"
    "v_mfma_f32_16x16x32_bf16 v[52:55], v[200:203], v[28:31], v[52:55]\n"
    "s_waitcnt lgkmcnt(2)\n"
    "buffer_load_dwordx4 v1, s[36:39], s76 offen sc1 lds\n"
    "v_mfma_f32_16x16x32_bf16 v[64:67], v[204:207], v[28:31], v[64:67]\n"
    "ds_read_b128 v[28:31], v0 offset:35840\n"
    "v_mfma_f32_16x16x32_bf16 v[48:51], v[208:211], v[16:19], v[48:51]\n"
    "s_add_u32 m0, s59, 65536\n"
    "s_add_u32 s76, s76, 16384\n"
    "v_mfma_f32_16x16x32_bf16 v[52:55], v[212:215], v[16:19], v[52:55]\n"
    "s_waitcnt lgkmcnt(2)\n"
    "buffer_load_dwordx4 v1, s[36:39], s76 offen sc1 lds\n"
    "s_add_u32 m0, s59, 69632\n"
    "s_add_u32 s76, s76, 16384\n"
    "v_mfma_f32_16x16x32_bf16 v[64:67], v[216:219], v[16:19], v[64:67]\n"
    "buffer_load_dwordx4 v1, s[36:39], s76 offen sc1 lds\n"
    "v_mfma_f32_16x16x32_bf16 v[48:51], v[220:223], v[20:23], v[48:51]\n"
    "v_mfma_f32_16x16x32_bf16 v[52:55], v[224:227], v[20:23], v[52:55]\n"
    "s_waitcnt lgkmcnt(1)\n"
    "v_mfma_f32_16x16x32_bf16 v[64:67], v[228:231], v[20:23], v[64:67]\n"
    "v_mfma_f32_16x16x32_bf16 v[48:51], v[232:235], v[24:27], v[48:51]\n"
    "v_mfma_f32_16x16x32_bf16 v[52:55], v[236:239], v[24:27], v[52:55]\n"
    "s_waitcnt lgkmcnt(0)\n"
    "v_mfma_f32_16x16x32_bf16 v[64:67], v[240:243], v[24:27], v[64:67]\n"
    "v_mfma_f32_16x16x32_bf16 v[48:51], v[244:247], v[28:31], v[48:51]\n"
    "v_mfma_f32_16x16x32_bf16 v[52:55], v[248:251], v[28:31], v[52:55]\n"
    "v_mfma_f32_16x16x32_bf16 v[64:67], v[252:255], v[28:31], v[64:67]\n"
    "v_cvt_pk_bf16_f32 v48, v48, v49\n"
    "v_cvt_pk_bf16_f32 v49, v50, v51\n"
    "s_nop 1\n"
    "v_cvt_pk_bf16_f32 v50, v52, v53\n"
    "v_cvt_pk_bf16_f32 v51, v54, v55\n"
    "buffer_store_dwordx4 v[48:51], v15, s[36:39], s73 offen sc1\n"
    "s_nop 0\n"
    "v_cvt_pk_bf16_f32 v64, v64, v65\n"
    "v_cvt_pk_bf16_f32 v65, v66, v67\n"
    "s_nop 0\n"
    "v_permlane16_swap_b32 v64, v66\n"
    "v_permlane16_swap_b32 v65, v67\n"
    "buffer_store_dwordx4 v[64:67], v4, s[36:39], s73 offen sc1\n"
    "v_mov_b32 v12, s68\n"
    "s_mov_b32 s73, 0x80000000\n"
    "s_mov_b32 s68, s71\n"
    "s_mov_b32 s69, 0\n"
    "s_mov_b32 s70, 0x80000000\n"
    "s_waitcnt vmcnt(12)\n"
    "s_add_u32 s71, s68, 1\n"
    "v_add_u32 v13, v11, v3\n"
    "v_cmp_gt_i32 vcc, s71, v13\n"
    "s_nop 0\n"
    "s_or_b64 exec, vcc, s[62:63]\n"
    "buffer_atomic_smax v11, v2, s[36:39], 0 offen sc0 sc1\n"
    "s_mov_b64 exec, -1\n"
    "s_waitcnt vmcnt(3)\n"
    "s_branch .LgDOWN_bar1_%=\n"
    ".LgDOWN_dma0_89_%=:\n"
    "v_mfma_f32_16x16x32_bf16 v[64:67], v[180:183], v[20:23], v[64:67]\n"
    "ds_read_b128 v[20:23], v0 offset:33792\n"
    "v_mfma_f32_16x16x32_bf16 v[48:51], v[184:187], v[24:27], v[48:51]\n"
    "s_add_u32 m0, s59, 36864\n"
    "v_mfma_f32_16x16x32_bf16 v[52:55], v[188:191], v[24:27], v[52:55]\n"
    "s_waitcnt lgkmcnt(2)\n"
    "buffer_load_dwordx4 v1, s[36:39], s76 offen sc1 lds\n"
    "v_mfma_f32_16x16x32_bf16 v[64:67], v[192:195], v[24:27], v[64:67]\n"
    "ds_read_b128 v[24:27], v0 offset:34816\n"
    "v_mfma_f32_16x16x32_bf16 v[48:51], v[196:199], v[28:31], v[48:51]\n"
    "v_mfma_f32_16x16x32_bf16 v[52:55], v[200:203], v[28:31], v[52:55]\n"
    "s_waitcnt lgkmcnt(2)\n"
    "v_mfma_f32_16x16x32_bf16 v[64:67], v[204:207], v[28:31], v[64:67]\n"
    "ds_read_b128 v[28:31], v0 offset:35840\n"
    "v_mfma_f32_16x16x32_bf16 v[48:51], v[208:211], v[16:19], v[48:51]\n"
    "s_add_u32 m0, s59, 40960\n"
    "s_add_u32 s76, s76, 16384\n"
    "v_mfma_f32_16x16x32_bf16 v[52:55], v[212:215], v[16:19], v[52:55]\n"
    "s_waitcnt lgkmcnt(2)\n"
    "buffer_load_dwordx4 v1, s[36:39], s76 offen sc1 lds\n"
    "v_mfma_f32_16x16x32_bf16 v[64:67], v[216:219], v[16:19], v[64:67]\n"
    "v_mfma_f32_16x16x32_bf16 v[48:51], v[220:223], v[20:23], v[48:51]\n"
    "v_mfma_f32_16x16x32_bf16 v[52:55], v[224:227], v[20:23], v[52:55]\n"
    "s_waitcnt lgkmcnt(1)\n"
    "v_mfma_f32_16x16x32_bf16 v[64:67], v[228:231], v[20:23], v[64:67]\n"
    "v_mfma_f32_16x16x32_bf16 v[48:51], v[232:235], v[24:27], v[48:51]\n"
    "s_add_u32 m0, s59, 45056\n"
    "s_add_u32 s76, s76, 16384\n"
    "v_mfma_f32_16x16x32_bf16 v[52:55], v[236:239], v[24:27], v[52:55]\n"
    "s_waitcnt lgkmcnt(0)\n"
    "buffer_load_dwordx4 v1, s[36:39], s76 offen sc1 lds\n"
    "v_mfma_f32_16x16x32_bf16 v[64:67], v[240:243], v[24:27], v[64:67]\n"
    "v_mfma_f32_16x16x32_bf16 v[48:51], v[244:247], v[28:31], v[48:51]\n"
    "v_mfma_f32_16x16x32_bf16 v[52:55], v[248:251], v[28:31], v[52:55]\n"
    "v_mfma_f32_16x16x32_bf16 v[64:67], v[252:255], v[28:31], v[64:67]\n"
    "v_cvt_pk_bf16_f32 v48, v48, v49\n"
    "v_cvt_pk_bf16_f32 v49, v50, v51\n"
    "s_nop 1\n"
    "v_cvt_pk_bf16_f32 v50, v52, v53\n"
    "v_cvt_pk_bf16_f32 v51, v54, v55\n"
    "buffer_store_dwordx4 v[48:51], v15, s[36:39], s73 offen sc1\n"
    "s_nop 0\n"
    "v_cvt_pk_bf16_f32 v64, v64, v65\n"
    "v_cvt_pk_bf16_f32 v65, v66, v67\n"
    "s_nop 0\n"
    "v_permlane16_swap_b32 v64, v66\n"
    "v_permlane16_swap_b32 v65, v67\n"
    "buffer_store_dwordx4 v[64:67], v4, s[36:39], s73 offen sc1\n"
    "v_mov_b32 v12, s68\n"
    "s_mov_b32 s73, 0x80000000\n"
    "s_mov_b32 s68, s71\n"
    "s_add_u32 m0, s59, 49152\n"
    "s_add_u32 s76, s76, 16384\n"
    "buffer_load_dwordx4 v1, s[36:39], s76 offen sc1 lds\n"
    "s_add_u32 m0, s59, 53248\n"
    "s_add_u32 s76, s76, 16384\n"
    "buffer_load_dwordx4 v1, s[36:39], s76 offen sc1 lds\n"
    "s_add_u32 m0, s59, 57344\n"
    "s_add_u32 s76, s76, 16384\n"
    "buffer_load_dwordx4 v1, s[36:39], s76 offen sc1 lds\n"
    "s_add_u32 m0, s59, 61440\n"
    "s_add_u32 s76, s76, 16384\n"
    "buffer_load_dwordx4 v1, s[36:39], s76 offen sc1 lds\n"
    "s_add_u32 m0, s59, 65536\n"
    "s_add_u32 s76, s76, 16384\n"
    "buffer_load_dwordx4 v1, s[36:39], s76 offen sc1 lds\n"
    "s_add_u32 m0, s59, 69632\n"
    "s_add_u32 s76, s76, 16384\n"
    "buffer_load_dwordx4 v1, s[36:39], s76 offen sc1 lds\n"
    "s_waitcnt vmcnt(6)\n"
    "buffer_atomic_smax v12, v14, s[36:39], 0 offen sc1\n"
    "s_mov_b32 s69, 0x80000000\n"
    "s_mov_b32 s70, 0x80000000\n"
    "s_waitcnt vmcnt(12)\n"
    "s_add_u32 s71, s68, 1\n"
    "v_add_u32 v13, v11, v3\n"
    "v_cmp_gt_i32 vcc, s71, v13\n"
    "s_nop 0\n"
    "s_or_b64 exec, vcc, s[62:63]\n"
    "buffer_atomic_smax v11, v2, s[36:39], 0 offen sc0 sc1\n"
    "s_mov_b64 exec, -1\n"
    "s_waitcnt vmcnt(2)\n"
    "s_branch .LgDOWN_bar1_%=\n"
    ".LgDOWN_idle0_%=:\n"
    "s_or_b64 exec, vcc, s[62:63]\n"
    "buffer_atomic_smax v5, v2, s[36:39], 0 offen sc0 sc1\n"
    "s_mov_b64 exec, -1\n"
    "s_waitcnt vmcnt(1)\n"
    "buffer_atomic_smax v12, v14, s[36:39], 0 offen sc1\n"
    "s_branch .LgDOWN_block0_1_%=\n"
    ".LgDOWN_block0_%=:\n"
    "v_add_u32 v13, v11, v3\n"
    "v_cmp_gt_i32 vcc, s71, v13\n"
    "s_nop 0\n"
    "s_or_b64 exec, vcc, s[62:63]\n"
    "buffer_atomic_smax v11, v2, s[36:39], 0 offen sc0 sc1\n"
    "s_mov_b64 exec, -1\n"
    "s_sleep 3\n"
    "s_waitcnt vmcnt(1)\n"
    ".LgDOWN_block0_0_%=:\n"
    "v_add_u32 v13, v5, v3\n"
    "v_cmp_gt_i32 vcc, s71, v13\n"
    "s_cbranch_vccz .LgDOWN_go0_%=\n"
    "v_add_u32 v13, v5, v3\n"
    "v_cmp_gt_i32 vcc, s71, v13\n"
    "s_nop 0\n"
    "s_or_b64 exec, vcc, s[62:63]\n"
    "buffer_atomic_smax v5, v2, s[36:39], 0 offen sc0 sc1\n"
    "s_mov_b64 exec, -1\n"
    "s_sleep 3\n"
    "s_waitcnt vmcnt(1)\n"
    ".LgDOWN_block0_1_%=:\n"
    "v_add_u32 v13, v11, v3\n"
    "v_cmp_gt_i32 vcc, s71, v13\n"
    "s_cbranch_vccz .LgDOWN_go0_%=\n"
    "s_sub_u32 s72, s72, 1\n"
    "s_cbranch_scc0 .LgDOWN_block0_%=\n"
    "s_mov_b32 s72, 0\n"
    ".LgDOWN_go0_%=:\n"
    "s_lshr_b32 s76, s71, 1\n"
    "s_and_b32 s76, s76, s54\n"
    "s_mul_i32 s76, s76, s53\n"
    "s_add_u32 s76, s76, s52\n"
    "s_add_u32 s76, s76, 1024\n"
    "s_add_u32 m0, s59, 36864\n"
    "s_nop 0\n"
    "buffer_load_dwordx4 v1, s[36:39], s76 offen sc1 lds\n"
    "s_add_u32 m0, s59, 40960\n"
    "s_add_u32 s76, s76, 16384\n"
    "buffer_load_dwordx4 v1, s[36:39], s76 offen sc1 lds\n"
    "s_add_u32 m0, s59, 45056\n"
    "s_add_u32 s76, s76, 16384\n"
    "buffer_load_dwordx4 v1, s[36:39], s76 offen sc1 lds\n"
    "s_add_u32 m0, s59, 49152\n"
    "s_add_u32 s76, s76, 16384\n"
    "buffer_load_dwordx4 v1, s[36:39], s76 offen sc1 lds\n"
    "s_add_u32 m0, s59, 53248\n"
    "s_add_u32 s76, s76, 16384\n"
    "buffer_load_dwordx4 v1, s[36:39], s76 offen sc1 lds\n"
    "s_add_u32 m0, s59, 57344\n"
    "s_add_u32 s76, s76, 16384\n"
    "buffer_load_dwordx4 v1, s[36:39], s76 offen sc1 lds\n"
    "s_add_u32 m0, s59, 61440\n"
    "s_add_u32 s76, s76, 16384\n"
    "buffer_load_dwordx4 v1, s[36:39], s76 offen sc1 lds\n"
    "s_add_u32 m0, s59, 65536\n"
    "s_add_u32 s76, s76, 16384\n"
    "buffer_load_dwordx4 v1, s[36:39], s76 offen sc1 lds\n"
    "s_add_u32 m0, s59, 69632\n"
    "s_add_u32 s76, s76, 16384\n"
    "buffer_load_dwordx4 v1, s[36:39], s76 offen sc1 lds\n"
    "s_mov_b32 s69, 0x80000000\n"
    "s_mov_b32 s70, 0x80000000\n"
    "s_waitcnt vmcnt(9)\n"
    "s_add_u32 s71, s68, 1\n"
    "v_add_u32 v13, v11, v3\n"
    "v_cmp_gt_i32 vcc, s71, v13\n"
    "s_nop 0\n"
    "s_or_b64 exec, vcc, s[62:63]\n"
    "buffer_atomic_smax v11, v2, s[36:39], 0 offen sc0 sc1\n"
    "s_mov_b64 exec, -1\n"
    "s_waitcnt vmcnt(1)\n"
    "s_branch .LgDOWN_bar1_%=\n"
    ".LgDOWN_bar0_%=:\n"
    "s_barrier\n"
    "ds_read_b128 v[16:19], v0 offset:0\n"
    "ds_read_b128 v[20:23], v0 offset:1024\n"
    "ds_read_b128 v[24:27], v0 offset:2048\n"
    "ds_read_b128 v[28:31], v0 offset:3072\n"
    "v_add_u32 v13, v5, v3\n"
    "v_cmp_gt_i32 vcc, s71, v13\n"
    "s_nop 0\n"
    "s_or_b64 exec, vcc, s[62:63]\n"
    "buffer_atomic_smax v5, v2, s[36:39], 0 offen sc0 sc1\n"
    "s_mov_b64 exec, -1\n"
    "s_lshr_b32 s76, s71, 1\n"
    "s_and_b32 s76, s76, s54\n"
    "s_mul_i32 s76, s76, s53\n"
    "s_add_u32 s76, s76, s52\n"
    "s_add_u32 s76, s76, 1024\n"
    "s_waitcnt lgkmcnt(3)\n"
    "s_branch .LgDOWN_head0_%=\n"
    ".LgDOWN_poll1_%=:\n"
    "v_mfma_f32_16x16x32_bf16 v[56:59], a[36:39], v[28:31], v[56:59]\n"
    "s_or_b64 exec, vcc, s[62:63]\n"
    "buffer_atomic_smax v11, v2, s[36:39], 0 offen sc0 sc1\n"
    "s_mov_b64 exec, -1\n"
    "s_nop 0\n"
    "v_permlane16_swap_b32 v65, v67\n"
    "v_mfma_f32_16x16x32_bf16 v[60:63], a[40:43], v[28:31], v[60:63]\n"
    "s_waitcnt lgkmcnt(2)\n"
    "buffer_store_dwordx4 v[64:67], v4, s[36:39], s73 offen sc1\n"
    "v_mfma_f32_16x16x32_bf16 v[68:71], a[44:47], v[28:31], v[68:71]\n"
    "ds_read_b128 v[28:31], v9 offset:7168\n"
    "v_mfma_f32_16x16x32_bf16 v[56:59], a[48:51], v[16:19], v[56:59]\n"
    "s_lshr_b32 s73, s68, 1\n"
    "v_mfma_f32_16x16x32_bf16 v[60:63], a[52:55], v[16:19], v[60:63]\n"
    "s_waitcnt lgkmcnt(2)\n"
    "v_mfma_f32_16x16x32_bf16 v[68:71], a[56:59], v[16:19], v[68:71]\n"
    "ds_read_b128 v[16:19], v9 offset:8192\n"
    "v_mfma_f32_16x16x32_bf16 v[56:59], a[60:63], v[20:23], v[56:59]\n"
    "v_mfma_f32_16x16x32_bf16 v[60:63], a[64:67], v[20:23], v[60:63]\n"
    "s_waitcnt lgkmcnt(2)\n"
    "v_mfma_f32_16x16x32_bf16 v[68:71], a[68:71], v[20:23], v[68:71]\n"
    "ds_read_b128 v[20:23], v9 offset:9216\n"
    "v_mfma_f32_16x16x32_bf16 v[56:59], a[72:75], v[24:27], v[56:59]\n"
    "s_and_b32 s73, s73, s57\n"
    "v_mfma_f32_16x16x32_bf16 v[60:63], a[76:79], v[24:27], v[60:63]\n"
    "s_waitcnt lgkmcnt(2)\n"
    "v_mfma_f32_16x16x32_bf16 v[68:71], a[80:83], v[24:27], v[68:71]\n"
    "ds_read_b128 v[24:27], v9 offset:10240\n"
    "v_mfma_f32_16x16x32_bf16 v[56:59], a[84:87], v[28:31], v[56:59]\n"
    "v_mfma_f32_16x16x32_bf16 v[60:63], a[88:91], v[28:31], v[60:63]\n"
    "s_waitcnt lgkmcnt(2)\n"
    "v_mfma_f32_16x16x32_bf16 v[68:71], a[92:95], v[28:31], v[68:71]\n"
    "ds_read_b128 v[28:31], v9 offset:11264\n"
    "v_mfma_f32_16x16x32_bf16 v[56:59], a[96:99], v[16:19], v[56:59]\n"
    "s_mul_i32 s73, s73, s56\n"
    "s_waitcnt vmcnt(3)\n"
    "v_add_u32 v13, v5, v3\n"
    "v_mfma_f32_16x16x32_bf16 v[60:63], a[100:103], v[16:19], v[60:63]\n"
    "s_waitcnt lgkmcnt(2)\n"
    "v_cmp_gt_i32 vcc, s71, v13\n"
    "v_mfma_f32_16x16x32_bf16 v[68:71], a[104:107], v[16:19], v[68:71]\n"
    "ds_read_b128 v[16:19], v9 offset:12288\n"
    "v_mfma_f32_16x16x32_bf16 v[56:59], a[108:111], v[20:23], v[56:59]\n"
    "v_mfma_f32_16x16x32_bf16 v[60:63], a[112:115], v[20:23], v[60:63]\n"
    "s_waitcnt lgkmcnt(2)\n"
    "s_cbranch_vccz .LgDOWN_dma1_29_%=\n"
    "v_mfma_f32_16x16x32_bf16 v[68:71], a[116:119], v[20:23], v[68:71]\n"
    "ds_read_b128 v[20:23], v9 offset:13312\n"
    "s_or_b64 exec, vcc, s[62:63]\n"
    "buffer_atomic_smax v5, v2, s[36:39], 0 offen sc0 sc1\n"
    "s_mov_b64 exec, -1\n"
    "v_mfma_f32_16x16x32_bf16 v[56:59], a[120:123], v[24:27], v[56:59]\n"
    "s_add_u32 s73, s73, s55\n"
    "s_waitcnt vmcnt(6)\n"
    "buffer_atomic_smax v12, v14, s[36:39], s69 offen sc1\n"
    "v_mfma_f32_16x16x32_bf16 v[60:63], a[124:127], v[24:27], v[60:63]\n"
    "s_waitcnt lgkmcnt(2)\n"
    "v_mfma_f32_16x16x32_bf16 v[68:71], a[128:131], v[24:27], v[68:71]\n"
    "ds_read_b128 v[24:27], v9 offset:14336\n"
    "v_mfma_f32_16x16x32_bf16 v[56:59], a[132:135], v[28:31], v[56:59]\n"
    "v_mfma_f32_16x16x32_bf16 v[60:63], a[136:139], v[28:31], v[60:63]\n"
    "s_waitcnt lgkmcnt(2)\n"
    "v_mfma_f32_16x16x32_bf16 v[68:71], a[140:143], v[28:31], v[68:71]\n"
    "ds_read_b128 v[28:31], v9 offset:15360\n"
    "v_mfma_f32_16x16x32_bf16 v[56:59], a[144:147], v[16:19], v[56:59]\n"
    "v_mfma_f32_16x16x32_bf16 v[60:63], a[148:151], v[16:19], v[60:63]\n"
    "s_waitcnt lgkmcnt(2)\n"
    "v_mfma_f32_16x16x32_bf16 v[68:71], a[152:155], v[16:19], v[68:71]\n"
    "ds_read_b128 v[16:19], v9 offset:16384\n"
    "v_mfma_f32_16x16x32_bf16 v[56:59], a[156:159], v[20:23], v[56:59]\n"
    "v_mfma_f32_16x16x32_bf16 v[60:63], a[160:163], v[20:23], v[60:63]\n"
    "s_waitcnt lgkmcnt(2)\n"
    "v_mfma_f32_16x16x32_bf16 v[68:71], a[164:167], v[20:23], v[68:71]\n"
    "ds_read_b128 v[20:23], v9 offset:17408\n"
    "v_mfma_f32_16x16x32_bf16 v[56:59], a[168:171], v[24:27], v[56:59]\n"
    "v_mfma_f32_16x16x32_bf16 v[60:63], a[172:175], v[24:27], v[60:63]\n"
    "s_waitcnt lgkmcnt(2)\n"
    "v_mfma_f32_16x16x32_bf16 v[68:71], a[176:179], v[24:27], v[68:71]\n"
    "ds_read_b128 v[24:27], v9 offset:18432\n"
    "s_waitcnt vmcnt(3)\n"
    "v_add_u32 v13, v11, v3\n"
    "v_mfma_f32_16x16x32_bf16 v[56:59], a[180:183], v[28:31], v[56:59]\n"
    "v_cmp_gt_i32 vcc, s71, v13\n"
    "v_mfma_f32_16x16x32_bf16 v[60:63], a[184:187], v[28:31], v[60:63]\n"
    "s_waitcnt lgkmcnt(2)\n"
    "v_mfma_f32_16x16x32_bf16 v[68:71], a[188:191], v[28:31], v[68:71]\n"
    "ds_read_b128 v[28:31], v9 offset:19456\n"
    "v_mfma_f32_16x16x32_bf16 v[56:59], a[192:195], v[16:19], v[56:59]\n"
    "s_cbranch_vccz .LgDOWN_dma1_49_%=\n"
    "v_mfma_f32_16x16x32_bf16 v[60:63], a[196:199], v[16:19], v[60:63]\n"
    "s_waitcnt lgkmcnt(2)\n"
    "s_or_b64 exec, vcc, s[62:63]\n"
    "buffer_atomic_smax v11, v2, s[36:39], 0 offen sc0 sc1\n"
    "s_mov_b64 exec, -1\n"
    "v_mfma_f32_16x16x32_bf16 v[68:71], a[200:203], v[16:19], v[68:71]\n"
    "ds_read_b128 v[16:19], v9 offset:20480\n"
    "s_waitcnt vmcnt(3)\n"
    "buffer_atomic_smax v12, v14, s[36:39], s70 offen sc1\n"
    "v_mfma_f32_16x16x32_bf16 v[56:59], a[204:207], v[20:23], v[56:59]\n"
    "v_mfma_f32_16x16x32_bf16 v[60:63], a[208:211], v[20:23], v[60:63]\n"
    "s_waitcnt lgkmcnt(2)\n"
    "v_mfma_f32_16x16x32_bf16 v[68:71], a[212:215], v[20:23], v[68:71]\n"
    "ds_read_b128 v[20:23], v9 offset:21504\n"
    "v_mfma_f32_16x16x32_bf16 v[56:59], a[216:219], v[24:27], v[56:59]\n"
    "v_mfma_f32_16x16x32_bf16 v[60:63], a[220:223], v[24:27], v[60:63]\n"
    "s_waitcnt lgkmcnt(2)\n"
    "v_mfma_f32_16x16x32_bf16 v[68:71], a[224:227], v[24:27], v[68:71]\n"
    "ds_read_b128 v[24:27], v9 offset:22528\n"
    "v_mfma_f32_16x16x32_bf16 v[56:59], a[228:231], v[28:31], v[56:59]\n"
    "v_mfma_f32_16x16x32_bf16 v[60:63], a[232:235], v[28:31], v[60:63]\n"
    "s_waitcnt lgkmcnt(2)\n"
    "v_mfma_f32_16x16x32_bf16 v[68:71], a[236:239], v[28:31], v[68:71]\n"
    "ds_read_b128 v[28:31], v9 offset:23552\n"
    "v_mfma_f32_16x16x32_bf16 v[56:59], a[240:243], v[16:19], v[56:59]\n"
    "v_mfma_f32_16x16x32_bf16 v[60:63], a[244:247], v[16:19], v[60:63]\n"
    "s_waitcnt lgkmcnt(2)\n"
    "v_mfma_f32_16x16x32_bf16 v[68:71], a[248:251], v[16:19], v[68:71]\n"
    "ds_read_b128 v[16:19], v9 offset:24576\n"
    "v_mfma_f32_16x16x32_bf16 v[56:59], a[252:255], v[20:23], v[56:59]\n"
    "v_mfma_f32_16x16x32_bf16 v[60:63], v[80:83], v[20:23], v[60:63]\n"
    "s_waitcnt lgkmcnt(2)\n"
    "s_waitcnt vmcnt(3)\n"
    "v_add_u32 v13, v5, v3\n"
    "v_mfma_f32_16x16x32_bf16 v[68:71], v[84:87], v[20:23], v[68:71]\n"
    "ds_read_b128 v[20:23], v9 offset:25600\n"
    "v_cmp_gt_i32 vcc, s71, v13\n"
    "v_mfma_f32_16x16x32_bf16 v[56:59], v[88:91], v[24:27], v[56:59]\n"
    "v_mfma_f32_16x16x32_bf16 v[60:63], v[92:95], v[24:27], v[60:63]\n"
    "s_waitcnt lgkmcnt(2)\n"
    "v_mfma_f32_16x16x32_bf16 v[68:71], v[96:99], v[24:27], v[68:71]\n"
    "ds_read_b128 v[24:27], v9 offset:26624\n"
    "s_cbranch_vccz .LgDOWN_dma1_69_%=\n"
    "v_mfma_f32_16x16x32_bf16 v[56:59], v[100:103], v[28:31], v[56:59]\n"
    "s_or_b64 exec, vcc, s[62:63]\n"
    "buffer_atomic_smax v5, v2, s[36:39], 0 offen sc0 sc1\n"
    "s_mov_b64 exec, -1\n"
    "v_mfma_f32_16x16x32_bf16 v[60:63], v[104:107], v[28:31], v[60:63]\n"
    "s_waitcnt lgkmcnt(2)\n"
    "v_mfma_f32_16x16x32_bf16 v[68:71], v[108:111], v[28:31], v[68:71]\n"
    "ds_read_b128 v[28:31], v9 offset:27648\n"
    "v_mfma_f32_16x16x32_bf16 v[56:59], v[112:115], v[16:19], v[56:59]\n"
    "v_mfma_f32_16x16x32_bf16 v[60:63], v[116:119], v[16:19], v[60:63]\n"
    "s_waitcnt lgkmcnt(2)\n"
    "v_mfma_f32_16x16x32_bf16 v[68:71], v[120:123], v[16:19], v[68:71]\n"
    "ds_read_b128 v[16:19], v9 offset:28672\n"
    "v_mfma_f32_16x16x32_bf16 v[56:59], v[124:127], v[20:23], v[56:59]\n"
    "v_mfma_f32_16x16x32_bf16 v[60:63], v[128:131], v[20:23], v[60:63]\n"
    "s_waitcnt lgkmcnt(2)\n"
    "v_mfma_f32_16x16x32_bf16 v[68:71], v[132:135], v[20:23], v[68:71]\n"
    "ds_read_b128 v[20:23], v9 offset:29696\n"
    "v_mfma_f32_16x16x32_bf16 v[56:59], v[136:139], v[24:27], v[56:59]\n"
    "v_mfma_f32_16x16x32_bf16 v[60:63], v[140:143], v[24:27], v[60:63]\n"
    "s_waitcnt lgkmcnt(2)\n"
    "v_mfma_f32_16x16x32_bf16 v[68:71], v[144:147], v[24:27], v[68:71]\n"
    "ds_read_b128 v[24:27], v9 offset:30720\n"
    "v_mfma_f32_16x16x32_bf16 v[56:59], v[148:151], v[28:31], v[56:59]\n"
    "v_mfma_f32_16x16x32_bf16 v[60:63], v[152:155], v[28:31], v[60:63]\n"
    "s_waitcnt lgkmcnt(2)\n"
    "v_mfma_f32_16x16x32_bf16 v[68:71], v[156:159], v[28:31], v[68:71]\n"
    "ds_read_b128 v[28:31], v9 offset:31744\n"
    "v_mfma_f32_16x16x32_bf16 v[56:59], v[160:163], v[16:19], v[56:59]\n"
    "s_waitcnt vmcnt(2)\n"
    "v_add_u32 v13, v11, v3\n"
    "v_mfma_f32_16x16x32_bf16 v[60:63], v[164:167], v[16:19], v[60:63]\n"
    "s_waitcnt lgkmcnt(2)\n"
    "v_cmp_gt_i32 vcc, s71, v13\n"
    "v_mfma_f32_16x16x32_bf16 v[68:71], v[168:171], v[16:19], v[68:71]\n"
    "ds_read_b128 v[16:19], v9 offset:32768\n"
    "v_mfma_f32_16x16x32_bf16 v[56:59], v[172:175], v[20:23], v[56:59]\n"
    "v_mfma_f32_16x16x32_bf16 v[60:63], v[176:179], v[20:23], v[60:63]\n"
    "s_waitcnt lgkmcnt(2)\n"
    "s_cbranch_vccz .LgDOWN_dma1_89_%=\n"
    "v_mfma_f32_16x16x32_bf16 v[68:71], v[180:183], v[20:23], v[68:71]\n"
    "ds_read_b128 v[20:23], v9 offset:33792\n"
    "s_or_b64 exec, vcc, s[62:63]\n"
    "buffer_atomic_smax v11, v2, s[36:39], 0 offen sc0 sc1\n"
    "s_mov_b64 exec, -1\n"
    "v_mfma_f32_16x16x32_bf16 v[56:59], v[184:187], v[24:27], v[56:59]\n"
    "v_mfma_f32_16x16x32_bf16 v[60:63], v[188:191], v[24:27], v[60:63]\n"
    "s_waitcnt lgkmcnt(2)\n"
    "v_mfma_f32_16x16x32_bf16 v[68:71], v[192:195], v[24:27], v[68:71]\n"
    "ds_read_b128 v[24:27], v9 offset:34816\n"
    "v_mfma_f32_16x16x32_bf16 v[56:59], v[196:199], v[28:31], v[56:59]\n"
    "v_mfma_f32_16x16x32_bf16 v[60:63], v[200:203], v[28:31], v[60:63]\n"
    "s_waitcnt lgkmcnt(2)\n"
    "v_mfma_f32_16x16x32_bf16 v[68:71], v[204:207], v[28:31], v[68:71]\n"
    "ds_read_b128 v[28:31], v9 offset:35840\n"
    "v_mfma_f32_16x16x32_bf16 v[56:59], v[208:211], v[16:19], v[56:59]\n"
    "v_mfma_f32_16x16x32_bf16 v[60:63], v[212:215], v[16:19], v[60:63]\n"
    "s_waitcnt lgkmcnt(2)\n"
    "v_mfma_f32_16x16x32_bf16 v[68:71], v[216:219], v[16:19], v[68:71]\n"
    "v_mfma_f32_16x16x32_bf16 v[56:59], v[220:223], v[20:23], v[56:59]\n"
    "v_mfma_f32_16x16x32_bf16 v[60:63], v[224:227], v[20:23], v[60:63]\n"
    "s_waitcnt lgkmcnt(1)\n"
    "v_mfma_f32_16x16x32_bf16 v[68:71], v[228:231], v[20:23], v[68:71]\n"
    "v_mfma_f32_16x16x32_bf16 v[56:59], v[232:235], v[24:27], v[56:59]\n"
    "v_mfma_f32_16x16x32_bf16 v[60:63], v[236:239], v[24:27], v[60:63]\n"
    "s_waitcnt lgkmcnt(0)\n"
    "v_mfma_f32_16x16x32_bf16 v[68:71], v[240:243], v[24:27], v[68:71]\n"
    "v_mfma_f32_16x16x32_bf16 v[56:59], v[244:247], v[28:31], v[56:59]\n"
    "v_mfma_f32_16x16x32_bf16 v[60:63], v[248:251], v[28:31], v[60:63]\n"
    "v_mfma_f32_16x16x32_bf16 v[68:71], v[252:255], v[28:31], v[68:71]\n"
    "v_cvt_pk_bf16_f32 v56, v56, v57\n"
    "v_cvt_pk_bf16_f32 v57, v58, v59\n"
    "s_nop 1\n"
    "v_cvt_pk_bf16_f32 v58, v60, v61\n"
    "v_cvt_pk_bf16_f32 v59, v62, v63\n"
    "buffer_store_dwordx4 v[56:59], v15, s[36:39], s73 offen offset:1024 sc1\n"
    "s_nop 0\n"
    "v_cvt_pk_bf16_f32 v68, v68, v69\n"
    "v_cvt_pk_bf16_f32 v69, v70, v71\n"
    "s_nop 0\n"
    "v_permlane16_swap_b32 v68, v70\n"
    "v_permlane16_swap_b32 v69, v71\n"
    "buffer_store_dwordx4 v[68:71], v4, s[36:39], s73 offen offset:1024 sc1\n"
    "v_mov_b32 v12, s68\n"
    "s_mov_b32 s73, 0x80000000\n"
    "s_mov_b32 s68, s71\n"
    "s_cmp_eq_u32 s68, 128\n"
    "s_cbranch_scc1 .LgDOWN_done_%=\n"
    "s_waitcnt vmcnt(3)\n"
    "v_add_u32 v13, v5, v3\n"
    "v_cmp_gt_i32 vcc, s71, v13\n"
    "s_cbranch_vccnz .LgDOWN_idle1_%=\n"
    "s_add_u32 m0, s59, 0\n"
    "s_nop 0\n"
    "buffer_load_dwordx4 v1, s[36:39], s76 offen sc1 lds\n"
    "s_add_u32 m0, s59, 4096\n"
    "s_add_u32 s76, s76, 16384\n"
    "buffer_load_dwordx4 v1, s[36:39], s76 offen sc1 lds\n"
    "s_add_u32 m0, s59, 8192\n"
    "s_add_u32 s76, s76, 16384\n"
    "buffer_load_dwordx4 v1, s[36:39], s76 offen sc1 lds\n"
    "s_add_u32 m0, s59, 12288\n"
    "s_add_u32 s76, s76, 16384\n"
    "buffer_load_dwordx4 v1, s[36:39], s76 offen sc1 lds\n"
    "s_add_u32 m0, s59, 16384\n"
    "s_add_u32 s76, s76, 16384\n"
    "buffer_load_dwordx4 v1, s[36:39], s76 offen sc1 lds\n"
    "s_add_u32 m0, s59, 20480\n"
    "s_add_u32 s76, s76, 16384\n"
    "buffer_load_dwordx4 v1, s[36:39], s76 offen sc1 lds\n"
    "s_add_u32 m0, s59, 24576\n"
    "s_add_u32 s76, s76, 16384\n"
    "buffer_load_dwordx4 v1, s[36:39], s76 offen sc1 lds\n"
    "s_add_u32 m0, s59, 28672\n"
    "s_add_u32 s76, s76, 16384\n"
    "buffer_load_dwordx4 v1, s[36:39], s76 offen sc1 lds\n"
    "s_add_u32 m0, s59, 32768\n"
    "s_add_u32 s76, s76, 16384\n"
    "buffer_load_dwordx4 v1, s[36:39], s76 offen sc1 lds\n"
    "s_waitcnt vmcnt(9)\n"
    "buffer_atomic_smax v12, v10, s[36:39], 0 offen sc1\n"
    "s_mov_b32 s69, 0x80000000\n"
    "s_mov_b32 s70, 0x80000000\n"
    "s_waitcnt vmcnt(12)\n"
    "s_add_u32 s71, s68, 1\n"
    "v_add_u32 v13, v11, v3\n"
    "v_cmp_gt_i32 vcc, s71, v13\n"
    "s_nop 0\n"
    "s_or_b64 exec, vcc, s[62:63]\n"
    "buffer_atomic_smax v11, v2, s[36:39], 0 offen sc0 sc1\n"
    "s_mov_b64 exec, -1\n"
    "s_waitcnt vmcnt(2)\n"
    "s_branch .LgDOWN_bar0_%=\n"
    ".LgDOWN_dma1_29_%=:\n"
    "v_mfma_f32_16x16x32_bf16 v[68:71], a[116:119], v[20:23], v[68:71]\n"
    "ds_read_b128 v[20:23], v9 offset:13312\n"
    "v_mfma_f32_16x16x32_bf16 v[56:59], a[120:123], v[24:27], v[56:59]\n"
    "s_add_u32 m0, s59, 0\n"
    "s_add_u32 s73, s73, s55\n"
    "s_waitcnt vmcnt(5)\n"
    "buffer_atomic_smax v12, v14, s[36:39], s69 offen sc1\n"
    "v_mfma_f32_16x16x32_bf16 v[60:63], a[124:127], v[24:27], v[60:63]\n"
    "s_waitcnt lgkmcnt(2)\n"
    "buffer_load_dwordx4 v1, s[36:39], s76 offen sc1 lds\n"
    "v_mfma_f32_16x16x32_bf16 v[68:71], a[128:131], v[24:27], v[68:71]\n"
    "ds_read_b128 v[24:27], v9 offset:14336\n"
    "v_mfma_f32_16x16x32_bf16 v[56:59], a[132:135], v[28:31], v[56:59]\n"
    "s_add_u32 m0, s59, 4096\n"
    "s_add_u32 s76, s76, 16384\n"
    "v_mfma_f32_16x16x32_bf16 v[60:63], a[136:139], v[28:31], v[60:63]\n"
    "s_waitcnt lgkmcnt(2)\n"
    "buffer_load_dwordx4 v1, s[36:39], s76 offen sc1 lds\n"
    "v_mfma_f32_16x16x32_bf16 v[68:71], a[140:143], v[28:31], v[68:71]\n"
    "ds_read_b128 v[28:31], v9 offset:15360\n"
    "v_mfma_f32_16x16x32_bf16 v[56:59], a[144:147], v[16:19], v[56:59]\n"
    "s_add_u32 m0, s59, 8192\n"
    "s_add_u32 s76, s76, 16384\n"
    "v_mfma_f32_16x16x32_bf16 v[60:63], a[148:151], v[16:19], v[60:63]\n"
    "s_waitcnt lgkmcnt(2)\n"
    "buffer_load_dwordx4 v1, s[36:39], s76 offen sc1 lds\n"
    "v_mfma_f32_16x16x32_bf16 v[68:71], a[152:155], v[16:19], v[68:71]\n"
    "ds_read_b128 v[16:19], v9 offset:16384\n"
    "v_mfma_f32_16x16x32_bf16 v[56:59], a[156:159], v[20:23], v[56:59]\n"
    "v_mfma_f32_16x16x32_bf16 v[60:63], a[160:163], v[20:23], v[60:63]\n"
    "s_waitcnt lgkmcnt(2)\n"
    "v_mfma_f32_16x16x32_bf16 v[68:71], a[164:167], v[20:23], v[68:71]\n"
    "ds_read_b128 v[20:23], v9 offset:17408\n"
    "v_mfma_f32_16x16x32_bf16 v[56:59], a[168:171], v[24:27], v[56:59]\n"
    "s_add_u32 m0, s59, 12288\n"
    "s_add_u32 s76, s76, 16384\n"
    "v_mfma_f32_16x16x32_bf16 v[60:63], a[172:175], v[24:27], v[60:63]\n"
    "s_waitcnt lgkmcnt(2)\n"
    "buffer_load_dwordx4 v1, s[36:39], s76 offen sc1 lds\n"
    "v_mfma_f32_16x16x32_bf16 v[68:71], a[176:179], v[24:27], v[68:71]\n"
    "ds_read_b128 v[24:27], v9 offset:18432\n"
    "v_mfma_f32_16x16x32_bf16 v[56:59], a[180:183], v[28:31], v[56:59]\n"
    "s_add_u32 m0, s59, 16384\n"
    "s_add_u32 s76, s76, 16384\n"
    "v_mfma_f32_16x16x32_bf16 v[60:63], a[184:187], v[28:31], v[60:63]\n"
    "s_waitcnt lgkmcnt(2)\n"
    "buffer_load_dwordx4 v1, s[36:39], s76 offen sc1 lds\n"
    "v_mfma_f32_16x16x32_bf16 v[68:71], a[188:191], v[28:31], v[68:71]\n"
    "ds_read_b128 v[28:31], v9 offset:19456\n"
    "v_mfma_f32_16x16x32_bf16 v[56:59], a[192:195], v[16:19], v[56:59]\n"
    "s_add_u32 m0, s59, 20480\n"
    "s_add_u32 s76, s76, 16384\n"
    "v_mfma_f32_16x16x32_bf16 v[60:63], a[196:199], v[16:19], v[60:63]\n"
    "s_waitcnt lgkmcnt(2)\n"
    "buffer_load_dwordx4 v1, s[36:39], s76 offen sc1 lds\n"
    "v_mfma_f32_16x16x32_bf16 v[68:71], a[200:203], v[16:19], v[68:71]\n"
    "ds_read_b128 v[16:19], v9 offset:20480\n"
    "s_waitcnt vmcnt(7)\n"
    "buffer_atomic_smax v12, v14, s[36:39], s70 offen sc1\n"
    "v_mfma_f32_16x16x32_bf16 v[56:59], a[204:207], v[20:23], v[56:59]\n"
    "v_mfma_f32_16x16x32_bf16 v[60:63], a[208:211], v[20:23], v[60:63]\n"
    "s_waitcnt lgkmcnt(2)\n"
    "v_mfma_f32_16x16x32_bf16 v[68:71], a[212:215], v[20:23], v[68:71]\n"
    "ds_read_b128 v[20:23], v9 offset:21504\n"
    "v_mfma_f32_16x16x32_bf16 v[56:59], a[216:219], v[24:27], v[56:59]\n"
    "s_add_u32 m0, s59, 24576\n"
    "s_add_u32 s76, s76, 16384\n"
    "v_mfma_f32_16x16x32_bf16 v[60:63], a[220:223], v[24:27], v[60:63]\n"
    "s_waitcnt lgkmcnt(2)\n"
    "buffer_load_dwordx4 v1, s[36:39], s76 offen sc1 lds\n"
    "v_mfma_f32_16x16x32_bf16 v[68:71], a[224:227], v[24:27], v[68:71]\n"
    "ds_read_b128 v[24:27], v9 offset:22528\n"
    "v_mfma_f32_16x16x32_bf16 v[56:59], a[228:231], v[28:31], v[56:59]\n"
    "s_add_u32 m0, s59, 28672\n"
    "s_add_u32 s76, s76, 16384\n"
    "v_mfma_f32_16x16x32_bf16 v[60:63], a[232:235], v[28:31], v[60:63]\n"
    "s_waitcnt lgkmcnt(2)\n"
    "buffer_load_dwordx4 v1, s[36:39], s76 offen sc1 lds\n"
    "v_mfma_f32_16x16x32_bf16 v[68:71], a[236:239], v[28:31], v[68:71]\n"
    "ds_read_b128 v[28:31], v9 offset:23552\n"
    "v_mfma_f32_16x16x32_bf16 v[56:59], a[240:243], v[16:19], v[56:59]\n"
    "s_add_u32 m0, s59, 32768\n"
    "s_add_u32 s76, s76, 16384\n"
    "v_mfma_f32_16x16x32_bf16 v[60:63], a[244:247], v[16:19], v[60:63]\n"
    "s_waitcnt lgkmcnt(2)\n"
    "buffer_load_dwordx4 v1, s[36:39], s76 offen sc1 lds\n"
    "v_mfma_f32_16x16x32_bf16 v[68:71], a[248:251], v[16:19], v[68:71]\n"
    "ds_read_b128 v[16:19], v9 offset:24576\n"
    "v_mfma_f32_16x16x32_bf16 v[56:59], a[252:255], v[20:23], v[56:59]\n"
    "v_mfma_f32_16x16x32_bf16 v[60:63], v[80:83], v[20:23], v[60:63]\n"
    "s_waitcnt lgkmcnt(2)\n"
    "v_mfma_f32_16x16x32_bf16 v[68:71], v[84:87], v[20:23], v[68:71]\n"
    "ds_read_b128 v[20:23], v9 offset:25600\n"
    "v_mfma_f32_16x16x32_bf16 v[56:59], v[88:91], v[24:27], v[56:59]\n"
    "v_mfma_f32_16x16x32_bf16 v[60:63], v[92:95], v[24:27], v[60:63]\n"
    "s_waitcnt lgkmcnt(2)\n"
    "v_mfma_f32_16x16x32_bf16 v[68:71], v[96:99], v[24:27], v[68:71]\n"
    "ds_read_b128 v[24:27], v9 offset:26624\n"
    "v_mfma_f32_16x16x32_bf16 v[56:59], v[100:103], v[28:31], v[56:59]\n"
    "v_mfma_f32_16x16x32_bf16 v[60:63], v[104:107], v[28:31], v[60:63]\n"
    "s_waitcnt lgkmcnt(2)\n"
    "v_mfma_f32_16x16x32_bf16 v[68:71], v[108:111], v[28:31], v[68:71]\n"
    "ds_read_b128 v[28:31], v9 offset:27648\n"
    "s_branch .LgDOWN_join1_%=\n"
    ".LgDOWN_dma1_49_%=:\n"
    "v_mfma_f32_16x16x32_bf16 v[60:63], a[196:199], v[16:19], v[60:63]\n"
    "s_waitcnt lgkmcnt(2)\n"
    "v_mfma_f32_16x16x32_bf16 v[68:71], a[200:203], v[16:19], v[68:71]\n"
    "ds_read_b128 v[16:19], v9 offset:20480\n"
    "s_waitcnt vmcnt(2)\n"
    "buffer_atomic_smax v12, v14, s[36:39], s70 offen sc1\n"
    "v_mfma_f32_16x16x32_bf16 v[56:59], a[204:207], v[20:23], v[56:59]\n"
    "s_add_u32 m0, s59, 0\n"
    "v_mfma_f32_16x16x32_bf16 v[60:63], a[208:211], v[20:23], v[60:63]\n"
    "s_waitcnt lgkmcnt(2)\n"
    "buffer_load_dwordx4 v1, s[36:39], s76 offen sc1 lds\n"
    "v_mfma_f32_16x16x32_bf16 v[68:71], a[212:215], v[20:23], v[68:71]\n"
    "ds_read_b128 v[20:23], v9 offset:21504\n"
    "v_mfma_f32_16x16x32_bf16 v[56:59], a[216:219], v[24:27], v[56:59]\n"
    "s_add_u32 m0, s59, 4096\n"
    "s_add_u32 s76, s76, 16384\n"
    "v_mfma_f32_16x16x32_bf16 v[60:63], a[220:223], v[24:27], v[60:63]\n"
    "s_waitcnt lgkmcnt(2)\n"
    "buffer_load_dwordx4 v1, s[36:39], s76 offen sc1 lds\n"
    "v_mfma_f32_16x16x32_bf16 v[68:71], a[224:227], v[24:27], v[68:71]\n"
    "ds_read_b128 v[24:27], v9 offset:22528\n"
    "v_mfma_f32_16x16x32_bf16 v[56:59], a[228:231], v[28:31], v[56:59]\n"
    "s_add_u32 m0, s59, 8192\n"
    "s_add_u32 s76, s76, 16384\n"
    "v_mfma_f32_16x16x32_bf16 v[60:63], a[232:235], v[28:31], v[60:63]\n"
    "s_waitcnt lgkmcnt(2)\n"
    "buffer_load_dwordx4 v1, s[36:39], s76 offen sc1 lds\n"
    "v_mfma_f32_16x16x32_bf16 v[68:71], a[236:239], v[28:31], v[68:71]\n"
    "ds_read_b128 v[28:31], v9 offset:23552\n"
    "v_mfma_f32_16x16x32_bf16 v[56:59], a[240:243], v[16:19], v[56:59]\n"
    "s_add_u32 m0, s59, 12288\n"
    "s_add_u32 s76, s76, 16384\n"
    "v_mfma_f32_16x16x32_bf16 v[60:63], a[244:247], v[16:19], v[60:63]\n"
    "s_waitcnt lgkmcnt(2)\n"
    "buffer_load_dwordx4 v1, s[36:39], s76 offen sc1 lds\n"
    "v_mfma_f32_16x16x32_bf16 v[68:71], a[248:251], v[16:19], v[68:71]\n"
    "ds_read_b128 v[16:19], v9 offset:24576\n"
    "v_mfma_f32_16x16x32_bf16 v[56:59], a[252:255], v[20:23], v[56:59]\n"
    "s_add_u32 m0, s59, 16384\n"
    "s_add_u32 s76, s76, 16384\n"
    "v_mfma_f32_16x16x32_bf16 v[60:63], v[80:83], v[20:23], v[60:63]\n"
    "s_waitcnt lgkmcnt(2)\n"
    "buffer_load_dwordx4 v1, s[36:39], s76 offen sc1 lds\n"
    "v_mfma_f32_16x16x32_bf16 v[68:71], v[84:87], v[20:23], v[68:71]\n"
    "ds_read_b128 v[20:23], v9 offset:25600\n"
    "v_mfma_f32_16x16x32_bf16 v[56:59], v[88:91], v[24:27], v[56:59]\n"
    "v_mfma_f32_16x16x32_bf16 v[60:63], v[92:95], v[24:27], v[60:63]\n"
    "s_waitcnt lgkmcnt(2)\n"
    "v_mfma_f32_16x16x32_bf16 v[68:71], v[96:99], v[24:27], v[68:71]\n"
    "ds_read_b128 v[24:27], v9 offset:26624\n"
    "v_mfma_f32_16x16x32_bf16 v[56:59], v[100:103], v[28:31], v[56:59]\n"
    "s_add_u32 m0, s59, 20480\n"
    "s_add_u32 s76, s76, 16384\n"
    "v_mfma_f32_16x16x32_bf16 v[60:63], v[104:107], v[28:31], v[60:63]\n"
    "s_waitcnt lgkmcnt(2)\n"
    "buffer_load_dwordx4 v1, s[36:39], s76 offen sc1 lds\n"
    "v_mfma_f32_16x16x32_bf16 v[68:71], v[108:111], v[28:31], v[68:71]\n"
    "ds_read_b128 v[28:31], v9 offset:27648\n"
    "v_mfma_f32_16x16x32_bf16 v[56:59], v[112:115], v[16:19], v[56:59]\n"
    "s_add_u32 m0, s59, 24576\n"
    "s_add_u32 s76, s76, 16384\n"
    "v_mfma_f32_16x16x32_bf16 v[60:63], v[116:119], v[16:19], v[60:63]\n"
    "s_waitcnt lgkmcnt(2)\n"
    "buffer_load_dwordx4 v1, s[36:39], s76 offen sc1 lds\n"
    "v_mfma_f32_16x16x32_bf16 v[68:71], v[120:123], v[16:19], v[68:71]\n"
    "ds_read_b128 v[16:19], v9 offset:28672\n"
    "v_mfma_f32_16x16x32_bf16 v[56:59], v[124:127], v[20:23], v[56:59]\n"
    "s_add_u32 m0, s59, 28672\n"
    "s_add_u32 s76, s76, 16384\n"
    "v_mfma_f32_16x16x32_bf16 v[60:63], v[128:131], v[20:23], v[60:63]\n"
    "s_waitcnt lgkmcnt(2)\n"
    "buffer_load_dwordx4 v1, s[36:39], s76 offen sc1 lds\n"
    "v_mfma_f32_16x16x32_bf16 v[68:71], v[132:135], v[20:23], v[68:71]\n"
    "ds_read_b128 v[20:23], v9 offset:29696\n"
    "v_mfma_f32_16x16x32_bf16 v[56:59], v[136:139], v[24:27], v[56:59]\n"
    "s_add_u32 m0, s59, 32768\n"
    "s_add_u32 s76, s76, 16384\n"
    "v_mfma_f32_16x16x32_bf16 v[60:63], v[140:143], v[24:27], v[60:63]\n"
    "s_waitcnt lgkmcnt(2)\n"
    "buffer_load_dwordx4 v1, s[36:39], s76 offen sc1 lds\n"
    "v_mfma_f32_16x16x32_bf16 v[68:71], v[144:147], v[24:27], v[68:71]\n"
    "ds_read_b128 v[24:27], v9 offset:30720\n"
    "v_mfma_f32_16x16x32_bf16 v[56:59], v[148:151], v[28:31], v[56:59]\n"
    "v_mfma_f32_16x16x32_bf16 v[60:63], v[152:155], v[28:31], v[60:63]\n"
    "s_waitcnt lgkmcnt(2)\n"
    "v_mfma_f32_16x16x32_bf16 v[68:71], v[156:159], v[28:31], v[68:71]\n"
    "ds_read_b128 v[28:31], v9 offset:31744\n"
    "v_mfma_f32_16x16x32_bf16 v[56:59], v[160:163], v[16:19], v[56:59]\n"
    "v_mfma_f32_16x16x32_bf16 v[60:63], v[164:167], v[16:19], v[60:63]\n"
    "s_waitcnt lgkmcnt(2)\n"
    "v_mfma_f32_16x16x32_bf16 v[68:71], v[168:171], v[16:19], v[68:71]\n"
    "ds_read_b128 v[16:19], v9 offset:32768\n"
    "v_mfma_f32_16x16x32_bf16 v[56:59], v[172:175], v[20:23], v[56:59]\n"
    "v_mfma_f32_16x16x32_bf16 v[60:63], v[176:179], v[20:23], v[60:63]\n"
    "s_waitcnt lgkmcnt(2)\n"
    "v_mfma_f32_16x16x32_bf16 v[68:71], v[180:183], v[20:23], v[68:71]\n"
    "ds_read_b128 v[20:23], v9 offset:33792\n"
    "v_mfma_f32_16x16x32_bf16 v[56:59], v[184:187], v[24:27], v[56:59]\n"
    "v_mfma_f32_16x16x32_bf16 v[60:63], v[188:191], v[24:27], v[60:63]\n"
    "s_waitcnt lgkmcnt(2)\n"
    "v_mfma_f32_16x16x32_bf16 v[68:71], v[192:195], v[24:27], v[68:71]\n"
    "ds_read_b128 v[24:27], v9 offset:34816\n"
    "v_mfma_f32_16x16x32_bf16 v[56:59], v[196:199], v[28:31], v[56:59]\n"
    "v_mfma_f32_16x16x32_bf16 v[60:63], v[200:203], v[28:31], v[60:63]\n"
    "s_waitcnt lgkmcnt(2)\n"
    "v_mfma_f32_16x16x32_bf16 v[68:71], v[204:207], v[28:31], v[68:71]\n"
    "ds_read_b128 v[28:31], v9 offset:35840\n"
    "v_mfma_f32_16x16x32_bf16 v[56:59], v[208:211], v[16:19], v[56:59]\n"
    "v_mfma_f32_16x16x32_bf16 v[60:63], v[212:215], v[16:19], v[60:63]\n"
    "s_waitcnt lgkmcnt(2)\n"
    "v_mfma_f32_16x16x32_bf16 v[68:71], v[216:219], v[16:19], v[68:71]\n"
    "v_mfma_f32_16x16x32_bf16 v[56:59], v[220:223], v[20:23], v[56:59]\n"
    "v_mfma_f32_16x16x32_bf16 v[60:63], v[224:227], v[20:23], v[60:63]\n"
    "s_waitcnt lgkmcnt(1)\n"
    "v_mfma_f32_16x16x32_bf16 v[68:71], v[228:231], v[20:23], v[68:71]\n"
    "v_mfma_f32_16x16x32_bf16 v[56:59], v[232:235], v[24:27], v[56:59]\n"
    "v_mfma_f32_16x16x32_bf16 v[60:63], v[236:239], v[24:27], v[60:63]\n"
    "s_waitcnt lgkmcnt(0)\n"
    "v_mfma_f32_16x16x32_bf16 v[68:71], v[240:243], v[24:27], v[68:71]\n"
    "v_mfma_f32_16x16x32_bf16 v[56:59], v[244:247], v[28:31], v[56:59]\n"
    "v_mfma_f32_16x16x32_bf16 v[60:63], v[248:251], v[28:31], v[60:63]\n"
    "v_mfma_f32_16x16x32_bf16 v[68:71], v[252:255], v[28:31], v[68:71]\n"
    "v_cvt_pk_bf16_f32 v56, v56, v57\n"
    "v_cvt_pk_bf16_f32 v57, v58, v59\n"
    "s_nop 1\n"
    "v_cvt_pk_bf16_f32 v58, v60, v61\n"
    "v_cvt_pk_bf16_f32 v59, v62, v63\n"
    "buffer_store_dwordx4 v[56:59], v15, s[36:39], s73 offen offset:1024 sc1\n"
    "s_nop 0\n"
    "v_cvt_pk_bf16_f32 v68, v68, v69\n"
    "v_cvt_pk_bf16_f32 v69, v70, v71\n"
    "s_nop 0\n"
    "v_permlane16_swap_b32 v68, v70\n"
    "v_permlane16_swap_b32 v69, v71\n"
    "buffer_store_dwordx4 v[68:71], v4, s[36:39], s73 offen offset:1024 sc1\n"
    "v_mov_b32 v12, s68\n"
    "s_mov_b32 s73, 0x80000000\n"
    "s_mov_b32 s68, s71\n"
    "s_mov_b32 s69, 0\n"
    "s_mov_b32 s70, 0x80000000\n"
    "s_waitcnt vmcnt(13)\n"
    "s_add_u32 s71, s68, 1\n"
    "v_add_u32 v13, v11, v3\n"
    "v_cmp_gt_i32 vcc, s71, v13\n"
    "s_nop 0\n"
    "s_or_b64 exec, vcc, s[62:63]\n"
    "buffer_atomic_smax v11, v2, s[36:39], 0 offen sc0 sc1\n"
    "s_mov_b64 exec, -1\n"
    "s_waitcnt vmcnt(3)\n"
    "s_branch .LgDOWN_bar0_%=\n"
    ".LgDOWN_dma1_69_%=:\n"
    "v_mfma_f32_16x16x32_bf16 v[56:59], v[100:103], v[28:31], v[56:59]\n"
    "s_add_u32 m0, s59, 0\n"
    "v_mfma_f32_16x16x32_bf16 v[60:63], v[104:107], v[28:31], v[60:63]\n"
    "s_waitcnt lgkmcnt(2)\n"
    "buffer_load_dwordx4 v1, s[36:39], s76 offen sc1 lds\n"
    "v_mfma_f32_16x16x32_bf16 v[68:71], v[108:111], v[28:31], v[68:71]\n"
    "ds_read_b128 v[28:31], v9 offset:27648\n"
    "v_mfma_f32_16x16x32_bf16 v[56:59], v[112:115], v[16:19], v[56:59]\n"
    "s_add_u32 m0, s59, 4096\n"
    "s_add_u32 s76, s76, 16384\n"
    "v_mfma_f32_16x16x32_bf16 v[60:63], v[116:119], v[16:19], v[60:63]\n"
    "s_waitcnt lgkmcnt(2)\n"
    "buffer_load_dwordx4 v1, s[36:39], s76 offen sc1 lds\n"
    "v_mfma_f32_16x16x32_bf16 v[68:71], v[120:123], v[16:19], v[68:71]\n"
    "ds_read_b128 v[16:19], v9 offset:28672\n"
    "v_mfma_f32_16x16x32_bf16 v[56:59], v[124:127], v[20:23], v[56:59]\n"
    "s_add_u32 m0, s59, 8192\n"
    "s_add_u32 s76, s76, 16384\n"
    "v_mfma_f32_16x16x32_bf16 v[60:63], v[128:131], v[20:23], v[60:63]\n"
    "s_waitcnt lgkmcnt(2)\n"
    "buffer_load_dwordx4 v1, s[36:39], s76 offen sc1 lds\n"
    "v_mfma_f32_16x16x32_bf16 v[68:71], v[132:135], v[20:23], v[68:71]\n"
    "ds_read_b128 v[20:23], v9 offset:29696\n"
    "v_mfma_f32_16x16x32_bf16 v[56:59], v[136:139], v[24:27], v[56:59]\n"
    "v_mfma_f32_16x16x32_bf16 v[60:63], v[140:143], v[24:27], v[60:63]\n"
    "s_waitcnt lgkmcnt(2)\n"
    "v_mfma_f32_16x16x32_bf16 v[68:71], v[144:147], v[24:27], v[68:71]\n"
    "ds_read_b128 v[24:27], v9 offset:30720\n"
    "v_mfma_f32_16x16x32_bf16 v[56:59], v[148:151], v[28:31], v[56:59]\n"
    "s_add_u32 m0, s59, 12288\n"
    "s_add_u32 s76, s76, 16384\n"
    "v_mfma_f32_16x16x32_bf16 v[60:63], v[152:155], v[28:31], v[60:63]\n"
    "s_waitcnt lgkmcnt(2)\n"
    "buffer_load_dwordx4 v1, s[36:39], s76 offen sc1 lds\n"
    "v_mfma_f32_16x16x32_bf16 v[68:71], v[156:159], v[28:31], v[68:71]\n"
    "ds_read_b128 v[28:31], v9 offset:31744\n"
    "v_mfma_f32_16x16x32_bf16 v[56:59], v[160:163], v[16:19], v[56:59]\n"
    "s_add_u32 m0, s59, 16384\n"
    "s_add_u32 s76, s76, 16384\n"
    "v_mfma_f32_16x16x32_bf16 v[60:63], v[164:167], v[16:19], v[60:63]\n"
    "s_waitcnt lgkmcnt(2)\n"
    "buffer_load_dwordx4 v1, s[36:39], s76 offen sc1 lds\n"
    "v_mfma_f32_16x16x32_bf16 v[68:71], v[168:171], v[16:19], v[68:71]\n"
    "ds_read_b128 v[16:19], v9 offset:32768\n"
    "v_mfma_f32_16x16x32_bf16 v[56:59], v[172:175], v[20:23], v[56:59]\n"
    "s_add_u32 m0, s59, 20480\n"
    "s_add_u32 s76, s76, 16384\n"
    "v_mfma_f32_16x16x32_bf16 v[60:63], v[176:179], v[20:23], v[60:63]\n"
    "s_waitcnt lgkmcnt(2)\n"
    "buffer_load_dwordx4 v1, s[36:39], s76 offen sc1 lds\n"
    "v_mfma_f32_16x16x32_bf16 v[68:71], v[180:183], v[20:23], v[68:71]\n"
    "ds_read_b128 v[20:23], v9 offset:33792\n"
    "v_mfma_f32_16x16x32_bf16 v[56:59], v[184:187], v[24:27], v[56:59]\n"
    "v_mfma_f32_16x16x32_bf16 v[60:63], v[188:191], v[24:27], v[60:63]\n"
    "s_waitcnt lgkmcnt(2)\n"
    "v_mfma_f32_16x16x32_bf16 v[68:71], v[192:195], v[24:27], v[68:71]\n"
    "ds_read_b128 v[24:27], v9 offset:34816\n"
    "v_mfma_f32_16x16x32_bf16 v[56:59], v[196:199], v[28:31], v[56:59]\n"
    "s_add_u32 m0, s59, 24576\n"
    "s_add_u32 s76, s76, 16384\n"
    "v_mfma_f32_16x16x32_bf16 v[60:63], v[200:203], v[28:31], v[60:63]\n"
    "s_waitcnt lgkmcnt(2)\n"
    "buffer_load_dwordx4 v1, s[36:39], s76 offen sc1 lds\n"
    "v_mfma_f32_16x16x32_bf16 v[68:71], v[204:207], v[28:31], v[68:71]\n"
    "ds_read_b128 v[28:31], v9 offset:35840\n"
    "v_mfma_f32_16x16x32_bf16 v[56:59], v[208:211], v[16:19], v[56:59]\n"
    "s_add_u32 m0, s59, 28672\n"
    "s_add_u32 s76, s76, 16384\n"
    "v_mfma_f32_16x16x32_bf16 v[60:63], v[212:215], v[16:19], v[60:63]\n"
    "s_waitcnt lgkmcnt(2)\n"
    "buffer_load_dwordx4 v1, s[36:39], s76 offen sc1 lds\n"
    "s_add_u32 m0, s59, 32768\n"
    "s_add_u32 s76, s76, 16384\n"
    "v_mfma_f32_16x16x32_bf16 v[68:71], v[216:219], v[16:19], v[68:71]\n"
    "buffer_load_dwordx4 v1, s[36:39], s76 offen sc1 lds\n"
    "v_mfma_f32_16x16x32_bf16 v[56:59], v[220:223], v[20:23], v[56:59]\n"
    "v_mfma_f32_16x16x32_bf16 v[60:63], v[224:227], v[20:23], v[60:63]\n"
    "s_waitcnt lgkmcnt(1)\n"
    "v_mfma_f32_16x16x32_bf16 v[68:71], v[228:231], v[20:23], v[68:71]\n"
    "v_mfma_f32_16x16x32_bf16 v[56:59], v[232:235], v[24:27], v[56:59]\n"
    "v_mfma_f32_16x16x32_bf16 v[60:63], v[236:239], v[24:27], v[60:63]\n"
    "s_waitcnt lgkmcnt(0)\n"
    "v_mfma_f32_16x16x32_bf16 v[68:71], v[240:243], v[24:27], v[68:71]\n"
    "v_mfma_f32_16x16x32_bf16 v[56:59], v[244:247], v[28:31], v[56:59]\n"
    "v_mfma_f32_16x16x32_bf16 v[60:63], v[248:251], v[28:31], v[60:63]\n"
    "v_mfma_f32_16x16x32_bf16 v[68:71], v[252:255], v[28:31], v[68:71]\n"
    "v_cvt_pk_bf16_f32 v56, v56, v57\n"
    "v_cvt_pk_bf16_f32 v57, v58, v59\n"
    "s_nop 1\n"
    "v_cvt_pk_bf16_f32 v58, v60, v61\n"
    "v_cvt_pk_bf16_f32 v59, v62, v63\n"
    "buffer_store_dwordx4 v[56:59], v15, s[36:39], s73 offen offset:1024 sc1\n"
    "s_nop 0\n"
    "v_cvt_pk_bf16_f32 v68, v68, v69\n"
    "v_cvt_pk_bf16_f32 v69, v70, v71\n"
    "s_nop 0\n"
    "v_permlane16_swap_b32 v68, v70\n"
    "v_permlane16_swap_b32 v69, v71\n"
    "buffer_store_dwordx4 v[68:71], v4, s[36:39], s73 offen offset:1024 sc1\n"
    "v_mov_b32 v12, s68\n"
    "s_mov_b32 s73, 0x80000000\n"
    "s_mov_b32 s68, s71\n"
    "s_mov_b32 s69, 0\n"
    "s_mov_b32 s70, 0x80000000\n"
    "s_waitcnt vmcnt(12)\n"
    "s_add_u32 s71, s68, 1\n"
    "v_add_u32 v13, v11, v3\n"
    "v_cmp_gt_i32 vcc, s71, v13\n"
    "s_nop 0\n"
    "s_or_b64 exec, vcc, s[62:63]\n"
    "buffer_atomic_smax v11, v2, s[36:39], 0 offen sc0 sc1\n"
    "s_mov_b64 exec, -1\n"
    "s_waitcnt vmcnt(3)\n"
    "s_branch .LgDOWN_bar0_%=\n"
    ".LgDOWN_dma1_89_%=:\n"
    "v_mfma_f32_16x16x32_bf16 v[68:71], v[180:183], v[20:23], v[68:71]\n"
    "ds_read_b128 v[20:23], v9 offset:33792\n"
    "v_mfma_f32_16x16x32_bf16 v[56:59], v[184:187], v[24:27], v[56:59]\n"
    "s_add_u32 m0, s59, 0\n"
    "v_mfma_f32_16x16x32_bf16 v[60:63], v[188:191], v[24:27], v[60:63]\n"
    "s_waitcnt lgkmcnt(2)\n"
    "buffer_load_dwordx4 v1, s[36:39], s76 offen sc1 lds\n"
    "v_mfma_f32_16x16x32_bf16 v[68:71], v[192:195], v[24:27], v[68:71]\n"
    "ds_read_b128 v[24:27], v9 offset:34816\n"
    "v_mfma_f32_16x16x32_bf16 v[56:59], v[196:199], v[28:31], v[56:59]\n"
    "v_mfma_f32_16x16x32_bf16 v[60:63], v[200:203], v[28:31], v[60:63]\n"
    "s_waitcnt lgkmcnt(2)\n"
    "v_mfma_f32_16x16x32_bf16 v[68:71], v[204:207], v[28:31], v[68:71]\n"
    "ds_read_b128 v[28:31], v9 offset:35840\n"
    "v_mfma_f32_16x16x32_bf16 v[56:59], v[208:211], v[16:19], v[56:59]\n"
    "s_add_u32 m0, s59, 4096\n"
    "s_add_u32 s76, s76, 16384\n"
    "v_mfma_f32_16x16x32_bf16 v[60:63], v[212:215], v[16:19], v[60:63]\n"
    "s_waitcnt lgkmcnt(2)\n"
    "buffer_load_dwordx4 v1, s[36:39], s76 offen sc1 lds\n"
    "v_mfma_f32_16x16x32_bf16 v[68:71], v[216:219], v[16:19], v[68:71]\n"
    "v_mfma_f32_16x16x32_bf16 v[56:59], v[220:223], v[20:23], v[56:59]\n"
    "v_mfma_f32_16x16x32_bf16 v[60:63], v[224:227], v[20:23], v[60:63]\n"
    "s_waitcnt lgkmcnt(1)\n"
    "v_mfma_f32_16x16x32_bf16 v[68:71], v[228:231], v[20:23], v[68:71]\n"
    "v_mfma_f32_16x16x32_bf16 v[56:59], v[232:235], v[24:27], v[56:59]\n"
    "s_add_u32 m0, s59, 8192\n"
    "s_add_u32 s76, s76, 16384\n"
    "v_mfma_f32_16x16x32_bf16 v[60:63], v[236:239], v[24:27], v[60:63]\n"
    "s_waitcnt lgkmcnt(0)\n"
    "buffer_load_dwordx4 v1, s[36:39], s76 offen sc1 lds\n"
    "v_mfma_f32_16x16x32_bf16 v[68:71], v[240:243], v[24:27], v[68:71]\n"
    "v_mfma_f32_16x16x32_bf16 v[56:59], v[244:247], v[28:31], v[56:59]\n"
    "v_mfma_f32_16x16x32_bf16 v[60:63], v[248:251], v[28:31], v[60:63]\n"
    "v_mfma_f32_16x16x32_bf16 v[68:71], v[252:255], v[28:31], v[68:71]\n"
    "v_cvt_pk_bf16_f32 v56, v56, v57\n"
    "v_cvt_pk_bf16_f32 v57, v58, v59\n"
    "s_nop 1\n"
    "v_cvt_pk_bf16_f32 v58, v60, v61\n"
    "v_cvt_pk_bf16_f32 v59, v62, v63\n"
    "buffer_store_dwordx4 v[56:59], v15, s[36:39], s73 offen offset:1024 sc1\n"
    "s_nop 0\n"
    "v_cvt_pk_bf16_f32 v68, v68, v69\n"
    "v_cvt_pk_bf16_f32 v69, v70, v71\n"
    "s_nop 0\n"
    "v_permlane16_swap_b32 v68, v70\n"
    "v_permlane16_swap_b32 v69, v71\n"
    "buffer_store_dwordx4 v[68:71], v4, s[36:39], s73 offen offset:1024 sc1\n"
    "v_mov_b32 v12, s68\n"
    "s_mov_b32 s73, 0x80000000\n"
    "s_mov_b32 s68, s71\n"
    "s_add_u32 m0, s59, 12288\n"
    "s_add_u32 s76, s76, 16384\n"
    "buffer_load_dwordx4 v1, s[36:39], s76 offen sc1 lds\n"
    "s_add_u32 m0, s59, 16384\n"
    "s_add_u32 s76, s76, 16384\n"
    "buffer_load_dwordx4 v1, s[36:39], s76 offen sc1 lds\n"
    "s_add_u32 m0, s59, 20480\n"
    "s_add_u32 s76, s76, 16384\n"
    "buffer_load_dwordx4 v1, s[36:39], s76 offen sc1 lds\n"
    "s_add_u32 m0, s59, 24576\n"
    "s_add_u32 s76, s76, 16384\n"
    "buffer_load_dwordx4 v1, s[36:39], s76 offen sc1 lds\n"
    "s_add_u32 m0, s59, 28672\n"
    "s_add_u32 s76, s76, 16384\n"
    "buffer_load_dwordx4 v1, s[36:39], s76 offen sc1 lds\n"
    "s_add_u32 m0, s59, 32768\n"
    "s_add_u32 s76, s76, 16384\n"
    "buffer_load_dwordx4 v1, s[36:39], s76 offen sc1 lds\n"
    "s_waitcnt vmcnt(6)\n"
    "buffer_atomic_smax v12, v10, s[36:39], 0 offen sc1\n"
    "s_mov_b32 s69, 0x80000000\n"
    "s_mov_b32 s70, 0x80000000\n"
    "s_waitcnt vmcnt(12)\n"
    "s_add_u32 s71, s68, 1\n"
    "v_add_u32 v13, v11, v3\n"
    "v_cmp_gt_i32 vcc, s71, v13\n"
    "s_nop 0\n"
    "s_or_b64 exec, vcc, s[62:63]\n"
    "buffer_atomic_smax v11, v2, s[36:39], 0 offen sc0 sc1\n"
    "s_mov_b64 exec, -1\n"
    "s_waitcnt vmcnt(2)\n"
    "s_branch .LgDOWN_bar0_%=\n"
    ".LgDOWN_idle1_%=:\n"
    "s_or_b64 exec, vcc, s[62:63]\n"
    "buffer_atomic_smax v5, v2, s[36:39], 0 offen sc0 sc1\n"
    "s_mov_b64 exec, -1\n"
    "s_waitcnt vmcnt(1)\n"
    "buffer_atomic_smax v12, v10, s[36:39], 0 offen sc1\n"
    "s_branch .LgDOWN_block1_1_%=\n"
    ".LgDOWN_block1_%=:\n"
    "v_add_u32 v13, v11, v3\n"
    "v_cmp_gt_i32 vcc, s71, v13\n"
    "s_nop 0\n"
    "s_or_b64 exec, vcc, s[62:63]\n"
    "buffer_atomic_smax v11, v2, s[36:39], 0 offen sc0 sc1\n"
    "s_mov_b64 exec, -1\n"
    "s_sleep 3\n"
    "s_waitcnt vmcnt(1)\n"
    ".LgDOWN_block1_0_%=:\n"
    "v_add_u32 v13, v5, v3\n"
    "v_cmp_gt_i32 vcc, s71, v13\n"
    "s_cbranch_vccz .LgDOWN_go1_%=\n"
    "v_add_u32 v13, v5, v3\n"
    "v_cmp_gt_i32 vcc, s71, v13\n"
    "s_nop 0\n"
    "s_or_b64 exec, vcc, s[62:63]\n"
    "buffer_atomic_smax v5, v2, s[36:39], 0 offen sc0 sc1\n"
    "s_mov_b64 exec, -1\n"
    "s_sleep 3\n"
    "s_waitcnt vmcnt(1)\n"
    ".LgDOWN_block1_1_%=:\n"
    "v_add_u32 v13, v11, v3\n"
    "v_cmp_gt_i32 vcc, s71, v13\n"
    "s_cbranch_vccz .LgDOWN_go1_%=\n"
    "s_sub_u32 s72, s72, 1\n"
    "s_cbranch_scc0 .LgDOWN_block1_%=\n"
    "s_mov_b32 s72, 0\n"
    ".LgDOWN_go1_%=:\n"
    "s_lshr_b32 s76, s71, 1\n"
    "s_and_b32 s76, s76, s54\n"
    "s_mul_i32 s76, s76, s53\n"
    "s_add_u32 s76, s76, s52\n"
    "s_add_u32 m0, s59, 0\n"
    "s_nop 0\n"
    "buffer_load_dwordx4 v1, s[36:39], s76 offen sc1 lds\n"
    "s_add_u32 m0, s59, 4096\n"
    "s_add_u32 s76, s76, 16384\n"
    "buffer_load_dwordx4 v1, s[36:39], s76 offen sc1 lds\n"
    "s_add_u32 m0, s59, 8192\n"
    "s_add_u32 s76, s76, 16384\n"
    "buffer_load_dwordx4 v1, s[36:39], s76 offen sc1 lds\n"
    "s_add_u32 m0, s59, 12288\n"
    "s_add_u32 s76, s76, 16384\n"
    "buffer_load_dwordx4 v1, s[36:39], s76 offen sc1 lds\n"
    "s_add_u32 m0, s59, 16384\n"
    "s_add_u32 s76, s76, 16384\n"
    "buffer_load_dwordx4 v1, s[36:39], s76 offen sc1 lds\n"
    "s_add_u32 m0, s59, 20480\n"
    "s_add_u32 s76, s76, 16384\n"
    "buffer_load_dwordx4 v1, s[36:39], s76 offen sc1 lds\n"
    "s_add_u32 m0, s59, 24576\n"
    "s_add_u32 s76, s76, 16384\n"
    "buffer_load_dwordx4 v1, s[36:39], s76 offen sc1 lds\n"
    "s_add_u32 m0, s59, 28672\n"
    "s_add_u32 s76, s76, 16384\n"
    "buffer_load_dwordx4 v1, s[36:39], s76 offen sc1 lds\n"
    "s_add_u32 m0, s59, 32768\n"
    "s_add_u32 s76, s76, 16384\n"
    "buffer_load_dwordx4 v1, s[36:39], s76 offen sc1 lds\n"
    "s_mov_b32 s69, 0x80000000\n"
    "s_mov_b32 s70, 0x80000000\n"
    "s_waitcnt vmcnt(9)\n"
    "s_add_u32 s71, s68, 1\n"
    "v_add_u32 v13, v11, v3\n"
    "v_cmp_gt_i32 vcc, s71, v13\n"
    "s_nop 0\n"
    "s_or_b64 exec, vcc, s[62:63]\n"
    "buffer_atomic_smax v11, v2, s[36:39], 0 offen sc0 sc1\n"
    "s_mov_b64 exec, -1\n"
    "s_waitcnt vmcnt(1)\n"
    "s_branch .LgDOWN_bar0_%=\n"
    ".LgDOWN_bar1_%=:\n"
    "s_barrier\n"
    "ds_read_b128 v[16:19], v9 offset:0\n"
    "ds_read_b128 v[20:23], v9 offset:1024\n"
    "ds_read_b128 v[24:27], v9 offset:2048\n"
    "ds_read_b128 v[28:31], v9 offset:3072\n"
    "v_add_u32 v13, v5, v3\n"
    "v_cmp_gt_i32 vcc, s71, v13\n"
    "s_nop 0\n"
    "s_or_b64 exec, vcc, s[62:63]\n"
    "buffer_atomic_smax v5, v2, s[36:39], 0 offen sc0 sc1\n"
    "s_mov_b64 exec, -1\n"
    "s_lshr_b32 s76, s71, 1\n"
    "s_and_b32 s76, s76, s54\n"
    "s_mul_i32 s76, s76, s53\n"
    "s_add_u32 s76, s76, s52\n"
    "s_waitcnt lgkmcnt(3)\n"
    "s_branch .LgDOWN_head1_%=\n"
    ".LgDOWN_FIN_%=:\n"
  );
}

DEVI void role_rec(const Params& p, int L, int j, int i, u32 dbg, int& rid, unsigned long long& tag, bool& sure, unsigned char* smem) {
  const int tid = threadIdx.x, lane = tid & 63, w = (int)rfl(tid >> 6), h = 8 * j + i;
  const int c = lane & 15, g = lane >> 4, pp = lane & 3;
  {
    v4u t;
    const int q0 = w == 0 ? 4 * (c >> 3) + (c & 3) : (c & 7);
    const u32 one = (w == 0 && ((c >> 2) & 1) != (g & 1)) ? 0u : 0x3F80u << (16 * (q0 & 1));
    const v4u t1 = {(u32)(((g & 1) * 16 + c) * 8), (u32)(g * 128 + (c & 7) * 16 + (c >> 3) * 8), c < 8 ? (u32)(g * 128 + c * 16) : OOB, 0u};
    const v4u t2 = {(u32)(g * 16384 + c * 16), g == 0 ? (u32)(c * 16) : OOB,
                    (u32)((g >> 1) * 128 + (c & 7) * 16 + (2 * (g & 1) + (c >> 3)) * 32768), lane < 32 ? (u32)lane * 64 : OOB};
#pragma unroll
    for (int d = 0; d < 4; ++d) t[d] = w == 1 ? t1[d] : (w == 2 ? t2[d] : ((q0 >> 1) == d ? one : 0u));
    *(v4u*)(smem + 12288 + tid * 16) = t;
    asm volatile("s_waitcnt lgkmcnt(0)" ::: "memory");
  }
  const u32 wb = 576u * 4096 * 2 - h * 256;
  v16u sa = cat4(make_srd(p.ws, WS_END), make_srd(p.Wq + h * 128, wb), make_srd(p.Wk + h * 128, wb), make_srd(p.Wv + h * 128, wb));
  const v4u so = make_srd(p.Wo + h * 128 * 576, 128u * 1152);
  const int mine = 4 * i + j, unit = w == 0 ? mine : ((w == 1 && i == 3) ? 32 + j : -1);
  const u32 hot = HOT + L * 8192, hot2 = HOT2 + (1 - L) * 8192, img = hot2 + j * 2 * CHUNK;
  v16u sb = {so.x, so.y, so.z, so.w,
             pga(L, B_QKV + 3 * h + 1) + w * 2048, pga(L, B_KVM + h) + w * 1536, mbox_att(L, j, i, w), mbox(L, j, SL_REC + i, w),
             (u32)w * 1024, dbg + w * 64, INITF + L * 8192, hot + 4 * w * CHUNK + h * 256,
             unit < 0 ? OOB : hot + unit * CHUNK, unit < 0 ? OOB : hot2 + (unit >> 5) * CHUNK + (unit & 31) * 256, img, hot + (32 + w) * CHUNK + h * 256};
  const int jr = lane >> 3;
  const u32 row = (u32)(16 * (w >> 1) + 8 * ((jr >> 1) & 1) + 4 * (w & 1) + 2 * (jr >> 2) + (jr & 1));
  const u32 ch = (((u32)lane & 7) ^ wswz(jr, w)) << 4;
  v16u va = {0, 0, 0, 0, 0, 0, 0, 0, 0, 0, 0, 0, 0, 0, 0, 0};
  va[0] = lane * 16;
  va[1] = 16384 + w * 2048 + lane * 16;
  va[2] = (u32)((c >> 3) * 0xAAAA) | 0x4444u;
  const int qq = (lane >> 2) & 3;
  va[3] = (u32)((g >> 1) * 4096 + (g & 1) * 2048 + (4 * (qq >> 1) + (qq & 1)) * 128 + ((2 * w + (pp >> 1)) << 4) + 8 * (pp & 1));
  va[4] = row * 8192 + ch;
  va[5] = row * 1152 + ch;
  va[6] = (u32)(w >> 1) * 4096 + tr_base(lane, 4 * (w & 1) + pp);
  va[7] = (u32)w * 4096 + tr_base(lane, 4 * (qq >> 1) + (pp >> 1)) + 8 * (pp & 1);
  va[8] = hot2 + 32 * CHUNK + ((w == 0 && i < 4) ? 0 : 1);
  va[9] = (u32)(h * 64 + 4 * w);
  va[10] = hot + mine * CHUNK + w * 2048;
  va[11] = w == 3 ? hot + (32 + j) * CHUNK + i * 1024 : (w == 2 ? img + i * 1024 : ((w == 1 && i == 1) ? img + CHUNK : OOB));
  va[12] = w == 3 ? ~0u : 0u;
  va[13] = w == 3 ? 0u : ~0u;
  asm volatile("" : "+v"(va));
  if (!confirm(rid, tag, sure, smem)) return;
  va[14] = (u32)(2 * tag); va[15] = (u32)((2 * tag) >> 32);
  RUN_ASM(
    "s_nop 4\n"
    "s_getpc_b64 s[88:89]\n"
    ".Lrec_PC_%=:\n"
    "s_and_b32 s89, s89, 0xffff\n"
    "s_mov_b32 s90, .Lrec_FIN_%=-.Lrec_PC_%=\n"
    "s_mov_b32 s91, 0x20000\n"
    "v_mbcnt_lo_u32_b32 v78, -1, 0\n"
    "v_mbcnt_hi_u32_b32 v78, -1, v78\n"
    "v_lshlrev_b32 v78, 7, v78\n"
    "buffer_load_dword v79, v78, s[88:91], 0 offen\n"
    "v_add_u32 v78, 8192, v78\n"
    "buffer_load_dword v79, v78, s[88:91], 0 offen\n"
    "v_readfirstlane_b32 s76, v8\n"
    "v_readfirstlane_b32 s77, v9\n"
    "v_readfirstlane_b32 s78, v10\n"
    "v_readfirstlane_b32 s79, v11\n"
    "v_readfirstlane_b32 s80, v12\n"
    "v_readfirstlane_b32 s81, v13\n"
    "v_readfirstlane_b32 s30, v14\n"
    "v_readfirstlane_b32 s31, v15\n"
    "s_memtime s[34:35]\n"
    "s_memrealtime s[92:93]\n"
    "s_mov_b32 s69, 262144\n"
    "s_add_u32 m0, s60, 16384\n"
    "s_mov_b32 s73, 0\n"
    "buffer_load_dwordx4 v4, s[40:43], s73 offen lds\n"
    "s_add_u32 m0, s60, 20480\n"
    "s_mov_b32 s73, 128\n"
    "buffer_load_dwordx4 v4, s[40:43], s73 offen lds\n"
    "s_add_u32 m0, s60, 24576\n"
    "s_mov_b32 s73, 0\n"
    "buffer_load_dwordx4 v4, s[44:47], s73 offen lds\n"
    "s_add_u32 m0, s60, 28672\n"
    "s_mov_b32 s73, 128\n"
    "buffer_load_dwordx4 v4, s[44:47], s73 offen lds\n"
    "s_add_u32 m0, s60, 32768\n"
    "s_mov_b32 s73, 0\n"
    "buffer_load_dwordx4 v4, s[48:51], s73 offen lds\n"
    "s_add_u32 m0, s60, 36864\n"
    "s_mov_b32 s73, 128\n"
    "buffer_load_dwordx4 v4, s[48:51], s73 offen lds\n"
    "s_add_u32 m0, s60, 40960\n"
    "s_mov_b32 s73, 262144\n"
    "buffer_load_dwordx4 v4, s[40:43], s73 offen lds\n"
    "s_add_u32 m0, s60, 45056\n"
    "s_mov_b32 s73, 262272\n"
    "buffer_load_dwordx4 v4, s[40:43], s73 offen lds\n"
    "s_add_u32 m0, s60, 49152\n"
    "s_mov_b32 s73, 262144\n"
    "buffer_load_dwordx4 v4, s[44:47], s73 offen lds\n"
    "s_add_u32 m0, s60, 53248\n"
    "s_mov_b32 s73, 262272\n"
    "buffer_load_dwordx4 v4, s[44:47], s73 offen lds\n"
    "s_add_u32 m0, s60, 57344\n"
    "s_mov_b32 s73, 262144\n"
    "buffer_load_dwordx4 v4, s[48:51], s73 offen lds\n"
    "s_add_u32 m0, s60, 61440\n"
    "s_mov_b32 s73, 262272\n"
    "buffer_load_dwordx4 v4, s[48:51], s73 offen lds\n"
    "s_add_u32 m0, s60, 65536\n"
    "s_mov_b32 s73, 524288\n"
    "buffer_load_dwordx4 v4, s[40:43], s73 offen lds\n"
    "s_add_u32 m0, s60, 69632\n"
    "s_mov_b32 s73, 524416\n"
    "buffer_load_dwordx4 v4, s[40:43], s73 offen lds\n"
    "s_add_u32 m0, s60, 73728\n"
    "s_mov_b32 s73, 524288\n"
    "buffer_load_dwordx4 v4, s[44:47], s73 offen lds\n"
    "s_add_u32 m0, s60, 77824\n"
    "s_mov_b32 s73, 524416\n"
    "buffer_load_dwordx4 v4, s[44:47], s73 offen lds\n"
    "s_add_u32 m0, s60, 81920\n"
    "s_mov_b32 s73, 524288\n"
    "buffer_load_dwordx4 v4, s[48:51], s73 offen lds\n"
    "s_add_u32 m0, s60, 86016\n"
    "s_mov_b32 s73, 524416\n"
    "buffer_load_dwordx4 v4, s[48:51], s73 offen lds\n"
    "s_add_u32 m0, s60, 90112\n"
    "s_mov_b32 s73, 786432\n"
    "buffer_load_dwordx4 v4, s[40:43], s73 offen lds\n"
    "s_add_u32 m0, s60, 94208\n"
    "s_mov_b32 s73, 786560\n"
    "buffer_load_dwordx4 v4, s[40:43], s73 offen lds\n"
    "s_add_u32 m0, s60, 98304\n"
    "s_mov_b32 s73, 786432\n"
    "buffer_load_dwordx4 v4, s[44:47], s73 offen lds\n"
    "s_add_u32 m0, s60, 102400\n"
    "s_mov_b32 s73, 786560\n"
    "buffer_load_dwordx4 v4, s[44:47], s73 offen lds\n"
    "s_add_u32 m0, s60, 106496\n"
    "s_mov_b32 s73, 786432\n"
    "buffer_load_dwordx4 v4, s[48:51], s73 offen lds\n"
    "s_add_u32 m0, s60, 110592\n"
    "s_mov_b32 s73, 786560\n"
    "buffer_load_dwordx4 v4, s[48:51], s73 offen lds\n"
    "s_add_u32 m0, s60, 114688\n"
    "s_mov_b32 s73, 1048576\n"
    "buffer_load_dwordx4 v4, s[40:43], s73 offen lds\n"
    "s_add_u32 m0, s60, 118784\n"
    "s_mov_b32 s73, 1048704\n"
    "buffer_load_dwordx4 v4, s[40:43], s73 offen lds\n"
    "s_add_u32 m0, s60, 122880\n"
    "s_mov_b32 s73, 1048576\n"
    "buffer_load_dwordx4 v4, s[44:47], s73 offen lds\n"
    "s_add_u32 m0, s60, 126976\n"
    "s_mov_b32 s73, 1048704\n"
    "buffer_load_dwordx4 v4, s[44:47], s73 offen lds\n"
    "s_add_u32 m0, s60, 131072\n"
    "s_mov_b32 s73, 1048576\n"
    "buffer_load_dwordx4 v4, s[48:51], s73 offen lds\n"
    "s_add_u32 m0, s60, 135168\n"
    "s_mov_b32 s73, 1048704\n"
    "buffer_load_dwordx4 v4, s[48:51], s73 offen lds\n"
    "s_add_u32 m0, s60, 139264\n"
    "s_mov_b32 s73, 1310720\n"
    "buffer_load_dwordx4 v4, s[40:43], s73 offen lds\n"
    "s_add_u32 m0, s60, 143360\n"
    "s_mov_b32 s73, 1310848\n"
    "buffer_load_dwordx4 v4, s[40:43], s73 offen lds\n"
    "s_add_u32 m0, s60, 147456\n"
    "s_mov_b32 s73, 1310720\n"
    "buffer_load_dwordx4 v4, s[44:47], s73 offen lds\n"
    "s_add_u32 m0, s60, 151552\n"
    "s_mov_b32 s73, 1310848\n"
    "buffer_load_dwordx4 v4, s[44:47], s73 offen lds\n"
    "s_add_u32 m0, s60, 155648\n"
    "s_mov_b32 s73, 1310720\n"
    "buffer_load_dwordx4 v4, s[48:51], s73 offen lds\n"
    "s_add_u32 m0, s60, 159744\n"
    "s_mov_b32 s73, 1310848\n"
    "buffer_load_dwordx4 v4, s[48:51], s73 offen lds\n"
    "s_waitcnt vmcnt(30) lgkmcnt(0)\n"
    "s_barrier\n"
    "v_add_u32 v8, 16384, v6\n"
    "ds_read_b64_tr_b16 a[0:1], v8 offset:0\n"
    "ds_read_b64_tr_b16 a[2:3], v8 offset:512\n"
    "ds_read_b64_tr_b16 a[8:9], v8 offset:8\n"
    "ds_read_b64_tr_b16 a[10:11], v8 offset:520\n"
    "ds_read_b64_tr_b16 a[144:145], v8 offset:8192\n"
    "ds_read_b64_tr_b16 a[146:147], v8 offset:8704\n"
    "ds_read_b64_tr_b16 a[152:153], v8 offset:8200\n"
    "ds_read_b64_tr_b16 a[154:155], v8 offset:8712\n"
    "ds_read_b64_tr_b16 v[112:113], v8 offset:16384\n"
    "ds_read_b64_tr_b16 v[114:115], v8 offset:16896\n"
    "ds_read_b64_tr_b16 v[224:225], v8 offset:16392\n"
    "ds_read_b64_tr_b16 v[226:227], v8 offset:16904\n"
    "s_waitcnt vmcnt(24) lgkmcnt(0)\n"
    "s_barrier\n"
    "s_add_u32 m0, s60, 16384\n"
    "s_mov_b32 s73, 1572864\n"
    "buffer_load_dwordx4 v4, s[40:43], s73 offen lds\n"
    "s_add_u32 m0, s60, 20480\n"
    "s_mov_b32 s73, 1572992\n"
    "buffer_load_dwordx4 v4, s[40:43], s73 offen lds\n"
    "s_add_u32 m0, s60, 24576\n"
    "s_mov_b32 s73, 1572864\n"
    "buffer_load_dwordx4 v4, s[44:47], s73 offen lds\n"
    "s_add_u32 m0, s60, 28672\n"
    "s_mov_b32 s73, 1572992\n"
    "buffer_load_dwordx4 v4, s[44:47], s73 offen lds\n"
    "s_add_u32 m0, s60, 32768\n"
    "s_mov_b32 s73, 1572864\n"
    "buffer_load_dwordx4 v4, s[48:51], s73 offen lds\n"
    "s_add_u32 m0, s60, 36864\n"
    "s_mov_b32 s73, 1572992\n"
    "buffer_load_dwordx4 v4, s[48:51], s73 offen lds\n"
    "v_add_u32 v8, 40960, v6\n"
    "ds_read_b64_tr_b16 a[4:5], v8 offset:0\n"
    "ds_read_b64_tr_b16 a[6:7], v8 offset:512\n"
    "ds_read_b64_tr_b16 a[12:13], v8 offset:8\n"
    "ds_read_b64_tr_b16 a[14:15], v8 offset:520\n"
    "ds_read_b64_tr_b16 a[148:149], v8 offset:8192\n"
    "ds_read_b64_tr_b16 a[150:151], v8 offset:8704\n"
    "ds_read_b64_tr_b16 a[156:157], v8 offset:8200\n"
    "ds_read_b64_tr_b16 a[158:159], v8 offset:8712\n"
    "ds_read_b64_tr_b16 v[116:117], v8 offset:16384\n"
    "ds_read_b64_tr_b16 v[118:119], v8 offset:16896\n"
    "ds_read_b64_tr_b16 v[228:229], v8 offset:16392\n"
    "ds_read_b64_tr_b16 v[230:231], v8 offset:16904\n"
    "s_waitcnt vmcnt(24) lgkmcnt(0)\n"
    "s_barrier\n"
    "s_add_u32 m0, s60, 40960\n"
    "s_mov_b32 s73, 1835008\n"
    "buffer_load_dwordx4 v4, s[40:43], s73 offen lds\n"
    "s_add_u32 m0, s60, 45056\n"
    "s_mov_b32 s73, 1835136\n"
    "buffer_load_dwordx4 v4, s[40:43], s73 offen lds\n"
    "s_add_u32 m0, s60, 49152\n"
    "s_mov_b32 s73, 1835008\n"
    "buffer_load_dwordx4 v4, s[44:47], s73 offen lds\n"
    "s_add_u32 m0, s60, 53248\n"
    "s_mov_b32 s73, 1835136\n"
    "buffer_load_dwordx4 v4, s[44:47], s73 offen lds\n"
    "s_add_u32 m0, s60, 57344\n"
    "s_mov_b32 s73, 1835008\n"
    "buffer_load_dwordx4 v4, s[48:51], s73 offen lds\n"
    "s_add_u32 m0, s60, 61440\n"
    "s_mov_b32 s73, 1835136\n"
    "buffer_load_dwordx4 v4, s[48:51], s73 offen lds\n"
    "v_add_u32 v8, 65536, v6\n"
    "ds_read_b64_tr_b16 a[16:17], v8 offset:0\n"
    "ds_read_b64_tr_b16 a[18:19], v8 offset:512\n"
    "ds_read_b64_tr_b16 a[24:25], v8 offset:8\n"
    "ds_read_b64_tr_b16 a[26:27], v8 offset:520\n"
    "ds_read_b64_tr_b16 a[160:161], v8 offset:8192\n"
    "ds_read_b64_tr_b16 a[162:163], v8 offset:8704\n"
    "ds_read_b64_tr_b16 a[168:169], v8 offset:8200\n"
    "ds_read_b64_tr_b16 a[170:171], v8 offset:8712\n"
    "ds_read_b64_tr_b16 v[120:121], v8 offset:16384\n"
    "ds_read_b64_tr_b16 v[122:123], v8 offset:16896\n"
    "ds_read_b64_tr_b16 v[232:233], v8 offset:16392\n"
    "ds_read_b64_tr_b16 v[234:235], v8 offset:16904\n"
    "s_waitcnt vmcnt(24) lgkmcnt(0)\n"
    "s_barrier\n"
    "s_add_u32 m0, s60, 65536\n"
    "s_mov_b32 s73, 2097152\n"
    "buffer_load_dwordx4 v4, s[40:43], s73 offen lds\n"
    "s_add_u32 m0, s60, 69632\n"
    "s_mov_b32 s73, 2097280\n"
    "buffer_load_dwordx4 v4, s[40:43], s73 offen lds\n"
    "s_add_u32 m0, s60, 73728\n"
    "s_mov_b32 s73, 2097152\n"
    "buffer_load_dwordx4 v4, s[44:47], s73 offen lds\n"
    "s_add_u32 m0, s60, 77824\n"
    "s_mov_b32 s73, 2097280\n"
    "buffer_load_dwordx4 v4, s[44:47], s73 offen lds\n"
    "s_add_u32 m0, s60, 81920\n"
    "s_mov_b32 s73, 2097152\n"
    "buffer_load_dwordx4 v4, s[48:51], s73 offen lds\n"
    "s_add_u32 m0, s60, 86016\n"
    "s_mov_b32 s73, 2097280\n"
    "buffer_load_dwordx4 v4, s[48:51], s73 offen lds\n"
    "v_add_u32 v8, 90112, v6\n"
    "ds_read_b64_tr_b16 a[20:21], v8 offset:0\n"
    "ds_read_b64_tr_b16 a[22:23], v8 offset:512\n"
    "ds_read_b64_tr_b16 a[28:29], v8 offset:8\n"
    "ds_read_b64_tr_b16 a[30:31], v8 offset:520\n"
    "ds_read_b64_tr_b16 a[164:165], v8 offset:8192\n"
    "ds_read_b64_tr_b16 a[166:167], v8 offset:8704\n"
    "ds_read_b64_tr_b16 a[172:173], v8 offset:8200\n"
    "ds_read_b64_tr_b16 a[174:175], v8 offset:8712\n"
    "ds_read_b64_tr_b16 v[124:125], v8 offset:16384\n"
    "ds_read_b64_tr_b16 v[126:127], v8 offset:16896\n"
    "ds_read_b64_tr_b16 v[236:237], v8 offset:16392\n"
    "ds_read_b64_tr_b16 v[238:239], v8 offset:16904\n"
    "s_waitcnt vmcnt(24) lgkmcnt(0)\n"
    "s_barrier\n"
    "s_add_u32 m0, s60, 90112\n"
    "s_mov_b32 s73, 2359296\n"
    "buffer_load_dwordx4 v4, s[40:43], s73 offen lds\n"
    "s_add_u32 m0, s60, 94208\n"
    "s_mov_b32 s73, 2359424\n"
    "buffer_load_dwordx4 v4, s[40:43], s73 offen lds\n"
    "s_add_u32 m0, s60, 98304\n"
    "s_mov_b32 s73, 2359296\n"
    "buffer_load_dwordx4 v4, s[44:47], s73 offen lds\n"
    "s_add_u32 m0, s60, 102400\n"
    "s_mov_b32 s73, 2359424\n"
    "buffer_load_dwordx4 v4, s[44:47], s73 offen lds\n"
    "s_add_u32 m0, s60, 106496\n"
    "s_mov_b32 s73, 2359296\n"
    "buffer_load_dwordx4 v4, s[48:51], s73 offen lds\n"
    "s_add_u32 m0, s60, 110592\n"
    "s_mov_b32 s73, 2359424\n"
    "buffer_load_dwordx4 v4, s[48:51], s73 offen lds\n"
    "v_add_u32 v8, 114688, v6\n"
    "ds_read_b64_tr_b16 a[32:33], v8 offset:0\n"
    "ds_read_b64_tr_b16 a[34:35], v8 offset:512\n"
    "ds_read_b64_tr_b16 a[40:41], v8 offset:8\n"
    "ds_read_b64_tr_b16 a[42:43], v8 offset:520\n"
    "ds_read_b64_tr_b16 a[176:177], v8 offset:8192\n"
    "ds_read_b64_tr_b16 a[178:179], v8 offset:8704\n"
    "ds_read_b64_tr_b16 a[184:185], v8 offset:8200\n"
    "ds_read_b64_tr_b16 a[186:187], v8 offset:8712\n"
    "ds_read_b64_tr_b16 v[128:129], v8 offset:16384\n"
    "ds_read_b64_tr_b16 v[130:131], v8 offset:16896\n"
    "ds_read_b64_tr_b16 v[240:241], v8 offset:16392\n"
    "ds_read_b64_tr_b16 v[242:243], v8 offset:16904\n"
    "s_waitcnt vmcnt(24) lgkmcnt(0)\n"
    "s_barrier\n"
    "s_add_u32 m0, s60, 114688\n"
    "s_mov_b32 s73, 2621440\n"
    "buffer_load_dwordx4 v4, s[40:43], s73 offen lds\n"
    "s_add_u32 m0, s60, 118784\n"
    "s_mov_b32 s73, 2621568\n"
    "buffer_load_dwordx4 v4, s[40:43], s73 offen lds\n"
    "s_add_u32 m0, s60, 122880\n"
    "s_mov_b32 s73, 2621440\n"
    "buffer_load_dwordx4 v4, s[44:47], s73 offen lds\n"
    "s_add_u32 m0, s60, 126976\n"
    "s_mov_b32 s73, 2621568\n"
    "buffer_load_dwordx4 v4, s[44:47], s73 offen lds\n"
    "s_add_u32 m0, s60, 131072\n"
    "s_mov_b32 s73, 2621440\n"
    "buffer_load_dwordx4 v4, s[48:51], s73 offen lds\n"
    "s_add_u32 m0, s60, 135168\n"
    "s_mov_b32 s73, 2621568\n"
    "buffer_load_dwordx4 v4, s[48:51], s73 offen lds\n"
    "v_add_u32 v8, 139264, v6\n"
    "ds_read_b64_tr_b16 a[36:37], v8 offset:0\n"
    "ds_read_b64_tr_b16 a[38:39], v8 offset:512\n"
    "ds_read_b64_tr_b16 a[44:45], v8 offset:8\n"
    "ds_read_b64_tr_b16 a[46:47], v8 offset:520\n"
    "ds_read_b64_tr_b16 a[180:181], v8 offset:8192\n"
    "ds_read_b64_tr_b16 a[182:183], v8 offset:8704\n"
    "ds_read_b64_tr_b16 a[188:189], v8 offset:8200\n"
    "ds_read_b64_tr_b16 a[190:191], v8 offset:8712\n"
    "ds_read_b64_tr_b16 v[132:133], v8 offset:16384\n"
    "ds_read_b64_tr_b16 v[134:135], v8 offset:16896\n"
    "ds_read_b64_tr_b16 v[244:245], v8 offset:16392\n"
    "ds_read_b64_tr_b16 v[246:247], v8 offset:16904\n"
    "s_waitcnt vmcnt(24) lgkmcnt(0)\n"
    "s_barrier\n"
    "s_add_u32 m0, s60, 139264\n"
    "s_mov_b32 s73, 2883584\n"
    "buffer_load_dwordx4 v4, s[40:43], s73 offen lds\n"
    "s_add_u32 m0, s60, 143360\n"
    "s_mov_b32 s73, 2883712\n"
    "buffer_load_dwordx4 v4, s[40:43], s73 offen lds\n"
    "s_add_u32 m0, s60, 147456\n"
    "s_mov_b32 s73, 2883584\n"
    "buffer_load_dwordx4 v4, s[44:47], s73 offen lds\n"
    "s_add_u32 m0, s60, 151552\n"
    "s_mov_b32 s73, 2883712\n"
    "buffer_load_dwordx4 v4, s[44:47], s73 offen lds\n"
    "s_add_u32 m0, s60, 155648\n"
    "s_mov_b32 s73, 2883584\n"
    "buffer_load_dwordx4 v4, s[48:51], s73 offen lds\n"
    "s_add_u32 m0, s60, 159744\n"
    "s_mov_b32 s73, 2883712\n"
    "buffer_load_dwordx4 v4, s[48:51], s73 offen lds\n"
    "v_add_u32 v8, 16384, v6\n"
    "ds_read_b64_tr_b16 a[48:49], v8 offset:0\n"
    "ds_read_b64_tr_b16 a[50:51], v8 offset:512\n"
    "ds_read_b64_tr_b16 a[56:57], v8 offset:8\n"
    "ds_read_b64_tr_b16 a[58:59], v8 offset:520\n"
    "ds_read_b64_tr_b16 a[192:193], v8 offset:8192\n"
    "ds_read_b64_tr_b16 a[194:195], v8 offset:8704\n"
    "ds_read_b64_tr_b16 a[200:201], v8 offset:8200\n"
    "ds_read_b64_tr_b16 a[202:203], v8 offset:8712\n"
    "ds_read_b64_tr_b16 v[136:137], v8 offset:16384\n"
    "ds_read_b64_tr_b16 v[138:139], v8 offset:16896\n"
    "ds_read_b64_tr_b16 v[248:249], v8 offset:16392\n"
    "ds_read_b64_tr_b16 v[250:251], v8 offset:16904\n"
    "s_waitcnt vmcnt(24) lgkmcnt(0)\n"
    "s_barrier\n"
    "s_add_u32 m0, s60, 16384\n"
    "s_mov_b32 s73, 3145728\n"
    "buffer_load_dwordx4 v4, s[40:43], s73 offen lds\n"
    "s_add_u32 m0, s60, 20480\n"
    "s_mov_b32 s73, 3145856\n"
    "buffer_load_dwordx4 v4, s[40:43], s73 offen lds\n"
    "s_add_u32 m0, s60, 24576\n"
    "s_mov_b32 s73, 3145728\n"
    "buffer_load_dwordx4 v4, s[44:47], s73 offen lds\n"
    "s_add_u32 m0, s60, 28672\n"
    "s_mov_b32 s73, 3145856\n"
    "buffer_load_dwordx4 v4, s[44:47], s73 offen lds\n"
    "s_add_u32 m0, s60, 32768\n"
    "s_mov_b32 s73, 3145728\n"
    "buffer_load_dwordx4 v4, s[48:51], s73 offen lds\n"
    "s_add_u32 m0, s60, 36864\n"
    "s_mov_b32 s73, 3145856\n"
    "buffer_load_dwordx4 v4, s[48:51], s73 offen lds\n"
    "v_add_u32 v8, 40960, v6\n"
    "ds_read_b64_tr_b16 a[52:53], v8 offset:0\n"
    "ds_read_b64_tr_b16 a[54:55], v8 offset:512\n"
    "ds_read_b64_tr_b16 a[60:61], v8 offset:8\n"
    "ds_read_b64_tr_b16 a[62:63], v8 offset:520\n"
    "ds_read_b64_tr_b16 a[196:197], v8 offset:8192\n"
    "ds_read_b64_tr_b16 a[198:199], v8 offset:8704\n"
    "ds_read_b64_tr_b16 a[204:205], v8 offset:8200\n"
    "ds_read_b64_tr_b16 a[206:207], v8 offset:8712\n"
    "ds_read_b64_tr_b16 v[140:141], v8 offset:16384\n"
    "ds_read_b64_tr_b16 v[142:143], v8 offset:16896\n"
    "ds_read_b64_tr_b16 v[252:253], v8 offset:16392\n"
    "ds_read_b64_tr_b16 v[254:255], v8 offset:16904\n"
    "s_waitcnt vmcnt(24) lgkmcnt(0)\n"
    "s_barrier\n"
    "s_add_u32 m0, s60, 40960\n"
    "s_mov_b32 s73, 3407872\n"
    "buffer_load_dwordx4 v4, s[40:43], s73 offen lds\n"
    "s_add_u32 m0, s60, 45056\n"
    "s_mov_b32 s73, 3408000\n"
    "buffer_load_dwordx4 v4, s[40:43], s73 offen lds\n"
    "s_add_u32 m0, s60, 49152\n"
    "s_mov_b32 s73, 3407872\n"
    "buffer_load_dwordx4 v4, s[44:47], s73 offen lds\n"
    "s_add_u32 m0, s60, 53248\n"
    "s_mov_b32 s73, 3408000\n"
    "buffer_load_dwordx4 v4, s[44:47], s73 offen lds\n"
    "s_add_u32 m0, s60, 57344\n"
    "s_mov_b32 s73, 3407872\n"
    "buffer_load_dwordx4 v4, s[48:51], s73 offen lds\n"
    "s_add_u32 m0, s60, 61440\n"
    "s_mov_b32 s73, 3408000\n"
    "buffer_load_dwordx4 v4, s[48:51], s73 offen lds\n"
    "v_add_u32 v8, 65536, v6\n"
    "ds_read_b64_tr_b16 a[64:65], v8 offset:0\n"
    "ds_read_b64_tr_b16 a[66:67], v8 offset:512\n"
    "ds_read_b64_tr_b16 a[72:73], v8 offset:8\n"
    "ds_read_b64_tr_b16 a[74:75], v8 offset:520\n"
    "ds_read_b64_tr_b16 a[208:209], v8 offset:8192\n"
    "ds_read_b64_tr_b16 a[210:211], v8 offset:8704\n"
    "ds_read_b64_tr_b16 a[216:217], v8 offset:8200\n"
    "ds_read_b64_tr_b16 a[218:219], v8 offset:8712\n"
    "ds_read_b64_tr_b16 v[144:145], v8 offset:16384\n"
    "ds_read_b64_tr_b16 v[146:147], v8 offset:16896\n"
    "ds_read_b64_tr_b16 v[208:209], v8 offset:16392\n"
    "ds_read_b64_tr_b16 v[210:211], v8 offset:16904\n"
    "s_waitcnt vmcnt(24) lgkmcnt(0)\n"
    "s_barrier\n"
    "s_add_u32 m0, s60, 65536\n"
    "s_mov_b32 s73, 3670016\n"
    "buffer_load_dwordx4 v4, s[40:43], s73 offen lds\n"
    "s_add_u32 m0, s60, 69632\n"
    "s_mov_b32 s73, 3670144\n"
    "buffer_load_dwordx4 v4, s[40:43], s73 offen lds\n"
    "s_add_u32 m0, s60, 73728\n"
    "s_mov_b32 s73, 3670016\n"
    "buffer_load_dwordx4 v4, s[44:47], s73 offen lds\n"
    "s_add_u32 m0, s60, 77824\n"
    "s_mov_b32 s73, 3670144\n"
    "buffer_load_dwordx4 v4, s[44:47], s73 offen lds\n"
    "s_add_u32 m0, s60, 81920\n"
    "s_mov_b32 s73, 3670016\n"
    "buffer_load_dwordx4 v4, s[48:51], s73 offen lds\n"
    "s_add_u32 m0, s60, 86016\n"
    "s_mov_b32 s73, 3670144\n"
    "buffer_load_dwordx4 v4, s[48:51], s73 offen lds\n"
    "v_add_u32 v8, 90112, v6\n"
    "ds_read_b64_tr_b16 a[68:69], v8 offset:0\n"
    "ds_read_b64_tr_b16 a[70:71], v8 offset:512\n"
    "ds_read_b64_tr_b16 a[76:77], v8 offset:8\n"
    "ds_read_b64_tr_b16 a[78:79], v8 offset:520\n"
    "ds_read_b64_tr_b16 a[212:213], v8 offset:8192\n"
    "ds_read_b64_tr_b16 a[214:215], v8 offset:8704\n"
    "ds_read_b64_tr_b16 a[220:221], v8 offset:8200\n"
    "ds_read_b64_tr_b16 a[222:223], v8 offset:8712\n"
    "ds_read_b64_tr_b16 v[148:149], v8 offset:16384\n"
    "ds_read_b64_tr_b16 v[150:151], v8 offset:16896\n"
    "ds_read_b64_tr_b16 v[212:213], v8 offset:16392\n"
    "ds_read_b64_tr_b16 v[214:215], v8 offset:16904\n"
    "s_waitcnt vmcnt(24) lgkmcnt(0)\n"
    "s_barrier\n"
    "s_add_u32 m0, s60, 90112\n"
    "s_mov_b32 s73, 3932160\n"
    "buffer_load_dwordx4 v4, s[40:43], s73 offen lds\n"
    "s_add_u32 m0, s60, 94208\n"
    "s_mov_b32 s73, 3932288\n"
    "buffer_load_dwordx4 v4, s[40:43], s73 offen lds\n"
    "s_add_u32 m0, s60, 98304\n"
    "s_mov_b32 s73, 3932160\n"
    "buffer_load_dwordx4 v4, s[44:47], s73 offen lds\n"
    "s_add_u32 m0, s60, 102400\n"
    "s_mov_b32 s73, 3932288\n"
    "buffer_load_dwordx4 v4, s[44:47], s73 offen lds\n"
    "s_add_u32 m0, s60, 106496\n"
    "s_mov_b32 s73, 3932160\n"
    "buffer_load_dwordx4 v4, s[48:51], s73 offen lds\n"
    "s_add_u32 m0, s60, 110592\n"
    "s_mov_b32 s73, 3932288\n"
    "buffer_load_dwordx4 v4, s[48:51], s73 offen lds\n"
    "v_add_u32 v8, 114688, v6\n"
    "ds_read_b64_tr_b16 a[80:81], v8 offset:0\n"
    "ds_read_b64_tr_b16 a[82:83], v8 offset:512\n"
    "ds_read_b64_tr_b16 a[88:89], v8 offset:8\n"
    "ds_read_b64_tr_b16 a[90:91], v8 offset:520\n"
    "ds_read_b64_tr_b16 a[224:225], v8 offset:8192\n"
    "ds_read_b64_tr_b16 a[226:227], v8 offset:8704\n"
    "ds_read_b64_tr_b16 a[232:233], v8 offset:8200\n"
    "ds_read_b64_tr_b16 a[234:235], v8 offset:8712\n"
    "ds_read_b64_tr_b16 v[152:153], v8 offset:16384\n"
    "ds_read_b64_tr_b16 v[154:155], v8 offset:16896\n"
    "ds_read_b64_tr_b16 v[216:217], v8 offset:16392\n"
    "ds_read_b64_tr_b16 v[218:219], v8 offset:16904\n"
    "s_waitcnt vmcnt(24) lgkmcnt(0)\n"
    "s_barrier\n"
    "s_add_u32 m0, s60, 114688\n"
    "s_mov_b32 s73, 4194304\n"
    "buffer_load_dwordx4 v4, s[40:43], s73 offen lds\n"
    "s_add_u32 m0, s60, 118784\n"
    "s_mov_b32 s73, 4194432\n"
    "buffer_load_dwordx4 v4, s[40:43], s73 offen lds\n"
    "s_add_u32 m0, s60, 122880\n"
    "s_mov_b32 s73, 4194304\n"
    "buffer_load_dwordx4 v4, s[44:47], s73 offen lds\n"
    "s_add_u32 m0, s60, 126976\n"
    "s_mov_b32 s73, 4194432\n"
    "buffer_load_dwordx4 v4, s[44:47], s73 offen lds\n"
    "s_add_u32 m0, s60, 131072\n"
    "s_mov_b32 s73, 4194304\n"
    "buffer_load_dwordx4 v4, s[48:51], s73 offen lds\n"
    "s_add_u32 m0, s60, 135168\n"
    "s_mov_b32 s73, 4194432\n"
    "buffer_load_dwordx4 v4, s[48:51], s73 offen lds\n"
    "v_add_u32 v8, 139264, v6\n"
    "ds_read_b64_tr_b16 a[84:85], v8 offset:0\n"
    "ds_read_b64_tr_b16 a[86:87], v8 offset:512\n"
    "ds_read_b64_tr_b16 a[92:93], v8 offset:8\n"
    "ds_read_b64_tr_b16 a[94:95], v8 offset:520\n"
    "ds_read_b64_tr_b16 a[228:229], v8 offset:8192\n"
    "ds_read_b64_tr_b16 a[230:231], v8 offset:8704\n"
    "ds_read_b64_tr_b16 a[236:237], v8 offset:8200\n"
    "ds_read_b64_tr_b16 a[238:239], v8 offset:8712\n"
    "ds_read_b64_tr_b16 v[156:157], v8 offset:16384\n"
    "ds_read_b64_tr_b16 v[158:159], v8 offset:16896\n"
    "ds_read_b64_tr_b16 v[220:221], v8 offset:16392\n"
    "ds_read_b64_tr_b16 v[222:223], v8 offset:16904\n"
    "s_waitcnt vmcnt(24) lgkmcnt(0)\n"
    "s_barrier\n"
    "s_add_u32 m0, s60, 139264\n"
    "s_mov_b32 s73, 4456448\n"
    "buffer_load_dwordx4 v4, s[40:43], s73 offen lds\n"
    "s_add_u32 m0, s60, 143360\n"
    "s_mov_b32 s73, 4456576\n"
    "buffer_load_dwordx4 v4, s[40:43], s73 offen lds\n"
    "s_add_u32 m0, s60, 147456\n"
    "s_mov_b32 s73, 4456448\n"
    "buffer_load_dwordx4 v4, s[44:47], s73 offen lds\n"
    "s_add_u32 m0, s60, 151552\n"
    "s_mov_b32 s73, 4456576\n"
    "buffer_load_dwordx4 v4, s[44:47], s73 offen lds\n"
    "s_add_u32 m0, s60, 155648\n"
    "s_mov_b32 s73, 4456448\n"
    "buffer_load_dwordx4 v4, s[48:51], s73 offen lds\n"
    "s_add_u32 m0, s60, 159744\n"
    "s_mov_b32 s73, 4456576\n"
    "buffer_load_dwordx4 v4, s[48:51], s73 offen lds\n"
    "v_add_u32 v8, 16384, v6\n"
    "ds_read_b64_tr_b16 a[96:97], v8 offset:0\n"
    "ds_read_b64_tr_b16 a[98:99], v8 offset:512\n"
    "ds_read_b64_tr_b16 a[104:105], v8 offset:8\n"
    "ds_read_b64_tr_b16 a[106:107], v8 offset:520\n"
    "ds_read_b64_tr_b16 a[240:241], v8 offset:8192\n"
    "ds_read_b64_tr_b16 a[242:243], v8 offset:8704\n"
    "ds_read_b64_tr_b16 a[248:249], v8 offset:8200\n"
    "ds_read_b64_tr_b16 a[250:251], v8 offset:8712\n"
    "ds_read_b64_tr_b16 v[160:161], v8 offset:16384\n"
    "ds_read_b64_tr_b16 v[162:163], v8 offset:16896\n"
    "ds_read_b64_tr_b16 v[168:169], v8 offset:16392\n"
    "ds_read_b64_tr_b16 v[170:171], v8 offset:16904\n"
    "s_waitcnt vmcnt(24) lgkmcnt(0)\n"
    "s_barrier\n"
    "v_add_u32 v8, 40960, v6\n"
    "ds_read_b64_tr_b16 a[100:101], v8 offset:0\n"
    "ds_read_b64_tr_b16 a[102:103], v8 offset:512\n"
    "ds_read_b64_tr_b16 a[108:109], v8 offset:8\n"
    "ds_read_b64_tr_b16 a[110:111], v8 offset:520\n"
    "ds_read_b64_tr_b16 a[244:245], v8 offset:8192\n"
    "ds_read_b64_tr_b16 a[246:247], v8 offset:8704\n"
    "ds_read_b64_tr_b16 a[252:253], v8 offset:8200\n"
    "ds_read_b64_tr_b16 a[254:255], v8 offset:8712\n"
    "ds_read_b64_tr_b16 v[164:165], v8 offset:16384\n"
    "ds_read_b64_tr_b16 v[166:167], v8 offset:16896\n"
    "ds_read_b64_tr_b16 v[172:173], v8 offset:16392\n"
    "ds_read_b64_tr_b16 v[174:175], v8 offset:16904\n"
    "s_waitcnt vmcnt(18) lgkmcnt(0)\n"
    "s_barrier\n"
    "s_add_u32 m0, s60, 16384\n"
    "s_mov_b32 s73, 74240\n"
    "buffer_load_dwordx4 v5, s[52:55], s73 offen lds\n"
    "s_add_u32 m0, s60, 20480\n"
    "s_mov_b32 s73, 74368\n"
    "buffer_load_dwordx4 v5, s[52:55], s73 offen lds\n"
    "s_add_u32 m0, s60, 24576\n"
    "s_mov_b32 s73, 74496\n"
    "buffer_load_dwordx4 v5, s[52:55], s73 offen lds\n"
    "s_add_u32 m0, s60, 28672\n"
    "s_mov_b32 s73, 74624\n"
    "buffer_load_dwordx4 v5, s[52:55], s73 offen lds\n"
    "s_add_u32 m0, s60, 32768\n"
    "s_mov_b32 s73, 111104\n"
    "buffer_load_dwordx4 v5, s[52:55], s73 offen lds\n"
    "s_add_u32 m0, s60, 36864\n"
    "s_mov_b32 s73, 111232\n"
    "buffer_load_dwordx4 v5, s[52:55], s73 offen lds\n"
    "s_add_u32 m0, s60, 40960\n"
    "s_mov_b32 s73, 111360\n"
    "buffer_load_dwordx4 v5, s[52:55], s73 offen lds\n"
    "s_add_u32 m0, s60, 45056\n"
    "s_mov_b32 s73, 111488\n"
    "buffer_load_dwordx4 v5, s[52:55], s73 offen lds\n"
    "v_add_u32 v8, 65536, v6\n"
    "ds_read_b64_tr_b16 a[112:113], v8 offset:0\n"
    "ds_read_b64_tr_b16 a[114:115], v8 offset:512\n"
    "ds_read_b64_tr_b16 a[120:121], v8 offset:8\n"
    "ds_read_b64_tr_b16 a[122:123], v8 offset:520\n"
    "ds_read_b64_tr_b16 v[80:81], v8 offset:8192\n"
    "ds_read_b64_tr_b16 v[82:83], v8 offset:8704\n"
    "ds_read_b64_tr_b16 v[88:89], v8 offset:8200\n"
    "ds_read_b64_tr_b16 v[90:91], v8 offset:8712\n"
    "ds_read_b64_tr_b16 v[176:177], v8 offset:16384\n"
    "ds_read_b64_tr_b16 v[178:179], v8 offset:16896\n"
    "ds_read_b64_tr_b16 v[184:185], v8 offset:16392\n"
    "ds_read_b64_tr_b16 v[186:187], v8 offset:16904\n"
    "s_waitcnt vmcnt(20) lgkmcnt(0)\n"
    "s_barrier\n"
    "s_add_u32 m0, s60, 49152\n"
    "s_mov_b32 s73, 73728\n"
    "buffer_load_dwordx4 v5, s[52:55], s73 offen lds\n"
    "s_add_u32 m0, s60, 53248\n"
    "s_mov_b32 s73, 73856\n"
    "buffer_load_dwordx4 v5, s[52:55], s73 offen lds\n"
    "s_add_u32 m0, s60, 57344\n"
    "s_mov_b32 s73, 73984\n"
    "buffer_load_dwordx4 v5, s[52:55], s73 offen lds\n"
    "s_add_u32 m0, s60, 61440\n"
    "s_mov_b32 s73, 74112\n"
    "buffer_load_dwordx4 v5, s[52:55], s73 offen lds\n"
    "s_add_u32 m0, s60, 65536\n"
    "s_mov_b32 s73, 110592\n"
    "buffer_load_dwordx4 v5, s[52:55], s73 offen lds\n"
    "s_add_u32 m0, s60, 69632\n"
    "s_mov_b32 s73, 110720\n"
    "buffer_load_dwordx4 v5, s[52:55], s73 offen lds\n"
    "s_add_u32 m0, s60, 73728\n"
    "s_mov_b32 s73, 110848\n"
    "buffer_load_dwordx4 v5, s[52:55], s73 offen lds\n"
    "s_add_u32 m0, s60, 77824\n"
    "s_mov_b32 s73, 110976\n"
    "buffer_load_dwordx4 v5, s[52:55], s73 offen lds\n"
    "v_add_u32 v8, 90112, v6\n"
    "ds_read_b64_tr_b16 a[116:117], v8 offset:0\n"
    "ds_read_b64_tr_b16 a[118:119], v8 offset:512\n"
    "ds_read_b64_tr_b16 a[124:125], v8 offset:8\n"
    "ds_read_b64_tr_b16 a[126:127], v8 offset:520\n"
    "ds_read_b64_tr_b16 v[84:85], v8 offset:8192\n"
    "ds_read_b64_tr_b16 v[86:87], v8 offset:8704\n"
    "ds_read_b64_tr_b16 v[92:93], v8 offset:8200\n"
    "ds_read_b64_tr_b16 v[94:95], v8 offset:8712\n"
    "ds_read_b64_tr_b16 v[180:181], v8 offset:16384\n"
    "ds_read_b64_tr_b16 v[182:183], v8 offset:16896\n"
    "ds_read_b64_tr_b16 v[188:189], v8 offset:16392\n"
    "ds_read_b64_tr_b16 v[190:191], v8 offset:16904\n"
    "s_waitcnt vmcnt(22) lgkmcnt(0)\n"
    "s_barrier\n"
    "s_add_u32 m0, s60, 81920\n"
    "s_mov_b32 s73, 0\n"
    "buffer_load_dwordx4 v5, s[52:55], s73 offen lds\n"
    "s_add_u32 m0, s60, 86016\n"
    "s_mov_b32 s73, 128\n"
    "buffer_load_dwordx4 v5, s[52:55], s73 offen lds\n"
    "s_add_u32 m0, s60, 90112\n"
    "s_mov_b32 s73, 256\n"
    "buffer_load_dwordx4 v5, s[52:55], s73 offen lds\n"
    "s_add_u32 m0, s60, 94208\n"
    "s_mov_b32 s73, 384\n"
    "buffer_load_dwordx4 v5, s[52:55], s73 offen lds\n"
    "s_add_u32 m0, s60, 98304\n"
    "s_mov_b32 s73, 36864\n"
    "buffer_load_dwordx4 v5, s[52:55], s73 offen lds\n"
    "s_add_u32 m0, s60, 102400\n"
    "s_mov_b32 s73, 36992\n"
    "buffer_load_dwordx4 v5, s[52:55], s73 offen lds\n"
    "s_add_u32 m0, s60, 106496\n"
    "s_mov_b32 s73, 37120\n"
    "buffer_load_dwordx4 v5, s[52:55], s73 offen lds\n"
    "s_add_u32 m0, s60, 110592\n"
    "s_mov_b32 s73, 37248\n"
    "buffer_load_dwordx4 v5, s[52:55], s73 offen lds\n"
    "v_add_u32 v8, 114688, v6\n"
    "ds_read_b64_tr_b16 a[128:129], v8 offset:0\n"
    "ds_read_b64_tr_b16 a[130:131], v8 offset:512\n"
    "ds_read_b64_tr_b16 a[136:137], v8 offset:8\n"
    "ds_read_b64_tr_b16 a[138:139], v8 offset:520\n"
    "ds_read_b64_tr_b16 v[96:97], v8 offset:8192\n"
    "ds_read_b64_tr_b16 v[98:99], v8 offset:8704\n"
    "ds_read_b64_tr_b16 v[104:105], v8 offset:8200\n"
    "ds_read_b64_tr_b16 v[106:107], v8 offset:8712\n"
    "ds_read_b64_tr_b16 v[192:193], v8 offset:16384\n"
    "ds_read_b64_tr_b16 v[194:195], v8 offset:16896\n"
    "ds_read_b64_tr_b16 v[200:201], v8 offset:16392\n"
    "ds_read_b64_tr_b16 v[202:203], v8 offset:16904\n"
    "s_waitcnt vmcnt(24) lgkmcnt(0)\n"
    "s_barrier\n"
    "v_add_u32 v8, 139264, v6\n"
    "ds_read_b64_tr_b16 a[132:133], v8 offset:0\n"
    "ds_read_b64_tr_b16 a[134:135], v8 offset:512\n"
    "ds_read_b64_tr_b16 a[140:141], v8 offset:8\n"
    "ds_read_b64_tr_b16 a[142:143], v8 offset:520\n"
    "ds_read_b64_tr_b16 v[100:101], v8 offset:8192\n"
    "ds_read_b64_tr_b16 v[102:103], v8 offset:8704\n"
    "ds_read_b64_tr_b16 v[108:109], v8 offset:8200\n"
    "ds_read_b64_tr_b16 v[110:111], v8 offset:8712\n"
    "ds_read_b64_tr_b16 v[196:197], v8 offset:16384\n"
    "ds_read_b64_tr_b16 v[198:199], v8 offset:16896\n"
    "ds_read_b64_tr_b16 v[204:205], v8 offset:16392\n"
    "ds_read_b64_tr_b16 v[206:207], v8 offset:16904\n"
    "s_waitcnt vmcnt(16) lgkmcnt(0)\n"
    "s_barrier\n"
    "s_add_u32 m0, s60, 114688\n"
    "s_mov_b32 s73, 512\n"
    "buffer_load_dwordx4 v5, s[52:55], s73 offen lds\n"
    "s_add_u32 m0, s60, 118784\n"
    "s_mov_b32 s73, 640\n"
    "buffer_load_dwordx4 v5, s[52:55], s73 offen lds\n"
    "s_add_u32 m0, s60, 122880\n"
    "s_mov_b32 s73, 768\n"
    "buffer_load_dwordx4 v5, s[52:55], s73 offen lds\n"
    "s_add_u32 m0, s60, 126976\n"
    "s_mov_b32 s73, 896\n"
    "buffer_load_dwordx4 v5, s[52:55], s73 offen lds\n"
    "s_add_u32 m0, s60, 131072\n"
    "s_mov_b32 s73, 37376\n"
    "buffer_load_dwordx4 v5, s[52:55], s73 offen lds\n"
    "s_add_u32 m0, s60, 135168\n"
    "s_mov_b32 s73, 37504\n"
    "buffer_load_dwordx4 v5, s[52:55], s73 offen lds\n"
    "s_add_u32 m0, s60, 139264\n"
    "s_mov_b32 s73, 37632\n"
    "buffer_load_dwordx4 v5, s[52:55], s73 offen lds\n"
    "s_add_u32 m0, s60, 143360\n"
    "s_mov_b32 s73, 37760\n"
    "buffer_load_dwordx4 v5, s[52:55], s73 offen lds\n"
    "s_add_u32 m0, s60, 147456\n"
    "s_mov_b32 s73, 1024\n"
    "buffer_load_dwordx4 v5, s[52:55], s73 offen lds\n"
    "s_add_u32 m0, s60, 151552\n"
    "s_mov_b32 s73, 37888\n"
    "buffer_load_dwordx4 v5, s[52:55], s73 offen lds\n"
    "s_add_u32 m0, s60, 155648\n"
    "s_mov_b32 s73, 74752\n"
    "buffer_load_dwordx4 v5, s[52:55], s73 offen lds\n"
    "s_add_u32 m0, s60, 159744\n"
    "s_mov_b32 s73, 111616\n"
    "buffer_load_dwordx4 v5, s[52:55], s73 offen lds\n"
    "buffer_load_dwordx2 v[12:13], off, s[36:39], 0 sc1\n"
    "v_add_u32 v8, 16384, v7\n"
    "v_xor_b32 v9, 32, v8\n"
    "v_xor_b32 v10, 320, v8\n"
    "v_xor_b32 v11, 32, v10\n"
    "ds_read_b64_tr_b16 v[16:17], v8 offset:0\n"
    "ds_read_b64_tr_b16 v[18:19], v8 offset:512\n"
    "ds_read_b64_tr_b16 v[20:21], v8 offset:16384\n"
    "ds_read_b64_tr_b16 v[22:23], v8 offset:16896\n"
    "ds_read_b64_tr_b16 v[24:25], v9 offset:0\n"
    "ds_read_b64_tr_b16 v[26:27], v9 offset:512\n"
    "ds_read_b64_tr_b16 v[28:29], v9 offset:16384\n"
    "ds_read_b64_tr_b16 v[30:31], v9 offset:16896\n"
    "s_waitcnt lgkmcnt(4)\n"
    "ds_read_b64_tr_b16 v[32:33], v10 offset:0\n"
    "ds_read_b64_tr_b16 v[34:35], v10 offset:512\n"
    "ds_read_b64_tr_b16 v[36:37], v10 offset:16384\n"
    "ds_read_b64_tr_b16 v[38:39], v10 offset:16896\n"
    "ds_read_b64_tr_b16 v[40:41], v11 offset:0\n"
    "ds_read_b64_tr_b16 v[42:43], v11 offset:512\n"
    "ds_read_b64_tr_b16 v[44:45], v11 offset:16384\n"
    "ds_read_b64_tr_b16 v[46:47], v11 offset:16896\n"
    "s_waitcnt vmcnt(21) lgkmcnt(0)\n"
    "s_barrier\n"
    "v_add_u32 v14, 0, v1\n"
    "ds_write_b128 v14, v[224:227] offset:0\n"
    "ds_write_b128 v14, v[228:231] offset:1024\n"
    "ds_write_b128 v14, v[232:235] offset:8192\n"
    "ds_write_b128 v14, v[236:239] offset:9216\n"
    "ds_write_b128 v14, v[240:243] offset:16384\n"
    "ds_write_b128 v14, v[244:247] offset:17408\n"
    "ds_write_b128 v14, v[248:251] offset:24576\n"
    "ds_write_b128 v14, v[252:255] offset:25600\n"
    "s_waitcnt lgkmcnt(4)\n"
    "v_add_u32 v8, 49152, v7\n"
    "v_xor_b32 v9, 32, v8\n"
    "v_xor_b32 v10, 320, v8\n"
    "v_xor_b32 v11, 32, v10\n"
    "ds_read_b64_tr_b16 v[48:49], v10 offset:0\n"
    "ds_read_b64_tr_b16 v[50:51], v10 offset:512\n"
    "ds_read_b64_tr_b16 v[52:53], v10 offset:16384\n"
    "ds_read_b64_tr_b16 v[54:55], v10 offset:16896\n"
    "ds_read_b64_tr_b16 v[56:57], v11 offset:0\n"
    "ds_read_b64_tr_b16 v[58:59], v11 offset:512\n"
    "ds_read_b64_tr_b16 v[60:61], v11 offset:16384\n"
    "ds_read_b64_tr_b16 v[62:63], v11 offset:16896\n"
    "s_waitcnt lgkmcnt(4)\n"
    "ds_read_b64_tr_b16 v[64:65], v8 offset:0\n"
    "ds_read_b64_tr_b16 v[66:67], v8 offset:512\n"
    "ds_read_b64_tr_b16 v[68:69], v8 offset:16384\n"
    "ds_read_b64_tr_b16 v[70:71], v8 offset:16896\n"
    "ds_read_b64_tr_b16 v[72:73], v9 offset:0\n"
    "ds_read_b64_tr_b16 v[74:75], v9 offset:512\n"
    "ds_read_b64_tr_b16 v[76:77], v9 offset:16384\n"
    "ds_read_b64_tr_b16 v[78:79], v9 offset:16896\n"
    "s_waitcnt vmcnt(13) lgkmcnt(0)\n"
    "s_barrier\n"
    "v_mov_b64 v[224:225], v[16:17]\n"
    "v_mov_b64 v[226:227], v[18:19]\n"
    "v_mov_b64 v[228:229], v[20:21]\n"
    "v_mov_b64 v[230:231], v[22:23]\n"
    "v_mov_b64 v[232:233], v[24:25]\n"
    "v_mov_b64 v[234:235], v[26:27]\n"
    "v_mov_b64 v[236:237], v[28:29]\n"
    "v_mov_b64 v[238:239], v[30:31]\n"
    "v_mov_b64 v[240:241], v[32:33]\n"
    "v_mov_b64 v[242:243], v[34:35]\n"
    "v_mov_b64 v[244:245], v[36:37]\n"
    "v_mov_b64 v[246:247], v[38:39]\n"
    "v_mov_b64 v[248:249], v[40:41]\n"
    "v_mov_b64 v[250:251], v[42:43]\n"
    "v_mov_b64 v[252:253], v[44:45]\n"
    "v_mov_b64 v[254:255], v[46:47]\n"
    "v_add_u32 v14, 32768, v1\n"
    "ds_write_b128 v14, v[208:211] offset:0\n"
    "ds_write_b128 v14, v[212:215] offset:1024\n"
    "ds_write_b128 v14, v[216:219] offset:8192\n"
    "ds_write_b128 v14, v[220:223] offset:9216\n"
    "ds_write_b128 v14, v[64:67] offset:16384\n"
    "ds_write_b128 v14, v[68:71] offset:17408\n"
    "ds_write_b128 v14, v[72:75] offset:24576\n"
    "ds_write_b128 v14, v[76:79] offset:25600\n"
    "s_waitcnt lgkmcnt(4)\n"
    "v_add_u32 v8, 81920, v7\n"
    "v_xor_b32 v9, 32, v8\n"
    "v_xor_b32 v10, 320, v8\n"
    "v_xor_b32 v11, 32, v10\n"
    "ds_read_b64_tr_b16 v[16:17], v8 offset:0\n"
    "ds_read_b64_tr_b16 v[18:19], v8 offset:512\n"
    "ds_read_b64_tr_b16 v[20:21], v8 offset:16384\n"
    "ds_read_b64_tr_b16 v[22:23], v8 offset:16896\n"
    "ds_read_b64_tr_b16 v[24:25], v9 offset:0\n"
    "ds_read_b64_tr_b16 v[26:27], v9 offset:512\n"
    "ds_read_b64_tr_b16 v[28:29], v9 offset:16384\n"
    "ds_read_b64_tr_b16 v[30:31], v9 offset:16896\n"
    "s_waitcnt lgkmcnt(4)\n"
    "ds_read_b64_tr_b16 v[32:33], v10 offset:0\n"
    "ds_read_b64_tr_b16 v[34:35], v10 offset:512\n"
    "ds_read_b64_tr_b16 v[36:37], v10 offset:16384\n"
    "ds_read_b64_tr_b16 v[38:39], v10 offset:16896\n"
    "ds_read_b64_tr_b16 v[40:41], v11 offset:0\n"
    "ds_read_b64_tr_b16 v[42:43], v11 offset:512\n"
    "ds_read_b64_tr_b16 v[44:45], v11 offset:16384\n"
    "ds_read_b64_tr_b16 v[46:47], v11 offset:16896\n"
    "s_waitcnt vmcnt(5) lgkmcnt(0)\n"
    "s_barrier\n"
    "v_mov_b64 v[208:209], v[48:49]\n"
    "v_mov_b64 v[210:211], v[50:51]\n"
    "v_mov_b64 v[212:213], v[52:53]\n"
    "v_mov_b64 v[214:215], v[54:55]\n"
    "v_mov_b64 v[216:217], v[56:57]\n"
    "v_mov_b64 v[218:219], v[58:59]\n"
    "v_mov_b64 v[220:221], v[60:61]\n"
    "v_mov_b64 v[222:223], v[62:63]\n"
    "v_add_u32 v14, 65536, v1\n"
    "ds_write_b128 v14, v[16:19] offset:0\n"
    "ds_write_b128 v14, v[20:23] offset:1024\n"
    "ds_write_b128 v14, v[24:27] offset:8192\n"
    "ds_write_b128 v14, v[28:31] offset:9216\n"
    "ds_write_b128 v14, v[32:35] offset:16384\n"
    "ds_write_b128 v14, v[36:39] offset:17408\n"
    "ds_write_b128 v14, v[40:43] offset:24576\n"
    "ds_write_b128 v14, v[44:47] offset:25600\n"
    "s_waitcnt lgkmcnt(4)\n"
    "v_add_u32 v8, 114688, v7\n"
    "v_xor_b32 v9, 32, v8\n"
    "v_xor_b32 v10, 320, v8\n"
    "v_xor_b32 v11, 32, v10\n"
    "ds_read_b64_tr_b16 v[48:49], v8 offset:0\n"
    "ds_read_b64_tr_b16 v[50:51], v8 offset:512\n"
    "ds_read_b64_tr_b16 v[52:53], v8 offset:16384\n"
    "ds_read_b64_tr_b16 v[54:55], v8 offset:16896\n"
    "ds_read_b64_tr_b16 v[56:57], v9 offset:0\n"
    "ds_read_b64_tr_b16 v[58:59], v9 offset:512\n"
    "ds_read_b64_tr_b16 v[60:61], v9 offset:16384\n"
    "ds_read_b64_tr_b16 v[62:63], v9 offset:16896\n"
    "s_waitcnt lgkmcnt(4)\n"
    "ds_read_b64_tr_b16 v[64:65], v10 offset:0\n"
    "ds_read_b64_tr_b16 v[66:67], v10 offset:512\n"
    "ds_read_b64_tr_b16 v[68:69], v10 offset:16384\n"
    "ds_read_b64_tr_b16 v[70:71], v10 offset:16896\n"
    "ds_read_b64_tr_b16 v[72:73], v11 offset:0\n"
    "ds_read_b64_tr_b16 v[74:75], v11 offset:512\n"
    "ds_read_b64_tr_b16 v[76:77], v11 offset:16384\n"
    "ds_read_b64_tr_b16 v[78:79], v11 offset:16896\n"
    "s_waitcnt vmcnt(0) lgkmcnt(0)\n"
    "s_barrier\n"
    ".Lrec_par_%=:\n"
    "v_readfirstlane_b32 s73, v12\n"
    "v_readfirstlane_b32 s74, v13\n"
    "s_xor_b32 s73, s73, s30\n"
    "s_xor_b32 s74, s74, s31\n"
    "s_cselect_b32 s74, 2, 0\n"
    "s_or_b32 s74, s74, s73\n"
    "s_cmp_lt_u32 s74, 2\n"
    "s_cbranch_scc1 .Lrec_par1_%=\n"
    "s_sleep 4\n"
    "buffer_load_dwordx2 v[12:13], off, s[36:39], 0 sc1\n"
    "s_waitcnt vmcnt(0)\n"
    "s_sub_u32 s69, s69, 1\n"
    "s_cbranch_scc0 .Lrec_par_%=\n"
    "s_mov_b32 s69, 0\n"
    ".Lrec_par1_%=:\n"
    "s_and_b32 s73, s73, 1\n"
    "s_lshl_b32 s73, s73, 13\n"
    "s_add_u32 s36, s36, s73\n"
    "s_addc_u32 s37, s37, 0\n"
    "v_lshlrev_b32 v44, 3, v0\n"
    "v_mov_b32 v45, 0x80000000\n"
    "v_cmp_gt_u32 vcc, 192, v0\n"
    "s_nop 1\n"
    "v_cndmask_b32 v44, v45, v44, vcc\n"
    "v_mov_b32 v46, -1\n"
    "buffer_load_dword v45, v44, s[36:39], s62 offen sc1\n"
    "s_mov_b64 exec, 1\n"
    "buffer_atomic_smax v46, off, s[36:39], s59 sc0 sc1\n"
    "s_mov_b64 exec, -1\n"
    "v_add_u32 v14, 98304, v1\n"
    "ds_write_b128 v14, v[48:51] offset:0\n"
    "ds_write_b128 v14, v[52:55] offset:1024\n"
    "ds_write_b128 v14, v[56:59] offset:8192\n"
    "ds_write_b128 v14, v[60:63] offset:9216\n"
    "ds_write_b128 v14, v[64:67] offset:16384\n"
    "ds_write_b128 v14, v[68:71] offset:17408\n"
    "ds_write_b128 v14, v[72:75] offset:24576\n"
    "ds_write_b128 v14, v[76:79] offset:25600\n"
    "s_waitcnt lgkmcnt(4)\n"
    "v_add_u32 v8, 147456, v3\n"
    "v_xor_b32 v9, 32, v8\n"
    "v_xor_b32 v10, 320, v8\n"
    "v_xor_b32 v11, 32, v10\n"
    "ds_read_b64_tr_b16 v[16:17], v8 offset:0\n"
    "ds_read_b64_tr_b16 v[18:19], v9 offset:1024\n"
    "ds_read_b64_tr_b16 v[20:21], v10 offset:0\n"
    "ds_read_b64_tr_b16 v[22:23], v11 offset:1024\n"
    "ds_read_b64_tr_b16 v[24:25], v8 offset:8192\n"
    "ds_read_b64_tr_b16 v[26:27], v9 offset:9216\n"
    "ds_read_b64_tr_b16 v[28:29], v10 offset:8192\n"
    "ds_read_b64_tr_b16 v[30:31], v11 offset:9216\n"
    "s_waitcnt lgkmcnt(4)\n"
    "s_waitcnt lgkmcnt(0)\n"
    "s_barrier\n"
    "v_add_u32 v14, 131072, v1\n"
    "ds_write_b128 v14, v[16:19] offset:0\n"
    "ds_write_b128 v14, v[20:23] offset:1024\n"
    "ds_write_b128 v14, v[24:27] offset:8192\n"
    "ds_write_b128 v14, v[28:31] offset:9216\n"
    "s_memtime s[32:33]\n"
    "s_memrealtime s[28:29]\n"
    "s_mov_b32 s84, 0x3e0293ee\n"
    "s_waitcnt vmcnt(0)\n"
    ".Lrec_init_%=:\n"
    "v_cmp_gt_i32 vcc, 0, v45\n"
    "s_cbranch_vccz .Lrec_init1_%=\n"
    "s_sleep 2\n"
    "buffer_load_dword v45, v44, s[36:39], s62 offen sc1\n"
    "s_waitcnt vmcnt(0)\n"
    "s_sub_u32 s69, s69, 1\n"
    "s_cbranch_scc0 .Lrec_init_%=\n"
    "s_mov_b32 s69, 0\n"
    ".Lrec_init1_%=:\n"
    "v_mov_b32 v3, v46\n"
    "s_mov_b32 s40, s76\n"
    "s_mov_b32 s41, s77\n"
    "s_mov_b32 s42, s78\n"
    "s_mov_b32 s43, s79\n"
    "s_mov_b32 s44, s80\n"
    "s_mov_b32 s45, s81\n"
    "s_mov_b32 s50, 0\n"
    "s_waitcnt lgkmcnt(0)\n"
    "s_mov_b32 s46, s34\n"
    "s_lshl_b32 s47, s92, 2\n"
    "s_mov_b32 s33, s32\n"
    "s_lshl_b32 s28, s28, 2\n"
    "s_mov_b32 s52, 900\n"
    "s_and_b32 s35, s40, 1\n"
    "s_and_b32 s40, s40, -2\n"
    "s_mul_i32 s35, s35, 0\n"
    "s_add_u32 s34, s35, 70\n"
    "s_lshl_b32 s35, s35, 2\n"
    "s_mov_b32 s53, 0\n"
    "s_mov_b32 s54, 0x7fffffff\n"
    "s_mov_b32 s55, 0\n"
    "s_mov_b32 s68, -1\n"
    "s_add_u32 s68, s68, 1\n"
    "s_and_b32 s70, s68, 3\n"
    "s_lshl_b32 s70, s70, 17\n"
    "s_add_u32 s70, s70, s66\n"
    "s_add_u32 s71, s70, 4096\n"
    "s_add_u32 s72, s70, 16384\n"
    "s_sub_u32 s46, s33, s46\n"
    "s_sub_u32 s47, s28, s47\n"
    "v_cvt_f32_u32 v40, s46\n"
    "v_cvt_f32_u32 v41, s47\n"
    "v_rcp_f32 v41, v41\n"
    "s_mov_b32 s46, s33\n"
    "s_mov_b32 s47, s28\n"
    "v_mul_f32 v40, v40, v41\n"
    "v_mov_b32 v42, 0x40c80000\n"
    "v_med3_f32 v40, v40, 4.0, v42\n"
    "v_rcp_f32 v41, v40\n"
    "v_mul_f32 v40, 0x43800000, v40\n"
    "v_mul_f32 v41, 0x44b54000, v41\n"
    "v_cvt_u32_f32 v40, v40\n"
    "v_cvt_u32_f32 v41, v41\n"
    "v_readfirstlane_b32 s48, v40\n"
    "v_readfirstlane_b32 s49, v41\n"
    "v_mov_b64 v[40:41], 0\n"
    "v_mov_b64 v[42:43], 0\n"
    "v_mov_b64 v[44:45], 0\n"
    "v_mov_b64 v[46:47], 0\n"
    "v_mov_b64 v[48:49], 0\n"
    "v_mov_b64 v[50:51], 0\n"
    "v_mov_b64 v[52:53], 0\n"
    "v_mov_b64 v[54:55], 0\n"
    "v_mov_b64 v[56:57], 0\n"
    "v_mov_b64 v[58:59], 0\n"
    "v_mov_b64 v[60:61], 0\n"
    "v_mov_b64 v[62:63], 0\n"
    "s_waitcnt lgkmcnt(0)\n"
    "s_barrier\n"
    "s_branch .Lrec_entry_%=\n"
    ".Lrec_LOOPB_%=:\n"
    ".Lrec_top_%=:\n"
    "buffer_load_dwordx4 v[4:7], v0, s[36:39], s70 offen sc1\n"
    "s_mov_b32 s82, s50\n"
    "s_add_u32 s50, s50, 589824\n"
    "s_cmp_eq_u32 s50, 1769472\n"
    "s_cselect_b32 s50, 0, s50\n"
    "s_add_u32 s80, s68, 2\n"
    "s_and_b32 s80, s80, 3\n"
    "s_lshl_b32 s80, s80, 17\n"
    "buffer_load_dwordx4 v[8:11], v0, s[36:39], s70 offen offset:1024 sc1\n"
    "s_and_b32 s80, s80, s45\n"
    "s_and_b32 s91, s50, s44\n"
    "s_add_u32 s80, s80, s91\n"
    "s_add_u32 s80, s80, s43\n"
    "s_add_u32 s89, s42, s50\n"
    "s_lshr_b32 s90, s68, 2\n"
    "s_lshl_b32 s90, s90, 14\n"
    "buffer_load_dwordx4 v[12:15], v0, s[36:39], s70 offen offset:2048 sc1\n"
    "s_and_b32 s91, s68, 3\n"
    "s_lshl_b32 s91, s91, 11\n"
    "s_add_u32 s90, s90, s91\n"
    "s_add_u32 s90, s90, s40\n"
    "s_add_u32 s51, s90, s41\n"
    "s_and_b32 s76, s68, 7\n"
    "s_mul_i32 s76, s76, 4194304\n"
    "buffer_load_dwordx4 v[16:19], v0, s[36:39], s70 offen offset:3072 sc1\n"
    "s_add_u32 s75, s76, s56\n"
    "s_add_u32 s83, s75, 16384\n"
    "s_add_u32 s76, s76, s57\n"
    "s_add_u32 s77, s63, s82\n"
    "s_add_u32 s86, s77, 262144\n"
    "s_add_u32 s87, s67, s82\n"
    "s_add_u32 s91, s68, 1\n"
    "buffer_load_dwordx4 v[20:23], v0, s[36:39], s71 offen sc1\n"
    "s_and_b32 s79, s91, 3\n"
    "s_lshl_b32 s79, s79, 17\n"
    "s_add_u32 s79, s79, s65\n"
    "s_add_u32 s78, s64, s82\n"
    "s_add_u32 s88, s78, 4096\n"
    "s_nop 1\n"
    "buffer_load_dwordx4 v[24:27], v0, s[36:39], s71 offen offset:1024 sc1\n"
    "s_nop 6\n"
    "buffer_load_dwordx4 v[28:31], v0, s[36:39], s71 offen offset:2048 sc1\n"
    "s_nop 6\n"
    "buffer_load_dwordx4 v[32:35], v0, s[36:39], s71 offen offset:3072 sc1\n"
    "s_waitcnt vmcnt(7)\n"
    ".Lrec_start_%=:\n"
    "v_smfmac_f32_16x16x64_bf16 v[40:43], v[4:7], a[0:7], v2\n"
    "v_max3_u32 v3, v4, v5, v6\n"
    "v_smfmac_f32_16x16x64_bf16 v[44:47], v[4:7], a[8:15], v2\n"
    "v_max_u32 v3, v3, v7\n"
    "v_smfmac_f32_16x16x64_bf16 v[48:51], v[4:7], a[144:151], v2\n"
    "buffer_load_dwordx4 v[36:39], v0, s[36:39], s72 offen sc1\n"
    "v_cmp_eq_u32 vcc, -1, v3\n"
    "v_smfmac_f32_16x16x64_bf16 v[52:55], v[4:7], a[152:159], v2\n"
    "v_smfmac_f32_16x16x64_bf16 v[56:59], v[4:7], v[112:119], v2\n"
    "s_cbranch_vccnz .Lrec_retry0_%=\n"
    ".Lrec_back0_%=:\n"
    "s_waitcnt vmcnt(7)\n"
    "s_waitcnt lgkmcnt(0)\n"
    "v_smfmac_f32_16x16x64_bf16 v[60:63], v[4:7], v[72:79], v2\n"
    "ds_read_b128 v[72:75], v1 offset:8192\n"
    "v_smfmac_f32_16x16x64_bf16 v[40:43], v[8:11], a[16:23], v2\n"
    "ds_read_b128 v[76:79], v1 offset:9216\n"
    "v_max3_u32 v3, v3, v8, v9\n"
    "v_smfmac_f32_16x16x64_bf16 v[44:47], v[8:11], a[24:31], v2\n"
    "buffer_load_dwordx4 v[68:71], v0, s[36:39], s75 offen offset:1024 sc1\n"
    "v_max3_u32 v3, v3, v10, v11\n"
    "v_smfmac_f32_16x16x64_bf16 v[48:51], v[8:11], a[160:167], v2\n"
    "v_smfmac_f32_16x16x64_bf16 v[52:55], v[8:11], a[168:175], v2\n"
    "buffer_load_dwordx4 v[64:67], v0, s[36:39], s75 offen sc1\n"
    "v_smfmac_f32_16x16x64_bf16 v[56:59], v[8:11], v[120:127], v2\n"
    "s_waitcnt vmcnt(8)\n"
    "s_waitcnt lgkmcnt(0)\n"
    "v_smfmac_f32_16x16x64_bf16 v[60:63], v[8:11], v[72:79], v2\n"
    "ds_read_b128 v[72:75], v1 offset:16384\n"
    "v_smfmac_f32_16x16x64_bf16 v[40:43], v[12:15], a[32:39], v2\n"
    "ds_read_b128 v[76:79], v1 offset:17408\n"
    "v_max3_u32 v3, v3, v12, v13\n"
    "v_smfmac_f32_16x16x64_bf16 v[44:47], v[12:15], a[40:47], v2\n"
    "v_max3_u32 v3, v3, v14, v15\n"
    "v_smfmac_f32_16x16x64_bf16 v[48:51], v[12:15], a[176:183], v2\n"
    "buffer_load_dwordx4 v[4:7], v0, s[36:39], s83 offen sc1\n"
    "v_smfmac_f32_16x16x64_bf16 v[52:55], v[12:15], a[184:191], v2\n"
    "v_smfmac_f32_16x16x64_bf16 v[56:59], v[12:15], v[128:135], v2\n"
    "s_waitcnt vmcnt(8)\n"
    "s_waitcnt lgkmcnt(0)\n"
    "v_max3_u32 v3, v3, v16, v17\n"
    "v_smfmac_f32_16x16x64_bf16 v[60:63], v[12:15], v[72:79], v2\n"
    "ds_read_b128 v[72:75], v1 offset:24576\n"
    "v_max3_u32 v3, v3, v18, v19\n"
    "v_smfmac_f32_16x16x64_bf16 v[40:43], v[16:19], a[48:55], v2\n"
    "ds_read_b128 v[76:79], v1 offset:25600\n"
    "v_cmp_eq_u32 vcc, -1, v3\n"
    "v_smfmac_f32_16x16x64_bf16 v[44:47], v[16:19], a[56:63], v2\n"
    "buffer_load_dwordx4 v[8:11], v0, s[36:39], s83 offen offset:1024 sc1\n"
    "v_smfmac_f32_16x16x64_bf16 v[48:51], v[16:19], a[192:199], v2\n"
    "s_cbranch_vccnz .Lrec_retry1_%=\n"
    ".Lrec_back1_%=:\n"
    "v_smfmac_f32_16x16x64_bf16 v[52:55], v[16:19], a[200:207], v2\n"
    "s_waitcnt vmcnt(8)\n"
    "s_waitcnt lgkmcnt(0)\n"
    "v_max3_u32 v3, v3, v20, v21\n"
    "v_smfmac_f32_16x16x64_bf16 v[60:63], v[16:19], v[72:79], v2\n"
    "ds_read_b128 v[72:75], v1 offset:32768\n"
    "v_max3_u32 v3, v3, v22, v23\n"
    "v_smfmac_f32_16x16x64_bf16 v[40:43], v[20:23], a[64:71], v2\n"
    "ds_read_b128 v[76:79], v1 offset:33792\n"
    "v_mov_b64 v[12:13], -1\n"
    "v_smfmac_f32_16x16x64_bf16 v[44:47], v[20:23], a[72:79], v2\n"
    "v_mov_b64 v[14:15], -1\n"
    "v_smfmac_f32_16x16x64_bf16 v[48:51], v[20:23], a[208:215], v2\n"
    "v_smfmac_f32_16x16x64_bf16 v[52:55], v[20:23], a[216:223], v2\n"
    "s_waitcnt vmcnt(7)\n"
    "s_waitcnt lgkmcnt(0)\n"
    "buffer_store_dwordx4 v[12:15], v0, s[36:39], s89 offen sc1\n"
    "v_max3_u32 v3, v3, v24, v25\n"
    "v_smfmac_f32_16x16x64_bf16 v[60:63], v[20:23], v[72:79], v2\n"
    "ds_read_b128 v[72:75], v1 offset:40960\n"
    "v_max3_u32 v3, v3, v26, v27\n"
    "v_smfmac_f32_16x16x64_bf16 v[40:43], v[24:27], a[80:87], v2\n"
    "ds_read_b128 v[76:79], v1 offset:41984\n"
    "v_add_u32 v1, 49152, v1\n"
    "v_smfmac_f32_16x16x64_bf16 v[44:47], v[24:27], a[88:95], v2\n"
    "s_waitcnt vmcnt(7)\n"
    "buffer_store_dwordx4 v[12:15], v0, s[36:39], s89 offen offset:1024 sc1\n"
    "v_max3_u32 v3, v3, v28, v29\n"
    "v_smfmac_f32_16x16x64_bf16 v[48:51], v[24:27], a[224:231], v2\n"
    "s_waitcnt vmcnt(7)\n"
    "v_max3_u32 v3, v3, v30, v31\n"
    "v_smfmac_f32_16x16x64_bf16 v[52:55], v[24:27], a[232:239], v2\n"
    "s_waitcnt lgkmcnt(0)\n"
    "buffer_store_dwordx4 v[12:15], v0, s[36:39], s80 offen sc1\n"
    "v_max3_u32 v3, v3, v32, v33\n"
    "v_smfmac_f32_16x16x64_bf16 v[60:63], v[24:27], v[72:79], v2\n"
    "ds_read_b128 v[72:75], v0 offset:13312\n"
    "v_max3_u32 v3, v3, v34, v35\n"
    "v_smfmac_f32_16x16x64_bf16 v[40:43], v[28:31], a[96:103], v2\n"
    "s_waitcnt vmcnt(7)\n"
    "v_max3_u32 v3, v3, v36, v37\n"
    "v_smfmac_f32_16x16x64_bf16 v[44:47], v[28:31], a[104:111], v2\n"
    "ds_read_b128 v[12:15], v0 offset:12288\n"
    "v_max3_u32 v3, v3, v38, v39\n"
    "v_smfmac_f32_16x16x64_bf16 v[40:43], v[32:35], a[112:119], v2\n"
    "s_lshr_b32 s73, s60, 1\n"
    "v_cmp_eq_u32 vcc, -1, v3\n"
    "v_smfmac_f32_16x16x64_bf16 v[44:47], v[32:35], a[120:127], v2\n"
    "v_add_u32 v79, s60, v0\n"
    "v_smfmac_f32_16x16x64_bf16 v[48:51], v[28:31], a[240:247], v2\n"
    "s_cbranch_vccnz .Lrec_retry2_%=\n"
    ".Lrec_back2_%=:\n"
    "v_xor_b32 v76, 1024, v79\n"
    "v_smfmac_f32_16x16x64_bf16 v[40:43], v[36:39], a[128:135], v2\n"
    "s_waitcnt lgkmcnt(0)\n"
    "s_memrealtime s[28:29]\n"
    "v_xor_b32 v77, 2048, v79\n"
    "v_smfmac_f32_16x16x64_bf16 v[44:47], v[36:39], a[136:143], v2\n"
    "s_memrealtime s[30:31]\n"
    "v_xor_b32 v78, 3072, v79\n"
    "v_smfmac_f32_16x16x64_bf16 v[52:55], v[28:31], a[248:255], v2\n"
    "s_memtime s[32:33]\n"
    "v_cvt_pk_bf16_f32 v40, v40, v41\n"
    "v_smfmac_f32_16x16x64_bf16 v[48:51], v[32:35], v[80:87], v2\n"
    "s_memrealtime s[96:97]\n"
    "v_cvt_pk_bf16_f32 v41, v42, v43\n"
    "v_smfmac_f32_16x16x64_bf16 v[52:55], v[32:35], v[88:95], v2\n"
    "s_memrealtime s[98:99]\n"
    "v_cvt_pk_bf16_f32 v42, v44, v45\n"
    "v_smfmac_f32_16x16x64_bf16 v[48:51], v[36:39], v[96:103], v2\n"
    "s_waitcnt vmcnt(5)\n"
    "v_cvt_pk_bf16_f32 v43, v46, v47\n"
    "v_smfmac_f32_16x16x64_bf16 v[52:55], v[36:39], v[104:111], v2\n"
    "v_lshrrev_b32 v3, 1, v79\n"
    "v_mfma_f32_16x16x32_bf16 v[44:47], v[40:43], v[12:15], 0\n"
    "s_nop 0\n"
    "v_cvt_pk_bf16_f32 v48, v48, v49\n"
    "v_cvt_pk_bf16_f32 v49, v50, v51\n"
    "s_waitcnt lgkmcnt(0)\n"
    "v_cvt_pk_bf16_f32 v50, v52, v53\n"
    "v_cvt_pk_bf16_f32 v51, v54, v55\n"
    "s_add_u32 s28, s28, s30\n"
    "s_add_u32 s96, s96, s98\n"
    "v_mfma_f32_16x16x32_bf16 v[52:55], v[48:51], v[12:15], 0\n"
    "s_add_u32 s28, s28, s96\n"
    "v_cvt_pk_bf16_f32 v40, v44, v45\n"
    "v_cvt_pk_bf16_f32 v41, v46, v47\n"
    "v_mov_b32 v75, s28\n"
    "v_mov_b32_dpp v42, v40 row_ror:8 row_mask:0xf bank_mask:0xf\n"
    "v_mov_b32_dpp v43, v41 row_ror:8 row_mask:0xf bank_mask:0xf\n"
    "v_add_u32 v75, s49, v75\n"
    "buffer_store_dword v75, off, s[36:39], s51 sc1\n"
    "v_mfma_f32_16x16x32_bf16 v[44:47], v[68:71], v[40:43], 0\n"
    "v_cvt_pk_bf16_f32 v48, v52, v53\n"
    "v_cvt_pk_bf16_f32 v49, v54, v55\n"
    "v_mfma_f32_16x16x32_bf16 v[68:71], v[64:67], v[40:43], 0\n"
    "v_mov_b32_dpp v50, v48 row_ror:8 row_mask:0xf bank_mask:0xf\n"
    "v_mov_b32_dpp v51, v49 row_ror:8 row_mask:0xf bank_mask:0xf\n"
    "s_nop 1\n"
    "v_mfma_f32_16x16x32_bf16 v[52:55], v[48:51], v[40:43], 0\n"
    "s_nop 2\n"
    "v_mov_b32_dpp v68, v44 row_ror:8 row_mask:0xf bank_mask:0xc\n"
    "v_mov_b32_dpp v69, v45 row_ror:8 row_mask:0xf bank_mask:0xc\n"
    "v_mov_b32_dpp v70, v46 row_ror:8 row_mask:0xf bank_mask:0xc\n"
    "v_mov_b32_dpp v71, v47 row_ror:8 row_mask:0xf bank_mask:0xc\n"
    "ds_write_b128 v79, v[68:71] offset:0\n"
    "v_mov_b32_dpp v52, v54 row_ror:8 row_mask:0xf bank_mask:0xc\n"
    "v_mov_b32_dpp v53, v55 row_ror:8 row_mask:0xf bank_mask:0xc\n"
    "ds_write_b64 v3, v[52:53] offset:4096\n"
    "v_smfmac_f32_16x16x64_bf16 v[56:59], v[16:19], v[136:143], v2\n"
    "buffer_store_dwordx4 v[48:51], v74, s[36:39], s76 offen sc1\n"
    "v_smfmac_f32_16x16x64_bf16 v[56:59], v[20:23], v[144:151], v2\n"
    "ds_read_b128 v[16:19], v1 offset:16384\n"
    "v_smfmac_f32_16x16x64_bf16 v[56:59], v[24:27], v[152:159], v2\n"
    "ds_read_b128 v[20:23], v1 offset:17408\n"
    "v_smfmac_f32_16x16x64_bf16 v[56:59], v[28:31], v[160:167], v2\n"
    "s_waitcnt lgkmcnt(2)\n"
    "s_barrier\n"
    "ds_read_b128 v[64:67], v76 offset:0\n"
    "v_smfmac_f32_16x16x64_bf16 v[60:63], v[28:31], v[168:175], v2\n"
    "ds_read_b128 v[40:43], v77 offset:0\n"
    "v_smfmac_f32_16x16x64_bf16 v[56:59], v[32:35], v[176:183], v2\n"
    "ds_read_b128 v[44:47], v78 offset:0\n"
    "v_smfmac_f32_16x16x64_bf16 v[60:63], v[32:35], v[184:191], v2\n"
    "ds_read_b64 v[12:13], v72 offset:4096\n"
    "ds_read_b64 v[14:15], v72 offset:4608\n"
    "v_smfmac_f32_16x16x64_bf16 v[56:59], v[36:39], v[192:199], v2\n"
    "ds_read_b64 v[52:53], v72 offset:5120\n"
    "ds_read_b64 v[54:55], v72 offset:5632\n"
    "v_smfmac_f32_16x16x64_bf16 v[60:63], v[36:39], v[200:207], v2\n"
    "ds_read_b128 v[24:27], v1 offset:0\n"
    "s_cmp_eq_u32 s68, 63\n"
    "s_waitcnt lgkmcnt(5)\n"
    "v_add_f32 v68, v68, v64\n"
    "v_add_f32 v69, v69, v65\n"
    "v_add_f32 v70, v70, v66\n"
    "v_add_f32 v71, v71, v67\n"
    "v_pk_add_f32 v[40:41], v[40:41], v[44:45]\n"
    "v_pk_add_f32 v[42:43], v[42:43], v[46:47]\n"
    "s_waitcnt lgkmcnt(1)\n"
    "v_pk_add_f32 v[12:13], v[12:13], v[14:15]\n"
    "v_pk_add_f32 v[52:53], v[52:53], v[54:55]\n"
    "v_pk_add_f32 v[68:69], v[68:69], v[40:41]\n"
    "v_pk_add_f32 v[70:71], v[70:71], v[42:43]\n"
    "v_pk_add_f32 v[12:13], v[12:13], v[52:53]\n"
    "v_max3_f32 v46, v68, v69, v70\n"
    "v_max3_f32 v47, v71, v12, v13\n"
    "v_max_f32 v3, v46, v47\n"
    "v_cvt_pk_bf16_f32 v56, v56, v57\n"
    "v_cvt_pk_bf16_f32 v57, v58, v59\n"
    "v_max_f32_dpp v46, v3, v3 row_ror:8 row_mask:0xf bank_mask:0xf\n"
    "v_max_f32_dpp v47, v3, v3 row_ror:8 row_mask:0xf bank_mask:0xf\n"
    "v_cvt_pk_bf16_f32 v58, v60, v61\n"
    "v_cvt_pk_bf16_f32 v59, v62, v63\n"
    "v_permlane16_swap_b32 v46, v47\n"
    "v_max_f32 v3, v46, v47\n"
    "v_max_f32 v46, v46, v47\n"
    "v_add_u32 v14, s73, v73\n"
    "v_xor_b32 v15, 128, v0\n"
    "v_permlane32_swap_b32 v3, v46\n"
    "v_max_f32 v3, v3, v46\n"
    "v_mul_f32 v46, 0xbe0293ee, v3\n"
    "v_pk_fma_f32 v[68:69], v[68:69], s[84:85], v[46:47] op_sel_hi:[1,0,0]\n"
    "v_pk_fma_f32 v[70:71], v[70:71], s[84:85], v[46:47] op_sel_hi:[1,0,0]\n"
    "v_pk_fma_f32 v[12:13], v[12:13], s[84:85], v[46:47] op_sel_hi:[1,0,0]\n"
    "v_exp_f32 v68, v68\n"
    "v_exp_f32 v69, v69\n"
    "v_exp_f32 v70, v70\n"
    "v_exp_f32 v71, v71\n"
    "v_exp_f32 v12, v12\n"
    "v_cvt_pk_bf16_f32 v40, v68, v69\n"
    "v_cvt_pk_bf16_f32 v41, v70, v71\n"
    "v_exp_f32 v13, v13\n"
    "v_mov_b32_dpp v42, v40 row_ror:8 row_mask:0xf bank_mask:0xf\n"
    "v_mov_b32_dpp v43, v41 row_ror:8 row_mask:0xf bank_mask:0xf\n"
    "v_cvt_pk_bf16_f32 v44, v12, v13\n"
    "v_pk_add_f32 v[68:69], v[68:69], v[70:71]\n"
    "s_waitcnt vmcnt(5)\n"
    "v_mfma_f32_16x16x32_bf16 v[48:51], v[4:7], v[40:43], 0\n"
    "ds_read_b128 v[28:31], v1 offset:1024\n"
    "v_mov_b32_dpp v45, v44 row_ror:8 row_mask:0xf bank_mask:0xf\n"
    "v_add_f32 v3, v12, v13\n"
    "v_mfma_f32_16x16x32_bf16 v[52:55], v[8:11], v[40:43], 0\n"
    "s_cbranch_scc1 .Lrec_fin_%=\n"
    "v_add_f32 v46, v68, v69\n"
    "v_fma_f32 v3, v3, 0.5, v46\n"
    "v_mfma_f32_16x16x16_bf16 v[48:51], v[56:57], v[44:45], v[48:51]\n"
    "ds_read_b128 v[32:35], v1 offset:24576\n"
    "v_add_f32_dpp v46, v3, v3 row_ror:8 row_mask:0xf bank_mask:0xf\n"
    "v_add_f32_dpp v47, v3, v3 row_ror:8 row_mask:0xf bank_mask:0xf\n"
    "v_mfma_f32_16x16x16_bf16 v[52:55], v[58:59], v[44:45], v[52:55]\n"
    "ds_read_b128 v[36:39], v1 offset:25600\n"
    "v_permlane16_swap_b32 v46, v47\n"
    "v_add_f32 v3, v46, v47\n"
    "v_add_f32 v46, v46, v47\n"
    "v_mov_b64 v[64:65], 0\n"
    "v_mov_b64 v[66:67], 0\n"
    "v_permlane32_swap_b32 v3, v46\n"
    "v_add_f32 v3, v3, v46\n"
    "v_rcp_f32 v46, v3\n"
    "v_mov_b32_dpp v48, v52 row_ror:8 row_mask:0xf bank_mask:0xc\n"
    "v_mov_b32_dpp v49, v53 row_ror:8 row_mask:0xf bank_mask:0xc\n"
    "v_mov_b32_dpp v50, v54 row_ror:8 row_mask:0xf bank_mask:0xc\n"
    "v_mov_b32_dpp v51, v55 row_ror:8 row_mask:0xf bank_mask:0xc\n"
    "v_pk_mul_f32 v[48:49], v[48:49], v[46:47] op_sel_hi:[1,0]\n"
    "v_pk_mul_f32 v[50:51], v[50:51], v[46:47] op_sel_hi:[1,0]\n"
    "v_cvt_pk_bf16_f32 v48, v48, v49\n"
    "v_cvt_pk_bf16_f32 v49, v50, v51\n"
    "ds_write_b64 v14, v[48:49] offset:8192\n"
    "buffer_store_dwordx4 v[56:59], v0, s[36:39], s76 offen offset:512 sc1\n"
    "ds_read_b128 v[72:75], v1 offset:8192\n"
    "ds_read_b128 v[76:79], v1 offset:9216\n"
    "ds_read_b128 v[4:7], v1 offset:32768\n"
    "ds_read_b128 v[8:11], v1 offset:33792\n"
    "v_lshlrev_b32 v12, 2, v0\n"
    "s_waitcnt vmcnt(3) lgkmcnt(4)\n"
    "s_barrier\n"
    "ds_read_b128 v[40:43], v0 offset:8192\n"
    "ds_read_b128 v[44:47], v0 offset:9216\n"
    "ds_read_b128 v[48:51], v15 offset:8192\n"
    "ds_read_b128 v[52:55], v15 offset:9216\n"
    "v_mov_b64 v[56:57], 0\n"
    "v_mov_b64 v[58:59], 0\n"
    "v_mov_b64 v[60:61], 0\n"
    "v_mov_b64 v[62:63], 0\n"
    "s_mov_b32 exec_hi, 0\n"
    "buffer_load_dwordx4 v[12:15], v12, s[36:39], s90 offen sc1\n"
    "s_mov_b32 exec_hi, -1\n"
    "s_waitcnt lgkmcnt(3)\n"
    "v_smfmac_f32_16x16x64_bf16 v[56:59], v[40:43], v[16:23], v2\n"
    "ds_read_b128 v[16:19], v1 offset:40960\n"
    "s_waitcnt lgkmcnt(3)\n"
    "v_smfmac_f32_16x16x64_bf16 v[56:59], v[44:47], v[24:31], v2\n"
    "ds_read_b128 v[20:23], v1 offset:41984\n"
    "v_smfmac_f32_16x16x64_bf16 v[60:63], v[40:43], v[32:39], v2\n"
    "ds_read_b128 v[24:27], v1 offset:49152\n"
    "v_mov_b64 v[70:71], 0\n"
    "v_smfmac_f32_16x16x64_bf16 v[60:63], v[44:47], v[72:79], v2\n"
    "ds_read_b128 v[28:31], v1 offset:50176\n"
    "s_waitcnt lgkmcnt(5)\n"
    "v_smfmac_f32_16x16x64_bf16 v[56:59], v[48:51], v[4:11], v2\n"
    "ds_read_b128 v[32:35], v1 offset:57344\n"
    "s_waitcnt lgkmcnt(5)\n"
    "v_smfmac_f32_16x16x64_bf16 v[56:59], v[52:55], v[208:215], v2\n"
    "ds_read_b128 v[36:39], v1 offset:58368\n"
    "ds_read_b128 v[8:11], v0 offset:14336\n"
    "s_waitcnt lgkmcnt(5)\n"
    "v_smfmac_f32_16x16x64_bf16 v[60:63], v[48:51], v[16:23], v2\n"
    "v_add_u32 v1, 32768, v1\n"
    "ds_read_b128 v[72:75], v1 offset:32768\n"
    "v_mov_b64 v[68:69], 0\n"
    "v_smfmac_f32_16x16x64_bf16 v[60:63], v[52:55], v[216:223], v2\n"
    "ds_read_b128 v[76:79], v1 offset:33792\n"
    "v_cvt_pk_bf16_f32 v56, v56, v57\n"
    "s_waitcnt lgkmcnt(5)\n"
    "v_smfmac_f32_16x16x64_bf16 v[64:67], v[40:43], v[24:31], v2\n"
    "ds_read_b128 v[16:19], v1 offset:40960\n"
    "v_cvt_pk_bf16_f32 v57, v58, v59\n"
    "v_smfmac_f32_16x16x64_bf16 v[64:67], v[44:47], v[224:231], v2\n"
    "ds_read_b128 v[20:23], v1 offset:41984\n"
    "v_cvt_pk_bf16_f32 v58, v60, v61\n"
    "v_cvt_pk_bf16_f32 v59, v62, v63\n"
    "s_waitcnt lgkmcnt(5)\n"
    "v_smfmac_f32_16x16x64_bf16 v[68:71], v[40:43], v[32:39], v2\n"
    "ds_read_b128 v[24:27], v1 offset:49152\n"
    "v_permlane16_swap_b32 v56, v58\n"
    "v_smfmac_f32_16x16x64_bf16 v[68:71], v[44:47], v[232:239], v2\n"
    "ds_read_b128 v[28:31], v1 offset:50176\n"
    "v_permlane16_swap_b32 v57, v59\n"
    "s_waitcnt lgkmcnt(4)\n"
    "v_smfmac_f32_16x16x64_bf16 v[64:67], v[48:51], v[72:79], v2\n"
    "ds_read_b128 v[32:35], v1 offset:57344\n"
    "buffer_store_dwordx4 v[56:59], v8, s[36:39], s77 offen sc1\n"
    "v_smfmac_f32_16x16x64_bf16 v[64:67], v[52:55], v[240:247], v2\n"
    "ds_read_b128 v[36:39], v1 offset:58368\n"
    "s_waitcnt lgkmcnt(4)\n"
    "v_smfmac_f32_16x16x64_bf16 v[68:71], v[48:51], v[16:23], v2\n"
    "v_smfmac_f32_16x16x64_bf16 v[68:71], v[52:55], v[248:255], v2\n"
    "v_cvt_pk_bf16_f32 v64, v64, v65\n"
    "s_waitcnt lgkmcnt(2)\n"
    "v_mfma_f32_16x16x32_bf16 v[4:7], v[40:43], v[24:27], 0\n"
    "v_cvt_pk_bf16_f32 v65, v66, v67\n"
    "v_mfma_f32_16x16x32_bf16 v[4:7], v[48:51], v[28:31], v[4:7]\n"
    "v_cvt_pk_bf16_f32 v66, v68, v69\n"
    "v_cvt_pk_bf16_f32 v67, v70, v71\n"
    "s_waitcnt lgkmcnt(0)\n"
    "v_mfma_f32_16x16x32_bf16 v[4:7], v[44:47], v[32:35], v[4:7]\n"
    "v_permlane16_swap_b32 v64, v66\n"
    "v_mfma_f32_16x16x32_bf16 v[4:7], v[52:55], v[36:39], v[4:7]\n"
    "v_permlane16_swap_b32 v65, v67\n"
    "buffer_store_dwordx4 v[64:67], v8, s[36:39], s86 offen sc1\n"
    "v_subrev_u32 v1, 81920, v1\n"
    "v_mov_b32 v76, v10\n"
    "ds_read_b128 v[72:75], v0 offset:15360\n"
    "v_lshl_or_b32 v77, s68, 1, 1\n"
    "s_nop 1\n"
    "v_cvt_pk_bf16_f32 v4, v4, v5\n"
    "v_cvt_pk_bf16_f32 v5, v6, v7\n"
    "s_mov_b32 s33, s32\n"
    "v_permlane16_swap_b32 v4, v6\n"
    "v_permlane16_swap_b32 v5, v7\n"
    "buffer_store_dwordx4 v[4:7], v9, s[36:39], s87 offen sc1\n"
    "s_waitcnt vmcnt(3)\n"
    "v_max3_u32 v12, v12, v13, v14\n"
    "v_max_u32 v12, v12, v15\n"
    "v_mov_b32 v3, -1\n"
    "s_nop 0\n"
    "v_max_u32_dpp v12, v12, v12 row_ror:8 row_mask:0xf bank_mask:0xf\n"
    "s_nop 0\n"
    "s_nop 0\n"
    "v_max_u32_dpp v12, v12, v12 row_ror:4 row_mask:0xf bank_mask:0xf\n"
    "s_nop 0\n"
    "s_nop 0\n"
    "v_max_u32_dpp v12, v12, v12 row_ror:2 row_mask:0xf bank_mask:0xf\n"
    "s_nop 0\n"
    "s_nop 0\n"
    "v_max_u32_dpp v12, v12, v12 row_ror:1 row_mask:0xf bank_mask:0xf\n"
    "s_mov_b64 exec, 1\n"
    "buffer_atomic_smax v77, off, s[36:39], s58 sc1\n"
    "v_readlane_b32 s95, v12, 0\n"
    "v_readlane_b32 s99, v12, 16\n"
    "s_mov_b64 exec, -1\n"
    "s_max_u32 s95, s95, s99\n"
    "s_cmp_eq_u32 s95, -1\n"
    "s_cbranch_scc1 .Lrec_tsretry_%=\n"
    ".Lrec_tsgo_%=:\n"
    "s_memtime s[96:97]\n"
    "s_mov_b32 s31, s95\n"
    "s_sub_u32 s95, s95, s28\n"
    "s_min_u32 s95, s95, 4000\n"
    "s_add_u32 s95, s95, s34\n"
    "s_mul_i32 s95, s95, s48\n"
    "s_lshr_b32 s95, s95, 8\n"
    "s_add_u32 s32, s32, s95\n"
    "s_waitcnt lgkmcnt(0)\n"
    "s_sub_u32 s95, s32, s96\n"
    "s_mul_i32 s95, s95, 43\n"
    "s_ashr_i32 s95, s95, 10\n"
    "s_max_i32 s95, s95, 0\n"
    "s_min_i32 s95, s95, 100\n"
    ".Lrec_tsloop_%=:\n"
    "s_sub_u32 s95, s95, 1\n"
    "s_cbranch_scc0 .Lrec_tsloop_%=\n"
    "s_cmp_lg_u32 s60, 0\n"
    "s_cbranch_scc1 .Lrec_nored_%=\n"
    ".Lrec_red_%=:\n"
    "buffer_load_dwordx4 v[4:7], v0, s[36:39], s78 offen sc1\n"
    "buffer_load_dwordx4 v[8:11], v0, s[36:39], s78 offen offset:1024 sc1\n"
    "buffer_load_dwordx4 v[12:15], v0, s[36:39], s78 offen offset:2048 sc1\n"
    "buffer_load_dwordx4 v[16:19], v0, s[36:39], s78 offen offset:3072 sc1\n"
    "buffer_load_dwordx4 v[20:23], v0, s[36:39], s88 offen sc1\n"
    "buffer_load_dwordx4 v[24:27], v0, s[36:39], s88 offen offset:1024 sc1\n"
    "buffer_load_dwordx4 v[28:31], v0, s[36:39], s88 offen offset:2048 sc1\n"
    "buffer_load_dwordx4 v[32:35], v0, s[36:39], s88 offen offset:3072 sc1\n"
    "s_sub_u32 s95, s52, s35\n"
    "s_mul_i32 s95, s48, s95\n"
    "s_lshr_b32 s95, s95, 10\n"
    "s_add_u32 s32, s32, s95\n"
    "s_add_u32 s68, s68, 1\n"
    "s_and_b32 s70, s68, 3\n"
    "s_lshl_b32 s70, s70, 17\n"
    "s_add_u32 s70, s70, s66\n"
    "s_add_u32 s71, s70, 4096\n"
    "s_add_u32 s72, s70, 16384\n"
    "s_sub_u32 s46, s33, s46\n"
    "s_sub_u32 s47, s28, s47\n"
    "v_cvt_f32_u32 v40, s46\n"
    "v_cvt_f32_u32 v41, s47\n"
    "v_rcp_f32 v41, v41\n"
    "s_mov_b32 s46, s33\n"
    "s_mov_b32 s47, s28\n"
    "v_mul_f32 v40, v40, v41\n"
    "v_mov_b32 v42, 0x40c80000\n"
    "v_med3_f32 v40, v40, 4.0, v42\n"
    "v_rcp_f32 v41, v40\n"
    "v_mul_f32 v40, 0x43800000, v40\n"
    "v_mul_f32 v41, 0x44b54000, v41\n"
    "v_cvt_u32_f32 v40, v40\n"
    "v_cvt_u32_f32 v41, v41\n"
    "v_readfirstlane_b32 s48, v40\n"
    "v_readfirstlane_b32 s49, v41\n"
    "v_mov_b64 v[40:41], 0\n"
    "v_mov_b64 v[42:43], 0\n"
    "v_mov_b64 v[44:45], 0\n"
    "v_mov_b64 v[46:47], 0\n"
    "v_mov_b64 v[48:49], 0\n"
    "v_mov_b64 v[50:51], 0\n"
    "v_mov_b64 v[52:53], 0\n"
    "v_mov_b64 v[54:55], 0\n"
    "v_mov_b64 v[56:57], 0\n"
    "v_mov_b64 v[58:59], 0\n"
    "v_mov_b64 v[60:61], 0\n"
    "v_mov_b64 v[62:63], 0\n"
    "s_mov_b64 exec, 1\n"
    "buffer_atomic_smax v3, off, s[36:39], s59 sc0 sc1\n"
    "s_mov_b64 exec, -1\n"
    ".Lrec_rsum_%=:\n"
    "s_waitcnt vmcnt(8)\n"
    "v_mfma_f32_16x16x32_bf16 v[36:39], v[4:7], v[72:75], 0\n"
    "v_max3_u32 v78, v4, v5, v6\n"
    "v_max_u32 v78, v78, v7\n"
    "s_waitcnt vmcnt(7)\n"
    "v_mfma_f32_16x16x32_bf16 v[36:39], v[8:11], v[72:75], v[36:39]\n"
    "v_max3_u32 v78, v78, v8, v9\n"
    "v_max3_u32 v78, v78, v10, v11\n"
    "s_waitcnt vmcnt(6)\n"
    "v_mfma_f32_16x16x32_bf16 v[36:39], v[12:15], v[72:75], v[36:39]\n"
    "v_max3_u32 v78, v78, v12, v13\n"
    "v_max3_u32 v78, v78, v14, v15\n"
    "s_waitcnt vmcnt(5)\n"
    "v_mfma_f32_16x16x32_bf16 v[36:39], v[16:19], v[72:75], v[36:39]\n"
    "v_max3_u32 v78, v78, v16, v17\n"
    "v_max3_u32 v78, v78, v18, v19\n"
    "s_waitcnt vmcnt(4)\n"
    "v_mfma_f32_16x16x32_bf16 v[36:39], v[20:23], v[72:75], v[36:39]\n"
    "v_max3_u32 v78, v78, v20, v21\n"
    "v_max3_u32 v78, v78, v22, v23\n"
    "s_waitcnt vmcnt(3)\n"
    "v_mfma_f32_16x16x32_bf16 v[36:39], v[24:27], v[72:75], v[36:39]\n"
    "v_max3_u32 v78, v78, v24, v25\n"
    "v_max3_u32 v78, v78, v26, v27\n"
    "s_waitcnt vmcnt(2)\n"
    "v_mfma_f32_16x16x32_bf16 v[36:39], v[28:31], v[72:75], v[36:39]\n"
    "v_max3_u32 v78, v78, v28, v29\n"
    "v_max3_u32 v78, v78, v30, v31\n"
    "s_waitcnt vmcnt(1)\n"
    "v_mfma_f32_16x16x32_bf16 v[36:39], v[32:35], v[72:75], v[36:39]\n"
    "v_max3_u32 v78, v78, v32, v33\n"
    "v_max3_u32 v78, v78, v34, v35\n"
    "v_cmp_eq_u32 vcc, -1, v78\n"
    "s_nop 4\n"
    "v_cvt_pk_bf16_f32 v4, v36, v37\n"
    "v_cvt_pk_bf16_f32 v6, v36, v37\n"
    "v_cvt_pk_bf16_f32 v5, v38, v39\n"
    "v_cvt_pk_bf16_f32 v7, v38, v39\n"
    "s_cbranch_vccnz .Lrec_rretry_%=\n"
    ".Lrec_rgo_%=:\n"
    "v_permlane16_swap_b32 v4, v6\n"
    "v_permlane16_swap_b32 v5, v7\n"
    "buffer_store_dwordx4 v[4:7], v76, s[36:39], s79 offen sc1\n"
    "s_waitcnt vmcnt(1)\n"
    ".Lrec_b3_%=:\n"
    ".Lrec_entry_%=:\n"
    "s_memtime s[96:97]\n"
    "ds_read_b128 v[72:75], v1 offset:0\n"
    "ds_read_b128 v[76:79], v1 offset:1024\n"
    "v_readfirstlane_b32 s81, v3\n"
    "s_lshl_b32 s73, s68, 1\n"
    "s_cmp_le_i32 s81, s73\n"
    "s_cbranch_scc1 .Lrec_waitp_%=\n"
    "s_waitcnt lgkmcnt(0)\n"
    "s_sub_u32 s95, s32, s96\n"
    "s_mul_i32 s95, s95, 43\n"
    "s_ashr_i32 s95, s95, 10\n"
    "s_max_i32 s95, s95, 0\n"
    "s_min_i32 s95, s95, 100\n"
    ".Lrec_gloop_%=:\n"
    "s_sub_u32 s95, s95, 1\n"
    "s_cbranch_scc0 .Lrec_gloop_%=\n"
    "s_branch .Lrec_top_%=\n"
    ".Lrec_LOOPE_%=:\n"
    ".Lrec_fin_%=:\n"
    "buffer_store_dwordx4 v[56:59], v0, s[36:39], s76 offen offset:512 sc1\n"
    "s_waitcnt vmcnt(0) lgkmcnt(0)\n"
    "v_lshl_or_b32 v46, s68, 1, 1\n"
    "s_mov_b64 exec, 1\n"
    "buffer_atomic_smax v46, off, s[36:39], s58 sc1\n"
    "s_mov_b64 exec, -1\n"
    "s_waitcnt vmcnt(0)\n"
    "s_branch .Lrec_FIN_%=\n"
    ".Lrec_retry0_%=:\n"
    "s_cmp_eq_u32 s69, 0\n"
    "s_cbranch_scc1 .Lrec_back0_%=\n"
    "s_waitcnt lgkmcnt(0)\n"
    "ds_read_b128 v[72:75], v1 offset:0\n"
    "ds_read_b128 v[76:79], v1 offset:1024\n"
    ".Lrec_again0_%=:\n"
    "buffer_load_dwordx4 v[4:7], v0, s[36:39], s70 offen sc1\n"
    "s_nop 7\n"
    "v_mov_b64 v[40:41], 0\n"
    "v_mov_b64 v[42:43], 0\n"
    "v_mov_b64 v[44:45], 0\n"
    "v_mov_b64 v[46:47], 0\n"
    "v_mov_b64 v[48:49], 0\n"
    "v_mov_b64 v[50:51], 0\n"
    "v_mov_b64 v[52:53], 0\n"
    "v_mov_b64 v[54:55], 0\n"
    "v_mov_b64 v[56:57], 0\n"
    "v_mov_b64 v[58:59], 0\n"
    "v_mov_b64 v[60:61], 0\n"
    "v_mov_b64 v[62:63], 0\n"
    "s_waitcnt vmcnt(0)\n"
    "v_max3_u32 v3, v4, v5, v6\n"
    "v_max_u32 v3, v3, v7\n"
    "v_cmp_eq_u32 vcc, -1, v3\n"
    "s_cbranch_vccz .Lrec_start_%=\n"
    "s_sub_u32 s69, s69, 1\n"
    "s_cbranch_scc0 .Lrec_again0_%=\n"
    "s_mov_b32 s69, 0\n"
    "s_branch .Lrec_start_%=\n"
    ".Lrec_retry1_%=:\n"
    "s_cmp_eq_u32 s69, 0\n"
    "s_cbranch_scc1 .Lrec_back1_%=\n"
    "s_waitcnt lgkmcnt(0)\n"
    "ds_read_b128 v[72:75], v1 offset:0\n"
    "ds_read_b128 v[76:79], v1 offset:1024\n"
    ".Lrec_again1_%=:\n"
    "buffer_load_dwordx4 v[4:7], v0, s[36:39], s70 offen sc1\n"
    "buffer_load_dwordx4 v[8:11], v0, s[36:39], s70 offen offset:1024 sc1\n"
    "buffer_load_dwordx4 v[12:15], v0, s[36:39], s70 offen offset:2048 sc1\n"
    "buffer_load_dwordx4 v[16:19], v0, s[36:39], s70 offen offset:3072 sc1\n"
    "s_nop 7\n"
    "v_mov_b64 v[40:41], 0\n"
    "v_mov_b64 v[42:43], 0\n"
    "v_mov_b64 v[44:45], 0\n"
    "v_mov_b64 v[46:47], 0\n"
    "v_mov_b64 v[48:49], 0\n"
    "v_mov_b64 v[50:51], 0\n"
    "v_mov_b64 v[52:53], 0\n"
    "v_mov_b64 v[54:55], 0\n"
    "v_mov_b64 v[56:57], 0\n"
    "v_mov_b64 v[58:59], 0\n"
    "v_mov_b64 v[60:61], 0\n"
    "v_mov_b64 v[62:63], 0\n"
    "s_waitcnt vmcnt(0)\n"
    "v_max3_u32 v3, v4, v5, v6\n"
    "v_max3_u32 v3, v3, v7, v8\n"
    "v_max3_u32 v3, v3, v9, v10\n"
    "v_max3_u32 v3, v3, v11, v12\n"
    "v_max3_u32 v3, v3, v13, v14\n"
    "v_max3_u32 v3, v3, v15, v16\n"
    "v_max3_u32 v3, v3, v17, v18\n"
    "v_max_u32 v3, v3, v19\n"
    "v_cmp_eq_u32 vcc, -1, v3\n"
    "s_cbranch_vccz .Lrec_start_%=\n"
    "s_sub_u32 s69, s69, 1\n"
    "s_cbranch_scc0 .Lrec_again1_%=\n"
    "s_mov_b32 s69, 0\n"
    "s_branch .Lrec_start_%=\n"
    ".Lrec_retry2_%=:\n"
    "s_cmp_eq_u32 s69, 0\n"
    "s_cbranch_scc1 .Lrec_back2_%=\n"
    "v_subrev_u32 v1, 49152, v1\n"
    "s_waitcnt lgkmcnt(0)\n"
    "ds_read_b128 v[72:75], v1 offset:0\n"
    "ds_read_b128 v[76:79], v1 offset:1024\n"
    ".Lrec_again2_%=:\n"
    "buffer_load_dwordx4 v[4:7], v0, s[36:39], s70 offen sc1\n"
    "buffer_load_dwordx4 v[8:11], v0, s[36:39], s70 offen offset:1024 sc1\n"
    "buffer_load_dwordx4 v[12:15], v0, s[36:39], s70 offen offset:2048 sc1\n"
    "buffer_load_dwordx4 v[16:19], v0, s[36:39], s70 offen offset:3072 sc1\n"
    "buffer_load_dwordx4 v[20:23], v0, s[36:39], s71 offen sc1\n"
    "buffer_load_dwordx4 v[24:27], v0, s[36:39], s71 offen offset:1024 sc1\n"
    "buffer_load_dwordx4 v[28:31], v0, s[36:39], s71 offen offset:2048 sc1\n"
    "buffer_load_dwordx4 v[32:35], v0, s[36:39], s71 offen offset:3072 sc1\n"
    "buffer_load_dwordx4 v[36:39], v0, s[36:39], s72 offen sc1\n"
    "s_nop 7\n"
    "v_mov_b64 v[40:41], 0\n"
    "v_mov_b64 v[42:43], 0\n"
    "v_mov_b64 v[44:45], 0\n"
    "v_mov_b64 v[46:47], 0\n"
    "v_mov_b64 v[48:49], 0\n"
    "v_mov_b64 v[50:51], 0\n"
    "v_mov_b64 v[52:53], 0\n"
    "v_mov_b64 v[54:55], 0\n"
    "v_mov_b64 v[56:57], 0\n"
    "v_mov_b64 v[58:59], 0\n"
    "v_mov_b64 v[60:61], 0\n"
    "v_mov_b64 v[62:63], 0\n"
    "s_waitcnt vmcnt(0)\n"
    "v_max3_u32 v3, v4, v5, v6\n"
    "v_max3_u32 v3, v3, v7, v8\n"
    "v_max3_u32 v3, v3, v9, v10\n"
    "v_max3_u32 v3, v3, v11, v12\n"
    "v_max3_u32 v3, v3, v13, v14\n"
    "v_max3_u32 v3, v3, v15, v16\n"
    "v_max3_u32 v3, v3, v17, v18\n"
    "v_max3_u32 v3, v3, v19, v20\n"
    "v_max3_u32 v3, v3, v21, v22\n"
    "v_max3_u32 v3, v3, v23, v24\n"
    "v_max3_u32 v3, v3, v25, v26\n"
    "v_max3_u32 v3, v3, v27, v28\n"
    "v_max3_u32 v3, v3, v29, v30\n"
    "v_max3_u32 v3, v3, v31, v32\n"
    "v_max3_u32 v3, v3, v33, v34\n"
    "v_max3_u32 v3, v3, v35, v36\n"
    "v_max3_u32 v3, v3, v37, v38\n"
    "v_max_u32 v3, v3, v39\n"
    "v_cmp_eq_u32 vcc, -1, v3\n"
    "s_cbranch_vccz .Lrec_start_%=\n"
    "s_sub_u32 s69, s69, 1\n"
    "s_cbranch_scc0 .Lrec_again2_%=\n"
    "s_mov_b32 s69, 0\n"
    "s_branch .Lrec_start_%=\n"
    ".Lrec_tsretry_%=:\n"
    "s_mov_b32 s98, 24\n"
    ".Lrec_tsretry1_%=:\n"
    "s_mov_b32 s95, s28\n"
    "s_sub_u32 s98, s98, 1\n"
    "s_cbranch_scc1 .Lrec_tsgo_%=\n"
    "s_sleep 2\n"
    "buffer_load_dwordx4 v[12:15], v11, s[36:39], s90 offen sc1\n"
    "s_waitcnt vmcnt(0)\n"
    "v_max3_u32 v12, v12, v13, v14\n"
    "v_max_u32 v12, v12, v15\n"
    "s_nop 1\n"
    "v_max_u32_dpp v12, v12, v12 row_ror:8 row_mask:0xf bank_mask:0xf\n"
    "s_nop 1\n"
    "v_max_u32_dpp v12, v12, v12 row_ror:4 row_mask:0xf bank_mask:0xf\n"
    "s_nop 1\n"
    "v_max_u32_dpp v12, v12, v12 row_ror:2 row_mask:0xf bank_mask:0xf\n"
    "s_nop 1\n"
    "v_max_u32_dpp v12, v12, v12 row_ror:1 row_mask:0xf bank_mask:0xf\n"
    "s_nop 0\n"
    "v_readlane_b32 s95, v12, 0\n"
    "v_readlane_b32 s99, v12, 16\n"
    "s_nop 0\n"
    "s_max_u32 s95, s95, s99\n"
    "s_cmp_eq_u32 s95, -1\n"
    "s_cbranch_scc1 .Lrec_tsretry1_%=\n"
    "s_branch .Lrec_tsgo_%=\n"
    ".Lrec_rretry_%=:\n"
    "s_sub_u32 s69, s69, 1\n"
    "s_cbranch_scc1 .Lrec_rdead_%=\n"
    "s_nop 7\n"
    "buffer_load_dwordx4 v[4:7], v0, s[36:39], s78 offen sc1\n"
    "buffer_load_dwordx4 v[8:11], v0, s[36:39], s78 offen offset:1024 sc1\n"
    "buffer_load_dwordx4 v[12:15], v0, s[36:39], s78 offen offset:2048 sc1\n"
    "buffer_load_dwordx4 v[16:19], v0, s[36:39], s78 offen offset:3072 sc1\n"
    "buffer_load_dwordx4 v[20:23], v0, s[36:39], s88 offen sc1\n"
    "buffer_load_dwordx4 v[24:27], v0, s[36:39], s88 offen offset:1024 sc1\n"
    "buffer_load_dwordx4 v[28:31], v0, s[36:39], s88 offen offset:2048 sc1\n"
    "buffer_load_dwordx4 v[32:35], v0, s[36:39], s88 offen offset:3072 sc1\n"
    "s_waitcnt vmcnt(0)\n"
    "s_branch .Lrec_rsum_%=\n"
    ".Lrec_rdead_%=:\n"
    "s_mov_b32 s69, 0\n"
    "s_nop 7\n"
    "s_branch .Lrec_rgo_%=\n"
    ".Lrec_nored_%=:\n"
    "s_nop 8\n"
    "s_cmp_lt_i32 s64, 0\n"
    "s_cbranch_scc0 .Lrec_red_%=\n"
    "s_sub_u32 s95, s52, s35\n"
    "s_mul_i32 s95, s48, s95\n"
    "s_lshr_b32 s95, s95, 10\n"
    "s_add_u32 s32, s32, s95\n"
    "s_add_u32 s68, s68, 1\n"
    "s_and_b32 s70, s68, 3\n"
    "s_lshl_b32 s70, s70, 17\n"
    "s_add_u32 s70, s70, s66\n"
    "s_add_u32 s71, s70, 4096\n"
    "s_add_u32 s72, s70, 16384\n"
    "s_sub_u32 s46, s33, s46\n"
    "s_sub_u32 s47, s28, s47\n"
    "v_cvt_f32_u32 v40, s46\n"
    "v_cvt_f32_u32 v41, s47\n"
    "v_rcp_f32 v41, v41\n"
    "s_mov_b32 s46, s33\n"
    "s_mov_b32 s47, s28\n"
    "v_mul_f32 v40, v40, v41\n"
    "v_mov_b32 v42, 0x40c80000\n"
    "v_med3_f32 v40, v40, 4.0, v42\n"
    "v_rcp_f32 v41, v40\n"
    "v_mul_f32 v40, 0x43800000, v40\n"
    "v_mul_f32 v41, 0x44b54000, v41\n"
    "v_cvt_u32_f32 v40, v40\n"
    "v_cvt_u32_f32 v41, v41\n"
    "v_readfirstlane_b32 s48, v40\n"
    "v_readfirstlane_b32 s49, v41\n"
    "v_mov_b64 v[40:41], 0\n"
    "v_mov_b64 v[42:43], 0\n"
    "v_mov_b64 v[44:45], 0\n"
    "v_mov_b64 v[46:47], 0\n"
    "v_mov_b64 v[48:49], 0\n"
    "v_mov_b64 v[50:51], 0\n"
    "v_mov_b64 v[52:53], 0\n"
    "v_mov_b64 v[54:55], 0\n"
    "v_mov_b64 v[56:57], 0\n"
    "v_mov_b64 v[58:59], 0\n"
    "v_mov_b64 v[60:61], 0\n"
    "v_mov_b64 v[62:63], 0\n"
    "s_mov_b64 exec, 1\n"
    "buffer_atomic_smax v3, off, s[36:39], s59 sc0 sc1\n"
    "s_mov_b64 exec, -1\n"
    "s_waitcnt vmcnt(0)\n"
    "s_branch .Lrec_b3_%=\n"
    ".Lrec_waitp_%=:\n"
    "v_mov_b32 v3, -1\n"
    "s_mov_b64 exec, 1\n"
    "buffer_atomic_smax v3, off, s[36:39], s59 sc0 sc1\n"
    "s_mov_b64 exec, -1\n"
    "s_waitcnt vmcnt(0)\n"
    "v_readfirstlane_b32 s81, v3\n"
    "s_nop 0\n"
    "s_cmp_gt_i32 s81, s73\n"
    "s_cbranch_scc1 .Lrec_waitp1_%=\n"
    "s_sleep 1\n"
    "s_sub_u32 s69, s69, 1\n"
    "s_cbranch_scc0 .Lrec_waitp_%=\n"
    "s_mov_b32 s69, 0\n"
    ".Lrec_waitp1_%=:\n"
    "s_memtime s[96:97]\n"
    "s_waitcnt lgkmcnt(0)\n"
    "s_sub_u32 s95, s32, s96\n"
    "s_mul_i32 s95, s95, 43\n"
    "s_ashr_i32 s95, s95, 10\n"
    "s_max_i32 s95, s95, 0\n"
    "s_min_i32 s95, s95, 100\n"
    ".Lrec_gloop2_%=:\n"
    "s_sub_u32 s95, s95, 1\n"
    "s_cbranch_scc0 .Lrec_gloop2_%=\n"
    "s_branch .Lrec_top_%=\n"
    ".Lrec_FIN_%=:\n"
  );
}

typedef unsigned long long u64;
DEVI u64 ld8u(rsrc_t r, u32 voff) { v2u v = ld8(r, voff, 0); asm volatile("" : "+v"(v)); return (u64)v.x | ((u64)v.y << 32); }
DEVI u32 find_shift(rsrc_t rw, bool decide, u64 tag) {
  const int lane = threadIdx.x & 63;
  unsigned budget = SPIN_LIMIT;
  if (decide) {
    u32 best[2] = {~0u, ~0u};
    const u32 probe = 126 * CHUNK + 8192;
#pragma unroll 1
    for (int r = 0; r < 5; ++r) {
      const u32 a = lane == 0 ? probe + (u32)(r & 1) * 8192 : OOB;
      u32 v = 0;
      const unsigned long long t0 = __builtin_amdgcn_s_memtime();
      asm volatile("s_waitcnt lgkmcnt(0)\n\tbuffer_atomic_or %0, %1, %2, 0 offen sc0 sc1\n\ts_waitcnt vmcnt(0)" : "+v"(v) : "v"(a), "s"(rw) : "memory");
      const u32 dt = (u32)(__builtin_amdgcn_s_memtime() - t0);
      if (r & 1) best[1] = min(best[1], dt); else best[0] = min(best[0], dt);
    }
    for (;;) {
      v2u v[4];
      const u32 a = READY + (u32)lane * 8;
#pragma unroll
      for (int k = 0; k < 4; ++k) v[k] = (v2u){0u, 0u};
      asm volatile("buffer_atomic_or_x2 %0, %4, %5, 0 offen sc0 sc1\n\tbuffer_atomic_or_x2 %1, %4, %5, 0 offen offset:512 sc0 sc1\n\t"
                   "buffer_atomic_or_x2 %2, %4, %5, 0 offen offset:1024 sc0 sc1\n\tbuffer_atomic_or_x2 %3, %4, %5, 0 offen offset:1536 sc0 sc1\n\ts_waitcnt vmcnt(0)"
                   : "+v"(v[0]), "+v"(v[1]), "+v"(v[2]), "+v"(v[3]) : "v"(a), "s"(rw) : "memory");
      bool ok = true;
#pragma unroll
      for (int k = 0; k < 4; ++k) ok = ok & (v[k].x == (u32)tag) & (v[k].y == (u32)(tag >> 32));
      if (__all(ok) || budget == 0) break;
      --budget;
    }
    const u64 v = lane == 0 ? 2 * tag + (best[0] < best[1] ? 0u : 1u) : tag;
    st8(rw, (v2u){(u32)v, (u32)(v >> 32)}, lane == 0 ? PARW : (lane == 1 ? EPOCH : OOB), 0);
  }
  u64 v;
  for (;;) {
    cbar();
    v = ld8u(rw, PARW) ^ (2 * tag);
    if (__all(v < 2) || budget == 0) break;
    --budget;
    __builtin_amdgcn_s_sleep(4);
  }
  cbar();
  return (rfl((u32)v) & 1u) * 8192;
}
DEVI void hot_init(const Params& p, rsrc_t rw, int L, int n, u32 shift) {
  const int tid = threadIdx.x, w = (int)rfl(tid >> 6);
  const u32 hot = HOT + L * 8192 + shift, hot2 = HOT2 + (1 - L) * 8192 + shift;
  const v4u ones = {~0u, ~0u, ~0u, ~0u};
#pragma unroll
  for (int k = 0; k < 9; ++k) {
    st16(rw, ones, tid * 16, hot + (u32)(9 * n + k) * CHUNK);
    st16(rw, ones, tid * 16 + 4096, hot + (u32)(9 * n + k) * CHUNK);
  }
  {
    const u32 b = hot2 + (u32)(4 + n) * 2 * CHUNK;
    st16(rw, ones, tid * 16, b); st16(rw, ones, tid * 16 + 4096, b);
    st16(rw, ones, w == 0 ? (u32)tid * 16 : OOB, b + CHUNK);
  }
  if (n < 8) {
#pragma unroll
    for (int k = 0; k < 2; ++k) {
      st16(rw, ones, tid * 16, hot2 + (u32)(32 + 2 * n + k) * CHUNK);
      st16(rw, ones, tid * 16 + 4096, hot2 + (u32)(32 + 2 * n + k) * CHUNK);
    }
  }
  if (n >= 8) {
    const u32 b = hot2 + (u32)(n - 8) * 2 * CHUNK;
#pragma unroll
    for (int k = 0; k < 3; ++k) {
      const int pc = tid + 256 * k;
      if (pc < 576) {
        v4u v = *(const v4u*)(p.mem0 + (pc & 7) * 576 + (pc >> 3) * 8);
#pragma unroll
        for (int d = 0; d < 4; ++d) v[d] = min(v[d], 0xFFFFFFFEu);
        st16(rw, v, pc < 512 ? (u32)pc * 16 : CHUNK + (u32)(pc - 512) * 16, b);
      }
    }
  }
  drain();
  __syncthreads();
  if (tid == 0) st4(rw, 0u, INITF + L * 8192 + n * 128 + shift, 0);
}

DEVI void role_att(const Params& p, int L, int j, int a, u32 dbg, u32 shift) {
  const int tid = threadIdx.x, lane = tid & 63, w = (int)rfl(tid >> 6);
  const int c = lane & 15, g = lane >> 4;
  const int hl = 4 * a + w, h = 8 * j + hl;
  const v4u ws = make_srd(p.ws + shift, WS_END);
  v16u sa = cat4(ws, ws, ws, ws);
  v16u sb = {pga(L, B_QKV + 3 * h), pga(L, B_KVM + h), pgb(L, B_O + h), 0, dbg + w * 64,
             0, 0, 0, 0, 0, 0, 0, 0, 0, 0, 0};
  v16u va = {0, 0, 0, 0, 0, 0, 0, 0, 0, 0, 0, 0, 0, 0, 0, 0};
  va[0] = lane * 16;
  va[1] = (u32)(g * 128 + (c & 7) * 16);
  va[2] = lane < 3 ? mbox(L, j, SL_WO + lane, hl) : mbox(L, j, SL_P + hl, 48);
  va[4] = lane < 16 ? mbox_att(L, j, hl, lane) : OOB;
  va[5] = (u32)(lane < 4 ? 0 : (lane < 7 ? 2 * RO : ((lane >> 2) == 2 ? -2 : 1000)));
  va[6] = (u32)(c - 4 * g);
  va[7] = 0;
  RUN_ASM(
    "s_nop 4\n"
    "s_mov_b32 s20, 0\n"
    "s_mov_b32 s21, 0x80000000\n"
    "s_getpc_b64 s[84:85]\n"
    ".Latt_PC_%=:\n"
    "s_and_b32 s85, s85, 0xffff\n"
    "s_mov_b32 s86, .Latt_FIN_%=-.Latt_PC_%=\n"
    "s_mov_b32 s87, 0x20000\n"
    "v_mbcnt_lo_u32_b32 v240, -1, 0\n"
    "v_mbcnt_hi_u32_b32 v240, -1, v240\n"
    "v_lshlrev_b32 v240, 7, v240\n"
    "buffer_load_dword v241, v240, s[84:87], 0 offen\n"
    "v_add_u32 v240, 8192, v240\n"
    "buffer_load_dword v241, v240, s[84:87], 0 offen\n"
    "v_cmp_gt_i32 s[10:11], 0, v6\n"
    "v_cmp_gt_i32 s[12:13], 1, v6\n"
    "v_cmp_gt_i32 s[14:15], 2, v6\n"
    "v_cmp_gt_i32 s[16:17], 3, v6\n"
    "v_mov_b32 v10, 0xff61b1e6\n"
    "s_mov_b32 s18, 0x3e0293ee\n"
    "s_mov_b32 s68, 0\n"
    "s_mov_b32 s77, 1\n"
    "v_mov_b32 v8, -1\n"
    "v_mov_b32 v12, -1\n"
    "s_mov_b32 s69, 262144\n"
    "s_branch .Latt_waitp_%=\n"
    ".Latt_LOOPB_%=:\n"
    ".Latt_top_%=:\n"
    "s_waitcnt vmcnt(15)\n"
    "v_mfma_f32_16x16x32_bf16 v[160:163], v[16:19], v[48:51], 0\n"
    "v_mfma_f32_16x16x32_bf16 v[164:167], v[16:19], v[52:55], 0\n"
    "v_mfma_f32_16x16x32_bf16 v[168:171], v[20:23], v[52:55], 0\n"
    "s_waitcnt vmcnt(12)\n"
    "v_mfma_f32_16x16x32_bf16 v[160:163], v[24:27], v[56:59], v[160:163]\n"
    "v_mfma_f32_16x16x32_bf16 v[164:167], v[24:27], v[60:63], v[164:167]\n"
    "v_mfma_f32_16x16x32_bf16 v[168:171], v[28:31], v[60:63], v[168:171]\n"
    "s_waitcnt vmcnt(10)\n"
    "v_mfma_f32_16x16x32_bf16 v[160:163], v[32:35], v[64:67], v[160:163]\n"
    "v_mfma_f32_16x16x32_bf16 v[164:167], v[32:35], v[68:71], v[164:167]\n"
    "v_mfma_f32_16x16x32_bf16 v[168:171], v[36:39], v[68:71], v[168:171]\n"
    "s_waitcnt vmcnt(8)\n"
    "v_mfma_f32_16x16x32_bf16 v[160:163], v[40:43], v[72:75], v[160:163]\n"
    "v_mfma_f32_16x16x32_bf16 v[164:167], v[40:43], v[76:79], v[164:167]\n"
    "v_mfma_f32_16x16x32_bf16 v[168:171], v[44:47], v[76:79], v[168:171]\n"
    "v_add_u32 v11, v8, v5\n"
    "v_cmp_gt_i32 vcc, s77, v11\n"
    "s_nop 0\n"
    "s_or_b64 exec, vcc, s[20:21]\n"
    "buffer_atomic_smax v8, v4, s[36:39], 0 offen sc0 sc1\n"
    "s_mov_b64 exec, -1\n"
    "s_add_u32 s72, s68, 1\n"
    "s_and_b32 s4, s72, 7\n"
    "s_mul_i32 s4, s4, 4194304\n"
    "s_add_u32 s4, s4, s52\n"
    "s_add_u32 s5, s4, 4096\n"
    "s_add_u32 s6, s4, 16384\n"
    "s_add_u32 s7, s4, 20480\n"
    "s_add_u32 s8, s4, 32768\n"
    "s_add_u32 s9, s4, 36864\n"
    "s_and_b32 s73, s68, 7\n"
    "s_mul_i32 s73, s73, 4194304\n"
    "s_add_u32 s73, s73, s53\n"
    "s_add_u32 s74, s73, 3072\n"
    "s_and_b32 s75, s68, 7\n"
    "s_lshl_b32 s75, s75, 22\n"
    "s_add_u32 s75, s75, s54\n"
    "s_add_u32 s76, s75, 4096\n"
    "v_cndmask_b32 v160, v160, v10, s[10:11]\n"
    "v_cndmask_b32 v161, v161, v10, s[12:13]\n"
    "v_cndmask_b32 v162, v162, v10, s[14:15]\n"
    "v_cndmask_b32 v163, v163, v10, s[16:17]\n"
    "v_cndmask_b32 v168, v168, v10, s[10:11]\n"
    "v_cndmask_b32 v169, v169, v10, s[12:13]\n"
    "v_cndmask_b32 v170, v170, v10, s[14:15]\n"
    "v_cndmask_b32 v171, v171, v10, s[16:17]\n"
    "v_max3_f32 v224, v160, v161, v162\n"
    "v_max3_f32 v226, v164, v165, v166\n"
    "v_max_f32 v224, v224, v163\n"
    "v_max3_f32 v226, v226, v167, v168\n"
    "v_max3_f32 v226, v226, v169, v170\n"
    "v_max_f32 v226, v226, v171\n"
    "v_mov_b32 v238, v224\n"
    "v_mov_b32 v239, v226\n"
    "s_nop 0\n"
    "v_permlane16_swap_b32 v224, v238\n"
    "v_permlane16_swap_b32 v226, v239\n"
    "v_max_f32 v224, v224, v238\n"
    "v_max_f32 v226, v226, v239\n"
    "v_mov_b32 v238, v224\n"
    "v_mov_b32 v239, v226\n"
    "s_nop 0\n"
    "v_permlane32_swap_b32 v224, v238\n"
    "v_permlane32_swap_b32 v226, v239\n"
    "v_max_f32 v224, v224, v238\n"
    "v_max_f32 v226, v226, v239\n"
    "v_mul_f32 v230, 0xbe0293ee, v224\n"
    "v_mul_f32 v232, 0xbe0293ee, v226\n"
    "v_pk_fma_f32 v[160:161], v[160:161], s[18:19], v[230:231] op_sel_hi:[1,0,0]\n"
    "v_pk_fma_f32 v[162:163], v[162:163], s[18:19], v[230:231] op_sel_hi:[1,0,0]\n"
    "v_pk_fma_f32 v[164:165], v[164:165], s[18:19], v[232:233] op_sel_hi:[1,0,0]\n"
    "v_pk_fma_f32 v[166:167], v[166:167], s[18:19], v[232:233] op_sel_hi:[1,0,0]\n"
    "v_pk_fma_f32 v[168:169], v[168:169], s[18:19], v[232:233] op_sel_hi:[1,0,0]\n"
    "v_pk_fma_f32 v[170:171], v[170:171], s[18:19], v[232:233] op_sel_hi:[1,0,0]\n"
    "v_exp_f32 v160, v160\n"
    "v_exp_f32 v161, v161\n"
    "v_exp_f32 v162, v162\n"
    "v_exp_f32 v163, v163\n"
    "v_exp_f32 v164, v164\n"
    "v_exp_f32 v165, v165\n"
    "v_exp_f32 v166, v166\n"
    "v_exp_f32 v167, v167\n"
    "v_exp_f32 v168, v168\n"
    "v_exp_f32 v169, v169\n"
    "v_exp_f32 v170, v170\n"
    "v_exp_f32 v171, v171\n"
    "v_pk_add_f32 v[230:231], v[160:161], v[162:163]\n"
    "v_pk_add_f32 v[232:233], v[164:165], v[166:167]\n"
    "v_pk_add_f32 v[234:235], v[168:169], v[170:171]\n"
    "v_pk_add_f32 v[232:233], v[232:233], v[234:235]\n"
    "v_add_f32 v228, v230, v231\n"
    "v_add_f32 v229, v232, v233\n"
    "v_mov_b32 v238, v228\n"
    "v_mov_b32 v239, v229\n"
    "s_nop 0\n"
    "v_permlane16_swap_b32 v228, v238\n"
    "v_permlane16_swap_b32 v229, v239\n"
    "v_add_f32 v228, v228, v238\n"
    "v_add_f32 v229, v229, v239\n"
    "v_mov_b32 v238, v228\n"
    "v_mov_b32 v239, v229\n"
    "s_nop 0\n"
    "v_permlane32_swap_b32 v228, v238\n"
    "v_permlane32_swap_b32 v229, v239\n"
    "v_add_f32 v228, v228, v238\n"
    "v_add_f32 v229, v229, v239\n"
    "v_add_u32 v11, v12, v5\n"
    "v_cmp_gt_i32 vcc, s77, v11\n"
    "s_nop 0\n"
    "s_or_b64 exec, vcc, s[20:21]\n"
    "buffer_atomic_smax v12, v4, s[36:39], 0 offen sc0 sc1\n"
    "s_mov_b64 exec, -1\n"
    "s_waitcnt vmcnt(1)\n"
    "v_add_u32 v11, v8, v5\n"
    "v_cmp_gt_i32 vcc, s77, v11\n"
    "s_nop 0\n"
    "s_and_b32 s71, vcc_lo, 0xff\n"
    "s_cbranch_scc1 .Latt_waitr_%=\n"
    ".Latt_gor_%=:\n"
    "buffer_load_dwordx4 v[128:131], v1, s[36:39], s73 offen sc1\n"
    "buffer_load_dwordx4 v[132:135], v1, s[36:39], s73 offen offset:1536 sc1\n"
    "buffer_load_dwordx4 v[136:139], v1, s[36:39], s74 offen sc1\n"
    "buffer_load_dwordx4 v[140:143], v1, s[36:39], s74 offen offset:1536 sc1\n"
    "buffer_load_dwordx4 v[144:147], v0, s[36:39], s73 offen offset:512 sc1\n"
    "buffer_load_dwordx4 v[148:151], v0, s[36:39], s73 offen offset:2048 sc1\n"
    "buffer_load_dwordx4 v[152:155], v0, s[36:39], s74 offen offset:512 sc1\n"
    "buffer_load_dwordx4 v[156:159], v0, s[36:39], s74 offen offset:2048 sc1\n"
    "s_and_b32 s71, vcc_lo, 0xf00\n"
    "s_cselect_b32 s70, 0, 1\n"
    "s_cselect_b32 s71, 0x80000000, 0\n"
    "v_or_b32 v7, s71, v0\n"
    "s_nop 15\n"
    "buffer_load_dwordx4 v[16:19], v7, s[36:39], s6 offen sc1\n"
    "s_nop 15\n"
    "buffer_load_dwordx4 v[20:23], v7, s[36:39], s6 offen offset:1024 sc1\n"
    "s_waitcnt vmcnt(9)\n"
    "v_mfma_f32_16x16x32_bf16 v[172:175], v[128:131], v[48:51], 0\n"
    "v_mfma_f32_16x16x32_bf16 v[176:179], v[128:131], v[52:55], 0\n"
    "s_waitcnt vmcnt(8)\n"
    "v_mfma_f32_16x16x32_bf16 v[172:175], v[132:135], v[56:59], v[172:175]\n"
    "v_mfma_f32_16x16x32_bf16 v[176:179], v[132:135], v[60:63], v[176:179]\n"
    "s_waitcnt vmcnt(7)\n"
    "v_mfma_f32_16x16x32_bf16 v[172:175], v[136:139], v[64:67], v[172:175]\n"
    "v_mfma_f32_16x16x32_bf16 v[176:179], v[136:139], v[68:71], v[176:179]\n"
    "s_waitcnt vmcnt(6)\n"
    "v_mfma_f32_16x16x32_bf16 v[172:175], v[140:143], v[72:75], v[172:175]\n"
    "v_mfma_f32_16x16x32_bf16 v[176:179], v[140:143], v[76:79], v[176:179]\n"
    "s_waitcnt vmcnt(2)\n"
    "v_mov_b64 v[80:81], v[144:145]\n"
    "v_mov_b64 v[86:87], v[146:147]\n"
    "v_mov_b64 v[92:93], v[148:149]\n"
    "v_mov_b64 v[98:99], v[150:151]\n"
    "v_mov_b64 v[104:105], v[152:153]\n"
    "v_mov_b64 v[110:111], v[154:155]\n"
    "v_mov_b64 v[116:117], v[156:157]\n"
    "v_mov_b64 v[122:123], v[158:159]\n"
    "v_max3_f32 v230, v172, v173, v174\n"
    "v_max3_f32 v232, v176, v177, v178\n"
    "v_max3_f32 v230, v230, v175, v224\n"
    "v_max3_f32 v232, v232, v179, v226\n"
    "v_mov_b32 v238, v230\n"
    "v_mov_b32 v239, v232\n"
    "s_nop 0\n"
    "v_permlane16_swap_b32 v230, v238\n"
    "v_permlane16_swap_b32 v232, v239\n"
    "v_max_f32 v230, v230, v238\n"
    "v_max_f32 v232, v232, v239\n"
    "v_mul_f32 v230, 0xbe0293ee, v230\n"
    "v_mul_f32 v232, 0xbe0293ee, v232\n"
    "v_pk_fma_f32 v[172:173], v[172:173], s[18:19], v[230:231] op_sel_hi:[1,0,0]\n"
    "v_pk_fma_f32 v[174:175], v[174:175], s[18:19], v[230:231] op_sel_hi:[1,0,0]\n"
    "v_pk_fma_f32 v[176:177], v[176:177], s[18:19], v[232:233] op_sel_hi:[1,0,0]\n"
    "v_pk_fma_f32 v[178:179], v[178:179], s[18:19], v[232:233] op_sel_hi:[1,0,0]\n"
    "v_fma_f32 v224, v224, s18, v230\n"
    "v_fma_f32 v226, v226, s18, v232\n"
    "v_exp_f32 v172, v172\n"
    "v_exp_f32 v173, v173\n"
    "v_exp_f32 v174, v174\n"
    "v_exp_f32 v175, v175\n"
    "buffer_load_dwordx4 v[24:27], v7, s[36:39], s6 offen offset:2048 sc1\n"
    "v_exp_f32 v176, v176\n"
    "v_exp_f32 v177, v177\n"
    "v_exp_f32 v178, v178\n"
    "v_exp_f32 v179, v179\n"
    "buffer_load_dwordx4 v[28:31], v7, s[36:39], s6 offen offset:3072 sc1\n"
    "v_exp_f32 v224, v224\n"
    "v_exp_f32 v226, v226\n"
    "v_pk_add_f32 v[230:231], v[172:173], v[174:175]\n"
    "v_pk_add_f32 v[232:233], v[176:177], v[178:179]\n"
    "v_add_f32 v230, v230, v231\n"
    "v_add_f32 v232, v232, v233\n"
    "v_mov_b32 v238, v230\n"
    "v_mov_b32 v239, v232\n"
    "s_nop 0\n"
    "v_permlane16_swap_b32 v230, v238\n"
    "v_permlane16_swap_b32 v232, v239\n"
    "v_add_f32 v230, v230, v238\n"
    "v_add_f32 v232, v232, v239\n"
    "v_fmac_f32 v230, v228, v224\n"
    "v_fmac_f32 v232, v229, v226\n"
    "v_rcp_f32 v230, v230\n"
    "v_rcp_f32 v232, v232\n"
    "buffer_load_dwordx4 v[32:35], v7, s[36:39], s7 offen sc1\n"
    "v_mul_f32 v224, v224, v230\n"
    "v_mul_f32 v226, v226, v232\n"
    "v_pk_mul_f32 v[164:165], v[164:165], v[226:227] op_sel_hi:[1,0]\n"
    "v_pk_mul_f32 v[166:167], v[166:167], v[226:227] op_sel_hi:[1,0]\n"
    "v_pk_mul_f32 v[168:169], v[168:169], v[226:227] op_sel_hi:[1,0]\n"
    "v_pk_mul_f32 v[170:171], v[170:171], v[226:227] op_sel_hi:[1,0]\n"
    "v_pk_mul_f32 v[176:177], v[176:177], v[232:233] op_sel_hi:[1,0]\n"
    "v_pk_mul_f32 v[178:179], v[178:179], v[232:233] op_sel_hi:[1,0]\n"
    "v_pk_mul_f32 v[172:173], v[172:173], v[230:231] op_sel_hi:[1,0]\n"
    "v_pk_mul_f32 v[174:175], v[174:175], v[230:231] op_sel_hi:[1,0]\n"
    "v_pk_mul_f32 v[160:161], v[160:161], v[224:225] op_sel_hi:[1,0]\n"
    "v_pk_mul_f32 v[162:163], v[162:163], v[224:225] op_sel_hi:[1,0]\n"
    "v_cvt_pk_bf16_f32 v216, v164, v165\n"
    "v_cvt_pk_bf16_f32 v217, v166, v167\n"
    "v_cvt_pk_bf16_f32 v218, v168, v169\n"
    "v_cvt_pk_bf16_f32 v219, v170, v171\n"
    "v_cvt_pk_bf16_f32 v220, v176, v177\n"
    "v_cvt_pk_bf16_f32 v221, v178, v179\n"
    "v_cvt_pk_bf16_f32 v212, v172, v173\n"
    "v_cvt_pk_bf16_f32 v213, v174, v175\n"
    "v_cvt_pk_bf16_f32 v214, v160, v161\n"
    "v_cvt_pk_bf16_f32 v215, v162, v163\n"
    "v_mfma_f32_16x16x32_bf16 v[188:191], v[82:85], v[216:219], 0\n"
    "v_mfma_f32_16x16x32_bf16 v[192:195], v[88:91], v[216:219], 0\n"
    "v_mfma_f32_16x16x32_bf16 v[180:183], v[80:83], v[212:215], 0\n"
    "v_mfma_f32_16x16x32_bf16 v[184:187], v[86:89], v[212:215], 0\n"
    "v_mfma_f32_16x16x16_bf16 v[188:191], v[80:81], v[220:221], v[188:191]\n"
    "v_mfma_f32_16x16x16_bf16 v[192:195], v[86:87], v[220:221], v[192:195]\n"
    "v_mfma_f32_16x16x32_bf16 v[204:207], v[94:97], v[216:219], 0\n"
    "v_mfma_f32_16x16x32_bf16 v[208:211], v[100:103], v[216:219], 0\n"
    "v_cvt_pk_bf16_f32 v180, v180, v181\n"
    "v_cvt_pk_bf16_f32 v181, v182, v183\n"
    "v_mfma_f32_16x16x32_bf16 v[196:199], v[92:95], v[212:215], 0\n"
    "v_cvt_pk_bf16_f32 v182, v184, v185\n"
    "v_cvt_pk_bf16_f32 v183, v186, v187\n"
    "v_mfma_f32_16x16x32_bf16 v[200:203], v[98:101], v[212:215], 0\n"
    "buffer_store_dwordx4 v[180:183], v0, s[36:39], s75 offen sc1\n"
    "v_cvt_pk_bf16_f32 v188, v188, v189\n"
    "v_mfma_f32_16x16x16_bf16 v[204:207], v[92:93], v[220:221], v[204:207]\n"
    "v_cvt_pk_bf16_f32 v189, v190, v191\n"
    "v_cvt_pk_bf16_f32 v190, v192, v193\n"
    "v_mfma_f32_16x16x16_bf16 v[208:211], v[98:99], v[220:221], v[208:211]\n"
    "v_cvt_pk_bf16_f32 v191, v194, v195\n"
    "buffer_store_dwordx4 v[188:191], v0, s[36:39], s75 offen offset:1024 sc1\n"
    "s_nop 1\n"
    "v_mfma_f32_16x16x32_bf16 v[188:191], v[106:109], v[216:219], 0\n"
    "v_mfma_f32_16x16x32_bf16 v[192:195], v[112:115], v[216:219], 0\n"
    "v_cvt_pk_bf16_f32 v196, v196, v197\n"
    "v_cvt_pk_bf16_f32 v197, v198, v199\n"
    "v_mfma_f32_16x16x32_bf16 v[180:183], v[104:107], v[212:215], 0\n"
    "v_cvt_pk_bf16_f32 v198, v200, v201\n"
    "v_cvt_pk_bf16_f32 v199, v202, v203\n"
    "v_mfma_f32_16x16x32_bf16 v[184:187], v[110:113], v[212:215], 0\n"
    "buffer_store_dwordx4 v[196:199], v0, s[36:39], s75 offen offset:2048 sc1\n"
    "v_cvt_pk_bf16_f32 v204, v204, v205\n"
    "v_mfma_f32_16x16x16_bf16 v[188:191], v[104:105], v[220:221], v[188:191]\n"
    "v_cvt_pk_bf16_f32 v205, v206, v207\n"
    "v_cvt_pk_bf16_f32 v206, v208, v209\n"
    "v_mfma_f32_16x16x16_bf16 v[192:195], v[110:111], v[220:221], v[192:195]\n"
    "v_cvt_pk_bf16_f32 v207, v210, v211\n"
    "buffer_store_dwordx4 v[204:207], v0, s[36:39], s75 offen offset:3072 sc1\n"
    "s_nop 1\n"
    "v_mfma_f32_16x16x32_bf16 v[204:207], v[118:121], v[216:219], 0\n"
    "v_mfma_f32_16x16x32_bf16 v[208:211], v[124:127], v[216:219], 0\n"
    "v_cvt_pk_bf16_f32 v180, v180, v181\n"
    "v_cvt_pk_bf16_f32 v181, v182, v183\n"
    "v_mfma_f32_16x16x32_bf16 v[196:199], v[116:119], v[212:215], 0\n"
    "v_cvt_pk_bf16_f32 v182, v184, v185\n"
    "v_cvt_pk_bf16_f32 v183, v186, v187\n"
    "v_mfma_f32_16x16x32_bf16 v[200:203], v[122:125], v[212:215], 0\n"
    "buffer_store_dwordx4 v[180:183], v0, s[36:39], s76 offen sc1\n"
    "v_cvt_pk_bf16_f32 v188, v188, v189\n"
    "v_mfma_f32_16x16x16_bf16 v[204:207], v[116:117], v[220:221], v[204:207]\n"
    "v_cvt_pk_bf16_f32 v189, v190, v191\n"
    "v_cvt_pk_bf16_f32 v190, v192, v193\n"
    "v_mfma_f32_16x16x16_bf16 v[208:211], v[122:123], v[220:221], v[208:211]\n"
    "v_cvt_pk_bf16_f32 v191, v194, v195\n"
    "buffer_store_dwordx4 v[188:191], v0, s[36:39], s76 offen offset:1024 sc1\n"
    "v_cvt_pk_bf16_f32 v196, v196, v197\n"
    "v_cvt_pk_bf16_f32 v197, v198, v199\n"
    "v_cvt_pk_bf16_f32 v198, v200, v201\n"
    "v_cvt_pk_bf16_f32 v199, v202, v203\n"
    "buffer_store_dwordx4 v[196:199], v0, s[36:39], s76 offen offset:2048 sc1\n"
    "v_cvt_pk_bf16_f32 v204, v204, v205\n"
    "v_cvt_pk_bf16_f32 v205, v206, v207\n"
    "v_cvt_pk_bf16_f32 v206, v208, v209\n"
    "v_cvt_pk_bf16_f32 v207, v210, v211\n"
    "buffer_store_dwordx4 v[204:207], v0, s[36:39], s76 offen offset:3072 sc1\n"
    "s_nop 13\n"
    "buffer_load_dwordx4 v[36:39], v7, s[36:39], s7 offen offset:1024 sc1\n"
    "s_nop 13\n"
    "buffer_load_dwordx4 v[40:43], v7, s[36:39], s7 offen offset:2048 sc1\n"
    "s_nop 13\n"
    "buffer_load_dwordx4 v[44:47], v7, s[36:39], s7 offen offset:3072 sc1\n"
    "s_nop 13\n"
    "buffer_load_dwordx4 v[48:51], v7, s[36:39], s4 offen sc1\n"
    "s_nop 13\n"
    "buffer_load_dwordx4 v[52:55], v7, s[36:39], s4 offen offset:1024 sc1\n"
    "v_mov_b32 v9, s77\n"
    "s_waitcnt vmcnt(5)\n"
    "s_mov_b64 exec, 15\n"
    "buffer_atomic_smax v9, v2, s[36:39], 0 offen sc1\n"
    "s_mov_b64 exec, -1\n"
    "buffer_load_dwordx4 v[56:59], v7, s[36:39], s4 offen offset:2048 sc1\n"
    "buffer_load_dwordx4 v[60:63], v7, s[36:39], s4 offen offset:3072 sc1\n"
    "buffer_load_dwordx4 v[64:67], v7, s[36:39], s5 offen sc1\n"
    "buffer_load_dwordx4 v[68:71], v7, s[36:39], s5 offen offset:1024 sc1\n"
    "buffer_load_dwordx4 v[72:75], v7, s[36:39], s5 offen offset:2048 sc1\n"
    "buffer_load_dwordx4 v[76:79], v7, s[36:39], s5 offen offset:3072 sc1\n"
    "buffer_load_dwordx4 v[82:85], v7, s[36:39], s8 offen sc1\n"
    "buffer_load_dwordx4 v[88:91], v7, s[36:39], s8 offen offset:1024 sc1\n"
    "buffer_load_dwordx4 v[94:97], v7, s[36:39], s8 offen offset:2048 sc1\n"
    "buffer_load_dwordx4 v[100:103], v7, s[36:39], s8 offen offset:3072 sc1\n"
    "buffer_load_dwordx4 v[106:109], v7, s[36:39], s9 offen sc1\n"
    "buffer_load_dwordx4 v[112:115], v7, s[36:39], s9 offen offset:1024 sc1\n"
    "buffer_load_dwordx4 v[118:121], v7, s[36:39], s9 offen offset:2048 sc1\n"
    "buffer_load_dwordx4 v[124:127], v7, s[36:39], s9 offen offset:3072 sc1\n"
    "s_add_u32 s68, s68, 1\n"
    "s_add_u32 s77, s77, 2\n"
    "s_cmp_lg_u32 s70, 0\n"
    "s_cbranch_scc1 .Latt_top_%=\n"
    ".Latt_LOOPE_%=:\n"
    "s_cmp_lg_u32 s68, 64\n"
    "s_cbranch_scc1 .Latt_waitp_%=\n"
    "s_waitcnt vmcnt(0)\n"
    "s_branch .Latt_FIN_%=\n"
    ".Latt_waitp_%=:\n"
    "s_sub_u32 s72, s77, 2\n"
    "v_add_u32 v11, v8, v5\n"
    "v_cmp_gt_i32 vcc, s72, v11\n"
    "s_nop 0\n"
    "s_or_b64 exec, vcc, s[20:21]\n"
    "buffer_atomic_smax v8, v4, s[36:39], 0 offen sc0 sc1\n"
    "s_mov_b64 exec, -1\n"
    "s_waitcnt vmcnt(0)\n"
    "v_add_u32 v11, v8, v5\n"
    "v_cmp_gt_i32 vcc, s72, v11\n"
    "s_cbranch_vccz .Latt_gop_%=\n"
    "s_sleep 3\n"
    "s_sub_u32 s69, s69, 1\n"
    "s_cbranch_scc0 .Latt_waitp_%=\n"
    "s_mov_b32 s69, 0\n"
    ".Latt_gop_%=:\n"
    "s_and_b32 s4, s68, 7\n"
    "s_mul_i32 s4, s4, 4194304\n"
    "s_add_u32 s4, s4, s52\n"
    "s_add_u32 s5, s4, 4096\n"
    "s_add_u32 s6, s4, 16384\n"
    "s_add_u32 s7, s4, 20480\n"
    "s_add_u32 s8, s4, 32768\n"
    "s_add_u32 s9, s4, 36864\n"
    "buffer_load_dwordx4 v[16:19], v0, s[36:39], s6 offen sc1\n"
    "buffer_load_dwordx4 v[20:23], v0, s[36:39], s6 offen offset:1024 sc1\n"
    "buffer_load_dwordx4 v[24:27], v0, s[36:39], s6 offen offset:2048 sc1\n"
    "buffer_load_dwordx4 v[28:31], v0, s[36:39], s6 offen offset:3072 sc1\n"
    "buffer_load_dwordx4 v[32:35], v0, s[36:39], s7 offen sc1\n"
    "buffer_load_dwordx4 v[36:39], v0, s[36:39], s7 offen offset:1024 sc1\n"
    "buffer_load_dwordx4 v[40:43], v0, s[36:39], s7 offen offset:2048 sc1\n"
    "buffer_load_dwordx4 v[44:47], v0, s[36:39], s7 offen offset:3072 sc1\n"
    "buffer_load_dwordx4 v[48:51], v0, s[36:39], s4 offen sc1\n"
    "buffer_load_dwordx4 v[52:55], v0, s[36:39], s4 offen offset:1024 sc1\n"
    "buffer_load_dwordx4 v[56:59], v0, s[36:39], s4 offen offset:2048 sc1\n"
    "buffer_load_dwordx4 v[60:63], v0, s[36:39], s4 offen offset:3072 sc1\n"
    "buffer_load_dwordx4 v[64:67], v0, s[36:39], s5 offen sc1\n"
    "buffer_load_dwordx4 v[68:71], v0, s[36:39], s5 offen offset:1024 sc1\n"
    "buffer_load_dwordx4 v[72:75], v0, s[36:39], s5 offen offset:2048 sc1\n"
    "buffer_load_dwordx4 v[76:79], v0, s[36:39], s5 offen offset:3072 sc1\n"
    "buffer_load_dwordx4 v[82:85], v0, s[36:39], s8 offen sc1\n"
    "buffer_load_dwordx4 v[88:91], v0, s[36:39], s8 offen offset:1024 sc1\n"
    "buffer_load_dwordx4 v[94:97], v0, s[36:39], s8 offen offset:2048 sc1\n"
    "buffer_load_dwordx4 v[100:103], v0, s[36:39], s8 offen offset:3072 sc1\n"
    "buffer_load_dwordx4 v[106:109], v0, s[36:39], s9 offen sc1\n"
    "buffer_load_dwordx4 v[112:115], v0, s[36:39], s9 offen offset:1024 sc1\n"
    "buffer_load_dwordx4 v[118:121], v0, s[36:39], s9 offen offset:2048 sc1\n"
    "buffer_load_dwordx4 v[124:127], v0, s[36:39], s9 offen offset:3072 sc1\n"
    "s_waitcnt vmcnt(8)\n"
    "s_branch .Latt_top_%=\n"
    ".Latt_waitr_%=:\n"
    "s_or_b64 exec, vcc, s[20:21]\n"
    "buffer_atomic_smax v8, v4, s[36:39], 0 offen sc0 sc1\n"
    "s_mov_b64 exec, -1\n"
    "s_sleep 3\n"
    "s_waitcnt vmcnt(1)\n"
    "v_add_u32 v11, v12, v5\n"
    "v_cmp_gt_i32 vcc, s77, v11\n"
    "s_nop 0\n"
    "s_and_b32 s71, vcc_lo, 0xff\n"
    "s_cbranch_scc0 .Latt_gor_%=\n"
    "s_or_b64 exec, vcc, s[20:21]\n"
    "buffer_atomic_smax v12, v4, s[36:39], 0 offen sc0 sc1\n"
    "s_mov_b64 exec, -1\n"
    "s_sleep 3\n"
    "s_waitcnt vmcnt(1)\n"
    "v_add_u32 v11, v8, v5\n"
    "v_cmp_gt_i32 vcc, s77, v11\n"
    "s_nop 0\n"
    "s_and_b32 s71, vcc_lo, 0xff\n"
    "s_cbranch_scc0 .Latt_gor_%=\n"
    "s_sub_u32 s69, s69, 1\n"
    "s_cbranch_scc0 .Latt_waitr_%=\n"
    ".Latt_deadr_%=:\n"
    "s_mov_b32 s69, 0\n"
    "s_branch .Latt_gor_%=\n"
    ".Latt_FIN_%=:\n"
  );
}

template <int MODE>
DEVI void red_args(const Params& p, int L, int q, u32 dbg, u64 tag, v16u& sa, v16u& sb, v16u& va, v16u& vb) {
  const int tid = threadIdx.x, lane = tid & 63, w = (int)rfl(tid >> 6);
  const int c = lane & 15, g = lane >> 4;
  const bool rowres = MODE == 0 || (MODE == 1 && L == 0);
  const bool fin = MODE == 2 && L == 1;
  const v4u ws = make_srd(p.ws, WS_END), rx = make_srd(p.x, 2048 * 1152), ro = make_srd(p.out, 2048 * 1152);
  u32 ft = OOB;
  if (lane < 32) {
    if (MODE == 1) { if (lane < 24) ft = mbox(L, lane / 6, SL_UP + lane % 6, 4 * q + w); }
    else if (!fin) ft = mbox(MODE == 0 ? 0 : 1, lane >> 3, SL_P + (lane & 7), (MODE == 0 ? 16 * L : 0) + 4 * q + w);
  } else if (MODE != 0) {
    if (lane < 44) ft = mbox(L, (lane - 32) / 3, (MODE == 1 ? SL_WO : SL_DOWN) + (lane - 32) % 3, (MODE == 1 ? 8 : 24) + 4 * q + w);
    else if (lane == 44 && !rowres) ft = MODE == 1 ? mbox(0, q, SL_REDF, 48 + w) : mbox(L, q, SL_REDA, 48 + w);
  }
  const unsigned long long fm = __ballot(ft != OOB);
  sa = cat4(ws, rowres ? rx : ws, fin ? ro : ws, (v4u){(u32)(2 * tag), (u32)((2 * tag) >> 32), 0u, 0u});
  sb = (v16u){pgb(L, MODE == 1 ? B_PA : B_PF),
              rowres ? 0u : (MODE == 1 ? pgb(1, B_X) : pgb(L, B_X1)), rowres ? 36864u : SLOT, (u32)(rowres ? 63 : (MODE == 1 ? RX2 - 1 : RX - 1)), rowres ? 128u : 1024u,
              MODE == 1 ? pgb(L, B_X1) : pgb(1, B_X), (u32)((MODE == 0 || fin) ? 63 : (MODE == 1 ? RX - 1 : RX2 - 1)),
              fin ? 0u : (MODE == 0 ? OFF_XN0 : (MODE == 1 ? pgb(L, B_XN2) : pgb(1, B_XN))), 4096u,
              rowres ? 0u : ~0u, dbg + w * 64, fin ? 0u : ~0u, (u32)fm, (u32)(fm >> 32), (u32)(L == 0 ? 0 : (L == 1 ? LN_B : LN_C)), (u32)(L == 0 ? LN_B - 1 : (L == 1 ? LN_C - 1 : 63))};
  const int need = w == 0 ? 0x10F : (w == 1 ? 0x23F : (w == 2 ? 0x4F0 : 0xF00));
  const bool in = MODE != 0 && lane < 48 && ((need >> (lane % 12)) & 1);
  const bool back = MODE != 0 && lane >= 48 && lane < 52 && !fin;
  auto frag = [&](int I) { return kb_off(2 * (I >> 1)) + (u32)((I & 1) * 512 + (q >> 1) * 1024 + (c >> 3) * 256 + (8 * (q & 1) + (c & 7)) * 16); };
  auto rowm = [&](int I) { return (u32)((8 * q + (c & 7)) * 1152 + (2 * I + (c >> 3)) * 16); };
  const int I0 = 8 * w + 2 * (g & 1) + (g >> 1);
  const u32 pv = (u32)(q * CHUNK + w * 2048 + lane * 16), pv8 = (u32)(4 * CHUNK + (4 * q + w) * 256 + lane * 16);
  va[0] = lane == 63 ? OOB : mbox(L, q, MODE == 1 ? SL_REDA : SL_REDF, lane);
  va[1] = (u32)(in ? -(q >> 1) : (back ? 2 * (MODE == 1 ? RX : RX2) : 1000));
  va[2] = (u32)g * 5 * CHUNK + frag(8 * w);
  va[3] = (u32)g * 5 * CHUNK + frag(32 + w);
  va[4] = fin ? (u32)(q * 9216 + tid * 16) : frag(I0);
  va[5] = fin ? (w == 0 ? (u32)(q * 9216 + 8192 + tid * 16) : OOB) : (g == 0 ? frag(32 + w) : OOB);
  va[6] = rowres ? rowm(I0) : pv;
  va[7] = g == 0 ? (rowres ? rowm(32 + w) : pv8) : OOB;
  va[8] = pv;
  va[9] = g == 0 ? pv8 : OOB;
  va[10] = fin ? rowm(I0) - q * 9216 : (u32)(((c & 7) * 4 + w) * 8);
  va[11] = fin ? (u32)tid * 16 : (u32)((c & 7) * 32);
  va[12] = fin ? (g == 0 ? rowm(32 + w) - q * 9216 : (u32)(12288 + tid * 16)) : (g < 2 ? 0x3F800000u : 0u);
  va[13] = ft; va[14] = 0; va[15] = 0;
  const u32 one = 0x3F80u << (16 * (c & 1));
#pragma unroll
  for (int d = 0; d < 4; ++d) {
    const bool on = ((c & 7) >> 1) == d;
    vb[d] = (on && c < 8) ? one : 0u;
    vb[4 + d] = (on && c >= 8) ? one : 0u;
    vb[8 + d] = (on && g == 2 * (c >> 3)) ? one : 0u;
    vb[12 + d] = (on && g == 2 * (c >> 3) + 1) ? one : 0u;
  }
#pragma unroll
  for (int i = 0; i < 16; ++i) { sa[i] = rfl(sa[i]); sb[i] = rfl(sb[i]); }
}
#define RUN_RED(TEXT) asm volatile(TEXT : "+{s[36:51]}"(sa), "+{s[52:67]}"(sb), "+{v[0:15]}"(va), "+{v[16:31]}"(vb) :: CLOB_RED)
#define CLOB_RED \
  "v32", "v33", "v34", "v35", "v36", "v37", "v38", "v39", "v40", "v41", "v42", "v43", "v44", "v45", "v46", "v47", "v48", "v49", "v50", "v51", "v52", "v53", "v54", "v55", \
  "v56", "v57", "v58", "v59", "v60", "v61", "v62", "v63", "v64", "v65", "v66", "v67", "v68", "v69", "v70", "v71", "v72", "v73", "v74", "v75", "v76", "v77", "v78", "v79", \
  "v80", "v81", "v82", "v83", "v84", "v85", "v86", "v87", "v88", "v89", "v90", "v91", "v92", "v93", "v94", "v95", "v96", "v97", "v98", "v99", "v100", "v101", "v102", "v103", \
  "v104", "v105", "v106", "v107", "v108", "v109", "v110", "v111", "v112", "v113", "v114", "v115", "v116", "v117", "v118", "v119", "v120", "v121", "v122", "v123", "v124", "v125", "v126", "v127", \
  "v128", "v129", "v130", "v131", "v132", "v133", "v134", "v135", "s68", "s69", "s70", "s71", "s72", "s73", "s74", "s75", "s76", "s77", "s78", "s79", "s80", "s81", "s82", "s83", \
  "s84", "s85", "s86", "s87", "s88", "s89", "s90", "s91", "s92", "s93", "s94", "s95", "s96", "s97", "s98", "s99", "vcc", "scc", "memory"
DEVI void role_ln0(const Params& p, int grp, int q, u32 dbg, u64 tag) {
  v16u sa, sb, va, vb;
  red_args<0>(p, grp, q, dbg, tag, sa, sb, va, vb);
  RUN_RED(
    "s_nop 4\n"
    "s_mov_b32 s50, 0\n"
    "s_mov_b32 s51, 0x80000000\n"
    "s_getpc_b64 s[84:85]\n"
    ".LrLN0_PC_%=:\n"
    "s_and_b32 s85, s85, 0xffff\n"
    "s_mov_b32 s86, .LrLN0_FIN_%=-.LrLN0_PC_%=\n"
    "s_mov_b32 s87, 0x20000\n"
    "v_mbcnt_lo_u32_b32 v134, -1, 0\n"
    "v_mbcnt_hi_u32_b32 v134, -1, v134\n"
    "v_lshlrev_b32 v134, 7, v134\n"
    "buffer_load_dword v135, v134, s[84:87], 0 offen\n"
    "v_add_u32 v134, 8192, v134\n"
    "buffer_load_dword v135, v134, s[84:87], 0 offen\n"
    "s_mov_b32 s69, 0\n"
    "v_mov_b32 v122, -1\n"
    "v_mov_b32 v123, -1\n"
    "s_mov_b32 s70, 262144\n"
    "s_add_u32 s69, s66, 0\n"
    "s_and_b32 s73, s69, s55\n"
    "s_mul_i32 s73, s73, s54\n"
    "s_add_u32 s73, s73, s53\n"
    "s_add_u32 s74, s73, s56\n"
    "buffer_load_dwordx4 v[40:43], v6, s[40:43], s73 offen sc1\n"
    "buffer_load_dwordx4 v[44:47], v6, s[40:43], s74 offen sc1\n"
    "buffer_load_dwordx4 v[48:51], v7, s[40:43], s73 offen sc1\n"
    "s_add_u32 s69, s66, 1\n"
    "s_and_b32 s73, s69, s55\n"
    "s_mul_i32 s73, s73, s54\n"
    "s_add_u32 s73, s73, s53\n"
    "s_add_u32 s74, s73, s56\n"
    "buffer_load_dwordx4 v[52:55], v6, s[40:43], s73 offen sc1\n"
    "buffer_load_dwordx4 v[56:59], v6, s[40:43], s74 offen sc1\n"
    "buffer_load_dwordx4 v[60:63], v7, s[40:43], s73 offen sc1\n"
    "s_add_u32 s69, s66, 2\n"
    "s_and_b32 s73, s69, s55\n"
    "s_mul_i32 s73, s73, s54\n"
    "s_add_u32 s73, s73, s53\n"
    "s_add_u32 s74, s73, s56\n"
    "buffer_load_dwordx4 v[68:71], v6, s[40:43], s73 offen sc1\n"
    "buffer_load_dwordx4 v[72:75], v6, s[40:43], s74 offen sc1\n"
    "buffer_load_dwordx4 v[76:79], v7, s[40:43], s73 offen sc1\n"
    "s_add_u32 s69, s66, 3\n"
    "s_and_b32 s73, s69, s55\n"
    "s_mul_i32 s73, s73, s54\n"
    "s_add_u32 s73, s73, s53\n"
    "s_add_u32 s74, s73, s56\n"
    "buffer_load_dwordx4 v[124:127], v6, s[40:43], s73 offen sc1\n"
    "buffer_load_dwordx4 v[128:131], v6, s[40:43], s74 offen sc1\n"
    "buffer_load_dwordx4 v[132:135], v7, s[40:43], s73 offen sc1\n"
    "s_sub_u32 s68, s66, 1\n"
    ".LrLN0_par_%=:\n"
    "buffer_load_dwordx2 v[14:15], off, s[36:39], 0 sc1\n"
    "s_waitcnt vmcnt(0)\n"
    "v_readfirstlane_b32 s75, v14\n"
    "v_readfirstlane_b32 s76, v15\n"
    "s_xor_b32 s75, s75, s48\n"
    "s_xor_b32 s76, s76, s49\n"
    "s_cselect_b32 s76, 2, 0\n"
    "s_or_b32 s76, s76, s75\n"
    "s_cmp_lt_u32 s76, 2\n"
    "s_cbranch_scc1 .LrLN0_par1_%=\n"
    "s_sleep 4\n"
    "s_sub_u32 s70, s70, 1\n"
    "s_cbranch_scc0 .LrLN0_par_%=\n"
    "s_mov_b32 s70, 0\n"
    ".LrLN0_par1_%=:\n"
    "s_and_b32 s75, s75, 1\n"
    "s_lshl_b32 s75, s75, 13\n"
    "s_add_u32 s36, s36, s75\n"
    "s_addc_u32 s37, s37, 0\n"
    "s_and_b32 s76, s75, s61\n"
    "s_add_u32 s40, s40, s76\n"
    "s_addc_u32 s41, s41, 0\n"
    "s_and_b32 s76, s75, s63\n"
    "s_add_u32 s44, s44, s76\n"
    "s_addc_u32 s45, s45, 0\n"
    ".LrLN0_LOOPB_%=:\n"
    ".LrLN0_top_%=:\n"
    "s_add_u32 s68, s68, 1\n"
    "s_add_u32 s69, s68, 4\n"
    "s_and_b32 s75, s68, s58\n"
    "s_mul_i32 s77, s75, 81920\n"
    "s_add_u32 s77, s77, s59\n"
    "s_add_u32 s78, s77, s60\n"
    "s_and_b32 s73, s69, s55\n"
    "s_mul_i32 s73, s73, s54\n"
    "s_add_u32 s73, s73, s53\n"
    "s_add_u32 s74, s73, s56\n"
    "s_waitcnt vmcnt(27)\n"
    "v_mfma_f32_16x16x32_bf16 v[80:83], v[24:27], v[40:43], 0\n"
    "v_mfma_f32_16x16x32_bf16 v[84:87], v[28:31], v[40:43], 0\n"
    "s_waitcnt vmcnt(26)\n"
    "v_mfma_f32_16x16x32_bf16 v[88:91], v[24:27], v[44:47], 0\n"
    "v_mfma_f32_16x16x32_bf16 v[92:95], v[28:31], v[44:47], 0\n"
    "s_waitcnt vmcnt(25)\n"
    "v_mfma_f32_16x16x32_bf16 v[96:99], v[24:27], v[48:51], 0\n"
    "buffer_load_dwordx4 v[40:43], v6, s[40:43], s73 offen sc1\n"
    "buffer_load_dwordx4 v[44:47], v6, s[40:43], s74 offen sc1\n"
    "buffer_load_dwordx4 v[48:51], v7, s[40:43], s73 offen sc1\n"
    "v_pk_add_f32 v[112:113], v[80:81], v[82:83]\n"
    "v_pk_add_f32 v[114:115], v[84:85], v[86:87]\n"
    "v_pk_add_f32 v[112:113], v[112:113], v[88:89]\n"
    "v_pk_add_f32 v[114:115], v[114:115], v[90:91]\n"
    "v_pk_add_f32 v[112:113], v[112:113], v[92:93]\n"
    "v_pk_add_f32 v[114:115], v[114:115], v[94:95]\n"
    "v_pk_add_f32 v[112:113], v[112:113], v[96:97]\n"
    "v_pk_add_f32 v[114:115], v[114:115], v[98:99]\n"
    "v_pk_add_f32 v[112:113], v[112:113], v[114:115]\n"
    "v_add_f32 v112, v112, v113\n"
    "s_nop 1\n"
    "v_add_f32_dpp v14, v112, v112 row_ror:8 row_mask:0xf bank_mask:0xf\n"
    "v_add_f32_dpp v15, v112, v112 row_ror:8 row_mask:0xf bank_mask:0xf\n"
    "s_nop 1\n"
    "v_permlane16_swap_b32 v14, v15\n"
    "v_add_f32 v112, v14, v15\n"
    "v_add_f32 v14, v14, v15\n"
    "s_nop 1\n"
    "v_permlane32_swap_b32 v112, v14\n"
    "v_add_f32 v112, v112, v14\n"
    "v_mul_f32 v112, 0x3be38e39, v112\n"
    "v_mul_f32 v114, v112, v12\n"
    "v_pk_add_f32 v[80:81], v[80:81], v[112:113] op_sel_hi:[1,0] neg_lo:[0,1] neg_hi:[0,1]\n"
    "v_pk_add_f32 v[82:83], v[82:83], v[112:113] op_sel_hi:[1,0] neg_lo:[0,1] neg_hi:[0,1]\n"
    "v_pk_add_f32 v[84:85], v[84:85], v[112:113] op_sel_hi:[1,0] neg_lo:[0,1] neg_hi:[0,1]\n"
    "v_pk_add_f32 v[86:87], v[86:87], v[112:113] op_sel_hi:[1,0] neg_lo:[0,1] neg_hi:[0,1]\n"
    "v_pk_add_f32 v[88:89], v[88:89], v[112:113] op_sel_hi:[1,0] neg_lo:[0,1] neg_hi:[0,1]\n"
    "v_pk_add_f32 v[90:91], v[90:91], v[112:113] op_sel_hi:[1,0] neg_lo:[0,1] neg_hi:[0,1]\n"
    "v_pk_add_f32 v[92:93], v[92:93], v[112:113] op_sel_hi:[1,0] neg_lo:[0,1] neg_hi:[0,1]\n"
    "v_pk_add_f32 v[94:95], v[94:95], v[112:113] op_sel_hi:[1,0] neg_lo:[0,1] neg_hi:[0,1]\n"
    "v_pk_add_f32 v[96:97], v[96:97], v[114:115] op_sel_hi:[1,0] neg_lo:[0,1] neg_hi:[0,1]\n"
    "v_pk_add_f32 v[98:99], v[98:99], v[114:115] op_sel_hi:[1,0] neg_lo:[0,1] neg_hi:[0,1]\n"
    "v_pk_mul_f32 v[116:117], v[80:81], v[80:81]\n"
    "v_pk_mul_f32 v[118:119], v[82:83], v[82:83]\n"
    "v_pk_fma_f32 v[116:117], v[84:85], v[84:85], v[116:117]\n"
    "v_pk_fma_f32 v[118:119], v[86:87], v[86:87], v[118:119]\n"
    "v_pk_fma_f32 v[116:117], v[88:89], v[88:89], v[116:117]\n"
    "v_pk_fma_f32 v[118:119], v[90:91], v[90:91], v[118:119]\n"
    "v_pk_fma_f32 v[116:117], v[92:93], v[92:93], v[116:117]\n"
    "v_pk_fma_f32 v[118:119], v[94:95], v[94:95], v[118:119]\n"
    "v_pk_fma_f32 v[116:117], v[96:97], v[96:97], v[116:117]\n"
    "v_pk_fma_f32 v[118:119], v[98:99], v[98:99], v[118:119]\n"
    "v_pk_add_f32 v[116:117], v[116:117], v[118:119]\n"
    "v_add_f32 v113, v116, v117\n"
    "s_nop 1\n"
    "v_add_f32_dpp v14, v113, v113 row_ror:8 row_mask:0xf bank_mask:0xf\n"
    "v_add_f32_dpp v15, v113, v113 row_ror:8 row_mask:0xf bank_mask:0xf\n"
    "s_nop 1\n"
    "v_permlane16_swap_b32 v14, v15\n"
    "v_add_f32 v113, v14, v15\n"
    "v_add_f32 v14, v14, v15\n"
    "s_nop 1\n"
    "v_permlane32_swap_b32 v113, v14\n"
    "v_add_f32 v113, v113, v14\n"
    "ds_write_b64 v10, v[112:113]\n"
    "s_waitcnt vmcnt(17)\n"
    "s_add_u32 s75, s66, 1\n"
    "s_min_i32 s75, s75, s68\n"
    "s_sub_u32 s76, s68, 2\n"
    "s_max_i32 s75, s75, s76\n"
    "s_sub_u32 s75, s75, 1\n"
    "v_lshl_or_b32 v14, s75, 1, 1\n"
    "s_mov_b64 exec, s[64:65]\n"
    "buffer_atomic_smax v14, v13, s[36:39], 0 offen sc1\n"
    "s_mov_b64 exec, -1\n"
    "s_waitcnt lgkmcnt(0)\n"
    "s_barrier\n"
    "ds_read_b128 v[32:35], v11\n"
    "ds_read_b128 v[36:39], v11 offset:16\n"
    "v_xor_b32 v10, 256, v10\n"
    "v_xor_b32 v11, 256, v11\n"
    "s_waitcnt lgkmcnt(0)\n"
    "v_pk_add_f32 v[114:115], v[32:33], v[34:35]\n"
    "v_pk_add_f32 v[116:117], v[36:37], v[38:39]\n"
    "v_pk_add_f32 v[114:115], v[114:115], v[116:117]\n"
    "v_fmac_f32 v32, 0xbe800000, v114\n"
    "v_fmac_f32 v34, 0xbe800000, v114\n"
    "v_fmac_f32 v36, 0xbe800000, v114\n"
    "v_fmac_f32 v38, 0xbe800000, v114\n"
    "v_fmac_f32 v112, 0xbe800000, v114\n"
    "v_mul_f32 v116, v32, v32\n"
    "v_fmac_f32 v116, v34, v34\n"
    "v_fmac_f32 v116, v36, v36\n"
    "v_fmac_f32 v116, v38, v38\n"
    "v_fmac_f32 v115, 0x43100000, v116\n"
    "v_mov_b32 v116, 0x3727c5ac\n"
    "v_fmac_f32 v116, 0x3ae38e39, v115\n"
    "v_rsq_f32 v116, v116\n"
    "s_nop 0\n"
    "v_mul_f32 v114, v112, v116\n"
    "v_pk_fma_f32 v[80:81], v[80:81], v[116:117], v[114:115] op_sel_hi:[1,0,0]\n"
    "v_pk_fma_f32 v[82:83], v[82:83], v[116:117], v[114:115] op_sel_hi:[1,0,0]\n"
    "v_pk_fma_f32 v[84:85], v[84:85], v[116:117], v[114:115] op_sel_hi:[1,0,0]\n"
    "v_pk_fma_f32 v[86:87], v[86:87], v[116:117], v[114:115] op_sel_hi:[1,0,0]\n"
    "v_pk_fma_f32 v[88:89], v[88:89], v[116:117], v[114:115] op_sel_hi:[1,0,0]\n"
    "v_pk_fma_f32 v[90:91], v[90:91], v[116:117], v[114:115] op_sel_hi:[1,0,0]\n"
    "v_pk_fma_f32 v[92:93], v[92:93], v[116:117], v[114:115] op_sel_hi:[1,0,0]\n"
    "v_pk_fma_f32 v[94:95], v[94:95], v[116:117], v[114:115] op_sel_hi:[1,0,0]\n"
    "v_pk_fma_f32 v[96:97], v[96:97], v[116:117], v[114:115] op_sel_hi:[1,0,0]\n"
    "v_pk_fma_f32 v[98:99], v[98:99], v[116:117], v[114:115] op_sel_hi:[1,0,0]\n"
    "v_cvt_pk_bf16_f32 v100, v80, v81\n"
    "v_cvt_pk_bf16_f32 v101, v82, v83\n"
    "v_cvt_pk_bf16_f32 v102, v84, v85\n"
    "v_cvt_pk_bf16_f32 v103, v86, v87\n"
    "v_cvt_pk_bf16_f32 v104, v88, v89\n"
    "v_cvt_pk_bf16_f32 v105, v90, v91\n"
    "v_cvt_pk_bf16_f32 v106, v92, v93\n"
    "v_cvt_pk_bf16_f32 v107, v94, v95\n"
    "v_cvt_pk_bf16_f32 v108, v96, v97\n"
    "v_cvt_pk_bf16_f32 v109, v98, v99\n"
    "v_permlane16_swap_b32 v100, v102\n"
    "v_permlane16_swap_b32 v101, v103\n"
    "buffer_store_dwordx4 v[100:103], v4, s[44:47], s77 offen sc1\n"
    "v_permlane16_swap_b32 v104, v106\n"
    "v_permlane16_swap_b32 v105, v107\n"
    "buffer_store_dwordx4 v[104:107], v4, s[44:47], s78 offen sc1\n"
    "v_permlane16_swap_b32 v108, v110\n"
    "v_permlane16_swap_b32 v109, v111\n"
    "buffer_store_dwordx4 v[108:111], v5, s[44:47], s77 offen sc1\n"
    "s_cmp_eq_u32 s68, s66\n"
    "s_cbranch_scc1 .LrLN0_first_%=\n"
    ".LrLN0_first1_%=:\n"
    "s_add_u32 s68, s68, 1\n"
    "s_add_u32 s69, s68, 4\n"
    "s_and_b32 s75, s68, s58\n"
    "s_mul_i32 s77, s75, 81920\n"
    "s_add_u32 s77, s77, s59\n"
    "s_add_u32 s78, s77, s60\n"
    "s_and_b32 s73, s69, s55\n"
    "s_mul_i32 s73, s73, s54\n"
    "s_add_u32 s73, s73, s53\n"
    "s_add_u32 s74, s73, s56\n"
    "s_waitcnt vmcnt(27)\n"
    "v_mfma_f32_16x16x32_bf16 v[80:83], v[24:27], v[52:55], 0\n"
    "v_mfma_f32_16x16x32_bf16 v[84:87], v[28:31], v[52:55], 0\n"
    "s_waitcnt vmcnt(26)\n"
    "v_mfma_f32_16x16x32_bf16 v[88:91], v[24:27], v[56:59], 0\n"
    "v_mfma_f32_16x16x32_bf16 v[92:95], v[28:31], v[56:59], 0\n"
    "s_waitcnt vmcnt(25)\n"
    "v_mfma_f32_16x16x32_bf16 v[96:99], v[24:27], v[60:63], 0\n"
    "buffer_load_dwordx4 v[52:55], v6, s[40:43], s73 offen sc1\n"
    "buffer_load_dwordx4 v[56:59], v6, s[40:43], s74 offen sc1\n"
    "buffer_load_dwordx4 v[60:63], v7, s[40:43], s73 offen sc1\n"
    "v_pk_add_f32 v[112:113], v[80:81], v[82:83]\n"
    "v_pk_add_f32 v[114:115], v[84:85], v[86:87]\n"
    "v_pk_add_f32 v[112:113], v[112:113], v[88:89]\n"
    "v_pk_add_f32 v[114:115], v[114:115], v[90:91]\n"
    "v_pk_add_f32 v[112:113], v[112:113], v[92:93]\n"
    "v_pk_add_f32 v[114:115], v[114:115], v[94:95]\n"
    "v_pk_add_f32 v[112:113], v[112:113], v[96:97]\n"
    "v_pk_add_f32 v[114:115], v[114:115], v[98:99]\n"
    "v_pk_add_f32 v[112:113], v[112:113], v[114:115]\n"
    "v_add_f32 v112, v112, v113\n"
    "s_nop 1\n"
    "v_add_f32_dpp v14, v112, v112 row_ror:8 row_mask:0xf bank_mask:0xf\n"
    "v_add_f32_dpp v15, v112, v112 row_ror:8 row_mask:0xf bank_mask:0xf\n"
    "s_nop 1\n"
    "v_permlane16_swap_b32 v14, v15\n"
    "v_add_f32 v112, v14, v15\n"
    "v_add_f32 v14, v14, v15\n"
    "s_nop 1\n"
    "v_permlane32_swap_b32 v112, v14\n"
    "v_add_f32 v112, v112, v14\n"
    "v_mul_f32 v112, 0x3be38e39, v112\n"
    "v_mul_f32 v114, v112, v12\n"
    "v_pk_add_f32 v[80:81], v[80:81], v[112:113] op_sel_hi:[1,0] neg_lo:[0,1] neg_hi:[0,1]\n"
    "v_pk_add_f32 v[82:83], v[82:83], v[112:113] op_sel_hi:[1,0] neg_lo:[0,1] neg_hi:[0,1]\n"
    "v_pk_add_f32 v[84:85], v[84:85], v[112:113] op_sel_hi:[1,0] neg_lo:[0,1] neg_hi:[0,1]\n"
    "v_pk_add_f32 v[86:87], v[86:87], v[112:113] op_sel_hi:[1,0] neg_lo:[0,1] neg_hi:[0,1]\n"
    "v_pk_add_f32 v[88:89], v[88:89], v[112:113] op_sel_hi:[1,0] neg_lo:[0,1] neg_hi:[0,1]\n"
    "v_pk_add_f32 v[90:91], v[90:91], v[112:113] op_sel_hi:[1,0] neg_lo:[0,1] neg_hi:[0,1]\n"
    "v_pk_add_f32 v[92:93], v[92:93], v[112:113] op_sel_hi:[1,0] neg_lo:[0,1] neg_hi:[0,1]\n"
    "v_pk_add_f32 v[94:95], v[94:95], v[112:113] op_sel_hi:[1,0] neg_lo:[0,1] neg_hi:[0,1]\n"
    "v_pk_add_f32 v[96:97], v[96:97], v[114:115] op_sel_hi:[1,0] neg_lo:[0,1] neg_hi:[0,1]\n"
    "v_pk_add_f32 v[98:99], v[98:99], v[114:115] op_sel_hi:[1,0] neg_lo:[0,1] neg_hi:[0,1]\n"
    "v_pk_mul_f32 v[116:117], v[80:81], v[80:81]\n"
    "v_pk_mul_f32 v[118:119], v[82:83], v[82:83]\n"
    "v_pk_fma_f32 v[116:117], v[84:85], v[84:85], v[116:117]\n"
    "v_pk_fma_f32 v[118:119], v[86:87], v[86:87], v[118:119]\n"
    "v_pk_fma_f32 v[116:117], v[88:89], v[88:89], v[116:117]\n"
    "v_pk_fma_f32 v[118:119], v[90:91], v[90:91], v[118:119]\n"
    "v_pk_fma_f32 v[116:117], v[92:93], v[92:93], v[116:117]\n"
    "v_pk_fma_f32 v[118:119], v[94:95], v[94:95], v[118:119]\n"
    "v_pk_fma_f32 v[116:117], v[96:97], v[96:97], v[116:117]\n"
    "v_pk_fma_f32 v[118:119], v[98:99], v[98:99], v[118:119]\n"
    "v_pk_add_f32 v[116:117], v[116:117], v[118:119]\n"
    "v_add_f32 v113, v116, v117\n"
    "s_nop 1\n"
    "v_add_f32_dpp v14, v113, v113 row_ror:8 row_mask:0xf bank_mask:0xf\n"
    "v_add_f32_dpp v15, v113, v113 row_ror:8 row_mask:0xf bank_mask:0xf\n"
    "s_nop 1\n"
    "v_permlane16_swap_b32 v14, v15\n"
    "v_add_f32 v113, v14, v15\n"
    "v_add_f32 v14, v14, v15\n"
    "s_nop 1\n"
    "v_permlane32_swap_b32 v113, v14\n"
    "v_add_f32 v113, v113, v14\n"
    "ds_write_b64 v10, v[112:113]\n"
    "s_waitcnt vmcnt(17)\n"
    "s_add_u32 s75, s66, 1\n"
    "s_min_i32 s75, s75, s68\n"
    "s_sub_u32 s76, s68, 2\n"
    "s_max_i32 s75, s75, s76\n"
    "s_sub_u32 s75, s75, 1\n"
    "v_lshl_or_b32 v14, s75, 1, 1\n"
    "s_mov_b64 exec, s[64:65]\n"
    "buffer_atomic_smax v14, v13, s[36:39], 0 offen sc1\n"
    "s_mov_b64 exec, -1\n"
    "s_waitcnt lgkmcnt(0)\n"
    "s_barrier\n"
    "ds_read_b128 v[32:35], v11\n"
    "ds_read_b128 v[36:39], v11 offset:16\n"
    "v_xor_b32 v10, 256, v10\n"
    "v_xor_b32 v11, 256, v11\n"
    "s_waitcnt lgkmcnt(0)\n"
    "v_pk_add_f32 v[114:115], v[32:33], v[34:35]\n"
    "v_pk_add_f32 v[116:117], v[36:37], v[38:39]\n"
    "v_pk_add_f32 v[114:115], v[114:115], v[116:117]\n"
    "v_fmac_f32 v32, 0xbe800000, v114\n"
    "v_fmac_f32 v34, 0xbe800000, v114\n"
    "v_fmac_f32 v36, 0xbe800000, v114\n"
    "v_fmac_f32 v38, 0xbe800000, v114\n"
    "v_fmac_f32 v112, 0xbe800000, v114\n"
    "v_mul_f32 v116, v32, v32\n"
    "v_fmac_f32 v116, v34, v34\n"
    "v_fmac_f32 v116, v36, v36\n"
    "v_fmac_f32 v116, v38, v38\n"
    "v_fmac_f32 v115, 0x43100000, v116\n"
    "v_mov_b32 v116, 0x3727c5ac\n"
    "v_fmac_f32 v116, 0x3ae38e39, v115\n"
    "v_rsq_f32 v116, v116\n"
    "s_nop 0\n"
    "v_mul_f32 v114, v112, v116\n"
    "v_pk_fma_f32 v[80:81], v[80:81], v[116:117], v[114:115] op_sel_hi:[1,0,0]\n"
    "v_pk_fma_f32 v[82:83], v[82:83], v[116:117], v[114:115] op_sel_hi:[1,0,0]\n"
    "v_pk_fma_f32 v[84:85], v[84:85], v[116:117], v[114:115] op_sel_hi:[1,0,0]\n"
    "v_pk_fma_f32 v[86:87], v[86:87], v[116:117], v[114:115] op_sel_hi:[1,0,0]\n"
    "v_pk_fma_f32 v[88:89], v[88:89], v[116:117], v[114:115] op_sel_hi:[1,0,0]\n"
    "v_pk_fma_f32 v[90:91], v[90:91], v[116:117], v[114:115] op_sel_hi:[1,0,0]\n"
    "v_pk_fma_f32 v[92:93], v[92:93], v[116:117], v[114:115] op_sel_hi:[1,0,0]\n"
    "v_pk_fma_f32 v[94:95], v[94:95], v[116:117], v[114:115] op_sel_hi:[1,0,0]\n"
    "v_pk_fma_f32 v[96:97], v[96:97], v[116:117], v[114:115] op_sel_hi:[1,0,0]\n"
    "v_pk_fma_f32 v[98:99], v[98:99], v[116:117], v[114:115] op_sel_hi:[1,0,0]\n"
    "v_cvt_pk_bf16_f32 v100, v80, v81\n"
    "v_cvt_pk_bf16_f32 v101, v82, v83\n"
    "v_cvt_pk_bf16_f32 v102, v84, v85\n"
    "v_cvt_pk_bf16_f32 v103, v86, v87\n"
    "v_cvt_pk_bf16_f32 v104, v88, v89\n"
    "v_cvt_pk_bf16_f32 v105, v90, v91\n"
    "v_cvt_pk_bf16_f32 v106, v92, v93\n"
    "v_cvt_pk_bf16_f32 v107, v94, v95\n"
    "v_cvt_pk_bf16_f32 v108, v96, v97\n"
    "v_cvt_pk_bf16_f32 v109, v98, v99\n"
    "v_permlane16_swap_b32 v100, v102\n"
    "v_permlane16_swap_b32 v101, v103\n"
    "buffer_store_dwordx4 v[100:103], v4, s[44:47], s77 offen sc1\n"
    "v_permlane16_swap_b32 v104, v106\n"
    "v_permlane16_swap_b32 v105, v107\n"
    "buffer_store_dwordx4 v[104:107], v4, s[44:47], s78 offen sc1\n"
    "v_permlane16_swap_b32 v108, v110\n"
    "v_permlane16_swap_b32 v109, v111\n"
    "buffer_store_dwordx4 v[108:111], v5, s[44:47], s77 offen sc1\n"
    "s_add_u32 s68, s68, 1\n"
    "s_add_u32 s69, s68, 4\n"
    "s_and_b32 s75, s68, s58\n"
    "s_mul_i32 s77, s75, 81920\n"
    "s_add_u32 s77, s77, s59\n"
    "s_add_u32 s78, s77, s60\n"
    "s_and_b32 s73, s69, s55\n"
    "s_mul_i32 s73, s73, s54\n"
    "s_add_u32 s73, s73, s53\n"
    "s_add_u32 s74, s73, s56\n"
    "s_waitcnt vmcnt(27)\n"
    "v_mfma_f32_16x16x32_bf16 v[80:83], v[24:27], v[68:71], 0\n"
    "v_mfma_f32_16x16x32_bf16 v[84:87], v[28:31], v[68:71], 0\n"
    "s_waitcnt vmcnt(26)\n"
    "v_mfma_f32_16x16x32_bf16 v[88:91], v[24:27], v[72:75], 0\n"
    "v_mfma_f32_16x16x32_bf16 v[92:95], v[28:31], v[72:75], 0\n"
    "s_waitcnt vmcnt(25)\n"
    "v_mfma_f32_16x16x32_bf16 v[96:99], v[24:27], v[76:79], 0\n"
    "buffer_load_dwordx4 v[68:71], v6, s[40:43], s73 offen sc1\n"
    "buffer_load_dwordx4 v[72:75], v6, s[40:43], s74 offen sc1\n"
    "buffer_load_dwordx4 v[76:79], v7, s[40:43], s73 offen sc1\n"
    "v_pk_add_f32 v[112:113], v[80:81], v[82:83]\n"
    "v_pk_add_f32 v[114:115], v[84:85], v[86:87]\n"
    "v_pk_add_f32 v[112:113], v[112:113], v[88:89]\n"
    "v_pk_add_f32 v[114:115], v[114:115], v[90:91]\n"
    "v_pk_add_f32 v[112:113], v[112:113], v[92:93]\n"
    "v_pk_add_f32 v[114:115], v[114:115], v[94:95]\n"
    "v_pk_add_f32 v[112:113], v[112:113], v[96:97]\n"
    "v_pk_add_f32 v[114:115], v[114:115], v[98:99]\n"
    "v_pk_add_f32 v[112:113], v[112:113], v[114:115]\n"
    "v_add_f32 v112, v112, v113\n"
    "s_nop 1\n"
    "v_add_f32_dpp v14, v112, v112 row_ror:8 row_mask:0xf bank_mask:0xf\n"
    "v_add_f32_dpp v15, v112, v112 row_ror:8 row_mask:0xf bank_mask:0xf\n"
    "s_nop 1\n"
    "v_permlane16_swap_b32 v14, v15\n"
    "v_add_f32 v112, v14, v15\n"
    "v_add_f32 v14, v14, v15\n"
    "s_nop 1\n"
    "v_permlane32_swap_b32 v112, v14\n"
    "v_add_f32 v112, v112, v14\n"
    "v_mul_f32 v112, 0x3be38e39, v112\n"
    "v_mul_f32 v114, v112, v12\n"
    "v_pk_add_f32 v[80:81], v[80:81], v[112:113] op_sel_hi:[1,0] neg_lo:[0,1] neg_hi:[0,1]\n"
    "v_pk_add_f32 v[82:83], v[82:83], v[112:113] op_sel_hi:[1,0] neg_lo:[0,1] neg_hi:[0,1]\n"
    "v_pk_add_f32 v[84:85], v[84:85], v[112:113] op_sel_hi:[1,0] neg_lo:[0,1] neg_hi:[0,1]\n"
    "v_pk_add_f32 v[86:87], v[86:87], v[112:113] op_sel_hi:[1,0] neg_lo:[0,1] neg_hi:[0,1]\n"
    "v_pk_add_f32 v[88:89], v[88:89], v[112:113] op_sel_hi:[1,0] neg_lo:[0,1] neg_hi:[0,1]\n"
    "v_pk_add_f32 v[90:91], v[90:91], v[112:113] op_sel_hi:[1,0] neg_lo:[0,1] neg_hi:[0,1]\n"
    "v_pk_add_f32 v[92:93], v[92:93], v[112:113] op_sel_hi:[1,0] neg_lo:[0,1] neg_hi:[0,1]\n"
    "v_pk_add_f32 v[94:95], v[94:95], v[112:113] op_sel_hi:[1,0] neg_lo:[0,1] neg_hi:[0,1]\n"
    "v_pk_add_f32 v[96:97], v[96:97], v[114:115] op_sel_hi:[1,0] neg_lo:[0,1] neg_hi:[0,1]\n"
    "v_pk_add_f32 v[98:99], v[98:99], v[114:115] op_sel_hi:[1,0] neg_lo:[0,1] neg_hi:[0,1]\n"
    "v_pk_mul_f32 v[116:117], v[80:81], v[80:81]\n"
    "v_pk_mul_f32 v[118:119], v[82:83], v[82:83]\n"
    "v_pk_fma_f32 v[116:117], v[84:85], v[84:85], v[116:117]\n"
    "v_pk_fma_f32 v[118:119], v[86:87], v[86:87], v[118:119]\n"
    "v_pk_fma_f32 v[116:117], v[88:89], v[88:89], v[116:117]\n"
    "v_pk_fma_f32 v[118:119], v[90:91], v[90:91], v[118:119]\n"
    "v_pk_fma_f32 v[116:117], v[92:93], v[92:93], v[116:117]\n"
    "v_pk_fma_f32 v[118:119], v[94:95], v[94:95], v[118:119]\n"
    "v_pk_fma_f32 v[116:117], v[96:97], v[96:97], v[116:117]\n"
    "v_pk_fma_f32 v[118:119], v[98:99], v[98:99], v[118:119]\n"
    "v_pk_add_f32 v[116:117], v[116:117], v[118:119]\n"
    "v_add_f32 v113, v116, v117\n"
    "s_nop 1\n"
    "v_add_f32_dpp v14, v113, v113 row_ror:8 row_mask:0xf bank_mask:0xf\n"
    "v_add_f32_dpp v15, v113, v113 row_ror:8 row_mask:0xf bank_mask:0xf\n"
    "s_nop 1\n"
    "v_permlane16_swap_b32 v14, v15\n"
    "v_add_f32 v113, v14, v15\n"
    "v_add_f32 v14, v14, v15\n"
    "s_nop 1\n"
    "v_permlane32_swap_b32 v113, v14\n"
    "v_add_f32 v113, v113, v14\n"
    "ds_write_b64 v10, v[112:113]\n"
    "s_waitcnt vmcnt(17)\n"
    "s_add_u32 s75, s66, 1\n"
    "s_min_i32 s75, s75, s68\n"
    "s_sub_u32 s76, s68, 2\n"
    "s_max_i32 s75, s75, s76\n"
    "s_sub_u32 s75, s75, 1\n"
    "v_lshl_or_b32 v14, s75, 1, 1\n"
    "s_mov_b64 exec, s[64:65]\n"
    "buffer_atomic_smax v14, v13, s[36:39], 0 offen sc1\n"
    "s_mov_b64 exec, -1\n"
    "s_waitcnt lgkmcnt(0)\n"
    "s_barrier\n"
    "ds_read_b128 v[32:35], v11\n"
    "ds_read_b128 v[36:39], v11 offset:16\n"
    "v_xor_b32 v10, 256, v10\n"
    "v_xor_b32 v11, 256, v11\n"
    "s_waitcnt lgkmcnt(0)\n"
    "v_pk_add_f32 v[114:115], v[32:33], v[34:35]\n"
    "v_pk_add_f32 v[116:117], v[36:37], v[38:39]\n"
    "v_pk_add_f32 v[114:115], v[114:115], v[116:117]\n"
    "v_fmac_f32 v32, 0xbe800000, v114\n"
    "v_fmac_f32 v34, 0xbe800000, v114\n"
    "v_fmac_f32 v36, 0xbe800000, v114\n"
    "v_fmac_f32 v38, 0xbe800000, v114\n"
    "v_fmac_f32 v112, 0xbe800000, v114\n"
    "v_mul_f32 v116, v32, v32\n"
    "v_fmac_f32 v116, v34, v34\n"
    "v_fmac_f32 v116, v36, v36\n"
    "v_fmac_f32 v116, v38, v38\n"
    "v_fmac_f32 v115, 0x43100000, v116\n"
    "v_mov_b32 v116, 0x3727c5ac\n"
    "v_fmac_f32 v116, 0x3ae38e39, v115\n"
    "v_rsq_f32 v116, v116\n"
    "s_nop 0\n"
    "v_mul_f32 v114, v112, v116\n"
    "v_pk_fma_f32 v[80:81], v[80:81], v[116:117], v[114:115] op_sel_hi:[1,0,0]\n"
    "v_pk_fma_f32 v[82:83], v[82:83], v[116:117], v[114:115] op_sel_hi:[1,0,0]\n"
    "v_pk_fma_f32 v[84:85], v[84:85], v[116:117], v[114:115] op_sel_hi:[1,0,0]\n"
    "v_pk_fma_f32 v[86:87], v[86:87], v[116:117], v[114:115] op_sel_hi:[1,0,0]\n"
    "v_pk_fma_f32 v[88:89], v[88:89], v[116:117], v[114:115] op_sel_hi:[1,0,0]\n"
    "v_pk_fma_f32 v[90:91], v[90:91], v[116:117], v[114:115] op_sel_hi:[1,0,0]\n"
    "v_pk_fma_f32 v[92:93], v[92:93], v[116:117], v[114:115] op_sel_hi:[1,0,0]\n"
    "v_pk_fma_f32 v[94:95], v[94:95], v[116:117], v[114:115] op_sel_hi:[1,0,0]\n"
    "v_pk_fma_f32 v[96:97], v[96:97], v[116:117], v[114:115] op_sel_hi:[1,0,0]\n"
    "v_pk_fma_f32 v[98:99], v[98:99], v[116:117], v[114:115] op_sel_hi:[1,0,0]\n"
    "v_cvt_pk_bf16_f32 v100, v80, v81\n"
    "v_cvt_pk_bf16_f32 v101, v82, v83\n"
    "v_cvt_pk_bf16_f32 v102, v84, v85\n"
    "v_cvt_pk_bf16_f32 v103, v86, v87\n"
    "v_cvt_pk_bf16_f32 v104, v88, v89\n"
    "v_cvt_pk_bf16_f32 v105, v90, v91\n"
    "v_cvt_pk_bf16_f32 v106, v92, v93\n"
    "v_cvt_pk_bf16_f32 v107, v94, v95\n"
    "v_cvt_pk_bf16_f32 v108, v96, v97\n"
    "v_cvt_pk_bf16_f32 v109, v98, v99\n"
    "v_permlane16_swap_b32 v100, v102\n"
    "v_permlane16_swap_b32 v101, v103\n"
    "buffer_store_dwordx4 v[100:103], v4, s[44:47], s77 offen sc1\n"
    "v_permlane16_swap_b32 v104, v106\n"
    "v_permlane16_swap_b32 v105, v107\n"
    "buffer_store_dwordx4 v[104:107], v4, s[44:47], s78 offen sc1\n"
    "v_permlane16_swap_b32 v108, v110\n"
    "v_permlane16_swap_b32 v109, v111\n"
    "buffer_store_dwordx4 v[108:111], v5, s[44:47], s77 offen sc1\n"
    "s_add_u32 s68, s68, 1\n"
    "s_add_u32 s69, s68, 4\n"
    "s_and_b32 s75, s68, s58\n"
    "s_mul_i32 s77, s75, 81920\n"
    "s_add_u32 s77, s77, s59\n"
    "s_add_u32 s78, s77, s60\n"
    "s_and_b32 s73, s69, s55\n"
    "s_mul_i32 s73, s73, s54\n"
    "s_add_u32 s73, s73, s53\n"
    "s_add_u32 s74, s73, s56\n"
    "s_waitcnt vmcnt(27)\n"
    "v_mfma_f32_16x16x32_bf16 v[80:83], v[24:27], v[124:127], 0\n"
    "v_mfma_f32_16x16x32_bf16 v[84:87], v[28:31], v[124:127], 0\n"
    "s_waitcnt vmcnt(26)\n"
    "v_mfma_f32_16x16x32_bf16 v[88:91], v[24:27], v[128:131], 0\n"
    "v_mfma_f32_16x16x32_bf16 v[92:95], v[28:31], v[128:131], 0\n"
    "s_waitcnt vmcnt(25)\n"
    "v_mfma_f32_16x16x32_bf16 v[96:99], v[24:27], v[132:135], 0\n"
    "buffer_load_dwordx4 v[124:127], v6, s[40:43], s73 offen sc1\n"
    "buffer_load_dwordx4 v[128:131], v6, s[40:43], s74 offen sc1\n"
    "buffer_load_dwordx4 v[132:135], v7, s[40:43], s73 offen sc1\n"
    "v_pk_add_f32 v[112:113], v[80:81], v[82:83]\n"
    "v_pk_add_f32 v[114:115], v[84:85], v[86:87]\n"
    "v_pk_add_f32 v[112:113], v[112:113], v[88:89]\n"
    "v_pk_add_f32 v[114:115], v[114:115], v[90:91]\n"
    "v_pk_add_f32 v[112:113], v[112:113], v[92:93]\n"
    "v_pk_add_f32 v[114:115], v[114:115], v[94:95]\n"
    "v_pk_add_f32 v[112:113], v[112:113], v[96:97]\n"
    "v_pk_add_f32 v[114:115], v[114:115], v[98:99]\n"
    "v_pk_add_f32 v[112:113], v[112:113], v[114:115]\n"
    "v_add_f32 v112, v112, v113\n"
    "s_nop 1\n"
    "v_add_f32_dpp v14, v112, v112 row_ror:8 row_mask:0xf bank_mask:0xf\n"
    "v_add_f32_dpp v15, v112, v112 row_ror:8 row_mask:0xf bank_mask:0xf\n"
    "s_nop 1\n"
    "v_permlane16_swap_b32 v14, v15\n"
    "v_add_f32 v112, v14, v15\n"
    "v_add_f32 v14, v14, v15\n"
    "s_nop 1\n"
    "v_permlane32_swap_b32 v112, v14\n"
    "v_add_f32 v112, v112, v14\n"
    "v_mul_f32 v112, 0x3be38e39, v112\n"
    "v_mul_f32 v114, v112, v12\n"
    "v_pk_add_f32 v[80:81], v[80:81], v[112:113] op_sel_hi:[1,0] neg_lo:[0,1] neg_hi:[0,1]\n"
    "v_pk_add_f32 v[82:83], v[82:83], v[112:113] op_sel_hi:[1,0] neg_lo:[0,1] neg_hi:[0,1]\n"
    "v_pk_add_f32 v[84:85], v[84:85], v[112:113] op_sel_hi:[1,0] neg_lo:[0,1] neg_hi:[0,1]\n"
    "v_pk_add_f32 v[86:87], v[86:87], v[112:113] op_sel_hi:[1,0] neg_lo:[0,1] neg_hi:[0,1]\n"
    "v_pk_add_f32 v[88:89], v[88:89], v[112:113] op_sel_hi:[1,0] neg_lo:[0,1] neg_hi:[0,1]\n"
    "v_pk_add_f32 v[90:91], v[90:91], v[112:113] op_sel_hi:[1,0] neg_lo:[0,1] neg_hi:[0,1]\n"
    "v_pk_add_f32 v[92:93], v[92:93], v[112:113] op_sel_hi:[1,0] neg_lo:[0,1] neg_hi:[0,1]\n"
    "v_pk_add_f32 v[94:95], v[94:95], v[112:113] op_sel_hi:[1,0] neg_lo:[0,1] neg_hi:[0,1]\n"
    "v_pk_add_f32 v[96:97], v[96:97], v[114:115] op_sel_hi:[1,0] neg_lo:[0,1] neg_hi:[0,1]\n"
    "v_pk_add_f32 v[98:99], v[98:99], v[114:115] op_sel_hi:[1,0] neg_lo:[0,1] neg_hi:[0,1]\n"
    "v_pk_mul_f32 v[116:117], v[80:81], v[80:81]\n"
    "v_pk_mul_f32 v[118:119], v[82:83], v[82:83]\n"
    "v_pk_fma_f32 v[116:117], v[84:85], v[84:85], v[116:117]\n"
    "v_pk_fma_f32 v[118:119], v[86:87], v[86:87], v[118:119]\n"
    "v_pk_fma_f32 v[116:117], v[88:89], v[88:89], v[116:117]\n"
    "v_pk_fma_f32 v[118:119], v[90:91], v[90:91], v[118:119]\n"
    "v_pk_fma_f32 v[116:117], v[92:93], v[92:93], v[116:117]\n"
    "v_pk_fma_f32 v[118:119], v[94:95], v[94:95], v[118:119]\n"
    "v_pk_fma_f32 v[116:117], v[96:97], v[96:97], v[116:117]\n"
    "v_pk_fma_f32 v[118:119], v[98:99], v[98:99], v[118:119]\n"
    "v_pk_add_f32 v[116:117], v[116:117], v[118:119]\n"
    "v_add_f32 v113, v116, v117\n"
    "s_nop 1\n"
    "v_add_f32_dpp v14, v113, v113 row_ror:8 row_mask:0xf bank_mask:0xf\n"
    "v_add_f32_dpp v15, v113, v113 row_ror:8 row_mask:0xf bank_mask:0xf\n"
    "s_nop 1\n"
    "v_permlane16_swap_b32 v14, v15\n"
    "v_add_f32 v113, v14, v15\n"
    "v_add_f32 v14, v14, v15\n"
    "s_nop 1\n"
    "v_permlane32_swap_b32 v113, v14\n"
    "v_add_f32 v113, v113, v14\n"
    "ds_write_b64 v10, v[112:113]\n"
    "s_waitcnt vmcnt(17)\n"
    "s_add_u32 s75, s66, 1\n"
    "s_min_i32 s75, s75, s68\n"
    "s_sub_u32 s76, s68, 2\n"
    "s_max_i32 s75, s75, s76\n"
    "s_sub_u32 s75, s75, 1\n"
    "v_lshl_or_b32 v14, s75, 1, 1\n"
    "s_mov_b64 exec, s[64:65]\n"
    "buffer_atomic_smax v14, v13, s[36:39], 0 offen sc1\n"
    "s_mov_b64 exec, -1\n"
    "s_waitcnt lgkmcnt(0)\n"
    "s_barrier\n"
    "ds_read_b128 v[32:35], v11\n"
    "ds_read_b128 v[36:39], v11 offset:16\n"
    "v_xor_b32 v10, 256, v10\n"
    "v_xor_b32 v11, 256, v11\n"
    "s_waitcnt lgkmcnt(0)\n"
    "v_pk_add_f32 v[114:115], v[32:33], v[34:35]\n"
    "v_pk_add_f32 v[116:117], v[36:37], v[38:39]\n"
    "v_pk_add_f32 v[114:115], v[114:115], v[116:117]\n"
    "v_fmac_f32 v32, 0xbe800000, v114\n"
    "v_fmac_f32 v34, 0xbe800000, v114\n"
    "v_fmac_f32 v36, 0xbe800000, v114\n"
    "v_fmac_f32 v38, 0xbe800000, v114\n"
    "v_fmac_f32 v112, 0xbe800000, v114\n"
    "v_mul_f32 v116, v32, v32\n"
    "v_fmac_f32 v116, v34, v34\n"
    "v_fmac_f32 v116, v36, v36\n"
    "v_fmac_f32 v116, v38, v38\n"
    "v_fmac_f32 v115, 0x43100000, v116\n"
    "v_mov_b32 v116, 0x3727c5ac\n"
    "v_fmac_f32 v116, 0x3ae38e39, v115\n"
    "v_rsq_f32 v116, v116\n"
    "s_nop 0\n"
    "v_mul_f32 v114, v112, v116\n"
    "v_pk_fma_f32 v[80:81], v[80:81], v[116:117], v[114:115] op_sel_hi:[1,0,0]\n"
    "v_pk_fma_f32 v[82:83], v[82:83], v[116:117], v[114:115] op_sel_hi:[1,0,0]\n"
    "v_pk_fma_f32 v[84:85], v[84:85], v[116:117], v[114:115] op_sel_hi:[1,0,0]\n"
    "v_pk_fma_f32 v[86:87], v[86:87], v[116:117], v[114:115] op_sel_hi:[1,0,0]\n"
    "v_pk_fma_f32 v[88:89], v[88:89], v[116:117], v[114:115] op_sel_hi:[1,0,0]\n"
    "v_pk_fma_f32 v[90:91], v[90:91], v[116:117], v[114:115] op_sel_hi:[1,0,0]\n"
    "v_pk_fma_f32 v[92:93], v[92:93], v[116:117], v[114:115] op_sel_hi:[1,0,0]\n"
    "v_pk_fma_f32 v[94:95], v[94:95], v[116:117], v[114:115] op_sel_hi:[1,0,0]\n"
    "v_pk_fma_f32 v[96:97], v[96:97], v[116:117], v[114:115] op_sel_hi:[1,0,0]\n"
    "v_pk_fma_f32 v[98:99], v[98:99], v[116:117], v[114:115] op_sel_hi:[1,0,0]\n"
    "v_cvt_pk_bf16_f32 v100, v80, v81\n"
    "v_cvt_pk_bf16_f32 v101, v82, v83\n"
    "v_cvt_pk_bf16_f32 v102, v84, v85\n"
    "v_cvt_pk_bf16_f32 v103, v86, v87\n"
    "v_cvt_pk_bf16_f32 v104, v88, v89\n"
    "v_cvt_pk_bf16_f32 v105, v90, v91\n"
    "v_cvt_pk_bf16_f32 v106, v92, v93\n"
    "v_cvt_pk_bf16_f32 v107, v94, v95\n"
    "v_cvt_pk_bf16_f32 v108, v96, v97\n"
    "v_cvt_pk_bf16_f32 v109, v98, v99\n"
    "v_permlane16_swap_b32 v100, v102\n"
    "v_permlane16_swap_b32 v101, v103\n"
    "buffer_store_dwordx4 v[100:103], v4, s[44:47], s77 offen sc1\n"
    "v_permlane16_swap_b32 v104, v106\n"
    "v_permlane16_swap_b32 v105, v107\n"
    "buffer_store_dwordx4 v[104:107], v4, s[44:47], s78 offen sc1\n"
    "v_permlane16_swap_b32 v108, v110\n"
    "v_permlane16_swap_b32 v109, v111\n"
    "buffer_store_dwordx4 v[108:111], v5, s[44:47], s77 offen sc1\n"
    "s_cmp_eq_u32 s68, s67\n"
    "s_cbranch_scc1 .LrLN0_fin_%=\n"
    "s_branch .LrLN0_top_%=\n"
    ".LrLN0_LOOPE_%=:\n"
    ".LrLN0_fin_%=:\n"
    "s_waitcnt vmcnt(0)\n"
    "s_mov_b32 s68, 63\n"
    "v_lshl_or_b32 v14, s68, 1, 1\n"
    "s_mov_b64 exec, s[64:65]\n"
    "buffer_atomic_smax v14, v13, s[36:39], 0 offen sc1\n"
    "s_mov_b64 exec, -1\n"
    "s_waitcnt vmcnt(0)\n"
    "s_barrier\n"
    "s_branch .LrLN0_FIN_%=\n"
    ".LrLN0_first_%=:\n"
    "s_waitcnt vmcnt(0)\n"
    "v_lshl_or_b32 v14, s68, 1, 1\n"
    "s_mov_b64 exec, s[64:65]\n"
    "buffer_atomic_smax v14, v13, s[36:39], 0 offen sc1\n"
    "s_mov_b64 exec, -1\n"
    "s_branch .LrLN0_first1_%=\n"
    ".LrLN0_FIN_%=:\n"
  );
}
DEVI void role_mid(v16u sa, v16u sb, v16u va, v16u vb) {
  RUN_RED(
    "s_nop 4\n"
    "s_mov_b32 s50, 0\n"
    "s_mov_b32 s51, 0x80000000\n"
    "s_getpc_b64 s[84:85]\n"
    ".LrMID_PC_%=:\n"
    "s_and_b32 s85, s85, 0xffff\n"
    "s_mov_b32 s86, .LrMID_FIN_%=-.LrMID_PC_%=\n"
    "s_mov_b32 s87, 0x20000\n"
    "v_mbcnt_lo_u32_b32 v134, -1, 0\n"
    "v_mbcnt_hi_u32_b32 v134, -1, v134\n"
    "v_lshlrev_b32 v134, 7, v134\n"
    "buffer_load_dword v135, v134, s[84:87], 0 offen\n"
    "v_add_u32 v134, 8192, v134\n"
    "buffer_load_dword v135, v134, s[84:87], 0 offen\n"
    "s_mov_b32 s69, 0\n"
    "v_mov_b32 v122, -1\n"
    "v_mov_b32 v123, -1\n"
    "s_mov_b32 s70, 262144\n"
    ".LrMID_par_%=:\n"
    "buffer_load_dwordx2 v[14:15], off, s[36:39], 0 sc1\n"
    "s_waitcnt vmcnt(0)\n"
    "v_readfirstlane_b32 s75, v14\n"
    "v_readfirstlane_b32 s76, v15\n"
    "s_xor_b32 s75, s75, s48\n"
    "s_xor_b32 s76, s76, s49\n"
    "s_cselect_b32 s76, 2, 0\n"
    "s_or_b32 s76, s76, s75\n"
    "s_cmp_lt_u32 s76, 2\n"
    "s_cbranch_scc1 .LrMID_par1_%=\n"
    "s_sleep 4\n"
    "s_sub_u32 s70, s70, 1\n"
    "s_cbranch_scc0 .LrMID_par_%=\n"
    "s_mov_b32 s70, 0\n"
    ".LrMID_par1_%=:\n"
    "s_and_b32 s75, s75, 1\n"
    "s_lshl_b32 s75, s75, 13\n"
    "s_add_u32 s36, s36, s75\n"
    "s_addc_u32 s37, s37, 0\n"
    "s_and_b32 s76, s75, s61\n"
    "s_add_u32 s40, s40, s76\n"
    "s_addc_u32 s41, s41, 0\n"
    "s_and_b32 s76, s75, s63\n"
    "s_add_u32 s44, s44, s76\n"
    "s_addc_u32 s45, s45, 0\n"
    "s_branch .LrMID_block_%=\n"
    ".LrMID_LOOPB_%=:\n"
    ".LrMID_top_%=:\n"
    "s_mov_b32 s68, s69\n"
    "s_add_u32 s69, s68, 1\n"
    "s_and_b32 s75, s68, s58\n"
    "s_mul_i32 s77, s75, 4194304\n"
    "s_add_u32 s77, s77, s59\n"
    "s_add_u32 s78, s77, s60\n"
    "s_and_b32 s71, s69, 7\n"
    "s_mul_i32 s71, s71, 4194304\n"
    "s_add_u32 s71, s71, s52\n"
    "s_add_u32 s72, s71, 4096\n"
    "s_and_b32 s73, s69, s55\n"
    "s_mul_i32 s73, s73, s54\n"
    "s_add_u32 s73, s73, s53\n"
    "s_add_u32 s74, s73, s56\n"
    "s_mul_i32 s75, s75, 4194304\n"
    "s_add_u32 s75, s75, s57\n"
    "s_add_u32 s76, s75, 1024\n"
    "s_waitcnt vmcnt(12)\n"
    "v_mfma_f32_16x16x32_bf16 v[80:83], v[16:19], v[32:35], 0\n"
    "s_waitcnt vmcnt(11)\n"
    "v_mfma_f32_16x16x32_bf16 v[80:83], v[20:23], v[36:39], v[80:83]\n"
    "s_waitcnt vmcnt(10)\n"
    "v_mfma_f32_16x16x32_bf16 v[84:87], v[16:19], v[40:43], 0\n"
    "s_waitcnt vmcnt(9)\n"
    "v_mfma_f32_16x16x32_bf16 v[84:87], v[20:23], v[44:47], v[84:87]\n"
    "s_waitcnt vmcnt(8)\n"
    "v_mfma_f32_16x16x32_bf16 v[88:91], v[16:19], v[48:51], 0\n"
    "s_waitcnt vmcnt(7)\n"
    "v_mfma_f32_16x16x32_bf16 v[88:91], v[20:23], v[52:55], v[88:91]\n"
    "s_waitcnt vmcnt(6)\n"
    "v_mfma_f32_16x16x32_bf16 v[92:95], v[16:19], v[56:59], 0\n"
    "s_waitcnt vmcnt(5)\n"
    "v_mfma_f32_16x16x32_bf16 v[92:95], v[20:23], v[60:63], v[92:95]\n"
    "s_waitcnt vmcnt(4)\n"
    "v_mfma_f32_16x16x32_bf16 v[96:99], v[16:19], v[64:67], 0\n"
    "s_waitcnt vmcnt(3)\n"
    "v_mfma_f32_16x16x32_bf16 v[80:83], v[24:27], v[68:71], v[80:83]\n"
    "v_mfma_f32_16x16x32_bf16 v[84:87], v[28:31], v[68:71], v[84:87]\n"
    "s_waitcnt vmcnt(2)\n"
    "v_mfma_f32_16x16x32_bf16 v[88:91], v[24:27], v[72:75], v[88:91]\n"
    "v_mfma_f32_16x16x32_bf16 v[92:95], v[28:31], v[72:75], v[92:95]\n"
    "s_waitcnt vmcnt(1)\n"
    "v_mfma_f32_16x16x32_bf16 v[96:99], v[24:27], v[76:79], v[96:99]\n"
    "v_pk_add_f32 v[112:113], v[80:81], v[82:83]\n"
    "v_pk_add_f32 v[114:115], v[84:85], v[86:87]\n"
    "v_pk_add_f32 v[112:113], v[112:113], v[88:89]\n"
    "v_pk_add_f32 v[114:115], v[114:115], v[90:91]\n"
    "v_pk_add_f32 v[112:113], v[112:113], v[92:93]\n"
    "v_pk_add_f32 v[114:115], v[114:115], v[94:95]\n"
    "s_nop 1\n"
    "v_pk_add_f32 v[112:113], v[112:113], v[96:97]\n"
    "v_pk_add_f32 v[114:115], v[114:115], v[98:99]\n"
    "v_pk_add_f32 v[112:113], v[112:113], v[114:115]\n"
    "v_add_f32 v112, v112, v113\n"
    "v_cvt_pk_bf16_f32 v100, v80, v81\n"
    "v_cvt_pk_bf16_f32 v101, v82, v83\n"
    "v_cvt_pk_bf16_f32 v102, v84, v85\n"
    "v_cvt_pk_bf16_f32 v103, v86, v87\n"
    "v_cvt_pk_bf16_f32 v104, v88, v89\n"
    "v_cvt_pk_bf16_f32 v105, v90, v91\n"
    "v_cvt_pk_bf16_f32 v106, v92, v93\n"
    "v_cvt_pk_bf16_f32 v107, v94, v95\n"
    "v_cvt_pk_bf16_f32 v108, v96, v97\n"
    "v_cvt_pk_bf16_f32 v109, v98, v99\n"
    "v_add_f32_dpp v14, v112, v112 row_ror:8 row_mask:0xf bank_mask:0xf\n"
    "v_add_f32_dpp v15, v112, v112 row_ror:8 row_mask:0xf bank_mask:0xf\n"
    "s_nop 1\n"
    "v_permlane16_swap_b32 v14, v15\n"
    "v_add_f32 v112, v14, v15\n"
    "v_add_f32 v14, v14, v15\n"
    "s_nop 1\n"
    "v_permlane32_swap_b32 v112, v14\n"
    "v_add_f32 v112, v112, v14\n"
    "v_mul_f32 v112, 0x3be38e39, v112\n"
    "v_mul_f32 v114, v112, v12\n"
    "v_pk_add_f32 v[80:81], v[80:81], v[112:113] op_sel_hi:[1,0] neg_lo:[0,1] neg_hi:[0,1]\n"
    "v_pk_add_f32 v[82:83], v[82:83], v[112:113] op_sel_hi:[1,0] neg_lo:[0,1] neg_hi:[0,1]\n"
    "v_pk_add_f32 v[84:85], v[84:85], v[112:113] op_sel_hi:[1,0] neg_lo:[0,1] neg_hi:[0,1]\n"
    "v_pk_add_f32 v[86:87], v[86:87], v[112:113] op_sel_hi:[1,0] neg_lo:[0,1] neg_hi:[0,1]\n"
    "v_pk_add_f32 v[88:89], v[88:89], v[112:113] op_sel_hi:[1,0] neg_lo:[0,1] neg_hi:[0,1]\n"
    "v_pk_add_f32 v[90:91], v[90:91], v[112:113] op_sel_hi:[1,0] neg_lo:[0,1] neg_hi:[0,1]\n"
    "v_pk_add_f32 v[92:93], v[92:93], v[112:113] op_sel_hi:[1,0] neg_lo:[0,1] neg_hi:[0,1]\n"
    "v_pk_add_f32 v[94:95], v[94:95], v[112:113] op_sel_hi:[1,0] neg_lo:[0,1] neg_hi:[0,1]\n"
    "v_pk_add_f32 v[96:97], v[96:97], v[114:115] op_sel_hi:[1,0] neg_lo:[0,1] neg_hi:[0,1]\n"
    "v_pk_add_f32 v[98:99], v[98:99], v[114:115] op_sel_hi:[1,0] neg_lo:[0,1] neg_hi:[0,1]\n"
    "v_pk_mul_f32 v[116:117], v[80:81], v[80:81]\n"
    "v_pk_mul_f32 v[118:119], v[82:83], v[82:83]\n"
    "v_pk_fma_f32 v[116:117], v[84:85], v[84:85], v[116:117]\n"
    "v_pk_fma_f32 v[118:119], v[86:87], v[86:87], v[118:119]\n"
    "v_pk_fma_f32 v[116:117], v[88:89], v[88:89], v[116:117]\n"
    "v_pk_fma_f32 v[118:119], v[90:91], v[90:91], v[118:119]\n"
    "v_pk_fma_f32 v[116:117], v[92:93], v[92:93], v[116:117]\n"
    "v_pk_fma_f32 v[118:119], v[94:95], v[94:95], v[118:119]\n"
    "v_pk_fma_f32 v[116:117], v[96:97], v[96:97], v[116:117]\n"
    "v_pk_fma_f32 v[118:119], v[98:99], v[98:99], v[118:119]\n"
    "v_pk_add_f32 v[116:117], v[116:117], v[118:119]\n"
    "v_add_f32 v113, v116, v117\n"
    "s_nop 1\n"
    "v_add_f32_dpp v14, v113, v113 row_ror:8 row_mask:0xf bank_mask:0xf\n"
    "v_add_f32_dpp v15, v113, v113 row_ror:8 row_mask:0xf bank_mask:0xf\n"
    "s_nop 1\n"
    "v_permlane16_swap_b32 v14, v15\n"
    "v_add_f32 v113, v14, v15\n"
    "v_add_f32 v14, v14, v15\n"
    "s_nop 1\n"
    "v_permlane32_swap_b32 v113, v14\n"
    "v_add_f32 v113, v113, v14\n"
    "ds_write_b64 v10, v[112:113]\n"
    "v_add_u32 v120, v122, v1\n"
    "v_ashrrev_i32 v120, 1, v120\n"
    "v_cmp_gt_i32 vcc, s69, v120\n"
    "s_nop 0\n"
    "s_or_b64 exec, vcc, s[50:51]\n"
    "buffer_atomic_smax v122, v0, s[36:39], 0 offen sc0 sc1\n"
    "s_mov_b64 exec, -1\n"
    "s_nop 0\n"
    "v_permlane16_swap_b32 v100, v102\n"
    "v_permlane16_swap_b32 v101, v103\n"
    "v_permlane16_swap_b32 v104, v106\n"
    "v_permlane16_swap_b32 v105, v107\n"
    "v_permlane16_swap_b32 v108, v110\n"
    "v_permlane16_swap_b32 v109, v111\n"
    "s_waitcnt lgkmcnt(0)\n"
    "s_barrier\n"
    "ds_read_b128 v[32:35], v11\n"
    "ds_read_b128 v[36:39], v11 offset:16\n"
    "buffer_store_dwordx4 v[100:103], v8, s[36:39], s75 offen sc1\n"
    "buffer_store_dwordx4 v[104:107], v8, s[36:39], s76 offen sc1\n"
    "buffer_store_dwordx4 v[108:111], v9, s[36:39], s75 offen sc1\n"
    "v_xor_b32 v10, 256, v10\n"
    "v_xor_b32 v11, 256, v11\n"
    "s_waitcnt lgkmcnt(0)\n"
    "v_pk_add_f32 v[114:115], v[32:33], v[34:35]\n"
    "v_pk_add_f32 v[116:117], v[36:37], v[38:39]\n"
    "v_pk_add_f32 v[114:115], v[114:115], v[116:117]\n"
    "v_fmac_f32 v32, 0xbe800000, v114\n"
    "v_fmac_f32 v34, 0xbe800000, v114\n"
    "v_fmac_f32 v36, 0xbe800000, v114\n"
    "v_fmac_f32 v38, 0xbe800000, v114\n"
    "v_fmac_f32 v112, 0xbe800000, v114\n"
    "v_mul_f32 v116, v32, v32\n"
    "v_fmac_f32 v116, v34, v34\n"
    "v_fmac_f32 v116, v36, v36\n"
    "v_fmac_f32 v116, v38, v38\n"
    "v_fmac_f32 v115, 0x43100000, v116\n"
    "v_mov_b32 v116, 0x3727c5ac\n"
    "v_fmac_f32 v116, 0x3ae38e39, v115\n"
    "v_rsq_f32 v116, v116\n"
    "s_nop 0\n"
    "v_mul_f32 v114, v112, v116\n"
    "v_pk_fma_f32 v[80:81], v[80:81], v[116:117], v[114:115] op_sel_hi:[1,0,0]\n"
    "v_pk_fma_f32 v[82:83], v[82:83], v[116:117], v[114:115] op_sel_hi:[1,0,0]\n"
    "v_pk_fma_f32 v[84:85], v[84:85], v[116:117], v[114:115] op_sel_hi:[1,0,0]\n"
    "v_pk_fma_f32 v[86:87], v[86:87], v[116:117], v[114:115] op_sel_hi:[1,0,0]\n"
    "v_pk_fma_f32 v[88:89], v[88:89], v[116:117], v[114:115] op_sel_hi:[1,0,0]\n"
    "v_pk_fma_f32 v[90:91], v[90:91], v[116:117], v[114:115] op_sel_hi:[1,0,0]\n"
    "v_pk_fma_f32 v[92:93], v[92:93], v[116:117], v[114:115] op_sel_hi:[1,0,0]\n"
    "v_pk_fma_f32 v[94:95], v[94:95], v[116:117], v[114:115] op_sel_hi:[1,0,0]\n"
    "v_pk_fma_f32 v[96:97], v[96:97], v[116:117], v[114:115] op_sel_hi:[1,0,0]\n"
    "v_pk_fma_f32 v[98:99], v[98:99], v[116:117], v[114:115] op_sel_hi:[1,0,0]\n"
    "v_cvt_pk_bf16_f32 v100, v80, v81\n"
    "v_cvt_pk_bf16_f32 v101, v82, v83\n"
    "v_cvt_pk_bf16_f32 v102, v84, v85\n"
    "v_cvt_pk_bf16_f32 v103, v86, v87\n"
    "v_cvt_pk_bf16_f32 v104, v88, v89\n"
    "v_cvt_pk_bf16_f32 v105, v90, v91\n"
    "v_cvt_pk_bf16_f32 v106, v92, v93\n"
    "v_cvt_pk_bf16_f32 v107, v94, v95\n"
    "v_cvt_pk_bf16_f32 v108, v96, v97\n"
    "v_cvt_pk_bf16_f32 v109, v98, v99\n"
    "v_permlane16_swap_b32 v100, v102\n"
    "v_permlane16_swap_b32 v101, v103\n"
    "buffer_store_dwordx4 v[100:103], v4, s[44:47], s77 offen sc1\n"
    "v_permlane16_swap_b32 v104, v106\n"
    "v_permlane16_swap_b32 v105, v107\n"
    "buffer_store_dwordx4 v[104:107], v4, s[44:47], s78 offen sc1\n"
    "v_permlane16_swap_b32 v108, v110\n"
    "v_permlane16_swap_b32 v109, v111\n"
    "buffer_store_dwordx4 v[108:111], v5, s[44:47], s77 offen sc1\n"
    "s_cmp_eq_u32 s69, 64\n"
    "s_cbranch_scc1 .LrMID_fin_%=\n"
    "s_waitcnt vmcnt(6)\n"
    "v_add_u32 v120, v122, v1\n"
    "v_ashrrev_i32 v120, 1, v120\n"
    "v_cmp_gt_i32 vcc, s69, v120\n"
    "s_cbranch_vccnz .LrMID_idle_%=\n"
    "buffer_load_dwordx4 v[32:35], v2, s[36:39], s71 offen sc1\n"
    "buffer_load_dwordx4 v[36:39], v2, s[36:39], s71 offen offset:512 sc1\n"
    "buffer_load_dwordx4 v[40:43], v2, s[36:39], s71 offen offset:2048 sc1\n"
    "buffer_load_dwordx4 v[44:47], v2, s[36:39], s71 offen offset:2560 sc1\n"
    "buffer_load_dwordx4 v[48:51], v2, s[36:39], s72 offen sc1\n"
    "buffer_load_dwordx4 v[52:55], v2, s[36:39], s72 offen offset:512 sc1\n"
    "buffer_load_dwordx4 v[56:59], v2, s[36:39], s72 offen offset:2048 sc1\n"
    "buffer_load_dwordx4 v[60:63], v2, s[36:39], s72 offen offset:2560 sc1\n"
    "buffer_load_dwordx4 v[64:67], v3, s[36:39], s71 offen sc1\n"
    "buffer_load_dwordx4 v[68:71], v6, s[40:43], s73 offen sc1\n"
    "buffer_load_dwordx4 v[72:75], v6, s[40:43], s74 offen sc1\n"
    "buffer_load_dwordx4 v[76:79], v7, s[40:43], s73 offen sc1\n"
    "s_waitcnt vmcnt(12)\n"
    "v_lshl_or_b32 v14, s68, 1, 1\n"
    "s_mov_b64 exec, s[64:65]\n"
    "buffer_atomic_smax v14, v13, s[36:39], 0 offen sc1\n"
    "s_mov_b64 exec, -1\n"
    "s_branch .LrMID_top_%=\n"
    ".LrMID_LOOPE_%=:\n"
    ".LrMID_fin_%=:\n"
    "s_waitcnt vmcnt(0)\n"
    "s_mov_b32 s68, 63\n"
    "v_lshl_or_b32 v14, s68, 1, 1\n"
    "s_mov_b64 exec, s[64:65]\n"
    "buffer_atomic_smax v14, v13, s[36:39], 0 offen sc1\n"
    "s_mov_b64 exec, -1\n"
    "s_waitcnt vmcnt(0)\n"
    "s_barrier\n"
    "s_branch .LrMID_FIN_%=\n"
    ".LrMID_idle_%=:\n"
    "v_add_u32 v120, v123, v1\n"
    "v_ashrrev_i32 v120, 1, v120\n"
    "v_cmp_gt_i32 vcc, s69, v120\n"
    "s_nop 0\n"
    "s_or_b64 exec, vcc, s[50:51]\n"
    "buffer_atomic_smax v123, v0, s[36:39], 0 offen sc0 sc1\n"
    "s_mov_b64 exec, -1\n"
    "s_waitcnt vmcnt(1)\n"
    "v_lshl_or_b32 v14, s68, 1, 1\n"
    "s_mov_b64 exec, s[64:65]\n"
    "buffer_atomic_smax v14, v13, s[36:39], 0 offen sc1\n"
    "s_mov_b64 exec, -1\n"
    ".LrMID_block_%=:\n"
    "v_add_u32 v120, v122, v1\n"
    "v_ashrrev_i32 v120, 1, v120\n"
    "v_cmp_gt_i32 vcc, s69, v120\n"
    "s_nop 0\n"
    "s_or_b64 exec, vcc, s[50:51]\n"
    "buffer_atomic_smax v122, v0, s[36:39], 0 offen sc0 sc1\n"
    "s_mov_b64 exec, -1\n"
    "s_sleep 3\n"
    "s_waitcnt vmcnt(1)\n"
    "v_add_u32 v120, v123, v1\n"
    "v_ashrrev_i32 v120, 1, v120\n"
    "v_cmp_gt_i32 vcc, s69, v120\n"
    "s_cbranch_vccz .LrMID_go_%=\n"
    "v_add_u32 v120, v123, v1\n"
    "v_ashrrev_i32 v120, 1, v120\n"
    "v_cmp_gt_i32 vcc, s69, v120\n"
    "s_nop 0\n"
    "s_or_b64 exec, vcc, s[50:51]\n"
    "buffer_atomic_smax v123, v0, s[36:39], 0 offen sc0 sc1\n"
    "s_mov_b64 exec, -1\n"
    "s_sleep 3\n"
    "s_waitcnt vmcnt(1)\n"
    "v_add_u32 v120, v122, v1\n"
    "v_ashrrev_i32 v120, 1, v120\n"
    "v_cmp_gt_i32 vcc, s69, v120\n"
    "s_cbranch_vccz .LrMID_go_%=\n"
    "s_sub_u32 s70, s70, 1\n"
    "s_cbranch_scc0 .LrMID_block_%=\n"
    "s_mov_b32 s70, 0\n"
    ".LrMID_go_%=:\n"
    "s_and_b32 s71, s69, 7\n"
    "s_mul_i32 s71, s71, 4194304\n"
    "s_add_u32 s71, s71, s52\n"
    "s_add_u32 s72, s71, 4096\n"
    "s_and_b32 s73, s69, s55\n"
    "s_mul_i32 s73, s73, s54\n"
    "s_add_u32 s73, s73, s53\n"
    "s_add_u32 s74, s73, s56\n"
    "buffer_load_dwordx4 v[32:35], v2, s[36:39], s71 offen sc1\n"
    "buffer_load_dwordx4 v[36:39], v2, s[36:39], s71 offen offset:512 sc1\n"
    "buffer_load_dwordx4 v[40:43], v2, s[36:39], s71 offen offset:2048 sc1\n"
    "buffer_load_dwordx4 v[44:47], v2, s[36:39], s71 offen offset:2560 sc1\n"
    "buffer_load_dwordx4 v[48:51], v2, s[36:39], s72 offen sc1\n"
    "buffer_load_dwordx4 v[52:55], v2, s[36:39], s72 offen offset:512 sc1\n"
    "buffer_load_dwordx4 v[56:59], v2, s[36:39], s72 offen offset:2048 sc1\n"
    "buffer_load_dwordx4 v[60:63], v2, s[36:39], s72 offen offset:2560 sc1\n"
    "buffer_load_dwordx4 v[64:67], v3, s[36:39], s71 offen sc1\n"
    "buffer_load_dwordx4 v[68:71], v6, s[40:43], s73 offen sc1\n"
    "buffer_load_dwordx4 v[72:75], v6, s[40:43], s74 offen sc1\n"
    "buffer_load_dwordx4 v[76:79], v7, s[40:43], s73 offen sc1\n"
    "buffer_load_dword v121, v7, s[40:43], s73 offen\n"
    "s_branch .LrMID_top_%=\n"
    ".LrMID_FIN_%=:\n"
  );
}
DEVI void role_fin(const Params& p, int q, u32 dbg, u64 tag) {
  v16u sa, sb, va, vb;
  red_args<2>(p, 1, q, dbg, tag, sa, sb, va, vb);
  RUN_RED(
    "s_nop 4\n"
    "s_mov_b32 s50, 0\n"
    "s_mov_b32 s51, 0x80000000\n"
    "s_getpc_b64 s[84:85]\n"
    ".LrFIN_PC_%=:\n"
    "s_and_b32 s85, s85, 0xffff\n"
    "s_mov_b32 s86, .LrFIN_FIN_%=-.LrFIN_PC_%=\n"
    "s_mov_b32 s87, 0x20000\n"
    "v_mbcnt_lo_u32_b32 v134, -1, 0\n"
    "v_mbcnt_hi_u32_b32 v134, -1, v134\n"
    "v_lshlrev_b32 v134, 7, v134\n"
    "buffer_load_dword v135, v134, s[84:87], 0 offen\n"
    "v_add_u32 v134, 8192, v134\n"
    "buffer_load_dword v135, v134, s[84:87], 0 offen\n"
    "s_mov_b32 s69, 0\n"
    "v_mov_b32 v122, -1\n"
    "v_mov_b32 v123, -1\n"
    "s_mov_b32 s70, 262144\n"
    ".LrFIN_par_%=:\n"
    "buffer_load_dwordx2 v[14:15], off, s[36:39], 0 sc1\n"
    "s_waitcnt vmcnt(0)\n"
    "v_readfirstlane_b32 s75, v14\n"
    "v_readfirstlane_b32 s76, v15\n"
    "s_xor_b32 s75, s75, s48\n"
    "s_xor_b32 s76, s76, s49\n"
    "s_cselect_b32 s76, 2, 0\n"
    "s_or_b32 s76, s76, s75\n"
    "s_cmp_lt_u32 s76, 2\n"
    "s_cbranch_scc1 .LrFIN_par1_%=\n"
    "s_sleep 4\n"
    "s_sub_u32 s70, s70, 1\n"
    "s_cbranch_scc0 .LrFIN_par_%=\n"
    "s_mov_b32 s70, 0\n"
    ".LrFIN_par1_%=:\n"
    "s_and_b32 s75, s75, 1\n"
    "s_lshl_b32 s75, s75, 13\n"
    "s_add_u32 s36, s36, s75\n"
    "s_addc_u32 s37, s37, 0\n"
    "s_and_b32 s76, s75, s61\n"
    "s_add_u32 s40, s40, s76\n"
    "s_addc_u32 s41, s41, 0\n"
    "s_and_b32 s76, s75, s63\n"
    "s_add_u32 s44, s44, s76\n"
    "s_addc_u32 s45, s45, 0\n"
    "s_branch .LrFIN_block_%=\n"
    ".LrFIN_LOOPB_%=:\n"
    ".LrFIN_top_%=:\n"
    "s_mov_b32 s68, s69\n"
    "s_add_u32 s69, s68, 1\n"
    "s_and_b32 s75, s68, s58\n"
    "s_mul_i32 s77, s75, 36864\n"
    "s_add_u32 s77, s77, s59\n"
    "s_add_u32 s78, s77, s60\n"
    "s_and_b32 s71, s69, 7\n"
    "s_mul_i32 s71, s71, 4194304\n"
    "s_add_u32 s71, s71, s52\n"
    "s_add_u32 s72, s71, 4096\n"
    "s_and_b32 s73, s69, s55\n"
    "s_mul_i32 s73, s73, s54\n"
    "s_add_u32 s73, s73, s53\n"
    "s_add_u32 s74, s73, s56\n"
    "s_waitcnt vmcnt(11)\n"
    "v_mfma_f32_16x16x32_bf16 v[80:83], v[16:19], v[32:35], 0\n"
    "s_waitcnt vmcnt(10)\n"
    "v_mfma_f32_16x16x32_bf16 v[80:83], v[20:23], v[36:39], v[80:83]\n"
    "s_waitcnt vmcnt(9)\n"
    "v_mfma_f32_16x16x32_bf16 v[84:87], v[16:19], v[40:43], 0\n"
    "s_waitcnt vmcnt(8)\n"
    "v_mfma_f32_16x16x32_bf16 v[84:87], v[20:23], v[44:47], v[84:87]\n"
    "s_waitcnt vmcnt(7)\n"
    "v_mfma_f32_16x16x32_bf16 v[88:91], v[16:19], v[48:51], 0\n"
    "s_waitcnt vmcnt(6)\n"
    "v_mfma_f32_16x16x32_bf16 v[88:91], v[20:23], v[52:55], v[88:91]\n"
    "s_waitcnt vmcnt(5)\n"
    "v_mfma_f32_16x16x32_bf16 v[92:95], v[16:19], v[56:59], 0\n"
    "s_waitcnt vmcnt(4)\n"
    "v_mfma_f32_16x16x32_bf16 v[92:95], v[20:23], v[60:63], v[92:95]\n"
    "s_waitcnt vmcnt(3)\n"
    "v_mfma_f32_16x16x32_bf16 v[96:99], v[16:19], v[64:67], 0\n"
    "s_waitcnt vmcnt(2)\n"
    "v_mfma_f32_16x16x32_bf16 v[80:83], v[24:27], v[68:71], v[80:83]\n"
    "v_mfma_f32_16x16x32_bf16 v[84:87], v[28:31], v[68:71], v[84:87]\n"
    "s_waitcnt vmcnt(1)\n"
    "v_mfma_f32_16x16x32_bf16 v[88:91], v[24:27], v[72:75], v[88:91]\n"
    "v_mfma_f32_16x16x32_bf16 v[92:95], v[28:31], v[72:75], v[92:95]\n"
    "s_waitcnt vmcnt(0)\n"
    "v_mfma_f32_16x16x32_bf16 v[96:99], v[24:27], v[76:79], v[96:99]\n"
    "v_add_u32 v120, v122, v1\n"
    "v_ashrrev_i32 v120, 1, v120\n"
    "v_cmp_gt_i32 vcc, s69, v120\n"
    "s_nop 0\n"
    "s_or_b64 exec, vcc, s[50:51]\n"
    "buffer_atomic_smax v122, v0, s[36:39], 0 offen sc0 sc1\n"
    "s_mov_b64 exec, -1\n"
    "v_lshl_or_b32 v14, s68, 1, 1\n"
    "s_mov_b64 exec, s[64:65]\n"
    "buffer_atomic_smax v14, v13, s[36:39], 0 offen sc1\n"
    "s_mov_b64 exec, -1\n"
    "v_cvt_pk_bf16_f32 v100, v80, v81\n"
    "v_cvt_pk_bf16_f32 v101, v82, v83\n"
    "v_cvt_pk_bf16_f32 v102, v84, v85\n"
    "v_cvt_pk_bf16_f32 v103, v86, v87\n"
    "v_cvt_pk_bf16_f32 v104, v88, v89\n"
    "v_cvt_pk_bf16_f32 v105, v90, v91\n"
    "v_cvt_pk_bf16_f32 v106, v92, v93\n"
    "v_cvt_pk_bf16_f32 v107, v94, v95\n"
    "v_cvt_pk_bf16_f32 v108, v96, v97\n"
    "v_cvt_pk_bf16_f32 v109, v98, v99\n"
    "v_permlane16_swap_b32 v100, v102\n"
    "v_permlane16_swap_b32 v101, v103\n"
    "ds_write_b128 v10, v[100:103]\n"
    "v_permlane16_swap_b32 v104, v106\n"
    "v_permlane16_swap_b32 v105, v107\n"
    "ds_write_b128 v10, v[104:107] offset:128\n"
    "v_permlane16_swap_b32 v108, v110\n"
    "v_permlane16_swap_b32 v109, v111\n"
    "ds_write_b128 v12, v[108:111]\n"
    "v_xor_b32 v10, 16384, v10\n"
    "v_xor_b32 v12, 16384, v12\n"
    "s_waitcnt lgkmcnt(0)\n"
    "s_barrier\n"
    "ds_read_b128 v[100:103], v11 offset:0\n"
    "ds_read_b128 v[104:107], v11 offset:4096\n"
    "ds_read_b128 v[108:111], v11 offset:8192\n"
    "v_xor_b32 v11, 16384, v11\n"
    "s_waitcnt lgkmcnt(2)\n"
    "buffer_store_dwordx4 v[100:103], v4, s[44:47], s77 offen sc1\n"
    "s_waitcnt lgkmcnt(1)\n"
    "buffer_store_dwordx4 v[104:107], v4, s[44:47], s78 offen sc1\n"
    "s_waitcnt lgkmcnt(0)\n"
    "buffer_store_dwordx4 v[108:111], v5, s[44:47], s77 offen sc1\n"
    "s_cmp_eq_u32 s69, 64\n"
    "s_cbranch_scc1 .LrFIN_fin_%=\n"
    "s_waitcnt vmcnt(4)\n"
    "v_add_u32 v120, v122, v1\n"
    "v_ashrrev_i32 v120, 1, v120\n"
    "v_cmp_gt_i32 vcc, s69, v120\n"
    "s_cbranch_vccnz .LrFIN_idle_%=\n"
    "buffer_load_dwordx4 v[32:35], v2, s[36:39], s71 offen sc1\n"
    "buffer_load_dwordx4 v[36:39], v2, s[36:39], s71 offen offset:512 sc1\n"
    "buffer_load_dwordx4 v[40:43], v2, s[36:39], s71 offen offset:2048 sc1\n"
    "buffer_load_dwordx4 v[44:47], v2, s[36:39], s71 offen offset:2560 sc1\n"
    "buffer_load_dwordx4 v[48:51], v2, s[36:39], s72 offen sc1\n"
    "buffer_load_dwordx4 v[52:55], v2, s[36:39], s72 offen offset:512 sc1\n"
    "buffer_load_dwordx4 v[56:59], v2, s[36:39], s72 offen offset:2048 sc1\n"
    "buffer_load_dwordx4 v[60:63], v2, s[36:39], s72 offen offset:2560 sc1\n"
    "buffer_load_dwordx4 v[64:67], v3, s[36:39], s71 offen sc1\n"
    "buffer_load_dwordx4 v[68:71], v6, s[40:43], s73 offen sc1\n"
    "buffer_load_dwordx4 v[72:75], v6, s[40:43], s74 offen sc1\n"
    "buffer_load_dwordx4 v[76:79], v7, s[40:43], s73 offen sc1\n"
    "s_branch .LrFIN_top_%=\n"
    ".LrFIN_LOOPE_%=:\n"
    ".LrFIN_fin_%=:\n"
    "s_waitcnt vmcnt(0)\n"
    "s_branch .LrFIN_FIN_%=\n"
    ".LrFIN_idle_%=:\n"
    ".LrFIN_block_%=:\n"
    "v_add_u32 v120, v122, v1\n"
    "v_ashrrev_i32 v120, 1, v120\n"
    "v_cmp_gt_i32 vcc, s69, v120\n"
    "s_nop 0\n"
    "s_or_b64 exec, vcc, s[50:51]\n"
    "buffer_atomic_smax v122, v0, s[36:39], 0 offen sc0 sc1\n"
    "s_mov_b64 exec, -1\n"
    "s_sleep 3\n"
    "s_waitcnt vmcnt(1)\n"
    "v_add_u32 v120, v123, v1\n"
    "v_ashrrev_i32 v120, 1, v120\n"
    "v_cmp_gt_i32 vcc, s69, v120\n"
    "s_cbranch_vccz .LrFIN_go_%=\n"
    "v_add_u32 v120, v123, v1\n"
    "v_ashrrev_i32 v120, 1, v120\n"
    "v_cmp_gt_i32 vcc, s69, v120\n"
    "s_nop 0\n"
    "s_or_b64 exec, vcc, s[50:51]\n"
    "buffer_atomic_smax v123, v0, s[36:39], 0 offen sc0 sc1\n"
    "s_mov_b64 exec, -1\n"
    "s_sleep 3\n"
    "s_waitcnt vmcnt(1)\n"
    "v_add_u32 v120, v122, v1\n"
    "v_ashrrev_i32 v120, 1, v120\n"
    "v_cmp_gt_i32 vcc, s69, v120\n"
    "s_cbranch_vccz .LrFIN_go_%=\n"
    "s_sub_u32 s70, s70, 1\n"
    "s_cbranch_scc0 .LrFIN_block_%=\n"
    "s_mov_b32 s70, 0\n"
    ".LrFIN_go_%=:\n"
    "s_and_b32 s71, s69, 7\n"
    "s_mul_i32 s71, s71, 4194304\n"
    "s_add_u32 s71, s71, s52\n"
    "s_add_u32 s72, s71, 4096\n"
    "s_and_b32 s73, s69, s55\n"
    "s_mul_i32 s73, s73, s54\n"
    "s_add_u32 s73, s73, s53\n"
    "s_add_u32 s74, s73, s56\n"
    "buffer_load_dwordx4 v[32:35], v2, s[36:39], s71 offen sc1\n"
    "buffer_load_dwordx4 v[36:39], v2, s[36:39], s71 offen offset:512 sc1\n"
    "buffer_load_dwordx4 v[40:43], v2, s[36:39], s71 offen offset:2048 sc1\n"
    "buffer_load_dwordx4 v[44:47], v2, s[36:39], s71 offen offset:2560 sc1\n"
    "buffer_load_dwordx4 v[48:51], v2, s[36:39], s72 offen sc1\n"
    "buffer_load_dwordx4 v[52:55], v2, s[36:39], s72 offen offset:512 sc1\n"
    "buffer_load_dwordx4 v[56:59], v2, s[36:39], s72 offen offset:2048 sc1\n"
    "buffer_load_dwordx4 v[60:63], v2, s[36:39], s72 offen offset:2560 sc1\n"
    "buffer_load_dwordx4 v[64:67], v3, s[36:39], s71 offen sc1\n"
    "buffer_load_dwordx4 v[68:71], v6, s[40:43], s73 offen sc1\n"
    "buffer_load_dwordx4 v[72:75], v6, s[40:43], s74 offen sc1\n"
    "buffer_load_dwordx4 v[76:79], v7, s[40:43], s73 offen sc1\n"
    "s_branch .LrFIN_top_%=\n"
    ".LrFIN_FIN_%=:\n"
  );
}

__global__ void __launch_bounds__(256, 1) mega(Params p) {
  extern __shared__ __attribute__((aligned(16))) unsigned char smem[];
  const rsrc_t rw = __builtin_amdgcn_make_buffer_rsrc(p.ws, 0, WS_END + 8192, 0x00020000);
  const unsigned long long t00 = __builtin_amdgcn_s_memrealtime();
  if (threadIdx.x < 64) {
    const int lane = threadIdx.x, b = blockIdx.x;
    if (p.role_mask & 0x40000) st16(rw, (v4u){~0u, ~0u, ~0u, ~0u}, lane < 22 ? CHUNK + (u32)b * 352 + lane * 16 : OOB, 0);
    const u64 tg = ld8u(rw, EPOCH) + 1;
    drain();
    if (lane == 0) {
      {
        const v2u tv = {(u32)tg, (u32)(tg >> 32)};
        const u32 a = READY + (u32)b * 8;
        asm volatile("buffer_atomic_swap_x2 %0, %1, %2, 0 offen sc1" :: "v"(tv), "v"(a), "s"(rw) : "memory");
      }
      int rid = (int)(__builtin_amdgcn_s_getreg(20 | (3 << 11)) & 7u) * 32 + (b >> 3);
#pragma unroll 1
      for (int k = 0; k < 256; ++k) {
        if (__hip_atomic_exchange((u64*)(p.ws + CLAIM) + ((rid & 31) * 8 + (rid >> 5)), tg, __ATOMIC_RELAXED, __HIP_MEMORY_SCOPE_AGENT) != tg) break;
        rid = (rid + 1) & 255;
      }
      *(volatile v4u*)smem = (v4u){(u32)rid, (u32)tg, (u32)(tg >> 32), 0u};
    }
  }
  int rid = (int)(__builtin_amdgcn_s_getreg(20 | (3 << 11)) & 7u) * 32 + (int)(blockIdx.x >> 3);
  u64 tag = 0;
  bool sure = false;
  for (;;) {
  if (!sure && (rid >= 128 || (rid & 31) >= SL_ATT)) { confirm(rid, tag, sure, smem); continue; }
  const int L = rid >> 7, j = (rid >> 5) & 3, sl = rid & 31;
  const u32 dbg = OFF_DBG + (u32)rid * 66 * 256;
  auto on = [&](int r) { return ((p.role_mask >> (2 + 8 * L + r)) & 1) != 0; };
  STAMP(65, 8);
  STAMP(65 + 32, 9);
  if (((PULL >> L) & 1) && sl >= SL_WO && sl < SL_REDA) pull_weights(p, j, sl - SL_WO);
  if (sl < SL_P) { delay_start(L ? DL_R1 : 0, t00); if (on(1)) { role_rec(p, L, j, sl, dbg, rid, tag, sure, smem); continue; } if (!confirm(rid, tag, sure, smem)) continue; }
  else if (sl < SL_ATT) { delay_start(L ? DL_R1 : 0, t00); if (on(0)) { role_p(p, L, j, sl - SL_P, dbg, rid, tag, sure, smem); continue; } if (!confirm(rid, tag, sure, smem)) continue; }
  else if (sl < SL_WO) {
    const u32 shift = find_shift(rw, rid == 128 + SL_ATT && threadIdx.x < 64, tag);
    if (p.role_mask & 2) hot_init(p, rw, L, 2 * j + sl - SL_ATT, shift);
    if (on(2)) role_att(p, L, j, sl - SL_ATT, dbg, shift);
  }
  else if (sl < SL_UP) { delay_start(L ? DL_W1 : DL_W0, t00); if (on(3)) role_wo(p, L, j, sl - SL_WO, dbg, tag); }
  else if (sl < SL_DOWN) { delay_start(L ? DL_U1 : DL_U0, t00); if (on(5)) role_up(p, L, j, sl - SL_UP, dbg, tag); }
  else if (sl < SL_REDA) { delay_start(L ? DL_D1 : DL_D0, t00); if (on(6)) role_down(p, L, j, sl - SL_DOWN, dbg, tag); }
  else {
    if (sl == SL_REDA && (p.role_mask & 2)) hot_init(p, rw, L, 8 + j, find_shift(rw, false, tag));
    if ((p.role_mask & 1) && (L == 1 || sl == SL_REDF)) role_ln0(p, L == 0 ? 2 : (sl == SL_REDA ? 1 : 0), j, dbg, tag);
    if (sl == SL_REDF && L == 1) {
      if (on(7)) role_fin(p, j, dbg, tag);
    } else {
      v16u sa, sb, va, vb;
      if (sl == SL_REDA) red_args<1>(p, L, j, dbg, tag, sa, sb, va, vb);
      else red_args<2>(p, 0, j, dbg, tag, sa, sb, va, vb);
      if (on(sl == SL_REDA ? 4 : 7)) role_mid(sa, sb, va, vb);
    }
  }
  break;
  }
}

static void fill_params(Params& p, void* const* d_in, void* d_out, void* d_ws) {
  p.x = (const u16*)d_in[0]; p.mem0 = (const u16*)d_in[1];
  p.Wq = (const u16*)d_in[2]; p.Wk = (const u16*)d_in[3]; p.Wv = (const u16*)d_in[4]; p.Wo = (const u16*)d_in[5];
  p.w1 = (const u16*)d_in[8]; p.w3 = (const u16*)d_in[9]; p.w2 = (const u16*)d_in[10];
  p.out = (u16*)d_out; p.ws = (unsigned char*)d_ws;
}

extern "C" void kernel_launch(void* const* d_in, const int* in_sizes, int n_in, void* d_out, int out_size, void* d_ws,
                              size_t ws_size, hipStream_t stream) {
  Params p;
  fill_params(p, d_in, d_out, d_ws);
  (void)hipFuncSetAttribute((const void*)mega, hipFuncAttributeMaxDynamicSharedMemorySize, LDS_BYTES);
  p.role_mask = 0x7FFFF; p.unused = 0;
  mega<<<256, 256, LDS_BYTES, stream>>>(p);
}
```

```cpp
#include <hip/hip_runtime.h>
#include <stdint.h>
#include <type_traits>
#include <utility>

typedef unsigned short u16;
typedef unsigned int u32;
typedef short bf16x8 __attribute__((ext_vector_type(8)));
typedef short bf16x4 __attribute__((ext_vector_type(4)));
typedef float f32x4 __attribute__((ext_vector_type(4)));
typedef float f32x2 __attribute__((ext_vector_type(2)));
typedef __bf16 bf2_t __attribute__((ext_vector_type(2)));
typedef unsigned v16u __attribute__((ext_vector_type(16)));
typedef unsigned v8u __attribute__((ext_vector_type(8)));
typedef unsigned v4u __attribute__((ext_vector_type(4)));
typedef unsigned v2u __attribute__((ext_vector_type(2)));
typedef __amdgpu_buffer_rsrc_t rsrc_t;
#define DEVI __device__ __forceinline__

static constexpr int NSEG = 64;
static constexpr int PSLOW = 1, PSKIP = 2, PULL = 1, RQ = 8, RO = 8, RP = 8, RH = 8, RX = 8, RX2 = 16, LN_B = 28, LN_C = 52, PS_ATT = 3, PS_RED = 3, DL_R1 = 12, DL_W0 = 5, DL_W1 = 14, DL_U0 = 8, DL_U1 = 3, DL_D0 = 11, DL_D1 = 5;

static constexpr u32 PARW = 0, EPOCH = 128, READY = 2048, CLAIM = 4096;
static constexpr u32 CHUNK = 16384;
static constexpr u32 MBOX = CHUNK;
static constexpr u32 INITF = 5 * CHUNK;
static constexpr u32 CTRL_BYTES = 6 * CHUNK + 8192;
static constexpr u32 HOT = 6 * CHUNK, HOT2 = 2u << 20;
static constexpr u32 SLOT = 4u << 20;
static constexpr u32 PGA = SLOT, PGB = SLOT + (2u << 20);
static constexpr int cmax(int a, int b) { return a > b ? a : b; }
static constexpr int NSLOT = cmax(cmax(cmax(RQ, RO), cmax(RP, RH)), cmax(RX, RX2));
DEVI u32 pga(int L, int blk) { return PGA + (u32)blk * CHUNK + (u32)L * 8192; }
DEVI u32 pgb(int L, int blk) { return PGB + (u32)blk * CHUNK + (u32)(1 - L) * 8192; }
static constexpr int B_QKV = 0, B_KVM = 96;
static constexpr int B_O = 0, B_H = 32, B_PA = 68, B_PF = 88, B_XN2 = 108, B_X1 = 113, B_XN = 118, B_X = 123;
static constexpr u32 OFF_XN0 = SLOT * (1 + NSLOT);
static constexpr u32 OFF_DBG = OFF_XN0 + 320 * CHUNK;
static constexpr u32 WS_END = OFF_DBG + 256u * 66 * 256;
DEVI u32 kb_off(int kb) { return (u32)((kb >> 3) * CHUNK + (kb & 7) * 1024); }
static constexpr u32 OOB = 0x80000000u;
static constexpr int LDS_BYTES = 163840;
static constexpr unsigned SPIN_LIMIT = 1u << 18;
static constexpr int SL_REC = 0, SL_P = 8, SL_ATT = 16, SL_WO = 18, SL_UP = 21, SL_DOWN = 27, SL_REDA = 30, SL_REDF = 31;
DEVI u32 mbox(int L, int j, int role, int slot) { return MBOX + (u32)(j * CHUNK + L * 8192 + role * 256 + 4 * slot); }
DEVI u32 mbox_att(int L, int j, int hl, int slot) { return mbox(L, j, SL_ATT + (hl >> 2), 16 * (hl & 3) + slot); }

struct Params {
  const u16* x; const u16* mem0; const u16* Wq; const u16* Wk; const u16* Wv; const u16* Wo;
  const u16* w1; const u16* w3; const u16* w2; u16* out; unsigned char* ws;
  int role_mask; int unused;
};

template <class F, int... I> DEVI void static_for_impl(F&& f, std::integer_sequence<int, I...>) { (f(std::integral_constant<int, I>{}), ...); }
template <int N, class F> DEVI void static_for(F&& f) { static_for_impl(f, std::make_integer_sequence<int, N>{}); }
DEVI float bflo(u32 d) { return __uint_as_float(d << 16); }
DEVI float bfhi(u32 d) { return __uint_as_float(d & 0xffff0000u); }
DEVI u32 pk2(float a, float b) {
  f32x2 f = {a, b};
  bf2_t r = __builtin_convertvector(f, bf2_t);
  return __builtin_bit_cast(u32, r);
}
DEVI bf16x8 as_frag(v4u v) { return __builtin_bit_cast(bf16x8, v); }
DEVI bf16x4 as_frag4(v2u v) { return __builtin_bit_cast(bf16x4, v); }
DEVI f32x4 mfma32(v4u a, v4u b, f32x4 c) { return __builtin_amdgcn_mfma_f32_16x16x32_bf16(as_frag(a), as_frag(b), c, 0, 0, 0); }
DEVI f32x4 mfma16(v2u a, v2u b, f32x4 c) { return __builtin_amdgcn_mfma_f32_16x16x16bf16_1k(as_frag4(a), as_frag4(b), c, 0, 0, 0); }
DEVI v4u ld16(rsrc_t r, u32 voff, u32 soff) { return __builtin_amdgcn_raw_buffer_load_b128(r, voff, soff, 16); }
DEVI v2u ld8(rsrc_t r, u32 voff, u32 soff) { return __builtin_amdgcn_raw_buffer_load_b64(r, voff, soff, 16); }
DEVI u32 ld4(rsrc_t r, u32 voff, u32 soff) { return __builtin_amdgcn_raw_buffer_load_b32(r, voff, soff, 16); }
DEVI void st16(rsrc_t r, v4u v, u32 voff, u32 soff) { __builtin_amdgcn_raw_buffer_store_b128(v, r, voff, soff, 16); }
DEVI void st8(rsrc_t r, v2u v, u32 voff, u32 soff) { __builtin_amdgcn_raw_buffer_store_b64(v, r, voff, soff, 16); }
DEVI void st4(rsrc_t r, u32 v, u32 voff, u32 soff) { __builtin_amdgcn_raw_buffer_store_b32(v, r, voff, soff, 16); }
DEVI void drain() { asm volatile("s_waitcnt vmcnt(0)" ::: "memory"); }
DEVI void cbar() { asm volatile("" ::: "memory"); }
DEVI void sbar() { __builtin_amdgcn_sched_barrier(0); }
DEVI u32 rfl(u32 v) { return (u32)__builtin_amdgcn_readfirstlane((int)v); }
DEVI float xmax16_32(float x) {
  u32 a = __float_as_uint(x);
  auto r = __builtin_amdgcn_permlane16_swap(a, a, false, false);
  x = fmaxf(__uint_as_float(r[0]), __uint_as_float(r[1]));
  a = __float_as_uint(x);
  auto q = __builtin_amdgcn_permlane32_swap(a, a, false, false);
  return fmaxf(__uint_as_float(q[0]), __uint_as_float(q[1]));
}
DEVI float xsum16_32(float x) {
  u32 a = __float_as_uint(x);
  auto r = __builtin_amdgcn_permlane16_swap(a, a, false, false);
  x = __uint_as_float(r[0]) + __uint_as_float(r[1]);
  a = __float_as_uint(x);
  auto q = __builtin_amdgcn_permlane32_swap(a, a, false, false);
  return __uint_as_float(q[0]) + __uint_as_float(q[1]);
}
DEVI float ror8(float x) { return __int_as_float(__builtin_amdgcn_update_dpp(0, __float_as_int(x), 0x128, 0xf, 0xf, false)); }
DEVI v4u swap16(v2u a, v2u b) {
  auto s0 = __builtin_amdgcn_permlane16_swap(a.x, b.x, false, false);
  auto s1 = __builtin_amdgcn_permlane16_swap(a.y, b.y, false, false);
  return (v4u){s0[0], s1[0], s0[1], s1[1]};
}

#define STAMP(row, slot) do { } while (0)

DEVI bool flags_ok(int v, int bias, int s) {
  asm volatile("" : "+v"(v));
  return __all(v + bias >= s);
}
template <int SLP> DEVI void wait_flags(rsrc_t r, u32 addr, int bias, int s, unsigned& budget) {
  for (;;) {
    cbar();
    const int v = (int)ld4(r, addr, 0);
    if (flags_ok(v, bias, s) || budget == 0) break;
    --budget;
    __builtin_amdgcn_s_sleep(SLP);
  }
  cbar();
}
DEVI void publish(rsrc_t r, u32 flag, int s, int lane) {
  drain();
  if (lane == 0) st4(r, (u32)s, flag, 0);
  cbar();
}
DEVI void delay_start(int us, unsigned long long t0) {
  if (us == 0) return;
  while (__builtin_amdgcn_s_memrealtime() - t0 < (unsigned long long)us * 100u) __builtin_amdgcn_s_sleep(16);
}
DEVI void pull_weights(const Params& p, int j, int n) {
  const int tid = threadIdx.x;
  u32 acc = 0;
  {
    const u16* w = n % 3 == 0 ? p.Wq : (n % 3 == 1 ? p.Wk : p.Wv);
    const rsrc_t r = __builtin_amdgcn_make_buffer_rsrc((void*)(w + j * 1024), 0, 576 * 8192 - j * 2048, 0x00020000);
    if (PSLOW) {
#pragma unroll
      for (int k = 0; k < 3; ++k) {
        const int row = (k * 4 + n / 3) * 64 + (tid >> 2);
        acc ^= __builtin_amdgcn_raw_buffer_load_b32(r, row < 576 ? (u32)(row * 8192 + 256 + (tid & 1) * 128 + (tid & 2) * 512) : OOB, 0, 0);
      }
    }
#pragma unroll
    for (int k = PSKIP; k < 9; ++k) {
      const int row = (k * 4 + n / 3) * 16 + (tid >> 4);
      acc ^= __builtin_amdgcn_raw_buffer_load_b32(r, (u32)(row * 8192 + (tid & 15) * 128), 0, 0);
    }
  }
  {
    const rsrc_t r = __builtin_amdgcn_make_buffer_rsrc((void*)(p.Wo + (size_t)j * 1024 * 576), 0, 1024 * 1152, 0x00020000);
#pragma unroll
    for (int k = 0; k < 3; ++k) acc ^= __builtin_amdgcn_raw_buffer_load_b32(r, (u32)((k * 12 + n) * 256 + tid) * 128, 0, 0);
  }
  asm volatile("" :: "v"(acc));
}
DEVI v4u make_srd(const void* p, u32 bytes) {
  const unsigned long long a = (unsigned long long)p;
  return (v4u){rfl((u32)a), rfl((u32)(a >> 32) & 0xffffu), bytes, 0x00020000u};
}
DEVI v16u cat4(v4u a, v4u b, v4u c, v4u d) {
  return (v16u){a.x, a.y, a.z, a.w, b.x, b.y, b.z, b.w, c.x, c.y, c.z, c.w, d.x, d.y, d.z, d.w};
}
DEVI u32 wswz(int j, int g) { return (u32)(4 * ((j >> 1) & 1) ^ 2 * (g & 1)); }
DEVI u32 tr_base(int lane, int chunk) {
  const int g = lane >> 4, q = (lane >> 2) & 3;
  return (u32)(g * 1024 + q * 128) + (((u32)chunk ^ wswz(q, g)) << 4);
}

#define RUN_ASM(TEXT) do { for (int i_ = 0; i_ < 16; ++i_) { sa[i_] = rfl(sa[i_]); sb[i_] = rfl(sb[i_]); } \
    asm volatile(TEXT "s_endpgm\n" :: "{s[36:51]}"(sa), "{s[52:67]}"(sb), "{v[0:15]}"(va) : "v255", "a255", "s99", "vcc", "memory"); \
    __builtin_unreachable(); } while (0)

#define GEMM_DECL v4u g_w0, g_w1, g_w2; u32 g_ldw, g_in_base, g_in_stride, g_in_mask, g_out_base, g_out_kb, g_out_mask, g_flag, g_paddr; int g_pbias, g_pinit = -1, g_n0 = 2, g_n1 = 2
#define GEMM_ARGS g_w0, g_w1, g_w2, g_ldw, g_in_base, g_in_stride, g_in_mask, g_out_base, g_out_kb, g_out_mask, g_flag, g_paddr, g_pbias, g_pinit, g_n0, g_n1
DEVI void gemm_args(v4u g_w0, v4u g_w1, v4u g_w2, u32 g_ldw, u32 g_in_base, u32 g_in_stride, u32 g_in_mask, u32 g_out_base, u32 g_out_kb,
                    u32 g_out_mask, u32 g_flag, u32 g_paddr, int g_pbias, int g_pinit, int g_n0, int g_n1, const Params& p, u32 dbg, unsigned long long tag, v16u& sa, v16u& sb, v16u& va) {
  const int tid = threadIdx.x, lane = tid & 63, w = (int)rfl(tid >> 6);
  const int c = lane & 15, gg = lane >> 4, pp = lane & 3;
  sa = cat4(make_srd(p.ws, WS_END), g_w0, g_w1, g_w2);
  sb = (v16u){g_in_base, g_in_stride, g_in_mask, g_out_base, SLOT, g_out_mask, 0, (u32)w * 1024, dbg + w * 64, 0, 0, 0x80000000u, 0, 0, (u32)(2 * tag), (u32)((2 * tag) >> 32)};
  va[0] = lane * 16;
  va[1] = lane * 16 + w * 2048;
  va[2] = lane == 63 ? OOB : g_paddr;
  va[3] = (u32)g_pbias;
  va[4] = (gg & 1) ? OOB : kb_off((int)g_out_kb + 8 + 2 * (w >> 1)) + (u32)(((2 * (w & 1) + (gg >> 1)) * 16 + c) * 16);
  va[5] = (u32)(8 * w + (lane >> 3)) * g_ldw + ((((u32)lane & 7) ^ wswz(lane >> 3, w)) << 4);
  va[6] = (u32)(w >> 1) * 4096 + tr_base(lane, 4 * (w & 1) + pp);
  va[7] = 2 * 4096 + tr_base(lane, 2 * w + (pp >> 1)) + 8 * (pp & 1);
  for (int i = 8; i < 16; ++i) va[i] = 0;
  va[8] = w < 2 ? va[1] : OOB;
  va[11] = (u32)g_pinit;
  va[14] = lane < g_n0 ? g_flag : OOB;
  va[10] = lane < g_n1 ? OOB : g_flag;
  va[15] = kb_off((int)g_out_kb + 2 * w) + lane * 16;
}

DEVI bool confirm(int& rid, unsigned long long& tag, bool& sure, unsigned char* smem) {
  if (sure) return true;
  asm volatile("s_waitcnt lgkmcnt(0)\n\ts_barrier" ::: "memory");
  const v4u boot = *(volatile v4u*)smem;
  const int r = (int)rfl(boot.x);
  tag = (unsigned long long)rfl(boot.y) | ((unsigned long long)rfl(boot.z) << 32);
  asm volatile("s_waitcnt lgkmcnt(0)\n\ts_barrier" ::: "memory");
  const bool ok = r == rid;
  rid = r; sure = true;
  return ok;
}
DEVI void role_p(const Params& p, int L, int j, int i, u32 dbg, int& rid, unsigned long long& tag, bool& sure, unsigned char* smem) {
  const int lane = threadIdx.x & 63, w = threadIdx.x >> 6, h = 8 * j + i;
  const u32 wb = 576u * 4096 * 2 - h * 256;
  GEMM_DECL;
  g_w0 = make_srd(p.Wq + h * 128, wb); g_w1 = make_srd(p.Wk + h * 128, wb); g_w2 = make_srd(p.Wv + h * 128, wb);
  g_ldw = 8192;
  g_in_base = L ? pgb(1, B_XN) : OFF_XN0; g_in_stride = L ? SLOT : 5 * CHUNK; g_in_mask = L ? RX2 - 1 : 63;
  g_out_base = pga(L, B_QKV + 3 * h); g_out_kb = 0; g_out_mask = RQ - 1;
  g_flag = lane == 0 ? mbox(L, j, SL_REC + i, w) : (lane == 1 ? mbox_att(L, j, i, 8 + w) : OOB);
  g_n0 = 0; g_n1 = 0;
  g_paddr = mbox(L, j, SL_P + i, lane);
  g_pbias = lane < (L ? 16 : 48) ? (lane >> 3) & 1 : (lane == 48 ? 2 * RQ : 1000);
  g_pinit = lane < 16 || lane >= 48 ? -1 : (lane < 32 ? 2 * LN_B - 1 : 2 * LN_C - 1);
  v16u sa, sb, va;
  gemm_args(GEMM_ARGS, p, dbg, tag, sa, sb, va);
  asm volatile("" : "+v"(va));
  if (!confirm(rid, tag, sure, smem)) return;
  sb[14] = (u32)(2 * tag); sb[15] = (u32)((2 * tag) >> 32);
  RUN_ASM(
    "s_nop 4\n"
    "s_getpc_b64 s[84:85]\n"
    ".LgP_PC_%=:\n"
    "s_and_b32 s85, s85, 0xffff\n"
    "s_mov_b32 s86, .LgP_FIN_%=-.LgP_PC_%=\n"
    "s_mov_b32 s87, 0x20000\n"
    "v_mbcnt_lo_u32_b32 v40, -1, 0\n"
    "v_mbcnt_hi_u32_b32 v40, -1, v40\n"
    "v_lshlrev_b32 v40, 7, v40\n"
    "buffer_load_dword v41, v40, s[84:87], 0 offen\n"
    "v_add_u32 v40, 8192, v40\n"
    "buffer_load_dword v41, v40, s[84:87], 0 offen\n"
    "v_add_u32 v40, 8192, v40\n"
    "buffer_load_dword v41, v40, s[84:87], 0 offen\n"
    "s_mov_b32 s68, 0\n"
    "s_mov_b32 s71, 0\n"
    "v_add_u32 v9, 20480, v0\n"
    "s_mov_b32 s64, 0xbfb8aa3b\n"
    "s_mov_b32 s72, 262144\n"
    "s_add_u32 m0, s59, 0\n"
    "s_mov_b32 s74, 0\n"
    "buffer_load_dwordx4 v5, s[40:43], s74 offen lds\n"
    "s_add_u32 m0, s59, 4096\n"
    "s_mov_b32 s74, 128\n"
    "buffer_load_dwordx4 v5, s[40:43], s74 offen lds\n"
    "s_add_u32 m0, s59, 8192\n"
    "s_mov_b32 s74, 0\n"
    "buffer_load_dwordx4 v5, s[44:47], s74 offen lds\n"
    "s_add_u32 m0, s59, 12288\n"
    "s_mov_b32 s74, 128\n"
    "buffer_load_dwordx4 v5, s[44:47], s74 offen lds\n"
    "s_add_u32 m0, s59, 16384\n"
    "s_mov_b32 s74, 0\n"
    "buffer_load_dwordx4 v5, s[48:51], s74 offen lds\n"
    "s_add_u32 m0, s59, 20480\n"
    "s_mov_b32 s74, 128\n"
    "buffer_load_dwordx4 v5, s[48:51], s74 offen lds\n"
    "s_add_u32 m0, s59, 24576\n"
    "s_mov_b32 s74, 262144\n"
    "buffer_load_dwordx4 v5, s[40:43], s74 offen lds\n"
    "s_add_u32 m0, s59, 28672\n"
    "s_mov_b32 s74, 262272\n"
    "buffer_load_dwordx4 v5, s[40:43], s74 offen lds\n"
    "s_add_u32 m0, s59, 32768\n"
    "s_mov_b32 s74, 262144\n"
    "buffer_load_dwordx4 v5, s[44:47], s74 offen lds\n"
    "s_add_u32 m0, s59, 36864\n"
    "s_mov_b32 s74, 262272\n"
    "buffer_load_dwordx4 v5, s[44:47], s74 offen lds\n"
    "s_add_u32 m0, s59, 40960\n"
    "s_mov_b32 s74, 262144\n"
    "buffer_load_dwordx4 v5, s[48:51], s74 offen lds\n"
    "s_add_u32 m0, s59, 45056\n"
    "s_mov_b32 s74, 262272\n"
    "buffer_load_dwordx4 v5, s[48:51], s74 offen lds\n"
    "s_add_u32 m0, s59, 49152\n"
    "s_mov_b32 s74, 524288\n"
    "buffer_load_dwordx4 v5, s[40:43], s74 offen lds\n"
    "s_add_u32 m0, s59, 53248\n"
    "s_mov_b32 s74, 524416\n"
    "buffer_load_dwordx4 v5, s[40:43], s74 offen lds\n"
    "s_add_u32 m0, s59, 57344\n"
    "s_mov_b32 s74, 524288\n"
    "buffer_load_dwordx4 v5, s[44:47], s74 offen lds\n"
    "s_add_u32 m0, s59, 61440\n"
    "s_mov_b32 s74, 524416\n"
    "buffer_load_dwordx4 v5, s[44:47], s74 offen lds\n"
    "s_add_u32 m0, s59, 65536\n"
    "s_mov_b32 s74, 524288\n"
    "buffer_load_dwordx4 v5, s[48:51], s74 offen lds\n"
    "s_add_u32 m0, s59, 69632\n"
    "s_mov_b32 s74, 524416\n"
    "buffer_load_dwordx4 v5, s[48:51], s74 offen lds\n"
    "s_add_u32 m0, s59, 73728\n"
    "s_mov_b32 s74, 786432\n"
    "buffer_load_dwordx4 v5, s[40:43], s74 offen lds\n"
    "s_add_u32 m0, s59, 77824\n"
    "s_mov_b32 s74, 786560\n"
    "buffer_load_dwordx4 v5, s[40:43], s74 offen lds\n"
    "s_add_u32 m0, s59, 81920\n"
    "s_mov_b32 s74, 786432\n"
    "buffer_load_dwordx4 v5, s[44:47], s74 offen lds\n"
    "s_add_u32 m0, s59, 86016\n"
    "s_mov_b32 s74, 786560\n"
    "buffer_load_dwordx4 v5, s[44:47], s74 offen lds\n"
    "s_add_u32 m0, s59, 90112\n"
    "s_mov_b32 s74, 786432\n"
    "buffer_load_dwordx4 v5, s[48:51], s74 offen lds\n"
    "s_add_u32 m0, s59, 94208\n"
    "s_mov_b32 s74, 786560\n"
    "buffer_load_dwordx4 v5, s[48:51], s74 offen lds\n"
    "s_add_u32 m0, s59, 98304\n"
    "s_mov_b32 s74, 1048576\n"
    "buffer_load_dwordx4 v5, s[40:43], s74 offen lds\n"
    "s_add_u32 m0, s59, 102400\n"
    "s_mov_b32 s74, 1048704\n"
    "buffer_load_dwordx4 v5, s[40:43], s74 offen lds\n"
    "s_add_u32 m0, s59, 106496\n"
    "s_mov_b32 s74, 1048576\n"
    "buffer_load_dwordx4 v5, s[44:47], s74 offen lds\n"
    "s_add_u32 m0, s59, 110592\n"
    "s_mov_b32 s74, 1048704\n"
    "buffer_load_dwordx4 v5, s[44:47], s74 offen lds\n"
    "s_add_u32 m0, s59, 114688\n"
    "s_mov_b32 s74, 1048576\n"
    "buffer_load_dwordx4 v5, s[48:51], s74 offen lds\n"
    "s_add_u32 m0, s59, 118784\n"
    "s_mov_b32 s74, 1048704\n"
    "buffer_load_dwordx4 v5, s[48:51], s74 offen lds\n"
    "v_add_u32 v16, 49152, v6\n"
    "v_add_u32 v17, 98304, v6\n"
    "v_add_u32 v18, 49152, v7\n"
    "v_add_u32 v19, 98304, v7\n"
    "s_waitcnt vmcnt(24) lgkmcnt(0)\n"
    "s_barrier\n"
    "s_add_u32 m0, s59, 122880\n"
    "s_mov_b32 s74, 1310720\n"
    "buffer_load_dwordx4 v5, s[40:43], s74 offen lds\n"
    "s_add_u32 m0, s59, 126976\n"
    "s_mov_b32 s74, 1310848\n"
    "buffer_load_dwordx4 v5, s[40:43], s74 offen lds\n"
    "s_add_u32 m0, s59, 131072\n"
    "s_mov_b32 s74, 1310720\n"
    "buffer_load_dwordx4 v5, s[44:47], s74 offen lds\n"
    "s_add_u32 m0, s59, 135168\n"
    "s_mov_b32 s74, 1310848\n"
    "buffer_load_dwordx4 v5, s[44:47], s74 offen lds\n"
    "s_add_u32 m0, s59, 139264\n"
    "s_mov_b32 s74, 1310720\n"
    "buffer_load_dwordx4 v5, s[48:51], s74 offen lds\n"
    "s_add_u32 m0, s59, 143360\n"
    "s_mov_b32 s74, 1310848\n"
    "buffer_load_dwordx4 v5, s[48:51], s74 offen lds\n"
    "ds_read_b64_tr_b16 a[0:1], v6 offset:8192\n"
    "ds_read_b64_tr_b16 a[2:3], v6 offset:8704\n"
    "ds_read_b64_tr_b16 a[4:5], v6 offset:8200\n"
    "ds_read_b64_tr_b16 a[6:7], v6 offset:8712\n"
    "ds_read_b64_tr_b16 a[8:9], v6 offset:16384\n"
    "ds_read_b64_tr_b16 a[10:11], v6 offset:16896\n"
    "ds_read_b64_tr_b16 a[12:13], v6 offset:16392\n"
    "ds_read_b64_tr_b16 a[14:15], v6 offset:16904\n"
    "ds_read_b64_tr_b16 a[16:17], v6 offset:0\n"
    "ds_read_b64_tr_b16 a[18:19], v6 offset:512\n"
    "ds_read_b64_tr_b16 a[20:21], v6 offset:8\n"
    "ds_read_b64_tr_b16 a[22:23], v6 offset:520\n"
    "s_waitcnt vmcnt(24) lgkmcnt(0)\n"
    "s_barrier\n"
    "s_add_u32 m0, s59, 0\n"
    "s_mov_b32 s74, 1572864\n"
    "buffer_load_dwordx4 v5, s[40:43], s74 offen lds\n"
    "s_add_u32 m0, s59, 4096\n"
    "s_mov_b32 s74, 1572992\n"
    "buffer_load_dwordx4 v5, s[40:43], s74 offen lds\n"
    "s_add_u32 m0, s59, 8192\n"
    "s_mov_b32 s74, 1572864\n"
    "buffer_load_dwordx4 v5, s[44:47], s74 offen lds\n"
    "s_add_u32 m0, s59, 12288\n"
    "s_mov_b32 s74, 1572992\n"
    "buffer_load_dwordx4 v5, s[44:47], s74 offen lds\n"
    "s_add_u32 m0, s59, 16384\n"
    "s_mov_b32 s74, 1572864\n"
    "buffer_load_dwordx4 v5, s[48:51], s74 offen lds\n"
    "s_add_u32 m0, s59, 20480\n"
    "s_mov_b32 s74, 1572992\n"
    "buffer_load_dwordx4 v5, s[48:51], s74 offen lds\n"
    "ds_read_b64_tr_b16 a[24:25], v6 offset:32768\n"
    "ds_read_b64_tr_b16 a[26:27], v6 offset:33280\n"
    "ds_read_b64_tr_b16 a[28:29], v6 offset:32776\n"
    "ds_read_b64_tr_b16 a[30:31], v6 offset:33288\n"
    "ds_read_b64_tr_b16 a[32:33], v6 offset:40960\n"
    "ds_read_b64_tr_b16 a[34:35], v6 offset:41472\n"
    "ds_read_b64_tr_b16 a[36:37], v6 offset:40968\n"
    "ds_read_b64_tr_b16 a[38:39], v6 offset:41480\n"
    "ds_read_b64_tr_b16 a[40:41], v6 offset:24576\n"
    "ds_read_b64_tr_b16 a[42:43], v6 offset:25088\n"
    "ds_read_b64_tr_b16 a[44:45], v6 offset:24584\n"
    "ds_read_b64_tr_b16 a[46:47], v6 offset:25096\n"
    "s_waitcnt vmcnt(24) lgkmcnt(0)\n"
    "s_barrier\n"
    "s_add_u32 m0, s59, 24576\n"
    "s_mov_b32 s74, 1835008\n"
    "buffer_load_dwordx4 v5, s[40:43], s74 offen lds\n"
    "s_add_u32 m0, s59, 28672\n"
    "s_mov_b32 s74, 1835136\n"
    "buffer_load_dwordx4 v5, s[40:43], s74 offen lds\n"
    "s_add_u32 m0, s59, 32768\n"
    "s_mov_b32 s74, 1835008\n"
    "buffer_load_dwordx4 v5, s[44:47], s74 offen lds\n"
    "s_add_u32 m0, s59, 36864\n"
    "s_mov_b32 s74, 1835136\n"
    "buffer_load_dwordx4 v5, s[44:47], s74 offen lds\n"
    "s_add_u32 m0, s59, 40960\n"
    "s_mov_b32 s74, 1835008\n"
    "buffer_load_dwordx4 v5, s[48:51], s74 offen lds\n"
    "s_add_u32 m0, s59, 45056\n"
    "s_mov_b32 s74, 1835136\n"
    "buffer_load_dwordx4 v5, s[48:51], s74 offen lds\n"
    "ds_read_b64_tr_b16 a[48:49], v16 offset:8192\n"
    "ds_read_b64_tr_b16 a[50:51], v16 offset:8704\n"
    "ds_read_b64_tr_b16 a[52:53], v16 offset:8200\n"
    "ds_read_b64_tr_b16 a[54:55], v16 offset:8712\n"
    "ds_read_b64_tr_b16 a[56:57], v16 offset:16384\n"
    "ds_read_b64_tr_b16 a[58:59], v16 offset:16896\n"
    "ds_read_b64_tr_b16 a[60:61], v16 offset:16392\n"
    "ds_read_b64_tr_b16 a[62:63], v16 offset:16904\n"
    "ds_read_b64_tr_b16 a[64:65], v16 offset:0\n"
    "ds_read_b64_tr_b16 a[66:67], v16 offset:512\n"
    "ds_read_b64_tr_b16 a[68:69], v16 offset:8\n"
    "ds_read_b64_tr_b16 a[70:71], v16 offset:520\n"
    "s_waitcnt vmcnt(24) lgkmcnt(0)\n"
    "s_barrier\n"
    "s_add_u32 m0, s59, 49152\n"
    "s_mov_b32 s74, 2097152\n"
    "buffer_load_dwordx4 v5, s[40:43], s74 offen lds\n"
    "s_add_u32 m0, s59, 53248\n"
    "s_mov_b32 s74, 2097280\n"
    "buffer_load_dwordx4 v5, s[40:43], s74 offen lds\n"
    "s_add_u32 m0, s59, 57344\n"
    "s_mov_b32 s74, 2097152\n"
    "buffer_load_dwordx4 v5, s[44:47], s74 offen lds\n"
    "s_add_u32 m0, s59, 61440\n"
    "s_mov_b32 s74, 2097280\n"
    "buffer_load_dwordx4 v5, s[44:47], s74 offen lds\n"
    "s_add_u32 m0, s59, 65536\n"
    "s_mov_b32 s74, 2097152\n"
    "buffer_load_dwordx4 v5, s[48:51], s74 offen lds\n"
    "s_add_u32 m0, s59, 69632\n"
    "s_mov_b32 s74, 2097280\n"
    "buffer_load_dwordx4 v5, s[48:51], s74 offen lds\n"
    "ds_read_b64_tr_b16 a[72:73], v16 offset:32768\n"
    "ds_read_b64_tr_b16 a[74:75], v16 offset:33280\n"
    "ds_read_b64_tr_b16 a[76:77], v16 offset:32776\n"
    "ds_read_b64_tr_b16 a[78:79], v16 offset:33288\n"
    "ds_read_b64_tr_b16 a[80:81], v16 offset:40960\n"
    "ds_read_b64_tr_b16 a[82:83], v16 offset:41472\n"
    "ds_read_b64_tr_b16 a[84:85], v16 offset:40968\n"
    "ds_read_b64_tr_b16 a[86:87], v16 offset:41480\n"
    "ds_read_b64_tr_b16 a[88:89], v16 offset:24576\n"
    "ds_read_b64_tr_b16 a[90:91], v16 offset:25088\n"
    "ds_read_b64_tr_b16 a[92:93], v16 offset:24584\n"
    "ds_read_b64_tr_b16 a[94:95], v16 offset:25096\n"
    "s_waitcnt vmcnt(24) lgkmcnt(0)\n"
    "s_barrier\n"
    "s_add_u32 m0, s59, 73728\n"
    "s_mov_b32 s74, 2359296\n"
    "buffer_load_dwordx4 v5, s[40:43], s74 offen lds\n"
    "s_add_u32 m0, s59, 77824\n"
    "s_mov_b32 s74, 2359424\n"
    "buffer_load_dwordx4 v5, s[40:43], s74 offen lds\n"
    "s_add_u32 m0, s59, 81920\n"
    "s_mov_b32 s74, 2359296\n"
    "buffer_load_dwordx4 v5, s[44:47], s74 offen lds\n"
    "s_add_u32 m0, s59, 86016\n"
    "s_mov_b32 s74, 2359424\n"
    "buffer_load_dwordx4 v5, s[44:47], s74 offen lds\n"
    "s_add_u32 m0, s59, 90112\n"
    "s_mov_b32 s74, 2359296\n"
    "buffer_load_dwordx4 v5, s[48:51], s74 offen lds\n"
    "s_add_u32 m0, s59, 94208\n"
    "s_mov_b32 s74, 2359424\n"
    "buffer_load_dwordx4 v5, s[48:51], s74 offen lds\n"
    "ds_read_b64_tr_b16 a[96:97], v17 offset:8192\n"
    "ds_read_b64_tr_b16 a[98:99], v17 offset:8704\n"
    "ds_read_b64_tr_b16 a[100:101], v17 offset:8200\n"
    "ds_read_b64_tr_b16 a[102:103], v17 offset:8712\n"
    "ds_read_b64_tr_b16 a[104:105], v17 offset:16384\n"
    "ds_read_b64_tr_b16 a[106:107], v17 offset:16896\n"
    "ds_read_b64_tr_b16 a[108:109], v17 offset:16392\n"
    "ds_read_b64_tr_b16 a[110:111], v17 offset:16904\n"
    "ds_read_b64_tr_b16 a[112:113], v17 offset:0\n"
    "ds_read_b64_tr_b16 a[114:115], v17 offset:512\n"
    "ds_read_b64_tr_b16 a[116:117], v17 offset:8\n"
    "ds_read_b64_tr_b16 a[118:119], v17 offset:520\n"
    "s_waitcnt vmcnt(24) lgkmcnt(0)\n"
    "s_barrier\n"
    "s_add_u32 m0, s59, 98304\n"
    "s_mov_b32 s74, 2621440\n"
    "buffer_load_dwordx4 v5, s[40:43], s74 offen lds\n"
    "s_add_u32 m0, s59, 102400\n"
    "s_mov_b32 s74, 2621568\n"
    "buffer_load_dwordx4 v5, s[40:43], s74 offen lds\n"
    "s_add_u32 m0, s59, 106496\n"
    "s_mov_b32 s74, 2621440\n"
    "buffer_load_dwordx4 v5, s[44:47], s74 offen lds\n"
    "s_add_u32 m0, s59, 110592\n"
    "s_mov_b32 s74, 2621568\n"
    "buffer_load_dwordx4 v5, s[44:47], s74 offen lds\n"
    "s_add_u32 m0, s59, 114688\n"
    "s_mov_b32 s74, 2621440\n"
    "buffer_load_dwordx4 v5, s[48:51], s74 offen lds\n"
    "s_add_u32 m0, s59, 118784\n"
    "s_mov_b32 s74, 2621568\n"
    "buffer_load_dwordx4 v5, s[48:51], s74 offen lds\n"
    "ds_read_b64_tr_b16 a[120:121], v17 offset:32768\n"
    "ds_read_b64_tr_b16 a[122:123], v17 offset:33280\n"
    "ds_read_b64_tr_b16 a[124:125], v17 offset:32776\n"
    "ds_read_b64_tr_b16 a[126:127], v17 offset:33288\n"
    "ds_read_b64_tr_b16 a[128:129], v17 offset:40960\n"
    "ds_read_b64_tr_b16 a[130:131], v17 offset:41472\n"
    "ds_read_b64_tr_b16 a[132:133], v17 offset:40968\n"
    "ds_read_b64_tr_b16 a[134:135], v17 offset:41480\n"
    "ds_read_b64_tr_b16 a[136:137], v17 offset:24576\n"
    "ds_read_b64_tr_b16 a[138:139], v17 offset:25088\n"
    "ds_read_b64_tr_b16 a[140:141], v17 offset:24584\n"
    "ds_read_b64_tr_b16 a[142:143], v17 offset:25096\n"
    "s_waitcnt vmcnt(24) lgkmcnt(0)\n"
    "s_barrier\n"
    "s_add_u32 m0, s59, 122880\n"
    "s_mov_b32 s74, 2883584\n"
    "buffer_load_dwordx4 v5, s[40:43], s74 offen lds\n"
    "s_add_u32 m0, s59, 126976\n"
    "s_mov_b32 s74, 2883712\n"
    "buffer_load_dwordx4 v5, s[40:43], s74 offen lds\n"
    "s_add_u32 m0, s59, 131072\n"
    "s_mov_b32 s74, 2883584\n"
    "buffer_load_dwordx4 v5, s[44:47], s74 offen lds\n"
    "s_add_u32 m0, s59, 135168\n"
    "s_mov_b32 s74, 2883712\n"
    "buffer_load_dwordx4 v5, s[44:47], s74 offen lds\n"
    "s_add_u32 m0, s59, 139264\n"
    "s_mov_b32 s74, 2883584\n"
    "buffer_load_dwordx4 v5, s[48:51], s74 offen lds\n"
    "s_add_u32 m0, s59, 143360\n"
    "s_mov_b32 s74, 2883712\n"
    "buffer_load_dwordx4 v5, s[48:51], s74 offen lds\n"
    "ds_read_b64_tr_b16 a[144:145], v6 offset:8192\n"
    "ds_read_b64_tr_b16 a[146:147], v6 offset:8704\n"
    "ds_read_b64_tr_b16 a[148:149], v6 offset:8200\n"
    "ds_read_b64_tr_b16 a[150:151], v6 offset:8712\n"
    "ds_read_b64_tr_b16 a[152:153], v6 offset:16384\n"
    "ds_read_b64_tr_b16 a[154:155], v6 offset:16896\n"
    "ds_read_b64_tr_b16 a[156:157], v6 offset:16392\n"
    "ds_read_b64_tr_b16 a[158:159], v6 offset:16904\n"
    "ds_read_b64_tr_b16 a[160:161], v6 offset:0\n"
    "ds_read_b64_tr_b16 a[162:163], v6 offset:512\n"
    "ds_read_b64_tr_b16 a[164:165], v6 offset:8\n"
    "ds_read_b64_tr_b16 a[166:167], v6 offset:520\n"
    "s_waitcnt vmcnt(24) lgkmcnt(0)\n"
    "s_barrier\n"
    "s_add_u32 m0, s59, 0\n"
    "s_mov_b32 s74, 3145728\n"
    "buffer_load_dwordx4 v5, s[40:43], s74 offen lds\n"
    "s_add_u32 m0, s59, 4096\n"
    "s_mov_b32 s74, 3145856\n"
    "buffer_load_dwordx4 v5, s[40:43], s74 offen lds\n"
    "s_add_u32 m0, s59, 8192\n"
    "s_mov_b32 s74, 3145728\n"
    "buffer_load_dwordx4 v5, s[44:47], s74 offen lds\n"
    "s_add_u32 m0, s59, 12288\n"
    "s_mov_b32 s74, 3145856\n"
    "buffer_load_dwordx4 v5, s[44:47], s74 offen lds\n"
    "s_add_u32 m0, s59, 16384\n"
    "s_mov_b32 s74, 3145728\n"
    "buffer_load_dwordx4 v5, s[48:51], s74 offen lds\n"
    "s_add_u32 m0, s59, 20480\n"
    "s_mov_b32 s74, 3145856\n"
    "buffer_load_dwordx4 v5, s[48:51], s74 offen lds\n"
    "ds_read_b64_tr_b16 a[168:169], v6 offset:32768\n"
    "ds_read_b64_tr_b16 a[170:171], v6 offset:33280\n"
    "ds_read_b64_tr_b16 a[172:173], v6 offset:32776\n"
    "ds_read_b64_tr_b16 a[174:175], v6 offset:33288\n"
    "ds_read_b64_tr_b16 a[176:177], v6 offset:40960\n"
    "ds_read_b64_tr_b16 a[178:179], v6 offset:41472\n"
    "ds_read_b64_tr_b16 a[180:181], v6 offset:40968\n"
    "ds_read_b64_tr_b16 a[182:183], v6 offset:41480\n"
    "ds_read_b64_tr_b16 a[184:185], v6 offset:24576\n"
    "ds_read_b64_tr_b16 a[186:187], v6 offset:25088\n"
    "ds_read_b64_tr_b16 a[188:189], v6 offset:24584\n"
    "ds_read_b64_tr_b16 a[190:191], v6 offset:25096\n"
    "s_waitcnt vmcnt(24) lgkmcnt(0)\n"
    "s_barrier\n"
    "s_add_u32 m0, s59, 24576\n"
    "s_mov_b32 s74, 3407872\n"
    "buffer_load_dwordx4 v5, s[40:43], s74 offen lds\n"
    "s_add_u32 m0, s59, 28672\n"
    "s_mov_b32 s74, 3408000\n"
    "buffer_load_dwordx4 v5, s[40:43], s74 offen lds\n"
    "s_add_u32 m0, s59, 32768\n"
    "s_mov_b32 s74, 3407872\n"
    "buffer_load_dwordx4 v5, s[44:47], s74 offen lds\n"
    "s_add_u32 m0, s59, 36864\n"
    "s_mov_b32 s74, 3408000\n"
    "buffer_load_dwordx4 v5, s[44:47], s74 offen lds\n"
    "s_add_u32 m0, s59, 40960\n"
    "s_mov_b32 s74, 3407872\n"
    "buffer_load_dwordx4 v5, s[48:51], s74 offen lds\n"
    "s_add_u32 m0, s59, 45056\n"
    "s_mov_b32 s74, 3408000\n"
    "buffer_load_dwordx4 v5, s[48:51], s74 offen lds\n"
    "ds_read_b64_tr_b16 a[192:193], v16 offset:8192\n"
    "ds_read_b64_tr_b16 a[194:195], v16 offset:8704\n"
    "ds_read_b64_tr_b16 a[196:197], v16 offset:8200\n"
    "ds_read_b64_tr_b16 a[198:199], v16 offset:8712\n"
    "ds_read_b64_tr_b16 a[200:201], v16 offset:16384\n"
    "ds_read_b64_tr_b16 a[202:203], v16 offset:16896\n"
    "ds_read_b64_tr_b16 a[204:205], v16 offset:16392\n"
    "ds_read_b64_tr_b16 a[206:207], v16 offset:16904\n"
    "ds_read_b64_tr_b16 a[208:209], v16 offset:0\n"
    "ds_read_b64_tr_b16 a[210:211], v16 offset:512\n"
    "ds_read_b64_tr_b16 a[212:213], v16 offset:8\n"
    "ds_read_b64_tr_b16 a[214:215], v16 offset:520\n"
    "s_waitcnt vmcnt(24) lgkmcnt(0)\n"
    "s_barrier\n"
    "s_add_u32 m0, s59, 49152\n"
    "s_mov_b32 s74, 3670016\n"
    "buffer_load_dwordx4 v5, s[40:43], s74 offen lds\n"
    "s_add_u32 m0, s59, 53248\n"
    "s_mov_b32 s74, 3670144\n"
    "buffer_load_dwordx4 v5, s[40:43], s74 offen lds\n"
    "s_add_u32 m0, s59, 57344\n"
    "s_mov_b32 s74, 3670016\n"
    "buffer_load_dwordx4 v5, s[44:47], s74 offen lds\n"
    "s_add_u32 m0, s59, 61440\n"
    "s_mov_b32 s74, 3670144\n"
    "buffer_load_dwordx4 v5, s[44:47], s74 offen lds\n"
    "s_add_u32 m0, s59, 65536\n"
    "s_mov_b32 s74, 3670016\n"
    "buffer_load_dwordx4 v5, s[48:51], s74 offen lds\n"
    "s_add_u32 m0, s59, 69632\n"
    "s_mov_b32 s74, 3670144\n"
    "buffer_load_dwordx4 v5, s[48:51], s74 offen lds\n"
    "ds_read_b64_tr_b16 a[216:217], v16 offset:32768\n"
    "ds_read_b64_tr_b16 a[218:219], v16 offset:33280\n"
    "ds_read_b64_tr_b16 a[220:221], v16 offset:32776\n"
    "ds_read_b64_tr_b16 a[222:223], v16 offset:33288\n"
    "ds_read_b64_tr_b16 a[224:225], v16 offset:40960\n"
    "ds_read_b64_tr_b16 a[226:227], v16 offset:41472\n"
    "ds_read_b64_tr_b16 a[228:229], v16 offset:40968\n"
    "ds_read_b64_tr_b16 a[230:231], v16 offset:41480\n"
    "ds_read_b64_tr_b16 a[232:233], v16 offset:24576\n"
    "ds_read_b64_tr_b16 a[234:235], v16 offset:25088\n"
    "ds_read_b64_tr_b16 a[236:237], v16 offset:24584\n"
    "ds_read_b64_tr_b16 a[238:239], v16 offset:25096\n"
    "s_waitcnt vmcnt(24) lgkmcnt(0)\n"
    "s_barrier\n"
    "s_add_u32 m0, s59, 73728\n"
    "s_mov_b32 s74, 3932160\n"
    "buffer_load_dwordx4 v5, s[40:43], s74 offen lds\n"
    "s_add_u32 m0, s59, 77824\n"
    "s_mov_b32 s74, 3932288\n"
    "buffer_load_dwordx4 v5, s[40:43], s74 offen lds\n"
    "s_add_u32 m0, s59, 81920\n"
    "s_mov_b32 s74, 3932160\n"
    "buffer_load_dwordx4 v5, s[44:47], s74 offen lds\n"
    "s_add_u32 m0, s59, 86016\n"
    "s_mov_b32 s74, 3932288\n"
    "buffer_load_dwordx4 v5, s[44:47], s74 offen lds\n"
    "s_add_u32 m0, s59, 90112\n"
    "s_mov_b32 s74, 3932160\n"
    "buffer_load_dwordx4 v5, s[48:51], s74 offen lds\n"
    "s_add_u32 m0, s59, 94208\n"
    "s_mov_b32 s74, 3932288\n"
    "buffer_load_dwordx4 v5, s[48:51], s74 offen lds\n"
    "ds_read_b64_tr_b16 a[240:241], v17 offset:8192\n"
    "ds_read_b64_tr_b16 a[242:243], v17 offset:8704\n"
    "ds_read_b64_tr_b16 a[244:245], v17 offset:8200\n"
    "ds_read_b64_tr_b16 a[246:247], v17 offset:8712\n"
    "ds_read_b64_tr_b16 a[248:249], v17 offset:16384\n"
    "ds_read_b64_tr_b16 a[250:251], v17 offset:16896\n"
    "ds_read_b64_tr_b16 a[252:253], v17 offset:16392\n"
    "ds_read_b64_tr_b16 a[254:255], v17 offset:16904\n"
    "ds_read_b64_tr_b16 v[80:81], v17 offset:0\n"
    "ds_read_b64_tr_b16 v[82:83], v17 offset:512\n"
    "ds_read_b64_tr_b16 v[84:85], v17 offset:8\n"
    "ds_read_b64_tr_b16 v[86:87], v17 offset:520\n"
    "s_waitcnt vmcnt(24) lgkmcnt(0)\n"
    "s_barrier\n"
    "s_add_u32 m0, s59, 98304\n"
    "s_mov_b32 s74, 4194304\n"
    "buffer_load_dwordx4 v5, s[40:43], s74 offen lds\n"
    "s_add_u32 m0, s59, 102400\n"
    "s_mov_b32 s74, 4194432\n"
    "buffer_load_dwordx4 v5, s[40:43], s74 offen lds\n"
    "s_add_u32 m0, s59, 106496\n"
    "s_mov_b32 s74, 4194304\n"
    "buffer_load_dwordx4 v5, s[44:47], s74 offen lds\n"
    "s_add_u32 m0, s59, 110592\n"
    "s_mov_b32 s74, 4194432\n"
    "buffer_load_dwordx4 v5, s[44:47], s74 offen lds\n"
    "s_add_u32 m0, s59, 114688\n"
    "s_mov_b32 s74, 4194304\n"
    "buffer_load_dwordx4 v5, s[48:51], s74 offen lds\n"
    "s_add_u32 m0, s59, 118784\n"
    "s_mov_b32 s74, 4194432\n"
    "buffer_load_dwordx4 v5, s[48:51], s74 offen lds\n"
    "ds_read_b64_tr_b16 v[88:89], v17 offset:32768\n"
    "ds_read_b64_tr_b16 v[90:91], v17 offset:33280\n"
    "ds_read_b64_tr_b16 v[92:93], v17 offset:32776\n"
    "ds_read_b64_tr_b16 v[94:95], v17 offset:33288\n"
    "ds_read_b64_tr_b16 v[96:97], v17 offset:40960\n"
    "ds_read_b64_tr_b16 v[98:99], v17 offset:41472\n"
    "ds_read_b64_tr_b16 v[100:101], v17 offset:40968\n"
    "ds_read_b64_tr_b16 v[102:103], v17 offset:41480\n"
    "ds_read_b64_tr_b16 v[104:105], v17 offset:24576\n"
    "ds_read_b64_tr_b16 v[106:107], v17 offset:25088\n"
    "ds_read_b64_tr_b16 v[108:109], v17 offset:24584\n"
    "ds_read_b64_tr_b16 v[110:111], v17 offset:25096\n"
    "s_waitcnt vmcnt(24) lgkmcnt(0)\n"
    "s_barrier\n"
    "s_add_u32 m0, s59, 122880\n"
    "s_mov_b32 s74, 4456448\n"
    "buffer_load_dwordx4 v5, s[40:43], s74 offen lds\n"
    "s_add_u32 m0, s59, 126976\n"
    "s_mov_b32 s74, 4456576\n"
    "buffer_load_dwordx4 v5, s[40:43], s74 offen lds\n"
    "s_add_u32 m0, s59, 131072\n"
    "s_mov_b32 s74, 4456448\n"
    "buffer_load_dwordx4 v5, s[44:47], s74 offen lds\n"
    "s_add_u32 m0, s59, 135168\n"
    "s_mov_b32 s74, 4456576\n"
    "buffer_load_dwordx4 v5, s[44:47], s74 offen lds\n"
    "s_add_u32 m0, s59, 139264\n"
    "s_mov_b32 s74, 4456448\n"
    "buffer_load_dwordx4 v5, s[48:51], s74 offen lds\n"
    "s_add_u32 m0, s59, 143360\n"
    "s_mov_b32 s74, 4456576\n"
    "buffer_load_dwordx4 v5, s[48:51], s74 offen lds\n"
    "buffer_load_dwordx2 v[12:13], off, s[36:39], 0 sc1\n"
    "ds_read_b64_tr_b16 v[112:113], v6 offset:8192\n"
    "ds_read_b64_tr_b16 v[114:115], v6 offset:8704\n"
    "ds_read_b64_tr_b16 v[116:117], v6 offset:8200\n"
    "ds_read_b64_tr_b16 v[118:119], v6 offset:8712\n"
    "ds_read_b64_tr_b16 v[120:121], v6 offset:16384\n"
    "ds_read_b64_tr_b16 v[122:123], v6 offset:16896\n"
    "ds_read_b64_tr_b16 v[124:125], v6 offset:16392\n"
    "ds_read_b64_tr_b16 v[126:127], v6 offset:16904\n"
    "ds_read_b64_tr_b16 v[128:129], v6 offset:0\n"
    "ds_read_b64_tr_b16 v[130:131], v6 offset:512\n"
    "ds_read_b64_tr_b16 v[132:133], v6 offset:8\n"
    "ds_read_b64_tr_b16 v[134:135], v6 offset:520\n"
    "s_waitcnt vmcnt(25) lgkmcnt(0)\n"
    "s_barrier\n"
    "ds_read_b64_tr_b16 v[136:137], v6 offset:32768\n"
    "ds_read_b64_tr_b16 v[138:139], v6 offset:33280\n"
    "ds_read_b64_tr_b16 v[140:141], v6 offset:32776\n"
    "ds_read_b64_tr_b16 v[142:143], v6 offset:33288\n"
    "ds_read_b64_tr_b16 v[144:145], v6 offset:40960\n"
    "ds_read_b64_tr_b16 v[146:147], v6 offset:41472\n"
    "ds_read_b64_tr_b16 v[148:149], v6 offset:40968\n"
    "ds_read_b64_tr_b16 v[150:151], v6 offset:41480\n"
    "ds_read_b64_tr_b16 v[152:153], v6 offset:24576\n"
    "ds_read_b64_tr_b16 v[154:155], v6 offset:25088\n"
    "ds_read_b64_tr_b16 v[156:157], v6 offset:24584\n"
    "ds_read_b64_tr_b16 v[158:159], v6 offset:25096\n"
    "s_waitcnt vmcnt(19) lgkmcnt(0)\n"
    "s_barrier\n"
    "ds_read_b64_tr_b16 v[160:161], v16 offset:8192\n"
    "ds_read_b64_tr_b16 v[162:163], v16 offset:8704\n"
    "ds_read_b64_tr_b16 v[164:165], v16 offset:8200\n"
    "ds_read_b64_tr_b16 v[166:167], v16 offset:8712\n"
    "ds_read_b64_tr_b16 v[168:169], v16 offset:16384\n"
    "ds_read_b64_tr_b16 v[170:171], v16 offset:16896\n"
    "ds_read_b64_tr_b16 v[172:173], v16 offset:16392\n"
    "ds_read_b64_tr_b16 v[174:175], v16 offset:16904\n"
    "ds_read_b64_tr_b16 v[176:177], v16 offset:0\n"
    "ds_read_b64_tr_b16 v[178:179], v16 offset:512\n"
    "ds_read_b64_tr_b16 v[180:181], v16 offset:8\n"
    "ds_read_b64_tr_b16 v[182:183], v16 offset:520\n"
    "s_waitcnt vmcnt(13) lgkmcnt(0)\n"
    "s_barrier\n"
    "ds_read_b64_tr_b16 v[184:185], v16 offset:32768\n"
    "ds_read_b64_tr_b16 v[186:187], v16 offset:33280\n"
    "ds_read_b64_tr_b16 v[188:189], v16 offset:32776\n"
    "ds_read_b64_tr_b16 v[190:191], v16 offset:33288\n"
    "ds_read_b64_tr_b16 v[192:193], v16 offset:40960\n"
    "ds_read_b64_tr_b16 v[194:195], v16 offset:41472\n"
    "ds_read_b64_tr_b16 v[196:197], v16 offset:40968\n"
    "ds_read_b64_tr_b16 v[198:199], v16 offset:41480\n"
    "ds_read_b64_tr_b16 v[200:201], v16 offset:24576\n"
    "ds_read_b64_tr_b16 v[202:203], v16 offset:25088\n"
    "ds_read_b64_tr_b16 v[204:205], v16 offset:24584\n"
    "ds_read_b64_tr_b16 v[206:207], v16 offset:25096\n"
    "s_waitcnt vmcnt(7) lgkmcnt(0)\n"
    "s_barrier\n"
    "ds_read_b64_tr_b16 v[208:209], v17 offset:8192\n"
    "ds_read_b64_tr_b16 v[210:211], v17 offset:8704\n"
    "ds_read_b64_tr_b16 v[212:213], v17 offset:8200\n"
    "ds_read_b64_tr_b16 v[214:215], v17 offset:8712\n"
    "ds_read_b64_tr_b16 v[216:217], v17 offset:16384\n"
    "ds_read_b64_tr_b16 v[218:219], v17 offset:16896\n"
    "ds_read_b64_tr_b16 v[220:221], v17 offset:16392\n"
    "ds_read_b64_tr_b16 v[222:223], v17 offset:16904\n"
    "ds_read_b64_tr_b16 v[224:225], v17 offset:0\n"
    "ds_read_b64_tr_b16 v[226:227], v17 offset:512\n"
    "ds_read_b64_tr_b16 v[228:229], v17 offset:8\n"
    "ds_read_b64_tr_b16 v[230:231], v17 offset:520\n"
    "s_waitcnt vmcnt(0) lgkmcnt(0)\n"
    "s_barrier\n"
    ".LgP_par_%=:\n"
    "v_readfirstlane_b32 s74, v12\n"
    "v_readfirstlane_b32 s75, v13\n"
    "s_xor_b32 s74, s74, s66\n"
    "s_xor_b32 s75, s75, s67\n"
    "s_cselect_b32 s75, 2, 0\n"
    "s_or_b32 s75, s75, s74\n"
    "s_cmp_lt_u32 s75, 2\n"
    "s_cbranch_scc1 .LgP_par1_%=\n"
    "s_sleep 4\n"
    "buffer_load_dwordx2 v[12:13], off, s[36:39], 0 sc1\n"
    "s_waitcnt vmcnt(0)\n"
    "s_sub_u32 s72, s72, 1\n"
    "s_cbranch_scc0 .LgP_par_%=\n"
    "s_mov_b32 s72, 0\n"
    ".LgP_par1_%=:\n"
    "s_and_b32 s74, s74, 1\n"
    "s_lshl_b32 s74, s74, 13\n"
    "s_add_u32 s36, s36, s74\n"
    "s_addc_u32 s37, s37, 0\n"
    "v_add_u32 v13, v11, v3\n"
    "v_cmp_gt_i32 vcc, s71, v13\n"
    "s_nop 0\n"
    "s_or_b64 exec, vcc, s[62:63]\n"
    "buffer_atomic_smax v11, v2, s[36:39], 0 offen sc0 sc1\n"
    "s_mov_b64 exec, -1\n"
    "s_getpc_b64 s[84:85]\n"
    ".LgP_PC2_%=:\n"
    "s_and_b32 s85, s85, 0xffff\n"
    "s_mov_b32 s86, .LgP_FIN_%=-.LgP_PC2_%=\n"
    "s_mov_b32 s87, 0x20000\n"
    "v_mbcnt_lo_u32_b32 v42, -1, 0\n"
    "v_mbcnt_hi_u32_b32 v42, -1, v42\n"
    "v_lshlrev_b32 v42, 7, v42\n"
    "buffer_load_dword v43, v42, s[84:87], 0 offen\n"
    "v_add_u32 v42, 8192, v42\n"
    "buffer_load_dword v43, v42, s[84:87], 0 offen\n"
    "v_add_u32 v42, 8192, v42\n"
    "buffer_load_dword v43, v42, s[84:87], 0 offen\n"
    "ds_read_b64_tr_b16 v[232:233], v17 offset:32768\n"
    "ds_read_b64_tr_b16 v[234:235], v17 offset:33280\n"
    "ds_read_b64_tr_b16 v[236:237], v17 offset:32776\n"
    "ds_read_b64_tr_b16 v[238:239], v17 offset:33288\n"
    "ds_read_b64_tr_b16 v[240:241], v17 offset:40960\n"
    "ds_read_b64_tr_b16 v[242:243], v17 offset:41472\n"
    "ds_read_b64_tr_b16 v[244:245], v17 offset:40968\n"
    "ds_read_b64_tr_b16 v[246:247], v17 offset:41480\n"
    "ds_read_b64_tr_b16 v[248:249], v17 offset:24576\n"
    "ds_read_b64_tr_b16 v[250:251], v17 offset:25088\n"
    "ds_read_b64_tr_b16 v[252:253], v17 offset:24584\n"
    "ds_read_b64_tr_b16 v[254:255], v17 offset:25096\n"
    "s_waitcnt lgkmcnt(0)\n"
    "s_barrier\n"
    "s_waitcnt vmcnt(0)\n"
    "v_mov_b32 v5, v11\n"
    "v_mov_b32 v12, -1\n"
    "s_mov_b32 s73, 0x80000000\n"
    "s_mov_b32 s79, 0x80000000\n"
    "s_mov_b32 s80, 0x80000000\n"
    "s_branch .LgP_block1_1_%=\n"
    ".LgP_LOOPB_%=:\n"
    ".LgP_head0_%=:\n"
    "v_mfma_f32_16x16x32_bf16 v[32:35], a[0:3], v[16:19], 0\n"
    "v_mfma_f32_16x16x32_bf16 v[36:39], a[4:7], v[16:19], 0\n"
    "v_cvt_pk_bf16_f32 v40, v40, v41\n"
    "v_cvt_pk_bf16_f32 v41, v42, v43\n"
    "v_mfma_f32_16x16x32_bf16 v[48:51], v[16:19], a[8:11], 0\n"
    "v_cvt_pk_bf16_f32 v42, v44, v45\n"
    "v_cvt_pk_bf16_f32 v43, v46, v47\n"
    "v_mfma_f32_16x16x32_bf16 v[56:59], v[16:19], a[12:15], 0\n"
    "buffer_store_dwordx4 v[40:43], v1, s[36:39], s79 offen offset:1024 sc1\n"
    "v_mfma_f32_16x16x32_bf16 v[64:67], a[16:19], v[16:19], 0\n"
    "s_waitcnt lgkmcnt(1)\n"
    "v_cvt_pk_bf16_f32 v55, v54, v55\n"
    "v_cvt_pk_bf16_f32 v54, v52, v53\n"
    "v_mfma_f32_16x16x32_bf16 v[68:71], a[20:23], v[16:19], 0\n"
    "ds_read_b128 v[16:19], v0 offset:3072\n"
    "v_mov_b64 v[52:53], v[28:29]\n"
    "v_mfma_f32_16x16x32_bf16 v[32:35], a[24:27], v[20:23], v[32:35]\n"
    "buffer_store_dwordx4 v[52:55], v1, s[36:39], s80 offen sc1\n"
    "v_mfma_f32_16x16x32_bf16 v[36:39], a[28:31], v[20:23], v[36:39]\n"
    "s_waitcnt vmcnt(3)\n"
    "v_cvt_pk_bf16_f32 v63, v62, v63\n"
    "v_add_u32 v13, v11, v3\n"
    "v_mfma_f32_16x16x32_bf16 v[48:51], v[20:23], a[32:35], v[48:51]\n"
    "v_cvt_pk_bf16_f32 v62, v60, v61\n"
    "v_cmp_gt_i32 vcc, s71, v13\n"
    "v_mfma_f32_16x16x32_bf16 v[56:59], v[20:23], a[36:39], v[56:59]\n"
    "buffer_atomic_smax v7, v10, s[36:39], s69 offen sc1\n"
    "v_mov_b64 v[60:61], v[30:31]\n"
    "v_mfma_f32_16x16x32_bf16 v[64:67], a[40:43], v[20:23], v[64:67]\n"
    "s_waitcnt lgkmcnt(1)\n"
    "v_cvt_pk_bf16_f32 v72, v72, v73\n"
    "v_cvt_pk_bf16_f32 v73, v74, v75\n"
    "v_mfma_f32_16x16x32_bf16 v[68:71], a[44:47], v[20:23], v[68:71]\n"
    "ds_read_b128 v[20:23], v0 offset:4096\n"
    "s_cbranch_vccnz .LgP_poll0_%=\n"
    "v_mfma_f32_16x16x32_bf16 v[32:35], a[48:51], v[24:27], v[32:35]\n"
    "s_add_u32 m0, s59, 20480\n"
    "v_mfma_f32_16x16x32_bf16 v[36:39], a[52:55], v[24:27], v[36:39]\n"
    "buffer_store_dwordx4 v[60:63], v1, s[36:39], s80 offen offset:1024 sc1\n"
    "v_mfma_f32_16x16x32_bf16 v[48:51], v[24:27], a[56:59], v[48:51]\n"
    "buffer_load_dwordx4 v1, s[36:39], s76 offen sc1 lds\n"
    "v_cvt_pk_bf16_f32 v74, v76, v77\n"
    "v_cvt_pk_bf16_f32 v75, v78, v79\n"
    "v_mfma_f32_16x16x32_bf16 v[56:59], v[24:27], a[60:63], v[56:59]\n"
    "v_mfma_f32_16x16x32_bf16 v[64:67], a[64:67], v[24:27], v[64:67]\n"
    "s_waitcnt lgkmcnt(1)\n"
    "v_mfma_f32_16x16x32_bf16 v[68:71], a[68:71], v[24:27], v[68:71]\n"
    "ds_read_b128 v[24:27], v0 offset:5120\n"
    "v_mfma_f32_16x16x32_bf16 v[32:35], a[72:75], v[16:19], v[32:35]\n"
    "buffer_store_dwordx4 v[72:75], v1, s[36:39], s73 offen offset:1024 sc1\n"
    "v_mfma_f32_16x16x32_bf16 v[36:39], a[76:79], v[16:19], v[36:39]\n"
    "s_lshr_b32 s73, s68, 1\n"
    "v_mfma_f32_16x16x32_bf16 v[48:51], v[16:19], a[80:83], v[48:51]\n"
    "s_add_u32 s76, s76, 16384\n"
    "s_add_u32 m0, s59, 24576\n"
    "s_and_b32 s73, s73, s57\n"
    "v_mfma_f32_16x16x32_bf16 v[56:59], v[16:19], a[84:87], v[56:59]\n"
    "buffer_load_dwordx4 v1, s[36:39], s76 offen sc1 lds\n"
    "s_mul_i32 s73, s73, s56\n"
    "v_mfma_f32_16x16x32_bf16 v[64:67], a[88:91], v[16:19], v[64:67]\n"
    "s_waitcnt lgkmcnt(1)\n"
    "s_add_u32 s73, s73, s55\n"
    "v_mfma_f32_16x16x32_bf16 v[68:71], a[92:95], v[16:19], v[68:71]\n"
    "ds_read_b128 v[16:19], v0 offset:6144\n"
    "s_add_u32 s79, s73, 16384\n"
    "v_mfma_f32_16x16x32_bf16 v[32:35], a[96:99], v[20:23], v[32:35]\n"
    "s_add_u32 s80, s73, 32768\n"
    "v_mfma_f32_16x16x32_bf16 v[36:39], a[100:103], v[20:23], v[36:39]\n"
    "v_mfma_f32_16x16x32_bf16 v[48:51], v[20:23], a[104:107], v[48:51]\n"
    "v_mfma_f32_16x16x32_bf16 v[56:59], v[20:23], a[108:111], v[56:59]\n"
    "v_mfma_f32_16x16x32_bf16 v[64:67], a[112:115], v[20:23], v[64:67]\n"
    "s_waitcnt lgkmcnt(1)\n"
    "v_mfma_f32_16x16x32_bf16 v[68:71], a[116:119], v[20:23], v[68:71]\n"
    "ds_read_b128 v[20:23], v0 offset:7168\n"
    "v_mfma_f32_16x16x32_bf16 v[32:35], a[120:123], v[24:27], v[32:35]\n"
    "s_add_u32 s76, s76, 16384\n"
    "s_add_u32 m0, s59, 28672\n"
    "v_mfma_f32_16x16x32_bf16 v[36:39], a[124:127], v[24:27], v[36:39]\n"
    "buffer_load_dwordx4 v1, s[36:39], s76 offen sc1 lds\n"
    "v_mfma_f32_16x16x32_bf16 v[48:51], v[24:27], a[128:131], v[48:51]\n"
    "v_mfma_f32_16x16x32_bf16 v[56:59], v[24:27], a[132:135], v[56:59]\n"
    "v_mfma_f32_16x16x32_bf16 v[64:67], a[136:139], v[24:27], v[64:67]\n"
    "s_waitcnt lgkmcnt(1)\n"
    "v_mfma_f32_16x16x32_bf16 v[68:71], a[140:143], v[24:27], v[68:71]\n"
    "ds_read_b128 v[24:27], v0 offset:8192\n"
    "v_mfma_f32_16x16x32_bf16 v[32:35], a[144:147], v[16:19], v[32:35]\n"
    "v_mfma_f32_16x16x32_bf16 v[36:39], a[148:151], v[16:19], v[36:39]\n"
    "v_mfma_f32_16x16x32_bf16 v[48:51], v[16:19], a[152:155], v[48:51]\n"
    "v_mfma_f32_16x16x32_bf16 v[56:59], v[16:19], a[156:159], v[56:59]\n"
    "v_mfma_f32_16x16x32_bf16 v[64:67], a[160:163], v[16:19], v[64:67]\n"
    "s_waitcnt lgkmcnt(1)\n"
    "s_add_u32 s76, s76, 16384\n"
    "s_add_u32 m0, s59, 32768\n"
    "v_mfma_f32_16x16x32_bf16 v[68:71], a[164:167], v[16:19], v[68:71]\n"
    "ds_read_b128 v[16:19], v0 offset:9216\n"
    "buffer_load_dwordx4 v1, s[36:39], s76 offen sc1 lds\n"
    "v_mfma_f32_16x16x32_bf16 v[32:35], a[168:171], v[20:23], v[32:35]\n"
    "v_mfma_f32_16x16x32_bf16 v[36:39], a[172:175], v[20:23], v[36:39]\n"
    "v_mfma_f32_16x16x32_bf16 v[48:51], v[20:23], a[176:179], v[48:51]\n"
    "v_mfma_f32_16x16x32_bf16 v[56:59], v[20:23], a[180:183], v[56:59]\n"
    "v_mfma_f32_16x16x32_bf16 v[64:67], a[184:187], v[20:23], v[64:67]\n"
    "s_waitcnt lgkmcnt(1)\n"
    "v_mfma_f32_16x16x32_bf16 v[68:71], a[188:191], v[20:23], v[68:71]\n"
    "ds_read_b128 v[20:23], v0 offset:10240\n"
    "v_mfma_f32_16x16x32_bf16 v[32:35], a[192:195], v[24:27], v[32:35]\n"
    "v_mfma_f32_16x16x32_bf16 v[36:39], a[196:199], v[24:27], v[36:39]\n"
    "v_mfma_f32_16x16x32_bf16 v[48:51], v[24:27], a[200:203], v[48:51]\n"
    "s_add_u32 s76, s76, 16384\n"
    "s_add_u32 m0, s59, 36864\n"
    "v_mfma_f32_16x16x32_bf16 v[56:59], v[24:27], a[204:207], v[56:59]\n"
    "buffer_load_dwordx4 v8, s[36:39], s76 offen sc1 lds\n"
    "v_mfma_f32_16x16x32_bf16 v[64:67], a[208:211], v[24:27], v[64:67]\n"
    "s_waitcnt lgkmcnt(1)\n"
    "v_mfma_f32_16x16x32_bf16 v[68:71], a[212:215], v[24:27], v[68:71]\n"
    "ds_read_b128 v[24:27], v0 offset:11264\n"
    "v_mfma_f32_16x16x32_bf16 v[32:35], a[216:219], v[16:19], v[32:35]\n"
    "v_mfma_f32_16x16x32_bf16 v[36:39], a[220:223], v[16:19], v[36:39]\n"
    "v_mfma_f32_16x16x32_bf16 v[48:51], v[16:19], a[224:227], v[48:51]\n"
    "v_mfma_f32_16x16x32_bf16 v[56:59], v[16:19], a[228:231], v[56:59]\n"
    "v_mfma_f32_16x16x32_bf16 v[64:67], a[232:235], v[16:19], v[64:67]\n"
    "s_waitcnt lgkmcnt(1)\n"
    "s_waitcnt vmcnt(4)\n"
    "buffer_atomic_smax v12, v10, s[36:39], s70 offen sc1\n"
    "v_mfma_f32_16x16x32_bf16 v[68:71], a[236:239], v[16:19], v[68:71]\n"
    "ds_read_b128 v[16:19], v0 offset:12288\n"
    "v_mfma_f32_16x16x32_bf16 v[32:35], a[240:243], v[20:23], v[32:35]\n"
    "v_mfma_f32_16x16x32_bf16 v[36:39], a[244:247], v[20:23], v[36:39]\n"
    "v_mfma_f32_16x16x32_bf16 v[48:51], v[20:23], a[248:251], v[48:51]\n"
    "v_mfma_f32_16x16x32_bf16 v[56:59], v[20:23], a[252:255], v[56:59]\n"
    "v_mfma_f32_16x16x32_bf16 v[64:67], v[80:83], v[20:23], v[64:67]\n"
    "s_waitcnt lgkmcnt(1)\n"
    "v_mfma_f32_16x16x32_bf16 v[68:71], v[84:87], v[20:23], v[68:71]\n"
    "ds_read_b128 v[20:23], v0 offset:13312\n"
    "v_mfma_f32_16x16x32_bf16 v[32:35], v[88:91], v[24:27], v[32:35]\n"
    "v_mfma_f32_16x16x32_bf16 v[36:39], v[92:95], v[24:27], v[36:39]\n"
    "v_mfma_f32_16x16x32_bf16 v[48:51], v[24:27], v[96:99], v[48:51]\n"
    "v_mfma_f32_16x16x32_bf16 v[56:59], v[24:27], v[100:103], v[56:59]\n"
    "v_mfma_f32_16x16x32_bf16 v[64:67], v[104:107], v[24:27], v[64:67]\n"
    "s_waitcnt lgkmcnt(1)\n"
    "v_mfma_f32_16x16x32_bf16 v[68:71], v[108:111], v[24:27], v[68:71]\n"
    "ds_read_b128 v[24:27], v0 offset:14336\n"
    "v_mfma_f32_16x16x32_bf16 v[32:35], v[112:115], v[16:19], v[32:35]\n"
    "v_mfma_f32_16x16x32_bf16 v[36:39], v[116:119], v[16:19], v[36:39]\n"
    "v_mfma_f32_16x16x32_bf16 v[48:51], v[16:19], v[120:123], v[48:51]\n"
    "v_mfma_f32_16x16x32_bf16 v[56:59], v[16:19], v[124:127], v[56:59]\n"
    "v_mfma_f32_16x16x32_bf16 v[64:67], v[128:131], v[16:19], v[64:67]\n"
    "s_waitcnt lgkmcnt(1)\n"
    "v_mfma_f32_16x16x32_bf16 v[68:71], v[132:135], v[16:19], v[68:71]\n"
    "ds_read_b128 v[16:19], v0 offset:15360\n"
    ".LgP_join0_%=:\n"
    "v_mfma_f32_16x16x32_bf16 v[32:35], v[136:139], v[20:23], v[32:35]\n"
    "s_add_u32 s71, s68, 2\n"
    "s_waitcnt vmcnt(5)\n"
    "v_mfma_f32_16x16x32_bf16 v[36:39], v[140:143], v[20:23], v[36:39]\n"
    "v_add_u32 v13, v11, v3\n"
    "v_mfma_f32_16x16x32_bf16 v[48:51], v[20:23], v[144:147], v[48:51]\n"
    "v_cmp_gt_i32 vcc, s71, v13\n"
    "v_mfma_f32_16x16x32_bf16 v[56:59], v[20:23], v[148:151], v[56:59]\n"
    "v_mfma_f32_16x16x32_bf16 v[64:67], v[152:155], v[20:23], v[64:67]\n"
    "s_waitcnt lgkmcnt(1)\n"
    "v_mfma_f32_16x16x32_bf16 v[68:71], v[156:159], v[20:23], v[68:71]\n"
    "ds_read_b128 v[20:23], v0 offset:16384\n"
    "v_mfma_f32_16x16x32_bf16 v[32:35], v[160:163], v[24:27], v[32:35]\n"
    "s_or_b64 exec, vcc, s[62:63]\n"
    "buffer_atomic_smax v11, v2, s[36:39], 0 offen sc0 sc1\n"
    "s_mov_b64 exec, -1\n"
    "v_mfma_f32_16x16x32_bf16 v[36:39], v[164:167], v[24:27], v[36:39]\n"
    "s_lshr_b32 s76, s71, 1\n"
    "v_mfma_f32_16x16x32_bf16 v[48:51], v[24:27], v[168:171], v[48:51]\n"
    "s_and_b32 s76, s76, s54\n"
    "v_mfma_f32_16x16x32_bf16 v[56:59], v[24:27], v[172:175], v[56:59]\n"
    "s_mul_i32 s76, s76, s53\n"
    "v_mfma_f32_16x16x32_bf16 v[64:67], v[176:179], v[24:27], v[64:67]\n"
    "s_waitcnt lgkmcnt(1)\n"
    "s_add_u32 s76, s76, s52\n"
    "v_mfma_f32_16x16x32_bf16 v[68:71], v[180:183], v[24:27], v[68:71]\n"
    "ds_read_b128 v[24:27], v0 offset:17408\n"
    "v_mfma_f32_16x16x32_bf16 v[32:35], v[184:187], v[16:19], v[32:35]\n"
    "v_mfma_f32_16x16x32_bf16 v[36:39], v[188:191], v[16:19], v[36:39]\n"
    "v_mfma_f32_16x16x32_bf16 v[48:51], v[16:19], v[192:195], v[48:51]\n"
    "v_mfma_f32_16x16x32_bf16 v[56:59], v[16:19], v[196:199], v[56:59]\n"
    "s_waitcnt vmcnt(1) lgkmcnt(0)\n"
    "s_barrier\n"
    "v_mfma_f32_16x16x32_bf16 v[64:67], v[200:203], v[16:19], v[64:67]\n"
    "v_mfma_f32_16x16x32_bf16 v[68:71], v[204:207], v[16:19], v[68:71]\n"
    "ds_read_b128 v[16:19], v9 offset:0\n"
    "v_mfma_f32_16x16x32_bf16 v[32:35], v[208:211], v[20:23], v[32:35]\n"
    "v_mfma_f32_16x16x32_bf16 v[36:39], v[212:215], v[20:23], v[36:39]\n"
    "v_add_u32 v13, v5, v3\n"
    "v_mfma_f32_16x16x32_bf16 v[48:51], v[20:23], v[216:219], v[48:51]\n"
    "v_cmp_gt_i32 vcc, s71, v13\n"
    "v_mfma_f32_16x16x32_bf16 v[56:59], v[20:23], v[220:223], v[56:59]\n"
    "v_mfma_f32_16x16x32_bf16 v[64:67], v[224:227], v[20:23], v[64:67]\n"
    "v_mfma_f32_16x16x32_bf16 v[68:71], v[228:231], v[20:23], v[68:71]\n"
    "ds_read_b128 v[20:23], v9 offset:1024\n"
    "v_mfma_f32_16x16x32_bf16 v[32:35], v[232:235], v[24:27], v[32:35]\n"
    "s_or_b64 exec, vcc, s[62:63]\n"
    "buffer_atomic_smax v5, v2, s[36:39], 0 offen sc0 sc1\n"
    "s_mov_b64 exec, -1\n"
    "v_mfma_f32_16x16x32_bf16 v[36:39], v[236:239], v[24:27], v[36:39]\n"
    "v_mfma_f32_16x16x32_bf16 v[48:51], v[24:27], v[240:243], v[48:51]\n"
    "v_mfma_f32_16x16x32_bf16 v[56:59], v[24:27], v[244:247], v[56:59]\n"
    "s_mov_b32 s69, 0x80000000\n"
    "s_mov_b32 s70, 0\n"
    "v_mov_b32 v12, s68\n"
    "v_mfma_f32_16x16x32_bf16 v[64:67], v[248:251], v[24:27], v[64:67]\n"
    "s_waitcnt lgkmcnt(1)\n"
    "s_add_u32 s68, s68, 1\n"
    "v_mfma_f32_16x16x32_bf16 v[68:71], v[252:255], v[24:27], v[68:71]\n"
    "ds_read_b128 v[24:27], v9 offset:2048\n"
    ".LgP_head1_%=:\n"
    "v_mfma_f32_16x16x32_bf16 v[40:43], a[0:3], v[16:19], 0\n"
    "v_mfma_f32_16x16x32_bf16 v[44:47], a[4:7], v[16:19], 0\n"
    "v_cvt_pk_bf16_f32 v32, v32, v33\n"
    "v_cvt_pk_bf16_f32 v33, v34, v35\n"
    "v_mfma_f32_16x16x32_bf16 v[52:55], v[16:19], a[8:11], 0\n"
    "v_cvt_pk_bf16_f32 v34, v36, v37\n"
    "v_cvt_pk_bf16_f32 v35, v38, v39\n"
    "v_mfma_f32_16x16x32_bf16 v[60:63], v[16:19], a[12:15], 0\n"
    "buffer_store_dwordx4 v[32:35], v1, s[36:39], s79 offen sc1\n"
    "v_mfma_f32_16x16x32_bf16 v[72:75], a[16:19], v[16:19], 0\n"
    "s_waitcnt lgkmcnt(1)\n"
    "v_cvt_pk_bf16_f32 v28, v48, v49\n"
    "v_cvt_pk_bf16_f32 v29, v50, v51\n"
    "v_mfma_f32_16x16x32_bf16 v[76:79], a[20:23], v[16:19], 0\n"
    "ds_read_b128 v[16:19], v9 offset:3072\n"
    "v_cvt_pk_bf16_f32 v30, v56, v57\n"
    "v_mfma_f32_16x16x32_bf16 v[40:43], a[24:27], v[20:23], v[40:43]\n"
    "v_cvt_pk_bf16_f32 v31, v58, v59\n"
    "v_mfma_f32_16x16x32_bf16 v[44:47], a[28:31], v[20:23], v[44:47]\n"
    "s_waitcnt vmcnt(2)\n"
    "v_cvt_pk_bf16_f32 v64, v64, v65\n"
    "v_add_u32 v13, v11, v3\n"
    "v_mfma_f32_16x16x32_bf16 v[52:55], v[20:23], a[32:35], v[52:55]\n"
    "v_cvt_pk_bf16_f32 v65, v66, v67\n"
    "v_cmp_gt_i32 vcc, s71, v13\n"
    "v_mfma_f32_16x16x32_bf16 v[60:63], v[20:23], a[36:39], v[60:63]\n"
    "buffer_atomic_smax v7, v14, s[36:39], s69 offen sc1\n"
    "v_cvt_pk_bf16_f32 v66, v68, v69\n"
    "v_cvt_pk_bf16_f32 v67, v70, v71\n"
    "v_mfma_f32_16x16x32_bf16 v[72:75], a[40:43], v[20:23], v[72:75]\n"
    "s_waitcnt lgkmcnt(1)\n"
    "v_mfma_f32_16x16x32_bf16 v[76:79], a[44:47], v[20:23], v[76:79]\n"
    "ds_read_b128 v[20:23], v9 offset:4096\n"
    "s_cbranch_vccnz .LgP_poll1_%=\n"
    "v_mfma_f32_16x16x32_bf16 v[40:43], a[48:51], v[24:27], v[40:43]\n"
    "s_add_u32 m0, s59, 0\n"
    "v_mfma_f32_16x16x32_bf16 v[44:47], a[52:55], v[24:27], v[44:47]\n"
    "buffer_store_dwordx4 v[64:67], v1, s[36:39], s73 offen sc1\n"
    "v_mfma_f32_16x16x32_bf16 v[52:55], v[24:27], a[56:59], v[52:55]\n"
    "buffer_load_dwordx4 v1, s[36:39], s76 offen sc1 lds\n"
    "s_lshr_b32 s73, s68, 1\n"
    "v_mfma_f32_16x16x32_bf16 v[60:63], v[24:27], a[60:63], v[60:63]\n"
    "s_and_b32 s73, s73, s57\n"
    "v_mfma_f32_16x16x32_bf16 v[72:75], a[64:67], v[24:27], v[72:75]\n"
    "s_waitcnt lgkmcnt(1)\n"
    "s_mul_i32 s73, s73, s56\n"
    "v_mfma_f32_16x16x32_bf16 v[76:79], a[68:71], v[24:27], v[76:79]\n"
    "ds_read_b128 v[24:27], v9 offset:5120\n"
    "s_add_u32 s73, s73, s55\n"
    "v_mfma_f32_16x16x32_bf16 v[40:43], a[72:75], v[16:19], v[40:43]\n"
    "s_add_u32 s79, s73, 16384\n"
    "v_mfma_f32_16x16x32_bf16 v[44:47], a[76:79], v[16:19], v[44:47]\n"
    "s_add_u32 s80, s73, 32768\n"
    "v_mfma_f32_16x16x32_bf16 v[52:55], v[16:19], a[80:83], v[52:55]\n"
    "s_add_u32 s76, s76, 16384\n"
    "s_add_u32 m0, s59, 4096\n"
    "v_mfma_f32_16x16x32_bf16 v[60:63], v[16:19], a[84:87], v[60:63]\n"
    "buffer_load_dwordx4 v1, s[36:39], s76 offen sc1 lds\n"
    "v_mfma_f32_16x16x32_bf16 v[72:75], a[88:91], v[16:19], v[72:75]\n"
    "s_waitcnt lgkmcnt(1)\n"
    "v_mfma_f32_16x16x32_bf16 v[76:79], a[92:95], v[16:19], v[76:79]\n"
    "ds_read_b128 v[16:19], v9 offset:6144\n"
    "v_mfma_f32_16x16x32_bf16 v[40:43], a[96:99], v[20:23], v[40:43]\n"
    "v_mfma_f32_16x16x32_bf16 v[44:47], a[100:103], v[20:23], v[44:47]\n"
    "v_mfma_f32_16x16x32_bf16 v[52:55], v[20:23], a[104:107], v[52:55]\n"
    "v_mfma_f32_16x16x32_bf16 v[60:63], v[20:23], a[108:111], v[60:63]\n"
    "v_mfma_f32_16x16x32_bf16 v[72:75], a[112:115], v[20:23], v[72:75]\n"
    "s_waitcnt lgkmcnt(1)\n"
    "v_mfma_f32_16x16x32_bf16 v[76:79], a[116:119], v[20:23], v[76:79]\n"
    "ds_read_b128 v[20:23], v9 offset:7168\n"
    "v_mfma_f32_16x16x32_bf16 v[40:43], a[120:123], v[24:27], v[40:43]\n"
    "s_add_u32 s76, s76, 16384\n"
    "s_add_u32 m0, s59, 8192\n"
    "v_mfma_f32_16x16x32_bf16 v[44:47], a[124:127], v[24:27], v[44:47]\n"
    "buffer_load_dwordx4 v1, s[36:39], s76 offen sc1 lds\n"
    "v_mfma_f32_16x16x32_bf16 v[52:55], v[24:27], a[128:131], v[52:55]\n"
    "v_mfma_f32_16x16x32_bf16 v[60:63], v[24:27], a[132:135], v[60:63]\n"
    "v_mfma_f32_16x16x32_bf16 v[72:75], a[136:139], v[24:27], v[72:75]\n"
    "s_waitcnt lgkmcnt(1)\n"
    "v_mfma_f32_16x16x32_bf16 v[76:79], a[140:143], v[24:27], v[76:79]\n"
    "ds_read_b128 v[24:27], v9 offset:8192\n"
    "v_mfma_f32_16x16x32_bf16 v[40:43], a[144:147], v[16:19], v[40:43]\n"
    "v_mfma_f32_16x16x32_bf16 v[44:47], a[148:151], v[16:19], v[44:47]\n"
    "v_mfma_f32_16x16x32_bf16 v[52:55], v[16:19], a[152:155], v[52:55]\n"
    "v_mfma_f32_16x16x32_bf16 v[60:63], v[16:19], a[156:159], v[60:63]\n"
    "v_mfma_f32_16x16x32_bf16 v[72:75], a[160:163], v[16:19], v[72:75]\n"
    "s_waitcnt lgkmcnt(1)\n"
    "s_add_u32 s76, s76, 16384\n"
    "s_add_u32 m0, s59, 12288\n"
    "v_mfma_f32_16x16x32_bf16 v[76:79], a[164:167], v[16:19], v[76:79]\n"
    "ds_read_b128 v[16:19], v9 offset:9216\n"
    "buffer_load_dwordx4 v1, s[36:39], s76 offen sc1 lds\n"
    "v_mfma_f32_16x16x32_bf16 v[40:43], a[168:171], v[20:23], v[40:43]\n"
    "v_mfma_f32_16x16x32_bf16 v[44:47], a[172:175], v[20:23], v[44:47]\n"
    "v_mfma_f32_16x16x32_bf16 v[52:55], v[20:23], a[176:179], v[52:55]\n"
    "v_mfma_f32_16x16x32_bf16 v[60:63], v[20:23], a[180:183], v[60:63]\n"
    "v_mfma_f32_16x16x32_bf16 v[72:75], a[184:187], v[20:23], v[72:75]\n"
    "s_waitcnt lgkmcnt(1)\n"
    "v_mfma_f32_16x16x32_bf16 v[76:79], a[188:191], v[20:23], v[76:79]\n"
    "ds_read_b128 v[20:23], v9 offset:10240\n"
    "v_mfma_f32_16x16x32_bf16 v[40:43], a[192:195], v[24:27], v[40:43]\n"
    "v_mfma_f32_16x16x32_bf16 v[44:47], a[196:199], v[24:27], v[44:47]\n"
    "v_mfma_f32_16x16x32_bf16 v[52:55], v[24:27], a[200:203], v[52:55]\n"
    "s_add_u32 s76, s76, 16384\n"
    "s_add_u32 m0, s59, 16384\n"
    "v_mfma_f32_16x16x32_bf16 v[60:63], v[24:27], a[204:207], v[60:63]\n"
    "buffer_load_dwordx4 v8, s[36:39], s76 offen sc1 lds\n"
    "v_mfma_f32_16x16x32_bf16 v[72:75], a[208:211], v[24:27], v[72:75]\n"
    "s_waitcnt lgkmcnt(1)\n"
    "s_waitcnt vmcnt(5)\n"
    "buffer_atomic_smax v12, v14, s[36:39], s70 offen sc1\n"
    "v_mfma_f32_16x16x32_bf16 v[76:79], a[212:215], v[24:27], v[76:79]\n"
    "ds_read_b128 v[24:27], v9 offset:11264\n"
    "v_mfma_f32_16x16x32_bf16 v[40:43], a[216:219], v[16:19], v[40:43]\n"
    "v_mfma_f32_16x16x32_bf16 v[44:47], a[220:223], v[16:19], v[44:47]\n"
    "v_mfma_f32_16x16x32_bf16 v[52:55], v[16:19], a[224:227], v[52:55]\n"
    "v_mfma_f32_16x16x32_bf16 v[60:63], v[16:19], a[228:231], v[60:63]\n"
    "v_mfma_f32_16x16x32_bf16 v[72:75], a[232:235], v[16:19], v[72:75]\n"
    "s_waitcnt lgkmcnt(1)\n"
    "v_mfma_f32_16x16x32_bf16 v[76:79], a[236:239], v[16:19], v[76:79]\n"
    "ds_read_b128 v[16:19], v9 offset:12288\n"
    "v_mfma_f32_16x16x32_bf16 v[40:43], a[240:243], v[20:23], v[40:43]\n"
    "v_mfma_f32_16x16x32_bf16 v[44:47], a[244:247], v[20:23], v[44:47]\n"
    "v_mfma_f32_16x16x32_bf16 v[52:55], v[20:23], a[248:251], v[52:55]\n"
    "v_mfma_f32_16x16x32_bf16 v[60:63], v[20:23], a[252:255], v[60:63]\n"
    "v_mfma_f32_16x16x32_bf16 v[72:75], v[80:83], v[20:23], v[72:75]\n"
    "s_waitcnt lgkmcnt(1)\n"
    "v_mfma_f32_16x16x32_bf16 v[76:79], v[84:87], v[20:23], v[76:79]\n"
    "ds_read_b128 v[20:23], v9 offset:13312\n"
    "v_mfma_f32_16x16x32_bf16 v[40:43], v[88:91], v[24:27], v[40:43]\n"
    "v_mfma_f32_16x16x32_bf16 v[44:47], v[92:95], v[24:27], v[44:47]\n"
    "v_mfma_f32_16x16x32_bf16 v[52:55], v[24:27], v[96:99], v[52:55]\n"
    "v_mfma_f32_16x16x32_bf16 v[60:63], v[24:27], v[100:103], v[60:63]\n"
    "v_mfma_f32_16x16x32_bf16 v[72:75], v[104:107], v[24:27], v[72:75]\n"
    "s_waitcnt lgkmcnt(1)\n"
    "v_mfma_f32_16x16x32_bf16 v[76:79], v[108:111], v[24:27], v[76:79]\n"
    "ds_read_b128 v[24:27], v9 offset:14336\n"
    "v_mfma_f32_16x16x32_bf16 v[40:43], v[112:115], v[16:19], v[40:43]\n"
    "v_mfma_f32_16x16x32_bf16 v[44:47], v[116:119], v[16:19], v[44:47]\n"
    "v_mfma_f32_16x16x32_bf16 v[52:55], v[16:19], v[120:123], v[52:55]\n"
    "v_mfma_f32_16x16x32_bf16 v[60:63], v[16:19], v[124:127], v[60:63]\n"
    "v_mfma_f32_16x16x32_bf16 v[72:75], v[128:131], v[16:19], v[72:75]\n"
    "s_waitcnt lgkmcnt(1)\n"
    "v_mfma_f32_16x16x32_bf16 v[76:79], v[132:135], v[16:19], v[76:79]\n"
    "ds_read_b128 v[16:19], v9 offset:15360\n"
    ".LgP_join1_%=:\n"
    "v_mfma_f32_16x16x32_bf16 v[40:43], v[136:139], v[20:23], v[40:43]\n"
    "s_add_u32 s71, s68, 2\n"
    "s_waitcnt vmcnt(5)\n"
    "v_mfma_f32_16x16x32_bf16 v[44:47], v[140:143], v[20:23], v[44:47]\n"
    "v_add_u32 v13, v11, v3\n"
    "v_mfma_f32_16x16x32_bf16 v[52:55], v[20:23], v[144:147], v[52:55]\n"
    "v_cmp_gt_i32 vcc, s71, v13\n"
    "v_mfma_f32_16x16x32_bf16 v[60:63], v[20:23], v[148:151], v[60:63]\n"
    "v_mfma_f32_16x16x32_bf16 v[72:75], v[152:155], v[20:23], v[72:75]\n"
    "s_waitcnt lgkmcnt(1)\n"
    "v_mfma_f32_16x16x32_bf16 v[76:79], v[156:159], v[20:23], v[76:79]\n"
    "ds_read_b128 v[20:23], v9 offset:16384\n"
    "v_mfma_f32_16x16x32_bf16 v[40:43], v[160:163], v[24:27], v[40:43]\n"
    "s_or_b64 exec, vcc, s[62:63]\n"
    "buffer_atomic_smax v11, v2, s[36:39], 0 offen sc0 sc1\n"
    "s_mov_b64 exec, -1\n"
    "v_mfma_f32_16x16x32_bf16 v[44:47], v[164:167], v[24:27], v[44:47]\n"
    "s_lshr_b32 s76, s71, 1\n"
    "v_mfma_f32_16x16x32_bf16 v[52:55], v[24:27], v[168:171], v[52:55]\n"
    "s_and_b32 s76, s76, s54\n"
    "v_mfma_f32_16x16x32_bf16 v[60:63], v[24:27], v[172:175], v[60:63]\n"
    "s_mul_i32 s76, s76, s53\n"
    "v_mfma_f32_16x16x32_bf16 v[72:75], v[176:179], v[24:27], v[72:75]\n"
    "s_waitcnt lgkmcnt(1)\n"
    "s_add_u32 s76, s76, s52\n"
    "v_mfma_f32_16x16x32_bf16 v[76:79], v[180:183], v[24:27], v[76:79]\n"
    "ds_read_b128 v[24:27], v9 offset:17408\n"
    "s_add_u32 s76, s76, 1024\n"
    "v_mfma_f32_16x16x32_bf16 v[40:43], v[184:187], v[16:19], v[40:43]\n"
    "v_mfma_f32_16x16x32_bf16 v[44:47], v[188:191], v[16:19], v[44:47]\n"
    "v_mfma_f32_16x16x32_bf16 v[52:55], v[16:19], v[192:195], v[52:55]\n"
    "v_mfma_f32_16x16x32_bf16 v[60:63], v[16:19], v[196:199], v[60:63]\n"
    "s_waitcnt vmcnt(1) lgkmcnt(0)\n"
    "s_barrier\n"
    "v_mfma_f32_16x16x32_bf16 v[72:75], v[200:203], v[16:19], v[72:75]\n"
    "v_mfma_f32_16x16x32_bf16 v[76:79], v[204:207], v[16:19], v[76:79]\n"
    "ds_read_b128 v[16:19], v0 offset:0\n"
    "v_mfma_f32_16x16x32_bf16 v[40:43], v[208:211], v[20:23], v[40:43]\n"
    "v_mfma_f32_16x16x32_bf16 v[44:47], v[212:215], v[20:23], v[44:47]\n"
    "v_add_u32 v13, v5, v3\n"
    "v_mfma_f32_16x16x32_bf16 v[52:55], v[20:23], v[216:219], v[52:55]\n"
    "v_cmp_gt_i32 vcc, s71, v13\n"
    "v_mfma_f32_16x16x32_bf16 v[60:63], v[20:23], v[220:223], v[60:63]\n"
    "v_mfma_f32_16x16x32_bf16 v[72:75], v[224:227], v[20:23], v[72:75]\n"
    "v_mfma_f32_16x16x32_bf16 v[76:79], v[228:231], v[20:23], v[76:79]\n"
    "ds_read_b128 v[20:23], v0 offset:1024\n"
    "v_mfma_f32_16x16x32_bf16 v[40:43], v[232:235], v[24:27], v[40:43]\n"
    "s_or_b64 exec, vcc, s[62:63]\n"
    "buffer_atomic_smax v5, v2, s[36:39], 0 offen sc0 sc1\n"
    "s_mov_b64 exec, -1\n"
    "v_mfma_f32_16x16x32_bf16 v[44:47], v[236:239], v[24:27], v[44:47]\n"
    "v_mfma_f32_16x16x32_bf16 v[52:55], v[24:27], v[240:243], v[52:55]\n"
    "v_mfma_f32_16x16x32_bf16 v[60:63], v[24:27], v[244:247], v[60:63]\n"
    "s_mov_b32 s69, 0x80000000\n"
    "s_mov_b32 s70, 0\n"
    "v_mov_b32 v12, s68\n"
    "v_mfma_f32_16x16x32_bf16 v[72:75], v[248:251], v[24:27], v[72:75]\n"
    "s_waitcnt lgkmcnt(1)\n"
    "s_add_u32 s68, s68, 1\n"
    "v_mfma_f32_16x16x32_bf16 v[76:79], v[252:255], v[24:27], v[76:79]\n"
    "ds_read_b128 v[24:27], v0 offset:2048\n"
    "s_branch .LgP_head0_%=\n"
    ".LgP_LOOPE_%=:\n"
    ".LgP_done_%=:\n"
    "s_waitcnt vmcnt(0)\n"
    "buffer_atomic_smax v12, v10, s[36:39], 0 offen sc1\n"
    "s_waitcnt vmcnt(0)\n"
    "s_branch .LgP_FIN_%=\n"
    ".LgP_poll0_%=:\n"
    "v_mfma_f32_16x16x32_bf16 v[32:35], a[48:51], v[24:27], v[32:35]\n"
    "s_or_b64 exec, vcc, s[62:63]\n"
    "buffer_atomic_smax v11, v2, s[36:39], 0 offen sc0 sc1\n"
    "s_mov_b64 exec, -1\n"
    "v_mfma_f32_16x16x32_bf16 v[36:39], a[52:55], v[24:27], v[36:39]\n"
    "buffer_store_dwordx4 v[60:63], v1, s[36:39], s80 offen offset:1024 sc1\n"
    "v_mfma_f32_16x16x32_bf16 v[48:51], v[24:27], a[56:59], v[48:51]\n"
    "v_cvt_pk_bf16_f32 v74, v76, v77\n"
    "v_cvt_pk_bf16_f32 v75, v78, v79\n"
    "v_mfma_f32_16x16x32_bf16 v[56:59], v[24:27], a[60:63], v[56:59]\n"
    "v_mfma_f32_16x16x32_bf16 v[64:67], a[64:67], v[24:27], v[64:67]\n"
    "s_waitcnt lgkmcnt(1)\n"
    "v_mfma_f32_16x16x32_bf16 v[68:71], a[68:71], v[24:27], v[68:71]\n"
    "ds_read_b128 v[24:27], v0 offset:5120\n"
    "v_mfma_f32_16x16x32_bf16 v[32:35], a[72:75], v[16:19], v[32:35]\n"
    "buffer_store_dwordx4 v[72:75], v1, s[36:39], s73 offen offset:1024 sc1\n"
    "v_mfma_f32_16x16x32_bf16 v[36:39], a[76:79], v[16:19], v[36:39]\n"
    "s_lshr_b32 s73, s68, 1\n"
    "v_mfma_f32_16x16x32_bf16 v[48:51], v[16:19], a[80:83], v[48:51]\n"
    "s_and_b32 s73, s73, s57\n"
    "v_mfma_f32_16x16x32_bf16 v[56:59], v[16:19], a[84:87], v[56:59]\n"
    "s_mul_i32 s73, s73, s56\n"
    "v_mfma_f32_16x16x32_bf16 v[64:67], a[88:91], v[16:19], v[64:67]\n"
    "s_waitcnt lgkmcnt(1)\n"
    "s_add_u32 s73, s73, s55\n"
    "v_mfma_f32_16x16x32_bf16 v[68:71], a[92:95], v[16:19], v[68:71]\n"
    "ds_read_b128 v[16:19], v0 offset:6144\n"
    "s_add_u32 s79, s73, 16384\n"
    "v_mfma_f32_16x16x32_bf16 v[32:35], a[96:99], v[20:23], v[32:35]\n"
    "s_add_u32 s80, s73, 32768\n"
    "v_mfma_f32_16x16x32_bf16 v[36:39], a[100:103], v[20:23], v[36:39]\n"
    "s_waitcnt vmcnt(6)\n"
    "v_add_u32 v13, v5, v3\n"
    "v_mfma_f32_16x16x32_bf16 v[48:51], v[20:23], a[104:107], v[48:51]\n"
    "v_cmp_gt_i32 vcc, s71, v13\n"
    "v_mfma_f32_16x16x32_bf16 v[56:59], v[20:23], a[108:111], v[56:59]\n"
    "v_mfma_f32_16x16x32_bf16 v[64:67], a[112:115], v[20:23], v[64:67]\n"
    "s_waitcnt lgkmcnt(1)\n"
    "v_mfma_f32_16x16x32_bf16 v[68:71], a[116:119], v[20:23], v[68:71]\n"
    "ds_read_b128 v[20:23], v0 offset:7168\n"
    "s_cbranch_vccz .LgP_dma0_30_%=\n"
    "v_mfma_f32_16x16x32_bf16 v[32:35], a[120:123], v[24:27], v[32:35]\n"
    "s_or_b64 exec, vcc, s[62:63]\n"
    "buffer_atomic_smax v5, v2, s[36:39], 0 offen sc0 sc1\n"
    "s_mov_b64 exec, -1\n"
    "v_mfma_f32_16x16x32_bf16 v[36:39], a[124:127], v[24:27], v[36:39]\n"
    "v_mfma_f32_16x16x32_bf16 v[48:51], v[24:27], a[128:131], v[48:51]\n"
    "v_mfma_f32_16x16x32_bf16 v[56:59], v[24:27], a[132:135], v[56:59]\n"
    "v_mfma_f32_16x16x32_bf16 v[64:67], a[136:139], v[24:27], v[64:67]\n"
    "s_waitcnt lgkmcnt(1)\n"
    "v_mfma_f32_16x16x32_bf16 v[68:71], a[140:143], v[24:27], v[68:71]\n"
    "ds_read_b128 v[24:27], v0 offset:8192\n"
    "v_mfma_f32_16x16x32_bf16 v[32:35], a[144:147], v[16:19], v[32:35]\n"
    "v_mfma_f32_16x16x32_bf16 v[36:39], a[148:151], v[16:19], v[36:39]\n"
    "v_mfma_f32_16x16x32_bf16 v[48:51], v[16:19], a[152:155], v[48:51]\n"
    "v_mfma_f32_16x16x32_bf16 v[56:59], v[16:19], a[156:159], v[56:59]\n"
    "v_mfma_f32_16x16x32_bf16 v[64:67], a[160:163], v[16:19], v[64:67]\n"
    "s_waitcnt lgkmcnt(1)\n"
    "v_mfma_f32_16x16x32_bf16 v[68:71], a[164:167], v[16:19], v[68:71]\n"
    "ds_read_b128 v[16:19], v0 offset:9216\n"
    "v_mfma_f32_16x16x32_bf16 v[32:35], a[168:171], v[20:23], v[32:35]\n"
    "v_mfma_f32_16x16x32_bf16 v[36:39], a[172:175], v[20:23], v[36:39]\n"
    "s_waitcnt vmcnt(3)\n"
    "v_add_u32 v13, v11, v3\n"
    "v_mfma_f32_16x16x32_bf16 v[48:51], v[20:23], a[176:179], v[48:51]\n"
    "v_cmp_gt_i32 vcc, s71, v13\n"
    "v_mfma_f32_16x16x32_bf16 v[56:59], v[20:23], a[180:183], v[56:59]\n"
    "v_mfma_f32_16x16x32_bf16 v[64:67], a[184:187], v[20:23], v[64:67]\n"
    "s_waitcnt lgkmcnt(1)\n"
    "v_mfma_f32_16x16x32_bf16 v[68:71], a[188:191], v[20:23], v[68:71]\n"
    "ds_read_b128 v[20:23], v0 offset:10240\n"
    "s_cbranch_vccz .LgP_dma0_48_%=\n"
    "v_mfma_f32_16x16x32_bf16 v[32:35], a[192:195], v[24:27], v[32:35]\n"
    "s_or_b64 exec, vcc, s[62:63]\n"
    "buffer_atomic_smax v11, v2, s[36:39], 0 offen sc0 sc1\n"
    "s_mov_b64 exec, -1\n"
    "v_mfma_f32_16x16x32_bf16 v[36:39], a[196:199], v[24:27], v[36:39]\n"
    "v_mfma_f32_16x16x32_bf16 v[48:51], v[24:27], a[200:203], v[48:51]\n"
    "v_mfma_f32_16x16x32_bf16 v[56:59], v[24:27], a[204:207], v[56:59]\n"
    "v_mfma_f32_16x16x32_bf16 v[64:67], a[208:211], v[24:27], v[64:67]\n"
    "s_waitcnt lgkmcnt(1)\n"
    "v_mfma_f32_16x16x32_bf16 v[68:71], a[212:215], v[24:27], v[68:71]\n"
    "ds_read_b128 v[24:27], v0 offset:11264\n"
    "v_mfma_f32_16x16x32_bf16 v[32:35], a[216:219], v[16:19], v[32:35]\n"
    "v_mfma_f32_16x16x32_bf16 v[36:39], a[220:223], v[16:19], v[36:39]\n"
    "v_mfma_f32_16x16x32_bf16 v[48:51], v[16:19], a[224:227], v[48:51]\n"
    "v_mfma_f32_16x16x32_bf16 v[56:59], v[16:19], a[228:231], v[56:59]\n"
    "v_mfma_f32_16x16x32_bf16 v[64:67], a[232:235], v[16:19], v[64:67]\n"
    "s_waitcnt lgkmcnt(1)\n"
    "s_waitcnt vmcnt(2)\n"
    "buffer_atomic_smax v12, v10, s[36:39], s70 offen sc1\n"
    "v_mfma_f32_16x16x32_bf16 v[68:71], a[236:239], v[16:19], v[68:71]\n"
    "ds_read_b128 v[16:19], v0 offset:12288\n"
    "v_mfma_f32_16x16x32_bf16 v[32:35], a[240:243], v[20:23], v[32:35]\n"
    "v_mfma_f32_16x16x32_bf16 v[36:39], a[244:247], v[20:23], v[36:39]\n"
    "s_waitcnt vmcnt(2)\n"
    "v_add_u32 v13, v5, v3\n"
    "v_mfma_f32_16x16x32_bf16 v[48:51], v[20:23], a[248:251], v[48:51]\n"
    "v_cmp_gt_i32 vcc, s71, v13\n"
    "v_mfma_f32_16x16x32_bf16 v[56:59], v[20:23], a[252:255], v[56:59]\n"
    "v_mfma_f32_16x16x32_bf16 v[64:67], v[80:83], v[20:23], v[64:67]\n"
    "s_waitcnt lgkmcnt(1)\n"
    "v_mfma_f32_16x16x32_bf16 v[68:71], v[84:87], v[20:23], v[68:71]\n"
    "ds_read_b128 v[20:23], v0 offset:13312\n"
    "s_cbranch_vccz .LgP_dma0_66_%=\n"
    "v_mfma_f32_16x16x32_bf16 v[32:35], v[88:91], v[24:27], v[32:35]\n"
    "s_or_b64 exec, vcc, s[62:63]\n"
    "buffer_atomic_smax v5, v2, s[36:39], 0 offen sc0 sc1\n"
    "s_mov_b64 exec, -1\n"
    "v_mfma_f32_16x16x32_bf16 v[36:39], v[92:95], v[24:27], v[36:39]\n"
    "v_mfma_f32_16x16x32_bf16 v[48:51], v[24:27], v[96:99], v[48:51]\n"
    "v_mfma_f32_16x16x32_bf16 v[56:59], v[24:27], v[100:103], v[56:59]\n"
    "v_mfma_f32_16x16x32_bf16 v[64:67], v[104:107], v[24:27], v[64:67]\n"
    "s_waitcnt lgkmcnt(1)\n"
    "v_mfma_f32_16x16x32_bf16 v[68:71], v[108:111], v[24:27], v[68:71]\n"
    "ds_read_b128 v[24:27], v0 offset:14336\n"
    "v_mfma_f32_16x16x32_bf16 v[32:35], v[112:115], v[16:19], v[32:35]\n"
    "v_mfma_f32_16x16x32_bf16 v[36:39], v[116:119], v[16:19], v[36:39]\n"
    "v_mfma_f32_16x16x32_bf16 v[48:51], v[16:19], v[120:123], v[48:51]\n"
    "v_mfma_f32_16x16x32_bf16 v[56:59], v[16:19], v[124:127], v[56:59]\n"
    "v_mfma_f32_16x16x32_bf16 v[64:67], v[128:131], v[16:19], v[64:67]\n"
    "s_waitcnt lgkmcnt(1)\n"
    "v_mfma_f32_16x16x32_bf16 v[68:71], v[132:135], v[16:19], v[68:71]\n"
    "ds_read_b128 v[16:19], v0 offset:15360\n"
    "v_mfma_f32_16x16x32_bf16 v[32:35], v[136:139], v[20:23], v[32:35]\n"
    "v_mfma_f32_16x16x32_bf16 v[36:39], v[140:143], v[20:23], v[36:39]\n"
    "s_waitcnt vmcnt(2)\n"
    "v_add_u32 v13, v11, v3\n"
    "v_mfma_f32_16x16x32_bf16 v[48:51], v[20:23], v[144:147], v[48:51]\n"
    "v_cmp_gt_i32 vcc, s71, v13\n"
    "v_mfma_f32_16x16x32_bf16 v[56:59], v[20:23], v[148:151], v[56:59]\n"
    "v_mfma_f32_16x16x32_bf16 v[64:67], v[152:155], v[20:23], v[64:67]\n"
    "s_waitcnt lgkmcnt(1)\n"
    "v_mfma_f32_16x16x32_bf16 v[68:71], v[156:159], v[20:23], v[68:71]\n"
    "ds_read_b128 v[20:23], v0 offset:16384\n"
    "s_cbranch_vccz .LgP_dma0_84_%=\n"
    "v_mfma_f32_16x16x32_bf16 v[32:35], v[160:163], v[24:27], v[32:35]\n"
    "s_or_b64 exec, vcc, s[62:63]\n"
    "buffer_atomic_smax v11, v2, s[36:39], 0 offen sc0 sc1\n"
    "s_mov_b64 exec, -1\n"
    "v_mfma_f32_16x16x32_bf16 v[36:39], v[164:167], v[24:27], v[36:39]\n"
    "v_mfma_f32_16x16x32_bf16 v[48:51], v[24:27], v[168:171], v[48:51]\n"
    "v_mfma_f32_16x16x32_bf16 v[56:59], v[24:27], v[172:175], v[56:59]\n"
    "v_mfma_f32_16x16x32_bf16 v[64:67], v[176:179], v[24:27], v[64:67]\n"
    "s_waitcnt lgkmcnt(1)\n"
    "v_mfma_f32_16x16x32_bf16 v[68:71], v[180:183], v[24:27], v[68:71]\n"
    "ds_read_b128 v[24:27], v0 offset:17408\n"
    "v_mfma_f32_16x16x32_bf16 v[32:35], v[184:187], v[16:19], v[32:35]\n"
    "v_mfma_f32_16x16x32_bf16 v[36:39], v[188:191], v[16:19], v[36:39]\n"
    "v_mfma_f32_16x16x32_bf16 v[48:51], v[16:19], v[192:195], v[48:51]\n"
    "v_mfma_f32_16x16x32_bf16 v[56:59], v[16:19], v[196:199], v[56:59]\n"
    "v_mfma_f32_16x16x32_bf16 v[64:67], v[200:203], v[16:19], v[64:67]\n"
    "s_waitcnt lgkmcnt(1)\n"
    "v_mfma_f32_16x16x32_bf16 v[68:71], v[204:207], v[16:19], v[68:71]\n"
    "v_mfma_f32_16x16x32_bf16 v[32:35], v[208:211], v[20:23], v[32:35]\n"
    "v_mfma_f32_16x16x32_bf16 v[36:39], v[212:215], v[20:23], v[36:39]\n"
    "s_waitcnt vmcnt(1)\n"
    "v_add_u32 v13, v5, v3\n"
    "v_mfma_f32_16x16x32_bf16 v[48:51], v[20:23], v[216:219], v[48:51]\n"
    "v_cmp_gt_i32 vcc, s71, v13\n"
    "v_mfma_f32_16x16x32_bf16 v[56:59], v[20:23], v[220:223], v[56:59]\n"
    "v_mfma_f32_16x16x32_bf16 v[64:67], v[224:227], v[20:23], v[64:67]\n"
    "s_waitcnt lgkmcnt(0)\n"
    "v_mfma_f32_16x16x32_bf16 v[68:71], v[228:231], v[20:23], v[68:71]\n"
    "s_cbranch_vccz .LgP_dma0_102_%=\n"
    "v_mfma_f32_16x16x32_bf16 v[32:35], v[232:235], v[24:27], v[32:35]\n"
    "s_or_b64 exec, vcc, s[62:63]\n"
    "buffer_atomic_smax v5, v2, s[36:39], 0 offen sc0 sc1\n"
    "s_mov_b64 exec, -1\n"
    "v_mfma_f32_16x16x32_bf16 v[36:39], v[236:239], v[24:27], v[36:39]\n"
    "v_mfma_f32_16x16x32_bf16 v[48:51], v[24:27], v[240:243], v[48:51]\n"
    "v_mfma_f32_16x16x32_bf16 v[56:59], v[24:27], v[244:247], v[56:59]\n"
    "v_mfma_f32_16x16x32_bf16 v[64:67], v[248:251], v[24:27], v[64:67]\n"
    "v_mfma_f32_16x16x32_bf16 v[68:71], v[252:255], v[24:27], v[68:71]\n"
    "v_cvt_pk_bf16_f32 v32, v32, v33\n"
    "v_cvt_pk_bf16_f32 v33, v34, v35\n"
    "v_cvt_pk_bf16_f32 v34, v36, v37\n"
    "v_cvt_pk_bf16_f32 v35, v38, v39\n"
    "buffer_store_dwordx4 v[32:35], v1, s[36:39], s79 offen sc1\n"
    "v_cvt_pk_bf16_f32 v28, v48, v49\n"
    "v_cvt_pk_bf16_f32 v29, v50, v51\n"
    "v_cvt_pk_bf16_f32 v30, v56, v57\n"
    "v_cvt_pk_bf16_f32 v31, v58, v59\n"
    "v_cvt_pk_bf16_f32 v64, v64, v65\n"
    "v_cvt_pk_bf16_f32 v65, v66, v67\n"
    "v_cvt_pk_bf16_f32 v66, v68, v69\n"
    "v_cvt_pk_bf16_f32 v67, v70, v71\n"
    "buffer_store_dwordx4 v[64:67], v1, s[36:39], s73 offen sc1\n"
    "v_mov_b32 v12, s68\n"
    "s_mov_b32 s73, 0x80000000\n"
    "s_mov_b32 s79, 0x80000000\n"
    "s_mov_b32 s80, 0x80000000\n"
    "s_mov_b32 s68, s71\n"
    "s_waitcnt vmcnt(3)\n"
    "v_add_u32 v13, v11, v3\n"
    "v_cmp_gt_i32 vcc, s71, v13\n"
    "s_cbranch_vccnz .LgP_idle0_%=\n"
    "s_add_u32 m0, s59, 20480\n"
    "s_nop 0\n"
    "buffer_load_dwordx4 v1, s[36:39], s76 offen sc1 lds\n"
    "s_add_u32 m0, s59, 24576\n"
    "s_add_u32 s76, s76, 16384\n"
    "buffer_load_dwordx4 v1, s[36:39], s76 offen sc1 lds\n"
    "s_add_u32 m0, s59, 28672\n"
    "s_add_u32 s76, s76, 16384\n"
    "buffer_load_dwordx4 v1, s[36:39], s76 offen sc1 lds\n"
    "s_add_u32 m0, s59, 32768\n"
    "s_add_u32 s76, s76, 16384\n"
    "buffer_load_dwordx4 v1, s[36:39], s76 offen sc1 lds\n"
    "s_add_u32 m0, s59, 36864\n"
    "s_add_u32 s76, s76, 16384\n"
    "buffer_load_dwordx4 v8, s[36:39], s76 offen sc1 lds\n"
    "s_waitcnt vmcnt(5)\n"
    "buffer_atomic_smax v12, v14, s[36:39], 0 offen sc1\n"
    "s_mov_b32 s69, 0x80000000\n"
    "s_mov_b32 s70, 0x80000000\n"
    "v_mov_b32 v7, v12\n"
    "s_waitcnt vmcnt(8)\n"
    "s_add_u32 s71, s68, 1\n"
    "v_add_u32 v13, v11, v3\n"
    "v_cmp_gt_i32 vcc, s71, v13\n"
    "s_nop 0\n"
    "s_or_b64 exec, vcc, s[62:63]\n"
    "buffer_atomic_smax v11, v2, s[36:39], 0 offen sc0 sc1\n"
    "s_mov_b64 exec, -1\n"
    "s_waitcnt vmcnt(2)\n"
    "s_branch .LgP_bar1_%=\n"
    ".LgP_dma0_30_%=:\n"
    "v_mfma_f32_16x16x32_bf16 v[32:35], a[120:123], v[24:27], v[32:35]\n"
    "s_add_u32 m0, s59, 20480\n"
    "v_mfma_f32_16x16x32_bf16 v[36:39], a[124:127], v[24:27], v[36:39]\n"
    "buffer_load_dwordx4 v1, s[36:39], s76 offen sc1 lds\n"
    "v_mfma_f32_16x16x32_bf16 v[48:51], v[24:27], a[128:131], v[48:51]\n"
    "s_add_u32 s76, s76, 16384\n"
    "s_add_u32 m0, s59, 24576\n"
    "v_mfma_f32_16x16x32_bf16 v[56:59], v[24:27], a[132:135], v[56:59]\n"
    "buffer_load_dwordx4 v1, s[36:39], s76 offen sc1 lds\n"
    "v_mfma_f32_16x16x32_bf16 v[64:67], a[136:139], v[24:27], v[64:67]\n"
    "s_waitcnt lgkmcnt(1)\n"
    "v_mfma_f32_16x16x32_bf16 v[68:71], a[140:143], v[24:27], v[68:71]\n"
    "ds_read_b128 v[24:27], v0 offset:8192\n"
    "v_mfma_f32_16x16x32_bf16 v[32:35], a[144:147], v[16:19], v[32:35]\n"
    "s_add_u32 s76, s76, 16384\n"
    "s_add_u32 m0, s59, 28672\n"
    "v_mfma_f32_16x16x32_bf16 v[36:39], a[148:151], v[16:19], v[36:39]\n"
    "buffer_load_dwordx4 v1, s[36:39], s76 offen sc1 lds\n"
    "v_mfma_f32_16x16x32_bf16 v[48:51], v[16:19], a[152:155], v[48:51]\n"
    "v_mfma_f32_16x16x32_bf16 v[56:59], v[16:19], a[156:159], v[56:59]\n"
    "v_mfma_f32_16x16x32_bf16 v[64:67], a[160:163], v[16:19], v[64:67]\n"
    "s_waitcnt lgkmcnt(1)\n"
    "s_add_u32 s76, s76, 16384\n"
    "s_add_u32 m0, s59, 32768\n"
    "v_mfma_f32_16x16x32_bf16 v[68:71], a[164:167], v[16:19], v[68:71]\n"
    "ds_read_b128 v[16:19], v0 offset:9216\n"
    "buffer_load_dwordx4 v1, s[36:39], s76 offen sc1 lds\n"
    "v_mfma_f32_16x16x32_bf16 v[32:35], a[168:171], v[20:23], v[32:35]\n"
    "v_mfma_f32_16x16x32_bf16 v[36:39], a[172:175], v[20:23], v[36:39]\n"
    "v_mfma_f32_16x16x32_bf16 v[48:51], v[20:23], a[176:179], v[48:51]\n"
    "s_add_u32 s76, s76, 16384\n"
    "s_add_u32 m0, s59, 36864\n"
    "v_mfma_f32_16x16x32_bf16 v[56:59], v[20:23], a[180:183], v[56:59]\n"
    "buffer_load_dwordx4 v8, s[36:39], s76 offen sc1 lds\n"
    "v_mfma_f32_16x16x32_bf16 v[64:67], a[184:187], v[20:23], v[64:67]\n"
    "s_waitcnt lgkmcnt(1)\n"
    "v_mfma_f32_16x16x32_bf16 v[68:71], a[188:191], v[20:23], v[68:71]\n"
    "ds_read_b128 v[20:23], v0 offset:10240\n"
    "v_mfma_f32_16x16x32_bf16 v[32:35], a[192:195], v[24:27], v[32:35]\n"
    "v_mfma_f32_16x16x32_bf16 v[36:39], a[196:199], v[24:27], v[36:39]\n"
    "v_mfma_f32_16x16x32_bf16 v[48:51], v[24:27], a[200:203], v[48:51]\n"
    "v_mfma_f32_16x16x32_bf16 v[56:59], v[24:27], a[204:207], v[56:59]\n"
    "v_mfma_f32_16x16x32_bf16 v[64:67], a[208:211], v[24:27], v[64:67]\n"
    "s_waitcnt lgkmcnt(1)\n"
    "v_mfma_f32_16x16x32_bf16 v[68:71], a[212:215], v[24:27], v[68:71]\n"
    "ds_read_b128 v[24:27], v0 offset:11264\n"
    "v_mfma_f32_16x16x32_bf16 v[32:35], a[216:219], v[16:19], v[32:35]\n"
    "v_mfma_f32_16x16x32_bf16 v[36:39], a[220:223], v[16:19], v[36:39]\n"
    "v_mfma_f32_16x16x32_bf16 v[48:51], v[16:19], a[224:227], v[48:51]\n"
    "v_mfma_f32_16x16x32_bf16 v[56:59], v[16:19], a[228:231], v[56:59]\n"
    "v_mfma_f32_16x16x32_bf16 v[64:67], a[232:235], v[16:19], v[64:67]\n"
    "s_waitcnt lgkmcnt(1)\n"
    "s_waitcnt vmcnt(5)\n"
    "buffer_atomic_smax v12, v10, s[36:39], s70 offen sc1\n"
    "v_mfma_f32_16x16x32_bf16 v[68:71], a[236:239], v[16:19], v[68:71]\n"
    "ds_read_b128 v[16:19], v0 offset:12288\n"
    "v_mfma_f32_16x16x32_bf16 v[32:35], a[240:243], v[20:23], v[32:35]\n"
    "v_mfma_f32_16x16x32_bf16 v[36:39], a[244:247], v[20:23], v[36:39]\n"
    "v_mfma_f32_16x16x32_bf16 v[48:51], v[20:23], a[248:251], v[48:51]\n"
    "v_mfma_f32_16x16x32_bf16 v[56:59], v[20:23], a[252:255], v[56:59]\n"
    "v_mfma_f32_16x16x32_bf16 v[64:67], v[80:83], v[20:23], v[64:67]\n"
    "s_waitcnt lgkmcnt(1)\n"
    "v_mfma_f32_16x16x32_bf16 v[68:71], v[84:87], v[20:23], v[68:71]\n"
    "ds_read_b128 v[20:23], v0 offset:13312\n"
    "v_mfma_f32_16x16x32_bf16 v[32:35], v[88:91], v[24:27], v[32:35]\n"
    "v_mfma_f32_16x16x32_bf16 v[36:39], v[92:95], v[24:27], v[36:39]\n"
    "v_mfma_f32_16x16x32_bf16 v[48:51], v[24:27], v[96:99], v[48:51]\n"
    "v_mfma_f32_16x16x32_bf16 v[56:59], v[24:27], v[100:103], v[56:59]\n"
    "v_mfma_f32_16x16x32_bf16 v[64:67], v[104:107], v[24:27], v[64:67]\n"
    "s_waitcnt lgkmcnt(1)\n"
    "v_mfma_f32_16x16x32_bf16 v[68:71], v[108:111], v[24:27], v[68:71]\n"
    "ds_read_b128 v[24:27], v0 offset:14336\n"
    "v_mfma_f32_16x16x32_bf16 v[32:35], v[112:115], v[16:19], v[32:35]\n"
    "v_mfma_f32_16x16x32_bf16 v[36:39], v[116:119], v[16:19], v[36:39]\n"
    "v_mfma_f32_16x16x32_bf16 v[48:51], v[16:19], v[120:123], v[48:51]\n"
    "v_mfma_f32_16x16x32_bf16 v[56:59], v[16:19], v[124:127], v[56:59]\n"
    "v_mfma_f32_16x16x32_bf16 v[64:67], v[128:131], v[16:19], v[64:67]\n"
    "s_waitcnt lgkmcnt(1)\n"
    "v_mfma_f32_16x16x32_bf16 v[68:71], v[132:135], v[16:19], v[68:71]\n"
    "ds_read_b128 v[16:19], v0 offset:15360\n"
    "s_branch .LgP_join0_%=\n"
    ".LgP_dma0_48_%=:\n"
    "v_mfma_f32_16x16x32_bf16 v[32:35], a[192:195], v[24:27], v[32:35]\n"
    "s_add_u32 m0, s59, 20480\n"
    "v_mfma_f32_16x16x32_bf16 v[36:39], a[196:199], v[24:27], v[36:39]\n"
    "buffer_load_dwordx4 v1, s[36:39], s76 offen sc1 lds\n"
    "v_mfma_f32_16x16x32_bf16 v[48:51], v[24:27], a[200:203], v[48:51]\n"
    "s_add_u32 s76, s76, 16384\n"
    "s_add_u32 m0, s59, 24576\n"
    "v_mfma_f32_16x16x32_bf16 v[56:59], v[24:27], a[204:207], v[56:59]\n"
    "buffer_load_dwordx4 v1, s[36:39], s76 offen sc1 lds\n"
    "v_mfma_f32_16x16x32_bf16 v[64:67], a[208:211], v[24:27], v[64:67]\n"
    "s_waitcnt lgkmcnt(1)\n"
    "v_mfma_f32_16x16x32_bf16 v[68:71], a[212:215], v[24:27], v[68:71]\n"
    "ds_read_b128 v[24:27], v0 offset:11264\n"
    "v_mfma_f32_16x16x32_bf16 v[32:35], a[216:219], v[16:19], v[32:35]\n"
    "s_add_u32 s76, s76, 16384\n"
    "s_add_u32 m0, s59, 28672\n"
    "v_mfma_f32_16x16x32_bf16 v[36:39], a[220:223], v[16:19], v[36:39]\n"
    "buffer_load_dwordx4 v1, s[36:39], s76 offen sc1 lds\n"
    "v_mfma_f32_16x16x32_bf16 v[48:51], v[16:19], a[224:227], v[48:51]\n"
    "v_mfma_f32_16x16x32_bf16 v[56:59], v[16:19], a[228:231], v[56:59]\n"
    "v_mfma_f32_16x16x32_bf16 v[64:67], a[232:235], v[16:19], v[64:67]\n"
    "s_waitcnt lgkmcnt(1)\n"
    "s_waitcnt vmcnt(4)\n"
    "buffer_atomic_smax v12, v10, s[36:39], s70 offen sc1\n"
    "v_mfma_f32_16x16x32_bf16 v[68:71], a[236:239], v[16:19], v[68:71]\n"
    "ds_read_b128 v[16:19], v0 offset:12288\n"
    "s_add_u32 s76, s76, 16384\n"
    "v_mfma_f32_16x16x32_bf16 v[32:35], a[240:243], v[20:23], v[32:35]\n"
    "s_add_u32 m0, s59, 32768\n"
    "v_mfma_f32_16x16x32_bf16 v[36:39], a[244:247], v[20:23], v[36:39]\n"
    "buffer_load_dwordx4 v1, s[36:39], s76 offen sc1 lds\n"
    "v_mfma_f32_16x16x32_bf16 v[48:51], v[20:23], a[248:251], v[48:51]\n"
    "s_add_u32 s76, s76, 16384\n"
    "s_add_u32 m0, s59, 36864\n"
    "v_mfma_f32_16x16x32_bf16 v[56:59], v[20:23], a[252:255], v[56:59]\n"
    "buffer_load_dwordx4 v8, s[36:39], s76 offen sc1 lds\n"
    "v_mfma_f32_16x16x32_bf16 v[64:67], v[80:83], v[20:23], v[64:67]\n"
    "s_waitcnt lgkmcnt(1)\n"
    "v_mfma_f32_16x16x32_bf16 v[68:71], v[84:87], v[20:23], v[68:71]\n"
    "ds_read_b128 v[20:23], v0 offset:13312\n"
    "v_mfma_f32_16x16x32_bf16 v[32:35], v[88:91], v[24:27], v[32:35]\n"
    "v_mfma_f32_16x16x32_bf16 v[36:39], v[92:95], v[24:27], v[36:39]\n"
    "v_mfma_f32_16x16x32_bf16 v[48:51], v[24:27], v[96:99], v[48:51]\n"
    "v_mfma_f32_16x16x32_bf16 v[56:59], v[24:27], v[100:103], v[56:59]\n"
    "v_mfma_f32_16x16x32_bf16 v[64:67], v[104:107], v[24:27], v[64:67]\n"
    "s_waitcnt lgkmcnt(1)\n"
    "v_mfma_f32_16x16x32_bf16 v[68:71], v[108:111], v[24:27], v[68:71]\n"
    "ds_read_b128 v[24:27], v0 offset:14336\n"
    "v_mfma_f32_16x16x32_bf16 v[32:35], v[112:115], v[16:19], v[32:35]\n"
    "v_mfma_f32_16x16x32_bf16 v[36:39], v[116:119], v[16:19], v[36:39]\n"
    "v_mfma_f32_16x16x32_bf16 v[48:51], v[16:19], v[120:123], v[48:51]\n"
    "v_mfma_f32_16x16x32_bf16 v[56:59], v[16:19], v[124:127], v[56:59]\n"
    "v_mfma_f32_16x16x32_bf16 v[64:67], v[128:131], v[16:19], v[64:67]\n"
    "s_waitcnt lgkmcnt(1)\n"
    "v_mfma_f32_16x16x32_bf16 v[68:71], v[132:135], v[16:19], v[68:71]\n"
    "ds_read_b128 v[16:19], v0 offset:15360\n"
    "s_branch .LgP_join0_%=\n"
    ".LgP_dma0_66_%=:\n"
    "v_mfma_f32_16x16x32_bf16 v[32:35], v[88:91], v[24:27], v[32:35]\n"
    "s_add_u32 m0, s59, 20480\n"
    "v_mfma_f32_16x16x32_bf16 v[36:39], v[92:95], v[24:27], v[36:39]\n"
    "buffer_load_dwordx4 v1, s[36:39], s76 offen sc1 lds\n"
    "v_mfma_f32_16x16x32_bf16 v[48:51], v[24:27], v[96:99], v[48:51]\n"
    "s_add_u32 s76, s76, 16384\n"
    "s_add_u32 m0, s59, 24576\n"
    "v_mfma_f32_16x16x32_bf16 v[56:59], v[24:27], v[100:103], v[56:59]\n"
    "buffer_load_dwordx4 v1, s[36:39], s76 offen sc1 lds\n"
    "v_mfma_f32_16x16x32_bf16 v[64:67], v[104:107], v[24:27], v[64:67]\n"
    "s_waitcnt lgkmcnt(1)\n"
    "v_mfma_f32_16x16x32_bf16 v[68:71], v[108:111], v[24:27], v[68:71]\n"
    "ds_read_b128 v[24:27], v0 offset:14336\n"
    "v_mfma_f32_16x16x32_bf16 v[32:35], v[112:115], v[16:19], v[32:35]\n"
    "s_add_u32 s76, s76, 16384\n"
    "s_add_u32 m0, s59, 28672\n"
    "v_mfma_f32_16x16x32_bf16 v[36:39], v[116:119], v[16:19], v[36:39]\n"
    "buffer_load_dwordx4 v1, s[36:39], s76 offen sc1 lds\n"
    "v_mfma_f32_16x16x32_bf16 v[48:51], v[16:19], v[120:123], v[48:51]\n"
    "v_mfma_f32_16x16x32_bf16 v[56:59], v[16:19], v[124:127], v[56:59]\n"
    "v_mfma_f32_16x16x32_bf16 v[64:67], v[128:131], v[16:19], v[64:67]\n"
    "s_waitcnt lgkmcnt(1)\n"
    "s_add_u32 s76, s76, 16384\n"
    "s_add_u32 m0, s59, 32768\n"
    "v_mfma_f32_16x16x32_bf16 v[68:71], v[132:135], v[16:19], v[68:71]\n"
    "ds_read_b128 v[16:19], v0 offset:15360\n"
    "buffer_load_dwordx4 v1, s[36:39], s76 offen sc1 lds\n"
    "v_mfma_f32_16x16x32_bf16 v[32:35], v[136:139], v[20:23], v[32:35]\n"
    "v_mfma_f32_16x16x32_bf16 v[36:39], v[140:143], v[20:23], v[36:39]\n"
    "v_mfma_f32_16x16x32_bf16 v[48:51], v[20:23], v[144:147], v[48:51]\n"
    "s_add_u32 s76, s76, 16384\n"
    "s_add_u32 m0, s59, 36864\n"
    "v_mfma_f32_16x16x32_bf16 v[56:59], v[20:23], v[148:151], v[56:59]\n"
    "buffer_load_dwordx4 v8, s[36:39], s76 offen sc1 lds\n"
    "v_mfma_f32_16x16x32_bf16 v[64:67], v[152:155], v[20:23], v[64:67]\n"
    "s_waitcnt lgkmcnt(1)\n"
    "v_mfma_f32_16x16x32_bf16 v[68:71], v[156:159], v[20:23], v[68:71]\n"
    "ds_read_b128 v[20:23], v0 offset:16384\n"
    "v_mfma_f32_16x16x32_bf16 v[32:35], v[160:163], v[24:27], v[32:35]\n"
    "v_mfma_f32_16x16x32_bf16 v[36:39], v[164:167], v[24:27], v[36:39]\n"
    "v_mfma_f32_16x16x32_bf16 v[48:51], v[24:27], v[168:171], v[48:51]\n"
    "v_mfma_f32_16x16x32_bf16 v[56:59], v[24:27], v[172:175], v[56:59]\n"
    "v_mfma_f32_16x16x32_bf16 v[64:67], v[176:179], v[24:27], v[64:67]\n"
    "s_waitcnt lgkmcnt(1)\n"
    "v_mfma_f32_16x16x32_bf16 v[68:71], v[180:183], v[24:27], v[68:71]\n"
    "ds_read_b128 v[24:27], v0 offset:17408\n"
    "v_mfma_f32_16x16x32_bf16 v[32:35], v[184:187], v[16:19], v[32:35]\n"
    "v_mfma_f32_16x16x32_bf16 v[36:39], v[188:191], v[16:19], v[36:39]\n"
    "v_mfma_f32_16x16x32_bf16 v[48:51], v[16:19], v[192:195], v[48:51]\n"
    "v_mfma_f32_16x16x32_bf16 v[56:59], v[16:19], v[196:199], v[56:59]\n"
    "v_mfma_f32_16x16x32_bf16 v[64:67], v[200:203], v[16:19], v[64:67]\n"
    "s_waitcnt lgkmcnt(1)\n"
    "v_mfma_f32_16x16x32_bf16 v[68:71], v[204:207], v[16:19], v[68:71]\n"
    "v_mfma_f32_16x16x32_bf16 v[32:35], v[208:211], v[20:23], v[32:35]\n"
    "v_mfma_f32_16x16x32_bf16 v[36:39], v[212:215], v[20:23], v[36:39]\n"
    "v_mfma_f32_16x16x32_bf16 v[48:51], v[20:23], v[216:219], v[48:51]\n"
    "v_mfma_f32_16x16x32_bf16 v[56:59], v[20:23], v[220:223], v[56:59]\n"
    "v_mfma_f32_16x16x32_bf16 v[64:67], v[224:227], v[20:23], v[64:67]\n"
    "s_waitcnt lgkmcnt(0)\n"
    "v_mfma_f32_16x16x32_bf16 v[68:71], v[228:231], v[20:23], v[68:71]\n"
    "v_mfma_f32_16x16x32_bf16 v[32:35], v[232:235], v[24:27], v[32:35]\n"
    "v_mfma_f32_16x16x32_bf16 v[36:39], v[236:239], v[24:27], v[36:39]\n"
    "v_mfma_f32_16x16x32_bf16 v[48:51], v[24:27], v[240:243], v[48:51]\n"
    "v_mfma_f32_16x16x32_bf16 v[56:59], v[24:27], v[244:247], v[56:59]\n"
    "v_mfma_f32_16x16x32_bf16 v[64:67], v[248:251], v[24:27], v[64:67]\n"
    "v_mfma_f32_16x16x32_bf16 v[68:71], v[252:255], v[24:27], v[68:71]\n"
    "v_cvt_pk_bf16_f32 v32, v32, v33\n"
    "v_cvt_pk_bf16_f32 v33, v34, v35\n"
    "v_cvt_pk_bf16_f32 v34, v36, v37\n"
    "v_cvt_pk_bf16_f32 v35, v38, v39\n"
    "buffer_store_dwordx4 v[32:35], v1, s[36:39], s79 offen sc1\n"
    "v_cvt_pk_bf16_f32 v28, v48, v49\n"
    "v_cvt_pk_bf16_f32 v29, v50, v51\n"
    "v_cvt_pk_bf16_f32 v30, v56, v57\n"
    "v_cvt_pk_bf16_f32 v31, v58, v59\n"
    "v_cvt_pk_bf16_f32 v64, v64, v65\n"
    "v_cvt_pk_bf16_f32 v65, v66, v67\n"
    "v_cvt_pk_bf16_f32 v66, v68, v69\n"
    "v_cvt_pk_bf16_f32 v67, v70, v71\n"
    "buffer_store_dwordx4 v[64:67], v1, s[36:39], s73 offen sc1\n"
    "v_mov_b32 v12, s68\n"
    "s_mov_b32 s73, 0x80000000\n"
    "s_mov_b32 s79, 0x80000000\n"
    "s_mov_b32 s80, 0x80000000\n"
    "s_mov_b32 s68, s71\n"
    "s_mov_b32 s69, 0\n"
    "s_mov_b32 s70, 0x80000000\n"
    "v_mov_b32 v7, v12\n"
    "s_waitcnt vmcnt(8)\n"
    "s_add_u32 s71, s68, 1\n"
    "v_add_u32 v13, v11, v3\n"
    "v_cmp_gt_i32 vcc, s71, v13\n"
    "s_nop 0\n"
    "s_or_b64 exec, vcc, s[62:63]\n"
    "buffer_atomic_smax v11, v2, s[36:39], 0 offen sc0 sc1\n"
    "s_mov_b64 exec, -1\n"
    "s_waitcnt vmcnt(3)\n"
    "s_branch .LgP_bar1_%=\n"
    ".LgP_dma0_84_%=:\n"
    "v_mfma_f32_16x16x32_bf16 v[32:35], v[160:163], v[24:27], v[32:35]\n"
    "s_add_u32 m0, s59, 20480\n"
    "v_mfma_f32_16x16x32_bf16 v[36:39], v[164:167], v[24:27], v[36:39]\n"
    "buffer_load_dwordx4 v1, s[36:39], s76 offen sc1 lds\n"
    "v_mfma_f32_16x16x32_bf16 v[48:51], v[24:27], v[168:171], v[48:51]\n"
    "s_add_u32 s76, s76, 16384\n"
    "s_add_u32 m0, s59, 24576\n"
    "v_mfma_f32_16x16x32_bf16 v[56:59], v[24:27], v[172:175], v[56:59]\n"
    "buffer_load_dwordx4 v1, s[36:39], s76 offen sc1 lds\n"
    "v_mfma_f32_16x16x32_bf16 v[64:67], v[176:179], v[24:27], v[64:67]\n"
    "s_waitcnt lgkmcnt(1)\n"
    "v_mfma_f32_16x16x32_bf16 v[68:71], v[180:183], v[24:27], v[68:71]\n"
    "ds_read_b128 v[24:27], v0 offset:17408\n"
    "v_mfma_f32_16x16x32_bf16 v[32:35], v[184:187], v[16:19], v[32:35]\n"
    "s_add_u32 s76, s76, 16384\n"
    "s_add_u32 m0, s59, 28672\n"
    "v_mfma_f32_16x16x32_bf16 v[36:39], v[188:191], v[16:19], v[36:39]\n"
    "buffer_load_dwordx4 v1, s[36:39], s76 offen sc1 lds\n"
    "v_mfma_f32_16x16x32_bf16 v[48:51], v[16:19], v[192:195], v[48:51]\n"
    "v_mfma_f32_16x16x32_bf16 v[56:59], v[16:19], v[196:199], v[56:59]\n"
    "v_mfma_f32_16x16x32_bf16 v[64:67], v[200:203], v[16:19], v[64:67]\n"
    "s_waitcnt lgkmcnt(1)\n"
    "s_add_u32 s76, s76, 16384\n"
    "s_add_u32 m0, s59, 32768\n"
    "v_mfma_f32_16x16x32_bf16 v[68:71], v[204:207], v[16:19], v[68:71]\n"
    "buffer_load_dwordx4 v1, s[36:39], s76 offen sc1 lds\n"
    "v_mfma_f32_16x16x32_bf16 v[32:35], v[208:211], v[20:23], v[32:35]\n"
    "v_mfma_f32_16x16x32_bf16 v[36:39], v[212:215], v[20:23], v[36:39]\n"
    "v_mfma_f32_16x16x32_bf16 v[48:51], v[20:23], v[216:219], v[48:51]\n"
    "s_add_u32 s76, s76, 16384\n"
    "s_add_u32 m0, s59, 36864\n"
    "v_mfma_f32_16x16x32_bf16 v[56:59], v[20:23], v[220:223], v[56:59]\n"
    "buffer_load_dwordx4 v8, s[36:39], s76 offen sc1 lds\n"
    "v_mfma_f32_16x16x32_bf16 v[64:67], v[224:227], v[20:23], v[64:67]\n"
    "s_waitcnt lgkmcnt(0)\n"
    "v_mfma_f32_16x16x32_bf16 v[68:71], v[228:231], v[20:23], v[68:71]\n"
    "v_mfma_f32_16x16x32_bf16 v[32:35], v[232:235], v[24:27], v[32:35]\n"
    "v_mfma_f32_16x16x32_bf16 v[36:39], v[236:239], v[24:27], v[36:39]\n"
    "v_mfma_f32_16x16x32_bf16 v[48:51], v[24:27], v[240:243], v[48:51]\n"
    "v_mfma_f32_16x16x32_bf16 v[56:59], v[24:27], v[244:247], v[56:59]\n"
    "v_mfma_f32_16x16x32_bf16 v[64:67], v[248:251], v[24:27], v[64:67]\n"
    "v_mfma_f32_16x16x32_bf16 v[68:71], v[252:255], v[24:27], v[68:71]\n"
    "v_cvt_pk_bf16_f32 v32, v32, v33\n"
    "v_cvt_pk_bf16_f32 v33, v34, v35\n"
    "v_cvt_pk_bf16_f32 v34, v36, v37\n"
    "v_cvt_pk_bf16_f32 v35, v38, v39\n"
    "buffer_store_dwordx4 v[32:35], v1, s[36:39], s79 offen sc1\n"
    "v_cvt_pk_bf16_f32 v28, v48, v49\n"
    "v_cvt_pk_bf16_f32 v29, v50, v51\n"
    "v_cvt_pk_bf16_f32 v30, v56, v57\n"
    "v_cvt_pk_bf16_f32 v31, v58, v59\n"
    "v_cvt_pk_bf16_f32 v64, v64, v65\n"
    "v_cvt_pk_bf16_f32 v65, v66, v67\n"
    "v_cvt_pk_bf16_f32 v66, v68, v69\n"
    "v_cvt_pk_bf16_f32 v67, v70, v71\n"
    "buffer_store_dwordx4 v[64:67], v1, s[36:39], s73 offen sc1\n"
    "v_mov_b32 v12, s68\n"
    "s_mov_b32 s73, 0x80000000\n"
    "s_mov_b32 s79, 0x80000000\n"
    "s_mov_b32 s80, 0x80000000\n"
    "s_mov_b32 s68, s71\n"
    "s_mov_b32 s69, 0\n"
    "s_mov_b32 s70, 0x80000000\n"
    "v_mov_b32 v7, v12\n"
    "s_waitcnt vmcnt(7)\n"
    "s_add_u32 s71, s68, 1\n"
    "v_add_u32 v13, v11, v3\n"
    "v_cmp_gt_i32 vcc, s71, v13\n"
    "s_nop 0\n"
    "s_or_b64 exec, vcc, s[62:63]\n"
    "buffer_atomic_smax v11, v2, s[36:39], 0 offen sc0 sc1\n"
    "s_mov_b64 exec, -1\n"
    "s_waitcnt vmcnt(3)\n"
    "s_branch .LgP_bar1_%=\n"
    ".LgP_dma0_102_%=:\n"
    "v_mfma_f32_16x16x32_bf16 v[32:35], v[232:235], v[24:27], v[32:35]\n"
    "s_add_u32 m0, s59, 20480\n"
    "v_mfma_f32_16x16x32_bf16 v[36:39], v[236:239], v[24:27], v[36:39]\n"
    "buffer_load_dwordx4 v1, s[36:39], s76 offen sc1 lds\n"
    "v_mfma_f32_16x16x32_bf16 v[48:51], v[24:27], v[240:243], v[48:51]\n"
    "s_add_u32 s76, s76, 16384\n"
    "s_add_u32 m0, s59, 24576\n"
    "v_mfma_f32_16x16x32_bf16 v[56:59], v[24:27], v[244:247], v[56:59]\n"
    "buffer_load_dwordx4 v1, s[36:39], s76 offen sc1 lds\n"
    "v_mfma_f32_16x16x32_bf16 v[64:67], v[248:251], v[24:27], v[64:67]\n"
    "v_mfma_f32_16x16x32_bf16 v[68:71], v[252:255], v[24:27], v[68:71]\n"
    "v_cvt_pk_bf16_f32 v32, v32, v33\n"
    "v_cvt_pk_bf16_f32 v33, v34, v35\n"
    "v_cvt_pk_bf16_f32 v34, v36, v37\n"
    "v_cvt_pk_bf16_f32 v35, v38, v39\n"
    "buffer_store_dwordx4 v[32:35], v1, s[36:39], s79 offen sc1\n"
    "v_cvt_pk_bf16_f32 v28, v48, v49\n"
    "v_cvt_pk_bf16_f32 v29, v50, v51\n"
    "v_cvt_pk_bf16_f32 v30, v56, v57\n"
    "v_cvt_pk_bf16_f32 v31, v58, v59\n"
    "v_cvt_pk_bf16_f32 v64, v64, v65\n"
    "v_cvt_pk_bf16_f32 v65, v66, v67\n"
    "v_cvt_pk_bf16_f32 v66, v68, v69\n"
    "v_cvt_pk_bf16_f32 v67, v70, v71\n"
    "buffer_store_dwordx4 v[64:67], v1, s[36:39], s73 offen sc1\n"
    "v_mov_b32 v12, s68\n"
    "s_mov_b32 s73, 0x80000000\n"
    "s_mov_b32 s79, 0x80000000\n"
    "s_mov_b32 s80, 0x80000000\n"
    "s_mov_b32 s68, s71\n"
    "s_add_u32 m0, s59, 28672\n"
    "s_add_u32 s76, s76, 16384\n"
    "buffer_load_dwordx4 v1, s[36:39], s76 offen sc1 lds\n"
    "s_add_u32 m0, s59, 32768\n"
    "s_add_u32 s76, s76, 16384\n"
    "buffer_load_dwordx4 v1, s[36:39], s76 offen sc1 lds\n"
    "s_add_u32 m0, s59, 36864\n"
    "s_add_u32 s76, s76, 16384\n"
    "buffer_load_dwordx4 v8, s[36:39], s76 offen sc1 lds\n"
    "s_waitcnt vmcnt(3)\n"
    "buffer_atomic_smax v12, v14, s[36:39], 0 offen sc1\n"
    "s_mov_b32 s69, 0x80000000\n"
    "s_mov_b32 s70, 0x80000000\n"
    "v_mov_b32 v7, v12\n"
    "s_waitcnt vmcnt(8)\n"
    "s_add_u32 s71, s68, 1\n"
    "v_add_u32 v13, v11, v3\n"
    "v_cmp_gt_i32 vcc, s71, v13\n"
    "s_nop 0\n"
    "s_or_b64 exec, vcc, s[62:63]\n"
    "buffer_atomic_smax v11, v2, s[36:39], 0 offen sc0 sc1\n"
    "s_mov_b64 exec, -1\n"
    "s_waitcnt vmcnt(2)\n"
    "s_branch .LgP_bar1_%=\n"
    ".LgP_idle0_%=:\n"
    "s_or_b64 exec, vcc, s[62:63]\n"
    "buffer_atomic_smax v11, v2, s[36:39], 0 offen sc0 sc1\n"
    "s_mov_b64 exec, -1\n"
    "s_waitcnt vmcnt(1)\n"
    "buffer_atomic_smax v12, v14, s[36:39], 0 offen sc1\n"
    "s_branch .LgP_block0_0_%=\n"
    ".LgP_block0_%=:\n"
    "v_add_u32 v13, v11, v3\n"
    "v_cmp_gt_i32 vcc, s71, v13\n"
    "s_nop 0\n"
    "s_or_b64 exec, vcc, s[62:63]\n"
    "buffer_atomic_smax v11, v2, s[36:39], 0 offen sc0 sc1\n"
    "s_mov_b64 exec, -1\n"
    "s_sleep 3\n"
    "s_waitcnt vmcnt(1)\n"
    ".LgP_block0_0_%=:\n"
    "v_add_u32 v13, v5, v3\n"
    "v_cmp_gt_i32 vcc, s71, v13\n"
    "s_cbranch_vccz .LgP_go0_%=\n"
    "v_add_u32 v13, v5, v3\n"
    "v_cmp_gt_i32 vcc, s71, v13\n"
    "s_nop 0\n"
    "s_or_b64 exec, vcc, s[62:63]\n"
    "buffer_atomic_smax v5, v2, s[36:39], 0 offen sc0 sc1\n"
    "s_mov_b64 exec, -1\n"
    "s_sleep 3\n"
    "s_waitcnt vmcnt(1)\n"
    ".LgP_block0_1_%=:\n"
    "v_add_u32 v13, v11, v3\n"
    "v_cmp_gt_i32 vcc, s71, v13\n"
    "s_cbranch_vccz .LgP_go0_%=\n"
    "s_sub_u32 s72, s72, 1\n"
    "s_cbranch_scc0 .LgP_block0_%=\n"
    "s_mov_b32 s72, 0\n"
    ".LgP_go0_%=:\n"
    "s_lshr_b32 s76, s71, 1\n"
    "s_and_b32 s76, s76, s54\n"
    "s_mul_i32 s76, s76, s53\n"
    "s_add_u32 s76, s76, s52\n"
    "s_add_u32 s76, s76, 1024\n"
    "s_add_u32 m0, s59, 20480\n"
    "s_nop 0\n"
    "buffer_load_dwordx4 v1, s[36:39], s76 offen sc1 lds\n"
    "s_add_u32 m0, s59, 24576\n"
    "s_add_u32 s76, s76, 16384\n"
    "buffer_load_dwordx4 v1, s[36:39], s76 offen sc1 lds\n"
    "s_add_u32 m0, s59, 28672\n"
    "s_add_u32 s76, s76, 16384\n"
    "buffer_load_dwordx4 v1, s[36:39], s76 offen sc1 lds\n"
    "s_add_u32 m0, s59, 32768\n"
    "s_add_u32 s76, s76, 16384\n"
    "buffer_load_dwordx4 v1, s[36:39], s76 offen sc1 lds\n"
    "s_add_u32 m0, s59, 36864\n"
    "s_add_u32 s76, s76, 16384\n"
    "buffer_load_dwordx4 v8, s[36:39], s76 offen sc1 lds\n"
    "s_mov_b32 s69, 0x80000000\n"
    "s_mov_b32 s70, 0x80000000\n"
    "v_mov_b32 v7, v12\n"
    "s_waitcnt vmcnt(5)\n"
    "s_add_u32 s71, s68, 1\n"
    "v_add_u32 v13, v11, v3\n"
    "v_cmp_gt_i32 vcc, s71, v13\n"
    "s_nop 0\n"
    "s_or_b64 exec, vcc, s[62:63]\n"
    "buffer_atomic_smax v11, v2, s[36:39], 0 offen sc0 sc1\n"
    "s_mov_b64 exec, -1\n"
    "s_waitcnt vmcnt(1)\n"
    "s_branch .LgP_bar1_%=\n"
    ".LgP_bar0_%=:\n"
    "s_barrier\n"
    "ds_read_b128 v[16:19], v0 offset:0\n"
    "ds_read_b128 v[20:23], v0 offset:1024\n"
    "ds_read_b128 v[24:27], v0 offset:2048\n"
    "v_add_u32 v13, v5, v3\n"
    "v_cmp_gt_i32 vcc, s71, v13\n"
    "s_nop 0\n"
    "s_or_b64 exec, vcc, s[62:63]\n"
    "buffer_atomic_smax v5, v2, s[36:39], 0 offen sc0 sc1\n"
    "s_mov_b64 exec, -1\n"
    "s_lshr_b32 s76, s71, 1\n"
    "s_and_b32 s76, s76, s54\n"
    "s_mul_i32 s76, s76, s53\n"
    "s_add_u32 s76, s76, s52\n"
    "s_add_u32 s76, s76, 1024\n"
    "s_waitcnt lgkmcnt(2)\n"
    "s_branch .LgP_head0_%=\n"
    ".LgP_poll1_%=:\n"
    "v_mfma_f32_16x16x32_bf16 v[40:43], a[48:51], v[24:27], v[40:43]\n"
    "s_or_b64 exec, vcc, s[62:63]\n"
    "buffer_atomic_smax v11, v2, s[36:39], 0 offen sc0 sc1\n"
    "s_mov_b64 exec, -1\n"
    "v_mfma_f32_16x16x32_bf16 v[44:47], a[52:55], v[24:27], v[44:47]\n"
    "buffer_store_dwordx4 v[64:67], v1, s[36:39], s73 offen sc1\n"
    "v_mfma_f32_16x16x32_bf16 v[52:55], v[24:27], a[56:59], v[52:55]\n"
    "s_lshr_b32 s73, s68, 1\n"
    "v_mfma_f32_16x16x32_bf16 v[60:63], v[24:27], a[60:63], v[60:63]\n"
    "s_and_b32 s73, s73, s57\n"
    "v_mfma_f32_16x16x32_bf16 v[72:75], a[64:67], v[24:27], v[72:75]\n"
    "s_waitcnt lgkmcnt(1)\n"
    "s_mul_i32 s73, s73, s56\n"
    "v_mfma_f32_16x16x32_bf16 v[76:79], a[68:71], v[24:27], v[76:79]\n"
    "ds_read_b128 v[24:27], v9 offset:5120\n"
    "s_add_u32 s73, s73, s55\n"
    "v_mfma_f32_16x16x32_bf16 v[40:43], a[72:75], v[16:19], v[40:43]\n"
    "s_add_u32 s79, s73, 16384\n"
    "v_mfma_f32_16x16x32_bf16 v[44:47], a[76:79], v[16:19], v[44:47]\n"
    "s_add_u32 s80, s73, 32768\n"
    "v_mfma_f32_16x16x32_bf16 v[52:55], v[16:19], a[80:83], v[52:55]\n"
    "v_mfma_f32_16x16x32_bf16 v[60:63], v[16:19], a[84:87], v[60:63]\n"
    "v_mfma_f32_16x16x32_bf16 v[72:75], a[88:91], v[16:19], v[72:75]\n"
    "s_waitcnt lgkmcnt(1)\n"
    "v_mfma_f32_16x16x32_bf16 v[76:79], a[92:95], v[16:19], v[76:79]\n"
    "ds_read_b128 v[16:19], v9 offset:6144\n"
    "v_mfma_f32_16x16x32_bf16 v[40:43], a[96:99], v[20:23], v[40:43]\n"
    "v_mfma_f32_16x16x32_bf16 v[44:47], a[100:103], v[20:23], v[44:47]\n"
    "s_waitcnt vmcnt(4)\n"
    "v_add_u32 v13, v5, v3\n"
    "v_mfma_f32_16x16x32_bf16 v[52:55], v[20:23], a[104:107], v[52:55]\n"
    "v_cmp_gt_i32 vcc, s71, v13\n"
    "v_mfma_f32_16x16x32_bf16 v[60:63], v[20:23], a[108:111], v[60:63]\n"
    "v_mfma_f32_16x16x32_bf16 v[72:75], a[112:115], v[20:23], v[72:75]\n"
    "s_waitcnt lgkmcnt(1)\n"
    "v_mfma_f32_16x16x32_bf16 v[76:79], a[116:119], v[20:23], v[76:79]\n"
    "ds_read_b128 v[20:23], v9 offset:7168\n"
    "s_cbranch_vccz .LgP_dma1_30_%=\n"
    "v_mfma_f32_16x16x32_bf16 v[40:43], a[120:123], v[24:27], v[40:43]\n"
    "s_or_b64 exec, vcc, s[62:63]\n"
    "buffer_atomic_smax v5, v2, s[36:39], 0 offen sc0 sc1\n"
    "s_mov_b64 exec, -1\n"
    "v_mfma_f32_16x16x32_bf16 v[44:47], a[124:127], v[24:27], v[44:47]\n"
    "v_mfma_f32_16x16x32_bf16 v[52:55], v[24:27], a[128:131], v[52:55]\n"
    "v_mfma_f32_16x16x32_bf16 v[60:63], v[24:27], a[132:135], v[60:63]\n"
    "v_mfma_f32_16x16x32_bf16 v[72:75], a[136:139], v[24:27], v[72:75]\n"
    "s_waitcnt lgkmcnt(1)\n"
    "v_mfma_f32_16x16x32_bf16 v[76:79], a[140:143], v[24:27], v[76:79]\n"
    "ds_read_b128 v[24:27], v9 offset:8192\n"
    "v_mfma_f32_16x16x32_bf16 v[40:43], a[144:147], v[16:19], v[40:43]\n"
    "v_mfma_f32_16x16x32_bf16 v[44:47], a[148:151], v[16:19], v[44:47]\n"
    "v_mfma_f32_16x16x32_bf16 v[52:55], v[16:19], a[152:155], v[52:55]\n"
    "v_mfma_f32_16x16x32_bf16 v[60:63], v[16:19], a[156:159], v[60:63]\n"
    "v_mfma_f32_16x16x32_bf16 v[72:75], a[160:163], v[16:19], v[72:75]\n"
    "s_waitcnt lgkmcnt(1)\n"
    "v_mfma_f32_16x16x32_bf16 v[76:79], a[164:167], v[16:19], v[76:79]\n"
    "ds_read_b128 v[16:19], v9 offset:9216\n"
    "v_mfma_f32_16x16x32_bf16 v[40:43], a[168:171], v[20:23], v[40:43]\n"
    "v_mfma_f32_16x16x32_bf16 v[44:47], a[172:175], v[20:23], v[44:47]\n"
    "s_waitcnt vmcnt(2)\n"
    "v_add_u32 v13, v11, v3\n"
    "v_mfma_f32_16x16x32_bf16 v[52:55], v[20:23], a[176:179], v[52:55]\n"
    "v_cmp_gt_i32 vcc, s71, v13\n"
    "v_mfma_f32_16x16x32_bf16 v[60:63], v[20:23], a[180:183], v[60:63]\n"
    "v_mfma_f32_16x16x32_bf16 v[72:75], a[184:187], v[20:23], v[72:75]\n"
    "s_waitcnt lgkmcnt(1)\n"
    "v_mfma_f32_16x16x32_bf16 v[76:79], a[188:191], v[20:23], v[76:79]\n"
    "ds_read_b128 v[20:23], v9 offset:10240\n"
    "s_cbranch_vccz .LgP_dma1_48_%=\n"
    "v_mfma_f32_16x16x32_bf16 v[40:43], a[192:195], v[24:27], v[40:43]\n"
    "s_or_b64 exec, vcc, s[62:63]\n"
    "buffer_atomic_smax v11, v2, s[36:39], 0 offen sc0 sc1\n"
    "s_mov_b64 exec, -1\n"
    "v_mfma_f32_16x16x32_bf16 v[44:47], a[196:199], v[24:27], v[44:47]\n"
    "v_mfma_f32_16x16x32_bf16 v[52:55], v[24:27], a[200:203], v[52:55]\n"
    "v_mfma_f32_16x16x32_bf16 v[60:63], v[24:27], a[204:207], v[60:63]\n"
    "v_mfma_f32_16x16x32_bf16 v[72:75], a[208:211], v[24:27], v[72:75]\n"
    "s_waitcnt lgkmcnt(1)\n"
    "s_waitcnt vmcnt(2)\n"
    "buffer_atomic_smax v12, v14, s[36:39], s70 offen sc1\n"
    "v_mfma_f32_16x16x32_bf16 v[76:79], a[212:215], v[24:27], v[76:79]\n"
    "ds_read_b128 v[24:27], v9 offset:11264\n"
    "v_mfma_f32_16x16x32_bf16 v[40:43], a[216:219], v[16:19], v[40:43]\n"
    "v_mfma_f32_16x16x32_bf16 v[44:47], a[220:223], v[16:19], v[44:47]\n"
    "v_mfma_f32_16x16x32_bf16 v[52:55], v[16:19], a[224:227], v[52:55]\n"
    "v_mfma_f32_16x16x32_bf16 v[60:63], v[16:19], a[228:231], v[60:63]\n"
    "v_mfma_f32_16x16x32_bf16 v[72:75], a[232:235], v[16:19], v[72:75]\n"
    "s_waitcnt lgkmcnt(1)\n"
    "v_mfma_f32_16x16x32_bf16 v[76:79], a[236:239], v[16:19], v[76:79]\n"
    "ds_read_b128 v[16:19], v9 offset:12288\n"
    "v_mfma_f32_16x16x32_bf16 v[40:43], a[240:243], v[20:23], v[40:43]\n"
    "v_mfma_f32_16x16x32_bf16 v[44:47], a[244:247], v[20:23], v[44:47]\n"
    "s_waitcnt vmcnt(2)\n"
    "v_add_u32 v13, v5, v3\n"
    "v_mfma_f32_16x16x32_bf16 v[52:55], v[20:23], a[248:251], v[52:55]\n"
    "v_cmp_gt_i32 vcc, s71, v13\n"
    "v_mfma_f32_16x16x32_bf16 v[60:63], v[20:23], a[252:255], v[60:63]\n"
    "v_mfma_f32_16x16x32_bf16 v[72:75], v[80:83], v[20:23], v[72:75]\n"
    "s_waitcnt lgkmcnt(1)\n"
    "v_mfma_f32_16x16x32_bf16 v[76:79], v[84:87], v[20:23], v[76:79]\n"
    "ds_read_b128 v[20:23], v9 offset:13312\n"
    "s_cbranch_vccz .LgP_dma1_66_%=\n"
    "v_mfma_f32_16x16x32_bf16 v[40:43], v[88:91], v[24:27], v[40:43]\n"
    "s_or_b64 exec, vcc, s[62:63]\n"
    "buffer_atomic_smax v5, v2, s[36:39], 0 offen sc0 sc1\n"
    "s_mov_b64 exec, -1\n"
    "v_mfma_f32_16x16x32_bf16 v[44:47], v[92:95], v[24:27], v[44:47]\n"
    "v_mfma_f32_16x16x32_bf16 v[52:55], v[24:27], v[96:99], v[52:55]\n"
    "v_mfma_f32_16x16x32_bf16 v[60:63], v[24:27], v[100:103], v[60:63]\n"
    "v_mfma_f32_16x16x32_bf16 v[72:75], v[104:107], v[24:27], v[72:75]\n"
    "s_waitcnt lgkmcnt(1)\n"
    "v_mfma_f32_16x16x32_bf16 v[76:79], v[108:111], v[24:27], v[76:79]\n"
    "ds_read_b128 v[24:27], v9 offset:14336\n"
    "v_mfma_f32_16x16x32_bf16 v[40:43], v[112:115], v[16:19], v[40:43]\n"
    "v_mfma_f32_16x16x32_bf16 v[44:47], v[116:119], v[16:19], v[44:47]\n"
    "v_mfma_f32_16x16x32_bf16 v[52:55], v[16:19], v[120:123], v[52:55]\n"
    "v_mfma_f32_16x16x32_bf16 v[60:63], v[16:19], v[124:127], v[60:63]\n"
    "v_mfma_f32_16x16x32_bf16 v[72:75], v[128:131], v[16:19], v[72:75]\n"
    "s_waitcnt lgkmcnt(1)\n"
    "v_mfma_f32_16x16x32_bf16 v[76:79], v[132:135], v[16:19], v[76:79]\n"
    "ds_read_b128 v[16:19], v9 offset:15360\n"
    "v_mfma_f32_16x16x32_bf16 v[40:43], v[136:139], v[20:23], v[40:43]\n"
    "v_mfma_f32_16x16x32_bf16 v[44:47], v[140:143], v[20:23], v[44:47]\n"
    "s_waitcnt vmcnt(2)\n"
    "v_add_u32 v13, v11, v3\n"
    "v_mfma_f32_16x16x32_bf16 v[52:55], v[20:23], v[144:147], v[52:55]\n"
    "v_cmp_gt_i32 vcc, s71, v13\n"
    "v_mfma_f32_16x16x32_bf16 v[60:63], v[20:23], v[148:151], v[60:63]\n"
    "v_mfma_f32_16x16x32_bf16 v[72:75], v[152:155], v[20:23], v[72:75]\n"
    "s_waitcnt lgkmcnt(1)\n"
    "v_mfma_f32_16x16x32_bf16 v[76:79], v[156:159], v[20:23], v[76:79]\n"
    "ds_read_b128 v[20:23], v9 offset:16384\n"
    "s_cbranch_vccz .LgP_dma1_84_%=\n"
    "v_mfma_f32_16x16x32_bf16 v[40:43], v[160:163], v[24:27], v[40:43]\n"
    "s_or_b64 exec, vcc, s[62:63]\n"
    "buffer_atomic_smax v11, v2, s[36:39], 0 offen sc0 sc1\n"
    "s_mov_b64 exec, -1\n"
    "v_mfma_f32_16x16x32_bf16 v[44:47], v[164:167], v[24:27], v[44:47]\n"
    "v_mfma_f32_16x16x32_bf16 v[52:55], v[24:27], v[168:171], v[52:55]\n"
    "v_mfma_f32_16x16x32_bf16 v[60:63], v[24:27], v[172:175], v[60:63]\n"
    "v_mfma_f32_16x16x32_bf16 v[72:75], v[176:179], v[24:27], v[72:75]\n"
    "s_waitcnt lgkmcnt(1)\n"
    "v_mfma_f32_16x16x32_bf16 v[76:79], v[180:183], v[24:27], v[76:79]\n"
    "ds_read_b128 v[24:27], v9 offset:17408\n"
    "v_mfma_f32_16x16x32_bf16 v[40:43], v[184:187], v[16:19], v[40:43]\n"
    "v_mfma_f32_16x16x32_bf16 v[44:47], v[188:191], v[16:19], v[44:47]\n"
    "v_mfma_f32_16x16x32_bf16 v[52:55], v[16:19], v[192:195], v[52:55]\n"
    "v_mfma_f32_16x16x32_bf16 v[60:63], v[16:19], v[196:199], v[60:63]\n"
    "v_mfma_f32_16x16x32_bf16 v[72:75], v[200:203], v[16:19], v[72:75]\n"
    "s_waitcnt lgkmcnt(1)\n"
    "v_mfma_f32_16x16x32_bf16 v[76:79], v[204:207], v[16:19], v[76:79]\n"
    "v_mfma_f32_16x16x32_bf16 v[40:43], v[208:211], v[20:23], v[40:43]\n"
    "v_mfma_f32_16x16x32_bf16 v[44:47], v[212:215], v[20:23], v[44:47]\n"
    "s_waitcnt vmcnt(1)\n"
    "v_add_u32 v13, v5, v3\n"
    "v_mfma_f32_16x16x32_bf16 v[52:55], v[20:23], v[216:219], v[52:55]\n"
    "v_cmp_gt_i32 vcc, s71, v13\n"
    "v_mfma_f32_16x16x32_bf16 v[60:63], v[20:23], v[220:223], v[60:63]\n"
    "v_mfma_f32_16x16x32_bf16 v[72:75], v[224:227], v[20:23], v[72:75]\n"
    "s_waitcnt lgkmcnt(0)\n"
    "v_mfma_f32_16x16x32_bf16 v[76:79], v[228:231], v[20:23], v[76:79]\n"
    "s_cbranch_vccz .LgP_dma1_102_%=\n"
    "v_mfma_f32_16x16x32_bf16 v[40:43], v[232:235], v[24:27], v[40:43]\n"
    "s_or_b64 exec, vcc, s[62:63]\n"
    "buffer_atomic_smax v5, v2, s[36:39], 0 offen sc0 sc1\n"
    "s_mov_b64 exec, -1\n"
    "v_mfma_f32_16x16x32_bf16 v[44:47], v[236:239], v[24:27], v[44:47]\n"
    "v_mfma_f32_16x16x32_bf16 v[52:55], v[24:27], v[240:243], v[52:55]\n"
    "v_mfma_f32_16x16x32_bf16 v[60:63], v[24:27], v[244:247], v[60:63]\n"
    "v_mfma_f32_16x16x32_bf16 v[72:75], v[248:251], v[24:27], v[72:75]\n"
    "v_mfma_f32_16x16x32_bf16 v[76:79], v[252:255], v[24:27], v[76:79]\n"
    "v_cvt_pk_bf16_f32 v40, v40, v41\n"
    "v_cvt_pk_bf16_f32 v41, v42, v43\n"
    "v_cvt_pk_bf16_f32 v42, v44, v45\n"
    "v_cvt_pk_bf16_f32 v43, v46, v47\n"
    "buffer_store_dwordx4 v[40:43], v1, s[36:39], s79 offen offset:1024 sc1\n"
    "v_cvt_pk_bf16_f32 v55, v54, v55\n"
    "v_cvt_pk_bf16_f32 v54, v52, v53\n"
    "v_mov_b64 v[52:53], v[28:29]\n"
    "buffer_store_dwordx4 v[52:55], v1, s[36:39], s80 offen sc1\n"
    "v_cvt_pk_bf16_f32 v63, v62, v63\n"
    "v_cvt_pk_bf16_f32 v62, v60, v61\n"
    "v_mov_b64 v[60:61], v[30:31]\n"
    "buffer_store_dwordx4 v[60:63], v1, s[36:39], s80 offen offset:1024 sc1\n"
    "v_cvt_pk_bf16_f32 v72, v72, v73\n"
    "v_cvt_pk_bf16_f32 v73, v74, v75\n"
    "v_cvt_pk_bf16_f32 v74, v76, v77\n"
    "v_cvt_pk_bf16_f32 v75, v78, v79\n"
    "buffer_store_dwordx4 v[72:75], v1, s[36:39], s73 offen offset:1024 sc1\n"
    "v_mov_b32 v12, s68\n"
    "s_mov_b32 s73, 0x80000000\n"
    "s_mov_b32 s79, 0x80000000\n"
    "s_mov_b32 s80, 0x80000000\n"
    "s_mov_b32 s68, s71\n"
    "s_cmp_eq_u32 s68, 128\n"
    "s_cbranch_scc1 .LgP_done_%=\n"
    "s_waitcnt vmcnt(5)\n"
    "v_add_u32 v13, v11, v3\n"
    "v_cmp_gt_i32 vcc, s71, v13\n"
    "s_cbranch_vccnz .LgP_idle1_%=\n"
    "s_add_u32 m0, s59, 0\n"
    "s_nop 0\n"
    "buffer_load_dwordx4 v1, s[36:39], s76 offen sc1 lds\n"
    "s_add_u32 m0, s59, 4096\n"
    "s_add_u32 s76, s76, 16384\n"
    "buffer_load_dwordx4 v1, s[36:39], s76 offen sc1 lds\n"
    "s_add_u32 m0, s59, 8192\n"
    "s_add_u32 s76, s76, 16384\n"
    "buffer_load_dwordx4 v1, s[36:39], s76 offen sc1 lds\n"
    "s_add_u32 m0, s59, 12288\n"
    "s_add_u32 s76, s76, 16384\n"
    "buffer_load_dwordx4 v1, s[36:39], s76 offen sc1 lds\n"
    "s_add_u32 m0, s59, 16384\n"
    "s_add_u32 s76, s76, 16384\n"
    "buffer_load_dwordx4 v8, s[36:39], s76 offen sc1 lds\n"
    "s_waitcnt vmcnt(5)\n"
    "buffer_atomic_smax v12, v10, s[36:39], 0 offen sc1\n"
    "s_mov_b32 s69, 0x80000000\n"
    "s_mov_b32 s70, 0x80000000\n"
    "v_mov_b32 v7, v12\n"
    "s_waitcnt vmcnt(10)\n"
    "s_add_u32 s71, s68, 1\n"
    "v_add_u32 v13, v11, v3\n"
    "v_cmp_gt_i32 vcc, s71, v13\n"
    "s_nop 0\n"
    "s_or_b64 exec, vcc, s[62:63]\n"
    "buffer_atomic_smax v11, v2, s[36:39], 0 offen sc0 sc1\n"
    "s_mov_b64 exec, -1\n"
    "s_waitcnt vmcnt(2)\n"
    "s_branch .LgP_bar0_%=\n"
    ".LgP_dma1_30_%=:\n"
    "v_mfma_f32_16x16x32_bf16 v[40:43], a[120:123], v[24:27], v[40:43]\n"
    "s_add_u32 m0, s59, 0\n"
    "v_mfma_f32_16x16x32_bf16 v[44:47], a[124:127], v[24:27], v[44:47]\n"
    "buffer_load_dwordx4 v1, s[36:39], s76 offen sc1 lds\n"
    "v_mfma_f32_16x16x32_bf16 v[52:55], v[24:27], a[128:131], v[52:55]\n"
    "s_add_u32 s76, s76, 16384\n"
    "s_add_u32 m0, s59, 4096\n"
    "v_mfma_f32_16x16x32_bf16 v[60:63], v[24:27], a[132:135], v[60:63]\n"
    "buffer_load_dwordx4 v1, s[36:39], s76 offen sc1 lds\n"
    "v_mfma_f32_16x16x32_bf16 v[72:75], a[136:139], v[24:27], v[72:75]\n"
    "s_waitcnt lgkmcnt(1)\n"
    "v_mfma_f32_16x16x32_bf16 v[76:79], a[140:143], v[24:27], v[76:79]\n"
    "ds_read_b128 v[24:27], v9 offset:8192\n"
    "v_mfma_f32_16x16x32_bf16 v[40:43], a[144:147], v[16:19], v[40:43]\n"
    "s_add_u32 s76, s76, 16384\n"
    "s_add_u32 m0, s59, 8192\n"
    "v_mfma_f32_16x16x32_bf16 v[44:47], a[148:151], v[16:19], v[44:47]\n"
    "buffer_load_dwordx4 v1, s[36:39], s76 offen sc1 lds\n"
    "v_mfma_f32_16x16x32_bf16 v[52:55], v[16:19], a[152:155], v[52:55]\n"
    "v_mfma_f32_16x16x32_bf16 v[60:63], v[16:19], a[156:159], v[60:63]\n"
    "v_mfma_f32_16x16x32_bf16 v[72:75], a[160:163], v[16:19], v[72:75]\n"
    "s_waitcnt lgkmcnt(1)\n"
    "s_add_u32 s76, s76, 16384\n"
    "s_add_u32 m0, s59, 12288\n"
    "v_mfma_f32_16x16x32_bf16 v[76:79], a[164:167], v[16:19], v[76:79]\n"
    "ds_read_b128 v[16:19], v9 offset:9216\n"
    "buffer_load_dwordx4 v1, s[36:39], s76 offen sc1 lds\n"
    "v_mfma_f32_16x16x32_bf16 v[40:43], a[168:171], v[20:23], v[40:43]\n"
    "v_mfma_f32_16x16x32_bf16 v[44:47], a[172:175], v[20:23], v[44:47]\n"
    "v_mfma_f32_16x16x32_bf16 v[52:55], v[20:23], a[176:179], v[52:55]\n"
    "s_add_u32 s76, s76, 16384\n"
    "s_add_u32 m0, s59, 16384\n"
    "v_mfma_f32_16x16x32_bf16 v[60:63], v[20:23], a[180:183], v[60:63]\n"
    "buffer_load_dwordx4 v8, s[36:39], s76 offen sc1 lds\n"
    "v_mfma_f32_16x16x32_bf16 v[72:75], a[184:187], v[20:23], v[72:75]\n"
    "s_waitcnt lgkmcnt(1)\n"
    "v_mfma_f32_16x16x32_bf16 v[76:79], a[188:191], v[20:23], v[76:79]\n"
    "ds_read_b128 v[20:23], v9 offset:10240\n"
    "v_mfma_f32_16x16x32_bf16 v[40:43], a[192:195], v[24:27], v[40:43]\n"
    "v_mfma_f32_16x16x32_bf16 v[44:47], a[196:199], v[24:27], v[44:47]\n"
    "v_mfma_f32_16x16x32_bf16 v[52:55], v[24:27], a[200:203], v[52:55]\n"
    "v_mfma_f32_16x16x32_bf16 v[60:63], v[24:27], a[204:207], v[60:63]\n"
    "v_mfma_f32_16x16x32_bf16 v[72:75], a[208:211], v[24:27], v[72:75]\n"
    "s_waitcnt lgkmcnt(1)\n"
    "s_waitcnt vmcnt(5)\n"
    "buffer_atomic_smax v12, v14, s[36:39], s70 offen sc1\n"
    "v_mfma_f32_16x16x32_bf16 v[76:79], a[212:215], v[24:27], v[76:79]\n"
    "ds_read_b128 v[24:27], v9 offset:11264\n"
    "v_mfma_f32_16x16x32_bf16 v[40:43], a[216:219], v[16:19], v[40:43]\n"
    "v_mfma_f32_16x16x32_bf16 v[44:47], a[220:223], v[16:19], v[44:47]\n"
    "v_mfma_f32_16x16x32_bf16 v[52:55], v[16:19], a[224:227], v[52:55]\n"
    "v_mfma_f32_16x16x32_bf16 v[60:63], v[16:19], a[228:231], v[60:63]\n"
    "v_mfma_f32_16x16x32_bf16 v[72:75], a[232:235], v[16:19], v[72:75]\n"
    "s_waitcnt lgkmcnt(1)\n"
    "v_mfma_f32_16x16x32_bf16 v[76:79], a[236:239], v[16:19], v[76:79]\n"
    "ds_read_b128 v[16:19], v9 offset:12288\n"
    "v_mfma_f32_16x16x32_bf16 v[40:43], a[240:243], v[20:23], v[40:43]\n"
    "v_mfma_f32_16x16x32_bf16 v[44:47], a[244:247], v[20:23], v[44:47]\n"
    "v_mfma_f32_16x16x32_bf16 v[52:55], v[20:23], a[248:251], v[52:55]\n"
    "v_mfma_f32_16x16x32_bf16 v[60:63], v[20:23], a[252:255], v[60:63]\n"
    "v_mfma_f32_16x16x32_bf16 v[72:75], v[80:83], v[20:23], v[72:75]\n"
    "s_waitcnt lgkmcnt(1)\n"
    "v_mfma_f32_16x16x32_bf16 v[76:79], v[84:87], v[20:23], v[76:79]\n"
    "ds_read_b128 v[20:23], v9 offset:13312\n"
    "v_mfma_f32_16x16x32_bf16 v[40:43], v[88:91], v[24:27], v[40:43]\n"
    "v_mfma_f32_16x16x32_bf16 v[44:47], v[92:95], v[24:27], v[44:47]\n"
    "v_mfma_f32_16x16x32_bf16 v[52:55], v[24:27], v[96:99], v[52:55]\n"
    "v_mfma_f32_16x16x32_bf16 v[60:63], v[24:27], v[100:103], v[60:63]\n"
    "v_mfma_f32_16x16x32_bf16 v[72:75], v[104:107], v[24:27], v[72:75]\n"
    "s_waitcnt lgkmcnt(1)\n"
    "v_mfma_f32_16x16x32_bf16 v[76:79], v[108:111], v[24:27], v[76:79]\n"
    "ds_read_b128 v[24:27], v9 offset:14336\n"
    "v_mfma_f32_16x16x32_bf16 v[40:43], v[112:115], v[16:19], v[40:43]\n"
    "v_mfma_f32_16x16x32_bf16 v[44:47], v[116:119], v[16:19], v[44:47]\n"
    "v_mfma_f32_16x16x32_bf16 v[52:55], v[16:19], v[120:123], v[52:55]\n"
    "v_mfma_f32_16x16x32_bf16 v[60:63], v[16:19], v[124:127], v[60:63]\n"
    "v_mfma_f32_16x16x32_bf16 v[72:75], v[128:131], v[16:19], v[72:75]\n"
    "s_waitcnt lgkmcnt(1)\n"
    "v_mfma_f32_16x16x32_bf16 v[76:79], v[132:135], v[16:19], v[76:79]\n"
    "ds_read_b128 v[16:19], v9 offset:15360\n"
    "s_branch .LgP_join1_%=\n"
    ".LgP_dma1_48_%=:\n"
    "v_mfma_f32_16x16x32_bf16 v[40:43], a[192:195], v[24:27], v[40:43]\n"
    "s_add_u32 m0, s59, 0\n"
    "v_mfma_f32_16x16x32_bf16 v[44:47], a[196:199], v[24:27], v[44:47]\n"
    "buffer_load_dwordx4 v1, s[36:39], s76 offen sc1 lds\n"
    "v_mfma_f32_16x16x32_bf16 v[52:55], v[24:27], a[200:203], v[52:55]\n"
    "s_add_u32 s76, s76, 16384\n"
    "s_add_u32 m0, s59, 4096\n"
    "v_mfma_f32_16x16x32_bf16 v[60:63], v[24:27], a[204:207], v[60:63]\n"
    "buffer_load_dwordx4 v1, s[36:39], s76 offen sc1 lds\n"
    "v_mfma_f32_16x16x32_bf16 v[72:75], a[208:211], v[24:27], v[72:75]\n"
    "s_waitcnt lgkmcnt(1)\n"
    "s_waitcnt vmcnt(3)\n"
    "buffer_atomic_smax v12, v14, s[36:39], s70 offen sc1\n"
    "v_mfma_f32_16x16x32_bf16 v[76:79], a[212:215], v[24:27], v[76:79]\n"
    "ds_read_b128 v[24:27], v9 offset:11264\n"
    "v_mfma_f32_16x16x32_bf16 v[40:43], a[216:219], v[16:19], v[40:43]\n"
    "s_add_u32 s76, s76, 16384\n"
    "s_add_u32 m0, s59, 8192\n"
    "v_mfma_f32_16x16x32_bf16 v[44:47], a[220:223], v[16:19], v[44:47]\n"
    "buffer_load_dwordx4 v1, s[36:39], s76 offen sc1 lds\n"
    "v_mfma_f32_16x16x32_bf16 v[52:55], v[16:19], a[224:227], v[52:55]\n"
    "v_mfma_f32_16x16x32_bf16 v[60:63], v[16:19], a[228:231], v[60:63]\n"
    "v_mfma_f32_16x16x32_bf16 v[72:75], a[232:235], v[16:19], v[72:75]\n"
    "s_waitcnt lgkmcnt(1)\n"
    "s_add_u32 s76, s76, 16384\n"
    "s_add_u32 m0, s59, 12288\n"
    "v_mfma_f32_16x16x32_bf16 v[76:79], a[236:239], v[16:19], v[76:79]\n"
    "ds_read_b128 v[16:19], v9 offset:12288\n"
    "buffer_load_dwordx4 v1, s[36:39], s76 offen sc1 lds\n"
    "v_mfma_f32_16x16x32_bf16 v[40:43], a[240:243], v[20:23], v[40:43]\n"
    "v_mfma_f32_16x16x32_bf16 v[44:47], a[244:247], v[20:23], v[44:47]\n"
    "v_mfma_f32_16x16x32_bf16 v[52:55], v[20:23], a[248:251], v[52:55]\n"
    "s_add_u32 s76, s76, 16384\n"
    "s_add_u32 m0, s59, 16384\n"
    "v_mfma_f32_16x16x32_bf16 v[60:63], v[20:23], a[252:255], v[60:63]\n"
    "buffer_load_dwordx4 v8, s[36:39], s76 offen sc1 lds\n"
    "v_mfma_f32_16x16x32_bf16 v[72:75], v[80:83], v[20:23], v[72:75]\n"
    "s_waitcnt lgkmcnt(1)\n"
    "v_mfma_f32_16x16x32_bf16 v[76:79], v[84:87], v[20:23], v[76:79]\n"
    "ds_read_b128 v[20:23], v9 offset:13312\n"
    "v_mfma_f32_16x16x32_bf16 v[40:43], v[88:91], v[24:27], v[40:43]\n"
    "v_mfma_f32_16x16x32_bf16 v[44:47], v[92:95], v[24:27], v[44:47]\n"
    "v_mfma_f32_16x16x32_bf16 v[52:55], v[24:27], v[96:99], v[52:55]\n"
    "v_mfma_f32_16x16x32_bf16 v[60:63], v[24:27], v[100:103], v[60:63]\n"
    "v_mfma_f32_16x16x32_bf16 v[72:75], v[104:107], v[24:27], v[72:75]\n"
    "s_waitcnt lgkmcnt(1)\n"
    "v_mfma_f32_16x16x32_bf16 v[76:79], v[108:111], v[24:27], v[76:79]\n"
    "ds_read_b128 v[24:27], v9 offset:14336\n"
    "v_mfma_f32_16x16x32_bf16 v[40:43], v[112:115], v[16:19], v[40:43]\n"
    "v_mfma_f32_16x16x32_bf16 v[44:47], v[116:119], v[16:19], v[44:47]\n"
    "v_mfma_f32_16x16x32_bf16 v[52:55], v[16:19], v[120:123], v[52:55]\n"
    "v_mfma_f32_16x16x32_bf16 v[60:63], v[16:19], v[124:127], v[60:63]\n"
    "v_mfma_f32_16x16x32_bf16 v[72:75], v[128:131], v[16:19], v[72:75]\n"
    "s_waitcnt lgkmcnt(1)\n"
    "v_mfma_f32_16x16x32_bf16 v[76:79], v[132:135], v[16:19], v[76:79]\n"
    "ds_read_b128 v[16:19], v9 offset:15360\n"
    "s_branch .LgP_join1_%=\n"
    ".LgP_dma1_66_%=:\n"
    "v_mfma_f32_16x16x32_bf16 v[40:43], v[88:91], v[24:27], v[40:43]\n"
    "s_add_u32 m0, s59, 0\n"
    "v_mfma_f32_16x16x32_bf16 v[44:47], v[92:95], v[24:27], v[44:47]\n"
    "buffer_load_dwordx4 v1, s[36:39], s76 offen sc1 lds\n"
    "v_mfma_f32_16x16x32_bf16 v[52:55], v[24:27], v[96:99], v[52:55]\n"
    "s_add_u32 s76, s76, 16384\n"
    "s_add_u32 m0, s59, 4096\n"
    "v_mfma_f32_16x16x32_bf16 v[60:63], v[24:27], v[100:103], v[60:63]\n"
    "buffer_load_dwordx4 v1, s[36:39], s76 offen sc1 lds\n"
    "v_mfma_f32_16x16x32_bf16 v[72:75], v[104:107], v[24:27], v[72:75]\n"
    "s_waitcnt lgkmcnt(1)\n"
    "v_mfma_f32_16x16x32_bf16 v[76:79], v[108:111], v[24:27], v[76:79]\n"
    "ds_read_b128 v[24:27], v9 offset:14336\n"
    "v_mfma_f32_16x16x32_bf16 v[40:43], v[112:115], v[16:19], v[40:43]\n"
    "s_add_u32 s76, s76, 16384\n"
    "s_add_u32 m0, s59, 8192\n"
    "v_mfma_f32_16x16x32_bf16 v[44:47], v[116:119], v[16:19], v[44:47]\n"
    "buffer_load_dwordx4 v1, s[36:39], s76 offen sc1 lds\n"
    "v_mfma_f32_16x16x32_bf16 v[52:55], v[16:19], v[120:123], v[52:55]\n"
    "v_mfma_f32_16x16x32_bf16 v[60:63], v[16:19], v[124:127], v[60:63]\n"
    "v_mfma_f32_16x16x32_bf16 v[72:75], v[128:131], v[16:19], v[72:75]\n"
    "s_waitcnt lgkmcnt(1)\n"
    "s_add_u32 s76, s76, 16384\n"
    "s_add_u32 m0, s59, 12288\n"
    "v_mfma_f32_16x16x32_bf16 v[76:79], v[132:135], v[16:19], v[76:79]\n"
    "ds_read_b128 v[16:19], v9 offset:15360\n"
    "buffer_load_dwordx4 v1, s[36:39], s76 offen sc1 lds\n"
    "v_mfma_f32_16x16x32_bf16 v[40:43], v[136:139], v[20:23], v[40:43]\n"
    "v_mfma_f32_16x16x32_bf16 v[44:47], v[140:143], v[20:23], v[44:47]\n"
    "v_mfma_f32_16x16x32_bf16 v[52:55], v[20:23], v[144:147], v[52:55]\n"
    "s_add_u32 s76, s76, 16384\n"
    "s_add_u32 m0, s59, 16384\n"
    "v_mfma_f32_16x16x32_bf16 v[60:63], v[20:23], v[148:151], v[60:63]\n"
    "buffer_load_dwordx4 v8, s[36:39], s76 offen sc1 lds\n"
    "v_mfma_f32_16x16x32_bf16 v[72:75], v[152:155], v[20:23], v[72:75]\n"
    "s_waitcnt lgkmcnt(1)\n"
    "v_mfma_f32_16x16x32_bf16 v[76:79], v[156:159], v[20:23], v[76:79]\n"
    "ds_read_b128 v[20:23], v9 offset:16384\n"
    "v_mfma_f32_16x16x32_bf16 v[40:43], v[160:163], v[24:27], v[40:43]\n"
    "v_mfma_f32_16x16x32_bf16 v[44:47], v[164:167], v[24:27], v[44:47]\n"
    "v_mfma_f32_16x16x32_bf16 v[52:55], v[24:27], v[168:171], v[52:55]\n"
    "v_mfma_f32_16x16x32_bf16 v[60:63], v[24:27], v[172:175], v[60:63]\n"
    "v_mfma_f32_16x16x32_bf16 v[72:75], v[176:179], v[24:27], v[72:75]\n"
    "s_waitcnt lgkmcnt(1)\n"
    "v_mfma_f32_16x16x32_bf16 v[76:79], v[180:183], v[24:27], v[76:79]\n"
    "ds_read_b128 v[24:27], v9 offset:17408\n"
    "v_mfma_f32_16x16x32_bf16 v[40:43], v[184:187], v[16:19], v[40:43]\n"
    "v_mfma_f32_16x16x32_bf16 v[44:47], v[188:191], v[16:19], v[44:47]\n"
    "v_mfma_f32_16x16x32_bf16 v[52:55], v[16:19], v[192:195], v[52:55]\n"
    "v_mfma_f32_16x16x32_bf16 v[60:63], v[16:19], v[196:199], v[60:63]\n"
    "v_mfma_f32_16x16x32_bf16 v[72:75], v[200:203], v[16:19], v[72:75]\n"
    "s_waitcnt lgkmcnt(1)\n"
    "v_mfma_f32_16x16x32_bf16 v[76:79], v[204:207], v[16:19], v[76:79]\n"
    "v_mfma_f32_16x16x32_bf16 v[40:43], v[208:211], v[20:23], v[40:43]\n"
    "v_mfma_f32_16x16x32_bf16 v[44:47], v[212:215], v[20:23], v[44:47]\n"
    "v_mfma_f32_16x16x32_bf16 v[52:55], v[20:23], v[216:219], v[52:55]\n"
    "v_mfma_f32_16x16x32_bf16 v[60:63], v[20:23], v[220:223], v[60:63]\n"
    "v_mfma_f32_16x16x32_bf16 v[72:75], v[224:227], v[20:23], v[72:75]\n"
    "s_waitcnt lgkmcnt(0)\n"
    "v_mfma_f32_16x16x32_bf16 v[76:79], v[228:231], v[20:23], v[76:79]\n"
    "v_mfma_f32_16x16x32_bf16 v[40:43], v[232:235], v[24:27], v[40:43]\n"
    "v_mfma_f32_16x16x32_bf16 v[44:47], v[236:239], v[24:27], v[44:47]\n"
    "v_mfma_f32_16x16x32_bf16 v[52:55], v[24:27], v[240:243], v[52:55]\n"
    "v_mfma_f32_16x16x32_bf16 v[60:63], v[24:27], v[244:247], v[60:63]\n"
    "v_mfma_f32_16x16x32_bf16 v[72:75], v[248:251], v[24:27], v[72:75]\n"
    "v_mfma_f32_16x16x32_bf16 v[76:79], v[252:255], v[24:27], v[76:79]\n"
    "v_cvt_pk_bf16_f32 v40, v40, v41\n"
    "v_cvt_pk_bf16_f32 v41, v42, v43\n"
    "v_cvt_pk_bf16_f32 v42, v44, v45\n"
    "v_cvt_pk_bf16_f32 v43, v46, v47\n"
    "buffer_store_dwordx4 v[40:43], v1, s[36:39], s79 offen offset:1024 sc1\n"
    "v_cvt_pk_bf16_f32 v55, v54, v55\n"
    "v_cvt_pk_bf16_f32 v54, v52, v53\n"
    "v_mov_b64 v[52:53], v[28:29]\n"
    "buffer_store_dwordx4 v[52:55], v1, s[36:39], s80 offen sc1\n"
    "v_cvt_pk_bf16_f32 v63, v62, v63\n"
    "v_cvt_pk_bf16_f32 v62, v60, v61\n"
    "v_mov_b64 v[60:61], v[30:31]\n"
    "buffer_store_dwordx4 v[60:63], v1, s[36:39], s80 offen offset:1024 sc1\n"
    "v_cvt_pk_bf16_f32 v72, v72, v73\n"
    "v_cvt_pk_bf16_f32 v73, v74, v75\n"
    "v_cvt_pk_bf16_f32 v74, v76, v77\n"
    "v_cvt_pk_bf16_f32 v75, v78, v79\n"
    "buffer_store_dwordx4 v[72:75], v1, s[36:39], s73 offen offset:1024 sc1\n"
    "v_mov_b32 v12, s68\n"
    "s_mov_b32 s73, 0x80000000\n"
    "s_mov_b32 s79, 0x80000000\n"
    "s_mov_b32 s80, 0x80000000\n"
    "s_mov_b32 s68, s71\n"
    "s_mov_b32 s69, 0\n"
    "s_mov_b32 s70, 0x80000000\n"
    "v_mov_b32 v7, v12\n"
    "s_waitcnt vmcnt(10)\n"
    "s_add_u32 s71, s68, 1\n"
    "v_add_u32 v13, v11, v3\n"
    "v_cmp_gt_i32 vcc, s71, v13\n"
    "s_nop 0\n"
    "s_or_b64 exec, vcc, s[62:63]\n"
    "buffer_atomic_smax v11, v2, s[36:39], 0 offen sc0 sc1\n"
    "s_mov_b64 exec, -1\n"
    "s_waitcnt vmcnt(5)\n"
    "s_branch .LgP_bar0_%=\n"
    ".LgP_dma1_84_%=:\n"
    "v_mfma_f32_16x16x32_bf16 v[40:43], v[160:163], v[24:27], v[40:43]\n"
    "s_add_u32 m0, s59, 0\n"
    "v_mfma_f32_16x16x32_bf16 v[44:47], v[164:167], v[24:27], v[44:47]\n"
    "buffer_load_dwordx4 v1, s[36:39], s76 offen sc1 lds\n"
    "v_mfma_f32_16x16x32_bf16 v[52:55], v[24:27], v[168:171], v[52:55]\n"
    "s_add_u32 s76, s76, 16384\n"
    "s_add_u32 m0, s59, 4096\n"
    "v_mfma_f32_16x16x32_bf16 v[60:63], v[24:27], v[172:175], v[60:63]\n"
    "buffer_load_dwordx4 v1, s[36:39], s76 offen sc1 lds\n"
    "v_mfma_f32_16x16x32_bf16 v[72:75], v[176:179], v[24:27], v[72:75]\n"
    "s_waitcnt lgkmcnt(1)\n"
    "v_mfma_f32_16x16x32_bf16 v[76:79], v[180:183], v[24:27], v[76:79]\n"
    "ds_read_b128 v[24:27], v9 offset:17408\n"
    "v_mfma_f32_16x16x32_bf16 v[40:43], v[184:187], v[16:19], v[40:43]\n"
    "s_add_u32 s76, s76, 16384\n"
    "s_add_u32 m0, s59, 8192\n"
    "v_mfma_f32_16x16x32_bf16 v[44:47], v[188:191], v[16:19], v[44:47]\n"
    "buffer_load_dwordx4 v1, s[36:39], s76 offen sc1 lds\n"
    "v_mfma_f32_16x16x32_bf16 v[52:55], v[16:19], v[192:195], v[52:55]\n"
    "v_mfma_f32_16x16x32_bf16 v[60:63], v[16:19], v[196:199], v[60:63]\n"
    "v_mfma_f32_16x16x32_bf16 v[72:75], v[200:203], v[16:19], v[72:75]\n"
    "s_waitcnt lgkmcnt(1)\n"
    "s_add_u32 s76, s76, 16384\n"
    "s_add_u32 m0, s59, 12288\n"
    "v_mfma_f32_16x16x32_bf16 v[76:79], v[204:207], v[16:19], v[76:79]\n"
    "buffer_load_dwordx4 v1, s[36:39], s76 offen sc1 lds\n"
    "v_mfma_f32_16x16x32_bf16 v[40:43], v[208:211], v[20:23], v[40:43]\n"
    "v_mfma_f32_16x16x32_bf16 v[44:47], v[212:215], v[20:23], v[44:47]\n"
    "v_mfma_f32_16x16x32_bf16 v[52:55], v[20:23], v[216:219], v[52:55]\n"
    "s_add_u32 s76, s76, 16384\n"
    "s_add_u32 m0, s59, 16384\n"
    "v_mfma_f32_16x16x32_bf16 v[60:63], v[20:23], v[220:223], v[60:63]\n"
    "buffer_load_dwordx4 v8, s[36:39], s76 offen sc1 lds\n"
    "v_mfma_f32_16x16x32_bf16 v[72:75], v[224:227], v[20:23], v[72:75]\n"
    "s_waitcnt lgkmcnt(0)\n"
    "v_mfma_f32_16x16x32_bf16 v[76:79], v[228:231], v[20:23], v[76:79]\n"
    "v_mfma_f32_16x16x32_bf16 v[40:43], v[232:235], v[24:27], v[40:43]\n"
    "v_mfma_f32_16x16x32_bf16 v[44:47], v[236:239], v[24:27], v[44:47]\n"
    "v_mfma_f32_16x16x32_bf16 v[52:55], v[24:27], v[240:243], v[52:55]\n"
    "v_mfma_f32_16x16x32_bf16 v[60:63], v[24:27], v[244:247], v[60:63]\n"
    "v_mfma_f32_16x16x32_bf16 v[72:75], v[248:251], v[24:27], v[72:75]\n"
    "v_mfma_f32_16x16x32_bf16 v[76:79], v[252:255], v[24:27], v[76:79]\n"
    "v_cvt_pk_bf16_f32 v40, v40, v41\n"
    "v_cvt_pk_bf16_f32 v41, v42, v43\n"
    "v_cvt_pk_bf16_f32 v42, v44, v45\n"
    "v_cvt_pk_bf16_f32 v43, v46, v47\n"
    "buffer_store_dwordx4 v[40:43], v1, s[36:39], s79 offen offset:1024 sc1\n"
    "v_cvt_pk_bf16_f32 v55, v54, v55\n"
    "v_cvt_pk_bf16_f32 v54, v52, v53\n"
    "v_mov_b64 v[52:53], v[28:29]\n"
    "buffer_store_dwordx4 v[52:55], v1, s[36:39], s80 offen sc1\n"
    "v_cvt_pk_bf16_f32 v63, v62, v63\n"
    "v_cvt_pk_bf16_f32 v62, v60, v61\n"
    "v_mov_b64 v[60:61], v[30:31]\n"
    "buffer_store_dwordx4 v[60:63], v1, s[36:39], s80 offen offset:1024 sc1\n"
    "v_cvt_pk_bf16_f32 v72, v72, v73\n"
    "v_cvt_pk_bf16_f32 v73, v74, v75\n"
    "v_cvt_pk_bf16_f32 v74, v76, v77\n"
    "v_cvt_pk_bf16_f32 v75, v78, v79\n"
    "buffer_store_dwordx4 v[72:75], v1, s[36:39], s73 offen offset:1024 sc1\n"
    "v_mov_b32 v12, s68\n"
    "s_mov_b32 s73, 0x80000000\n"
    "s_mov_b32 s79, 0x80000000\n"
    "s_mov_b32 s80, 0x80000000\n"
    "s_mov_b32 s68, s71\n"
    "s_mov_b32 s69, 0\n"
    "s_mov_b32 s70, 0x80000000\n"
    "v_mov_b32 v7, v12\n"
    "s_waitcnt vmcnt(9)\n"
    "s_add_u32 s71, s68, 1\n"
    "v_add_u32 v13, v11, v3\n"
    "v_cmp_gt_i32 vcc, s71, v13\n"
    "s_nop 0\n"
    "s_or_b64 exec, vcc, s[62:63]\n"
    "buffer_atomic_smax v11, v2, s[36:39], 0 offen sc0 sc1\n"
    "s_mov_b64 exec, -1\n"
    "s_waitcnt vmcnt(5)\n"
    "s_branch .LgP_bar0_%=\n"
    ".LgP_dma1_102_%=:\n"
    "v_mfma_f32_16x16x32_bf16 v[40:43], v[232:235], v[24:27], v[40:43]\n"
    "s_add_u32 m0, s59, 0\n"
    "v_mfma_f32_16x16x32_bf16 v[44:47], v[236:239], v[24:27], v[44:47]\n"
    "buffer_load_dwordx4 v1, s[36:39], s76 offen sc1 lds\n"
    "v_mfma_f32_16x16x32_bf16 v[52:55], v[24:27], v[240:243], v[52:55]\n"
    "s_add_u32 s76, s76, 16384\n"
    "s_add_u32 m0, s59, 4096\n"
    "v_mfma_f32_16x16x32_bf16 v[60:63], v[24:27], v[244:247], v[60:63]\n"
    "buffer_load_dwordx4 v1, s[36:39], s76 offen sc1 lds\n"
    "v_mfma_f32_16x16x32_bf16 v[72:75], v[248:251], v[24:27], v[72:75]\n"
    "v_mfma_f32_16x16x32_bf16 v[76:79], v[252:255], v[24:27], v[76:79]\n"
    "v_cvt_pk_bf16_f32 v40, v40, v41\n"
    "v_cvt_pk_bf16_f32 v41, v42, v43\n"
    "v_cvt_pk_bf16_f32 v42, v44, v45\n"
    "v_cvt_pk_bf16_f32 v43, v46, v47\n"
    "buffer_store_dwordx4 v[40:43], v1, s[36:39], s79 offen offset:1024 sc1\n"
    "v_cvt_pk_bf16_f32 v55, v54, v55\n"
    "v_cvt_pk_bf16_f32 v54, v52, v53\n"
    "v_mov_b64 v[52:53], v[28:29]\n"
    "buffer_store_dwordx4 v[52:55], v1, s[36:39], s80 offen sc1\n"
    "v_cvt_pk_bf16_f32 v63, v62, v63\n"
    "v_cvt_pk_bf16_f32 v62, v60, v61\n"
    "v_mov_b64 v[60:61], v[30:31]\n"
    "buffer_store_dwordx4 v[60:63], v1, s[36:39], s80 offen offset:1024 sc1\n"
    "v_cvt_pk_bf16_f32 v72, v72, v73\n"
    "v_cvt_pk_bf16_f32 v73, v74, v75\n"
    "v_cvt_pk_bf16_f32 v74, v76, v77\n"
    "v_cvt_pk_bf16_f32 v75, v78, v79\n"
    "buffer_store_dwordx4 v[72:75], v1, s[36:39], s73 offen offset:1024 sc1\n"
    "v_mov_b32 v12, s68\n"
    "s_mov_b32 s73, 0x80000000\n"
    "s_mov_b32 s79, 0x80000000\n"
    "s_mov_b32 s80, 0x80000000\n"
    "s_mov_b32 s68, s71\n"
    "s_add_u32 m0, s59, 8192\n"
    "s_add_u32 s76, s76, 16384\n"
    "buffer_load_dwordx4 v1, s[36:39], s76 offen sc1 lds\n"
    "s_add_u32 m0, s59, 12288\n"
    "s_add_u32 s76, s76, 16384\n"
    "buffer_load_dwordx4 v1, s[36:39], s76 offen sc1 lds\n"
    "s_add_u32 m0, s59, 16384\n"
    "s_add_u32 s76, s76, 16384\n"
    "buffer_load_dwordx4 v8, s[36:39], s76 offen sc1 lds\n"
    "s_waitcnt vmcnt(3)\n"
    "buffer_atomic_smax v12, v10, s[36:39], 0 offen sc1\n"
    "s_mov_b32 s69, 0x80000000\n"
    "s_mov_b32 s70, 0x80000000\n"
    "v_mov_b32 v7, v12\n"
    "s_waitcnt vmcnt(10)\n"
    "s_add_u32 s71, s68, 1\n"
    "v_add_u32 v13, v11, v3\n"
    "v_cmp_gt_i32 vcc, s71, v13\n"
    "s_nop 0\n"
    "s_or_b64 exec, vcc, s[62:63]\n"
    "buffer_atomic_smax v11, v2, s[36:39], 0 offen sc0 sc1\n"
    "s_mov_b64 exec, -1\n"
    "s_waitcnt vmcnt(2)\n"
    "s_branch .LgP_bar0_%=\n"
    ".LgP_idle1_%=:\n"
    "s_or_b64 exec, vcc, s[62:63]\n"
    "buffer_atomic_smax v11, v2, s[36:39], 0 offen sc0 sc1\n"
    "s_mov_b64 exec, -1\n"
    "s_waitcnt vmcnt(1)\n"
    "buffer_atomic_smax v12, v10, s[36:39], 0 offen sc1\n"
    "s_branch .LgP_block1_0_%=\n"
    ".LgP_block1_%=:\n"
    "v_add_u32 v13, v11, v3\n"
    "v_cmp_gt_i32 vcc, s71, v13\n"
    "s_nop 0\n"
    "s_or_b64 exec, vcc, s[62:63]\n"
    "buffer_atomic_smax v11, v2, s[36:39], 0 offen sc0 sc1\n"
    "s_mov_b64 exec, -1\n"
    "s_sleep 3\n"
    "s_waitcnt vmcnt(1)\n"
    ".LgP_block1_0_%=:\n"
    "v_add_u32 v13, v5, v3\n"
    "v_cmp_gt_i32 vcc, s71, v13\n"
    "s_cbranch_vccz .LgP_go1_%=\n"
    "v_add_u32 v13, v5, v3\n"
    "v_cmp_gt_i32 vcc, s71, v13\n"
    "s_nop 0\n"
    "s_or_b64 exec, vcc, s[62:63]\n"
    "buffer_atomic_smax v5, v2, s[36:39], 0 offen sc0 sc1\n"
    "s_mov_b64 exec, -1\n"
    "s_sleep 3\n"
    "s_waitcnt vmcnt(1)\n"
    ".LgP_block1_1_%=:\n"
    "v_add_u32 v13, v11, v3\n"
    "v_cmp_gt_i32 vcc, s71, v13\n"
    "s_cbranch_vccz .LgP_go1_%=\n"
    "s_sub_u32 s72, s72, 1\n"
    "s_cbranch_scc0 .LgP_block1_%=\n"
    "s_mov_b32 s72, 0\n"
    ".LgP_go1_%=:\n"
    "s_lshr_b32 s76, s71, 1\n"
    "s_and_b32 s76, s76, s54\n"
    "s_mul_i32 s76, s76, s53\n"
    "s_add_u32 s76, s76, s52\n"
    "s_add_u32 m0, s59, 0\n"
    "s_nop 0\n"
    "buffer_load_dwordx4 v1, s[36:39], s76 offen sc1 lds\n"
    "s_add_u32 m0, s59, 4096\n"
    "s_add_u32 s76, s76, 16384\n"
    "buffer_load_dwordx4 v1, s[36:39], s76 offen sc1 lds\n"
    "s_add_u32 m0, s59, 8192\n"
    "s_add_u32 s76, s76, 16384\n"
    "buffer_load_dwordx4 v1, s[36:39], s76 offen sc1 lds\n"
    "s_add_u32 m0, s59, 12288\n"
    "s_add_u32 s76, s76, 16384\n"
    "buffer_load_dwordx4 v1, s[36:39], s76 offen sc1 lds\n"
    "s_add_u32 m0, s59, 16384\n"
    "s_add_u32 s76, s76, 16384\n"
    "buffer_load_dwordx4 v8, s[36:39], s76 offen sc1 lds\n"
    "s_mov_b32 s69, 0x80000000\n"
    "s_mov_b32 s70, 0x80000000\n"
    "v_mov_b32 v7, v12\n"
    "s_waitcnt vmcnt(5)\n"
    "s_add_u32 s71, s68, 1\n"
    "v_add_u32 v13, v11, v3\n"
    "v_cmp_gt_i32 vcc, s71, v13\n"
    "s_nop 0\n"
    "s_or_b64 exec, vcc, s[62:63]\n"
    "buffer_atomic_smax v11, v2, s[36:39], 0 offen sc0 sc1\n"
    "s_mov_b64 exec, -1\n"
    "s_waitcnt vmcnt(1)\n"
    "s_branch .LgP_bar0_%=\n"
    ".LgP_bar1_%=:\n"
    "s_barrier\n"
    "ds_read_b128 v[16:19], v9 offset:0\n"
    "ds_read_b128 v[20:23], v9 offset:1024\n"
    "ds_read_b128 v[24:27], v9 offset:2048\n"
    "v_add_u32 v13, v5, v3\n"
    "v_cmp_gt_i32 vcc, s71, v13\n"
    "s_nop 0\n"
    "s_or_b64 exec, vcc, s[62:63]\n"
    "buffer_atomic_smax v5, v2, s[36:39], 0 offen sc0 sc1\n"
    "s_mov_b64 exec, -1\n"
    "s_lshr_b32 s76, s71, 1\n"
    "s_and_b32 s76, s76, s54\n"
    "s_mul_i32 s76, s76, s53\n"
    "s_add_u32 s76, s76, s52\n"
    "s_waitcnt lgkmcnt(2)\n"
    "s_branch .LgP_head1_%=\n"
    ".LgP_FIN_%=:\n"
  );
}
DEVI void role_up(const Params& p, int L, int j, int i, u32 dbg, unsigned long long tag) {
  const int lane = threadIdx.x & 63, w = threadIdx.x >> 6, col = (6 * j + i) * 192;
  const u32 wb = 576u * 4608 * 2 - col * 2;
  GEMM_DECL;
  g_w0 = make_srd(p.w1 + col, wb); g_w1 = make_srd(p.w3 + col, wb); g_w2 = g_w1;
  g_ldw = 9216;
  g_in_base = pgb(L, B_XN2); g_in_stride = SLOT; g_in_mask = RX - 1;
  g_out_base = pgb(L, B_H + 9 * j); g_out_kb = 12 * i; g_out_mask = RH - 1;
  g_flag = lane < 3 ? mbox(L, j, SL_DOWN + lane, 4 * i + w) : OOB;
  g_n0 = 64; g_n1 = 0;
  g_paddr = mbox(L, j, SL_UP + i, lane);
  g_pbias = lane < 16 ? lane >> 3 : (lane < 19 ? 2 * RH : 1000);
  v16u sa, sb, va;
  gemm_args(GEMM_ARGS, p, dbg, tag, sa, sb, va);
  RUN_ASM(
    "s_nop 4\n"
    "s_getpc_b64 s[84:85]\n"
    ".LgUP_PC_%=:\n"
    "s_and_b32 s85, s85, 0xffff\n"
    "s_mov_b32 s86, .LgUP_FIN_%=-.LgUP_PC_%=\n"
    "s_mov_b32 s87, 0x20000\n"
    "v_mbcnt_lo_u32_b32 v40, -1, 0\n"
    "v_mbcnt_hi_u32_b32 v40, -1, v40\n"
    "v_lshlrev_b32 v40, 7, v40\n"
    "buffer_load_dword v41, v40, s[84:87], 0 offen\n"
    "v_add_u32 v40, 8192, v40\n"
    "buffer_load_dword v41, v40, s[84:87], 0 offen\n"
    "v_add_u32 v40, 8192, v40\n"
    "buffer_load_dword v41, v40, s[84:87], 0 offen\n"
    "s_mov_b32 s68, 0\n"
    "s_mov_b32 s71, 0\n"
    "v_add_u32 v9, 20480, v0\n"
    "s_mov_b32 s64, 0xbfb8aa3b\n"
    "s_mov_b32 s72, 262144\n"
    "s_add_u32 m0, s59, 0\n"
    "s_mov_b32 s74, 0\n"
    "buffer_load_dwordx4 v5, s[40:43], s74 offen lds\n"
    "s_add_u32 m0, s59, 4096\n"
    "s_mov_b32 s74, 128\n"
    "buffer_load_dwordx4 v5, s[40:43], s74 offen lds\n"
    "s_add_u32 m0, s59, 8192\n"
    "s_mov_b32 s74, 256\n"
    "buffer_load_dwordx4 v5, s[40:43], s74 offen lds\n"
    "s_add_u32 m0, s59, 12288\n"
    "s_mov_b32 s74, 0\n"
    "buffer_load_dwordx4 v5, s[44:47], s74 offen lds\n"
    "s_add_u32 m0, s59, 16384\n"
    "s_mov_b32 s74, 128\n"
    "buffer_load_dwordx4 v5, s[44:47], s74 offen lds\n"
    "s_add_u32 m0, s59, 20480\n"
    "s_mov_b32 s74, 256\n"
    "buffer_load_dwordx4 v5, s[44:47], s74 offen lds\n"
    "s_add_u32 m0, s59, 24576\n"
    "s_mov_b32 s74, 294912\n"
    "buffer_load_dwordx4 v5, s[40:43], s74 offen lds\n"
    "s_add_u32 m0, s59, 28672\n"
    "s_mov_b32 s74, 295040\n"
    "buffer_load_dwordx4 v5, s[40:43], s74 offen lds\n"
    "s_add_u32 m0, s59, 32768\n"
    "s_mov_b32 s74, 295168\n"
    "buffer_load_dwordx4 v5, s[40:43], s74 offen lds\n"
    "s_add_u32 m0, s59, 36864\n"
    "s_mov_b32 s74, 294912\n"
    "buffer_load_dwordx4 v5, s[44:47], s74 offen lds\n"
    "s_add_u32 m0, s59, 40960\n"
    "s_mov_b32 s74, 295040\n"
    "buffer_load_dwordx4 v5, s[44:47], s74 offen lds\n"
    "s_add_u32 m0, s59, 45056\n"
    "s_mov_b32 s74, 295168\n"
    "buffer_load_dwordx4 v5, s[44:47], s74 offen lds\n"
    "s_add_u32 m0, s59, 49152\n"
    "s_mov_b32 s74, 589824\n"
    "buffer_load_dwordx4 v5, s[40:43], s74 offen lds\n"
    "s_add_u32 m0, s59, 53248\n"
    "s_mov_b32 s74, 589952\n"
    "buffer_load_dwordx4 v5, s[40:43], s74 offen lds\n"
    "s_add_u32 m0, s59, 57344\n"
    "s_mov_b32 s74, 590080\n"
    "buffer_load_dwordx4 v5, s[40:43], s74 offen lds\n"
    "s_add_u32 m0, s59, 61440\n"
    "s_mov_b32 s74, 589824\n"
    "buffer_load_dwordx4 v5, s[44:47], s74 offen lds\n"
    "s_add_u32 m0, s59, 65536\n"
    "s_mov_b32 s74, 589952\n"
    "buffer_load_dwordx4 v5, s[44:47], s74 offen lds\n"
    "s_add_u32 m0, s59, 69632\n"
    "s_mov_b32 s74, 590080\n"
    "buffer_load_dwordx4 v5, s[44:47], s74 offen lds\n"
    "s_add_u32 m0, s59, 73728\n"
    "s_mov_b32 s74, 884736\n"
    "buffer_load_dwordx4 v5, s[40:43], s74 offen lds\n"
    "s_add_u32 m0, s59, 77824\n"
    "s_mov_b32 s74, 884864\n"
    "buffer_load_dwordx4 v5, s[40:43], s74 offen lds\n"
    "s_add_u32 m0, s59, 81920\n"
    "s_mov_b32 s74, 884992\n"
    "buffer_load_dwordx4 v5, s[40:43], s74 offen lds\n"
    "s_add_u32 m0, s59, 86016\n"
    "s_mov_b32 s74, 884736\n"
    "buffer_load_dwordx4 v5, s[44:47], s74 offen lds\n"
    "s_add_u32 m0, s59, 90112\n"
    "s_mov_b32 s74, 884864\n"
    "buffer_load_dwordx4 v5, s[44:47], s74 offen lds\n"
    "s_add_u32 m0, s59, 94208\n"
    "s_mov_b32 s74, 884992\n"
    "buffer_load_dwordx4 v5, s[44:47], s74 offen lds\n"
    "s_add_u32 m0, s59, 98304\n"
    "s_mov_b32 s74, 1179648\n"
    "buffer_load_dwordx4 v5, s[40:43], s74 offen lds\n"
    "s_add_u32 m0, s59, 102400\n"
    "s_mov_b32 s74, 1179776\n"
    "buffer_load_dwordx4 v5, s[40:43], s74 offen lds\n"
    "s_add_u32 m0, s59, 106496\n"
    "s_mov_b32 s74, 1179904\n"
    "buffer_load_dwordx4 v5, s[40:43], s74 offen lds\n"
    "s_add_u32 m0, s59, 110592\n"
    "s_mov_b32 s74, 1179648\n"
    "buffer_load_dwordx4 v5, s[44:47], s74 offen lds\n"
    "s_add_u32 m0, s59, 114688\n"
    "s_mov_b32 s74, 1179776\n"
    "buffer_load_dwordx4 v5, s[44:47], s74 offen lds\n"
    "s_add_u32 m0, s59, 118784\n"
    "s_mov_b32 s74, 1179904\n"
    "buffer_load_dwordx4 v5, s[44:47], s74 offen lds\n"
    "v_add_u32 v16, 49152, v6\n"
    "v_add_u32 v17, 98304, v6\n"
    "v_add_u32 v18, 49152, v7\n"
    "v_add_u32 v19, 98304, v7\n"
    "s_waitcnt vmcnt(24) lgkmcnt(0)\n"
    "s_barrier\n"
    "s_add_u32 m0, s59, 122880\n"
    "s_mov_b32 s74, 1474560\n"
    "buffer_load_dwordx4 v5, s[40:43], s74 offen lds\n"
    "s_add_u32 m0, s59, 126976\n"
    "s_mov_b32 s74, 1474688\n"
    "buffer_load_dwordx4 v5, s[40:43], s74 offen lds\n"
    "s_add_u32 m0, s59, 131072\n"
    "s_mov_b32 s74, 1474816\n"
    "buffer_load_dwordx4 v5, s[40:43], s74 offen lds\n"
    "s_add_u32 m0, s59, 135168\n"
    "s_mov_b32 s74, 1474560\n"
    "buffer_load_dwordx4 v5, s[44:47], s74 offen lds\n"
    "s_add_u32 m0, s59, 139264\n"
    "s_mov_b32 s74, 1474688\n"
    "buffer_load_dwordx4 v5, s[44:47], s74 offen lds\n"
    "s_add_u32 m0, s59, 143360\n"
    "s_mov_b32 s74, 1474816\n"
    "buffer_load_dwordx4 v5, s[44:47], s74 offen lds\n"
    "ds_read_b64_tr_b16 a[0:1], v6 offset:0\n"
    "ds_read_b64_tr_b16 a[2:3], v6 offset:512\n"
    "ds_read_b64_tr_b16 a[4:5], v6 offset:8\n"
    "ds_read_b64_tr_b16 a[6:7], v6 offset:520\n"
    "ds_read_b64_tr_b16 a[8:9], v7 offset:0\n"
    "ds_read_b64_tr_b16 a[10:11], v7 offset:512\n"
    "ds_read_b64_tr_b16 a[12:13], v6 offset:12288\n"
    "ds_read_b64_tr_b16 a[14:15], v6 offset:12800\n"
    "ds_read_b64_tr_b16 a[16:17], v6 offset:12296\n"
    "ds_read_b64_tr_b16 a[18:19], v6 offset:12808\n"
    "ds_read_b64_tr_b16 a[20:21], v7 offset:12288\n"
    "ds_read_b64_tr_b16 a[22:23], v7 offset:12800\n"
    "s_waitcnt vmcnt(24) lgkmcnt(0)\n"
    "s_barrier\n"
    "s_add_u32 m0, s59, 0\n"
    "s_mov_b32 s74, 1769472\n"
    "buffer_load_dwordx4 v5, s[40:43], s74 offen lds\n"
    "s_add_u32 m0, s59, 4096\n"
    "s_mov_b32 s74, 1769600\n"
    "buffer_load_dwordx4 v5, s[40:43], s74 offen lds\n"
    "s_add_u32 m0, s59, 8192\n"
    "s_mov_b32 s74, 1769728\n"
    "buffer_load_dwordx4 v5, s[40:43], s74 offen lds\n"
    "s_add_u32 m0, s59, 12288\n"
    "s_mov_b32 s74, 1769472\n"
    "buffer_load_dwordx4 v5, s[44:47], s74 offen lds\n"
    "s_add_u32 m0, s59, 16384\n"
    "s_mov_b32 s74, 1769600\n"
    "buffer_load_dwordx4 v5, s[44:47], s74 offen lds\n"
    "s_add_u32 m0, s59, 20480\n"
    "s_mov_b32 s74, 1769728\n"
    "buffer_load_dwordx4 v5, s[44:47], s74 offen lds\n"
    "ds_read_b64_tr_b16 a[24:25], v6 offset:24576\n"
    "ds_read_b64_tr_b16 a[26:27], v6 offset:25088\n"
    "ds_read_b64_tr_b16 a[28:29], v6 offset:24584\n"
    "ds_read_b64_tr_b16 a[30:31], v6 offset:25096\n"
    "ds_read_b64_tr_b16 a[32:33], v7 offset:24576\n"
    "ds_read_b64_tr_b16 a[34:35], v7 offset:25088\n"
    "ds_read_b64_tr_b16 a[36:37], v6 offset:36864\n"
    "ds_read_b64_tr_b16 a[38:39], v6 offset:37376\n"
    "ds_read_b64_tr_b16 a[40:41], v6 offset:36872\n"
    "ds_read_b64_tr_b16 a[42:43], v6 offset:37384\n"
    "ds_read_b64_tr_b16 a[44:45], v7 offset:36864\n"
    "ds_read_b64_tr_b16 a[46:47], v7 offset:37376\n"
    "s_waitcnt vmcnt(24) lgkmcnt(0)\n"
    "s_barrier\n"
    "s_add_u32 m0, s59, 24576\n"
    "s_mov_b32 s74, 2064384\n"
    "buffer_load_dwordx4 v5, s[40:43], s74 offen lds\n"
    "s_add_u32 m0, s59, 28672\n"
    "s_mov_b32 s74, 2064512\n"
    "buffer_load_dwordx4 v5, s[40:43], s74 offen lds\n"
    "s_add_u32 m0, s59, 32768\n"
    "s_mov_b32 s74, 2064640\n"
    "buffer_load_dwordx4 v5, s[40:43], s74 offen lds\n"
    "s_add_u32 m0, s59, 36864\n"
    "s_mov_b32 s74, 2064384\n"
    "buffer_load_dwordx4 v5, s[44:47], s74 offen lds\n"
    "s_add_u32 m0, s59, 40960\n"
    "s_mov_b32 s74, 2064512\n"
    "buffer_load_dwordx4 v5, s[44:47], s74 offen lds\n"
    "s_add_u32 m0, s59, 45056\n"
    "s_mov_b32 s74, 2064640\n"
    "buffer_load_dwordx4 v5, s[44:47], s74 offen lds\n"
    "ds_read_b64_tr_b16 a[48:49], v16 offset:0\n"
    "ds_read_b64_tr_b16 a[50:51], v16 offset:512\n"
    "ds_read_b64_tr_b16 a[52:53], v16 offset:8\n"
    "ds_read_b64_tr_b16 a[54:55], v16 offset:520\n"
    "ds_read_b64_tr_b16 a[56:57], v18 offset:0\n"
    "ds_read_b64_tr_b16 a[58:59], v18 offset:512\n"
    "ds_read_b64_tr_b16 a[60:61], v16 offset:12288\n"
    "ds_read_b64_tr_b16 a[62:63], v16 offset:12800\n"
    "ds_read_b64_tr_b16 a[64:65], v16 offset:12296\n"
    "ds_read_b64_tr_b16 a[66:67], v16 offset:12808\n"
    "ds_read_b64_tr_b16 a[68:69], v18 offset:12288\n"
    "ds_read_b64_tr_b16 a[70:71], v18 offset:12800\n"
    "s_waitcnt vmcnt(24) lgkmcnt(0)\n"
    "s_barrier\n"
    "s_add_u32 m0, s59, 49152\n"
    "s_mov_b32 s74, 2359296\n"
    "buffer_load_dwordx4 v5, s[40:43], s74 offen lds\n"
    "s_add_u32 m0, s59, 53248\n"
    "s_mov_b32 s74, 2359424\n"
    "buffer_load_dwordx4 v5, s[40:43], s74 offen lds\n"
    "s_add_u32 m0, s59, 57344\n"
    "s_mov_b32 s74, 2359552\n"
    "buffer_load_dwordx4 v5, s[40:43], s74 offen lds\n"
    "s_add_u32 m0, s59, 61440\n"
    "s_mov_b32 s74, 2359296\n"
    "buffer_load_dwordx4 v5, s[44:47], s74 offen lds\n"
    "s_add_u32 m0, s59, 65536\n"
    "s_mov_b32 s74, 2359424\n"
    "buffer_load_dwordx4 v5, s[44:47], s74 offen lds\n"
    "s_add_u32 m0, s59, 69632\n"
    "s_mov_b32 s74, 2359552\n"
    "buffer_load_dwordx4 v5, s[44:47], s74 offen lds\n"
    "ds_read_b64_tr_b16 a[72:73], v16 offset:24576\n"
    "ds_read_b64_tr_b16 a[74:75], v16 offset:25088\n"
    "ds_read_b64_tr_b16 a[76:77], v16 offset:24584\n"
    "ds_read_b64_tr_b16 a[78:79], v16 offset:25096\n"
    "ds_read_b64_tr_b16 a[80:81], v18 offset:24576\n"
    "ds_read_b64_tr_b16 a[82:83], v18 offset:25088\n"
    "ds_read_b64_tr_b16 a[84:85], v16 offset:36864\n"
    "ds_read_b64_tr_b16 a[86:87], v16 offset:37376\n"
    "ds_read_b64_tr_b16 a[88:89], v16 offset:36872\n"
    "ds_read_b64_tr_b16 a[90:91], v16 offset:37384\n"
    "ds_read_b64_tr_b16 a[92:93], v18 offset:36864\n"
    "ds_read_b64_tr_b16 a[94:95], v18 offset:37376\n"
    "s_waitcnt vmcnt(24) lgkmcnt(0)\n"
    "s_barrier\n"
    "s_add_u32 m0, s59, 73728\n"
    "s_mov_b32 s74, 2654208\n"
    "buffer_load_dwordx4 v5, s[40:43], s74 offen lds\n"
    "s_add_u32 m0, s59, 77824\n"
    "s_mov_b32 s74, 2654336\n"
    "buffer_load_dwordx4 v5, s[40:43], s74 offen lds\n"
    "s_add_u32 m0, s59, 81920\n"
    "s_mov_b32 s74, 2654464\n"
    "buffer_load_dwordx4 v5, s[40:43], s74 offen lds\n"
    "s_add_u32 m0, s59, 86016\n"
    "s_mov_b32 s74, 2654208\n"
    "buffer_load_dwordx4 v5, s[44:47], s74 offen lds\n"
    "s_add_u32 m0, s59, 90112\n"
    "s_mov_b32 s74, 2654336\n"
    "buffer_load_dwordx4 v5, s[44:47], s74 offen lds\n"
    "s_add_u32 m0, s59, 94208\n"
    "s_mov_b32 s74, 2654464\n"
    "buffer_load_dwordx4 v5, s[44:47], s74 offen lds\n"
    "ds_read_b64_tr_b16 a[96:97], v17 offset:0\n"
    "ds_read_b64_tr_b16 a[98:99], v17 offset:512\n"
    "ds_read_b64_tr_b16 a[100:101], v17 offset:8\n"
    "ds_read_b64_tr_b16 a[102:103], v17 offset:520\n"
    "ds_read_b64_tr_b16 a[104:105], v19 offset:0\n"
    "ds_read_b64_tr_b16 a[106:107], v19 offset:512\n"
    "ds_read_b64_tr_b16 a[108:109], v17 offset:12288\n"
    "ds_read_b64_tr_b16 a[110:111], v17 offset:12800\n"
    "ds_read_b64_tr_b16 a[112:113], v17 offset:12296\n"
    "ds_read_b64_tr_b16 a[114:115], v17 offset:12808\n"
    "ds_read_b64_tr_b16 a[116:117], v19 offset:12288\n"
    "ds_read_b64_tr_b16 a[118:119], v19 offset:12800\n"
    "s_waitcnt vmcnt(24) lgkmcnt(0)\n"
    "s_barrier\n"
    "s_add_u32 m0, s59, 98304\n"
    "s_mov_b32 s74, 2949120\n"
    "buffer_load_dwordx4 v5, s[40:43], s74 offen lds\n"
    "s_add_u32 m0, s59, 102400\n"
    "s_mov_b32 s74, 2949248\n"
    "buffer_load_dwordx4 v5, s[40:43], s74 offen lds\n"
    "s_add_u32 m0, s59, 106496\n"
    "s_mov_b32 s74, 2949376\n"
    "buffer_load_dwordx4 v5, s[40:43], s74 offen lds\n"
    "s_add_u32 m0, s59, 110592\n"
    "s_mov_b32 s74, 2949120\n"
    "buffer_load_dwordx4 v5, s[44:47], s74 offen lds\n"
    "s_add_u32 m0, s59, 114688\n"
    "s_mov_b32 s74, 2949248\n"
    "buffer_load_dwordx4 v5, s[44:47], s74 offen lds\n"
    "s_add_u32 m0, s59, 118784\n"
    "s_mov_b32 s74, 2949376\n"
    "buffer_load_dwordx4 v5, s[44:47], s74 offen lds\n"
    "ds_read_b64_tr_b16 a[120:121], v17 offset:24576\n"
    "ds_read_b64_tr_b16 a[122:123], v17 offset:25088\n"
    "ds_read_b64_tr_b16 a[124:125], v17 offset:24584\n"
    "ds_read_b64_tr_b16 a[126:127], v17 offset:25096\n"
    "ds_read_b64_tr_b16 a[128:129], v19 offset:24576\n"
    "ds_read_b64_tr_b16 a[130:131], v19 offset:25088\n"
    "ds_read_b64_tr_b16 a[132:133], v17 offset:36864\n"
    "ds_read_b64_tr_b16 a[134:135], v17 offset:37376\n"
    "ds_read_b64_tr_b16 a[136:137], v17 offset:36872\n"
    "ds_read_b64_tr_b16 a[138:139], v17 offset:37384\n"
    "ds_read_b64_tr_b16 a[140:141], v19 offset:36864\n"
    "ds_read_b64_tr_b16 a[142:143], v19 offset:37376\n"
    "s_waitcnt vmcnt(24) lgkmcnt(0)\n"
    "s_barrier\n"
    "s_add_u32 m0, s59, 122880\n"
    "s_mov_b32 s74, 3244032\n"
    "buffer_load_dwordx4 v5, s[40:43], s74 offen lds\n"
    "s_add_u32 m0, s59, 126976\n"
    "s_mov_b32 s74, 3244160\n"
    "buffer_load_dwordx4 v5, s[40:43], s74 offen lds\n"
    "s_add_u32 m0, s59, 131072\n"
    "s_mov_b32 s74, 3244288\n"
    "buffer_load_dwordx4 v5, s[40:43], s74 offen lds\n"
    "s_add_u32 m0, s59, 135168\n"
    "s_mov_b32 s74, 3244032\n"
    "buffer_load_dwordx4 v5, s[44:47], s74 offen lds\n"
    "s_add_u32 m0, s59, 139264\n"
    "s_mov_b32 s74, 3244160\n"
    "buffer_load_dwordx4 v5, s[44:47], s74 offen lds\n"
    "s_add_u32 m0, s59, 143360\n"
    "s_mov_b32 s74, 3244288\n"
    "buffer_load_dwordx4 v5, s[44:47], s74 offen lds\n"
    "ds_read_b64_tr_b16 a[144:145], v6 offset:0\n"
    "ds_read_b64_tr_b16 a[146:147], v6 offset:512\n"
    "ds_read_b64_tr_b16 a[148:149], v6 offset:8\n"
    "ds_read_b64_tr_b16 a[150:151], v6 offset:520\n"
    "ds_read_b64_tr_b16 a[152:153], v7 offset:0\n"
    "ds_read_b64_tr_b16 a[154:155], v7 offset:512\n"
    "ds_read_b64_tr_b16 a[156:157], v6 offset:12288\n"
    "ds_read_b64_tr_b16 a[158:159], v6 offset:12800\n"
    "ds_read_b64_tr_b16 a[160:161], v6 offset:12296\n"
    "ds_read_b64_tr_b16 a[162:163], v6 offset:12808\n"
    "ds_read_b64_tr_b16 a[164:165], v7 offset:12288\n"
    "ds_read_b64_tr_b16 a[166:167], v7 offset:12800\n"
    "s_waitcnt vmcnt(24) lgkmcnt(0)\n"
    "s_barrier\n"
    "s_add_u32 m0, s59, 0\n"
    "s_mov_b32 s74, 3538944\n"
    "buffer_load_dwordx4 v5, s[40:43], s74 offen lds\n"
    "s_add_u32 m0, s59, 4096\n"
    "s_mov_b32 s74, 3539072\n"
    "buffer_load_dwordx4 v5, s[40:43], s74 offen lds\n"
    "s_add_u32 m0, s59, 8192\n"
    "s_mov_b32 s74, 3539200\n"
    "buffer_load_dwordx4 v5, s[40:43], s74 offen lds\n"
    "s_add_u32 m0, s59, 12288\n"
    "s_mov_b32 s74, 3538944\n"
    "buffer_load_dwordx4 v5, s[44:47], s74 offen lds\n"
    "s_add_u32 m0, s59, 16384\n"
    "s_mov_b32 s74, 3539072\n"
    "buffer_load_dwordx4 v5, s[44:47], s74 offen lds\n"
    "s_add_u32 m0, s59, 20480\n"
    "s_mov_b32 s74, 3539200\n"
    "buffer_load_dwordx4 v5, s[44:47], s74 offen lds\n"
    "ds_read_b64_tr_b16 a[168:169], v6 offset:24576\n"
    "ds_read_b64_tr_b16 a[170:171], v6 offset:25088\n"
    "ds_read_b64_tr_b16 a[172:173], v6 offset:24584\n"
    "ds_read_b64_tr_b16 a[174:175], v6 offset:25096\n"
    "ds_read_b64_tr_b16 a[176:177], v7 offset:24576\n"
    "ds_read_b64_tr_b16 a[178:179], v7 offset:25088\n"
    "ds_read_b64_tr_b16 a[180:181], v6 offset:36864\n"
    "ds_read_b64_tr_b16 a[182:183], v6 offset:37376\n"
    "ds_read_b64_tr_b16 a[184:185], v6 offset:36872\n"
    "ds_read_b64_tr_b16 a[186:187], v6 offset:37384\n"
    "ds_read_b64_tr_b16 a[188:189], v7 offset:36864\n"
    "ds_read_b64_tr_b16 a[190:191], v7 offset:37376\n"
    "s_waitcnt vmcnt(24) lgkmcnt(0)\n"
    "s_barrier\n"
    "s_add_u32 m0, s59, 24576\n"
    "s_mov_b32 s74, 3833856\n"
    "buffer_load_dwordx4 v5, s[40:43], s74 offen lds\n"
    "s_add_u32 m0, s59, 28672\n"
    "s_mov_b32 s74, 3833984\n"
    "buffer_load_dwordx4 v5, s[40:43], s74 offen lds\n"
    "s_add_u32 m0, s59, 32768\n"
    "s_mov_b32 s74, 3834112\n"
    "buffer_load_dwordx4 v5, s[40:43], s74 offen lds\n"
    "s_add_u32 m0, s59, 36864\n"
    "s_mov_b32 s74, 3833856\n"
    "buffer_load_dwordx4 v5, s[44:47], s74 offen lds\n"
    "s_add_u32 m0, s59, 40960\n"
    "s_mov_b32 s74, 3833984\n"
    "buffer_load_dwordx4 v5, s[44:47], s74 offen lds\n"
    "s_add_u32 m0, s59, 45056\n"
    "s_mov_b32 s74, 3834112\n"
    "buffer_load_dwordx4 v5, s[44:47], s74 offen lds\n"
    "ds_read_b64_tr_b16 a[192:193], v16 offset:0\n"
    "ds_read_b64_tr_b16 a[194:195], v16 offset:512\n"
    "ds_read_b64_tr_b16 a[196:197], v16 offset:8\n"
    "ds_read_b64_tr_b16 a[198:199], v16 offset:520\n"
    "ds_read_b64_tr_b16 a[200:201], v18 offset:0\n"
    "ds_read_b64_tr_b16 a[202:203], v18 offset:512\n"
    "ds_read_b64_tr_b16 a[204:205], v16 offset:12288\n"
    "ds_read_b64_tr_b16 a[206:207], v16 offset:12800\n"
    "ds_read_b64_tr_b16 a[208:209], v16 offset:12296\n"
    "ds_read_b64_tr_b16 a[210:211], v16 offset:12808\n"
    "ds_read_b64_tr_b16 a[212:213], v18 offset:12288\n"
    "ds_read_b64_tr_b16 a[214:215], v18 offset:12800\n"
    "s_waitcnt vmcnt(24) lgkmcnt(0)\n"
    "s_barrier\n"
    "s_add_u32 m0, s59, 49152\n"
    "s_mov_b32 s74, 4128768\n"
    "buffer_load_dwordx4 v5, s[40:43], s74 offen lds\n"
    "s_add_u32 m0, s59, 53248\n"
    "s_mov_b32 s74, 4128896\n"
    "buffer_load_dwordx4 v5, s[40:43], s74 offen lds\n"
    "s_add_u32 m0, s59, 57344\n"
    "s_mov_b32 s74, 4129024\n"
    "buffer_load_dwordx4 v5, s[40:43], s74 offen lds\n"
    "s_add_u32 m0, s59, 61440\n"
    "s_mov_b32 s74, 4128768\n"
    "buffer_load_dwordx4 v5, s[44:47], s74 offen lds\n"
    "s_add_u32 m0, s59, 65536\n"
    "s_mov_b32 s74, 4128896\n"
    "buffer_load_dwordx4 v5, s[44:47], s74 offen lds\n"
    "s_add_u32 m0, s59, 69632\n"
    "s_mov_b32 s74, 4129024\n"
    "buffer_load_dwordx4 v5, s[44:47], s74 offen lds\n"
    "ds_read_b64_tr_b16 a[216:217], v16 offset:24576\n"
    "ds_read_b64_tr_b16 a[218:219], v16 offset:25088\n"
    "ds_read_b64_tr_b16 a[220:221], v16 offset:24584\n"
    "ds_read_b64_tr_b16 a[222:223], v16 offset:25096\n"
    "ds_read_b64_tr_b16 a[224:225], v18 offset:24576\n"
    "ds_read_b64_tr_b16 a[226:227], v18 offset:25088\n"
    "ds_read_b64_tr_b16 a[228:229], v16 offset:36864\n"
    "ds_read_b64_tr_b16 a[230:231], v16 offset:37376\n"
    "ds_read_b64_tr_b16 a[232:233], v16 offset:36872\n"
    "ds_read_b64_tr_b16 a[234:235], v16 offset:37384\n"
    "ds_read_b64_tr_b16 a[236:237], v18 offset:36864\n"
    "ds_read_b64_tr_b16 a[238:239], v18 offset:37376\n"
    "s_waitcnt vmcnt(24) lgkmcnt(0)\n"
    "s_barrier\n"
    "s_add_u32 m0, s59, 73728\n"
    "s_mov_b32 s74, 4423680\n"
    "buffer_load_dwordx4 v5, s[40:43], s74 offen lds\n"
    "s_add_u32 m0, s59, 77824\n"
    "s_mov_b32 s74, 4423808\n"
    "buffer_load_dwordx4 v5, s[40:43], s74 offen lds\n"
    "s_add_u32 m0, s59, 81920\n"
    "s_mov_b32 s74, 4423936\n"
    "buffer_load_dwordx4 v5, s[40:43], s74 offen lds\n"
    "s_add_u32 m0, s59, 86016\n"
    "s_mov_b32 s74, 4423680\n"
    "buffer_load_dwordx4 v5, s[44:47], s74 offen lds\n"
    "s_add_u32 m0, s59, 90112\n"
    "s_mov_b32 s74, 4423808\n"
    "buffer_load_dwordx4 v5, s[44:47], s74 offen lds\n"
    "s_add_u32 m0, s59, 94208\n"
    "s_mov_b32 s74, 4423936\n"
    "buffer_load_dwordx4 v5, s[44:47], s74 offen lds\n"
    "ds_read_b64_tr_b16 a[240:241], v17 offset:0\n"
    "ds_read_b64_tr_b16 a[242:243], v17 offset:512\n"
    "ds_read_b64_tr_b16 a[244:245], v17 offset:8\n"
    "ds_read_b64_tr_b16 a[246:247], v17 offset:520\n"
    "ds_read_b64_tr_b16 a[248:249], v19 offset:0\n"
    "ds_read_b64_tr_b16 a[250:251], v19 offset:512\n"
    "ds_read_b64_tr_b16 a[252:253], v17 offset:12288\n"
    "ds_read_b64_tr_b16 a[254:255], v17 offset:12800\n"
    "ds_read_b64_tr_b16 v[80:81], v17 offset:12296\n"
    "ds_read_b64_tr_b16 v[82:83], v17 offset:12808\n"
    "ds_read_b64_tr_b16 v[84:85], v19 offset:12288\n"
    "ds_read_b64_tr_b16 v[86:87], v19 offset:12800\n"
    "s_waitcnt vmcnt(24) lgkmcnt(0)\n"
    "s_barrier\n"
    "s_add_u32 m0, s59, 98304\n"
    "s_mov_b32 s74, 4718592\n"
    "buffer_load_dwordx4 v5, s[40:43], s74 offen lds\n"
    "s_add_u32 m0, s59, 102400\n"
    "s_mov_b32 s74, 4718720\n"
    "buffer_load_dwordx4 v5, s[40:43], s74 offen lds\n"
    "s_add_u32 m0, s59, 106496\n"
    "s_mov_b32 s74, 4718848\n"
    "buffer_load_dwordx4 v5, s[40:43], s74 offen lds\n"
    "s_add_u32 m0, s59, 110592\n"
    "s_mov_b32 s74, 4718592\n"
    "buffer_load_dwordx4 v5, s[44:47], s74 offen lds\n"
    "s_add_u32 m0, s59, 114688\n"
    "s_mov_b32 s74, 4718720\n"
    "buffer_load_dwordx4 v5, s[44:47], s74 offen lds\n"
    "s_add_u32 m0, s59, 118784\n"
    "s_mov_b32 s74, 4718848\n"
    "buffer_load_dwordx4 v5, s[44:47], s74 offen lds\n"
    "ds_read_b64_tr_b16 v[88:89], v17 offset:24576\n"
    "ds_read_b64_tr_b16 v[90:91], v17 offset:25088\n"
    "ds_read_b64_tr_b16 v[92:93], v17 offset:24584\n"
    "ds_read_b64_tr_b16 v[94:95], v17 offset:25096\n"
    "ds_read_b64_tr_b16 v[96:97], v19 offset:24576\n"
    "ds_read_b64_tr_b16 v[98:99], v19 offset:25088\n"
    "ds_read_b64_tr_b16 v[100:101], v17 offset:36864\n"
    "ds_read_b64_tr_b16 v[102:103], v17 offset:37376\n"
    "ds_read_b64_tr_b16 v[104:105], v17 offset:36872\n"
    "ds_read_b64_tr_b16 v[106:107], v17 offset:37384\n"
    "ds_read_b64_tr_b16 v[108:109], v19 offset:36864\n"
    "ds_read_b64_tr_b16 v[110:111], v19 offset:37376\n"
    "s_waitcnt vmcnt(24) lgkmcnt(0)\n"
    "s_barrier\n"
    "s_add_u32 m0, s59, 122880\n"
    "s_mov_b32 s74, 5013504\n"
    "buffer_load_dwordx4 v5, s[40:43], s74 offen lds\n"
    "s_add_u32 m0, s59, 126976\n"
    "s_mov_b32 s74, 5013632\n"
    "buffer_load_dwordx4 v5, s[40:43], s74 offen lds\n"
    "s_add_u32 m0, s59, 131072\n"
    "s_mov_b32 s74, 5013760\n"
    "buffer_load_dwordx4 v5, s[40:43], s74 offen lds\n"
    "s_add_u32 m0, s59, 135168\n"
    "s_mov_b32 s74, 5013504\n"
    "buffer_load_dwordx4 v5, s[44:47], s74 offen lds\n"
    "s_add_u32 m0, s59, 139264\n"
    "s_mov_b32 s74, 5013632\n"
    "buffer_load_dwordx4 v5, s[44:47], s74 offen lds\n"
    "s_add_u32 m0, s59, 143360\n"
    "s_mov_b32 s74, 5013760\n"
    "buffer_load_dwordx4 v5, s[44:47], s74 offen lds\n"
    "buffer_load_dwordx2 v[12:13], off, s[36:39], 0 sc1\n"
    "ds_read_b64_tr_b16 v[112:113], v6 offset:0\n"
    "ds_read_b64_tr_b16 v[114:115], v6 offset:512\n"
    "ds_read_b64_tr_b16 v[116:117], v6 offset:8\n"
    "ds_read_b64_tr_b16 v[118:119], v6 offset:520\n"
    "ds_read_b64_tr_b16 v[120:121], v7 offset:0\n"
    "ds_read_b64_tr_b16 v[122:123], v7 offset:512\n"
    "ds_read_b64_tr_b16 v[124:125], v6 offset:12288\n"
    "ds_read_b64_tr_b16 v[126:127], v6 offset:12800\n"
    "ds_read_b64_tr_b16 v[128:129], v6 offset:12296\n"
    "ds_read_b64_tr_b16 v[130:131], v6 offset:12808\n"
    "ds_read_b64_tr_b16 v[132:133], v7 offset:12288\n"
    "ds_read_b64_tr_b16 v[134:135], v7 offset:12800\n"
    "s_waitcnt vmcnt(25) lgkmcnt(0)\n"
    "s_barrier\n"
    "ds_read_b64_tr_b16 v[136:137], v6 offset:24576\n"
    "ds_read_b64_tr_b16 v[138:139], v6 offset:25088\n"
    "ds_read_b64_tr_b16 v[140:141], v6 offset:24584\n"
    "ds_read_b64_tr_b16 v[142:143], v6 offset:25096\n"
    "ds_read_b64_tr_b16 v[144:145], v7 offset:24576\n"
    "ds_read_b64_tr_b16 v[146:147], v7 offset:25088\n"
    "ds_read_b64_tr_b16 v[148:149], v6 offset:36864\n"
    "ds_read_b64_tr_b16 v[150:151], v6 offset:37376\n"
    "ds_read_b64_tr_b16 v[152:153], v6 offset:36872\n"
    "ds_read_b64_tr_b16 v[154:155], v6 offset:37384\n"
    "ds_read_b64_tr_b16 v[156:157], v7 offset:36864\n"
    "ds_read_b64_tr_b16 v[158:159], v7 offset:37376\n"
    "s_waitcnt vmcnt(19) lgkmcnt(0)\n"
    "s_barrier\n"
    "ds_read_b64_tr_b16 v[160:161], v16 offset:0\n"
    "ds_read_b64_tr_b16 v[162:163], v16 offset:512\n"
    "ds_read_b64_tr_b16 v[164:165], v16 offset:8\n"
    "ds_read_b64_tr_b16 v[166:167], v16 offset:520\n"
    "ds_read_b64_tr_b16 v[168:169], v18 offset:0\n"
    "ds_read_b64_tr_b16 v[170:171], v18 offset:512\n"
    "ds_read_b64_tr_b16 v[172:173], v16 offset:12288\n"
    "ds_read_b64_tr_b16 v[174:175], v16 offset:12800\n"
    "ds_read_b64_tr_b16 v[176:177], v16 offset:12296\n"
    "ds_read_b64_tr_b16 v[178:179], v16 offset:12808\n"
    "ds_read_b64_tr_b16 v[180:181], v18 offset:12288\n"
    "ds_read_b64_tr_b16 v[182:183], v18 offset:12800\n"
    "s_waitcnt vmcnt(13) lgkmcnt(0)\n"
    "s_barrier\n"
    "ds_read_b64_tr_b16 v[184:185], v16 offset:24576\n"
    "ds_read_b64_tr_b16 v[186:187], v16 offset:25088\n"
    "ds_read_b64_tr_b16 v[188:189], v16 offset:24584\n"
    "ds_read_b64_tr_b16 v[190:191], v16 offset:25096\n"
    "ds_read_b64_tr_b16 v[192:193], v18 offset:24576\n"
    "ds_read_b64_tr_b16 v[194:195], v18 offset:25088\n"
    "ds_read_b64_tr_b16 v[196:197], v16 offset:36864\n"
    "ds_read_b64_tr_b16 v[198:199], v16 offset:37376\n"
    "ds_read_b64_tr_b16 v[200:201], v16 offset:36872\n"
    "ds_read_b64_tr_b16 v[202:203], v16 offset:37384\n"
    "ds_read_b64_tr_b16 v[204:205], v18 offset:36864\n"
    "ds_read_b64_tr_b16 v[206:207], v18 offset:37376\n"
    "s_waitcnt vmcnt(7) lgkmcnt(0)\n"
    "s_barrier\n"
    "ds_read_b64_tr_b16 v[208:209], v17 offset:0\n"
    "ds_read_b64_tr_b16 v[210:211], v17 offset:512\n"
    "ds_read_b64_tr_b16 v[212:213], v17 offset:8\n"
    "ds_read_b64_tr_b16 v[214:215], v17 offset:520\n"
    "ds_read_b64_tr_b16 v[216:217], v19 offset:0\n"
    "ds_read_b64_tr_b16 v[218:219], v19 offset:512\n"
    "ds_read_b64_tr_b16 v[220:221], v17 offset:12288\n"
    "ds_read_b64_tr_b16 v[222:223], v17 offset:12800\n"
    "ds_read_b64_tr_b16 v[224:225], v17 offset:12296\n"
    "ds_read_b64_tr_b16 v[226:227], v17 offset:12808\n"
    "ds_read_b64_tr_b16 v[228:229], v19 offset:12288\n"
    "ds_read_b64_tr_b16 v[230:231], v19 offset:12800\n"
    "s_waitcnt vmcnt(0) lgkmcnt(0)\n"
    "s_barrier\n"
    ".LgUP_par_%=:\n"
    "v_readfirstlane_b32 s74, v12\n"
    "v_readfirstlane_b32 s75, v13\n"
    "s_xor_b32 s74, s74, s66\n"
    "s_xor_b32 s75, s75, s67\n"
    "s_cselect_b32 s75, 2, 0\n"
    "s_or_b32 s75, s75, s74\n"
    "s_cmp_lt_u32 s75, 2\n"
    "s_cbranch_scc1 .LgUP_par1_%=\n"
    "s_sleep 4\n"
    "buffer_load_dwordx2 v[12:13], off, s[36:39], 0 sc1\n"
    "s_waitcnt vmcnt(0)\n"
    "s_sub_u32 s72, s72, 1\n"
    "s_cbranch_scc0 .LgUP_par_%=\n"
    "s_mov_b32 s72, 0\n"
    ".LgUP_par1_%=:\n"
    "s_and_b32 s74, s74, 1\n"
    "s_lshl_b32 s74, s74, 13\n"
    "s_add_u32 s36, s36, s74\n"
    "s_addc_u32 s37, s37, 0\n"
    "v_add_u32 v13, v11, v3\n"
    "v_cmp_gt_i32 vcc, s71, v13\n"
    "s_nop 0\n"
    "s_or_b64 exec, vcc, s[62:63]\n"
    "buffer_atomic_smax v11, v2, s[36:39], 0 offen sc0 sc1\n"
    "s_mov_b64 exec, -1\n"
    "s_getpc_b64 s[84:85]\n"
    ".LgUP_PC2_%=:\n"
    "s_and_b32 s85, s85, 0xffff\n"
    "s_mov_b32 s86, .LgUP_FIN_%=-.LgUP_PC2_%=\n"
    "s_mov_b32 s87, 0x20000\n"
    "v_mbcnt_lo_u32_b32 v42, -1, 0\n"
    "v_mbcnt_hi_u32_b32 v42, -1, v42\n"
    "v_lshlrev_b32 v42, 7, v42\n"
    "buffer_load_dword v43, v42, s[84:87], 0 offen\n"
    "v_add_u32 v42, 8192, v42\n"
    "buffer_load_dword v43, v42, s[84:87], 0 offen\n"
    "v_add_u32 v42, 8192, v42\n"
    "buffer_load_dword v43, v42, s[84:87], 0 offen\n"
    "ds_read_b64_tr_b16 v[232:233], v17 offset:24576\n"
    "ds_read_b64_tr_b16 v[234:235], v17 offset:25088\n"
    "ds_read_b64_tr_b16 v[236:237], v17 offset:24584\n"
    "ds_read_b64_tr_b16 v[238:239], v17 offset:25096\n"
    "ds_read_b64_tr_b16 v[240:241], v19 offset:24576\n"
    "ds_read_b64_tr_b16 v[242:243], v19 offset:25088\n"
    "ds_read_b64_tr_b16 v[244:245], v17 offset:36864\n"
    "ds_read_b64_tr_b16 v[246:247], v17 offset:37376\n"
    "ds_read_b64_tr_b16 v[248:249], v17 offset:36872\n"
    "ds_read_b64_tr_b16 v[250:251], v17 offset:37384\n"
    "ds_read_b64_tr_b16 v[252:253], v19 offset:36864\n"
    "ds_read_b64_tr_b16 v[254:255], v19 offset:37376\n"
    "s_waitcnt lgkmcnt(0)\n"
    "s_barrier\n"
    "s_waitcnt vmcnt(0)\n"
    "v_mov_b32 v5, v11\n"
    "v_mov_b32 v12, -1\n"
    "s_mov_b32 s73, 0x80000000\n"
    "s_branch .LgUP_block1_1_%=\n"
    ".LgUP_LOOPB_%=:\n"
    ".LgUP_head0_%=:\n"
    "v_mfma_f32_16x16x32_bf16 v[32:35], a[0:3], v[16:19], 0\n"
    "v_mfma_f32_16x16x32_bf16 v[36:39], a[4:7], v[16:19], 0\n"
    "v_mul_f32 v64, v40, v64\n"
    "v_mul_f32 v65, v41, v65\n"
    "v_mfma_f32_16x16x32_bf16 v[48:51], a[8:11], v[16:19], 0\n"
    "v_mul_f32 v66, v42, v66\n"
    "v_mul_f32 v67, v43, v67\n"
    "v_mfma_f32_16x16x32_bf16 v[56:59], a[12:15], v[16:19], 0\n"
    "v_mul_f32 v68, v44, v68\n"
    "v_mul_f32 v69, v45, v69\n"
    "v_mfma_f32_16x16x32_bf16 v[60:63], a[16:19], v[16:19], 0\n"
    "s_waitcnt lgkmcnt(1)\n"
    "v_mul_f32 v70, v46, v70\n"
    "v_mul_f32 v71, v47, v71\n"
    "v_mfma_f32_16x16x32_bf16 v[72:75], a[20:23], v[16:19], 0\n"
    "ds_read_b128 v[16:19], v0 offset:3072\n"
    "v_mul_f32 v40, 0xbfb8aa3b, v40\n"
    "v_mfma_f32_16x16x32_bf16 v[32:35], a[24:27], v[20:23], v[32:35]\n"
    "v_exp_f32 v40, v40\n"
    "v_mfma_f32_16x16x32_bf16 v[36:39], a[28:31], v[20:23], v[36:39]\n"
    "s_waitcnt vmcnt(1)\n"
    "v_mul_f32 v41, 0xbfb8aa3b, v41\n"
    "v_add_u32 v13, v11, v3\n"
    "v_mfma_f32_16x16x32_bf16 v[48:51], a[32:35], v[20:23], v[48:51]\n"
    "v_mul_f32 v42, 0xbfb8aa3b, v42\n"
    "v_cmp_gt_i32 vcc, s71, v13\n"
    "v_mfma_f32_16x16x32_bf16 v[56:59], a[36:39], v[20:23], v[56:59]\n"
    "buffer_atomic_smax v7, v10, s[36:39], s69 offen sc1\n"
    "v_exp_f32 v41, v41\n"
    "v_mfma_f32_16x16x32_bf16 v[60:63], a[40:43], v[20:23], v[60:63]\n"
    "s_waitcnt lgkmcnt(1)\n"
    "v_exp_f32 v42, v42\n"
    "v_mfma_f32_16x16x32_bf16 v[72:75], a[44:47], v[20:23], v[72:75]\n"
    "ds_read_b128 v[20:23], v0 offset:4096\n"
    "s_cbranch_vccnz .LgUP_poll0_%=\n"
    "v_mfma_f32_16x16x32_bf16 v[32:35], a[48:51], v[24:27], v[32:35]\n"
    "s_add_u32 m0, s59, 20480\n"
    "v_mfma_f32_16x16x32_bf16 v[36:39], a[52:55], v[24:27], v[36:39]\n"
    "buffer_load_dwordx4 v1, s[36:39], s76 offen sc1 lds\n"
    "v_mul_f32 v43, 0xbfb8aa3b, v43\n"
    "v_mul_f32 v44, 0xbfb8aa3b, v44\n"
    "v_mfma_f32_16x16x32_bf16 v[48:51], a[56:59], v[24:27], v[48:51]\n"
    "v_exp_f32 v43, v43\n"
    "v_mfma_f32_16x16x32_bf16 v[56:59], a[60:63], v[24:27], v[56:59]\n"
    "v_exp_f32 v44, v44\n"
    "v_mfma_f32_16x16x32_bf16 v[60:63], a[64:67], v[24:27], v[60:63]\n"
    "s_waitcnt lgkmcnt(1)\n"
    "v_mul_f32 v45, 0xbfb8aa3b, v45\n"
    "v_mul_f32 v46, 0xbfb8aa3b, v46\n"
    "v_mfma_f32_16x16x32_bf16 v[72:75], a[68:71], v[24:27], v[72:75]\n"
    "ds_read_b128 v[24:27], v0 offset:5120\n"
    "v_mul_f32 v47, 0xbfb8aa3b, v47\n"
    "v_mfma_f32_16x16x32_bf16 v[32:35], a[72:75], v[16:19], v[32:35]\n"
    "v_exp_f32 v45, v45\n"
    "v_mfma_f32_16x16x32_bf16 v[36:39], a[76:79], v[16:19], v[36:39]\n"
    "v_exp_f32 v46, v46\n"
    "v_mfma_f32_16x16x32_bf16 v[48:51], a[80:83], v[16:19], v[48:51]\n"
    "s_add_u32 s76, s76, 16384\n"
    "v_exp_f32 v47, v47\n"
    "v_mfma_f32_16x16x32_bf16 v[56:59], a[84:87], v[16:19], v[56:59]\n"
    "s_add_u32 m0, s59, 24576\n"
    "v_add_f32 v40, 1.0, v40\n"
    "v_add_f32 v41, 1.0, v41\n"
    "v_mfma_f32_16x16x32_bf16 v[60:63], a[88:91], v[16:19], v[60:63]\n"
    "s_waitcnt lgkmcnt(1)\n"
    "v_rcp_f32 v40, v40\n"
    "v_mfma_f32_16x16x32_bf16 v[72:75], a[92:95], v[16:19], v[72:75]\n"
    "ds_read_b128 v[16:19], v0 offset:6144\n"
    "v_add_f32 v42, 1.0, v42\n"
    "v_mfma_f32_16x16x32_bf16 v[32:35], a[96:99], v[20:23], v[32:35]\n"
    "buffer_load_dwordx4 v1, s[36:39], s76 offen sc1 lds\n"
    "v_rcp_f32 v41, v41\n"
    "v_mfma_f32_16x16x32_bf16 v[36:39], a[100:103], v[20:23], v[36:39]\n"
    "v_add_f32 v43, 1.0, v43\n"
    "v_mfma_f32_16x16x32_bf16 v[48:51], a[104:107], v[20:23], v[48:51]\n"
    "v_add_f32 v44, 1.0, v44\n"
    "v_mfma_f32_16x16x32_bf16 v[56:59], a[108:111], v[20:23], v[56:59]\n"
    "v_rcp_f32 v42, v42\n"
    "v_mfma_f32_16x16x32_bf16 v[60:63], a[112:115], v[20:23], v[60:63]\n"
    "s_waitcnt lgkmcnt(1)\n"
    "v_rcp_f32 v43, v43\n"
    "v_mfma_f32_16x16x32_bf16 v[72:75], a[116:119], v[20:23], v[72:75]\n"
    "ds_read_b128 v[20:23], v0 offset:7168\n"
    "v_mfma_f32_16x16x32_bf16 v[32:35], a[120:123], v[24:27], v[32:35]\n"
    "s_add_u32 s76, s76, 16384\n"
    "s_add_u32 m0, s59, 28672\n"
    "v_mfma_f32_16x16x32_bf16 v[36:39], a[124:127], v[24:27], v[36:39]\n"
    "buffer_load_dwordx4 v1, s[36:39], s76 offen sc1 lds\n"
    "v_rcp_f32 v44, v44\n"
    "v_mfma_f32_16x16x32_bf16 v[48:51], a[128:131], v[24:27], v[48:51]\n"
    "v_add_f32 v45, 1.0, v45\n"
    "v_add_f32 v46, 1.0, v46\n"
    "v_mfma_f32_16x16x32_bf16 v[56:59], a[132:135], v[24:27], v[56:59]\n"
    "v_rcp_f32 v45, v45\n"
    "v_mfma_f32_16x16x32_bf16 v[60:63], a[136:139], v[24:27], v[60:63]\n"
    "s_waitcnt lgkmcnt(1)\n"
    "v_rcp_f32 v46, v46\n"
    "v_mfma_f32_16x16x32_bf16 v[72:75], a[140:143], v[24:27], v[72:75]\n"
    "ds_read_b128 v[24:27], v0 offset:8192\n"
    "v_add_f32 v47, 1.0, v47\n"
    "v_mfma_f32_16x16x32_bf16 v[32:35], a[144:147], v[16:19], v[32:35]\n"
    "v_rcp_f32 v47, v47\n"
    "v_mfma_f32_16x16x32_bf16 v[36:39], a[148:151], v[16:19], v[36:39]\n"
    "v_mul_f32 v40, v40, v64\n"
    "v_mul_f32 v41, v41, v65\n"
    "v_mfma_f32_16x16x32_bf16 v[48:51], a[152:155], v[16:19], v[48:51]\n"
    "v_mul_f32 v42, v42, v66\n"
    "v_mul_f32 v43, v43, v67\n"
    "v_mfma_f32_16x16x32_bf16 v[56:59], a[156:159], v[16:19], v[56:59]\n"
    "v_mul_f32 v44, v44, v68\n"
    "v_mul_f32 v45, v45, v69\n"
    "v_mfma_f32_16x16x32_bf16 v[60:63], a[160:163], v[16:19], v[60:63]\n"
    "s_waitcnt lgkmcnt(1)\n"
    "v_mul_f32 v46, v46, v70\n"
    "v_mul_f32 v47, v47, v71\n"
    "v_mfma_f32_16x16x32_bf16 v[72:75], a[164:167], v[16:19], v[72:75]\n"
    "ds_read_b128 v[16:19], v0 offset:9216\n"
    "v_cvt_pk_bf16_f32 v40, v40, v41\n"
    "v_mfma_f32_16x16x32_bf16 v[32:35], a[168:171], v[20:23], v[32:35]\n"
    "s_add_u32 s76, s76, 16384\n"
    "v_cvt_pk_bf16_f32 v41, v42, v43\n"
    "v_cvt_pk_bf16_f32 v42, v44, v45\n"
    "v_mfma_f32_16x16x32_bf16 v[36:39], a[172:175], v[20:23], v[36:39]\n"
    "s_add_u32 m0, s59, 32768\n"
    "v_cvt_pk_bf16_f32 v43, v46, v47\n"
    "v_mfma_f32_16x16x32_bf16 v[48:51], a[176:179], v[20:23], v[48:51]\n"
    "buffer_load_dwordx4 v1, s[36:39], s76 offen sc1 lds\n"
    "v_mul_f32 v76, v52, v76\n"
    "v_mfma_f32_16x16x32_bf16 v[56:59], a[180:183], v[20:23], v[56:59]\n"
    "buffer_store_dwordx4 v[40:43], v15, s[36:39], s73 offen offset:1024 sc1\n"
    "v_mfma_f32_16x16x32_bf16 v[60:63], a[184:187], v[20:23], v[60:63]\n"
    "s_waitcnt lgkmcnt(1)\n"
    "v_mul_f32 v77, v53, v77\n"
    "v_mul_f32 v78, v54, v78\n"
    "v_mfma_f32_16x16x32_bf16 v[72:75], a[188:191], v[20:23], v[72:75]\n"
    "ds_read_b128 v[20:23], v0 offset:10240\n"
    "v_mfma_f32_16x16x32_bf16 v[32:35], a[192:195], v[24:27], v[32:35]\n"
    "v_mfma_f32_16x16x32_bf16 v[36:39], a[196:199], v[24:27], v[36:39]\n"
    "v_mul_f32 v79, v55, v79\n"
    "v_mul_f32 v52, 0xbfb8aa3b, v52\n"
    "v_mfma_f32_16x16x32_bf16 v[48:51], a[200:203], v[24:27], v[48:51]\n"
    "s_add_u32 s76, s76, 16384\n"
    "v_exp_f32 v52, v52\n"
    "v_mfma_f32_16x16x32_bf16 v[56:59], a[204:207], v[24:27], v[56:59]\n"
    "s_add_u32 m0, s59, 36864\n"
    "v_mul_f32 v53, 0xbfb8aa3b, v53\n"
    "v_mul_f32 v54, 0xbfb8aa3b, v54\n"
    "v_mfma_f32_16x16x32_bf16 v[60:63], a[208:211], v[24:27], v[60:63]\n"
    "s_waitcnt lgkmcnt(1)\n"
    "v_exp_f32 v53, v53\n"
    "v_mfma_f32_16x16x32_bf16 v[72:75], a[212:215], v[24:27], v[72:75]\n"
    "ds_read_b128 v[24:27], v0 offset:11264\n"
    "v_mul_f32 v55, 0xbfb8aa3b, v55\n"
    "v_mfma_f32_16x16x32_bf16 v[32:35], a[216:219], v[16:19], v[32:35]\n"
    "buffer_load_dwordx4 v8, s[36:39], s76 offen sc1 lds\n"
    "v_exp_f32 v54, v54\n"
    "v_mfma_f32_16x16x32_bf16 v[36:39], a[220:223], v[16:19], v[36:39]\n"
    "v_exp_f32 v55, v55\n"
    "v_mfma_f32_16x16x32_bf16 v[48:51], a[224:227], v[16:19], v[48:51]\n"
    "v_add_f32 v52, 1.0, v52\n"
    "v_add_f32 v53, 1.0, v53\n"
    "v_mfma_f32_16x16x32_bf16 v[56:59], a[228:231], v[16:19], v[56:59]\n"
    "v_rcp_f32 v52, v52\n"
    "v_mfma_f32_16x16x32_bf16 v[60:63], a[232:235], v[16:19], v[60:63]\n"
    "s_waitcnt lgkmcnt(1)\n"
    "v_rcp_f32 v53, v53\n"
    "v_mfma_f32_16x16x32_bf16 v[72:75], a[236:239], v[16:19], v[72:75]\n"
    "ds_read_b128 v[16:19], v0 offset:12288\n"
    "v_add_f32 v54, 1.0, v54\n"
    "v_mfma_f32_16x16x32_bf16 v[32:35], a[240:243], v[20:23], v[32:35]\n"
    "v_rcp_f32 v54, v54\n"
    "v_mfma_f32_16x16x32_bf16 v[36:39], a[244:247], v[20:23], v[36:39]\n"
    "v_add_f32 v55, 1.0, v55\n"
    "v_mfma_f32_16x16x32_bf16 v[48:51], a[248:251], v[20:23], v[48:51]\n"
    "v_mul_f32 v52, v52, v76\n"
    "v_mfma_f32_16x16x32_bf16 v[56:59], a[252:255], v[20:23], v[56:59]\n"
    "v_rcp_f32 v55, v55\n"
    "v_mfma_f32_16x16x32_bf16 v[60:63], v[80:83], v[20:23], v[60:63]\n"
    "s_waitcnt lgkmcnt(1)\n"
    "v_mul_f32 v53, v53, v77\n"
    "v_mul_f32 v54, v54, v78\n"
    "v_mfma_f32_16x16x32_bf16 v[72:75], v[84:87], v[20:23], v[72:75]\n"
    "ds_read_b128 v[20:23], v0 offset:13312\n"
    "v_mfma_f32_16x16x32_bf16 v[32:35], v[88:91], v[24:27], v[32:35]\n"
    "v_mfma_f32_16x16x32_bf16 v[36:39], v[92:95], v[24:27], v[36:39]\n"
    "v_mul_f32 v55, v55, v79\n"
    "v_cvt_pk_bf16_f32 v52, v52, v53\n"
    "v_mfma_f32_16x16x32_bf16 v[48:51], v[96:99], v[24:27], v[48:51]\n"
    "v_cvt_pk_bf16_f32 v53, v54, v55\n"
    "s_nop 0\n"
    "v_mfma_f32_16x16x32_bf16 v[56:59], v[100:103], v[24:27], v[56:59]\n"
    "v_permlane16_swap_b32 v52, v54\n"
    "v_mfma_f32_16x16x32_bf16 v[60:63], v[104:107], v[24:27], v[60:63]\n"
    "s_waitcnt lgkmcnt(1)\n"
    "v_permlane16_swap_b32 v53, v55\n"
    "v_mfma_f32_16x16x32_bf16 v[72:75], v[108:111], v[24:27], v[72:75]\n"
    "ds_read_b128 v[24:27], v0 offset:14336\n"
    ".LgUP_join0_%=:\n"
    "v_mfma_f32_16x16x32_bf16 v[32:35], v[112:115], v[16:19], v[32:35]\n"
    "buffer_store_dwordx4 v[52:55], v4, s[36:39], s73 offen offset:1024 sc1\n"
    "v_mfma_f32_16x16x32_bf16 v[36:39], v[116:119], v[16:19], v[36:39]\n"
    "s_lshr_b32 s73, s68, 1\n"
    "v_mfma_f32_16x16x32_bf16 v[48:51], v[120:123], v[16:19], v[48:51]\n"
    "s_and_b32 s73, s73, s57\n"
    "v_mfma_f32_16x16x32_bf16 v[56:59], v[124:127], v[16:19], v[56:59]\n"
    "s_mul_i32 s73, s73, s56\n"
    "v_mfma_f32_16x16x32_bf16 v[60:63], v[128:131], v[16:19], v[60:63]\n"
    "s_waitcnt lgkmcnt(1)\n"
    "s_add_u32 s73, s73, s55\n"
    "v_mfma_f32_16x16x32_bf16 v[72:75], v[132:135], v[16:19], v[72:75]\n"
    "ds_read_b128 v[16:19], v0 offset:15360\n"
    "v_mfma_f32_16x16x32_bf16 v[32:35], v[136:139], v[20:23], v[32:35]\n"
    "s_add_u32 s71, s68, 2\n"
    "s_waitcnt vmcnt(6)\n"
    "v_mfma_f32_16x16x32_bf16 v[36:39], v[140:143], v[20:23], v[36:39]\n"
    "v_add_u32 v13, v11, v3\n"
    "v_mfma_f32_16x16x32_bf16 v[48:51], v[144:147], v[20:23], v[48:51]\n"
    "v_cmp_gt_i32 vcc, s71, v13\n"
    "v_mfma_f32_16x16x32_bf16 v[56:59], v[148:151], v[20:23], v[56:59]\n"
    "v_mfma_f32_16x16x32_bf16 v[60:63], v[152:155], v[20:23], v[60:63]\n"
    "s_waitcnt lgkmcnt(1)\n"
    "v_mfma_f32_16x16x32_bf16 v[72:75], v[156:159], v[20:23], v[72:75]\n"
    "ds_read_b128 v[20:23], v0 offset:16384\n"
    "v_mfma_f32_16x16x32_bf16 v[32:35], v[160:163], v[24:27], v[32:35]\n"
    "s_or_b64 exec, vcc, s[62:63]\n"
    "buffer_atomic_smax v11, v2, s[36:39], 0 offen sc0 sc1\n"
    "s_mov_b64 exec, -1\n"
    "v_mfma_f32_16x16x32_bf16 v[36:39], v[164:167], v[24:27], v[36:39]\n"
    "s_lshr_b32 s76, s71, 1\n"
    "v_mfma_f32_16x16x32_bf16 v[48:51], v[168:171], v[24:27], v[48:51]\n"
    "s_and_b32 s76, s76, s54\n"
    "v_mfma_f32_16x16x32_bf16 v[56:59], v[172:175], v[24:27], v[56:59]\n"
    "s_mul_i32 s76, s76, s53\n"
    "v_mfma_f32_16x16x32_bf16 v[60:63], v[176:179], v[24:27], v[60:63]\n"
    "s_waitcnt lgkmcnt(1)\n"
    "s_add_u32 s76, s76, s52\n"
    "v_mfma_f32_16x16x32_bf16 v[72:75], v[180:183], v[24:27], v[72:75]\n"
    "ds_read_b128 v[24:27], v0 offset:17408\n"
    "v_mfma_f32_16x16x32_bf16 v[32:35], v[184:187], v[16:19], v[32:35]\n"
    "v_mfma_f32_16x16x32_bf16 v[36:39], v[188:191], v[16:19], v[36:39]\n"
    "v_mfma_f32_16x16x32_bf16 v[48:51], v[192:195], v[16:19], v[48:51]\n"
    "v_mfma_f32_16x16x32_bf16 v[56:59], v[196:199], v[16:19], v[56:59]\n"
    "s_waitcnt vmcnt(2) lgkmcnt(0)\n"
    "s_barrier\n"
    "v_mfma_f32_16x16x32_bf16 v[60:63], v[200:203], v[16:19], v[60:63]\n"
    "v_mfma_f32_16x16x32_bf16 v[72:75], v[204:207], v[16:19], v[72:75]\n"
    "ds_read_b128 v[16:19], v9 offset:0\n"
    "v_mfma_f32_16x16x32_bf16 v[32:35], v[208:211], v[20:23], v[32:35]\n"
    "v_mfma_f32_16x16x32_bf16 v[36:39], v[212:215], v[20:23], v[36:39]\n"
    "v_add_u32 v13, v5, v3\n"
    "v_mfma_f32_16x16x32_bf16 v[48:51], v[216:219], v[20:23], v[48:51]\n"
    "v_cmp_gt_i32 vcc, s71, v13\n"
    "v_mfma_f32_16x16x32_bf16 v[56:59], v[220:223], v[20:23], v[56:59]\n"
    "v_mfma_f32_16x16x32_bf16 v[60:63], v[224:227], v[20:23], v[60:63]\n"
    "v_mfma_f32_16x16x32_bf16 v[72:75], v[228:231], v[20:23], v[72:75]\n"
    "ds_read_b128 v[20:23], v9 offset:1024\n"
    "v_mfma_f32_16x16x32_bf16 v[32:35], v[232:235], v[24:27], v[32:35]\n"
    "s_or_b64 exec, vcc, s[62:63]\n"
    "buffer_atomic_smax v5, v2, s[36:39], 0 offen sc0 sc1\n"
    "s_mov_b64 exec, -1\n"
    "v_mfma_f32_16x16x32_bf16 v[36:39], v[236:239], v[24:27], v[36:39]\n"
    "v_mfma_f32_16x16x32_bf16 v[48:51], v[240:243], v[24:27], v[48:51]\n"
    "v_mov_b32 v7, v12\n"
    "v_mfma_f32_16x16x32_bf16 v[56:59], v[244:247], v[24:27], v[56:59]\n"
    "s_mov_b32 s69, s70\n"
    "s_mov_b32 s70, 0\n"
    "v_mov_b32 v12, s68\n"
    "v_mfma_f32_16x16x32_bf16 v[60:63], v[248:251], v[24:27], v[60:63]\n"
    "s_waitcnt lgkmcnt(1)\n"
    "s_add_u32 s68, s68, 1\n"
    "v_mfma_f32_16x16x32_bf16 v[72:75], v[252:255], v[24:27], v[72:75]\n"
    "ds_read_b128 v[24:27], v9 offset:2048\n"
    ".LgUP_head1_%=:\n"
    "v_mfma_f32_16x16x32_bf16 v[40:43], a[0:3], v[16:19], 0\n"
    "v_mfma_f32_16x16x32_bf16 v[44:47], a[4:7], v[16:19], 0\n"
    "v_mul_f32 v56, v32, v56\n"
    "v_mul_f32 v57, v33, v57\n"
    "v_mfma_f32_16x16x32_bf16 v[52:55], a[8:11], v[16:19], 0\n"
    "v_mul_f32 v58, v34, v58\n"
    "v_mul_f32 v59, v35, v59\n"
    "v_mfma_f32_16x16x32_bf16 v[64:67], a[12:15], v[16:19], 0\n"
    "v_mul_f32 v60, v36, v60\n"
    "v_mul_f32 v61, v37, v61\n"
    "v_mfma_f32_16x16x32_bf16 v[68:71], a[16:19], v[16:19], 0\n"
    "s_waitcnt lgkmcnt(1)\n"
    "v_mul_f32 v62, v38, v62\n"
    "v_mul_f32 v63, v39, v63\n"
    "v_mfma_f32_16x16x32_bf16 v[76:79], a[20:23], v[16:19], 0\n"
    "ds_read_b128 v[16:19], v9 offset:3072\n"
    "v_mul_f32 v32, 0xbfb8aa3b, v32\n"
    "v_mfma_f32_16x16x32_bf16 v[40:43], a[24:27], v[20:23], v[40:43]\n"
    "v_exp_f32 v32, v32\n"
    "v_mfma_f32_16x16x32_bf16 v[44:47], a[28:31], v[20:23], v[44:47]\n"
    "s_waitcnt vmcnt(1)\n"
    "v_mul_f32 v33, 0xbfb8aa3b, v33\n"
    "v_add_u32 v13, v11, v3\n"
    "v_mfma_f32_16x16x32_bf16 v[52:55], a[32:35], v[20:23], v[52:55]\n"
    "v_mul_f32 v34, 0xbfb8aa3b, v34\n"
    "v_cmp_gt_i32 vcc, s71, v13\n"
    "v_mfma_f32_16x16x32_bf16 v[64:67], a[36:39], v[20:23], v[64:67]\n"
    "buffer_atomic_smax v7, v14, s[36:39], s69 offen sc1\n"
    "v_exp_f32 v33, v33\n"
    "v_mfma_f32_16x16x32_bf16 v[68:71], a[40:43], v[20:23], v[68:71]\n"
    "s_waitcnt lgkmcnt(1)\n"
    "v_exp_f32 v34, v34\n"
    "v_mfma_f32_16x16x32_bf16 v[76:79], a[44:47], v[20:23], v[76:79]\n"
    "ds_read_b128 v[20:23], v9 offset:4096\n"
    "s_cbranch_vccnz .LgUP_poll1_%=\n"
    "v_mfma_f32_16x16x32_bf16 v[40:43], a[48:51], v[24:27], v[40:43]\n"
    "s_add_u32 m0, s59, 0\n"
    "v_mfma_f32_16x16x32_bf16 v[44:47], a[52:55], v[24:27], v[44:47]\n"
    "buffer_load_dwordx4 v1, s[36:39], s76 offen sc1 lds\n"
    "v_mul_f32 v35, 0xbfb8aa3b, v35\n"
    "v_mul_f32 v36, 0xbfb8aa3b, v36\n"
    "v_mfma_f32_16x16x32_bf16 v[52:55], a[56:59], v[24:27], v[52:55]\n"
    "v_exp_f32 v35, v35\n"
    "v_mfma_f32_16x16x32_bf16 v[64:67], a[60:63], v[24:27], v[64:67]\n"
    "v_exp_f32 v36, v36\n"
    "v_mfma_f32_16x16x32_bf16 v[68:71], a[64:67], v[24:27], v[68:71]\n"
    "s_waitcnt lgkmcnt(1)\n"
    "v_mul_f32 v37, 0xbfb8aa3b, v37\n"
    "v_mul_f32 v38, 0xbfb8aa3b, v38\n"
    "v_mfma_f32_16x16x32_bf16 v[76:79], a[68:71], v[24:27], v[76:79]\n"
    "ds_read_b128 v[24:27], v9 offset:5120\n"
    "v_mul_f32 v39, 0xbfb8aa3b, v39\n"
    "v_mfma_f32_16x16x32_bf16 v[40:43], a[72:75], v[16:19], v[40:43]\n"
    "v_exp_f32 v37, v37\n"
    "v_mfma_f32_16x16x32_bf16 v[44:47], a[76:79], v[16:19], v[44:47]\n"
    "v_exp_f32 v38, v38\n"
    "v_mfma_f32_16x16x32_bf16 v[52:55], a[80:83], v[16:19], v[52:55]\n"
    "s_add_u32 s76, s76, 16384\n"
    "v_exp_f32 v39, v39\n"
    "v_mfma_f32_16x16x32_bf16 v[64:67], a[84:87], v[16:19], v[64:67]\n"
    "s_add_u32 m0, s59, 4096\n"
    "v_add_f32 v32, 1.0, v32\n"
    "v_add_f32 v33, 1.0, v33\n"
    "v_mfma_f32_16x16x32_bf16 v[68:71], a[88:91], v[16:19], v[68:71]\n"
    "s_waitcnt lgkmcnt(1)\n"
    "v_rcp_f32 v32, v32\n"
    "v_mfma_f32_16x16x32_bf16 v[76:79], a[92:95], v[16:19], v[76:79]\n"
    "ds_read_b128 v[16:19], v9 offset:6144\n"
    "v_add_f32 v34, 1.0, v34\n"
    "v_mfma_f32_16x16x32_bf16 v[40:43], a[96:99], v[20:23], v[40:43]\n"
    "buffer_load_dwordx4 v1, s[36:39], s76 offen sc1 lds\n"
    "v_rcp_f32 v33, v33\n"
    "v_mfma_f32_16x16x32_bf16 v[44:47], a[100:103], v[20:23], v[44:47]\n"
    "v_add_f32 v35, 1.0, v35\n"
    "v_mfma_f32_16x16x32_bf16 v[52:55], a[104:107], v[20:23], v[52:55]\n"
    "v_add_f32 v36, 1.0, v36\n"
    "v_mfma_f32_16x16x32_bf16 v[64:67], a[108:111], v[20:23], v[64:67]\n"
    "v_rcp_f32 v34, v34\n"
    "v_mfma_f32_16x16x32_bf16 v[68:71], a[112:115], v[20:23], v[68:71]\n"
    "s_waitcnt lgkmcnt(1)\n"
    "v_rcp_f32 v35, v35\n"
    "v_mfma_f32_16x16x32_bf16 v[76:79], a[116:119], v[20:23], v[76:79]\n"
    "ds_read_b128 v[20:23], v9 offset:7168\n"
    "v_mfma_f32_16x16x32_bf16 v[40:43], a[120:123], v[24:27], v[40:43]\n"
    "s_add_u32 s76, s76, 16384\n"
    "s_add_u32 m0, s59, 8192\n"
    "v_mfma_f32_16x16x32_bf16 v[44:47], a[124:127], v[24:27], v[44:47]\n"
    "buffer_load_dwordx4 v1, s[36:39], s76 offen sc1 lds\n"
    "v_rcp_f32 v36, v36\n"
    "v_mfma_f32_16x16x32_bf16 v[52:55], a[128:131], v[24:27], v[52:55]\n"
    "v_add_f32 v37, 1.0, v37\n"
    "v_add_f32 v38, 1.0, v38\n"
    "v_mfma_f32_16x16x32_bf16 v[64:67], a[132:135], v[24:27], v[64:67]\n"
    "v_rcp_f32 v37, v37\n"
    "v_mfma_f32_16x16x32_bf16 v[68:71], a[136:139], v[24:27], v[68:71]\n"
    "s_waitcnt lgkmcnt(1)\n"
    "v_rcp_f32 v38, v38\n"
    "v_mfma_f32_16x16x32_bf16 v[76:79], a[140:143], v[24:27], v[76:79]\n"
    "ds_read_b128 v[24:27], v9 offset:8192\n"
    "v_add_f32 v39, 1.0, v39\n"
    "v_mfma_f32_16x16x32_bf16 v[40:43], a[144:147], v[16:19], v[40:43]\n"
    "v_rcp_f32 v39, v39\n"
    "v_mfma_f32_16x16x32_bf16 v[44:47], a[148:151], v[16:19], v[44:47]\n"
    "v_mul_f32 v32, v32, v56\n"
    "v_mul_f32 v33, v33, v57\n"
    "v_mfma_f32_16x16x32_bf16 v[52:55], a[152:155], v[16:19], v[52:55]\n"
    "v_mul_f32 v34, v34, v58\n"
    "v_mul_f32 v35, v35, v59\n"
    "v_mfma_f32_16x16x32_bf16 v[64:67], a[156:159], v[16:19], v[64:67]\n"
    "v_mul_f32 v36, v36, v60\n"
    "v_mul_f32 v37, v37, v61\n"
    "v_mfma_f32_16x16x32_bf16 v[68:71], a[160:163], v[16:19], v[68:71]\n"
    "s_waitcnt lgkmcnt(1)\n"
    "v_mul_f32 v38, v38, v62\n"
    "v_mul_f32 v39, v39, v63\n"
    "v_mfma_f32_16x16x32_bf16 v[76:79], a[164:167], v[16:19], v[76:79]\n"
    "ds_read_b128 v[16:19], v9 offset:9216\n"
    "v_cvt_pk_bf16_f32 v32, v32, v33\n"
    "v_mfma_f32_16x16x32_bf16 v[40:43], a[168:171], v[20:23], v[40:43]\n"
    "s_add_u32 s76, s76, 16384\n"
    "v_cvt_pk_bf16_f32 v33, v34, v35\n"
    "v_cvt_pk_bf16_f32 v34, v36, v37\n"
    "v_mfma_f32_16x16x32_bf16 v[44:47], a[172:175], v[20:23], v[44:47]\n"
    "s_add_u32 m0, s59, 12288\n"
    "v_cvt_pk_bf16_f32 v35, v38, v39\n"
    "v_mfma_f32_16x16x32_bf16 v[52:55], a[176:179], v[20:23], v[52:55]\n"
    "buffer_load_dwordx4 v1, s[36:39], s76 offen sc1 lds\n"
    "v_mul_f32 v72, v48, v72\n"
    "v_mfma_f32_16x16x32_bf16 v[64:67], a[180:183], v[20:23], v[64:67]\n"
    "buffer_store_dwordx4 v[32:35], v15, s[36:39], s73 offen sc1\n"
    "v_mfma_f32_16x16x32_bf16 v[68:71], a[184:187], v[20:23], v[68:71]\n"
    "s_waitcnt lgkmcnt(1)\n"
    "v_mul_f32 v73, v49, v73\n"
    "v_mul_f32 v74, v50, v74\n"
    "v_mfma_f32_16x16x32_bf16 v[76:79], a[188:191], v[20:23], v[76:79]\n"
    "ds_read_b128 v[20:23], v9 offset:10240\n"
    "v_mfma_f32_16x16x32_bf16 v[40:43], a[192:195], v[24:27], v[40:43]\n"
    "v_mfma_f32_16x16x32_bf16 v[44:47], a[196:199], v[24:27], v[44:47]\n"
    "v_mul_f32 v75, v51, v75\n"
    "v_mul_f32 v48, 0xbfb8aa3b, v48\n"
    "v_mfma_f32_16x16x32_bf16 v[52:55], a[200:203], v[24:27], v[52:55]\n"
    "s_add_u32 s76, s76, 16384\n"
    "v_exp_f32 v48, v48\n"
    "v_mfma_f32_16x16x32_bf16 v[64:67], a[204:207], v[24:27], v[64:67]\n"
    "s_add_u32 m0, s59, 16384\n"
    "v_mul_f32 v49, 0xbfb8aa3b, v49\n"
    "v_mul_f32 v50, 0xbfb8aa3b, v50\n"
    "v_mfma_f32_16x16x32_bf16 v[68:71], a[208:211], v[24:27], v[68:71]\n"
    "s_waitcnt lgkmcnt(1)\n"
    "v_exp_f32 v49, v49\n"
    "v_mfma_f32_16x16x32_bf16 v[76:79], a[212:215], v[24:27], v[76:79]\n"
    "ds_read_b128 v[24:27], v9 offset:11264\n"
    "v_mul_f32 v51, 0xbfb8aa3b, v51\n"
    "v_mfma_f32_16x16x32_bf16 v[40:43], a[216:219], v[16:19], v[40:43]\n"
    "buffer_load_dwordx4 v8, s[36:39], s76 offen sc1 lds\n"
    "v_exp_f32 v50, v50\n"
    "v_mfma_f32_16x16x32_bf16 v[44:47], a[220:223], v[16:19], v[44:47]\n"
    "v_exp_f32 v51, v51\n"
    "v_mfma_f32_16x16x32_bf16 v[52:55], a[224:227], v[16:19], v[52:55]\n"
    "v_add_f32 v48, 1.0, v48\n"
    "v_add_f32 v49, 1.0, v49\n"
    "v_mfma_f32_16x16x32_bf16 v[64:67], a[228:231], v[16:19], v[64:67]\n"
    "v_rcp_f32 v48, v48\n"
    "v_mfma_f32_16x16x32_bf16 v[68:71], a[232:235], v[16:19], v[68:71]\n"
    "s_waitcnt lgkmcnt(1)\n"
    "v_rcp_f32 v49, v49\n"
    "v_mfma_f32_16x16x32_bf16 v[76:79], a[236:239], v[16:19], v[76:79]\n"
    "ds_read_b128 v[16:19], v9 offset:12288\n"
    "v_add_f32 v50, 1.0, v50\n"
    "v_mfma_f32_16x16x32_bf16 v[40:43], a[240:243], v[20:23], v[40:43]\n"
    "v_rcp_f32 v50, v50\n"
    "v_mfma_f32_16x16x32_bf16 v[44:47], a[244:247], v[20:23], v[44:47]\n"
    "v_add_f32 v51, 1.0, v51\n"
    "v_mfma_f32_16x16x32_bf16 v[52:55], a[248:251], v[20:23], v[52:55]\n"
    "v_mul_f32 v48, v48, v72\n"
    "v_mfma_f32_16x16x32_bf16 v[64:67], a[252:255], v[20:23], v[64:67]\n"
    "v_rcp_f32 v51, v51\n"
    "v_mfma_f32_16x16x32_bf16 v[68:71], v[80:83], v[20:23], v[68:71]\n"
    "s_waitcnt lgkmcnt(1)\n"
    "v_mul_f32 v49, v49, v73\n"
    "v_mul_f32 v50, v50, v74\n"
    "v_mfma_f32_16x16x32_bf16 v[76:79], v[84:87], v[20:23], v[76:79]\n"
    "ds_read_b128 v[20:23], v9 offset:13312\n"
    "v_mfma_f32_16x16x32_bf16 v[40:43], v[88:91], v[24:27], v[40:43]\n"
    "v_mfma_f32_16x16x32_bf16 v[44:47], v[92:95], v[24:27], v[44:47]\n"
    "v_mul_f32 v51, v51, v75\n"
    "v_cvt_pk_bf16_f32 v48, v48, v49\n"
    "v_mfma_f32_16x16x32_bf16 v[52:55], v[96:99], v[24:27], v[52:55]\n"
    "v_cvt_pk_bf16_f32 v49, v50, v51\n"
    "s_nop 0\n"
    "v_mfma_f32_16x16x32_bf16 v[64:67], v[100:103], v[24:27], v[64:67]\n"
    "v_permlane16_swap_b32 v48, v50\n"
    "v_mfma_f32_16x16x32_bf16 v[68:71], v[104:107], v[24:27], v[68:71]\n"
    "s_waitcnt lgkmcnt(1)\n"
    "v_permlane16_swap_b32 v49, v51\n"
    "v_mfma_f32_16x16x32_bf16 v[76:79], v[108:111], v[24:27], v[76:79]\n"
    "ds_read_b128 v[24:27], v9 offset:14336\n"
    ".LgUP_join1_%=:\n"
    "v_mfma_f32_16x16x32_bf16 v[40:43], v[112:115], v[16:19], v[40:43]\n"
    "buffer_store_dwordx4 v[48:51], v4, s[36:39], s73 offen sc1\n"
    "v_mfma_f32_16x16x32_bf16 v[44:47], v[116:119], v[16:19], v[44:47]\n"
    "s_lshr_b32 s73, s68, 1\n"
    "v_mfma_f32_16x16x32_bf16 v[52:55], v[120:123], v[16:19], v[52:55]\n"
    "s_and_b32 s73, s73, s57\n"
    "v_mfma_f32_16x16x32_bf16 v[64:67], v[124:127], v[16:19], v[64:67]\n"
    "s_mul_i32 s73, s73, s56\n"
    "v_mfma_f32_16x16x32_bf16 v[68:71], v[128:131], v[16:19], v[68:71]\n"
    "s_waitcnt lgkmcnt(1)\n"
    "s_add_u32 s73, s73, s55\n"
    "v_mfma_f32_16x16x32_bf16 v[76:79], v[132:135], v[16:19], v[76:79]\n"
    "ds_read_b128 v[16:19], v9 offset:15360\n"
    "v_mfma_f32_16x16x32_bf16 v[40:43], v[136:139], v[20:23], v[40:43]\n"
    "s_add_u32 s71, s68, 2\n"
    "s_waitcnt vmcnt(6)\n"
    "v_mfma_f32_16x16x32_bf16 v[44:47], v[140:143], v[20:23], v[44:47]\n"
    "v_add_u32 v13, v11, v3\n"
    "v_mfma_f32_16x16x32_bf16 v[52:55], v[144:147], v[20:23], v[52:55]\n"
    "v_cmp_gt_i32 vcc, s71, v13\n"
    "v_mfma_f32_16x16x32_bf16 v[64:67], v[148:151], v[20:23], v[64:67]\n"
    "v_mfma_f32_16x16x32_bf16 v[68:71], v[152:155], v[20:23], v[68:71]\n"
    "s_waitcnt lgkmcnt(1)\n"
    "v_mfma_f32_16x16x32_bf16 v[76:79], v[156:159], v[20:23], v[76:79]\n"
    "ds_read_b128 v[20:23], v9 offset:16384\n"
    "v_mfma_f32_16x16x32_bf16 v[40:43], v[160:163], v[24:27], v[40:43]\n"
    "s_or_b64 exec, vcc, s[62:63]\n"
    "buffer_atomic_smax v11, v2, s[36:39], 0 offen sc0 sc1\n"
    "s_mov_b64 exec, -1\n"
    "v_mfma_f32_16x16x32_bf16 v[44:47], v[164:167], v[24:27], v[44:47]\n"
    "s_lshr_b32 s76, s71, 1\n"
    "v_mfma_f32_16x16x32_bf16 v[52:55], v[168:171], v[24:27], v[52:55]\n"
    "s_and_b32 s76, s76, s54\n"
    "v_mfma_f32_16x16x32_bf16 v[64:67], v[172:175], v[24:27], v[64:67]\n"
    "s_mul_i32 s76, s76, s53\n"
    "v_mfma_f32_16x16x32_bf16 v[68:71], v[176:179], v[24:27], v[68:71]\n"
    "s_waitcnt lgkmcnt(1)\n"
    "s_add_u32 s76, s76, s52\n"
    "v_mfma_f32_16x16x32_bf16 v[76:79], v[180:183], v[24:27], v[76:79]\n"
    "ds_read_b128 v[24:27], v9 offset:17408\n"
    "s_add_u32 s76, s76, 1024\n"
    "v_mfma_f32_16x16x32_bf16 v[40:43], v[184:187], v[16:19], v[40:43]\n"
    "v_mfma_f32_16x16x32_bf16 v[44:47], v[188:191], v[16:19], v[44:47]\n"
    "v_mfma_f32_16x16x32_bf16 v[52:55], v[192:195], v[16:19], v[52:55]\n"
    "v_mfma_f32_16x16x32_bf16 v[64:67], v[196:199], v[16:19], v[64:67]\n"
    "s_waitcnt vmcnt(2) lgkmcnt(0)\n"
    "s_barrier\n"
    "v_mfma_f32_16x16x32_bf16 v[68:71], v[200:203], v[16:19], v[68:71]\n"
    "v_mfma_f32_16x16x32_bf16 v[76:79], v[204:207], v[16:19], v[76:79]\n"
    "ds_read_b128 v[16:19], v0 offset:0\n"
    "v_mfma_f32_16x16x32_bf16 v[40:43], v[208:211], v[20:23], v[40:43]\n"
    "v_mfma_f32_16x16x32_bf16 v[44:47], v[212:215], v[20:23], v[44:47]\n"
    "v_add_u32 v13, v5, v3\n"
    "v_mfma_f32_16x16x32_bf16 v[52:55], v[216:219], v[20:23], v[52:55]\n"
    "v_cmp_gt_i32 vcc, s71, v13\n"
    "v_mfma_f32_16x16x32_bf16 v[64:67], v[220:223], v[20:23], v[64:67]\n"
    "v_mfma_f32_16x16x32_bf16 v[68:71], v[224:227], v[20:23], v[68:71]\n"
    "v_mfma_f32_16x16x32_bf16 v[76:79], v[228:231], v[20:23], v[76:79]\n"
    "ds_read_b128 v[20:23], v0 offset:1024\n"
    "v_mfma_f32_16x16x32_bf16 v[40:43], v[232:235], v[24:27], v[40:43]\n"
    "s_or_b64 exec, vcc, s[62:63]\n"
    "buffer_atomic_smax v5, v2, s[36:39], 0 offen sc0 sc1\n"
    "s_mov_b64 exec, -1\n"
    "v_mfma_f32_16x16x32_bf16 v[44:47], v[236:239], v[24:27], v[44:47]\n"
    "v_mfma_f32_16x16x32_bf16 v[52:55], v[240:243], v[24:27], v[52:55]\n"
    "v_mov_b32 v7, v12\n"
    "v_mfma_f32_16x16x32_bf16 v[64:67], v[244:247], v[24:27], v[64:67]\n"
    "s_mov_b32 s69, s70\n"
    "s_mov_b32 s70, 0\n"
    "v_mov_b32 v12, s68\n"
    "v_mfma_f32_16x16x32_bf16 v[68:71], v[248:251], v[24:27], v[68:71]\n"
    "s_waitcnt lgkmcnt(1)\n"
    "s_add_u32 s68, s68, 1\n"
    "v_mfma_f32_16x16x32_bf16 v[76:79], v[252:255], v[24:27], v[76:79]\n"
    "ds_read_b128 v[24:27], v0 offset:2048\n"
    "s_branch .LgUP_head0_%=\n"
    ".LgUP_LOOPE_%=:\n"
    ".LgUP_done_%=:\n"
    "s_waitcnt vmcnt(0)\n"
    "buffer_atomic_smax v12, v10, s[36:39], 0 offen sc1\n"
    "s_waitcnt vmcnt(0)\n"
    "s_branch .LgUP_FIN_%=\n"
    ".LgUP_poll0_%=:\n"
    "v_mfma_f32_16x16x32_bf16 v[32:35], a[48:51], v[24:27], v[32:35]\n"
    "s_or_b64 exec, vcc, s[62:63]\n"
    "buffer_atomic_smax v11, v2, s[36:39], 0 offen sc0 sc1\n"
    "s_mov_b64 exec, -1\n"
    "v_mfma_f32_16x16x32_bf16 v[36:39], a[52:55], v[24:27], v[36:39]\n"
    "v_mul_f32 v43, 0xbfb8aa3b, v43\n"
    "v_mul_f32 v44, 0xbfb8aa3b, v44\n"
    "v_mfma_f32_16x16x32_bf16 v[48:51], a[56:59], v[24:27], v[48:51]\n"
    "v_exp_f32 v43, v43\n"
    "v_mfma_f32_16x16x32_bf16 v[56:59], a[60:63], v[24:27], v[56:59]\n"
    "v_exp_f32 v44, v44\n"
    "v_mfma_f32_16x16x32_bf16 v[60:63], a[64:67], v[24:27], v[60:63]\n"
    "s_waitcnt lgkmcnt(1)\n"
    "v_mul_f32 v45, 0xbfb8aa3b, v45\n"
    "v_mul_f32 v46, 0xbfb8aa3b, v46\n"
    "v_mfma_f32_16x16x32_bf16 v[72:75], a[68:71], v[24:27], v[72:75]\n"
    "ds_read_b128 v[24:27], v0 offset:5120\n"
    "v_mul_f32 v47, 0xbfb8aa3b, v47\n"
    "v_mfma_f32_16x16x32_bf16 v[32:35], a[72:75], v[16:19], v[32:35]\n"
    "v_exp_f32 v45, v45\n"
    "v_mfma_f32_16x16x32_bf16 v[36:39], a[76:79], v[16:19], v[36:39]\n"
    "v_exp_f32 v46, v46\n"
    "v_mfma_f32_16x16x32_bf16 v[48:51], a[80:83], v[16:19], v[48:51]\n"
    "v_exp_f32 v47, v47\n"
    "v_mfma_f32_16x16x32_bf16 v[56:59], a[84:87], v[16:19], v[56:59]\n"
    "v_add_f32 v40, 1.0, v40\n"
    "v_add_f32 v41, 1.0, v41\n"
    "v_mfma_f32_16x16x32_bf16 v[60:63], a[88:91], v[16:19], v[60:63]\n"
    "s_waitcnt lgkmcnt(1)\n"
    "v_rcp_f32 v40, v40\n"
    "v_mfma_f32_16x16x32_bf16 v[72:75], a[92:95], v[16:19], v[72:75]\n"
    "ds_read_b128 v[16:19], v0 offset:6144\n"
    "v_add_f32 v42, 1.0, v42\n"
    "v_mfma_f32_16x16x32_bf16 v[32:35], a[96:99], v[20:23], v[32:35]\n"
    "v_rcp_f32 v41, v41\n"
    "v_mfma_f32_16x16x32_bf16 v[36:39], a[100:103], v[20:23], v[36:39]\n"
    "s_waitcnt vmcnt(2)\n"
    "v_add_f32 v43, 1.0, v43\n"
    "v_add_u32 v13, v5, v3\n"
    "v_mfma_f32_16x16x32_bf16 v[48:51], a[104:107], v[20:23], v[48:51]\n"
    "v_add_f32 v44, 1.0, v44\n"
    "v_cmp_gt_i32 vcc, s71, v13\n"
    "v_mfma_f32_16x16x32_bf16 v[56:59], a[108:111], v[20:23], v[56:59]\n"
    "v_rcp_f32 v42, v42\n"
    "v_mfma_f32_16x16x32_bf16 v[60:63], a[112:115], v[20:23], v[60:63]\n"
    "s_waitcnt lgkmcnt(1)\n"
    "v_rcp_f32 v43, v43\n"
    "v_mfma_f32_16x16x32_bf16 v[72:75], a[116:119], v[20:23], v[72:75]\n"
    "ds_read_b128 v[20:23], v0 offset:7168\n"
    "s_cbranch_vccz .LgUP_dma0_30_%=\n"
    "v_mfma_f32_16x16x32_bf16 v[32:35], a[120:123], v[24:27], v[32:35]\n"
    "s_or_b64 exec, vcc, s[62:63]\n"
    "buffer_atomic_smax v5, v2, s[36:39], 0 offen sc0 sc1\n"
    "s_mov_b64 exec, -1\n"
    "v_mfma_f32_16x16x32_bf16 v[36:39], a[124:127], v[24:27], v[36:39]\n"
    "v_rcp_f32 v44, v44\n"
    "v_mfma_f32_16x16x32_bf16 v[48:51], a[128:131], v[24:27], v[48:51]\n"
    "v_add_f32 v45, 1.0, v45\n"
    "v_add_f32 v46, 1.0, v46\n"
    "v_mfma_f32_16x16x32_bf16 v[56:59], a[132:135], v[24:27], v[56:59]\n"
    "v_rcp_f32 v45, v45\n"
    "v_mfma_f32_16x16x32_bf16 v[60:63], a[136:139], v[24:27], v[60:63]\n"
    "s_waitcnt lgkmcnt(1)\n"
    "v_rcp_f32 v46, v46\n"
    "v_mfma_f32_16x16x32_bf16 v[72:75], a[140:143], v[24:27], v[72:75]\n"
    "ds_read_b128 v[24:27], v0 offset:8192\n"
    "v_add_f32 v47, 1.0, v47\n"
    "v_mfma_f32_16x16x32_bf16 v[32:35], a[144:147], v[16:19], v[32:35]\n"
    "v_rcp_f32 v47, v47\n"
    "v_mfma_f32_16x16x32_bf16 v[36:39], a[148:151], v[16:19], v[36:39]\n"
    "v_mul_f32 v40, v40, v64\n"
    "v_mul_f32 v41, v41, v65\n"
    "v_mfma_f32_16x16x32_bf16 v[48:51], a[152:155], v[16:19], v[48:51]\n"
    "v_mul_f32 v42, v42, v66\n"
    "v_mul_f32 v43, v43, v67\n"
    "v_mfma_f32_16x16x32_bf16 v[56:59], a[156:159], v[16:19], v[56:59]\n"
    "v_mul_f32 v44, v44, v68\n"
    "v_mul_f32 v45, v45, v69\n"
    "v_mfma_f32_16x16x32_bf16 v[60:63], a[160:163], v[16:19], v[60:63]\n"
    "s_waitcnt lgkmcnt(1)\n"
    "v_mul_f32 v46, v46, v70\n"
    "v_mul_f32 v47, v47, v71\n"
    "v_mfma_f32_16x16x32_bf16 v[72:75], a[164:167], v[16:19], v[72:75]\n"
    "ds_read_b128 v[16:19], v0 offset:9216\n"
    "v_cvt_pk_bf16_f32 v40, v40, v41\n"
    "v_mfma_f32_16x16x32_bf16 v[32:35], a[168:171], v[20:23], v[32:35]\n"
    "v_cvt_pk_bf16_f32 v41, v42, v43\n"
    "v_cvt_pk_bf16_f32 v42, v44, v45\n"
    "v_mfma_f32_16x16x32_bf16 v[36:39], a[172:175], v[20:23], v[36:39]\n"
    "s_waitcnt vmcnt(1)\n"
    "v_cvt_pk_bf16_f32 v43, v46, v47\n"
    "v_add_u32 v13, v11, v3\n"
    "v_mfma_f32_16x16x32_bf16 v[48:51], a[176:179], v[20:23], v[48:51]\n"
    "v_mul_f32 v76, v52, v76\n"
    "v_cmp_gt_i32 vcc, s71, v13\n"
    "v_mfma_f32_16x16x32_bf16 v[56:59], a[180:183], v[20:23], v[56:59]\n"
    "buffer_store_dwordx4 v[40:43], v15, s[36:39], s73 offen offset:1024 sc1\n"
    "v_mfma_f32_16x16x32_bf16 v[60:63], a[184:187], v[20:23], v[60:63]\n"
    "s_waitcnt lgkmcnt(1)\n"
    "v_mul_f32 v77, v53, v77\n"
    "v_mul_f32 v78, v54, v78\n"
    "v_mfma_f32_16x16x32_bf16 v[72:75], a[188:191], v[20:23], v[72:75]\n"
    "ds_read_b128 v[20:23], v0 offset:10240\n"
    "s_cbranch_vccz .LgUP_dma0_48_%=\n"
    "v_mfma_f32_16x16x32_bf16 v[32:35], a[192:195], v[24:27], v[32:35]\n"
    "s_or_b64 exec, vcc, s[62:63]\n"
    "buffer_atomic_smax v11, v2, s[36:39], 0 offen sc0 sc1\n"
    "s_mov_b64 exec, -1\n"
    "v_mfma_f32_16x16x32_bf16 v[36:39], a[196:199], v[24:27], v[36:39]\n"
    "v_mul_f32 v79, v55, v79\n"
    "v_mul_f32 v52, 0xbfb8aa3b, v52\n"
    "v_mfma_f32_16x16x32_bf16 v[48:51], a[200:203], v[24:27], v[48:51]\n"
    "v_exp_f32 v52, v52\n"
    "v_mfma_f32_16x16x32_bf16 v[56:59], a[204:207], v[24:27], v[56:59]\n"
    "v_mul_f32 v53, 0xbfb8aa3b, v53\n"
    "v_mul_f32 v54, 0xbfb8aa3b, v54\n"
    "v_mfma_f32_16x16x32_bf16 v[60:63], a[208:211], v[24:27], v[60:63]\n"
    "s_waitcnt lgkmcnt(1)\n"
    "v_exp_f32 v53, v53\n"
    "v_mfma_f32_16x16x32_bf16 v[72:75], a[212:215], v[24:27], v[72:75]\n"
    "ds_read_b128 v[24:27], v0 offset:11264\n"
    "v_mul_f32 v55, 0xbfb8aa3b, v55\n"
    "v_mfma_f32_16x16x32_bf16 v[32:35], a[216:219], v[16:19], v[32:35]\n"
    "v_exp_f32 v54, v54\n"
    "v_mfma_f32_16x16x32_bf16 v[36:39], a[220:223], v[16:19], v[36:39]\n"
    "v_exp_f32 v55, v55\n"
    "v_mfma_f32_16x16x32_bf16 v[48:51], a[224:227], v[16:19], v[48:51]\n"
    "v_add_f32 v52, 1.0, v52\n"
    "v_add_f32 v53, 1.0, v53\n"
    "v_mfma_f32_16x16x32_bf16 v[56:59], a[228:231], v[16:19], v[56:59]\n"
    "v_rcp_f32 v52, v52\n"
    "v_mfma_f32_16x16x32_bf16 v[60:63], a[232:235], v[16:19], v[60:63]\n"
    "s_waitcnt lgkmcnt(1)\n"
    "v_rcp_f32 v53, v53\n"
    "v_mfma_f32_16x16x32_bf16 v[72:75], a[236:239], v[16:19], v[72:75]\n"
    "ds_read_b128 v[16:19], v0 offset:12288\n"
    "v_add_f32 v54, 1.0, v54\n"
    "v_mfma_f32_16x16x32_bf16 v[32:35], a[240:243], v[20:23], v[32:35]\n"
    "v_rcp_f32 v54, v54\n"
    "v_mfma_f32_16x16x32_bf16 v[36:39], a[244:247], v[20:23], v[36:39]\n"
    "s_waitcnt vmcnt(2)\n"
    "v_add_f32 v55, 1.0, v55\n"
    "v_add_u32 v13, v5, v3\n"
    "v_mfma_f32_16x16x32_bf16 v[48:51], a[248:251], v[20:23], v[48:51]\n"
    "v_mul_f32 v52, v52, v76\n"
    "v_cmp_gt_i32 vcc, s71, v13\n"
    "v_mfma_f32_16x16x32_bf16 v[56:59], a[252:255], v[20:23], v[56:59]\n"
    "v_rcp_f32 v55, v55\n"
    "v_mfma_f32_16x16x32_bf16 v[60:63], v[80:83], v[20:23], v[60:63]\n"
    "s_waitcnt lgkmcnt(1)\n"
    "v_mul_f32 v53, v53, v77\n"
    "v_mul_f32 v54, v54, v78\n"
    "v_mfma_f32_16x16x32_bf16 v[72:75], v[84:87], v[20:23], v[72:75]\n"
    "ds_read_b128 v[20:23], v0 offset:13312\n"
    "s_cbranch_vccz .LgUP_dma0_66_%=\n"
    "v_mfma_f32_16x16x32_bf16 v[32:35], v[88:91], v[24:27], v[32:35]\n"
    "s_or_b64 exec, vcc, s[62:63]\n"
    "buffer_atomic_smax v5, v2, s[36:39], 0 offen sc0 sc1\n"
    "s_mov_b64 exec, -1\n"
    "v_mfma_f32_16x16x32_bf16 v[36:39], v[92:95], v[24:27], v[36:39]\n"
    "v_mul_f32 v55, v55, v79\n"
    "v_cvt_pk_bf16_f32 v52, v52, v53\n"
    "v_mfma_f32_16x16x32_bf16 v[48:51], v[96:99], v[24:27], v[48:51]\n"
    "v_cvt_pk_bf16_f32 v53, v54, v55\n"
    "s_nop 0\n"
    "v_mfma_f32_16x16x32_bf16 v[56:59], v[100:103], v[24:27], v[56:59]\n"
    "v_permlane16_swap_b32 v52, v54\n"
    "v_mfma_f32_16x16x32_bf16 v[60:63], v[104:107], v[24:27], v[60:63]\n"
    "s_waitcnt lgkmcnt(1)\n"
    "v_permlane16_swap_b32 v53, v55\n"
    "v_mfma_f32_16x16x32_bf16 v[72:75], v[108:111], v[24:27], v[72:75]\n"
    "ds_read_b128 v[24:27], v0 offset:14336\n"
    "v_mfma_f32_16x16x32_bf16 v[32:35], v[112:115], v[16:19], v[32:35]\n"
    "buffer_store_dwordx4 v[52:55], v4, s[36:39], s73 offen offset:1024 sc1\n"
    "v_mfma_f32_16x16x32_bf16 v[36:39], v[116:119], v[16:19], v[36:39]\n"
    "s_lshr_b32 s73, s68, 1\n"
    "v_mfma_f32_16x16x32_bf16 v[48:51], v[120:123], v[16:19], v[48:51]\n"
    "s_and_b32 s73, s73, s57\n"
    "v_mfma_f32_16x16x32_bf16 v[56:59], v[124:127], v[16:19], v[56:59]\n"
    "s_mul_i32 s73, s73, s56\n"
    "v_mfma_f32_16x16x32_bf16 v[60:63], v[128:131], v[16:19], v[60:63]\n"
    "s_waitcnt lgkmcnt(1)\n"
    "s_add_u32 s73, s73, s55\n"
    "v_mfma_f32_16x16x32_bf16 v[72:75], v[132:135], v[16:19], v[72:75]\n"
    "ds_read_b128 v[16:19], v0 offset:15360\n"
    "v_mfma_f32_16x16x32_bf16 v[32:35], v[136:139], v[20:23], v[32:35]\n"
    "v_mfma_f32_16x16x32_bf16 v[36:39], v[140:143], v[20:23], v[36:39]\n"
    "s_waitcnt vmcnt(2)\n"
    "v_add_u32 v13, v11, v3\n"
    "v_mfma_f32_16x16x32_bf16 v[48:51], v[144:147], v[20:23], v[48:51]\n"
    "v_cmp_gt_i32 vcc, s71, v13\n"
    "v_mfma_f32_16x16x32_bf16 v[56:59], v[148:151], v[20:23], v[56:59]\n"
    "v_mfma_f32_16x16x32_bf16 v[60:63], v[152:155], v[20:23], v[60:63]\n"
    "s_waitcnt lgkmcnt(1)\n"
    "v_mfma_f32_16x16x32_bf16 v[72:75], v[156:159], v[20:23], v[72:75]\n"
    "ds_read_b128 v[20:23], v0 offset:16384\n"
    "s_cbranch_vccz .LgUP_dma0_84_%=\n"
    "v_mfma_f32_16x16x32_bf16 v[32:35], v[160:163], v[24:27], v[32:35]\n"
    "s_or_b64 exec, vcc, s[62:63]\n"
    "buffer_atomic_smax v11, v2, s[36:39], 0 offen sc0 sc1\n"
    "s_mov_b64 exec, -1\n"
    "v_mfma_f32_16x16x32_bf16 v[36:39], v[164:167], v[24:27], v[36:39]\n"
    "v_mfma_f32_16x16x32_bf16 v[48:51], v[168:171], v[24:27], v[48:51]\n"
    "v_mfma_f32_16x16x32_bf16 v[56:59], v[172:175], v[24:27], v[56:59]\n"
    "v_mfma_f32_16x16x32_bf16 v[60:63], v[176:179], v[24:27], v[60:63]\n"
    "s_waitcnt lgkmcnt(1)\n"
    "v_mfma_f32_16x16x32_bf16 v[72:75], v[180:183], v[24:27], v[72:75]\n"
    "ds_read_b128 v[24:27], v0 offset:17408\n"
    "v_mfma_f32_16x16x32_bf16 v[32:35], v[184:187], v[16:19], v[32:35]\n"
    "v_mfma_f32_16x16x32_bf16 v[36:39], v[188:191], v[16:19], v[36:39]\n"
    "v_mfma_f32_16x16x32_bf16 v[48:51], v[192:195], v[16:19], v[48:51]\n"
    "v_mfma_f32_16x16x32_bf16 v[56:59], v[196:199], v[16:19], v[56:59]\n"
    "v_mfma_f32_16x16x32_bf16 v[60:63], v[200:203], v[16:19], v[60:63]\n"
    "s_waitcnt lgkmcnt(1)\n"
    "v_mfma_f32_16x16x32_bf16 v[72:75], v[204:207], v[16:19], v[72:75]\n"
    "v_mfma_f32_16x16x32_bf16 v[32:35], v[208:211], v[20:23], v[32:35]\n"
    "v_mfma_f32_16x16x32_bf16 v[36:39], v[212:215], v[20:23], v[36:39]\n"
    "s_waitcnt vmcnt(2)\n"
    "v_add_u32 v13, v5, v3\n"
    "v_mfma_f32_16x16x32_bf16 v[48:51], v[216:219], v[20:23], v[48:51]\n"
    "v_cmp_gt_i32 vcc, s71, v13\n"
    "v_mfma_f32_16x16x32_bf16 v[56:59], v[220:223], v[20:23], v[56:59]\n"
    "v_mfma_f32_16x16x32_bf16 v[60:63], v[224:227], v[20:23], v[60:63]\n"
    "s_waitcnt lgkmcnt(0)\n"
    "v_mfma_f32_16x16x32_bf16 v[72:75], v[228:231], v[20:23], v[72:75]\n"
    "s_cbranch_vccz .LgUP_dma0_102_%=\n"
    "v_mfma_f32_16x16x32_bf16 v[32:35], v[232:235], v[24:27], v[32:35]\n"
    "s_or_b64 exec, vcc, s[62:63]\n"
    "buffer_atomic_smax v5, v2, s[36:39], 0 offen sc0 sc1\n"
    "s_mov_b64 exec, -1\n"
    "v_mfma_f32_16x16x32_bf16 v[36:39], v[236:239], v[24:27], v[36:39]\n"
    "v_mfma_f32_16x16x32_bf16 v[48:51], v[240:243], v[24:27], v[48:51]\n"
    "s_waitcnt vmcnt(2)\n"
    "buffer_atomic_smax v12, v10, s[36:39], s70 offen sc1\n"
    "v_mfma_f32_16x16x32_bf16 v[56:59], v[244:247], v[24:27], v[56:59]\n"
    "v_mfma_f32_16x16x32_bf16 v[60:63], v[248:251], v[24:27], v[60:63]\n"
    "v_mfma_f32_16x16x32_bf16 v[72:75], v[252:255], v[24:27], v[72:75]\n"
    "v_pk_mul_f32 v[56:57], v[32:33], v[56:57]\n"
    "v_pk_mul_f32 v[58:59], v[34:35], v[58:59]\n"
    "s_nop 1\n"
    "v_pk_mul_f32 v[60:61], v[36:37], v[60:61]\n"
    "v_pk_mul_f32 v[62:63], v[38:39], v[62:63]\n"
    "v_pk_mul_f32 v[32:33], v[32:33], s[64:65] op_sel_hi:[1,0]\n"
    "v_pk_mul_f32 v[34:35], v[34:35], s[64:65] op_sel_hi:[1,0]\n"
    "v_pk_mul_f32 v[36:37], v[36:37], s[64:65] op_sel_hi:[1,0]\n"
    "v_pk_mul_f32 v[38:39], v[38:39], s[64:65] op_sel_hi:[1,0]\n"
    "v_exp_f32 v32, v32\n"
    "v_exp_f32 v33, v33\n"
    "v_exp_f32 v34, v34\n"
    "v_exp_f32 v35, v35\n"
    "v_exp_f32 v36, v36\n"
    "v_exp_f32 v37, v37\n"
    "v_exp_f32 v38, v38\n"
    "v_exp_f32 v39, v39\n"
    "v_pk_add_f32 v[32:33], v[32:33], 1.0 op_sel_hi:[1,0]\n"
    "v_pk_add_f32 v[34:35], v[34:35], 1.0 op_sel_hi:[1,0]\n"
    "v_pk_add_f32 v[36:37], v[36:37], 1.0 op_sel_hi:[1,0]\n"
    "v_pk_add_f32 v[38:39], v[38:39], 1.0 op_sel_hi:[1,0]\n"
    "v_rcp_f32 v32, v32\n"
    "v_rcp_f32 v33, v33\n"
    "v_rcp_f32 v34, v34\n"
    "v_rcp_f32 v35, v35\n"
    "v_rcp_f32 v36, v36\n"
    "v_rcp_f32 v37, v37\n"
    "v_rcp_f32 v38, v38\n"
    "v_rcp_f32 v39, v39\n"
    "v_pk_mul_f32 v[32:33], v[32:33], v[56:57]\n"
    "v_pk_mul_f32 v[34:35], v[34:35], v[58:59]\n"
    "v_pk_mul_f32 v[36:37], v[36:37], v[60:61]\n"
    "v_pk_mul_f32 v[38:39], v[38:39], v[62:63]\n"
    "v_cvt_pk_bf16_f32 v32, v32, v33\n"
    "v_cvt_pk_bf16_f32 v33, v34, v35\n"
    "v_cvt_pk_bf16_f32 v34, v36, v37\n"
    "v_cvt_pk_bf16_f32 v35, v38, v39\n"
    "buffer_store_dwordx4 v[32:35], v15, s[36:39], s73 offen sc1\n"
    "v_pk_mul_f32 v[72:73], v[48:49], v[72:73]\n"
    "v_pk_mul_f32 v[74:75], v[50:51], v[74:75]\n"
    "v_pk_mul_f32 v[48:49], v[48:49], s[64:65] op_sel_hi:[1,0]\n"
    "v_pk_mul_f32 v[50:51], v[50:51], s[64:65] op_sel_hi:[1,0]\n"
    "v_exp_f32 v48, v48\n"
    "v_exp_f32 v49, v49\n"
    "v_exp_f32 v50, v50\n"
    "v_exp_f32 v51, v51\n"
    "v_pk_add_f32 v[48:49], v[48:49], 1.0 op_sel_hi:[1,0]\n"
    "v_pk_add_f32 v[50:51], v[50:51], 1.0 op_sel_hi:[1,0]\n"
    "v_rcp_f32 v48, v48\n"
    "v_rcp_f32 v49, v49\n"
    "v_rcp_f32 v50, v50\n"
    "v_rcp_f32 v51, v51\n"
    "v_pk_mul_f32 v[48:49], v[48:49], v[72:73]\n"
    "v_pk_mul_f32 v[50:51], v[50:51], v[74:75]\n"
    "v_cvt_pk_bf16_f32 v48, v48, v49\n"
    "v_cvt_pk_bf16_f32 v49, v50, v51\n"
    "s_nop 0\n"
    "v_permlane16_swap_b32 v48, v50\n"
    "v_permlane16_swap_b32 v49, v51\n"
    "buffer_store_dwordx4 v[48:51], v4, s[36:39], s73 offen sc1\n"
    "v_mov_b32 v12, s68\n"
    "s_mov_b32 s73, 0x80000000\n"
    "s_mov_b32 s68, s71\n"
    "s_waitcnt vmcnt(4)\n"
    "v_add_u32 v13, v11, v3\n"
    "v_cmp_gt_i32 vcc, s71, v13\n"
    "s_cbranch_vccnz .LgUP_idle0_%=\n"
    "s_add_u32 m0, s59, 20480\n"
    "s_nop 0\n"
    "buffer_load_dwordx4 v1, s[36:39], s76 offen sc1 lds\n"
    "s_add_u32 m0, s59, 24576\n"
    "s_add_u32 s76, s76, 16384\n"
    "buffer_load_dwordx4 v1, s[36:39], s76 offen sc1 lds\n"
    "s_add_u32 m0, s59, 28672\n"
    "s_add_u32 s76, s76, 16384\n"
    "buffer_load_dwordx4 v1, s[36:39], s76 offen sc1 lds\n"
    "s_add_u32 m0, s59, 32768\n"
    "s_add_u32 s76, s76, 16384\n"
    "buffer_load_dwordx4 v1, s[36:39], s76 offen sc1 lds\n"
    "s_add_u32 m0, s59, 36864\n"
    "s_add_u32 s76, s76, 16384\n"
    "buffer_load_dwordx4 v8, s[36:39], s76 offen sc1 lds\n"
    "s_waitcnt vmcnt(5)\n"
    "buffer_atomic_smax v12, v14, s[36:39], 0 offen sc1\n"
    "s_mov_b32 s69, 0x80000000\n"
    "s_mov_b32 s70, 0x80000000\n"
    "v_mov_b32 v7, v12\n"
    "s_waitcnt vmcnt(9)\n"
    "s_add_u32 s71, s68, 1\n"
    "v_add_u32 v13, v11, v3\n"
    "v_cmp_gt_i32 vcc, s71, v13\n"
    "s_nop 0\n"
    "s_or_b64 exec, vcc, s[62:63]\n"
    "buffer_atomic_smax v11, v2, s[36:39], 0 offen sc0 sc1\n"
    "s_mov_b64 exec, -1\n"
    "s_waitcnt vmcnt(2)\n"
    "s_branch .LgUP_bar1_%=\n"
    ".LgUP_dma0_30_%=:\n"
    "v_mfma_f32_16x16x32_bf16 v[32:35], a[120:123], v[24:27], v[32:35]\n"
    "s_add_u32 m0, s59, 20480\n"
    "v_mfma_f32_16x16x32_bf16 v[36:39], a[124:127], v[24:27], v[36:39]\n"
    "buffer_load_dwordx4 v1, s[36:39], s76 offen sc1 lds\n"
    "v_rcp_f32 v44, v44\n"
    "v_mfma_f32_16x16x32_bf16 v[48:51], a[128:131], v[24:27], v[48:51]\n"
    "s_add_u32 s76, s76, 16384\n"
    "v_add_f32 v45, 1.0, v45\n"
    "v_add_f32 v46, 1.0, v46\n"
    "v_mfma_f32_16x16x32_bf16 v[56:59], a[132:135], v[24:27], v[56:59]\n"
    "s_add_u32 m0, s59, 24576\n"
    "v_rcp_f32 v45, v45\n"
    "v_mfma_f32_16x16x32_bf16 v[60:63], a[136:139], v[24:27], v[60:63]\n"
    "s_waitcnt lgkmcnt(1)\n"
    "v_rcp_f32 v46, v46\n"
    "v_mfma_f32_16x16x32_bf16 v[72:75], a[140:143], v[24:27], v[72:75]\n"
    "ds_read_b128 v[24:27], v0 offset:8192\n"
    "v_add_f32 v47, 1.0, v47\n"
    "v_mfma_f32_16x16x32_bf16 v[32:35], a[144:147], v[16:19], v[32:35]\n"
    "buffer_load_dwordx4 v1, s[36:39], s76 offen sc1 lds\n"
    "v_rcp_f32 v47, v47\n"
    "v_mfma_f32_16x16x32_bf16 v[36:39], a[148:151], v[16:19], v[36:39]\n"
    "s_add_u32 s76, s76, 16384\n"
    "v_mul_f32 v40, v40, v64\n"
    "v_mul_f32 v41, v41, v65\n"
    "v_mfma_f32_16x16x32_bf16 v[48:51], a[152:155], v[16:19], v[48:51]\n"
    "s_add_u32 m0, s59, 28672\n"
    "v_mul_f32 v42, v42, v66\n"
    "v_mul_f32 v43, v43, v67\n"
    "v_mfma_f32_16x16x32_bf16 v[56:59], a[156:159], v[16:19], v[56:59]\n"
    "buffer_load_dwordx4 v1, s[36:39], s76 offen sc1 lds\n"
    "v_mul_f32 v44, v44, v68\n"
    "v_mul_f32 v45, v45, v69\n"
    "v_mfma_f32_16x16x32_bf16 v[60:63], a[160:163], v[16:19], v[60:63]\n"
    "s_waitcnt lgkmcnt(1)\n"
    "v_mul_f32 v46, v46, v70\n"
    "v_mul_f32 v47, v47, v71\n"
    "v_mfma_f32_16x16x32_bf16 v[72:75], a[164:167], v[16:19], v[72:75]\n"
    "ds_read_b128 v[16:19], v0 offset:9216\n"
    "v_cvt_pk_bf16_f32 v40, v40, v41\n"
    "v_mfma_f32_16x16x32_bf16 v[32:35], a[168:171], v[20:23], v[32:35]\n"
    "s_add_u32 s76, s76, 16384\n"
    "v_cvt_pk_bf16_f32 v41, v42, v43\n"
    "v_cvt_pk_bf16_f32 v42, v44, v45\n"
    "v_mfma_f32_16x16x32_bf16 v[36:39], a[172:175], v[20:23], v[36:39]\n"
    "s_add_u32 m0, s59, 32768\n"
    "v_cvt_pk_bf16_f32 v43, v46, v47\n"
    "v_mfma_f32_16x16x32_bf16 v[48:51], a[176:179], v[20:23], v[48:51]\n"
    "buffer_load_dwordx4 v1, s[36:39], s76 offen sc1 lds\n"
    "s_add_u32 s76, s76, 16384\n"
    "v_mul_f32 v76, v52, v76\n"
    "v_mfma_f32_16x16x32_bf16 v[56:59], a[180:183], v[20:23], v[56:59]\n"
    "buffer_store_dwordx4 v[40:43], v15, s[36:39], s73 offen offset:1024 sc1\n"
    "v_mfma_f32_16x16x32_bf16 v[60:63], a[184:187], v[20:23], v[60:63]\n"
    "s_waitcnt lgkmcnt(1)\n"
    "s_add_u32 m0, s59, 36864\n"
    "v_mul_f32 v77, v53, v77\n"
    "v_mul_f32 v78, v54, v78\n"
    "v_mfma_f32_16x16x32_bf16 v[72:75], a[188:191], v[20:23], v[72:75]\n"
    "ds_read_b128 v[20:23], v0 offset:10240\n"
    "buffer_load_dwordx4 v8, s[36:39], s76 offen sc1 lds\n"
    "v_mfma_f32_16x16x32_bf16 v[32:35], a[192:195], v[24:27], v[32:35]\n"
    "v_mfma_f32_16x16x32_bf16 v[36:39], a[196:199], v[24:27], v[36:39]\n"
    "v_mul_f32 v79, v55, v79\n"
    "v_mul_f32 v52, 0xbfb8aa3b, v52\n"
    "v_mfma_f32_16x16x32_bf16 v[48:51], a[200:203], v[24:27], v[48:51]\n"
    "v_exp_f32 v52, v52\n"
    "v_mfma_f32_16x16x32_bf16 v[56:59], a[204:207], v[24:27], v[56:59]\n"
    "v_mul_f32 v53, 0xbfb8aa3b, v53\n"
    "v_mul_f32 v54, 0xbfb8aa3b, v54\n"
    "v_mfma_f32_16x16x32_bf16 v[60:63], a[208:211], v[24:27], v[60:63]\n"
    "s_waitcnt lgkmcnt(1)\n"
    "v_exp_f32 v53, v53\n"
    "v_mfma_f32_16x16x32_bf16 v[72:75], a[212:215], v[24:27], v[72:75]\n"
    "ds_read_b128 v[24:27], v0 offset:11264\n"
    "v_mul_f32 v55, 0xbfb8aa3b, v55\n"
    "v_mfma_f32_16x16x32_bf16 v[32:35], a[216:219], v[16:19], v[32:35]\n"
    "v_exp_f32 v54, v54\n"
    "v_mfma_f32_16x16x32_bf16 v[36:39], a[220:223], v[16:19], v[36:39]\n"
    "v_exp_f32 v55, v55\n"
    "v_mfma_f32_16x16x32_bf16 v[48:51], a[224:227], v[16:19], v[48:51]\n"
    "v_add_f32 v52, 1.0, v52\n"
    "v_add_f32 v53, 1.0, v53\n"
    "v_mfma_f32_16x16x32_bf16 v[56:59], a[228:231], v[16:19], v[56:59]\n"
    "v_rcp_f32 v52, v52\n"
    "v_mfma_f32_16x16x32_bf16 v[60:63], a[232:235], v[16:19], v[60:63]\n"
    "s_waitcnt lgkmcnt(1)\n"
    "v_rcp_f32 v53, v53\n"
    "v_mfma_f32_16x16x32_bf16 v[72:75], a[236:239], v[16:19], v[72:75]\n"
    "ds_read_b128 v[16:19], v0 offset:12288\n"
    "v_add_f32 v54, 1.0, v54\n"
    "v_mfma_f32_16x16x32_bf16 v[32:35], a[240:243], v[20:23], v[32:35]\n"
    "v_rcp_f32 v54, v54\n"
    "v_mfma_f32_16x16x32_bf16 v[36:39], a[244:247], v[20:23], v[36:39]\n"
    "v_add_f32 v55, 1.0, v55\n"
    "v_mfma_f32_16x16x32_bf16 v[48:51], a[248:251], v[20:23], v[48:51]\n"
    "v_mul_f32 v52, v52, v76\n"
    "v_mfma_f32_16x16x32_bf16 v[56:59], a[252:255], v[20:23], v[56:59]\n"
    "v_rcp_f32 v55, v55\n"
    "v_mfma_f32_16x16x32_bf16 v[60:63], v[80:83], v[20:23], v[60:63]\n"
    "s_waitcnt lgkmcnt(1)\n"
    "v_mul_f32 v53, v53, v77\n"
    "v_mul_f32 v54, v54, v78\n"
    "v_mfma_f32_16x16x32_bf16 v[72:75], v[84:87], v[20:23], v[72:75]\n"
    "ds_read_b128 v[20:23], v0 offset:13312\n"
    "v_mfma_f32_16x16x32_bf16 v[32:35], v[88:91], v[24:27], v[32:35]\n"
    "v_mfma_f32_16x16x32_bf16 v[36:39], v[92:95], v[24:27], v[36:39]\n"
    "v_mul_f32 v55, v55, v79\n"
    "v_cvt_pk_bf16_f32 v52, v52, v53\n"
    "v_mfma_f32_16x16x32_bf16 v[48:51], v[96:99], v[24:27], v[48:51]\n"
    "v_cvt_pk_bf16_f32 v53, v54, v55\n"
    "s_nop 0\n"
    "v_mfma_f32_16x16x32_bf16 v[56:59], v[100:103], v[24:27], v[56:59]\n"
    "v_permlane16_swap_b32 v52, v54\n"
    "v_mfma_f32_16x16x32_bf16 v[60:63], v[104:107], v[24:27], v[60:63]\n"
    "s_waitcnt lgkmcnt(1)\n"
    "v_permlane16_swap_b32 v53, v55\n"
    "v_mfma_f32_16x16x32_bf16 v[72:75], v[108:111], v[24:27], v[72:75]\n"
    "ds_read_b128 v[24:27], v0 offset:14336\n"
    "s_branch .LgUP_join0_%=\n"
    ".LgUP_dma0_48_%=:\n"
    "v_mfma_f32_16x16x32_bf16 v[32:35], a[192:195], v[24:27], v[32:35]\n"
    "s_add_u32 m0, s59, 20480\n"
    "v_mfma_f32_16x16x32_bf16 v[36:39], a[196:199], v[24:27], v[36:39]\n"
    "buffer_load_dwordx4 v1, s[36:39], s76 offen sc1 lds\n"
    "v_mul_f32 v79, v55, v79\n"
    "v_mul_f32 v52, 0xbfb8aa3b, v52\n"
    "v_mfma_f32_16x16x32_bf16 v[48:51], a[200:203], v[24:27], v[48:51]\n"
    "s_add_u32 s76, s76, 16384\n"
    "v_exp_f32 v52, v52\n"
    "v_mfma_f32_16x16x32_bf16 v[56:59], a[204:207], v[24:27], v[56:59]\n"
    "s_add_u32 m0, s59, 24576\n"
    "v_mul_f32 v53, 0xbfb8aa3b, v53\n"
    "v_mul_f32 v54, 0xbfb8aa3b, v54\n"
    "v_mfma_f32_16x16x32_bf16 v[60:63], a[208:211], v[24:27], v[60:63]\n"
    "s_waitcnt lgkmcnt(1)\n"
    "v_exp_f32 v53, v53\n"
    "v_mfma_f32_16x16x32_bf16 v[72:75], a[212:215], v[24:27], v[72:75]\n"
    "ds_read_b128 v[24:27], v0 offset:11264\n"
    "v_mul_f32 v55, 0xbfb8aa3b, v55\n"
    "v_mfma_f32_16x16x32_bf16 v[32:35], a[216:219], v[16:19], v[32:35]\n"
    "buffer_load_dwordx4 v1, s[36:39], s76 offen sc1 lds\n"
    "v_exp_f32 v54, v54\n"
    "v_mfma_f32_16x16x32_bf16 v[36:39], a[220:223], v[16:19], v[36:39]\n"
    "s_add_u32 s76, s76, 16384\n"
    "v_exp_f32 v55, v55\n"
    "v_mfma_f32_16x16x32_bf16 v[48:51], a[224:227], v[16:19], v[48:51]\n"
    "s_add_u32 m0, s59, 28672\n"
    "v_add_f32 v52, 1.0, v52\n"
    "v_add_f32 v53, 1.0, v53\n"
    "v_mfma_f32_16x16x32_bf16 v[56:59], a[228:231], v[16:19], v[56:59]\n"
    "buffer_load_dwordx4 v1, s[36:39], s76 offen sc1 lds\n"
    "v_rcp_f32 v52, v52\n"
    "v_mfma_f32_16x16x32_bf16 v[60:63], a[232:235], v[16:19], v[60:63]\n"
    "s_waitcnt lgkmcnt(1)\n"
    "v_rcp_f32 v53, v53\n"
    "v_mfma_f32_16x16x32_bf16 v[72:75], a[236:239], v[16:19], v[72:75]\n"
    "ds_read_b128 v[16:19], v0 offset:12288\n"
    "v_add_f32 v54, 1.0, v54\n"
    "v_mfma_f32_16x16x32_bf16 v[32:35], a[240:243], v[20:23], v[32:35]\n"
    "s_add_u32 s76, s76, 16384\n"
    "v_rcp_f32 v54, v54\n"
    "v_mfma_f32_16x16x32_bf16 v[36:39], a[244:247], v[20:23], v[36:39]\n"
    "s_add_u32 m0, s59, 32768\n"
    "v_add_f32 v55, 1.0, v55\n"
    "v_mfma_f32_16x16x32_bf16 v[48:51], a[248:251], v[20:23], v[48:51]\n"
    "buffer_load_dwordx4 v1, s[36:39], s76 offen sc1 lds\n"
    "s_add_u32 s76, s76, 16384\n"
    "v_mul_f32 v52, v52, v76\n"
    "v_mfma_f32_16x16x32_bf16 v[56:59], a[252:255], v[20:23], v[56:59]\n"
    "s_add_u32 m0, s59, 36864\n"
    "v_rcp_f32 v55, v55\n"
    "v_mfma_f32_16x16x32_bf16 v[60:63], v[80:83], v[20:23], v[60:63]\n"
    "s_waitcnt lgkmcnt(1)\n"
    "v_mul_f32 v53, v53, v77\n"
    "v_mul_f32 v54, v54, v78\n"
    "v_mfma_f32_16x16x32_bf16 v[72:75], v[84:87], v[20:23], v[72:75]\n"
    "ds_read_b128 v[20:23], v0 offset:13312\n"
    "buffer_load_dwordx4 v8, s[36:39], s76 offen sc1 lds\n"
    "v_mfma_f32_16x16x32_bf16 v[32:35], v[88:91], v[24:27], v[32:35]\n"
    "v_mfma_f32_16x16x32_bf16 v[36:39], v[92:95], v[24:27], v[36:39]\n"
    "v_mul_f32 v55, v55, v79\n"
    "v_cvt_pk_bf16_f32 v52, v52, v53\n"
    "v_mfma_f32_16x16x32_bf16 v[48:51], v[96:99], v[24:27], v[48:51]\n"
    "v_cvt_pk_bf16_f32 v53, v54, v55\n"
    "s_nop 0\n"
    "v_mfma_f32_16x16x32_bf16 v[56:59], v[100:103], v[24:27], v[56:59]\n"
    "v_permlane16_swap_b32 v52, v54\n"
    "v_mfma_f32_16x16x32_bf16 v[60:63], v[104:107], v[24:27], v[60:63]\n"
    "s_waitcnt lgkmcnt(1)\n"
    "v_permlane16_swap_b32 v53, v55\n"
    "v_mfma_f32_16x16x32_bf16 v[72:75], v[108:111], v[24:27], v[72:75]\n"
    "ds_read_b128 v[24:27], v0 offset:14336\n"
    "s_branch .LgUP_join0_%=\n"
    ".LgUP_dma0_66_%=:\n"
    "v_mfma_f32_16x16x32_bf16 v[32:35], v[88:91], v[24:27], v[32:35]\n"
    "s_add_u32 m0, s59, 20480\n"
    "v_mfma_f32_16x16x32_bf16 v[36:39], v[92:95], v[24:27], v[36:39]\n"
    "buffer_load_dwordx4 v1, s[36:39], s76 offen sc1 lds\n"
    "v_mul_f32 v55, v55, v79\n"
    "v_cvt_pk_bf16_f32 v52, v52, v53\n"
    "v_mfma_f32_16x16x32_bf16 v[48:51], v[96:99], v[24:27], v[48:51]\n"
    "s_add_u32 s76, s76, 16384\n"
    "v_cvt_pk_bf16_f32 v53, v54, v55\n"
    "s_nop 0\n"
    "v_mfma_f32_16x16x32_bf16 v[56:59], v[100:103], v[24:27], v[56:59]\n"
    "s_add_u32 m0, s59, 24576\n"
    "v_permlane16_swap_b32 v52, v54\n"
    "v_mfma_f32_16x16x32_bf16 v[60:63], v[104:107], v[24:27], v[60:63]\n"
    "s_waitcnt lgkmcnt(1)\n"
    "v_permlane16_swap_b32 v53, v55\n"
    "v_mfma_f32_16x16x32_bf16 v[72:75], v[108:111], v[24:27], v[72:75]\n"
    "ds_read_b128 v[24:27], v0 offset:14336\n"
    "buffer_load_dwordx4 v1, s[36:39], s76 offen sc1 lds\n"
    "v_mfma_f32_16x16x32_bf16 v[32:35], v[112:115], v[16:19], v[32:35]\n"
    "buffer_store_dwordx4 v[52:55], v4, s[36:39], s73 offen offset:1024 sc1\n"
    "v_mfma_f32_16x16x32_bf16 v[36:39], v[116:119], v[16:19], v[36:39]\n"
    "s_add_u32 s76, s76, 16384\n"
    "s_add_u32 m0, s59, 28672\n"
    "s_lshr_b32 s73, s68, 1\n"
    "v_mfma_f32_16x16x32_bf16 v[48:51], v[120:123], v[16:19], v[48:51]\n"
    "buffer_load_dwordx4 v1, s[36:39], s76 offen sc1 lds\n"
    "s_and_b32 s73, s73, s57\n"
    "v_mfma_f32_16x16x32_bf16 v[56:59], v[124:127], v[16:19], v[56:59]\n"
    "s_mul_i32 s73, s73, s56\n"
    "v_mfma_f32_16x16x32_bf16 v[60:63], v[128:131], v[16:19], v[60:63]\n"
    "s_waitcnt lgkmcnt(1)\n"
    "s_add_u32 s76, s76, 16384\n"
    "s_add_u32 s73, s73, s55\n"
    "v_mfma_f32_16x16x32_bf16 v[72:75], v[132:135], v[16:19], v[72:75]\n"
    "ds_read_b128 v[16:19], v0 offset:15360\n"
    "s_add_u32 m0, s59, 32768\n"
    "v_mfma_f32_16x16x32_bf16 v[32:35], v[136:139], v[20:23], v[32:35]\n"
    "buffer_load_dwordx4 v1, s[36:39], s76 offen sc1 lds\n"
    "v_mfma_f32_16x16x32_bf16 v[36:39], v[140:143], v[20:23], v[36:39]\n"
    "v_mfma_f32_16x16x32_bf16 v[48:51], v[144:147], v[20:23], v[48:51]\n"
    "s_add_u32 s76, s76, 16384\n"
    "s_add_u32 m0, s59, 36864\n"
    "v_mfma_f32_16x16x32_bf16 v[56:59], v[148:151], v[20:23], v[56:59]\n"
    "buffer_load_dwordx4 v8, s[36:39], s76 offen sc1 lds\n"
    "v_mfma_f32_16x16x32_bf16 v[60:63], v[152:155], v[20:23], v[60:63]\n"
    "s_waitcnt lgkmcnt(1)\n"
    "v_mfma_f32_16x16x32_bf16 v[72:75], v[156:159], v[20:23], v[72:75]\n"
    "ds_read_b128 v[20:23], v0 offset:16384\n"
    "v_mfma_f32_16x16x32_bf16 v[32:35], v[160:163], v[24:27], v[32:35]\n"
    "v_mfma_f32_16x16x32_bf16 v[36:39], v[164:167], v[24:27], v[36:39]\n"
    "v_mfma_f32_16x16x32_bf16 v[48:51], v[168:171], v[24:27], v[48:51]\n"
    "v_mfma_f32_16x16x32_bf16 v[56:59], v[172:175], v[24:27], v[56:59]\n"
    "v_mfma_f32_16x16x32_bf16 v[60:63], v[176:179], v[24:27], v[60:63]\n"
    "s_waitcnt lgkmcnt(1)\n"
    "v_mfma_f32_16x16x32_bf16 v[72:75], v[180:183], v[24:27], v[72:75]\n"
    "ds_read_b128 v[24:27], v0 offset:17408\n"
    "v_mfma_f32_16x16x32_bf16 v[32:35], v[184:187], v[16:19], v[32:35]\n"
    "v_mfma_f32_16x16x32_bf16 v[36:39], v[188:191], v[16:19], v[36:39]\n"
    "v_mfma_f32_16x16x32_bf16 v[48:51], v[192:195], v[16:19], v[48:51]\n"
    "v_mfma_f32_16x16x32_bf16 v[56:59], v[196:199], v[16:19], v[56:59]\n"
    "v_mfma_f32_16x16x32_bf16 v[60:63], v[200:203], v[16:19], v[60:63]\n"
    "s_waitcnt lgkmcnt(1)\n"
    "v_mfma_f32_16x16x32_bf16 v[72:75], v[204:207], v[16:19], v[72:75]\n"
    "v_mfma_f32_16x16x32_bf16 v[32:35], v[208:211], v[20:23], v[32:35]\n"
    "v_mfma_f32_16x16x32_bf16 v[36:39], v[212:215], v[20:23], v[36:39]\n"
    "v_mfma_f32_16x16x32_bf16 v[48:51], v[216:219], v[20:23], v[48:51]\n"
    "v_mfma_f32_16x16x32_bf16 v[56:59], v[220:223], v[20:23], v[56:59]\n"
    "v_mfma_f32_16x16x32_bf16 v[60:63], v[224:227], v[20:23], v[60:63]\n"
    "s_waitcnt lgkmcnt(0)\n"
    "v_mfma_f32_16x16x32_bf16 v[72:75], v[228:231], v[20:23], v[72:75]\n"
    "v_mfma_f32_16x16x32_bf16 v[32:35], v[232:235], v[24:27], v[32:35]\n"
    "v_mfma_f32_16x16x32_bf16 v[36:39], v[236:239], v[24:27], v[36:39]\n"
    "v_mfma_f32_16x16x32_bf16 v[48:51], v[240:243], v[24:27], v[48:51]\n"
    "s_waitcnt vmcnt(3)\n"
    "buffer_atomic_smax v12, v10, s[36:39], s70 offen sc1\n"
    "v_mfma_f32_16x16x32_bf16 v[56:59], v[244:247], v[24:27], v[56:59]\n"
    "v_mfma_f32_16x16x32_bf16 v[60:63], v[248:251], v[24:27], v[60:63]\n"
    "v_mfma_f32_16x16x32_bf16 v[72:75], v[252:255], v[24:27], v[72:75]\n"
    "v_pk_mul_f32 v[56:57], v[32:33], v[56:57]\n"
    "v_pk_mul_f32 v[58:59], v[34:35], v[58:59]\n"
    "s_nop 1\n"
    "v_pk_mul_f32 v[60:61], v[36:37], v[60:61]\n"
    "v_pk_mul_f32 v[62:63], v[38:39], v[62:63]\n"
    "v_pk_mul_f32 v[32:33], v[32:33], s[64:65] op_sel_hi:[1,0]\n"
    "v_pk_mul_f32 v[34:35], v[34:35], s[64:65] op_sel_hi:[1,0]\n"
    "v_pk_mul_f32 v[36:37], v[36:37], s[64:65] op_sel_hi:[1,0]\n"
    "v_pk_mul_f32 v[38:39], v[38:39], s[64:65] op_sel_hi:[1,0]\n"
    "v_exp_f32 v32, v32\n"
    "v_exp_f32 v33, v33\n"
    "v_exp_f32 v34, v34\n"
    "v_exp_f32 v35, v35\n"
    "v_exp_f32 v36, v36\n"
    "v_exp_f32 v37, v37\n"
    "v_exp_f32 v38, v38\n"
    "v_exp_f32 v39, v39\n"
    "v_pk_add_f32 v[32:33], v[32:33], 1.0 op_sel_hi:[1,0]\n"
    "v_pk_add_f32 v[34:35], v[34:35], 1.0 op_sel_hi:[1,0]\n"
    "v_pk_add_f32 v[36:37], v[36:37], 1.0 op_sel_hi:[1,0]\n"
    "v_pk_add_f32 v[38:39], v[38:39], 1.0 op_sel_hi:[1,0]\n"
    "v_rcp_f32 v32, v32\n"
    "v_rcp_f32 v33, v33\n"
    "v_rcp_f32 v34, v34\n"
    "v_rcp_f32 v35, v35\n"
    "v_rcp_f32 v36, v36\n"
    "v_rcp_f32 v37, v37\n"
    "v_rcp_f32 v38, v38\n"
    "v_rcp_f32 v39, v39\n"
    "v_pk_mul_f32 v[32:33], v[32:33], v[56:57]\n"
    "v_pk_mul_f32 v[34:35], v[34:35], v[58:59]\n"
    "v_pk_mul_f32 v[36:37], v[36:37], v[60:61]\n"
    "v_pk_mul_f32 v[38:39], v[38:39], v[62:63]\n"
    "v_cvt_pk_bf16_f32 v32, v32, v33\n"
    "v_cvt_pk_bf16_f32 v33, v34, v35\n"
    "v_cvt_pk_bf16_f32 v34, v36, v37\n"
    "v_cvt_pk_bf16_f32 v35, v38, v39\n"
    "buffer_store_dwordx4 v[32:35], v15, s[36:39], s73 offen sc1\n"
    "v_pk_mul_f32 v[72:73], v[48:49], v[72:73]\n"
    "v_pk_mul_f32 v[74:75], v[50:51], v[74:75]\n"
    "v_pk_mul_f32 v[48:49], v[48:49], s[64:65] op_sel_hi:[1,0]\n"
    "v_pk_mul_f32 v[50:51], v[50:51], s[64:65] op_sel_hi:[1,0]\n"
    "v_exp_f32 v48, v48\n"
    "v_exp_f32 v49, v49\n"
    "v_exp_f32 v50, v50\n"
    "v_exp_f32 v51, v51\n"
    "v_pk_add_f32 v[48:49], v[48:49], 1.0 op_sel_hi:[1,0]\n"
    "v_pk_add_f32 v[50:51], v[50:51], 1.0 op_sel_hi:[1,0]\n"
    "v_rcp_f32 v48, v48\n"
    "v_rcp_f32 v49, v49\n"
    "v_rcp_f32 v50, v50\n"
    "v_rcp_f32 v51, v51\n"
    "v_pk_mul_f32 v[48:49], v[48:49], v[72:73]\n"
    "v_pk_mul_f32 v[50:51], v[50:51], v[74:75]\n"
    "v_cvt_pk_bf16_f32 v48, v48, v49\n"
    "v_cvt_pk_bf16_f32 v49, v50, v51\n"
    "s_nop 0\n"
    "v_permlane16_swap_b32 v48, v50\n"
    "v_permlane16_swap_b32 v49, v51\n"
    "buffer_store_dwordx4 v[48:51], v4, s[36:39], s73 offen sc1\n"
    "v_mov_b32 v12, s68\n"
    "s_mov_b32 s73, 0x80000000\n"
    "s_mov_b32 s68, s71\n"
    "s_mov_b32 s69, 0\n"
    "s_mov_b32 s70, 0x80000000\n"
    "v_mov_b32 v7, v12\n"
    "s_waitcnt vmcnt(9)\n"
    "s_add_u32 s71, s68, 1\n"
    "v_add_u32 v13, v11, v3\n"
    "v_cmp_gt_i32 vcc, s71, v13\n"
    "s_nop 0\n"
    "s_or_b64 exec, vcc, s[62:63]\n"
    "buffer_atomic_smax v11, v2, s[36:39], 0 offen sc0 sc1\n"
    "s_mov_b64 exec, -1\n"
    "s_waitcnt vmcnt(4)\n"
    "s_branch .LgUP_bar1_%=\n"
    ".LgUP_dma0_84_%=:\n"
    "v_mfma_f32_16x16x32_bf16 v[32:35], v[160:163], v[24:27], v[32:35]\n"
    "s_add_u32 m0, s59, 20480\n"
    "v_mfma_f32_16x16x32_bf16 v[36:39], v[164:167], v[24:27], v[36:39]\n"
    "buffer_load_dwordx4 v1, s[36:39], s76 offen sc1 lds\n"
    "v_mfma_f32_16x16x32_bf16 v[48:51], v[168:171], v[24:27], v[48:51]\n"
    "s_add_u32 s76, s76, 16384\n"
    "s_add_u32 m0, s59, 24576\n"
    "v_mfma_f32_16x16x32_bf16 v[56:59], v[172:175], v[24:27], v[56:59]\n"
    "buffer_load_dwordx4 v1, s[36:39], s76 offen sc1 lds\n"
    "v_mfma_f32_16x16x32_bf16 v[60:63], v[176:179], v[24:27], v[60:63]\n"
    "s_waitcnt lgkmcnt(1)\n"
    "v_mfma_f32_16x16x32_bf16 v[72:75], v[180:183], v[24:27], v[72:75]\n"
    "ds_read_b128 v[24:27], v0 offset:17408\n"
    "v_mfma_f32_16x16x32_bf16 v[32:35], v[184:187], v[16:19], v[32:35]\n"
    "s_add_u32 s76, s76, 16384\n"
    "s_add_u32 m0, s59, 28672\n"
    "v_mfma_f32_16x16x32_bf16 v[36:39], v[188:191], v[16:19], v[36:39]\n"
    "buffer_load_dwordx4 v1, s[36:39], s76 offen sc1 lds\n"
    "v_mfma_f32_16x16x32_bf16 v[48:51], v[192:195], v[16:19], v[48:51]\n"
    "v_mfma_f32_16x16x32_bf16 v[56:59], v[196:199], v[16:19], v[56:59]\n"
    "v_mfma_f32_16x16x32_bf16 v[60:63], v[200:203], v[16:19], v[60:63]\n"
    "s_waitcnt lgkmcnt(1)\n"
    "s_add_u32 s76, s76, 16384\n"
    "s_add_u32 m0, s59, 32768\n"
    "v_mfma_f32_16x16x32_bf16 v[72:75], v[204:207], v[16:19], v[72:75]\n"
    "buffer_load_dwordx4 v1, s[36:39], s76 offen sc1 lds\n"
    "v_mfma_f32_16x16x32_bf16 v[32:35], v[208:211], v[20:23], v[32:35]\n"
    "v_mfma_f32_16x16x32_bf16 v[36:39], v[212:215], v[20:23], v[36:39]\n"
    "v_mfma_f32_16x16x32_bf16 v[48:51], v[216:219], v[20:23], v[48:51]\n"
    "s_add_u32 s76, s76, 16384\n"
    "s_add_u32 m0, s59, 36864\n"
    "v_mfma_f32_16x16x32_bf16 v[56:59], v[220:223], v[20:23], v[56:59]\n"
    "buffer_load_dwordx4 v8, s[36:39], s76 offen sc1 lds\n"
    "v_mfma_f32_16x16x32_bf16 v[60:63], v[224:227], v[20:23], v[60:63]\n"
    "s_waitcnt lgkmcnt(0)\n"
    "v_mfma_f32_16x16x32_bf16 v[72:75], v[228:231], v[20:23], v[72:75]\n"
    "v_mfma_f32_16x16x32_bf16 v[32:35], v[232:235], v[24:27], v[32:35]\n"
    "v_mfma_f32_16x16x32_bf16 v[36:39], v[236:239], v[24:27], v[36:39]\n"
    "v_mfma_f32_16x16x32_bf16 v[48:51], v[240:243], v[24:27], v[48:51]\n"
    "s_waitcnt vmcnt(5)\n"
    "buffer_atomic_smax v12, v10, s[36:39], s70 offen sc1\n"
    "v_mfma_f32_16x16x32_bf16 v[56:59], v[244:247], v[24:27], v[56:59]\n"
    "v_mfma_f32_16x16x32_bf16 v[60:63], v[248:251], v[24:27], v[60:63]\n"
    "v_mfma_f32_16x16x32_bf16 v[72:75], v[252:255], v[24:27], v[72:75]\n"
    "v_pk_mul_f32 v[56:57], v[32:33], v[56:57]\n"
    "v_pk_mul_f32 v[58:59], v[34:35], v[58:59]\n"
    "s_nop 1\n"
    "v_pk_mul_f32 v[60:61], v[36:37], v[60:61]\n"
    "v_pk_mul_f32 v[62:63], v[38:39], v[62:63]\n"
    "v_pk_mul_f32 v[32:33], v[32:33], s[64:65] op_sel_hi:[1,0]\n"
    "v_pk_mul_f32 v[34:35], v[34:35], s[64:65] op_sel_hi:[1,0]\n"
    "v_pk_mul_f32 v[36:37], v[36:37], s[64:65] op_sel_hi:[1,0]\n"
    "v_pk_mul_f32 v[38:39], v[38:39], s[64:65] op_sel_hi:[1,0]\n"
    "v_exp_f32 v32, v32\n"
    "v_exp_f32 v33, v33\n"
    "v_exp_f32 v34, v34\n"
    "v_exp_f32 v35, v35\n"
    "v_exp_f32 v36, v36\n"
    "v_exp_f32 v37, v37\n"
    "v_exp_f32 v38, v38\n"
    "v_exp_f32 v39, v39\n"
    "v_pk_add_f32 v[32:33], v[32:33], 1.0 op_sel_hi:[1,0]\n"
    "v_pk_add_f32 v[34:35], v[34:35], 1.0 op_sel_hi:[1,0]\n"
    "v_pk_add_f32 v[36:37], v[36:37], 1.0 op_sel_hi:[1,0]\n"
    "v_pk_add_f32 v[38:39], v[38:39], 1.0 op_sel_hi:[1,0]\n"
    "v_rcp_f32 v32, v32\n"
    "v_rcp_f32 v33, v33\n"
    "v_rcp_f32 v34, v34\n"
    "v_rcp_f32 v35, v35\n"
    "v_rcp_f32 v36, v36\n"
    "v_rcp_f32 v37, v37\n"
    "v_rcp_f32 v38, v38\n"
    "v_rcp_f32 v39, v39\n"
    "v_pk_mul_f32 v[32:33], v[32:33], v[56:57]\n"
    "v_pk_mul_f32 v[34:35], v[34:35], v[58:59]\n"
    "v_pk_mul_f32 v[36:37], v[36:37], v[60:61]\n"
    "v_pk_mul_f32 v[38:39], v[38:39], v[62:63]\n"
    "v_cvt_pk_bf16_f32 v32, v32, v33\n"
    "v_cvt_pk_bf16_f32 v33, v34, v35\n"
    "v_cvt_pk_bf16_f32 v34, v36, v37\n"
    "v_cvt_pk_bf16_f32 v35, v38, v39\n"
    "buffer_store_dwordx4 v[32:35], v15, s[36:39], s73 offen sc1\n"
    "v_pk_mul_f32 v[72:73], v[48:49], v[72:73]\n"
    "v_pk_mul_f32 v[74:75], v[50:51], v[74:75]\n"
    "v_pk_mul_f32 v[48:49], v[48:49], s[64:65] op_sel_hi:[1,0]\n"
    "v_pk_mul_f32 v[50:51], v[50:51], s[64:65] op_sel_hi:[1,0]\n"
    "v_exp_f32 v48, v48\n"
    "v_exp_f32 v49, v49\n"
    "v_exp_f32 v50, v50\n"
    "v_exp_f32 v51, v51\n"
    "v_pk_add_f32 v[48:49], v[48:49], 1.0 op_sel_hi:[1,0]\n"
    "v_pk_add_f32 v[50:51], v[50:51], 1.0 op_sel_hi:[1,0]\n"
    "v_rcp_f32 v48, v48\n"
    "v_rcp_f32 v49, v49\n"
    "v_rcp_f32 v50, v50\n"
    "v_rcp_f32 v51, v51\n"
    "v_pk_mul_f32 v[48:49], v[48:49], v[72:73]\n"
    "v_pk_mul_f32 v[50:51], v[50:51], v[74:75]\n"
    "v_cvt_pk_bf16_f32 v48, v48, v49\n"
    "v_cvt_pk_bf16_f32 v49, v50, v51\n"
    "s_nop 0\n"
    "v_permlane16_swap_b32 v48, v50\n"
    "v_permlane16_swap_b32 v49, v51\n"
    "buffer_store_dwordx4 v[48:51], v4, s[36:39], s73 offen sc1\n"
    "v_mov_b32 v12, s68\n"
    "s_mov_b32 s73, 0x80000000\n"
    "s_mov_b32 s68, s71\n"
    "s_mov_b32 s69, 0\n"
    "s_mov_b32 s70, 0x80000000\n"
    "v_mov_b32 v7, v12\n"
    "s_waitcnt vmcnt(9)\n"
    "s_add_u32 s71, s68, 1\n"
    "v_add_u32 v13, v11, v3\n"
    "v_cmp_gt_i32 vcc, s71, v13\n"
    "s_nop 0\n"
    "s_or_b64 exec, vcc, s[62:63]\n"
    "buffer_atomic_smax v11, v2, s[36:39], 0 offen sc0 sc1\n"
    "s_mov_b64 exec, -1\n"
    "s_waitcnt vmcnt(4)\n"
    "s_branch .LgUP_bar1_%=\n"
    ".LgUP_dma0_102_%=:\n"
    "v_mfma_f32_16x16x32_bf16 v[32:35], v[232:235], v[24:27], v[32:35]\n"
    "s_add_u32 m0, s59, 20480\n"
    "v_mfma_f32_16x16x32_bf16 v[36:39], v[236:239], v[24:27], v[36:39]\n"
    "buffer_load_dwordx4 v1, s[36:39], s76 offen sc1 lds\n"
    "v_mfma_f32_16x16x32_bf16 v[48:51], v[240:243], v[24:27], v[48:51]\n"
    "s_add_u32 s76, s76, 16384\n"
    "s_waitcnt vmcnt(2)\n"
    "buffer_atomic_smax v12, v10, s[36:39], s70 offen sc1\n"
    "v_mfma_f32_16x16x32_bf16 v[56:59], v[244:247], v[24:27], v[56:59]\n"
    "s_add_u32 m0, s59, 24576\n"
    "v_mfma_f32_16x16x32_bf16 v[60:63], v[248:251], v[24:27], v[60:63]\n"
    "buffer_load_dwordx4 v1, s[36:39], s76 offen sc1 lds\n"
    "v_mfma_f32_16x16x32_bf16 v[72:75], v[252:255], v[24:27], v[72:75]\n"
    "v_pk_mul_f32 v[56:57], v[32:33], v[56:57]\n"
    "v_pk_mul_f32 v[58:59], v[34:35], v[58:59]\n"
    "s_nop 1\n"
    "v_pk_mul_f32 v[60:61], v[36:37], v[60:61]\n"
    "v_pk_mul_f32 v[62:63], v[38:39], v[62:63]\n"
    "v_pk_mul_f32 v[32:33], v[32:33], s[64:65] op_sel_hi:[1,0]\n"
    "v_pk_mul_f32 v[34:35], v[34:35], s[64:65] op_sel_hi:[1,0]\n"
    "v_pk_mul_f32 v[36:37], v[36:37], s[64:65] op_sel_hi:[1,0]\n"
    "v_pk_mul_f32 v[38:39], v[38:39], s[64:65] op_sel_hi:[1,0]\n"
    "v_exp_f32 v32, v32\n"
    "v_exp_f32 v33, v33\n"
    "v_exp_f32 v34, v34\n"
    "v_exp_f32 v35, v35\n"
    "v_exp_f32 v36, v36\n"
    "v_exp_f32 v37, v37\n"
    "v_exp_f32 v38, v38\n"
    "v_exp_f32 v39, v39\n"
    "v_pk_add_f32 v[32:33], v[32:33], 1.0 op_sel_hi:[1,0]\n"
    "v_pk_add_f32 v[34:35], v[34:35], 1.0 op_sel_hi:[1,0]\n"
    "v_pk_add_f32 v[36:37], v[36:37], 1.0 op_sel_hi:[1,0]\n"
    "v_pk_add_f32 v[38:39], v[38:39], 1.0 op_sel_hi:[1,0]\n"
    "v_rcp_f32 v32, v32\n"
    "v_rcp_f32 v33, v33\n"
    "v_rcp_f32 v34, v34\n"
    "v_rcp_f32 v35, v35\n"
    "v_rcp_f32 v36, v36\n"
    "v_rcp_f32 v37, v37\n"
    "v_rcp_f32 v38, v38\n"
    "v_rcp_f32 v39, v39\n"
    "v_pk_mul_f32 v[32:33], v[32:33], v[56:57]\n"
    "v_pk_mul_f32 v[34:35], v[34:35], v[58:59]\n"
    "v_pk_mul_f32 v[36:37], v[36:37], v[60:61]\n"
    "v_pk_mul_f32 v[38:39], v[38:39], v[62:63]\n"
    "v_cvt_pk_bf16_f32 v32, v32, v33\n"
    "v_cvt_pk_bf16_f32 v33, v34, v35\n"
    "v_cvt_pk_bf16_f32 v34, v36, v37\n"
    "v_cvt_pk_bf16_f32 v35, v38, v39\n"
    "buffer_store_dwordx4 v[32:35], v15, s[36:39], s73 offen sc1\n"
    "v_pk_mul_f32 v[72:73], v[48:49], v[72:73]\n"
    "v_pk_mul_f32 v[74:75], v[50:51], v[74:75]\n"
    "v_pk_mul_f32 v[48:49], v[48:49], s[64:65] op_sel_hi:[1,0]\n"
    "v_pk_mul_f32 v[50:51], v[50:51], s[64:65] op_sel_hi:[1,0]\n"
    "v_exp_f32 v48, v48\n"
    "v_exp_f32 v49, v49\n"
    "v_exp_f32 v50, v50\n"
    "v_exp_f32 v51, v51\n"
    "v_pk_add_f32 v[48:49], v[48:49], 1.0 op_sel_hi:[1,0]\n"
    "v_pk_add_f32 v[50:51], v[50:51], 1.0 op_sel_hi:[1,0]\n"
    "v_rcp_f32 v48, v48\n"
    "v_rcp_f32 v49, v49\n"
    "v_rcp_f32 v50, v50\n"
    "v_rcp_f32 v51, v51\n"
    "v_pk_mul_f32 v[48:49], v[48:49], v[72:73]\n"
    "v_pk_mul_f32 v[50:51], v[50:51], v[74:75]\n"
    "v_cvt_pk_bf16_f32 v48, v48, v49\n"
    "v_cvt_pk_bf16_f32 v49, v50, v51\n"
    "s_nop 0\n"
    "v_permlane16_swap_b32 v48, v50\n"
    "v_permlane16_swap_b32 v49, v51\n"
    "buffer_store_dwordx4 v[48:51], v4, s[36:39], s73 offen sc1\n"
    "v_mov_b32 v12, s68\n"
    "s_mov_b32 s73, 0x80000000\n"
    "s_mov_b32 s68, s71\n"
    "s_add_u32 m0, s59, 28672\n"
    "s_add_u32 s76, s76, 16384\n"
    "buffer_load_dwordx4 v1, s[36:39], s76 offen sc1 lds\n"
    "s_add_u32 m0, s59, 32768\n"
    "s_add_u32 s76, s76, 16384\n"
    "buffer_load_dwordx4 v1, s[36:39], s76 offen sc1 lds\n"
    "s_add_u32 m0, s59, 36864\n"
    "s_add_u32 s76, s76, 16384\n"
    "buffer_load_dwordx4 v8, s[36:39], s76 offen sc1 lds\n"
    "s_waitcnt vmcnt(3)\n"
    "buffer_atomic_smax v12, v14, s[36:39], 0 offen sc1\n"
    "s_mov_b32 s69, 0x80000000\n"
    "s_mov_b32 s70, 0x80000000\n"
    "v_mov_b32 v7, v12\n"
    "s_waitcnt vmcnt(9)\n"
    "s_add_u32 s71, s68, 1\n"
    "v_add_u32 v13, v11, v3\n"
    "v_cmp_gt_i32 vcc, s71, v13\n"
    "s_nop 0\n"
    "s_or_b64 exec, vcc, s[62:63]\n"
    "buffer_atomic_smax v11, v2, s[36:39], 0 offen sc0 sc1\n"
    "s_mov_b64 exec, -1\n"
    "s_waitcnt vmcnt(2)\n"
    "s_branch .LgUP_bar1_%=\n"
    ".LgUP_idle0_%=:\n"
    "s_or_b64 exec, vcc, s[62:63]\n"
    "buffer_atomic_smax v11, v2, s[36:39], 0 offen sc0 sc1\n"
    "s_mov_b64 exec, -1\n"
    "s_waitcnt vmcnt(1)\n"
    "buffer_atomic_smax v12, v14, s[36:39], 0 offen sc1\n"
    "s_branch .LgUP_block0_0_%=\n"
    ".LgUP_block0_%=:\n"
    "v_add_u32 v13, v11, v3\n"
    "v_cmp_gt_i32 vcc, s71, v13\n"
    "s_nop 0\n"
    "s_or_b64 exec, vcc, s[62:63]\n"
    "buffer_atomic_smax v11, v2, s[36:39], 0 offen sc0 sc1\n"
    "s_mov_b64 exec, -1\n"
    "s_sleep 3\n"
    "s_waitcnt vmcnt(1)\n"
    ".LgUP_block0_0_%=:\n"
    "v_add_u32 v13, v5, v3\n"
    "v_cmp_gt_i32 vcc, s71, v13\n"
    "s_cbranch_vccz .LgUP_go0_%=\n"
    "v_add_u32 v13, v5, v3\n"
    "v_cmp_gt_i32 vcc, s71, v13\n"
    "s_nop 0\n"
    "s_or_b64 exec, vcc, s[62:63]\n"
    "buffer_atomic_smax v5, v2, s[36:39], 0 offen sc0 sc1\n"
    "s_mov_b64 exec, -1\n"
    "s_sleep 3\n"
    "s_waitcnt vmcnt(1)\n"
    ".LgUP_block0_1_%=:\n"
    "v_add_u32 v13, v11, v3\n"
    "v_cmp_gt_i32 vcc, s71, v13\n"
    "s_cbranch_vccz .LgUP_go0_%=\n"
    "s_sub_u32 s72, s72, 1\n"
    "s_cbranch_scc0 .LgUP_block0_%=\n"
    "s_mov_b32 s72, 0\n"
    ".LgUP_go0_%=:\n"
    "s_lshr_b32 s76, s71, 1\n"
    "s_and_b32 s76, s76, s54\n"
    "s_mul_i32 s76, s76, s53\n"
    "s_add_u32 s76, s76, s52\n"
    "s_add_u32 s76, s76, 1024\n"
    "s_add_u32 m0, s59, 20480\n"
    "s_nop 0\n"
    "buffer_load_dwordx4 v1, s[36:39], s76 offen sc1 lds\n"
    "s_add_u32 m0, s59, 24576\n"
    "s_add_u32 s76, s76, 16384\n"
    "buffer_load_dwordx4 v1, s[36:39], s76 offen sc1 lds\n"
    "s_add_u32 m0, s59, 28672\n"
    "s_add_u32 s76, s76, 16384\n"
    "buffer_load_dwordx4 v1, s[36:39], s76 offen sc1 lds\n"
    "s_add_u32 m0, s59, 32768\n"
    "s_add_u32 s76, s76, 16384\n"
    "buffer_load_dwordx4 v1, s[36:39], s76 offen sc1 lds\n"
    "s_add_u32 m0, s59, 36864\n"
    "s_add_u32 s76, s76, 16384\n"
    "buffer_load_dwordx4 v8, s[36:39], s76 offen sc1 lds\n"
    "s_mov_b32 s69, 0x80000000\n"
    "s_mov_b32 s70, 0x80000000\n"
    "v_mov_b32 v7, v12\n"
    "s_waitcnt vmcnt(5)\n"
    "s_add_u32 s71, s68, 1\n"
    "v_add_u32 v13, v11, v3\n"
    "v_cmp_gt_i32 vcc, s71, v13\n"
    "s_nop 0\n"
    "s_or_b64 exec, vcc, s[62:63]\n"
    "buffer_atomic_smax v11, v2, s[36:39], 0 offen sc0 sc1\n"
    "s_mov_b64 exec, -1\n"
    "s_waitcnt vmcnt(1)\n"
    "s_branch .LgUP_bar1_%=\n"
    ".LgUP_bar0_%=:\n"
    "s_barrier\n"
    "ds_read_b128 v[16:19], v0 offset:0\n"
    "ds_read_b128 v[20:23], v0 offset:1024\n"
    "ds_read_b128 v[24:27], v0 offset:2048\n"
    "v_add_u32 v13, v5, v3\n"
    "v_cmp_gt_i32 vcc, s71, v13\n"
    "s_nop 0\n"
    "s_or_b64 exec, vcc, s[62:63]\n"
    "buffer_atomic_smax v5, v2, s[36:39], 0 offen sc0 sc1\n"
    "s_mov_b64 exec, -1\n"
    "s_lshr_b32 s76, s71, 1\n"
    "s_and_b32 s76, s76, s54\n"
    "s_mul_i32 s76, s76, s53\n"
    "s_add_u32 s76, s76, s52\n"
    "s_add_u32 s76, s76, 1024\n"
    "s_waitcnt lgkmcnt(2)\n"
    "s_branch .LgUP_head0_%=\n"
    ".LgUP_poll1_%=:\n"
    "v_mfma_f32_16x16x32_bf16 v[40:43], a[48:51], v[24:27], v[40:43]\n"
    "s_or_b64 exec, vcc, s[62:63]\n"
    "buffer_atomic_smax v11, v2, s[36:39], 0 offen sc0 sc1\n"
    "s_mov_b64 exec, -1\n"
    "v_mfma_f32_16x16x32_bf16 v[44:47], a[52:55], v[24:27], v[44:47]\n"
    "v_mul_f32 v35, 0xbfb8aa3b, v35\n"
    "v_mul_f32 v36, 0xbfb8aa3b, v36\n"
    "v_mfma_f32_16x16x32_bf16 v[52:55], a[56:59], v[24:27], v[52:55]\n"
    "v_exp_f32 v35, v35\n"
    "v_mfma_f32_16x16x32_bf16 v[64:67], a[60:63], v[24:27], v[64:67]\n"
    "v_exp_f32 v36, v36\n"
    "v_mfma_f32_16x16x32_bf16 v[68:71], a[64:67], v[24:27], v[68:71]\n"
    "s_waitcnt lgkmcnt(1)\n"
    "v_mul_f32 v37, 0xbfb8aa3b, v37\n"
    "v_mul_f32 v38, 0xbfb8aa3b, v38\n"
    "v_mfma_f32_16x16x32_bf16 v[76:79], a[68:71], v[24:27], v[76:79]\n"
    "ds_read_b128 v[24:27], v9 offset:5120\n"
    "v_mul_f32 v39, 0xbfb8aa3b, v39\n"
    "v_mfma_f32_16x16x32_bf16 v[40:43], a[72:75], v[16:19], v[40:43]\n"
    "v_exp_f32 v37, v37\n"
    "v_mfma_f32_16x16x32_bf16 v[44:47], a[76:79], v[16:19], v[44:47]\n"
    "v_exp_f32 v38, v38\n"
    "v_mfma_f32_16x16x32_bf16 v[52:55], a[80:83], v[16:19], v[52:55]\n"
    "v_exp_f32 v39, v39\n"
    "v_mfma_f32_16x16x32_bf16 v[64:67], a[84:87], v[16:19], v[64:67]\n"
    "v_add_f32 v32, 1.0, v32\n"
    "v_add_f32 v33, 1.0, v33\n"
    "v_mfma_f32_16x16x32_bf16 v[68:71], a[88:91], v[16:19], v[68:71]\n"
    "s_waitcnt lgkmcnt(1)\n"
    "v_rcp_f32 v32, v32\n"
    "v_mfma_f32_16x16x32_bf16 v[76:79], a[92:95], v[16:19], v[76:79]\n"
    "ds_read_b128 v[16:19], v9 offset:6144\n"
    "v_add_f32 v34, 1.0, v34\n"
    "v_mfma_f32_16x16x32_bf16 v[40:43], a[96:99], v[20:23], v[40:43]\n"
    "v_rcp_f32 v33, v33\n"
    "v_mfma_f32_16x16x32_bf16 v[44:47], a[100:103], v[20:23], v[44:47]\n"
    "s_waitcnt vmcnt(2)\n"
    "v_add_f32 v35, 1.0, v35\n"
    "v_add_u32 v13, v5, v3\n"
    "v_mfma_f32_16x16x32_bf16 v[52:55], a[104:107], v[20:23], v[52:55]\n"
    "v_add_f32 v36, 1.0, v36\n"
    "v_cmp_gt_i32 vcc, s71, v13\n"
    "v_mfma_f32_16x16x32_bf16 v[64:67], a[108:111], v[20:23], v[64:67]\n"
    "v_rcp_f32 v34, v34\n"
    "v_mfma_f32_16x16x32_bf16 v[68:71], a[112:115], v[20:23], v[68:71]\n"
    "s_waitcnt lgkmcnt(1)\n"
    "v_rcp_f32 v35, v35\n"
    "v_mfma_f32_16x16x32_bf16 v[76:79], a[116:119], v[20:23], v[76:79]\n"
    "ds_read_b128 v[20:23], v9 offset:7168\n"
    "s_cbranch_vccz .LgUP_dma1_30_%=\n"
    "v_mfma_f32_16x16x32_bf16 v[40:43], a[120:123], v[24:27], v[40:43]\n"
    "s_or_b64 exec, vcc, s[62:63]\n"
    "buffer_atomic_smax v5, v2, s[36:39], 0 offen sc0 sc1\n"
    "s_mov_b64 exec, -1\n"
    "v_mfma_f32_16x16x32_bf16 v[44:47], a[124:127], v[24:27], v[44:47]\n"
    "v_rcp_f32 v36, v36\n"
    "v_mfma_f32_16x16x32_bf16 v[52:55], a[128:131], v[24:27], v[52:55]\n"
    "v_add_f32 v37, 1.0, v37\n"
    "v_add_f32 v38, 1.0, v38\n"
    "v_mfma_f32_16x16x32_bf16 v[64:67], a[132:135], v[24:27], v[64:67]\n"
    "v_rcp_f32 v37, v37\n"
    "v_mfma_f32_16x16x32_bf16 v[68:71], a[136:139], v[24:27], v[68:71]\n"
    "s_waitcnt lgkmcnt(1)\n"
    "v_rcp_f32 v38, v38\n"
    "v_mfma_f32_16x16x32_bf16 v[76:79], a[140:143], v[24:27], v[76:79]\n"
    "ds_read_b128 v[24:27], v9 offset:8192\n"
    "v_add_f32 v39, 1.0, v39\n"
    "v_mfma_f32_16x16x32_bf16 v[40:43], a[144:147], v[16:19], v[40:43]\n"
    "v_rcp_f32 v39, v39\n"
    "v_mfma_f32_16x16x32_bf16 v[44:47], a[148:151], v[16:19], v[44:47]\n"
    "v_mul_f32 v32, v32, v56\n"
    "v_mul_f32 v33, v33, v57\n"
    "v_mfma_f32_16x16x32_bf16 v[52:55], a[152:155], v[16:19], v[52:55]\n"
    "v_mul_f32 v34, v34, v58\n"
    "v_mul_f32 v35, v35, v59\n"
    "v_mfma_f32_16x16x32_bf16 v[64:67], a[156:159], v[16:19], v[64:67]\n"
    "v_mul_f32 v36, v36, v60\n"
    "v_mul_f32 v37, v37, v61\n"
    "v_mfma_f32_16x16x32_bf16 v[68:71], a[160:163], v[16:19], v[68:71]\n"
    "s_waitcnt lgkmcnt(1)\n"
    "v_mul_f32 v38, v38, v62\n"
    "v_mul_f32 v39, v39, v63\n"
    "v_mfma_f32_16x16x32_bf16 v[76:79], a[164:167], v[16:19], v[76:79]\n"
    "ds_read_b128 v[16:19], v9 offset:9216\n"
    "v_cvt_pk_bf16_f32 v32, v32, v33\n"
    "v_mfma_f32_16x16x32_bf16 v[40:43], a[168:171], v[20:23], v[40:43]\n"
    "v_cvt_pk_bf16_f32 v33, v34, v35\n"
    "v_cvt_pk_bf16_f32 v34, v36, v37\n"
    "v_mfma_f32_16x16x32_bf16 v[44:47], a[172:175], v[20:23], v[44:47]\n"
    "s_waitcnt vmcnt(1)\n"
    "v_cvt_pk_bf16_f32 v35, v38, v39\n"
    "v_add_u32 v13, v11, v3\n"
    "v_mfma_f32_16x16x32_bf16 v[52:55], a[176:179], v[20:23], v[52:55]\n"
    "v_mul_f32 v72, v48, v72\n"
    "v_cmp_gt_i32 vcc, s71, v13\n"
    "v_mfma_f32_16x16x32_bf16 v[64:67], a[180:183], v[20:23], v[64:67]\n"
    "buffer_store_dwordx4 v[32:35], v15, s[36:39], s73 offen sc1\n"
    "v_mfma_f32_16x16x32_bf16 v[68:71], a[184:187], v[20:23], v[68:71]\n"
    "s_waitcnt lgkmcnt(1)\n"
    "v_mul_f32 v73, v49, v73\n"
    "v_mul_f32 v74, v50, v74\n"
    "v_mfma_f32_16x16x32_bf16 v[76:79], a[188:191], v[20:23], v[76:79]\n"
    "ds_read_b128 v[20:23], v9 offset:10240\n"
    "s_cbranch_vccz .LgUP_dma1_48_%=\n"
    "v_mfma_f32_16x16x32_bf16 v[40:43], a[192:195], v[24:27], v[40:43]\n"
    "s_or_b64 exec, vcc, s[62:63]\n"
    "buffer_atomic_smax v11, v2, s[36:39], 0 offen sc0 sc1\n"
    "s_mov_b64 exec, -1\n"
    "v_mfma_f32_16x16x32_bf16 v[44:47], a[196:199], v[24:27], v[44:47]\n"
    "v_mul_f32 v75, v51, v75\n"
    "v_mul_f32 v48, 0xbfb8aa3b, v48\n"
    "v_mfma_f32_16x16x32_bf16 v[52:55], a[200:203], v[24:27], v[52:55]\n"
    "v_exp_f32 v48, v48\n"
    "v_mfma_f32_16x16x32_bf16 v[64:67], a[204:207], v[24:27], v[64:67]\n"
    "v_mul_f32 v49, 0xbfb8aa3b, v49\n"
    "v_mul_f32 v50, 0xbfb8aa3b, v50\n"
    "v_mfma_f32_16x16x32_bf16 v[68:71], a[208:211], v[24:27], v[68:71]\n"
    "s_waitcnt lgkmcnt(1)\n"
    "v_exp_f32 v49, v49\n"
    "v_mfma_f32_16x16x32_bf16 v[76:79], a[212:215], v[24:27], v[76:79]\n"
    "ds_read_b128 v[24:27], v9 offset:11264\n"
    "v_mul_f32 v51, 0xbfb8aa3b, v51\n"
    "v_mfma_f32_16x16x32_bf16 v[40:43], a[216:219], v[16:19], v[40:43]\n"
    "v_exp_f32 v50, v50\n"
    "v_mfma_f32_16x16x32_bf16 v[44:47], a[220:223], v[16:19], v[44:47]\n"
    "v_exp_f32 v51, v51\n"
    "v_mfma_f32_16x16x32_bf16 v[52:55], a[224:227], v[16:19], v[52:55]\n"
    "v_add_f32 v48, 1.0, v48\n"
    "v_add_f32 v49, 1.0, v49\n"
    "v_mfma_f32_16x16x32_bf16 v[64:67], a[228:231], v[16:19], v[64:67]\n"
    "v_rcp_f32 v48, v48\n"
    "v_mfma_f32_16x16x32_bf16 v[68:71], a[232:235], v[16:19], v[68:71]\n"
    "s_waitcnt lgkmcnt(1)\n"
    "v_rcp_f32 v49, v49\n"
    "v_mfma_f32_16x16x32_bf16 v[76:79], a[236:239], v[16:19], v[76:79]\n"
    "ds_read_b128 v[16:19], v9 offset:12288\n"
    "v_add_f32 v50, 1.0, v50\n"
    "v_mfma_f32_16x16x32_bf16 v[40:43], a[240:243], v[20:23], v[40:43]\n"
    "v_rcp_f32 v50, v50\n"
    "v_mfma_f32_16x16x32_bf16 v[44:47], a[244:247], v[20:23], v[44:47]\n"
    "s_waitcnt vmcnt(2)\n"
    "v_add_f32 v51, 1.0, v51\n"
    "v_add_u32 v13, v5, v3\n"
    "v_mfma_f32_16x16x32_bf16 v[52:55], a[248:251], v[20:23], v[52:55]\n"
    "v_mul_f32 v48, v48, v72\n"
    "v_cmp_gt_i32 vcc, s71, v13\n"
    "v_mfma_f32_16x16x32_bf16 v[64:67], a[252:255], v[20:23], v[64:67]\n"
    "v_rcp_f32 v51, v51\n"
    "v_mfma_f32_16x16x32_bf16 v[68:71], v[80:83], v[20:23], v[68:71]\n"
    "s_waitcnt lgkmcnt(1)\n"
    "v_mul_f32 v49, v49, v73\n"
    "v_mul_f32 v50, v50, v74\n"
    "v_mfma_f32_16x16x32_bf16 v[76:79], v[84:87], v[20:23], v[76:79]\n"
    "ds_read_b128 v[20:23], v9 offset:13312\n"
    "s_cbranch_vccz .LgUP_dma1_66_%=\n"
    "v_mfma_f32_16x16x32_bf16 v[40:43], v[88:91], v[24:27], v[40:43]\n"
    "s_or_b64 exec, vcc, s[62:63]\n"
    "buffer_atomic_smax v5, v2, s[36:39], 0 offen sc0 sc1\n"
    "s_mov_b64 exec, -1\n"
    "v_mfma_f32_16x16x32_bf16 v[44:47], v[92:95], v[24:27], v[44:47]\n"
    "v_mul_f32 v51, v51, v75\n"
    "v_cvt_pk_bf16_f32 v48, v48, v49\n"
    "v_mfma_f32_16x16x32_bf16 v[52:55], v[96:99], v[24:27], v[52:55]\n"
    "v_cvt_pk_bf16_f32 v49, v50, v51\n"
    "s_nop 0\n"
    "v_mfma_f32_16x16x32_bf16 v[64:67], v[100:103], v[24:27], v[64:67]\n"
    "v_permlane16_swap_b32 v48, v50\n"
    "v_mfma_f32_16x16x32_bf16 v[68:71], v[104:107], v[24:27], v[68:71]\n"
    "s_waitcnt lgkmcnt(1)\n"
    "v_permlane16_swap_b32 v49, v51\n"
    "v_mfma_f32_16x16x32_bf16 v[76:79], v[108:111], v[24:27], v[76:79]\n"
    "ds_read_b128 v[24:27], v9 offset:14336\n"
    "v_mfma_f32_16x16x32_bf16 v[40:43], v[112:115], v[16:19], v[40:43]\n"
    "buffer_store_dwordx4 v[48:51], v4, s[36:39], s73 offen sc1\n"
    "v_mfma_f32_16x16x32_bf16 v[44:47], v[116:119], v[16:19], v[44:47]\n"
    "s_lshr_b32 s73, s68, 1\n"
    "v_mfma_f32_16x16x32_bf16 v[52:55], v[120:123], v[16:19], v[52:55]\n"
    "s_and_b32 s73, s73, s57\n"
    "v_mfma_f32_16x16x32_bf16 v[64:67], v[124:127], v[16:19], v[64:67]\n"
    "s_mul_i32 s73, s73, s56\n"
    "v_mfma_f32_16x16x32_bf16 v[68:71], v[128:131], v[16:19], v[68:71]\n"
    "s_waitcnt lgkmcnt(1)\n"
    "s_add_u32 s73, s73, s55\n"
    "v_mfma_f32_16x16x32_bf16 v[76:79], v[132:135], v[16:19], v[76:79]\n"
    "ds_read_b128 v[16:19], v9 offset:15360\n"
    "v_mfma_f32_16x16x32_bf16 v[40:43], v[136:139], v[20:23], v[40:43]\n"
    "v_mfma_f32_16x16x32_bf16 v[44:47], v[140:143], v[20:23], v[44:47]\n"
    "s_waitcnt vmcnt(2)\n"
    "v_add_u32 v13, v11, v3\n"
    "v_mfma_f32_16x16x32_bf16 v[52:55], v[144:147], v[20:23], v[52:55]\n"
    "v_cmp_gt_i32 vcc, s71, v13\n"
    "v_mfma_f32_16x16x32_bf16 v[64:67], v[148:151], v[20:23], v[64:67]\n"
    "v_mfma_f32_16x16x32_bf16 v[68:71], v[152:155], v[20:23], v[68:71]\n"
    "s_waitcnt lgkmcnt(1)\n"
    "v_mfma_f32_16x16x32_bf16 v[76:79], v[156:159], v[20:23], v[76:79]\n"
    "ds_read_b128 v[20:23], v9 offset:16384\n"
    "s_cbranch_vccz .LgUP_dma1_84_%=\n"
    "v_mfma_f32_16x16x32_bf16 v[40:43], v[160:163], v[24:27], v[40:43]\n"
    "s_or_b64 exec, vcc, s[62:63]\n"
    "buffer_atomic_smax v11, v2, s[36:39], 0 offen sc0 sc1\n"
    "s_mov_b64 exec, -1\n"
    "v_mfma_f32_16x16x32_bf16 v[44:47], v[164:167], v[24:27], v[44:47]\n"
    "v_mfma_f32_16x16x32_bf16 v[52:55], v[168:171], v[24:27], v[52:55]\n"
    "v_mfma_f32_16x16x32_bf16 v[64:67], v[172:175], v[24:27], v[64:67]\n"
    "v_mfma_f32_16x16x32_bf16 v[68:71], v[176:179], v[24:27], v[68:71]\n"
    "s_waitcnt lgkmcnt(1)\n"
    "v_mfma_f32_16x16x32_bf16 v[76:79], v[180:183], v[24:27], v[76:79]\n"
    "ds_read_b128 v[24:27], v9 offset:17408\n"
    "v_mfma_f32_16x16x32_bf16 v[40:43], v[184:187], v[16:19], v[40:43]\n"
    "v_mfma_f32_16x16x32_bf16 v[44:47], v[188:191], v[16:19], v[44:47]\n"
    "v_mfma_f32_16x16x32_bf16 v[52:55], v[192:195], v[16:19], v[52:55]\n"
    "v_mfma_f32_16x16x32_bf16 v[64:67], v[196:199], v[16:19], v[64:67]\n"
    "v_mfma_f32_16x16x32_bf16 v[68:71], v[200:203], v[16:19], v[68:71]\n"
    "s_waitcnt lgkmcnt(1)\n"
    "v_mfma_f32_16x16x32_bf16 v[76:79], v[204:207], v[16:19], v[76:79]\n"
    "v_mfma_f32_16x16x32_bf16 v[40:43], v[208:211], v[20:23], v[40:43]\n"
    "v_mfma_f32_16x16x32_bf16 v[44:47], v[212:215], v[20:23], v[44:47]\n"
    "s_waitcnt vmcnt(2)\n"
    "v_add_u32 v13, v5, v3\n"
    "v_mfma_f32_16x16x32_bf16 v[52:55], v[216:219], v[20:23], v[52:55]\n"
    "v_cmp_gt_i32 vcc, s71, v13\n"
    "v_mfma_f32_16x16x32_bf16 v[64:67], v[220:223], v[20:23], v[64:67]\n"
    "v_mfma_f32_16x16x32_bf16 v[68:71], v[224:227], v[20:23], v[68:71]\n"
    "s_waitcnt lgkmcnt(0)\n"
    "v_mfma_f32_16x16x32_bf16 v[76:79], v[228:231], v[20:23], v[76:79]\n"
    "s_cbranch_vccz .LgUP_dma1_102_%=\n"
    "v_mfma_f32_16x16x32_bf16 v[40:43], v[232:235], v[24:27], v[40:43]\n"
    "s_or_b64 exec, vcc, s[62:63]\n"
    "buffer_atomic_smax v5, v2, s[36:39], 0 offen sc0 sc1\n"
    "s_mov_b64 exec, -1\n"
    "v_mfma_f32_16x16x32_bf16 v[44:47], v[236:239], v[24:27], v[44:47]\n"
    "v_mfma_f32_16x16x32_bf16 v[52:55], v[240:243], v[24:27], v[52:55]\n"
    "s_waitcnt vmcnt(2)\n"
    "buffer_atomic_smax v12, v14, s[36:39], s70 offen sc1\n"
    "v_mfma_f32_16x16x32_bf16 v[64:67], v[244:247], v[24:27], v[64:67]\n"
    "v_mfma_f32_16x16x32_bf16 v[68:71], v[248:251], v[24:27], v[68:71]\n"
    "v_mfma_f32_16x16x32_bf16 v[76:79], v[252:255], v[24:27], v[76:79]\n"
    "v_pk_mul_f32 v[64:65], v[40:41], v[64:65]\n"
    "v_pk_mul_f32 v[66:67], v[42:43], v[66:67]\n"
    "s_nop 1\n"
    "v_pk_mul_f32 v[68:69], v[44:45], v[68:69]\n"
    "v_pk_mul_f32 v[70:71], v[46:47], v[70:71]\n"
    "v_pk_mul_f32 v[40:41], v[40:41], s[64:65] op_sel_hi:[1,0]\n"
    "v_pk_mul_f32 v[42:43], v[42:43], s[64:65] op_sel_hi:[1,0]\n"
    "v_pk_mul_f32 v[44:45], v[44:45], s[64:65] op_sel_hi:[1,0]\n"
    "v_pk_mul_f32 v[46:47], v[46:47], s[64:65] op_sel_hi:[1,0]\n"
    "v_exp_f32 v40, v40\n"
    "v_exp_f32 v41, v41\n"
    "v_exp_f32 v42, v42\n"
    "v_exp_f32 v43, v43\n"
    "v_exp_f32 v44, v44\n"
    "v_exp_f32 v45, v45\n"
    "v_exp_f32 v46, v46\n"
    "v_exp_f32 v47, v47\n"
    "v_pk_add_f32 v[40:41], v[40:41], 1.0 op_sel_hi:[1,0]\n"
    "v_pk_add_f32 v[42:43], v[42:43], 1.0 op_sel_hi:[1,0]\n"
    "v_pk_add_f32 v[44:45], v[44:45], 1.0 op_sel_hi:[1,0]\n"
    "v_pk_add_f32 v[46:47], v[46:47], 1.0 op_sel_hi:[1,0]\n"
    "v_rcp_f32 v40, v40\n"
    "v_rcp_f32 v41, v41\n"
    "v_rcp_f32 v42, v42\n"
    "v_rcp_f32 v43, v43\n"
    "v_rcp_f32 v44, v44\n"
    "v_rcp_f32 v45, v45\n"
    "v_rcp_f32 v46, v46\n"
    "v_rcp_f32 v47, v47\n"
    "v_pk_mul_f32 v[40:41], v[40:41], v[64:65]\n"
    "v_pk_mul_f32 v[42:43], v[42:43], v[66:67]\n"
    "v_pk_mul_f32 v[44:45], v[44:45], v[68:69]\n"
    "v_pk_mul_f32 v[46:47], v[46:47], v[70:71]\n"
    "v_cvt_pk_bf16_f32 v40, v40, v41\n"
    "v_cvt_pk_bf16_f32 v41, v42, v43\n"
    "v_cvt_pk_bf16_f32 v42, v44, v45\n"
    "v_cvt_pk_bf16_f32 v43, v46, v47\n"
    "buffer_store_dwordx4 v[40:43], v15, s[36:39], s73 offen offset:1024 sc1\n"
    "v_pk_mul_f32 v[76:77], v[52:53], v[76:77]\n"
    "v_pk_mul_f32 v[78:79], v[54:55], v[78:79]\n"
    "v_pk_mul_f32 v[52:53], v[52:53], s[64:65] op_sel_hi:[1,0]\n"
    "v_pk_mul_f32 v[54:55], v[54:55], s[64:65] op_sel_hi:[1,0]\n"
    "v_exp_f32 v52, v52\n"
    "v_exp_f32 v53, v53\n"
    "v_exp_f32 v54, v54\n"
    "v_exp_f32 v55, v55\n"
    "v_pk_add_f32 v[52:53], v[52:53], 1.0 op_sel_hi:[1,0]\n"
    "v_pk_add_f32 v[54:55], v[54:55], 1.0 op_sel_hi:[1,0]\n"
    "v_rcp_f32 v52, v52\n"
    "v_rcp_f32 v53, v53\n"
    "v_rcp_f32 v54, v54\n"
    "v_rcp_f32 v55, v55\n"
    "v_pk_mul_f32 v[52:53], v[52:53], v[76:77]\n"
    "v_pk_mul_f32 v[54:55], v[54:55], v[78:79]\n"
    "v_cvt_pk_bf16_f32 v52, v52, v53\n"
    "v_cvt_pk_bf16_f32 v53, v54, v55\n"
    "s_nop 0\n"
    "v_permlane16_swap_b32 v52, v54\n"
    "v_permlane16_swap_b32 v53, v55\n"
    "buffer_store_dwordx4 v[52:55], v4, s[36:39], s73 offen offset:1024 sc1\n"
    "v_mov_b32 v12, s68\n"
    "s_mov_b32 s73, 0x80000000\n"
    "s_mov_b32 s68, s71\n"
    "s_cmp_eq_u32 s68, 128\n"
    "s_cbranch_scc1 .LgUP_done_%=\n"
    "s_waitcnt vmcnt(4)\n"
    "v_add_u32 v13, v11, v3\n"
    "v_cmp_gt_i32 vcc, s71, v13\n"
    "s_cbranch_vccnz .LgUP_idle1_%=\n"
    "s_add_u32 m0, s59, 0\n"
    "s_nop 0\n"
    "buffer_load_dwordx4 v1, s[36:39], s76 offen sc1 lds\n"
    "s_add_u32 m0, s59, 4096\n"
    "s_add_u32 s76, s76, 16384\n"
    "buffer_load_dwordx4 v1, s[36:39], s76 offen sc1 lds\n"
    "s_add_u32 m0, s59, 8192\n"
    "s_add_u32 s76, s76, 16384\n"
    "buffer_load_dwordx4 v1, s[36:39], s76 offen sc1 lds\n"
    "s_add_u32 m0, s59, 12288\n"
    "s_add_u32 s76, s76, 16384\n"
    "buffer_load_dwordx4 v1, s[36:39], s76 offen sc1 lds\n"
    "s_add_u32 m0, s59, 16384\n"
    "s_add_u32 s76, s76, 16384\n"
    "buffer_load_dwordx4 v8, s[36:39], s76 offen sc1 lds\n"
    "s_waitcnt vmcnt(5)\n"
    "buffer_atomic_smax v12, v10, s[36:39], 0 offen sc1\n"
    "s_mov_b32 s69, 0x80000000\n"
    "s_mov_b32 s70, 0x80000000\n"
    "v_mov_b32 v7, v12\n"
    "s_waitcnt vmcnt(9)\n"
    "s_add_u32 s71, s68, 1\n"
    "v_add_u32 v13, v11, v3\n"
    "v_cmp_gt_i32 vcc, s71, v13\n"
    "s_nop 0\n"
    "s_or_b64 exec, vcc, s[62:63]\n"
    "buffer_atomic_smax v11, v2, s[36:39], 0 offen sc0 sc1\n"
    "s_mov_b64 exec, -1\n"
    "s_waitcnt vmcnt(2)\n"
    "s_branch .LgUP_bar0_%=\n"
    ".LgUP_dma1_30_%=:\n"
    "v_mfma_f32_16x16x32_bf16 v[40:43], a[120:123], v[24:27], v[40:43]\n"
    "s_add_u32 m0, s59, 0\n"
    "v_mfma_f32_16x16x32_bf16 v[44:47], a[124:127], v[24:27], v[44:47]\n"
    "buffer_load_dwordx4 v1, s[36:39], s76 offen sc1 lds\n"
    "v_rcp_f32 v36, v36\n"
    "v_mfma_f32_16x16x32_bf16 v[52:55], a[128:131], v[24:27], v[52:55]\n"
    "s_add_u32 s76, s76, 16384\n"
    "v_add_f32 v37, 1.0, v37\n"
    "v_add_f32 v38, 1.0, v38\n"
    "v_mfma_f32_16x16x32_bf16 v[64:67], a[132:135], v[24:27], v[64:67]\n"
    "s_add_u32 m0, s59, 4096\n"
    "v_rcp_f32 v37, v37\n"
    "v_mfma_f32_16x16x32_bf16 v[68:71], a[136:139], v[24:27], v[68:71]\n"
    "s_waitcnt lgkmcnt(1)\n"
    "v_rcp_f32 v38, v38\n"
    "v_mfma_f32_16x16x32_bf16 v[76:79], a[140:143], v[24:27], v[76:79]\n"
    "ds_read_b128 v[24:27], v9 offset:8192\n"
    "v_add_f32 v39, 1.0, v39\n"
    "v_mfma_f32_16x16x32_bf16 v[40:43], a[144:147], v[16:19], v[40:43]\n"
    "buffer_load_dwordx4 v1, s[36:39], s76 offen sc1 lds\n"
    "v_rcp_f32 v39, v39\n"
    "v_mfma_f32_16x16x32_bf16 v[44:47], a[148:151], v[16:19], v[44:47]\n"
    "s_add_u32 s76, s76, 16384\n"
    "v_mul_f32 v32, v32, v56\n"
    "v_mul_f32 v33, v33, v57\n"
    "v_mfma_f32_16x16x32_bf16 v[52:55], a[152:155], v[16:19], v[52:55]\n"
    "s_add_u32 m0, s59, 8192\n"
    "v_mul_f32 v34, v34, v58\n"
    "v_mul_f32 v35, v35, v59\n"
    "v_mfma_f32_16x16x32_bf16 v[64:67], a[156:159], v[16:19], v[64:67]\n"
    "buffer_load_dwordx4 v1, s[36:39], s76 offen sc1 lds\n"
    "v_mul_f32 v36, v36, v60\n"
    "v_mul_f32 v37, v37, v61\n"
    "v_mfma_f32_16x16x32_bf16 v[68:71], a[160:163], v[16:19], v[68:71]\n"
    "s_waitcnt lgkmcnt(1)\n"
    "v_mul_f32 v38, v38, v62\n"
    "v_mul_f32 v39, v39, v63\n"
    "v_mfma_f32_16x16x32_bf16 v[76:79], a[164:167], v[16:19], v[76:79]\n"
    "ds_read_b128 v[16:19], v9 offset:9216\n"
    "v_cvt_pk_bf16_f32 v32, v32, v33\n"
    "v_mfma_f32_16x16x32_bf16 v[40:43], a[168:171], v[20:23], v[40:43]\n"
    "s_add_u32 s76, s76, 16384\n"
    "v_cvt_pk_bf16_f32 v33, v34, v35\n"
    "v_cvt_pk_bf16_f32 v34, v36, v37\n"
    "v_mfma_f32_16x16x32_bf16 v[44:47], a[172:175], v[20:23], v[44:47]\n"
    "s_add_u32 m0, s59, 12288\n"
    "v_cvt_pk_bf16_f32 v35, v38, v39\n"
    "v_mfma_f32_16x16x32_bf16 v[52:55], a[176:179], v[20:23], v[52:55]\n"
    "buffer_load_dwordx4 v1, s[36:39], s76 offen sc1 lds\n"
    "s_add_u32 s76, s76, 16384\n"
    "v_mul_f32 v72, v48, v72\n"
    "v_mfma_f32_16x16x32_bf16 v[64:67], a[180:183], v[20:23], v[64:67]\n"
    "buffer_store_dwordx4 v[32:35], v15, s[36:39], s73 offen sc1\n"
    "v_mfma_f32_16x16x32_bf16 v[68:71], a[184:187], v[20:23], v[68:71]\n"
    "s_waitcnt lgkmcnt(1)\n"
    "s_add_u32 m0, s59, 16384\n"
    "v_mul_f32 v73, v49, v73\n"
    "v_mul_f32 v74, v50, v74\n"
    "v_mfma_f32_16x16x32_bf16 v[76:79], a[188:191], v[20:23], v[76:79]\n"
    "ds_read_b128 v[20:23], v9 offset:10240\n"
    "buffer_load_dwordx4 v8, s[36:39], s76 offen sc1 lds\n"
    "v_mfma_f32_16x16x32_bf16 v[40:43], a[192:195], v[24:27], v[40:43]\n"
    "v_mfma_f32_16x16x32_bf16 v[44:47], a[196:199], v[24:27], v[44:47]\n"
    "v_mul_f32 v75, v51, v75\n"
    "v_mul_f32 v48, 0xbfb8aa3b, v48\n"
    "v_mfma_f32_16x16x32_bf16 v[52:55], a[200:203], v[24:27], v[52:55]\n"
    "v_exp_f32 v48, v48\n"
    "v_mfma_f32_16x16x32_bf16 v[64:67], a[204:207], v[24:27], v[64:67]\n"
    "v_mul_f32 v49, 0xbfb8aa3b, v49\n"
    "v_mul_f32 v50, 0xbfb8aa3b, v50\n"
    "v_mfma_f32_16x16x32_bf16 v[68:71], a[208:211], v[24:27], v[68:71]\n"
    "s_waitcnt lgkmcnt(1)\n"
    "v_exp_f32 v49, v49\n"
    "v_mfma_f32_16x16x32_bf16 v[76:79], a[212:215], v[24:27], v[76:79]\n"
    "ds_read_b128 v[24:27], v9 offset:11264\n"
    "v_mul_f32 v51, 0xbfb8aa3b, v51\n"
    "v_mfma_f32_16x16x32_bf16 v[40:43], a[216:219], v[16:19], v[40:43]\n"
    "v_exp_f32 v50, v50\n"
    "v_mfma_f32_16x16x32_bf16 v[44:47], a[220:223], v[16:19], v[44:47]\n"
    "v_exp_f32 v51, v51\n"
    "v_mfma_f32_16x16x32_bf16 v[52:55], a[224:227], v[16:19], v[52:55]\n"
    "v_add_f32 v48, 1.0, v48\n"
    "v_add_f32 v49, 1.0, v49\n"
    "v_mfma_f32_16x16x32_bf16 v[64:67], a[228:231], v[16:19], v[64:67]\n"
    "v_rcp_f32 v48, v48\n"
    "v_mfma_f32_16x16x32_bf16 v[68:71], a[232:235], v[16:19], v[68:71]\n"
    "s_waitcnt lgkmcnt(1)\n"
    "v_rcp_f32 v49, v49\n"
    "v_mfma_f32_16x16x32_bf16 v[76:79], a[236:239], v[16:19], v[76:79]\n"
    "ds_read_b128 v[16:19], v9 offset:12288\n"
    "v_add_f32 v50, 1.0, v50\n"
    "v_mfma_f32_16x16x32_bf16 v[40:43], a[240:243], v[20:23], v[40:43]\n"
    "v_rcp_f32 v50, v50\n"
    "v_mfma_f32_16x16x32_bf16 v[44:47], a[244:247], v[20:23], v[44:47]\n"
    "v_add_f32 v51, 1.0, v51\n"
    "v_mfma_f32_16x16x32_bf16 v[52:55], a[248:251], v[20:23], v[52:55]\n"
    "v_mul_f32 v48, v48, v72\n"
    "v_mfma_f32_16x16x32_bf16 v[64:67], a[252:255], v[20:23], v[64:67]\n"
    "v_rcp_f32 v51, v51\n"
    "v_mfma_f32_16x16x32_bf16 v[68:71], v[80:83], v[20:23], v[68:71]\n"
    "s_waitcnt lgkmcnt(1)\n"
    "v_mul_f32 v49, v49, v73\n"
    "v_mul_f32 v50, v50, v74\n"
    "v_mfma_f32_16x16x32_bf16 v[76:79], v[84:87], v[20:23], v[76:79]\n"
    "ds_read_b128 v[20:23], v9 offset:13312\n"
    "v_mfma_f32_16x16x32_bf16 v[40:43], v[88:91], v[24:27], v[40:43]\n"
    "v_mfma_f32_16x16x32_bf16 v[44:47], v[92:95], v[24:27], v[44:47]\n"
    "v_mul_f32 v51, v51, v75\n"
    "v_cvt_pk_bf16_f32 v48, v48, v49\n"
    "v_mfma_f32_16x16x32_bf16 v[52:55], v[96:99], v[24:27], v[52:55]\n"
    "v_cvt_pk_bf16_f32 v49, v50, v51\n"
    "s_nop 0\n"
    "v_mfma_f32_16x16x32_bf16 v[64:67], v[100:103], v[24:27], v[64:67]\n"
    "v_permlane16_swap_b32 v48, v50\n"
    "v_mfma_f32_16x16x32_bf16 v[68:71], v[104:107], v[24:27], v[68:71]\n"
    "s_waitcnt lgkmcnt(1)\n"
    "v_permlane16_swap_b32 v49, v51\n"
    "v_mfma_f32_16x16x32_bf16 v[76:79], v[108:111], v[24:27], v[76:79]\n"
    "ds_read_b128 v[24:27], v9 offset:14336\n"
    "s_branch .LgUP_join1_%=\n"
    ".LgUP_dma1_48_%=:\n"
    "v_mfma_f32_16x16x32_bf16 v[40:43], a[192:195], v[24:27], v[40:43]\n"
    "s_add_u32 m0, s59, 0\n"
    "v_mfma_f32_16x16x32_bf16 v[44:47], a[196:199], v[24:27], v[44:47]\n"
    "buffer_load_dwordx4 v1, s[36:39], s76 offen sc1 lds\n"
    "v_mul_f32 v75, v51, v75\n"
    "v_mul_f32 v48, 0xbfb8aa3b, v48\n"
    "v_mfma_f32_16x16x32_bf16 v[52:55], a[200:203], v[24:27], v[52:55]\n"
    "s_add_u32 s76, s76, 16384\n"
    "v_exp_f32 v48, v48\n"
    "v_mfma_f32_16x16x32_bf16 v[64:67], a[204:207], v[24:27], v[64:67]\n"
    "s_add_u32 m0, s59, 4096\n"
    "v_mul_f32 v49, 0xbfb8aa3b, v49\n"
    "v_mul_f32 v50, 0xbfb8aa3b, v50\n"
    "v_mfma_f32_16x16x32_bf16 v[68:71], a[208:211], v[24:27], v[68:71]\n"
    "s_waitcnt lgkmcnt(1)\n"
    "v_exp_f32 v49, v49\n"
    "v_mfma_f32_16x16x32_bf16 v[76:79], a[212:215], v[24:27], v[76:79]\n"
    "ds_read_b128 v[24:27], v9 offset:11264\n"
    "v_mul_f32 v51, 0xbfb8aa3b, v51\n"
    "v_mfma_f32_16x16x32_bf16 v[40:43], a[216:219], v[16:19], v[40:43]\n"
    "buffer_load_dwordx4 v1, s[36:39], s76 offen sc1 lds\n"
    "v_exp_f32 v50, v50\n"
    "v_mfma_f32_16x16x32_bf16 v[44:47], a[220:223], v[16:19], v[44:47]\n"
    "s_add_u32 s76, s76, 16384\n"
    "v_exp_f32 v51, v51\n"
    "v_mfma_f32_16x16x32_bf16 v[52:55], a[224:227], v[16:19], v[52:55]\n"
    "s_add_u32 m0, s59, 8192\n"
    "v_add_f32 v48, 1.0, v48\n"
    "v_add_f32 v49, 1.0, v49\n"
    "v_mfma_f32_16x16x32_bf16 v[64:67], a[228:231], v[16:19], v[64:67]\n"
    "buffer_load_dwordx4 v1, s[36:39], s76 offen sc1 lds\n"
    "v_rcp_f32 v48, v48\n"
    "v_mfma_f32_16x16x32_bf16 v[68:71], a[232:235], v[16:19], v[68:71]\n"
    "s_waitcnt lgkmcnt(1)\n"
    "v_rcp_f32 v49, v49\n"
    "v_mfma_f32_16x16x32_bf16 v[76:79], a[236:239], v[16:19], v[76:79]\n"
    "ds_read_b128 v[16:19], v9 offset:12288\n"
    "v_add_f32 v50, 1.0, v50\n"
    "v_mfma_f32_16x16x32_bf16 v[40:43], a[240:243], v[20:23], v[40:43]\n"
    "s_add_u32 s76, s76, 16384\n"
    "v_rcp_f32 v50, v50\n"
    "v_mfma_f32_16x16x32_bf16 v[44:47], a[244:247], v[20:23], v[44:47]\n"
    "s_add_u32 m0, s59, 12288\n"
    "v_add_f32 v51, 1.0, v51\n"
    "v_mfma_f32_16x16x32_bf16 v[52:55], a[248:251], v[20:23], v[52:55]\n"
    "buffer_load_dwordx4 v1, s[36:39], s76 offen sc1 lds\n"
    "s_add_u32 s76, s76, 16384\n"
    "v_mul_f32 v48, v48, v72\n"
    "v_mfma_f32_16x16x32_bf16 v[64:67], a[252:255], v[20:23], v[64:67]\n"
    "s_add_u32 m0, s59, 16384\n"
    "v_rcp_f32 v51, v51\n"
    "v_mfma_f32_16x16x32_bf16 v[68:71], v[80:83], v[20:23], v[68:71]\n"
    "s_waitcnt lgkmcnt(1)\n"
    "v_mul_f32 v49, v49, v73\n"
    "v_mul_f32 v50, v50, v74\n"
    "v_mfma_f32_16x16x32_bf16 v[76:79], v[84:87], v[20:23], v[76:79]\n"
    "ds_read_b128 v[20:23], v9 offset:13312\n"
    "buffer_load_dwordx4 v8, s[36:39], s76 offen sc1 lds\n"
    "v_mfma_f32_16x16x32_bf16 v[40:43], v[88:91], v[24:27], v[40:43]\n"
    "v_mfma_f32_16x16x32_bf16 v[44:47], v[92:95], v[24:27], v[44:47]\n"
    "v_mul_f32 v51, v51, v75\n"
    "v_cvt_pk_bf16_f32 v48, v48, v49\n"
    "v_mfma_f32_16x16x32_bf16 v[52:55], v[96:99], v[24:27], v[52:55]\n"
    "v_cvt_pk_bf16_f32 v49, v50, v51\n"
    "s_nop 0\n"
    "v_mfma_f32_16x16x32_bf16 v[64:67], v[100:103], v[24:27], v[64:67]\n"
    "v_permlane16_swap_b32 v48, v50\n"
    "v_mfma_f32_16x16x32_bf16 v[68:71], v[104:107], v[24:27], v[68:71]\n"
    "s_waitcnt lgkmcnt(1)\n"
    "v_permlane16_swap_b32 v49, v51\n"
    "v_mfma_f32_16x16x32_bf16 v[76:79], v[108:111], v[24:27], v[76:79]\n"
    "ds_read_b128 v[24:27], v9 offset:14336\n"
    "s_branch .LgUP_join1_%=\n"
    ".LgUP_dma1_66_%=:\n"
    "v_mfma_f32_16x16x32_bf16 v[40:43], v[88:91], v[24:27], v[40:43]\n"
    "s_add_u32 m0, s59, 0\n"
    "v_mfma_f32_16x16x32_bf16 v[44:47], v[92:95], v[24:27], v[44:47]\n"
    "buffer_load_dwordx4 v1, s[36:39], s76 offen sc1 lds\n"
    "v_mul_f32 v51, v51, v75\n"
    "v_cvt_pk_bf16_f32 v48, v48, v49\n"
    "v_mfma_f32_16x16x32_bf16 v[52:55], v[96:99], v[24:27], v[52:55]\n"
    "s_add_u32 s76, s76, 16384\n"
    "v_cvt_pk_bf16_f32 v49, v50, v51\n"
    "s_nop 0\n"
    "v_mfma_f32_16x16x32_bf16 v[64:67], v[100:103], v[24:27], v[64:67]\n"
    "s_add_u32 m0, s59, 4096\n"
    "v_permlane16_swap_b32 v48, v50\n"
    "v_mfma_f32_16x16x32_bf16 v[68:71], v[104:107], v[24:27], v[68:71]\n"
    "s_waitcnt lgkmcnt(1)\n"
    "v_permlane16_swap_b32 v49, v51\n"
    "v_mfma_f32_16x16x32_bf16 v[76:79], v[108:111], v[24:27], v[76:79]\n"
    "ds_read_b128 v[24:27], v9 offset:14336\n"
    "buffer_load_dwordx4 v1, s[36:39], s76 offen sc1 lds\n"
    "v_mfma_f32_16x16x32_bf16 v[40:43], v[112:115], v[16:19], v[40:43]\n"
    "buffer_store_dwordx4 v[48:51], v4, s[36:39], s73 offen sc1\n"
    "v_mfma_f32_16x16x32_bf16 v[44:47], v[116:119], v[16:19], v[44:47]\n"
    "s_add_u32 s76, s76, 16384\n"
    "s_add_u32 m0, s59, 8192\n"
    "s_lshr_b32 s73, s68, 1\n"
    "v_mfma_f32_16x16x32_bf16 v[52:55], v[120:123], v[16:19], v[52:55]\n"
    "buffer_load_dwordx4 v1, s[36:39], s76 offen sc1 lds\n"
    "s_and_b32 s73, s73, s57\n"
    "v_mfma_f32_16x16x32_bf16 v[64:67], v[124:127], v[16:19], v[64:67]\n"
    "s_mul_i32 s73, s73, s56\n"
    "v_mfma_f32_16x16x32_bf16 v[68:71], v[128:131], v[16:19], v[68:71]\n"
    "s_waitcnt lgkmcnt(1)\n"
    "s_add_u32 s76, s76, 16384\n"
    "s_add_u32 s73, s73, s55\n"
    "v_mfma_f32_16x16x32_bf16 v[76:79], v[132:135], v[16:19], v[76:79]\n"
    "ds_read_b128 v[16:19], v9 offset:15360\n"
    "s_add_u32 m0, s59, 12288\n"
    "v_mfma_f32_16x16x32_bf16 v[40:43], v[136:139], v[20:23], v[40:43]\n"
    "buffer_load_dwordx4 v1, s[36:39], s76 offen sc1 lds\n"
    "v_mfma_f32_16x16x32_bf16 v[44:47], v[140:143], v[20:23], v[44:47]\n"
    "v_mfma_f32_16x16x32_bf16 v[52:55], v[144:147], v[20:23], v[52:55]\n"
    "s_add_u32 s76, s76, 16384\n"
    "s_add_u32 m0, s59, 16384\n"
    "v_mfma_f32_16x16x32_bf16 v[64:67], v[148:151], v[20:23], v[64:67]\n"
    "buffer_load_dwordx4 v8, s[36:39], s76 offen sc1 lds\n"
    "v_mfma_f32_16x16x32_bf16 v[68:71], v[152:155], v[20:23], v[68:71]\n"
    "s_waitcnt lgkmcnt(1)\n"
    "v_mfma_f32_16x16x32_bf16 v[76:79], v[156:159], v[20:23], v[76:79]\n"
    "ds_read_b128 v[20:23], v9 offset:16384\n"
    "v_mfma_f32_16x16x32_bf16 v[40:43], v[160:163], v[24:27], v[40:43]\n"
    "v_mfma_f32_16x16x32_bf16 v[44:47], v[164:167], v[24:27], v[44:47]\n"
    "v_mfma_f32_16x16x32_bf16 v[52:55], v[168:171], v[24:27], v[52:55]\n"
    "v_mfma_f32_16x16x32_bf16 v[64:67], v[172:175], v[24:27], v[64:67]\n"
    "v_mfma_f32_16x16x32_bf16 v[68:71], v[176:179], v[24:27], v[68:71]\n"
    "s_waitcnt lgkmcnt(1)\n"
    "v_mfma_f32_16x16x32_bf16 v[76:79], v[180:183], v[24:27], v[76:79]\n"
    "ds_read_b128 v[24:27], v9 offset:17408\n"
    "v_mfma_f32_16x16x32_bf16 v[40:43], v[184:187], v[16:19], v[40:43]\n"
    "v_mfma_f32_16x16x32_bf16 v[44:47], v[188:191], v[16:19], v[44:47]\n"
    "v_mfma_f32_16x16x32_bf16 v[52:55], v[192:195], v[16:19], v[52:55]\n"
    "v_mfma_f32_16x16x32_bf16 v[64:67], v[196:199], v[16:19], v[64:67]\n"
    "v_mfma_f32_16x16x32_bf16 v[68:71], v[200:203], v[16:19], v[68:71]\n"
    "s_waitcnt lgkmcnt(1)\n"
    "v_mfma_f32_16x16x32_bf16 v[76:79], v[204:207], v[16:19], v[76:79]\n"
    "v_mfma_f32_16x16x32_bf16 v[40:43], v[208:211], v[20:23], v[40:43]\n"
    "v_mfma_f32_16x16x32_bf16 v[44:47], v[212:215], v[20:23], v[44:47]\n"
    "v_mfma_f32_16x16x32_bf16 v[52:55], v[216:219], v[20:23], v[52:55]\n"
    "v_mfma_f32_16x16x32_bf16 v[64:67], v[220:223], v[20:23], v[64:67]\n"
    "v_mfma_f32_16x16x32_bf16 v[68:71], v[224:227], v[20:23], v[68:71]\n"
    "s_waitcnt lgkmcnt(0)\n"
    "v_mfma_f32_16x16x32_bf16 v[76:79], v[228:231], v[20:23], v[76:79]\n"
    "v_mfma_f32_16x16x32_bf16 v[40:43], v[232:235], v[24:27], v[40:43]\n"
    "v_mfma_f32_16x16x32_bf16 v[44:47], v[236:239], v[24:27], v[44:47]\n"
    "v_mfma_f32_16x16x32_bf16 v[52:55], v[240:243], v[24:27], v[52:55]\n"
    "s_waitcnt vmcnt(3)\n"
    "buffer_atomic_smax v12, v14, s[36:39], s70 offen sc1\n"
    "v_mfma_f32_16x16x32_bf16 v[64:67], v[244:247], v[24:27], v[64:67]\n"
    "v_mfma_f32_16x16x32_bf16 v[68:71], v[248:251], v[24:27], v[68:71]\n"
    "v_mfma_f32_16x16x32_bf16 v[76:79], v[252:255], v[24:27], v[76:79]\n"
    "v_pk_mul_f32 v[64:65], v[40:41], v[64:65]\n"
    "v_pk_mul_f32 v[66:67], v[42:43], v[66:67]\n"
    "s_nop 1\n"
    "v_pk_mul_f32 v[68:69], v[44:45], v[68:69]\n"
    "v_pk_mul_f32 v[70:71], v[46:47], v[70:71]\n"
    "v_pk_mul_f32 v[40:41], v[40:41], s[64:65] op_sel_hi:[1,0]\n"
    "v_pk_mul_f32 v[42:43], v[42:43], s[64:65] op_sel_hi:[1,0]\n"
    "v_pk_mul_f32 v[44:45], v[44:45], s[64:65] op_sel_hi:[1,0]\n"
    "v_pk_mul_f32 v[46:47], v[46:47], s[64:65] op_sel_hi:[1,0]\n"
    "v_exp_f32 v40, v40\n"
    "v_exp_f32 v41, v41\n"
    "v_exp_f32 v42, v42\n"
    "v_exp_f32 v43, v43\n"
    "v_exp_f32 v44, v44\n"
    "v_exp_f32 v45, v45\n"
    "v_exp_f32 v46, v46\n"
    "v_exp_f32 v47, v47\n"
    "v_pk_add_f32 v[40:41], v[40:41], 1.0 op_sel_hi:[1,0]\n"
    "v_pk_add_f32 v[42:43], v[42:43], 1.0 op_sel_hi:[1,0]\n"
    "v_pk_add_f32 v[44:45], v[44:45], 1.0 op_sel_hi:[1,0]\n"
    "v_pk_add_f32 v[46:47], v[46:47], 1.0 op_sel_hi:[1,0]\n"
    "v_rcp_f32 v40, v40\n"
    "v_rcp_f32 v41, v41\n"
    "v_rcp_f32 v42, v42\n"
    "v_rcp_f32 v43, v43\n"
    "v_rcp_f32 v44, v44\n"
    "v_rcp_f32 v45, v45\n"
    "v_rcp_f32 v46, v46\n"
    "v_rcp_f32 v47, v47\n"
    "v_pk_mul_f32 v[40:41], v[40:41], v[64:65]\n"
    "v_pk_mul_f32 v[42:43], v[42:43], v[66:67]\n"
    "v_pk_mul_f32 v[44:45], v[44:45], v[68:69]\n"
    "v_pk_mul_f32 v[46:47], v[46:47], v[70:71]\n"
    "v_cvt_pk_bf16_f32 v40, v40, v41\n"
    "v_cvt_pk_bf16_f32 v41, v42, v43\n"
    "v_cvt_pk_bf16_f32 v42, v44, v45\n"
    "v_cvt_pk_bf16_f32 v43, v46, v47\n"
    "buffer_store_dwordx4 v[40:43], v15, s[36:39], s73 offen offset:1024 sc1\n"
    "v_pk_mul_f32 v[76:77], v[52:53], v[76:77]\n"
    "v_pk_mul_f32 v[78:79], v[54:55], v[78:79]\n"
    "v_pk_mul_f32 v[52:53], v[52:53], s[64:65] op_sel_hi:[1,0]\n"
    "v_pk_mul_f32 v[54:55], v[54:55], s[64:65] op_sel_hi:[1,0]\n"
    "v_exp_f32 v52, v52\n"
    "v_exp_f32 v53, v53\n"
    "v_exp_f32 v54, v54\n"
    "v_exp_f32 v55, v55\n"
    "v_pk_add_f32 v[52:53], v[52:53], 1.0 op_sel_hi:[1,0]\n"
    "v_pk_add_f32 v[54:55], v[54:55], 1.0 op_sel_hi:[1,0]\n"
    "v_rcp_f32 v52, v52\n"
    "v_rcp_f32 v53, v53\n"
    "v_rcp_f32 v54, v54\n"
    "v_rcp_f32 v55, v55\n"
    "v_pk_mul_f32 v[52:53], v[52:53], v[76:77]\n"
    "v_pk_mul_f32 v[54:55], v[54:55], v[78:79]\n"
    "v_cvt_pk_bf16_f32 v52, v52, v53\n"
    "v_cvt_pk_bf16_f32 v53, v54, v55\n"
    "s_nop 0\n"
    "v_permlane16_swap_b32 v52, v54\n"
    "v_permlane16_swap_b32 v53, v55\n"
    "buffer_store_dwordx4 v[52:55], v4, s[36:39], s73 offen offset:1024 sc1\n"
    "v_mov_b32 v12, s68\n"
    "s_mov_b32 s73, 0x80000000\n"
    "s_mov_b32 s68, s71\n"
    "s_mov_b32 s69, 0\n"
    "s_mov_b32 s70, 0x80000000\n"
    "v_mov_b32 v7, v12\n"
    "s_waitcnt vmcnt(9)\n"
    "s_add_u32 s71, s68, 1\n"
    "v_add_u32 v13, v11, v3\n"
    "v_cmp_gt_i32 vcc, s71, v13\n"
    "s_nop 0\n"
    "s_or_b64 exec, vcc, s[62:63]\n"
    "buffer_atomic_smax v11, v2, s[36:39], 0 offen sc0 sc1\n"
    "s_mov_b64 exec, -1\n"
    "s_waitcnt vmcnt(4)\n"
    "s_branch .LgUP_bar0_%=\n"
    ".LgUP_dma1_84_%=:\n"
    "v_mfma_f32_16x16x32_bf16 v[40:43], v[160:163], v[24:27], v[40:43]\n"
    "s_add_u32 m0, s59, 0\n"
    "v_mfma_f32_16x16x32_bf16 v[44:47], v[164:167], v[24:27], v[44:47]\n"
    "buffer_load_dwordx4 v1, s[36:39], s76 offen sc1 lds\n"
    "v_mfma_f32_16x16x32_bf16 v[52:55], v[168:171], v[24:27], v[52:55]\n"
    "s_add_u32 s76, s76, 16384\n"
    "s_add_u32 m0, s59, 4096\n"
    "v_mfma_f32_16x16x32_bf16 v[64:67], v[172:175], v[24:27], v[64:67]\n"
    "buffer_load_dwordx4 v1, s[36:39], s76 offen sc1 lds\n"
    "v_mfma_f32_16x16x32_bf16 v[68:71], v[176:179], v[24:27], v[68:71]\n"
    "s_waitcnt lgkmcnt(1)\n"
    "v_mfma_f32_16x16x32_bf16 v[76:79], v[180:183], v[24:27], v[76:79]\n"
    "ds_read_b128 v[24:27], v9 offset:17408\n"
    "v_mfma_f32_16x16x32_bf16 v[40:43], v[184:187], v[16:19], v[40:43]\n"
    "s_add_u32 s76, s76, 16384\n"
    "s_add_u32 m0, s59, 8192\n"
    "v_mfma_f32_16x16x32_bf16 v[44:47], v[188:191], v[16:19], v[44:47]\n"
    "buffer_load_dwordx4 v1, s[36:39], s76 offen sc1 lds\n"
    "v_mfma_f32_16x16x32_bf16 v[52:55], v[192:195], v[16:19], v[52:55]\n"
    "v_mfma_f32_16x16x32_bf16 v[64:67], v[196:199], v[16:19], v[64:67]\n"
    "v_mfma_f32_16x16x32_bf16 v[68:71], v[200:203], v[16:19], v[68:71]\n"
    "s_waitcnt lgkmcnt(1)\n"
    "s_add_u32 s76, s76, 16384\n"
    "s_add_u32 m0, s59, 12288\n"
    "v_mfma_f32_16x16x32_bf16 v[76:79], v[204:207], v[16:19], v[76:79]\n"
    "buffer_load_dwordx4 v1, s[36:39], s76 offen sc1 lds\n"
    "v_mfma_f32_16x16x32_bf16 v[40:43], v[208:211], v[20:23], v[40:43]\n"
    "v_mfma_f32_16x16x32_bf16 v[44:47], v[212:215], v[20:23], v[44:47]\n"
    "v_mfma_f32_16x16x32_bf16 v[52:55], v[216:219], v[20:23], v[52:55]\n"
    "s_add_u32 s76, s76, 16384\n"
    "s_add_u32 m0, s59, 16384\n"
    "v_mfma_f32_16x16x32_bf16 v[64:67], v[220:223], v[20:23], v[64:67]\n"
    "buffer_load_dwordx4 v8, s[36:39], s76 offen sc1 lds\n"
    "v_mfma_f32_16x16x32_bf16 v[68:71], v[224:227], v[20:23], v[68:71]\n"
    "s_waitcnt lgkmcnt(0)\n"
    "v_mfma_f32_16x16x32_bf16 v[76:79], v[228:231], v[20:23], v[76:79]\n"
    "v_mfma_f32_16x16x32_bf16 v[40:43], v[232:235], v[24:27], v[40:43]\n"
    "v_mfma_f32_16x16x32_bf16 v[44:47], v[236:239], v[24:27], v[44:47]\n"
    "v_mfma_f32_16x16x32_bf16 v[52:55], v[240:243], v[24:27], v[52:55]\n"
    "s_waitcnt vmcnt(5)\n"
    "buffer_atomic_smax v12, v14, s[36:39], s70 offen sc1\n"
    "v_mfma_f32_16x16x32_bf16 v[64:67], v[244:247], v[24:27], v[64:67]\n"
    "v_mfma_f32_16x16x32_bf16 v[68:71], v[248:251], v[24:27], v[68:71]\n"
    "v_mfma_f32_16x16x32_bf16 v[76:79], v[252:255], v[24:27], v[76:79]\n"
    "v_pk_mul_f32 v[64:65], v[40:41], v[64:65]\n"
    "v_pk_mul_f32 v[66:67], v[42:43], v[66:67]\n"
    "s_nop 1\n"
    "v_pk_mul_f32 v[68:69], v[44:45], v[68:69]\n"
    "v_pk_mul_f32 v[70:71], v[46:47], v[70:71]\n"
    "v_pk_mul_f32 v[40:41], v[40:41], s[64:65] op_sel_hi:[1,0]\n"
    "v_pk_mul_f32 v[42:43], v[42:43], s[64:65] op_sel_hi:[1,0]\n"
    "v_pk_mul_f32 v[44:45], v[44:45], s[64:65] op_sel_hi:[1,0]\n"
    "v_pk_mul_f32 v[46:47], v[46:47], s[64:65] op_sel_hi:[1,0]\n"
    "v_exp_f32 v40, v40\n"
    "v_exp_f32 v41, v41\n"
    "v_exp_f32 v42, v42\n"
    "v_exp_f32 v43, v43\n"
    "v_exp_f32 v44, v44\n"
    "v_exp_f32 v45, v45\n"
    "v_exp_f32 v46, v46\n"
    "v_exp_f32 v47, v47\n"
    "v_pk_add_f32 v[40:41], v[40:41], 1.0 op_sel_hi:[1,0]\n"
    "v_pk_add_f32 v[42:43], v[42:43], 1.0 op_sel_hi:[1,0]\n"
    "v_pk_add_f32 v[44:45], v[44:45], 1.0 op_sel_hi:[1,0]\n"
    "v_pk_add_f32 v[46:47], v[46:47], 1.0 op_sel_hi:[1,0]\n"
    "v_rcp_f32 v40, v40\n"
    "v_rcp_f32 v41, v41\n"
    "v_rcp_f32 v42, v42\n"
    "v_rcp_f32 v43, v43\n"
    "v_rcp_f32 v44, v44\n"
    "v_rcp_f32 v45, v45\n"
    "v_rcp_f32 v46, v46\n"
    "v_rcp_f32 v47, v47\n"
    "v_pk_mul_f32 v[40:41], v[40:41], v[64:65]\n"
    "v_pk_mul_f32 v[42:43], v[42:43], v[66:67]\n"
    "v_pk_mul_f32 v[44:45], v[44:45], v[68:69]\n"
    "v_pk_mul_f32 v[46:47], v[46:47], v[70:71]\n"
    "v_cvt_pk_bf16_f32 v40, v40, v41\n"
    "v_cvt_pk_bf16_f32 v41, v42, v43\n"
    "v_cvt_pk_bf16_f32 v42, v44, v45\n"
    "v_cvt_pk_bf16_f32 v43, v46, v47\n"
    "buffer_store_dwordx4 v[40:43], v15, s[36:39], s73 offen offset:1024 sc1\n"
    "v_pk_mul_f32 v[76:77], v[52:53], v[76:77]\n"
    "v_pk_mul_f32 v[78:79], v[54:55], v[78:79]\n"
    "v_pk_mul_f32 v[52:53], v[52:53], s[64:65] op_sel_hi:[1,0]\n"
    "v_pk_mul_f32 v[54:55], v[54:55], s[64:65] op_sel_hi:[1,0]\n"
    "v_exp_f32 v52, v52\n"
    "v_exp_f32 v53, v53\n"
    "v_exp_f32 v54, v54\n"
    "v_exp_f32 v55, v55\n"
    "v_pk_add_f32 v[52:53], v[52:53], 1.0 op_sel_hi:[1,0]\n"
    "v_pk_add_f32 v[54:55], v[54:55], 1.0 op_sel_hi:[1,0]\n"
    "v_rcp_f32 v52, v52\n"
    "v_rcp_f32 v53, v53\n"
    "v_rcp_f32 v54, v54\n"
    "v_rcp_f32 v55, v55\n"
    "v_pk_mul_f32 v[52:53], v[52:53], v[76:77]\n"
    "v_pk_mul_f32 v[54:55], v[54:55], v[78:79]\n"
    "v_cvt_pk_bf16_f32 v52, v52, v53\n"
    "v_cvt_pk_bf16_f32 v53, v54, v55\n"
    "s_nop 0\n"
    "v_permlane16_swap_b32 v52, v54\n"
    "v_permlane16_swap_b32 v53, v55\n"
    "buffer_store_dwordx4 v[52:55], v4, s[36:39], s73 offen offset:1024 sc1\n"
    "v_mov_b32 v12, s68\n"
    "s_mov_b32 s73, 0x80000000\n"
    "s_mov_b32 s68, s71\n"
    "s_mov_b32 s69, 0\n"
    "s_mov_b32 s70, 0x80000000\n"
    "v_mov_b32 v7, v12\n"
    "s_waitcnt vmcnt(9)\n"
    "s_add_u32 s71, s68, 1\n"
    "v_add_u32 v13, v11, v3\n"
    "v_cmp_gt_i32 vcc, s71, v13\n"
    "s_nop 0\n"
    "s_or_b64 exec, vcc, s[62:63]\n"
    "buffer_atomic_smax v11, v2, s[36:39], 0 offen sc0 sc1\n"
    "s_mov_b64 exec, -1\n"
    "s_waitcnt vmcnt(4)\n"
    "s_branch .LgUP_bar0_%=\n"
    ".LgUP_dma1_102_%=:\n"
    "v_mfma_f32_16x16x32_bf16 v[40:43], v[232:235], v[24:27], v[40:43]\n"
    "s_add_u32 m0, s59, 0\n"
    "v_mfma_f32_16x16x32_bf16 v[44:47], v[236:239], v[24:27], v[44:47]\n"
    "buffer_load_dwordx4 v1, s[36:39], s76 offen sc1 lds\n"
    "v_mfma_f32_16x16x32_bf16 v[52:55], v[240:243], v[24:27], v[52:55]\n"
    "s_add_u32 s76, s76, 16384\n"
    "s_waitcnt vmcnt(2)\n"
    "buffer_atomic_smax v12, v14, s[36:39], s70 offen sc1\n"
    "v_mfma_f32_16x16x32_bf16 v[64:67], v[244:247], v[24:27], v[64:67]\n"
    "s_add_u32 m0, s59, 4096\n"
    "v_mfma_f32_16x16x32_bf16 v[68:71], v[248:251], v[24:27], v[68:71]\n"
    "buffer_load_dwordx4 v1, s[36:39], s76 offen sc1 lds\n"
    "v_mfma_f32_16x16x32_bf16 v[76:79], v[252:255], v[24:27], v[76:79]\n"
    "v_pk_mul_f32 v[64:65], v[40:41], v[64:65]\n"
    "v_pk_mul_f32 v[66:67], v[42:43], v[66:67]\n"
    "s_nop 1\n"
    "v_pk_mul_f32 v[68:69], v[44:45], v[68:69]\n"
    "v_pk_mul_f32 v[70:71], v[46:47], v[70:71]\n"
    "v_pk_mul_f32 v[40:41], v[40:41], s[64:65] op_sel_hi:[1,0]\n"
    "v_pk_mul_f32 v[42:43], v[42:43], s[64:65] op_sel_hi:[1,0]\n"
    "v_pk_mul_f32 v[44:45], v[44:45], s[64:65] op_sel_hi:[1,0]\n"
    "v_pk_mul_f32 v[46:47], v[46:47], s[64:65] op_sel_hi:[1,0]\n"
    "v_exp_f32 v40, v40\n"
    "v_exp_f32 v41, v41\n"
    "v_exp_f32 v42, v42\n"
    "v_exp_f32 v43, v43\n"
    "v_exp_f32 v44, v44\n"
    "v_exp_f32 v45, v45\n"
    "v_exp_f32 v46, v46\n"
    "v_exp_f32 v47, v47\n"
    "v_pk_add_f32 v[40:41], v[40:41], 1.0 op_sel_hi:[1,0]\n"
    "v_pk_add_f32 v[42:43], v[42:43], 1.0 op_sel_hi:[1,0]\n"
    "v_pk_add_f32 v[44:45], v[44:45], 1.0 op_sel_hi:[1,0]\n"
    "v_pk_add_f32 v[46:47], v[46:47], 1.0 op_sel_hi:[1,0]\n"
    "v_rcp_f32 v40, v40\n"
    "v_rcp_f32 v41, v41\n"
    "v_rcp_f32 v42, v42\n"
    "v_rcp_f32 v43, v43\n"
    "v_rcp_f32 v44, v44\n"
    "v_rcp_f32 v45, v45\n"
    "v_rcp_f32 v46, v46\n"
    "v_rcp_f32 v47, v47\n"
    "v_pk_mul_f32 v[40:41], v[40:41], v[64:65]\n"
    "v_pk_mul_f32 v[42:43], v[42:43], v[66:67]\n"
    "v_pk_mul_f32 v[44:45], v[44:45], v[68:69]\n"
    "v_pk_mul_f32 v[46:47], v[46:47], v[70:71]\n"
    "v_cvt_pk_bf16_f32 v40, v40, v41\n"
    "v_cvt_pk_bf16_f32 v41, v42, v43\n"
    "v_cvt_pk_bf16_f32 v42, v44, v45\n"
    "v_cvt_pk_bf16_f32 v43, v46, v47\n"
    "buffer_store_dwordx4 v[40:43], v15, s[36:39], s73 offen offset:1024 sc1\n"
    "v_pk_mul_f32 v[76:77], v[52:53], v[76:77]\n"
    "v_pk_mul_f32 v[78:79], v[54:55], v[78:79]\n"
    "v_pk_mul_f32 v[52:53], v[52:53], s[64:65] op_sel_hi:[1,0]\n"
    "v_pk_mul_f32 v[54:55], v[54:55], s[64:65] op_sel_hi:[1,0]\n"
    "v_exp_f32 v52, v52\n"
    "v_exp_f32 v53, v53\n"
    "v_exp_f32 v54, v54\n"
    "v_exp_f32 v55, v55\n"
    "v_pk_add_f32 v[52:53], v[52:53], 1.0 op_sel_hi:[1,0]\n"
    "v_pk_add_f32 v[54:55], v[54:55], 1.0 op_sel_hi:[1,0]\n"
    "v_rcp_f32 v52, v52\n"
    "v_rcp_f32 v53, v53\n"
    "v_rcp_f32 v54, v54\n"
    "v_rcp_f32 v55, v55\n"
    "v_pk_mul_f32 v[52:53], v[52:53], v[76:77]\n"
    "v_pk_mul_f32 v[54:55], v[54:55], v[78:79]\n"
    "v_cvt_pk_bf16_f32 v52, v52, v53\n"
    "v_cvt_pk_bf16_f32 v53, v54, v55\n"
    "s_nop 0\n"
    "v_permlane16_swap_b32 v52, v54\n"
    "v_permlane16_swap_b32 v53, v55\n"
    "buffer_store_dwordx4 v[52:55], v4, s[36:39], s73 offen offset:1024 sc1\n"
    "v_mov_b32 v12, s68\n"
    "s_mov_b32 s73, 0x80000000\n"
    "s_mov_b32 s68, s71\n"
    "s_add_u32 m0, s59, 8192\n"
    "s_add_u32 s76, s76, 16384\n"
    "buffer_load_dwordx4 v1, s[36:39], s76 offen sc1 lds\n"
    "s_add_u32 m0, s59, 12288\n"
    "s_add_u32 s76, s76, 16384\n"
    "buffer_load_dwordx4 v1, s[36:39], s76 offen sc1 lds\n"
    "s_add_u32 m0, s59, 16384\n"
    "s_add_u32 s76, s76, 16384\n"
    "buffer_load_dwordx4 v8, s[36:39], s76 offen sc1 lds\n"
    "s_waitcnt vmcnt(3)\n"
    "buffer_atomic_smax v12, v10, s[36:39], 0 offen sc1\n"
    "s_mov_b32 s69, 0x80000000\n"
    "s_mov_b32 s70, 0x80000000\n"
    "v_mov_b32 v7, v12\n"
    "s_waitcnt vmcnt(9)\n"
    "s_add_u32 s71, s68, 1\n"
    "v_add_u32 v13, v11, v3\n"
    "v_cmp_gt_i32 vcc, s71, v13\n"
    "s_nop 0\n"
    "s_or_b64 exec, vcc, s[62:63]\n"
    "buffer_atomic_smax v11, v2, s[36:39], 0 offen sc0 sc1\n"
    "s_mov_b64 exec, -1\n"
    "s_waitcnt vmcnt(2)\n"
    "s_branch .LgUP_bar0_%=\n"
    ".LgUP_idle1_%=:\n"
    "s_or_b64 exec, vcc, s[62:63]\n"
    "buffer_atomic_smax v11, v2, s[36:39], 0 offen sc0 sc1\n"
    "s_mov_b64 exec, -1\n"
    "s_waitcnt vmcnt(1)\n"
    "buffer_atomic_smax v12, v10, s[36:39], 0 offen sc1\n"
    "s_branch .LgUP_block1_0_%=\n"
    ".LgUP_block1_%=:\n"
    "v_add_u32 v13, v11, v3\n"
    "v_cmp_gt_i32 vcc, s71, v13\n"
    "s_nop 0\n"
    "s_or_b64 exec, vcc, s[62:63]\n"
    "buffer_atomic_smax v11, v2, s[36:39], 0 offen sc0 sc1\n"
    "s_mov_b64 exec, -1\n"
    "s_sleep 3\n"
    "s_waitcnt vmcnt(1)\n"
    ".LgUP_block1_0_%=:\n"
    "v_add_u32 v13, v5, v3\n"
    "v_cmp_gt_i32 vcc, s71, v13\n"
    "s_cbranch_vccz .LgUP_go1_%=\n"
    "v_add_u32 v13, v5, v3\n"
    "v_cmp_gt_i32 vcc, s71, v13\n"
    "s_nop 0\n"
    "s_or_b64 exec, vcc, s[62:63]\n"
    "buffer_atomic_smax v5, v2, s[36:39], 0 offen sc0 sc1\n"
    "s_mov_b64 exec, -1\n"
    "s_sleep 3\n"
    "s_waitcnt vmcnt(1)\n"
    ".LgUP_block1_1_%=:\n"
    "v_add_u32 v13, v11, v3\n"
    "v_cmp_gt_i32 vcc, s71, v13\n"
    "s_cbranch_vccz .LgUP_go1_%=\n"
    "s_sub_u32 s72, s72, 1\n"
    "s_cbranch_scc0 .LgUP_block1_%=\n"
    "s_mov_b32 s72, 0\n"
    ".LgUP_go1_%=:\n"
    "s_lshr_b32 s76, s71, 1\n"
    "s_and_b32 s76, s76, s54\n"
    "s_mul_i32 s76, s76, s53\n"
    "s_add_u32 s76, s76, s52\n"
    "s_add_u32 m0, s59, 0\n"
    "s_nop 0\n"
    "buffer_load_dwordx4 v1, s[36:39], s76 offen sc1 lds\n"
    "s_add_u32 m0, s59, 4096\n"
    "s_add_u32 s76, s76, 16384\n"
    "buffer_load_dwordx4 v1, s[36:39], s76 offen sc1 lds\n"
    "s_add_u32 m0, s59, 8192\n"
    "s_add_u32 s76, s76, 16384\n"
    "buffer_load_dwordx4 v1, s[36:39], s76 offen sc1 lds\n"
    "s_add_u32 m0, s59, 12288\n"
    "s_add_u32 s76, s76, 16384\n"
    "buffer_load_dwordx4 v1, s[36:39], s76 offen sc1 lds\n"
    "s_add_u32 m0, s59, 16384\n"
    "s_add_u32 s76, s76, 16384\n"
    "buffer_load_dwordx4 v8, s[36:39], s76 offen sc1 lds\n"
    "s_mov_b32 s69, 0x80000000\n"
    "s_mov_b32 s70, 0x80000000\n"
    "v_mov_b32 v7, v12\n"
    "s_waitcnt vmcnt(5)\n"
    "s_add_u32 s71, s68, 1\n"
    "v_add_u32 v13, v11, v3\n"
    "v_cmp_gt_i32 vcc, s71, v13\n"
    "s_nop 0\n"
    "s_or_b64 exec, vcc, s[62:63]\n"
    "buffer_atomic_smax v11, v2, s[36:39], 0 offen sc0 sc1\n"
    "s_mov_b64 exec, -1\n"
    "s_waitcnt vmcnt(1)\n"
    "s_branch .LgUP_bar0_%=\n"
    ".LgUP_bar1_%=:\n"
    "s_barrier\n"
    "ds_read_b128 v[16:19], v9 offset:0\n"
    "ds_read_b128 v[20:23], v9 offset:1024\n"
    "ds_read_b128 v[24:27], v9 offset:2048\n"
    "v_add_u32 v13, v5, v3\n"
    "v_cmp_gt_i32 vcc, s71, v13\n"
    "s_nop 0\n"
    "s_or_b64 exec, vcc, s[62:63]\n"
    "buffer_atomic_smax v5, v2, s[36:39], 0 offen sc0 sc1\n"
    "s_mov_b64 exec, -1\n"
    "s_lshr_b32 s76, s71, 1\n"
    "s_and_b32 s76, s76, s54\n"
    "s_mul_i32 s76, s76, s53\n"
    "s_add_u32 s76, s76, s52\n"
    "s_waitcnt lgkmcnt(2)\n"
    "s_branch .LgUP_head1_%=\n"
    ".LgUP_FIN_%=:\n"
  );
}
DEVI void role_wo(const Params& p, int L, int j, int i, u32 dbg, unsigned long long tag) {
  const int lane = threadIdx.x & 63, w = threadIdx.x >> 6, e0 = j * 1024 * 576 + i * 192;
  GEMM_DECL;
  g_w0 = make_srd(p.Wo + e0, 4096u * 576 * 2 - e0 * 2); g_w1 = g_w0; g_w2 = g_w0;
  g_ldw = 1152;
  g_in_base = pgb(L, B_O + 8 * j); g_in_stride = SLOT; g_in_mask = RO - 1;
  g_out_base = pgb(L, B_PA + 5 * j); g_out_kb = 12 * i; g_out_mask = RP - 1;
  g_flag = lane < 4 ? mbox(L, lane, SL_REDA, 12 * j + 4 * i + w) : ((lane < 12 && w == 0) ? mbox_att(L, j, lane - 4, 4 + i) : OOB);
  g_paddr = mbox(L, j, SL_WO + i, lane);
  g_pbias = lane < 8 ? 0 : (lane < 24 ? 2 * RP : 1000);
  v16u sa, sb, va;
  gemm_args(GEMM_ARGS, p, dbg, tag, sa, sb, va);
  RUN_ASM(
    "s_nop 4\n"
    "s_getpc_b64 s[84:85]\n"
    ".LgWO_PC_%=:\n"
    "s_and_b32 s85, s85, 0xffff\n"
    "s_mov_b32 s86, .LgWO_FIN_%=-.LgWO_PC_%=\n"
    "s_mov_b32 s87, 0x20000\n"
    "v_mbcnt_lo_u32_b32 v40, -1, 0\n"
    "v_mbcnt_hi_u32_b32 v40, -1, v40\n"
    "v_lshlrev_b32 v40, 7, v40\n"
    "buffer_load_dword v41, v40, s[84:87], 0 offen\n"
    "v_add_u32 v40, 8192, v40\n"
    "buffer_load_dword v41, v40, s[84:87], 0 offen\n"
    "v_add_u32 v40, 8192, v40\n"
    "buffer_load_dword v41, v40, s[84:87], 0 offen\n"
    "s_mov_b32 s68, 0\n"
    "s_mov_b32 s71, 0\n"
    "v_add_u32 v9, 32768, v0\n"
    "s_mov_b32 s64, 0xbfb8aa3b\n"
    "s_mov_b32 s72, 262144\n"
    "s_add_u32 m0, s59, 0\n"
    "s_mov_b32 s74, 0\n"
    "buffer_load_dwordx4 v5, s[40:43], s74 offen lds\n"
    "s_add_u32 m0, s59, 4096\n"
    "s_mov_b32 s74, 128\n"
    "buffer_load_dwordx4 v5, s[40:43], s74 offen lds\n"
    "s_add_u32 m0, s59, 8192\n"
    "s_mov_b32 s74, 256\n"
    "buffer_load_dwordx4 v5, s[40:43], s74 offen lds\n"
    "s_add_u32 m0, s59, 12288\n"
    "s_mov_b32 s74, 36864\n"
    "buffer_load_dwordx4 v5, s[40:43], s74 offen lds\n"
    "s_add_u32 m0, s59, 16384\n"
    "s_mov_b32 s74, 36992\n"
    "buffer_load_dwordx4 v5, s[40:43], s74 offen lds\n"
    "s_add_u32 m0, s59, 20480\n"
    "s_mov_b32 s74, 37120\n"
    "buffer_load_dwordx4 v5, s[40:43], s74 offen lds\n"
    "s_add_u32 m0, s59, 24576\n"
    "s_mov_b32 s74, 73728\n"
    "buffer_load_dwordx4 v5, s[40:43], s74 offen lds\n"
    "s_add_u32 m0, s59, 28672\n"
    "s_mov_b32 s74, 73856\n"
    "buffer_load_dwordx4 v5, s[40:43], s74 offen lds\n"
    "s_add_u32 m0, s59, 32768\n"
    "s_mov_b32 s74, 73984\n"
    "buffer_load_dwordx4 v5, s[40:43], s74 offen lds\n"
    "s_add_u32 m0, s59, 36864\n"
    "s_mov_b32 s74, 110592\n"
    "buffer_load_dwordx4 v5, s[40:43], s74 offen lds\n"
    "s_add_u32 m0, s59, 40960\n"
    "s_mov_b32 s74, 110720\n"
    "buffer_load_dwordx4 v5, s[40:43], s74 offen lds\n"
    "s_add_u32 m0, s59, 45056\n"
    "s_mov_b32 s74, 110848\n"
    "buffer_load_dwordx4 v5, s[40:43], s74 offen lds\n"
    "s_add_u32 m0, s59, 49152\n"
    "s_mov_b32 s74, 147456\n"
    "buffer_load_dwordx4 v5, s[40:43], s74 offen lds\n"
    "s_add_u32 m0, s59, 53248\n"
    "s_mov_b32 s74, 147584\n"
    "buffer_load_dwordx4 v5, s[40:43], s74 offen lds\n"
    "s_add_u32 m0, s59, 57344\n"
    "s_mov_b32 s74, 147712\n"
    "buffer_load_dwordx4 v5, s[40:43], s74 offen lds\n"
    "s_add_u32 m0, s59, 61440\n"
    "s_mov_b32 s74, 184320\n"
    "buffer_load_dwordx4 v5, s[40:43], s74 offen lds\n"
    "s_add_u32 m0, s59, 65536\n"
    "s_mov_b32 s74, 184448\n"
    "buffer_load_dwordx4 v5, s[40:43], s74 offen lds\n"
    "s_add_u32 m0, s59, 69632\n"
    "s_mov_b32 s74, 184576\n"
    "buffer_load_dwordx4 v5, s[40:43], s74 offen lds\n"
    "s_add_u32 m0, s59, 73728\n"
    "s_mov_b32 s74, 221184\n"
    "buffer_load_dwordx4 v5, s[40:43], s74 offen lds\n"
    "s_add_u32 m0, s59, 77824\n"
    "s_mov_b32 s74, 221312\n"
    "buffer_load_dwordx4 v5, s[40:43], s74 offen lds\n"
    "s_add_u32 m0, s59, 81920\n"
    "s_mov_b32 s74, 221440\n"
    "buffer_load_dwordx4 v5, s[40:43], s74 offen lds\n"
    "s_add_u32 m0, s59, 86016\n"
    "s_mov_b32 s74, 258048\n"
    "buffer_load_dwordx4 v5, s[40:43], s74 offen lds\n"
    "s_add_u32 m0, s59, 90112\n"
    "s_mov_b32 s74, 258176\n"
    "buffer_load_dwordx4 v5, s[40:43], s74 offen lds\n"
    "s_add_u32 m0, s59, 94208\n"
    "s_mov_b32 s74, 258304\n"
    "buffer_load_dwordx4 v5, s[40:43], s74 offen lds\n"
    "s_add_u32 m0, s59, 98304\n"
    "s_mov_b32 s74, 294912\n"
    "buffer_load_dwordx4 v5, s[40:43], s74 offen lds\n"
    "s_add_u32 m0, s59, 102400\n"
    "s_mov_b32 s74, 295040\n"
    "buffer_load_dwordx4 v5, s[40:43], s74 offen lds\n"
    "s_add_u32 m0, s59, 106496\n"
    "s_mov_b32 s74, 295168\n"
    "buffer_load_dwordx4 v5, s[40:43], s74 offen lds\n"
    "s_add_u32 m0, s59, 110592\n"
    "s_mov_b32 s74, 331776\n"
    "buffer_load_dwordx4 v5, s[40:43], s74 offen lds\n"
    "s_add_u32 m0, s59, 114688\n"
    "s_mov_b32 s74, 331904\n"
    "buffer_load_dwordx4 v5, s[40:43], s74 offen lds\n"
    "s_add_u32 m0, s59, 118784\n"
    "s_mov_b32 s74, 332032\n"
    "buffer_load_dwordx4 v5, s[40:43], s74 offen lds\n"
    "v_add_u32 v16, 49152, v6\n"
    "v_add_u32 v17, 98304, v6\n"
    "v_add_u32 v18, 49152, v7\n"
    "v_add_u32 v19, 98304, v7\n"
    "s_waitcnt vmcnt(24) lgkmcnt(0)\n"
    "s_barrier\n"
    "s_add_u32 m0, s59, 122880\n"
    "s_mov_b32 s74, 368640\n"
    "buffer_load_dwordx4 v5, s[40:43], s74 offen lds\n"
    "s_add_u32 m0, s59, 126976\n"
    "s_mov_b32 s74, 368768\n"
    "buffer_load_dwordx4 v5, s[40:43], s74 offen lds\n"
    "s_add_u32 m0, s59, 131072\n"
    "s_mov_b32 s74, 368896\n"
    "buffer_load_dwordx4 v5, s[40:43], s74 offen lds\n"
    "s_add_u32 m0, s59, 135168\n"
    "s_mov_b32 s74, 405504\n"
    "buffer_load_dwordx4 v5, s[40:43], s74 offen lds\n"
    "s_add_u32 m0, s59, 139264\n"
    "s_mov_b32 s74, 405632\n"
    "buffer_load_dwordx4 v5, s[40:43], s74 offen lds\n"
    "s_add_u32 m0, s59, 143360\n"
    "s_mov_b32 s74, 405760\n"
    "buffer_load_dwordx4 v5, s[40:43], s74 offen lds\n"
    "ds_read_b64_tr_b16 a[0:1], v6 offset:0\n"
    "ds_read_b64_tr_b16 a[2:3], v6 offset:512\n"
    "ds_read_b64_tr_b16 a[4:5], v6 offset:8\n"
    "ds_read_b64_tr_b16 a[6:7], v6 offset:520\n"
    "ds_read_b64_tr_b16 a[8:9], v7 offset:0\n"
    "ds_read_b64_tr_b16 a[10:11], v7 offset:512\n"
    "ds_read_b64_tr_b16 a[12:13], v6 offset:12288\n"
    "ds_read_b64_tr_b16 a[14:15], v6 offset:12800\n"
    "ds_read_b64_tr_b16 a[16:17], v6 offset:12296\n"
    "ds_read_b64_tr_b16 a[18:19], v6 offset:12808\n"
    "ds_read_b64_tr_b16 a[20:21], v7 offset:12288\n"
    "ds_read_b64_tr_b16 a[22:23], v7 offset:12800\n"
    "s_waitcnt vmcnt(24) lgkmcnt(0)\n"
    "s_barrier\n"
    "s_add_u32 m0, s59, 0\n"
    "s_mov_b32 s74, 442368\n"
    "buffer_load_dwordx4 v5, s[40:43], s74 offen lds\n"
    "s_add_u32 m0, s59, 4096\n"
    "s_mov_b32 s74, 442496\n"
    "buffer_load_dwordx4 v5, s[40:43], s74 offen lds\n"
    "s_add_u32 m0, s59, 8192\n"
    "s_mov_b32 s74, 442624\n"
    "buffer_load_dwordx4 v5, s[40:43], s74 offen lds\n"
    "s_add_u32 m0, s59, 12288\n"
    "s_mov_b32 s74, 479232\n"
    "buffer_load_dwordx4 v5, s[40:43], s74 offen lds\n"
    "s_add_u32 m0, s59, 16384\n"
    "s_mov_b32 s74, 479360\n"
    "buffer_load_dwordx4 v5, s[40:43], s74 offen lds\n"
    "s_add_u32 m0, s59, 20480\n"
    "s_mov_b32 s74, 479488\n"
    "buffer_load_dwordx4 v5, s[40:43], s74 offen lds\n"
    "ds_read_b64_tr_b16 a[24:25], v6 offset:24576\n"
    "ds_read_b64_tr_b16 a[26:27], v6 offset:25088\n"
    "ds_read_b64_tr_b16 a[28:29], v6 offset:24584\n"
    "ds_read_b64_tr_b16 a[30:31], v6 offset:25096\n"
    "ds_read_b64_tr_b16 a[32:33], v7 offset:24576\n"
    "ds_read_b64_tr_b16 a[34:35], v7 offset:25088\n"
    "ds_read_b64_tr_b16 a[36:37], v6 offset:36864\n"
    "ds_read_b64_tr_b16 a[38:39], v6 offset:37376\n"
    "ds_read_b64_tr_b16 a[40:41], v6 offset:36872\n"
    "ds_read_b64_tr_b16 a[42:43], v6 offset:37384\n"
    "ds_read_b64_tr_b16 a[44:45], v7 offset:36864\n"
    "ds_read_b64_tr_b16 a[46:47], v7 offset:37376\n"
    "s_waitcnt vmcnt(24) lgkmcnt(0)\n"
    "s_barrier\n"
    "s_add_u32 m0, s59, 24576\n"
    "s_mov_b32 s74, 516096\n"
    "buffer_load_dwordx4 v5, s[40:43], s74 offen lds\n"
    "s_add_u32 m0, s59, 28672\n"
    "s_mov_b32 s74, 516224\n"
    "buffer_load_dwordx4 v5, s[40:43], s74 offen lds\n"
    "s_add_u32 m0, s59, 32768\n"
    "s_mov_b32 s74, 516352\n"
    "buffer_load_dwordx4 v5, s[40:43], s74 offen lds\n"
    "s_add_u32 m0, s59, 36864\n"
    "s_mov_b32 s74, 552960\n"
    "buffer_load_dwordx4 v5, s[40:43], s74 offen lds\n"
    "s_add_u32 m0, s59, 40960\n"
    "s_mov_b32 s74, 553088\n"
    "buffer_load_dwordx4 v5, s[40:43], s74 offen lds\n"
    "s_add_u32 m0, s59, 45056\n"
    "s_mov_b32 s74, 553216\n"
    "buffer_load_dwordx4 v5, s[40:43], s74 offen lds\n"
    "ds_read_b64_tr_b16 a[48:49], v16 offset:0\n"
    "ds_read_b64_tr_b16 a[50:51], v16 offset:512\n"
    "ds_read_b64_tr_b16 a[52:53], v16 offset:8\n"
    "ds_read_b64_tr_b16 a[54:55], v16 offset:520\n"
    "ds_read_b64_tr_b16 a[56:57], v18 offset:0\n"
    "ds_read_b64_tr_b16 a[58:59], v18 offset:512\n"
    "ds_read_b64_tr_b16 a[60:61], v16 offset:12288\n"
    "ds_read_b64_tr_b16 a[62:63], v16 offset:12800\n"
    "ds_read_b64_tr_b16 a[64:65], v16 offset:12296\n"
    "ds_read_b64_tr_b16 a[66:67], v16 offset:12808\n"
    "ds_read_b64_tr_b16 a[68:69], v18 offset:12288\n"
    "ds_read_b64_tr_b16 a[70:71], v18 offset:12800\n"
    "s_waitcnt vmcnt(24) lgkmcnt(0)\n"
    "s_barrier\n"
    "s_add_u32 m0, s59, 49152\n"
    "s_mov_b32 s74, 589824\n"
    "buffer_load_dwordx4 v5, s[40:43], s74 offen lds\n"
    "s_add_u32 m0, s59, 53248\n"
    "s_mov_b32 s74, 589952\n"
    "buffer_load_dwordx4 v5, s[40:43], s74 offen lds\n"
    "s_add_u32 m0, s59, 57344\n"
    "s_mov_b32 s74, 590080\n"
    "buffer_load_dwordx4 v5, s[40:43], s74 offen lds\n"
    "s_add_u32 m0, s59, 61440\n"
    "s_mov_b32 s74, 626688\n"
    "buffer_load_dwordx4 v5, s[40:43], s74 offen lds\n"
    "s_add_u32 m0, s59, 65536\n"
    "s_mov_b32 s74, 626816\n"
    "buffer_load_dwordx4 v5, s[40:43], s74 offen lds\n"
    "s_add_u32 m0, s59, 69632\n"
    "s_mov_b32 s74, 626944\n"
    "buffer_load_dwordx4 v5, s[40:43], s74 offen lds\n"
    "ds_read_b64_tr_b16 a[72:73], v16 offset:24576\n"
    "ds_read_b64_tr_b16 a[74:75], v16 offset:25088\n"
    "ds_read_b64_tr_b16 a[76:77], v16 offset:24584\n"
    "ds_read_b64_tr_b16 a[78:79], v16 offset:25096\n"
    "ds_read_b64_tr_b16 a[80:81], v18 offset:24576\n"
    "ds_read_b64_tr_b16 a[82:83], v18 offset:25088\n"
    "ds_read_b64_tr_b16 a[84:85], v16 offset:36864\n"
    "ds_read_b64_tr_b16 a[86:87], v16 offset:37376\n"
    "ds_read_b64_tr_b16 a[88:89], v16 offset:36872\n"
    "ds_read_b64_tr_b16 a[90:91], v16 offset:37384\n"
    "ds_read_b64_tr_b16 a[92:93], v18 offset:36864\n"
    "ds_read_b64_tr_b16 a[94:95], v18 offset:37376\n"
    "s_waitcnt vmcnt(24) lgkmcnt(0)\n"
    "s_barrier\n"
    "s_add_u32 m0, s59, 73728\n"
    "s_mov_b32 s74, 663552\n"
    "buffer_load_dwordx4 v5, s[40:43], s74 offen lds\n"
    "s_add_u32 m0, s59, 77824\n"
    "s_mov_b32 s74, 663680\n"
    "buffer_load_dwordx4 v5, s[40:43], s74 offen lds\n"
    "s_add_u32 m0, s59, 81920\n"
    "s_mov_b32 s74, 663808\n"
    "buffer_load_dwordx4 v5, s[40:43], s74 offen lds\n"
    "s_add_u32 m0, s59, 86016\n"
    "s_mov_b32 s74, 700416\n"
    "buffer_load_dwordx4 v5, s[40:43], s74 offen lds\n"
    "s_add_u32 m0, s59, 90112\n"
    "s_mov_b32 s74, 700544\n"
    "buffer_load_dwordx4 v5, s[40:43], s74 offen lds\n"
    "s_add_u32 m0, s59, 94208\n"
    "s_mov_b32 s74, 700672\n"
    "buffer_load_dwordx4 v5, s[40:43], s74 offen lds\n"
    "ds_read_b64_tr_b16 a[96:97], v17 offset:0\n"
    "ds_read_b64_tr_b16 a[98:99], v17 offset:512\n"
    "ds_read_b64_tr_b16 a[100:101], v17 offset:8\n"
    "ds_read_b64_tr_b16 a[102:103], v17 offset:520\n"
    "ds_read_b64_tr_b16 a[104:105], v19 offset:0\n"
    "ds_read_b64_tr_b16 a[106:107], v19 offset:512\n"
    "ds_read_b64_tr_b16 a[108:109], v17 offset:12288\n"
    "ds_read_b64_tr_b16 a[110:111], v17 offset:12800\n"
    "ds_read_b64_tr_b16 a[112:113], v17 offset:12296\n"
    "ds_read_b64_tr_b16 a[114:115], v17 offset:12808\n"
    "ds_read_b64_tr_b16 a[116:117], v19 offset:12288\n"
    "ds_read_b64_tr_b16 a[118:119], v19 offset:12800\n"
    "s_waitcnt vmcnt(24) lgkmcnt(0)\n"
    "s_barrier\n"
    "s_add_u32 m0, s59, 98304\n"
    "s_mov_b32 s74, 737280\n"
    "buffer_load_dwordx4 v5, s[40:43], s74 offen lds\n"
    "s_add_u32 m0, s59, 102400\n"
    "s_mov_b32 s74, 737408\n"
    "buffer_load_dwordx4 v5, s[40:43], s74 offen lds\n"
    "s_add_u32 m0, s59, 106496\n"
    "s_mov_b32 s74, 737536\n"
    "buffer_load_dwordx4 v5, s[40:43], s74 offen lds\n"
    "s_add_u32 m0, s59, 110592\n"
    "s_mov_b32 s74, 774144\n"
    "buffer_load_dwordx4 v5, s[40:43], s74 offen lds\n"
    "s_add_u32 m0, s59, 114688\n"
    "s_mov_b32 s74, 774272\n"
    "buffer_load_dwordx4 v5, s[40:43], s74 offen lds\n"
    "s_add_u32 m0, s59, 118784\n"
    "s_mov_b32 s74, 774400\n"
    "buffer_load_dwordx4 v5, s[40:43], s74 offen lds\n"
    "ds_read_b64_tr_b16 a[120:121], v17 offset:24576\n"
    "ds_read_b64_tr_b16 a[122:123], v17 offset:25088\n"
    "ds_read_b64_tr_b16 a[124:125], v17 offset:24584\n"
    "ds_read_b64_tr_b16 a[126:127], v17 offset:25096\n"
    "ds_read_b64_tr_b16 a[128:129], v19 offset:24576\n"
    "ds_read_b64_tr_b16 a[130:131], v19 offset:25088\n"
    "ds_read_b64_tr_b16 a[132:133], v17 offset:36864\n"
    "ds_read_b64_tr_b16 a[134:135], v17 offset:37376\n"
    "ds_read_b64_tr_b16 a[136:137], v17 offset:36872\n"
    "ds_read_b64_tr_b16 a[138:139], v17 offset:37384\n"
    "ds_read_b64_tr_b16 a[140:141], v19 offset:36864\n"
    "ds_read_b64_tr_b16 a[142:143], v19 offset:37376\n"
    "s_waitcnt vmcnt(24) lgkmcnt(0)\n"
    "s_barrier\n"
    "s_add_u32 m0, s59, 122880\n"
    "s_mov_b32 s74, 811008\n"
    "buffer_load_dwordx4 v5, s[40:43], s74 offen lds\n"
    "s_add_u32 m0, s59, 126976\n"
    "s_mov_b32 s74, 811136\n"
    "buffer_load_dwordx4 v5, s[40:43], s74 offen lds\n"
    "s_add_u32 m0, s59, 131072\n"
    "s_mov_b32 s74, 811264\n"
    "buffer_load_dwordx4 v5, s[40:43], s74 offen lds\n"
    "s_add_u32 m0, s59, 135168\n"
    "s_mov_b32 s74, 847872\n"
    "buffer_load_dwordx4 v5, s[40:43], s74 offen lds\n"
    "s_add_u32 m0, s59, 139264\n"
    "s_mov_b32 s74, 848000\n"
    "buffer_load_dwordx4 v5, s[40:43], s74 offen lds\n"
    "s_add_u32 m0, s59, 143360\n"
    "s_mov_b32 s74, 848128\n"
    "buffer_load_dwordx4 v5, s[40:43], s74 offen lds\n"
    "ds_read_b64_tr_b16 a[144:145], v6 offset:0\n"
    "ds_read_b64_tr_b16 a[146:147], v6 offset:512\n"
    "ds_read_b64_tr_b16 a[148:149], v6 offset:8\n"
    "ds_read_b64_tr_b16 a[150:151], v6 offset:520\n"
    "ds_read_b64_tr_b16 a[152:153], v7 offset:0\n"
    "ds_read_b64_tr_b16 a[154:155], v7 offset:512\n"
    "ds_read_b64_tr_b16 a[156:157], v6 offset:12288\n"
    "ds_read_b64_tr_b16 a[158:159], v6 offset:12800\n"
    "ds_read_b64_tr_b16 a[160:161], v6 offset:12296\n"
    "ds_read_b64_tr_b16 a[162:163], v6 offset:12808\n"
    "ds_read_b64_tr_b16 a[164:165], v7 offset:12288\n"
    "ds_read_b64_tr_b16 a[166:167], v7 offset:12800\n"
    "s_waitcnt vmcnt(24) lgkmcnt(0)\n"
    "s_barrier\n"
    "s_add_u32 m0, s59, 0\n"
    "s_mov_b32 s74, 884736\n"
    "buffer_load_dwordx4 v5, s[40:43], s74 offen lds\n"
    "s_add_u32 m0, s59, 4096\n"
    "s_mov_b32 s74, 884864\n"
    "buffer_load_dwordx4 v5, s[40:43], s74 offen lds\n"
    "s_add_u32 m0, s59, 8192\n"
    "s_mov_b32 s74, 884992\n"
    "buffer_load_dwordx4 v5, s[40:43], s74 offen lds\n"
    "s_add_u32 m0, s59, 12288\n"
    "s_mov_b32 s74, 921600\n"
    "buffer_load_dwordx4 v5, s[40:43], s74 offen lds\n"
    "s_add_u32 m0, s59, 16384\n"
    "s_mov_b32 s74, 921728\n"
    "buffer_load_dwordx4 v5, s[40:43], s74 offen lds\n"
    "s_add_u32 m0, s59, 20480\n"
    "s_mov_b32 s74, 921856\n"
    "buffer_load_dwordx4 v5, s[40:43], s74 offen lds\n"
    "ds_read_b64_tr_b16 a[168:169], v6 offset:24576\n"
    "ds_read_b64_tr_b16 a[170:171], v6 offset:25088\n"
    "ds_read_b64_tr_b16 a[172:173], v6 offset:24584\n"
    "ds_read_b64_tr_b16 a[174:175], v6 offset:25096\n"
    "ds_read_b64_tr_b16 a[176:177], v7 offset:24576\n"
    "ds_read_b64_tr_b16 a[178:179], v7 offset:25088\n"
    "ds_read_b64_tr_b16 a[180:181], v6 offset:36864\n"
    "ds_read_b64_tr_b16 a[182:183], v6 offset:37376\n"
    "ds_read_b64_tr_b16 a[184:185], v6 offset:36872\n"
    "ds_read_b64_tr_b16 a[186:187], v6 offset:37384\n"
    "ds_read_b64_tr_b16 a[188:189], v7 offset:36864\n"
    "ds_read_b64_tr_b16 a[190:191], v7 offset:37376\n"
    "s_waitcnt vmcnt(24) lgkmcnt(0)\n"
    "s_barrier\n"
    "s_add_u32 m0, s59, 24576\n"
    "s_mov_b32 s74, 958464\n"
    "buffer_load_dwordx4 v5, s[40:43], s74 offen lds\n"
    "s_add_u32 m0, s59, 28672\n"
    "s_mov_b32 s74, 958592\n"
    "buffer_load_dwordx4 v5, s[40:43], s74 offen lds\n"
    "s_add_u32 m0, s59, 32768\n"
    "s_mov_b32 s74, 958720\n"
    "buffer_load_dwordx4 v5, s[40:43], s74 offen lds\n"
    "s_add_u32 m0, s59, 36864\n"
    "s_mov_b32 s74, 995328\n"
    "buffer_load_dwordx4 v5, s[40:43], s74 offen lds\n"
    "s_add_u32 m0, s59, 40960\n"
    "s_mov_b32 s74, 995456\n"
    "buffer_load_dwordx4 v5, s[40:43], s74 offen lds\n"
    "s_add_u32 m0, s59, 45056\n"
    "s_mov_b32 s74, 995584\n"
    "buffer_load_dwordx4 v5, s[40:43], s74 offen lds\n"
    "ds_read_b64_tr_b16 a[192:193], v16 offset:0\n"
    "ds_read_b64_tr_b16 a[194:195], v16 offset:512\n"
    "ds_read_b64_tr_b16 a[196:197], v16 offset:8\n"
    "ds_read_b64_tr_b16 a[198:199], v16 offset:520\n"
    "ds_read_b64_tr_b16 a[200:201], v18 offset:0\n"
    "ds_read_b64_tr_b16 a[202:203], v18 offset:512\n"
    "ds_read_b64_tr_b16 a[204:205], v16 offset:12288\n"
    "ds_read_b64_tr_b16 a[206:207], v16 offset:12800\n"
    "ds_read_b64_tr_b16 a[208:209], v16 offset:12296\n"
    "ds_read_b64_tr_b16 a[210:211], v16 offset:12808\n"
    "ds_read_b64_tr_b16 a[212:213], v18 offset:12288\n"
    "ds_read_b64_tr_b16 a[214:215], v18 offset:12800\n"
    "s_waitcnt vmcnt(24) lgkmcnt(0)\n"
    "s_barrier\n"
    "s_add_u32 m0, s59, 49152\n"
    "s_mov_b32 s74, 1032192\n"
    "buffer_load_dwordx4 v5, s[40:43], s74 offen lds\n"
    "s_add_u32 m0, s59, 53248\n"
    "s_mov_b32 s74, 1032320\n"
    "buffer_load_dwordx4 v5, s[40:43], s74 offen lds\n"
    "s_add_u32 m0, s59, 57344\n"
    "s_mov_b32 s74, 1032448\n"
    "buffer_load_dwordx4 v5, s[40:43], s74 offen lds\n"
    "s_add_u32 m0, s59, 61440\n"
    "s_mov_b32 s74, 1069056\n"
    "buffer_load_dwordx4 v5, s[40:43], s74 offen lds\n"
    "s_add_u32 m0, s59, 65536\n"
    "s_mov_b32 s74, 1069184\n"
    "buffer_load_dwordx4 v5, s[40:43], s74 offen lds\n"
    "s_add_u32 m0, s59, 69632\n"
    "s_mov_b32 s74, 1069312\n"
    "buffer_load_dwordx4 v5, s[40:43], s74 offen lds\n"
    "ds_read_b64_tr_b16 a[216:217], v16 offset:24576\n"
    "ds_read_b64_tr_b16 a[218:219], v16 offset:25088\n"
    "ds_read_b64_tr_b16 a[220:221], v16 offset:24584\n"
    "ds_read_b64_tr_b16 a[222:223], v16 offset:25096\n"
    "ds_read_b64_tr_b16 a[224:225], v18 offset:24576\n"
    "ds_read_b64_tr_b16 a[226:227], v18 offset:25088\n"
    "ds_read_b64_tr_b16 a[228:229], v16 offset:36864\n"
    "ds_read_b64_tr_b16 a[230:231], v16 offset:37376\n"
    "ds_read_b64_tr_b16 a[232:233], v16 offset:36872\n"
    "ds_read_b64_tr_b16 a[234:235], v16 offset:37384\n"
    "ds_read_b64_tr_b16 a[236:237], v18 offset:36864\n"
    "ds_read_b64_tr_b16 a[238:239], v18 offset:37376\n"
    "s_waitcnt vmcnt(24) lgkmcnt(0)\n"
    "s_barrier\n"
    "s_add_u32 m0, s59, 73728\n"
    "s_mov_b32 s74, 1105920\n"
    "buffer_load_dwordx4 v5, s[40:43], s74 offen lds\n"
    "s_add_u32 m0, s59, 77824\n"
    "s_mov_b32 s74, 1106048\n"
    "buffer_load_dwordx4 v5, s[40:43], s74 offen lds\n"
    "s_add_u32 m0, s59, 81920\n"
    "s_mov_b32 s74, 1106176\n"
    "buffer_load_dwordx4 v5, s[40:43], s74 offen lds\n"
    "s_add_u32 m0, s59, 86016\n"
    "s_mov_b32 s74, 1142784\n"
    "buffer_load_dwordx4 v5, s[40:43], s74 offen lds\n"
    "s_add_u32 m0, s59, 90112\n"
    "s_mov_b32 s74, 1142912\n"
    "buffer_load_dwordx4 v5, s[40:43], s74 offen lds\n"
    "s_add_u32 m0, s59, 94208\n"
    "s_mov_b32 s74, 1143040\n"
    "buffer_load_dwordx4 v5, s[40:43], s74 offen lds\n"
    "buffer_load_dwordx2 v[12:13], off, s[36:39], 0 sc1\n"
    "ds_read_b64_tr_b16 a[240:241], v17 offset:0\n"
    "ds_read_b64_tr_b16 a[242:243], v17 offset:512\n"
    "ds_read_b64_tr_b16 a[244:245], v17 offset:8\n"
    "ds_read_b64_tr_b16 a[246:247], v17 offset:520\n"
    "ds_read_b64_tr_b16 a[248:249], v19 offset:0\n"
    "ds_read_b64_tr_b16 a[250:251], v19 offset:512\n"
    "ds_read_b64_tr_b16 a[252:253], v17 offset:12288\n"
    "ds_read_b64_tr_b16 a[254:255], v17 offset:12800\n"
    "ds_read_b64_tr_b16 v[128:129], v17 offset:12296\n"
    "ds_read_b64_tr_b16 v[130:131], v17 offset:12808\n"
    "ds_read_b64_tr_b16 v[132:133], v19 offset:12288\n"
    "ds_read_b64_tr_b16 v[134:135], v19 offset:12800\n"
    "s_waitcnt vmcnt(25) lgkmcnt(0)\n"
    "s_barrier\n"
    "ds_read_b64_tr_b16 v[136:137], v17 offset:24576\n"
    "ds_read_b64_tr_b16 v[138:139], v17 offset:25088\n"
    "ds_read_b64_tr_b16 v[140:141], v17 offset:24584\n"
    "ds_read_b64_tr_b16 v[142:143], v17 offset:25096\n"
    "ds_read_b64_tr_b16 v[144:145], v19 offset:24576\n"
    "ds_read_b64_tr_b16 v[146:147], v19 offset:25088\n"
    "ds_read_b64_tr_b16 v[148:149], v17 offset:36864\n"
    "ds_read_b64_tr_b16 v[150:151], v17 offset:37376\n"
    "ds_read_b64_tr_b16 v[152:153], v17 offset:36872\n"
    "ds_read_b64_tr_b16 v[154:155], v17 offset:37384\n"
    "ds_read_b64_tr_b16 v[156:157], v19 offset:36864\n"
    "ds_read_b64_tr_b16 v[158:159], v19 offset:37376\n"
    "s_waitcnt vmcnt(19) lgkmcnt(0)\n"
    "s_barrier\n"
    "ds_read_b64_tr_b16 v[160:161], v6 offset:0\n"
    "ds_read_b64_tr_b16 v[162:163], v6 offset:512\n"
    "ds_read_b64_tr_b16 v[164:165], v6 offset:8\n"
    "ds_read_b64_tr_b16 v[166:167], v6 offset:520\n"
    "ds_read_b64_tr_b16 v[168:169], v7 offset:0\n"
    "ds_read_b64_tr_b16 v[170:171], v7 offset:512\n"
    "ds_read_b64_tr_b16 v[172:173], v6 offset:12288\n"
    "ds_read_b64_tr_b16 v[174:175], v6 offset:12800\n"
    "ds_read_b64_tr_b16 v[176:177], v6 offset:12296\n"
    "ds_read_b64_tr_b16 v[178:179], v6 offset:12808\n"
    "ds_read_b64_tr_b16 v[180:181], v7 offset:12288\n"
    "ds_read_b64_tr_b16 v[182:183], v7 offset:12800\n"
    "s_waitcnt vmcnt(13) lgkmcnt(0)\n"
    "s_barrier\n"
    "ds_read_b64_tr_b16 v[184:185], v6 offset:24576\n"
    "ds_read_b64_tr_b16 v[186:187], v6 offset:25088\n"
    "ds_read_b64_tr_b16 v[188:189], v6 offset:24584\n"
    "ds_read_b64_tr_b16 v[190:191], v6 offset:25096\n"
    "ds_read_b64_tr_b16 v[192:193], v7 offset:24576\n"
    "ds_read_b64_tr_b16 v[194:195], v7 offset:25088\n"
    "ds_read_b64_tr_b16 v[196:197], v6 offset:36864\n"
    "ds_read_b64_tr_b16 v[198:199], v6 offset:37376\n"
    "ds_read_b64_tr_b16 v[200:201], v6 offset:36872\n"
    "ds_read_b64_tr_b16 v[202:203], v6 offset:37384\n"
    "ds_read_b64_tr_b16 v[204:205], v7 offset:36864\n"
    "ds_read_b64_tr_b16 v[206:207], v7 offset:37376\n"
    "s_waitcnt vmcnt(7) lgkmcnt(0)\n"
    "s_barrier\n"
    "ds_read_b64_tr_b16 v[208:209], v16 offset:0\n"
    "ds_read_b64_tr_b16 v[210:211], v16 offset:512\n"
    "ds_read_b64_tr_b16 v[212:213], v16 offset:8\n"
    "ds_read_b64_tr_b16 v[214:215], v16 offset:520\n"
    "ds_read_b64_tr_b16 v[216:217], v18 offset:0\n"
    "ds_read_b64_tr_b16 v[218:219], v18 offset:512\n"
    "ds_read_b64_tr_b16 v[220:221], v16 offset:12288\n"
    "ds_read_b64_tr_b16 v[222:223], v16 offset:12800\n"
    "ds_read_b64_tr_b16 v[224:225], v16 offset:12296\n"
    "ds_read_b64_tr_b16 v[226:227], v16 offset:12808\n"
    "ds_read_b64_tr_b16 v[228:229], v18 offset:12288\n"
    "ds_read_b64_tr_b16 v[230:231], v18 offset:12800\n"
    "s_waitcnt vmcnt(0) lgkmcnt(0)\n"
    "s_barrier\n"
    ".LgWO_par_%=:\n"
    "v_readfirstlane_b32 s74, v12\n"
    "v_readfirstlane_b32 s75, v13\n"
    "s_xor_b32 s74, s74, s66\n"
    "s_xor_b32 s75, s75, s67\n"
    "s_cselect_b32 s75, 2, 0\n"
    "s_or_b32 s75, s75, s74\n"
    "s_cmp_lt_u32 s75, 2\n"
    "s_cbranch_scc1 .LgWO_par1_%=\n"
    "s_sleep 4\n"
    "buffer_load_dwordx2 v[12:13], off, s[36:39], 0 sc1\n"
    "s_waitcnt vmcnt(0)\n"
    "s_sub_u32 s72, s72, 1\n"
    "s_cbranch_scc0 .LgWO_par_%=\n"
    "s_mov_b32 s72, 0\n"
    ".LgWO_par1_%=:\n"
    "s_and_b32 s74, s74, 1\n"
    "s_lshl_b32 s74, s74, 13\n"
    "s_add_u32 s36, s36, s74\n"
    "s_addc_u32 s37, s37, 0\n"
    "v_add_u32 v13, v11, v3\n"
    "v_cmp_gt_i32 vcc, s71, v13\n"
    "s_nop 0\n"
    "s_or_b64 exec, vcc, s[62:63]\n"
    "buffer_atomic_smax v11, v2, s[36:39], 0 offen sc0 sc1\n"
    "s_mov_b64 exec, -1\n"
    "s_getpc_b64 s[84:85]\n"
    ".LgWO_PC2_%=:\n"
    "s_and_b32 s85, s85, 0xffff\n"
    "s_mov_b32 s86, .LgWO_FIN_%=-.LgWO_PC2_%=\n"
    "s_mov_b32 s87, 0x20000\n"
    "v_mbcnt_lo_u32_b32 v42, -1, 0\n"
    "v_mbcnt_hi_u32_b32 v42, -1, v42\n"
    "v_lshlrev_b32 v42, 7, v42\n"
    "buffer_load_dword v43, v42, s[84:87], 0 offen\n"
    "v_add_u32 v42, 8192, v42\n"
    "buffer_load_dword v43, v42, s[84:87], 0 offen\n"
    "v_add_u32 v42, 8192, v42\n"
    "buffer_load_dword v43, v42, s[84:87], 0 offen\n"
    "ds_read_b64_tr_b16 v[232:233], v16 offset:24576\n"
    "ds_read_b64_tr_b16 v[234:235], v16 offset:25088\n"
    "ds_read_b64_tr_b16 v[236:237], v16 offset:24584\n"
    "ds_read_b64_tr_b16 v[238:239], v16 offset:25096\n"
    "ds_read_b64_tr_b16 v[240:241], v18 offset:24576\n"
    "ds_read_b64_tr_b16 v[242:243], v18 offset:25088\n"
    "ds_read_b64_tr_b16 v[244:245], v16 offset:36864\n"
    "ds_read_b64_tr_b16 v[246:247], v16 offset:37376\n"
    "ds_read_b64_tr_b16 v[248:249], v16 offset:36872\n"
    "ds_read_b64_tr_b16 v[250:251], v16 offset:37384\n"
    "ds_read_b64_tr_b16 v[252:253], v18 offset:36864\n"
    "ds_read_b64_tr_b16 v[254:255], v18 offset:37376\n"
    "s_waitcnt lgkmcnt(0)\n"
    "s_barrier\n"
    "s_waitcnt vmcnt(0)\n"
    "v_mov_b32 v5, v11\n"
    "v_mov_b32 v12, -1\n"
    "s_mov_b32 s73, 0x80000000\n"
    "s_branch .LgWO_block1_1_%=\n"
    ".LgWO_LOOPB_%=:\n"
    ".LgWO_head0_%=:\n"
    "v_mfma_f32_16x16x32_bf16 v[48:51], a[0:3], v[16:19], 0\n"
    "v_mfma_f32_16x16x32_bf16 v[52:55], a[4:7], v[16:19], 0\n"
    "s_waitcnt lgkmcnt(2)\n"
    "v_cvt_pk_bf16_f32 v56, v56, v57\n"
    "v_cvt_pk_bf16_f32 v57, v58, v59\n"
    "v_mfma_f32_16x16x32_bf16 v[64:67], a[8:11], v[16:19], 0\n"
    "ds_read_b128 v[16:19], v0 offset:4096\n"
    "v_cvt_pk_bf16_f32 v58, v60, v61\n"
    "v_mfma_f32_16x16x32_bf16 v[48:51], a[12:15], v[20:23], v[48:51]\n"
    "v_cvt_pk_bf16_f32 v59, v62, v63\n"
    "v_mfma_f32_16x16x32_bf16 v[52:55], a[16:19], v[20:23], v[52:55]\n"
    "s_waitcnt lgkmcnt(2)\n"
    "v_cvt_pk_bf16_f32 v68, v68, v69\n"
    "v_cvt_pk_bf16_f32 v69, v70, v71\n"
    "v_mfma_f32_16x16x32_bf16 v[64:67], a[20:23], v[20:23], v[64:67]\n"
    "ds_read_b128 v[20:23], v0 offset:5120\n"
    "s_nop 0\n"
    "v_mfma_f32_16x16x32_bf16 v[48:51], a[24:27], v[24:27], v[48:51]\n"
    "buffer_store_dwordx4 v[56:59], v15, s[36:39], s73 offen offset:1024 sc1\n"
    "v_mfma_f32_16x16x32_bf16 v[52:55], a[28:31], v[24:27], v[52:55]\n"
    "s_waitcnt lgkmcnt(2)\n"
    "s_waitcnt vmcnt(2)\n"
    "v_add_u32 v13, v11, v3\n"
    "v_mfma_f32_16x16x32_bf16 v[64:67], a[32:35], v[24:27], v[64:67]\n"
    "ds_read_b128 v[24:27], v0 offset:6144\n"
    "v_cmp_gt_i32 vcc, s71, v13\n"
    "v_mfma_f32_16x16x32_bf16 v[48:51], a[36:39], v[28:31], v[48:51]\n"
    "buffer_atomic_smax v7, v10, s[36:39], s69 offen sc1\n"
    "v_permlane16_swap_b32 v68, v70\n"
    "v_mfma_f32_16x16x32_bf16 v[52:55], a[40:43], v[28:31], v[52:55]\n"
    "s_waitcnt lgkmcnt(2)\n"
    "v_permlane16_swap_b32 v69, v71\n"
    "v_mfma_f32_16x16x32_bf16 v[64:67], a[44:47], v[28:31], v[64:67]\n"
    "ds_read_b128 v[28:31], v0 offset:7168\n"
    "s_cbranch_vccnz .LgWO_poll0_%=\n"
    "v_mfma_f32_16x16x32_bf16 v[48:51], a[48:51], v[16:19], v[48:51]\n"
    "s_add_u32 m0, s59, 32768\n"
    "v_mfma_f32_16x16x32_bf16 v[52:55], a[52:55], v[16:19], v[52:55]\n"
    "s_waitcnt lgkmcnt(2)\n"
    "buffer_load_dwordx4 v1, s[36:39], s76 offen sc1 lds\n"
    "v_mfma_f32_16x16x32_bf16 v[64:67], a[56:59], v[16:19], v[64:67]\n"
    "ds_read_b128 v[16:19], v0 offset:8192\n"
    "v_mfma_f32_16x16x32_bf16 v[48:51], a[60:63], v[20:23], v[48:51]\n"
    "buffer_store_dwordx4 v[68:71], v4, s[36:39], s73 offen offset:1024 sc1\n"
    "v_mfma_f32_16x16x32_bf16 v[52:55], a[64:67], v[20:23], v[52:55]\n"
    "s_waitcnt lgkmcnt(2)\n"
    "s_add_u32 s76, s76, 16384\n"
    "s_lshr_b32 s73, s68, 1\n"
    "v_mfma_f32_16x16x32_bf16 v[64:67], a[68:71], v[20:23], v[64:67]\n"
    "ds_read_b128 v[20:23], v0 offset:9216\n"
    "s_and_b32 s73, s73, s57\n"
    "v_mfma_f32_16x16x32_bf16 v[48:51], a[72:75], v[24:27], v[48:51]\n"
    "s_add_u32 m0, s59, 36864\n"
    "s_mul_i32 s73, s73, s56\n"
    "v_mfma_f32_16x16x32_bf16 v[52:55], a[76:79], v[24:27], v[52:55]\n"
    "s_waitcnt lgkmcnt(2)\n"
    "buffer_load_dwordx4 v1, s[36:39], s76 offen sc1 lds\n"
    "s_add_u32 s73, s73, s55\n"
    "v_mfma_f32_16x16x32_bf16 v[64:67], a[80:83], v[24:27], v[64:67]\n"
    "ds_read_b128 v[24:27], v0 offset:10240\n"
    "v_mfma_f32_16x16x32_bf16 v[48:51], a[84:87], v[28:31], v[48:51]\n"
    "s_add_u32 s76, s76, 16384\n"
    "s_add_u32 m0, s59, 40960\n"
    "v_mfma_f32_16x16x32_bf16 v[52:55], a[88:91], v[28:31], v[52:55]\n"
    "s_waitcnt lgkmcnt(2)\n"
    "buffer_load_dwordx4 v1, s[36:39], s76 offen sc1 lds\n"
    "v_mfma_f32_16x16x32_bf16 v[64:67], a[92:95], v[28:31], v[64:67]\n"
    "ds_read_b128 v[28:31], v0 offset:11264\n"
    "v_mfma_f32_16x16x32_bf16 v[48:51], a[96:99], v[16:19], v[48:51]\n"
    "v_mfma_f32_16x16x32_bf16 v[52:55], a[100:103], v[16:19], v[52:55]\n"
    "s_waitcnt lgkmcnt(2)\n"
    "v_mfma_f32_16x16x32_bf16 v[64:67], a[104:107], v[16:19], v[64:67]\n"
    "ds_read_b128 v[16:19], v0 offset:12288\n"
    "s_add_u32 s76, s76, 16384\n"
    "v_mfma_f32_16x16x32_bf16 v[48:51], a[108:111], v[20:23], v[48:51]\n"
    "s_add_u32 m0, s59, 45056\n"
    "v_mfma_f32_16x16x32_bf16 v[52:55], a[112:115], v[20:23], v[52:55]\n"
    "s_waitcnt lgkmcnt(2)\n"
    "buffer_load_dwordx4 v1, s[36:39], s76 offen sc1 lds\n"
    "v_mfma_f32_16x16x32_bf16 v[64:67], a[116:119], v[20:23], v[64:67]\n"
    "ds_read_b128 v[20:23], v0 offset:13312\n"
    "v_mfma_f32_16x16x32_bf16 v[48:51], a[120:123], v[24:27], v[48:51]\n"
    "v_mfma_f32_16x16x32_bf16 v[52:55], a[124:127], v[24:27], v[52:55]\n"
    "s_waitcnt lgkmcnt(2)\n"
    "v_mfma_f32_16x16x32_bf16 v[64:67], a[128:131], v[24:27], v[64:67]\n"
    "ds_read_b128 v[24:27], v0 offset:14336\n"
    "s_add_u32 s76, s76, 16384\n"
    "v_mfma_f32_16x16x32_bf16 v[48:51], a[132:135], v[28:31], v[48:51]\n"
    "s_add_u32 m0, s59, 49152\n"
    "v_mfma_f32_16x16x32_bf16 v[52:55], a[136:139], v[28:31], v[52:55]\n"
    "s_waitcnt lgkmcnt(2)\n"
    "buffer_load_dwordx4 v1, s[36:39], s76 offen sc1 lds\n"
    "v_mfma_f32_16x16x32_bf16 v[64:67], a[140:143], v[28:31], v[64:67]\n"
    "ds_read_b128 v[28:31], v0 offset:15360\n"
    "v_mfma_f32_16x16x32_bf16 v[48:51], a[144:147], v[16:19], v[48:51]\n"
    "v_mfma_f32_16x16x32_bf16 v[52:55], a[148:151], v[16:19], v[52:55]\n"
    "s_waitcnt lgkmcnt(2)\n"
    "v_mfma_f32_16x16x32_bf16 v[64:67], a[152:155], v[16:19], v[64:67]\n"
    "ds_read_b128 v[16:19], v0 offset:16384\n"
    "s_add_u32 s76, s76, 16384\n"
    "v_mfma_f32_16x16x32_bf16 v[48:51], a[156:159], v[20:23], v[48:51]\n"
    "s_add_u32 m0, s59, 53248\n"
    "v_mfma_f32_16x16x32_bf16 v[52:55], a[160:163], v[20:23], v[52:55]\n"
    "s_waitcnt lgkmcnt(2)\n"
    "buffer_load_dwordx4 v1, s[36:39], s76 offen sc1 lds\n"
    "v_mfma_f32_16x16x32_bf16 v[64:67], a[164:167], v[20:23], v[64:67]\n"
    "ds_read_b128 v[20:23], v0 offset:17408\n"
    "v_mfma_f32_16x16x32_bf16 v[48:51], a[168:171], v[24:27], v[48:51]\n"
    "v_mfma_f32_16x16x32_bf16 v[52:55], a[172:175], v[24:27], v[52:55]\n"
    "s_waitcnt lgkmcnt(2)\n"
    "s_add_u32 s76, s76, 16384\n"
    "s_add_u32 m0, s59, 57344\n"
    "v_mfma_f32_16x16x32_bf16 v[64:67], a[176:179], v[24:27], v[64:67]\n"
    "ds_read_b128 v[24:27], v0 offset:18432\n"
    "buffer_load_dwordx4 v1, s[36:39], s76 offen sc1 lds\n"
    "v_mfma_f32_16x16x32_bf16 v[48:51], a[180:183], v[28:31], v[48:51]\n"
    "v_mfma_f32_16x16x32_bf16 v[52:55], a[184:187], v[28:31], v[52:55]\n"
    "s_waitcnt lgkmcnt(2)\n"
    "v_mfma_f32_16x16x32_bf16 v[64:67], a[188:191], v[28:31], v[64:67]\n"
    "ds_read_b128 v[28:31], v0 offset:19456\n"
    "v_mfma_f32_16x16x32_bf16 v[48:51], a[192:195], v[16:19], v[48:51]\n"
    "v_mfma_f32_16x16x32_bf16 v[52:55], a[196:199], v[16:19], v[52:55]\n"
    "s_waitcnt lgkmcnt(2)\n"
    "s_add_u32 s76, s76, 16384\n"
    "s_add_u32 m0, s59, 61440\n"
    "v_mfma_f32_16x16x32_bf16 v[64:67], a[200:203], v[16:19], v[64:67]\n"
    "ds_read_b128 v[16:19], v0 offset:20480\n"
    "buffer_load_dwordx4 v1, s[36:39], s76 offen sc1 lds\n"
    "v_mfma_f32_16x16x32_bf16 v[48:51], a[204:207], v[20:23], v[48:51]\n"
    "v_mfma_f32_16x16x32_bf16 v[52:55], a[208:211], v[20:23], v[52:55]\n"
    "s_waitcnt lgkmcnt(2)\n"
    "v_mfma_f32_16x16x32_bf16 v[64:67], a[212:215], v[20:23], v[64:67]\n"
    "ds_read_b128 v[20:23], v0 offset:21504\n"
    "v_mfma_f32_16x16x32_bf16 v[48:51], a[216:219], v[24:27], v[48:51]\n"
    "v_mfma_f32_16x16x32_bf16 v[52:55], a[220:223], v[24:27], v[52:55]\n"
    "s_waitcnt lgkmcnt(2)\n"
    "s_waitcnt vmcnt(7)\n"
    "buffer_atomic_smax v12, v10, s[36:39], s70 offen sc1\n"
    "v_mfma_f32_16x16x32_bf16 v[64:67], a[224:227], v[24:27], v[64:67]\n"
    "ds_read_b128 v[24:27], v0 offset:22528\n"
    "v_mfma_f32_16x16x32_bf16 v[48:51], a[228:231], v[28:31], v[48:51]\n"
    "v_mfma_f32_16x16x32_bf16 v[52:55], a[232:235], v[28:31], v[52:55]\n"
    "s_waitcnt lgkmcnt(2)\n"
    "v_mfma_f32_16x16x32_bf16 v[64:67], a[236:239], v[28:31], v[64:67]\n"
    "ds_read_b128 v[28:31], v0 offset:23552\n"
    ".LgWO_join0_%=:\n"
    "v_mfma_f32_16x16x32_bf16 v[48:51], a[240:243], v[16:19], v[48:51]\n"
    "s_add_u32 s71, s68, 2\n"
    "s_waitcnt vmcnt(8)\n"
    "v_mfma_f32_16x16x32_bf16 v[52:55], a[244:247], v[16:19], v[52:55]\n"
    "s_waitcnt lgkmcnt(2)\n"
    "v_add_u32 v13, v11, v3\n"
    "v_mfma_f32_16x16x32_bf16 v[64:67], a[248:251], v[16:19], v[64:67]\n"
    "ds_read_b128 v[16:19], v0 offset:24576\n"
    "v_cmp_gt_i32 vcc, s71, v13\n"
    "v_mfma_f32_16x16x32_bf16 v[48:51], a[252:255], v[20:23], v[48:51]\n"
    "v_mfma_f32_16x16x32_bf16 v[52:55], v[128:131], v[20:23], v[52:55]\n"
    "s_waitcnt lgkmcnt(2)\n"
    "v_mfma_f32_16x16x32_bf16 v[64:67], v[132:135], v[20:23], v[64:67]\n"
    "ds_read_b128 v[20:23], v0 offset:25600\n"
    "v_mfma_f32_16x16x32_bf16 v[48:51], v[136:139], v[24:27], v[48:51]\n"
    "s_or_b64 exec, vcc, s[62:63]\n"
    "buffer_atomic_smax v11, v2, s[36:39], 0 offen sc0 sc1\n"
    "s_mov_b64 exec, -1\n"
    "v_mfma_f32_16x16x32_bf16 v[52:55], v[140:143], v[24:27], v[52:55]\n"
    "s_waitcnt lgkmcnt(2)\n"
    "s_lshr_b32 s76, s71, 1\n"
    "v_mfma_f32_16x16x32_bf16 v[64:67], v[144:147], v[24:27], v[64:67]\n"
    "ds_read_b128 v[24:27], v0 offset:26624\n"
    "s_and_b32 s76, s76, s54\n"
    "v_mfma_f32_16x16x32_bf16 v[48:51], v[148:151], v[28:31], v[48:51]\n"
    "s_mul_i32 s76, s76, s53\n"
    "v_mfma_f32_16x16x32_bf16 v[52:55], v[152:155], v[28:31], v[52:55]\n"
    "s_waitcnt lgkmcnt(2)\n"
    "s_add_u32 s76, s76, s52\n"
    "v_mfma_f32_16x16x32_bf16 v[64:67], v[156:159], v[28:31], v[64:67]\n"
    "ds_read_b128 v[28:31], v0 offset:27648\n"
    "v_mfma_f32_16x16x32_bf16 v[48:51], v[160:163], v[16:19], v[48:51]\n"
    "v_mfma_f32_16x16x32_bf16 v[52:55], v[164:167], v[16:19], v[52:55]\n"
    "s_waitcnt lgkmcnt(2)\n"
    "v_mfma_f32_16x16x32_bf16 v[64:67], v[168:171], v[16:19], v[64:67]\n"
    "ds_read_b128 v[16:19], v0 offset:28672\n"
    "v_mfma_f32_16x16x32_bf16 v[48:51], v[172:175], v[20:23], v[48:51]\n"
    "v_mfma_f32_16x16x32_bf16 v[52:55], v[176:179], v[20:23], v[52:55]\n"
    "s_waitcnt lgkmcnt(2)\n"
    "v_mfma_f32_16x16x32_bf16 v[64:67], v[180:183], v[20:23], v[64:67]\n"
    "ds_read_b128 v[20:23], v0 offset:29696\n"
    "v_mfma_f32_16x16x32_bf16 v[48:51], v[184:187], v[24:27], v[48:51]\n"
    "v_mfma_f32_16x16x32_bf16 v[52:55], v[188:191], v[24:27], v[52:55]\n"
    "s_waitcnt lgkmcnt(2)\n"
    "v_mfma_f32_16x16x32_bf16 v[64:67], v[192:195], v[24:27], v[64:67]\n"
    "ds_read_b128 v[24:27], v0 offset:30720\n"
    "v_mfma_f32_16x16x32_bf16 v[48:51], v[196:199], v[28:31], v[48:51]\n"
    "v_mfma_f32_16x16x32_bf16 v[52:55], v[200:203], v[28:31], v[52:55]\n"
    "s_waitcnt lgkmcnt(2)\n"
    "v_add_u32 v13, v5, v3\n"
    "v_mfma_f32_16x16x32_bf16 v[64:67], v[204:207], v[28:31], v[64:67]\n"
    "ds_read_b128 v[28:31], v0 offset:31744\n"
    "v_cmp_gt_i32 vcc, s71, v13\n"
    "v_mfma_f32_16x16x32_bf16 v[48:51], v[208:211], v[16:19], v[48:51]\n"
    "v_mfma_f32_16x16x32_bf16 v[52:55], v[212:215], v[16:19], v[52:55]\n"
    "s_waitcnt lgkmcnt(2)\n"
    "v_mfma_f32_16x16x32_bf16 v[64:67], v[216:219], v[16:19], v[64:67]\n"
    "v_mfma_f32_16x16x32_bf16 v[48:51], v[220:223], v[20:23], v[48:51]\n"
    "s_waitcnt vmcnt(1) lgkmcnt(0)\n"
    "s_barrier\n"
    "ds_read_b128 v[16:19], v9 offset:0\n"
    "s_or_b64 exec, vcc, s[62:63]\n"
    "buffer_atomic_smax v5, v2, s[36:39], 0 offen sc0 sc1\n"
    "s_mov_b64 exec, -1\n"
    "v_mfma_f32_16x16x32_bf16 v[52:55], v[224:227], v[20:23], v[52:55]\n"
    "v_mfma_f32_16x16x32_bf16 v[64:67], v[228:231], v[20:23], v[64:67]\n"
    "ds_read_b128 v[20:23], v9 offset:1024\n"
    "v_mfma_f32_16x16x32_bf16 v[48:51], v[232:235], v[24:27], v[48:51]\n"
    "v_mfma_f32_16x16x32_bf16 v[52:55], v[236:239], v[24:27], v[52:55]\n"
    "v_mfma_f32_16x16x32_bf16 v[64:67], v[240:243], v[24:27], v[64:67]\n"
    "ds_read_b128 v[24:27], v9 offset:2048\n"
    "v_mfma_f32_16x16x32_bf16 v[48:51], v[244:247], v[28:31], v[48:51]\n"
    "s_mov_b32 s69, 0x80000000\n"
    "s_mov_b32 s70, 0\n"
    "v_mov_b32 v12, s68\n"
    "v_mfma_f32_16x16x32_bf16 v[52:55], v[248:251], v[28:31], v[52:55]\n"
    "s_waitcnt lgkmcnt(2)\n"
    "s_add_u32 s68, s68, 1\n"
    "v_mfma_f32_16x16x32_bf16 v[64:67], v[252:255], v[28:31], v[64:67]\n"
    "ds_read_b128 v[28:31], v9 offset:3072\n"
    ".LgWO_head1_%=:\n"
    "v_mfma_f32_16x16x32_bf16 v[56:59], a[0:3], v[16:19], 0\n"
    "v_mfma_f32_16x16x32_bf16 v[60:63], a[4:7], v[16:19], 0\n"
    "s_waitcnt lgkmcnt(2)\n"
    "v_cvt_pk_bf16_f32 v48, v48, v49\n"
    "v_cvt_pk_bf16_f32 v49, v50, v51\n"
    "v_mfma_f32_16x16x32_bf16 v[68:71], a[8:11], v[16:19], 0\n"
    "ds_read_b128 v[16:19], v9 offset:4096\n"
    "v_cvt_pk_bf16_f32 v50, v52, v53\n"
    "v_mfma_f32_16x16x32_bf16 v[56:59], a[12:15], v[20:23], v[56:59]\n"
    "v_cvt_pk_bf16_f32 v51, v54, v55\n"
    "v_mfma_f32_16x16x32_bf16 v[60:63], a[16:19], v[20:23], v[60:63]\n"
    "s_waitcnt lgkmcnt(2)\n"
    "v_cvt_pk_bf16_f32 v64, v64, v65\n"
    "v_cvt_pk_bf16_f32 v65, v66, v67\n"
    "v_mfma_f32_16x16x32_bf16 v[68:71], a[20:23], v[20:23], v[68:71]\n"
    "ds_read_b128 v[20:23], v9 offset:5120\n"
    "s_nop 0\n"
    "v_mfma_f32_16x16x32_bf16 v[56:59], a[24:27], v[24:27], v[56:59]\n"
    "buffer_store_dwordx4 v[48:51], v15, s[36:39], s73 offen sc1\n"
    "v_mfma_f32_16x16x32_bf16 v[60:63], a[28:31], v[24:27], v[60:63]\n"
    "s_waitcnt lgkmcnt(2)\n"
    "s_waitcnt vmcnt(2)\n"
    "v_add_u32 v13, v11, v3\n"
    "v_mfma_f32_16x16x32_bf16 v[68:71], a[32:35], v[24:27], v[68:71]\n"
    "ds_read_b128 v[24:27], v9 offset:6144\n"
    "v_cmp_gt_i32 vcc, s71, v13\n"
    "v_mfma_f32_16x16x32_bf16 v[56:59], a[36:39], v[28:31], v[56:59]\n"
    "buffer_atomic_smax v7, v14, s[36:39], s69 offen sc1\n"
    "v_permlane16_swap_b32 v64, v66\n"
    "v_mfma_f32_16x16x32_bf16 v[60:63], a[40:43], v[28:31], v[60:63]\n"
    "s_waitcnt lgkmcnt(2)\n"
    "v_permlane16_swap_b32 v65, v67\n"
    "v_mfma_f32_16x16x32_bf16 v[68:71], a[44:47], v[28:31], v[68:71]\n"
    "ds_read_b128 v[28:31], v9 offset:7168\n"
    "s_cbranch_vccnz .LgWO_poll1_%=\n"
    "v_mfma_f32_16x16x32_bf16 v[56:59], a[48:51], v[16:19], v[56:59]\n"
    "s_add_u32 m0, s59, 0\n"
    "v_mfma_f32_16x16x32_bf16 v[60:63], a[52:55], v[16:19], v[60:63]\n"
    "s_waitcnt lgkmcnt(2)\n"
    "buffer_load_dwordx4 v1, s[36:39], s76 offen sc1 lds\n"
    "v_mfma_f32_16x16x32_bf16 v[68:71], a[56:59], v[16:19], v[68:71]\n"
    "ds_read_b128 v[16:19], v9 offset:8192\n"
    "v_mfma_f32_16x16x32_bf16 v[56:59], a[60:63], v[20:23], v[56:59]\n"
    "buffer_store_dwordx4 v[64:67], v4, s[36:39], s73 offen sc1\n"
    "v_mfma_f32_16x16x32_bf16 v[60:63], a[64:67], v[20:23], v[60:63]\n"
    "s_waitcnt lgkmcnt(2)\n"
    "s_add_u32 s76, s76, 16384\n"
    "s_lshr_b32 s73, s68, 1\n"
    "v_mfma_f32_16x16x32_bf16 v[68:71], a[68:71], v[20:23], v[68:71]\n"
    "ds_read_b128 v[20:23], v9 offset:9216\n"
    "s_and_b32 s73, s73, s57\n"
    "v_mfma_f32_16x16x32_bf16 v[56:59], a[72:75], v[24:27], v[56:59]\n"
    "s_add_u32 m0, s59, 4096\n"
    "s_mul_i32 s73, s73, s56\n"
    "v_mfma_f32_16x16x32_bf16 v[60:63], a[76:79], v[24:27], v[60:63]\n"
    "s_waitcnt lgkmcnt(2)\n"
    "buffer_load_dwordx4 v1, s[36:39], s76 offen sc1 lds\n"
    "s_add_u32 s73, s73, s55\n"
    "v_mfma_f32_16x16x32_bf16 v[68:71], a[80:83], v[24:27], v[68:71]\n"
    "ds_read_b128 v[24:27], v9 offset:10240\n"
    "v_mfma_f32_16x16x32_bf16 v[56:59], a[84:87], v[28:31], v[56:59]\n"
    "s_add_u32 s76, s76, 16384\n"
    "s_add_u32 m0, s59, 8192\n"
    "v_mfma_f32_16x16x32_bf16 v[60:63], a[88:91], v[28:31], v[60:63]\n"
    "s_waitcnt lgkmcnt(2)\n"
    "buffer_load_dwordx4 v1, s[36:39], s76 offen sc1 lds\n"
    "v_mfma_f32_16x16x32_bf16 v[68:71], a[92:95], v[28:31], v[68:71]\n"
    "ds_read_b128 v[28:31], v9 offset:11264\n"
    "v_mfma_f32_16x16x32_bf16 v[56:59], a[96:99], v[16:19], v[56:59]\n"
    "v_mfma_f32_16x16x32_bf16 v[60:63], a[100:103], v[16:19], v[60:63]\n"
    "s_waitcnt lgkmcnt(2)\n"
    "v_mfma_f32_16x16x32_bf16 v[68:71], a[104:107], v[16:19], v[68:71]\n"
    "ds_read_b128 v[16:19], v9 offset:12288\n"
    "s_add_u32 s76, s76, 16384\n"
    "v_mfma_f32_16x16x32_bf16 v[56:59], a[108:111], v[20:23], v[56:59]\n"
    "s_add_u32 m0, s59, 12288\n"
    "v_mfma_f32_16x16x32_bf16 v[60:63], a[112:115], v[20:23], v[60:63]\n"
    "s_waitcnt lgkmcnt(2)\n"
    "buffer_load_dwordx4 v1, s[36:39], s76 offen sc1 lds\n"
    "v_mfma_f32_16x16x32_bf16 v[68:71], a[116:119], v[20:23], v[68:71]\n"
    "ds_read_b128 v[20:23], v9 offset:13312\n"
    "v_mfma_f32_16x16x32_bf16 v[56:59], a[120:123], v[24:27], v[56:59]\n"
    "v_mfma_f32_16x16x32_bf16 v[60:63], a[124:127], v[24:27], v[60:63]\n"
    "s_waitcnt lgkmcnt(2)\n"
    "v_mfma_f32_16x16x32_bf16 v[68:71], a[128:131], v[24:27], v[68:71]\n"
    "ds_read_b128 v[24:27], v9 offset:14336\n"
    "s_add_u32 s76, s76, 16384\n"
    "v_mfma_f32_16x16x32_bf16 v[56:59], a[132:135], v[28:31], v[56:59]\n"
    "s_add_u32 m0, s59, 16384\n"
    "v_mfma_f32_16x16x32_bf16 v[60:63], a[136:139], v[28:31], v[60:63]\n"
    "s_waitcnt lgkmcnt(2)\n"
    "buffer_load_dwordx4 v1, s[36:39], s76 offen sc1 lds\n"
    "v_mfma_f32_16x16x32_bf16 v[68:71], a[140:143], v[28:31], v[68:71]\n"
    "ds_read_b128 v[28:31], v9 offset:15360\n"
    "v_mfma_f32_16x16x32_bf16 v[56:59], a[144:147], v[16:19], v[56:59]\n"
    "v_mfma_f32_16x16x32_bf16 v[60:63], a[148:151], v[16:19], v[60:63]\n"
    "s_waitcnt lgkmcnt(2)\n"
    "v_mfma_f32_16x16x32_bf16 v[68:71], a[152:155], v[16:19], v[68:71]\n"
    "ds_read_b128 v[16:19], v9 offset:16384\n"
    "s_add_u32 s76, s76, 16384\n"
    "v_mfma_f32_16x16x32_bf16 v[56:59], a[156:159], v[20:23], v[56:59]\n"
    "s_add_u32 m0, s59, 20480\n"
    "v_mfma_f32_16x16x32_bf16 v[60:63], a[160:163], v[20:23], v[60:63]\n"
    "s_waitcnt lgkmcnt(2)\n"
    "buffer_load_dwordx4 v1, s[36:39], s76 offen sc1 lds\n"
    "v_mfma_f32_16x16x32_bf16 v[68:71], a[164:167], v[20:23], v[68:71]\n"
    "ds_read_b128 v[20:23], v9 offset:17408\n"
    "v_mfma_f32_16x16x32_bf16 v[56:59], a[168:171], v[24:27], v[56:59]\n"
    "v_mfma_f32_16x16x32_bf16 v[60:63], a[172:175], v[24:27], v[60:63]\n"
    "s_waitcnt lgkmcnt(2)\n"
    "s_add_u32 s76, s76, 16384\n"
    "s_add_u32 m0, s59, 24576\n"
    "v_mfma_f32_16x16x32_bf16 v[68:71], a[176:179], v[24:27], v[68:71]\n"
    "ds_read_b128 v[24:27], v9 offset:18432\n"
    "buffer_load_dwordx4 v1, s[36:39], s76 offen sc1 lds\n"
    "v_mfma_f32_16x16x32_bf16 v[56:59], a[180:183], v[28:31], v[56:59]\n"
    "v_mfma_f32_16x16x32_bf16 v[60:63], a[184:187], v[28:31], v[60:63]\n"
    "s_waitcnt lgkmcnt(2)\n"
    "v_mfma_f32_16x16x32_bf16 v[68:71], a[188:191], v[28:31], v[68:71]\n"
    "ds_read_b128 v[28:31], v9 offset:19456\n"
    "v_mfma_f32_16x16x32_bf16 v[56:59], a[192:195], v[16:19], v[56:59]\n"
    "v_mfma_f32_16x16x32_bf16 v[60:63], a[196:199], v[16:19], v[60:63]\n"
    "s_waitcnt lgkmcnt(2)\n"
    "s_add_u32 s76, s76, 16384\n"
    "s_add_u32 m0, s59, 28672\n"
    "v_mfma_f32_16x16x32_bf16 v[68:71], a[200:203], v[16:19], v[68:71]\n"
    "ds_read_b128 v[16:19], v9 offset:20480\n"
    "buffer_load_dwordx4 v1, s[36:39], s76 offen sc1 lds\n"
    "v_mfma_f32_16x16x32_bf16 v[56:59], a[204:207], v[20:23], v[56:59]\n"
    "v_mfma_f32_16x16x32_bf16 v[60:63], a[208:211], v[20:23], v[60:63]\n"
    "s_waitcnt lgkmcnt(2)\n"
    "v_mfma_f32_16x16x32_bf16 v[68:71], a[212:215], v[20:23], v[68:71]\n"
    "ds_read_b128 v[20:23], v9 offset:21504\n"
    "v_mfma_f32_16x16x32_bf16 v[56:59], a[216:219], v[24:27], v[56:59]\n"
    "v_mfma_f32_16x16x32_bf16 v[60:63], a[220:223], v[24:27], v[60:63]\n"
    "s_waitcnt lgkmcnt(2)\n"
    "s_waitcnt vmcnt(7)\n"
    "buffer_atomic_smax v12, v14, s[36:39], s70 offen sc1\n"
    "v_mfma_f32_16x16x32_bf16 v[68:71], a[224:227], v[24:27], v[68:71]\n"
    "ds_read_b128 v[24:27], v9 offset:22528\n"
    "v_mfma_f32_16x16x32_bf16 v[56:59], a[228:231], v[28:31], v[56:59]\n"
    "v_mfma_f32_16x16x32_bf16 v[60:63], a[232:235], v[28:31], v[60:63]\n"
    "s_waitcnt lgkmcnt(2)\n"
    "v_mfma_f32_16x16x32_bf16 v[68:71], a[236:239], v[28:31], v[68:71]\n"
    "ds_read_b128 v[28:31], v9 offset:23552\n"
    ".LgWO_join1_%=:\n"
    "v_mfma_f32_16x16x32_bf16 v[56:59], a[240:243], v[16:19], v[56:59]\n"
    "s_add_u32 s71, s68, 2\n"
    "s_waitcnt vmcnt(8)\n"
    "v_mfma_f32_16x16x32_bf16 v[60:63], a[244:247], v[16:19], v[60:63]\n"
    "s_waitcnt lgkmcnt(2)\n"
    "v_add_u32 v13, v11, v3\n"
    "v_mfma_f32_16x16x32_bf16 v[68:71], a[248:251], v[16:19], v[68:71]\n"
    "ds_read_b128 v[16:19], v9 offset:24576\n"
    "v_cmp_gt_i32 vcc, s71, v13\n"
    "v_mfma_f32_16x16x32_bf16 v[56:59], a[252:255], v[20:23], v[56:59]\n"
    "v_mfma_f32_16x16x32_bf16 v[60:63], v[128:131], v[20:23], v[60:63]\n"
    "s_waitcnt lgkmcnt(2)\n"
    "v_mfma_f32_16x16x32_bf16 v[68:71], v[132:135], v[20:23], v[68:71]\n"
    "ds_read_b128 v[20:23], v9 offset:25600\n"
    "v_mfma_f32_16x16x32_bf16 v[56:59], v[136:139], v[24:27], v[56:59]\n"
    "s_or_b64 exec, vcc, s[62:63]\n"
    "buffer_atomic_smax v11, v2, s[36:39], 0 offen sc0 sc1\n"
    "s_mov_b64 exec, -1\n"
    "v_mfma_f32_16x16x32_bf16 v[60:63], v[140:143], v[24:27], v[60:63]\n"
    "s_waitcnt lgkmcnt(2)\n"
    "s_lshr_b32 s76, s71, 1\n"
    "v_mfma_f32_16x16x32_bf16 v[68:71], v[144:147], v[24:27], v[68:71]\n"
    "ds_read_b128 v[24:27], v9 offset:26624\n"
    "s_and_b32 s76, s76, s54\n"
    "v_mfma_f32_16x16x32_bf16 v[56:59], v[148:151], v[28:31], v[56:59]\n"
    "s_mul_i32 s76, s76, s53\n"
    "v_mfma_f32_16x16x32_bf16 v[60:63], v[152:155], v[28:31], v[60:63]\n"
    "s_waitcnt lgkmcnt(2)\n"
    "s_add_u32 s76, s76, s52\n"
    "v_mfma_f32_16x16x32_bf16 v[68:71], v[156:159], v[28:31], v[68:71]\n"
    "ds_read_b128 v[28:31], v9 offset:27648\n"
    "s_add_u32 s76, s76, 1024\n"
    "v_mfma_f32_16x16x32_bf16 v[56:59], v[160:163], v[16:19], v[56:59]\n"
    "v_mfma_f32_16x16x32_bf16 v[60:63], v[164:167], v[16:19], v[60:63]\n"
    "s_waitcnt lgkmcnt(2)\n"
    "v_mfma_f32_16x16x32_bf16 v[68:71], v[168:171], v[16:19], v[68:71]\n"
    "ds_read_b128 v[16:19], v9 offset:28672\n"
    "v_mfma_f32_16x16x32_bf16 v[56:59], v[172:175], v[20:23], v[56:59]\n"
    "v_mfma_f32_16x16x32_bf16 v[60:63], v[176:179], v[20:23], v[60:63]\n"
    "s_waitcnt lgkmcnt(2)\n"
    "v_mfma_f32_16x16x32_bf16 v[68:71], v[180:183], v[20:23], v[68:71]\n"
    "ds_read_b128 v[20:23], v9 offset:29696\n"
    "v_mfma_f32_16x16x32_bf16 v[56:59], v[184:187], v[24:27], v[56:59]\n"
    "v_mfma_f32_16x16x32_bf16 v[60:63], v[188:191], v[24:27], v[60:63]\n"
    "s_waitcnt lgkmcnt(2)\n"
    "v_mfma_f32_16x16x32_bf16 v[68:71], v[192:195], v[24:27], v[68:71]\n"
    "ds_read_b128 v[24:27], v9 offset:30720\n"
    "v_mfma_f32_16x16x32_bf16 v[56:59], v[196:199], v[28:31], v[56:59]\n"
    "v_mfma_f32_16x16x32_bf16 v[60:63], v[200:203], v[28:31], v[60:63]\n"
    "s_waitcnt lgkmcnt(2)\n"
    "v_add_u32 v13, v5, v3\n"
    "v_mfma_f32_16x16x32_bf16 v[68:71], v[204:207], v[28:31], v[68:71]\n"
    "ds_read_b128 v[28:31], v9 offset:31744\n"
    "v_cmp_gt_i32 vcc, s71, v13\n"
    "v_mfma_f32_16x16x32_bf16 v[56:59], v[208:211], v[16:19], v[56:59]\n"
    "v_mfma_f32_16x16x32_bf16 v[60:63], v[212:215], v[16:19], v[60:63]\n"
    "s_waitcnt lgkmcnt(2)\n"
    "v_mfma_f32_16x16x32_bf16 v[68:71], v[216:219], v[16:19], v[68:71]\n"
    "v_mfma_f32_16x16x32_bf16 v[56:59], v[220:223], v[20:23], v[56:59]\n"
    "s_waitcnt vmcnt(1) lgkmcnt(0)\n"
    "s_barrier\n"
    "ds_read_b128 v[16:19], v0 offset:0\n"
    "s_or_b64 exec, vcc, s[62:63]\n"
    "buffer_atomic_smax v5, v2, s[36:39], 0 offen sc0 sc1\n"
    "s_mov_b64 exec, -1\n"
    "v_mfma_f32_16x16x32_bf16 v[60:63], v[224:227], v[20:23], v[60:63]\n"
    "v_mfma_f32_16x16x32_bf16 v[68:71], v[228:231], v[20:23], v[68:71]\n"
    "ds_read_b128 v[20:23], v0 offset:1024\n"
    "v_mfma_f32_16x16x32_bf16 v[56:59], v[232:235], v[24:27], v[56:59]\n"
    "v_mfma_f32_16x16x32_bf16 v[60:63], v[236:239], v[24:27], v[60:63]\n"
    "v_mfma_f32_16x16x32_bf16 v[68:71], v[240:243], v[24:27], v[68:71]\n"
    "ds_read_b128 v[24:27], v0 offset:2048\n"
    "v_mfma_f32_16x16x32_bf16 v[56:59], v[244:247], v[28:31], v[56:59]\n"
    "s_mov_b32 s69, 0x80000000\n"
    "s_mov_b32 s70, 0\n"
    "v_mov_b32 v12, s68\n"
    "v_mfma_f32_16x16x32_bf16 v[60:63], v[248:251], v[28:31], v[60:63]\n"
    "s_waitcnt lgkmcnt(2)\n"
    "s_add_u32 s68, s68, 1\n"
    "v_mfma_f32_16x16x32_bf16 v[68:71], v[252:255], v[28:31], v[68:71]\n"
    "ds_read_b128 v[28:31], v0 offset:3072\n"
    "s_branch .LgWO_head0_%=\n"
    ".LgWO_LOOPE_%=:\n"
    ".LgWO_done_%=:\n"
    "s_waitcnt vmcnt(0)\n"
    "buffer_atomic_smax v12, v10, s[36:39], 0 offen sc1\n"
    "s_waitcnt vmcnt(0)\n"
    "s_branch .LgWO_FIN_%=\n"
    ".LgWO_poll0_%=:\n"
    "v_mfma_f32_16x16x32_bf16 v[48:51], a[48:51], v[16:19], v[48:51]\n"
    "s_or_b64 exec, vcc, s[62:63]\n"
    "buffer_atomic_smax v11, v2, s[36:39], 0 offen sc0 sc1\n"
    "s_mov_b64 exec, -1\n"
    "v_mfma_f32_16x16x32_bf16 v[52:55], a[52:55], v[16:19], v[52:55]\n"
    "s_waitcnt lgkmcnt(2)\n"
    "v_mfma_f32_16x16x32_bf16 v[64:67], a[56:59], v[16:19], v[64:67]\n"
    "ds_read_b128 v[16:19], v0 offset:8192\n"
    "v_mfma_f32_16x16x32_bf16 v[48:51], a[60:63], v[20:23], v[48:51]\n"
    "buffer_store_dwordx4 v[68:71], v4, s[36:39], s73 offen offset:1024 sc1\n"
    "v_mfma_f32_16x16x32_bf16 v[52:55], a[64:67], v[20:23], v[52:55]\n"
    "s_waitcnt lgkmcnt(2)\n"
    "s_lshr_b32 s73, s68, 1\n"
    "v_mfma_f32_16x16x32_bf16 v[64:67], a[68:71], v[20:23], v[64:67]\n"
    "ds_read_b128 v[20:23], v0 offset:9216\n"
    "s_and_b32 s73, s73, s57\n"
    "v_mfma_f32_16x16x32_bf16 v[48:51], a[72:75], v[24:27], v[48:51]\n"
    "s_mul_i32 s73, s73, s56\n"
    "v_mfma_f32_16x16x32_bf16 v[52:55], a[76:79], v[24:27], v[52:55]\n"
    "s_waitcnt lgkmcnt(2)\n"
    "s_add_u32 s73, s73, s55\n"
    "v_mfma_f32_16x16x32_bf16 v[64:67], a[80:83], v[24:27], v[64:67]\n"
    "ds_read_b128 v[24:27], v0 offset:10240\n"
    "v_mfma_f32_16x16x32_bf16 v[48:51], a[84:87], v[28:31], v[48:51]\n"
    "v_mfma_f32_16x16x32_bf16 v[52:55], a[88:91], v[28:31], v[52:55]\n"
    "s_waitcnt lgkmcnt(2)\n"
    "v_mfma_f32_16x16x32_bf16 v[64:67], a[92:95], v[28:31], v[64:67]\n"
    "ds_read_b128 v[28:31], v0 offset:11264\n"
    "v_mfma_f32_16x16x32_bf16 v[48:51], a[96:99], v[16:19], v[48:51]\n"
    "v_mfma_f32_16x16x32_bf16 v[52:55], a[100:103], v[16:19], v[52:55]\n"
    "s_waitcnt lgkmcnt(2)\n"
    "v_mfma_f32_16x16x32_bf16 v[64:67], a[104:107], v[16:19], v[64:67]\n"
    "ds_read_b128 v[16:19], v0 offset:12288\n"
    "v_mfma_f32_16x16x32_bf16 v[48:51], a[108:111], v[20:23], v[48:51]\n"
    "v_mfma_f32_16x16x32_bf16 v[52:55], a[112:115], v[20:23], v[52:55]\n"
    "s_waitcnt lgkmcnt(2)\n"
    "s_waitcnt vmcnt(4)\n"
    "v_add_u32 v13, v5, v3\n"
    "v_mfma_f32_16x16x32_bf16 v[64:67], a[116:119], v[20:23], v[64:67]\n"
    "ds_read_b128 v[20:23], v0 offset:13312\n"
    "v_cmp_gt_i32 vcc, s71, v13\n"
    "v_mfma_f32_16x16x32_bf16 v[48:51], a[120:123], v[24:27], v[48:51]\n"
    "v_mfma_f32_16x16x32_bf16 v[52:55], a[124:127], v[24:27], v[52:55]\n"
    "s_waitcnt lgkmcnt(2)\n"
    "v_mfma_f32_16x16x32_bf16 v[64:67], a[128:131], v[24:27], v[64:67]\n"
    "ds_read_b128 v[24:27], v0 offset:14336\n"
    "s_cbranch_vccz .LgWO_dma0_33_%=\n"
    "v_mfma_f32_16x16x32_bf16 v[48:51], a[132:135], v[28:31], v[48:51]\n"
    "s_or_b64 exec, vcc, s[62:63]\n"
    "buffer_atomic_smax v5, v2, s[36:39], 0 offen sc0 sc1\n"
    "s_mov_b64 exec, -1\n"
    "v_mfma_f32_16x16x32_bf16 v[52:55], a[136:139], v[28:31], v[52:55]\n"
    "s_waitcnt lgkmcnt(2)\n"
    "v_mfma_f32_16x16x32_bf16 v[64:67], a[140:143], v[28:31], v[64:67]\n"
    "ds_read_b128 v[28:31], v0 offset:15360\n"
    "v_mfma_f32_16x16x32_bf16 v[48:51], a[144:147], v[16:19], v[48:51]\n"
    "v_mfma_f32_16x16x32_bf16 v[52:55], a[148:151], v[16:19], v[52:55]\n"
    "s_waitcnt lgkmcnt(2)\n"
    "v_mfma_f32_16x16x32_bf16 v[64:67], a[152:155], v[16:19], v[64:67]\n"
    "ds_read_b128 v[16:19], v0 offset:16384\n"
    "v_mfma_f32_16x16x32_bf16 v[48:51], a[156:159], v[20:23], v[48:51]\n"
    "v_mfma_f32_16x16x32_bf16 v[52:55], a[160:163], v[20:23], v[52:55]\n"
    "s_waitcnt lgkmcnt(2)\n"
    "v_mfma_f32_16x16x32_bf16 v[64:67], a[164:167], v[20:23], v[64:67]\n"
    "ds_read_b128 v[20:23], v0 offset:17408\n"
    "v_mfma_f32_16x16x32_bf16 v[48:51], a[168:171], v[24:27], v[48:51]\n"
    "v_mfma_f32_16x16x32_bf16 v[52:55], a[172:175], v[24:27], v[52:55]\n"
    "s_waitcnt lgkmcnt(2)\n"
    "v_mfma_f32_16x16x32_bf16 v[64:67], a[176:179], v[24:27], v[64:67]\n"
    "ds_read_b128 v[24:27], v0 offset:18432\n"
    "v_mfma_f32_16x16x32_bf16 v[48:51], a[180:183], v[28:31], v[48:51]\n"
    "v_mfma_f32_16x16x32_bf16 v[52:55], a[184:187], v[28:31], v[52:55]\n"
    "s_waitcnt lgkmcnt(2)\n"
    "v_mfma_f32_16x16x32_bf16 v[64:67], a[188:191], v[28:31], v[64:67]\n"
    "ds_read_b128 v[28:31], v0 offset:19456\n"
    "v_mfma_f32_16x16x32_bf16 v[48:51], a[192:195], v[16:19], v[48:51]\n"
    "v_mfma_f32_16x16x32_bf16 v[52:55], a[196:199], v[16:19], v[52:55]\n"
    "s_waitcnt lgkmcnt(2)\n"
    "s_waitcnt vmcnt(2)\n"
    "v_add_u32 v13, v11, v3\n"
    "v_mfma_f32_16x16x32_bf16 v[64:67], a[200:203], v[16:19], v[64:67]\n"
    "ds_read_b128 v[16:19], v0 offset:20480\n"
    "v_cmp_gt_i32 vcc, s71, v13\n"
    "v_mfma_f32_16x16x32_bf16 v[48:51], a[204:207], v[20:23], v[48:51]\n"
    "v_mfma_f32_16x16x32_bf16 v[52:55], a[208:211], v[20:23], v[52:55]\n"
    "s_waitcnt lgkmcnt(2)\n"
    "v_mfma_f32_16x16x32_bf16 v[64:67], a[212:215], v[20:23], v[64:67]\n"
    "ds_read_b128 v[20:23], v0 offset:21504\n"
    "s_cbranch_vccz .LgWO_dma0_54_%=\n"
    "v_mfma_f32_16x16x32_bf16 v[48:51], a[216:219], v[24:27], v[48:51]\n"
    "s_or_b64 exec, vcc, s[62:63]\n"
    "buffer_atomic_smax v11, v2, s[36:39], 0 offen sc0 sc1\n"
    "s_mov_b64 exec, -1\n"
    "v_mfma_f32_16x16x32_bf16 v[52:55], a[220:223], v[24:27], v[52:55]\n"
    "s_waitcnt lgkmcnt(2)\n"
    "s_waitcnt vmcnt(2)\n"
    "buffer_atomic_smax v12, v10, s[36:39], s70 offen sc1\n"
    "v_mfma_f32_16x16x32_bf16 v[64:67], a[224:227], v[24:27], v[64:67]\n"
    "ds_read_b128 v[24:27], v0 offset:22528\n"
    "v_mfma_f32_16x16x32_bf16 v[48:51], a[228:231], v[28:31], v[48:51]\n"
    "v_mfma_f32_16x16x32_bf16 v[52:55], a[232:235], v[28:31], v[52:55]\n"
    "s_waitcnt lgkmcnt(2)\n"
    "v_mfma_f32_16x16x32_bf16 v[64:67], a[236:239], v[28:31], v[64:67]\n"
    "ds_read_b128 v[28:31], v0 offset:23552\n"
    "v_mfma_f32_16x16x32_bf16 v[48:51], a[240:243], v[16:19], v[48:51]\n"
    "v_mfma_f32_16x16x32_bf16 v[52:55], a[244:247], v[16:19], v[52:55]\n"
    "s_waitcnt lgkmcnt(2)\n"
    "v_mfma_f32_16x16x32_bf16 v[64:67], a[248:251], v[16:19], v[64:67]\n"
    "ds_read_b128 v[16:19], v0 offset:24576\n"
    "v_mfma_f32_16x16x32_bf16 v[48:51], a[252:255], v[20:23], v[48:51]\n"
    "v_mfma_f32_16x16x32_bf16 v[52:55], v[128:131], v[20:23], v[52:55]\n"
    "s_waitcnt lgkmcnt(2)\n"
    "v_mfma_f32_16x16x32_bf16 v[64:67], v[132:135], v[20:23], v[64:67]\n"
    "ds_read_b128 v[20:23], v0 offset:25600\n"
    "v_mfma_f32_16x16x32_bf16 v[48:51], v[136:139], v[24:27], v[48:51]\n"
    "v_mfma_f32_16x16x32_bf16 v[52:55], v[140:143], v[24:27], v[52:55]\n"
    "s_waitcnt lgkmcnt(2)\n"
    "v_mfma_f32_16x16x32_bf16 v[64:67], v[144:147], v[24:27], v[64:67]\n"
    "ds_read_b128 v[24:27], v0 offset:26624\n"
    "v_mfma_f32_16x16x32_bf16 v[48:51], v[148:151], v[28:31], v[48:51]\n"
    "v_mfma_f32_16x16x32_bf16 v[52:55], v[152:155], v[28:31], v[52:55]\n"
    "s_waitcnt lgkmcnt(2)\n"
    "s_waitcnt vmcnt(2)\n"
    "v_add_u32 v13, v5, v3\n"
    "v_mfma_f32_16x16x32_bf16 v[64:67], v[156:159], v[28:31], v[64:67]\n"
    "ds_read_b128 v[28:31], v0 offset:27648\n"
    "v_cmp_gt_i32 vcc, s71, v13\n"
    "v_mfma_f32_16x16x32_bf16 v[48:51], v[160:163], v[16:19], v[48:51]\n"
    "v_mfma_f32_16x16x32_bf16 v[52:55], v[164:167], v[16:19], v[52:55]\n"
    "s_waitcnt lgkmcnt(2)\n"
    "v_mfma_f32_16x16x32_bf16 v[64:67], v[168:171], v[16:19], v[64:67]\n"
    "ds_read_b128 v[16:19], v0 offset:28672\n"
    "s_cbranch_vccz .LgWO_dma0_75_%=\n"
    "v_mfma_f32_16x16x32_bf16 v[48:51], v[172:175], v[20:23], v[48:51]\n"
    "s_or_b64 exec, vcc, s[62:63]\n"
    "buffer_atomic_smax v5, v2, s[36:39], 0 offen sc0 sc1\n"
    "s_mov_b64 exec, -1\n"
    "v_mfma_f32_16x16x32_bf16 v[52:55], v[176:179], v[20:23], v[52:55]\n"
    "s_waitcnt lgkmcnt(2)\n"
    "v_mfma_f32_16x16x32_bf16 v[64:67], v[180:183], v[20:23], v[64:67]\n"
    "ds_read_b128 v[20:23], v0 offset:29696\n"
    "v_mfma_f32_16x16x32_bf16 v[48:51], v[184:187], v[24:27], v[48:51]\n"
    "v_mfma_f32_16x16x32_bf16 v[52:55], v[188:191], v[24:27], v[52:55]\n"
    "s_waitcnt lgkmcnt(2)\n"
    "v_mfma_f32_16x16x32_bf16 v[64:67], v[192:195], v[24:27], v[64:67]\n"
    "ds_read_b128 v[24:27], v0 offset:30720\n"
    "v_mfma_f32_16x16x32_bf16 v[48:51], v[196:199], v[28:31], v[48:51]\n"
    "v_mfma_f32_16x16x32_bf16 v[52:55], v[200:203], v[28:31], v[52:55]\n"
    "s_waitcnt lgkmcnt(2)\n"
    "v_mfma_f32_16x16x32_bf16 v[64:67], v[204:207], v[28:31], v[64:67]\n"
    "ds_read_b128 v[28:31], v0 offset:31744\n"
    "v_mfma_f32_16x16x32_bf16 v[48:51], v[208:211], v[16:19], v[48:51]\n"
    "v_mfma_f32_16x16x32_bf16 v[52:55], v[212:215], v[16:19], v[52:55]\n"
    "s_waitcnt lgkmcnt(2)\n"
    "v_mfma_f32_16x16x32_bf16 v[64:67], v[216:219], v[16:19], v[64:67]\n"
    "v_mfma_f32_16x16x32_bf16 v[48:51], v[220:223], v[20:23], v[48:51]\n"
    "v_mfma_f32_16x16x32_bf16 v[52:55], v[224:227], v[20:23], v[52:55]\n"
    "s_waitcnt lgkmcnt(1)\n"
    "v_mfma_f32_16x16x32_bf16 v[64:67], v[228:231], v[20:23], v[64:67]\n"
    "v_mfma_f32_16x16x32_bf16 v[48:51], v[232:235], v[24:27], v[48:51]\n"
    "v_mfma_f32_16x16x32_bf16 v[52:55], v[236:239], v[24:27], v[52:55]\n"
    "s_waitcnt lgkmcnt(0)\n"
    "v_mfma_f32_16x16x32_bf16 v[64:67], v[240:243], v[24:27], v[64:67]\n"
    "v_mfma_f32_16x16x32_bf16 v[48:51], v[244:247], v[28:31], v[48:51]\n"
    "v_mfma_f32_16x16x32_bf16 v[52:55], v[248:251], v[28:31], v[52:55]\n"
    "v_mfma_f32_16x16x32_bf16 v[64:67], v[252:255], v[28:31], v[64:67]\n"
    "v_cvt_pk_bf16_f32 v48, v48, v49\n"
    "v_cvt_pk_bf16_f32 v49, v50, v51\n"
    "s_nop 1\n"
    "v_cvt_pk_bf16_f32 v50, v52, v53\n"
    "v_cvt_pk_bf16_f32 v51, v54, v55\n"
    "buffer_store_dwordx4 v[48:51], v15, s[36:39], s73 offen sc1\n"
    "s_nop 0\n"
    "v_cvt_pk_bf16_f32 v64, v64, v65\n"
    "v_cvt_pk_bf16_f32 v65, v66, v67\n"
    "s_nop 0\n"
    "v_permlane16_swap_b32 v64, v66\n"
    "v_permlane16_swap_b32 v65, v67\n"
    "buffer_store_dwordx4 v[64:67], v4, s[36:39], s73 offen sc1\n"
    "v_mov_b32 v12, s68\n"
    "s_mov_b32 s73, 0x80000000\n"
    "s_mov_b32 s68, s71\n"
    "s_waitcnt vmcnt(4)\n"
    "v_add_u32 v13, v11, v3\n"
    "v_cmp_gt_i32 vcc, s71, v13\n"
    "s_cbranch_vccnz .LgWO_idle0_%=\n"
    "s_add_u32 m0, s59, 32768\n"
    "s_nop 0\n"
    "buffer_load_dwordx4 v1, s[36:39], s76 offen sc1 lds\n"
    "s_add_u32 m0, s59, 36864\n"
    "s_add_u32 s76, s76, 16384\n"
    "buffer_load_dwordx4 v1, s[36:39], s76 offen sc1 lds\n"
    "s_add_u32 m0, s59, 40960\n"
    "s_add_u32 s76, s76, 16384\n"
    "buffer_load_dwordx4 v1, s[36:39], s76 offen sc1 lds\n"
    "s_add_u32 m0, s59, 45056\n"
    "s_add_u32 s76, s76, 16384\n"
    "buffer_load_dwordx4 v1, s[36:39], s76 offen sc1 lds\n"
    "s_add_u32 m0, s59, 49152\n"
    "s_add_u32 s76, s76, 16384\n"
    "buffer_load_dwordx4 v1, s[36:39], s76 offen sc1 lds\n"
    "s_add_u32 m0, s59, 53248\n"
    "s_add_u32 s76, s76, 16384\n"
    "buffer_load_dwordx4 v1, s[36:39], s76 offen sc1 lds\n"
    "s_add_u32 m0, s59, 57344\n"
    "s_add_u32 s76, s76, 16384\n"
    "buffer_load_dwordx4 v1, s[36:39], s76 offen sc1 lds\n"
    "s_add_u32 m0, s59, 61440\n"
    "s_add_u32 s76, s76, 16384\n"
    "buffer_load_dwordx4 v1, s[36:39], s76 offen sc1 lds\n"
    "s_waitcnt vmcnt(8)\n"
    "buffer_atomic_smax v12, v14, s[36:39], 0 offen sc1\n"
    "s_mov_b32 s69, 0x80000000\n"
    "s_mov_b32 s70, 0x80000000\n"
    "v_mov_b32 v7, v12\n"
    "s_waitcnt vmcnt(11)\n"
    "s_add_u32 s71, s68, 1\n"
    "v_add_u32 v13, v11, v3\n"
    "v_cmp_gt_i32 vcc, s71, v13\n"
    "s_nop 0\n"
    "s_or_b64 exec, vcc, s[62:63]\n"
    "buffer_atomic_smax v11, v2, s[36:39], 0 offen sc0 sc1\n"
    "s_mov_b64 exec, -1\n"
    "s_waitcnt vmcnt(2)\n"
    "s_branch .LgWO_bar1_%=\n"
    ".LgWO_dma0_33_%=:\n"
    "v_mfma_f32_16x16x32_bf16 v[48:51], a[132:135], v[28:31], v[48:51]\n"
    "s_add_u32 m0, s59, 32768\n"
    "v_mfma_f32_16x16x32_bf16 v[52:55], a[136:139], v[28:31], v[52:55]\n"
    "s_waitcnt lgkmcnt(2)\n"
    "buffer_load_dwordx4 v1, s[36:39], s76 offen sc1 lds\n"
    "v_mfma_f32_16x16x32_bf16 v[64:67], a[140:143], v[28:31], v[64:67]\n"
    "ds_read_b128 v[28:31], v0 offset:15360\n"
    "s_add_u32 s76, s76, 16384\n"
    "v_mfma_f32_16x16x32_bf16 v[48:51], a[144:147], v[16:19], v[48:51]\n"
    "s_add_u32 m0, s59, 36864\n"
    "v_mfma_f32_16x16x32_bf16 v[52:55], a[148:151], v[16:19], v[52:55]\n"
    "s_waitcnt lgkmcnt(2)\n"
    "buffer_load_dwordx4 v1, s[36:39], s76 offen sc1 lds\n"
    "v_mfma_f32_16x16x32_bf16 v[64:67], a[152:155], v[16:19], v[64:67]\n"
    "ds_read_b128 v[16:19], v0 offset:16384\n"
    "v_mfma_f32_16x16x32_bf16 v[48:51], a[156:159], v[20:23], v[48:51]\n"
    "s_add_u32 s76, s76, 16384\n"
    "s_add_u32 m0, s59, 40960\n"
    "v_mfma_f32_16x16x32_bf16 v[52:55], a[160:163], v[20:23], v[52:55]\n"
    "s_waitcnt lgkmcnt(2)\n"
    "buffer_load_dwordx4 v1, s[36:39], s76 offen sc1 lds\n"
    "v_mfma_f32_16x16x32_bf16 v[64:67], a[164:167], v[20:23], v[64:67]\n"
    "ds_read_b128 v[20:23], v0 offset:17408\n"
    "v_mfma_f32_16x16x32_bf16 v[48:51], a[168:171], v[24:27], v[48:51]\n"
    "v_mfma_f32_16x16x32_bf16 v[52:55], a[172:175], v[24:27], v[52:55]\n"
    "s_waitcnt lgkmcnt(2)\n"
    "s_add_u32 s76, s76, 16384\n"
    "s_add_u32 m0, s59, 45056\n"
    "v_mfma_f32_16x16x32_bf16 v[64:67], a[176:179], v[24:27], v[64:67]\n"
    "ds_read_b128 v[24:27], v0 offset:18432\n"
    "buffer_load_dwordx4 v1, s[36:39], s76 offen sc1 lds\n"
    "v_mfma_f32_16x16x32_bf16 v[48:51], a[180:183], v[28:31], v[48:51]\n"
    "v_mfma_f32_16x16x32_bf16 v[52:55], a[184:187], v[28:31], v[52:55]\n"
    "s_waitcnt lgkmcnt(2)\n"
    "v_mfma_f32_16x16x32_bf16 v[64:67], a[188:191], v[28:31], v[64:67]\n"
    "ds_read_b128 v[28:31], v0 offset:19456\n"
    "s_add_u32 s76, s76, 16384\n"
    "v_mfma_f32_16x16x32_bf16 v[48:51], a[192:195], v[16:19], v[48:51]\n"
    "s_add_u32 m0, s59, 49152\n"
    "v_mfma_f32_16x16x32_bf16 v[52:55], a[196:199], v[16:19], v[52:55]\n"
    "s_waitcnt lgkmcnt(2)\n"
    "buffer_load_dwordx4 v1, s[36:39], s76 offen sc1 lds\n"
    "v_mfma_f32_16x16x32_bf16 v[64:67], a[200:203], v[16:19], v[64:67]\n"
    "ds_read_b128 v[16:19], v0 offset:20480\n"
    "v_mfma_f32_16x16x32_bf16 v[48:51], a[204:207], v[20:23], v[48:51]\n"
    "s_add_u32 s76, s76, 16384\n"
    "s_add_u32 m0, s59, 53248\n"
    "v_mfma_f32_16x16x32_bf16 v[52:55], a[208:211], v[20:23], v[52:55]\n"
    "s_waitcnt lgkmcnt(2)\n"
    "buffer_load_dwordx4 v1, s[36:39], s76 offen sc1 lds\n"
    "v_mfma_f32_16x16x32_bf16 v[64:67], a[212:215], v[20:23], v[64:67]\n"
    "ds_read_b128 v[20:23], v0 offset:21504\n"
    "v_mfma_f32_16x16x32_bf16 v[48:51], a[216:219], v[24:27], v[48:51]\n"
    "v_mfma_f32_16x16x32_bf16 v[52:55], a[220:223], v[24:27], v[52:55]\n"
    "s_waitcnt lgkmcnt(2)\n"
    "s_waitcnt vmcnt(6)\n"
    "buffer_atomic_smax v12, v10, s[36:39], s70 offen sc1\n"
    "v_mfma_f32_16x16x32_bf16 v[64:67], a[224:227], v[24:27], v[64:67]\n"
    "ds_read_b128 v[24:27], v0 offset:22528\n"
    "s_add_u32 s76, s76, 16384\n"
    "v_mfma_f32_16x16x32_bf16 v[48:51], a[228:231], v[28:31], v[48:51]\n"
    "s_add_u32 m0, s59, 57344\n"
    "v_mfma_f32_16x16x32_bf16 v[52:55], a[232:235], v[28:31], v[52:55]\n"
    "s_waitcnt lgkmcnt(2)\n"
    "buffer_load_dwordx4 v1, s[36:39], s76 offen sc1 lds\n"
    "v_mfma_f32_16x16x32_bf16 v[64:67], a[236:239], v[28:31], v[64:67]\n"
    "ds_read_b128 v[28:31], v0 offset:23552\n"
    "s_add_u32 s76, s76, 16384\n"
    "v_mfma_f32_16x16x32_bf16 v[48:51], a[240:243], v[16:19], v[48:51]\n"
    "s_add_u32 m0, s59, 61440\n"
    "v_mfma_f32_16x16x32_bf16 v[52:55], a[244:247], v[16:19], v[52:55]\n"
    "s_waitcnt lgkmcnt(2)\n"
    "buffer_load_dwordx4 v1, s[36:39], s76 offen sc1 lds\n"
    "v_mfma_f32_16x16x32_bf16 v[64:67], a[248:251], v[16:19], v[64:67]\n"
    "ds_read_b128 v[16:19], v0 offset:24576\n"
    "v_mfma_f32_16x16x32_bf16 v[48:51], a[252:255], v[20:23], v[48:51]\n"
    "v_mfma_f32_16x16x32_bf16 v[52:55], v[128:131], v[20:23], v[52:55]\n"
    "s_waitcnt lgkmcnt(2)\n"
    "v_mfma_f32_16x16x32_bf16 v[64:67], v[132:135], v[20:23], v[64:67]\n"
    "ds_read_b128 v[20:23], v0 offset:25600\n"
    "v_mfma_f32_16x16x32_bf16 v[48:51], v[136:139], v[24:27], v[48:51]\n"
    "v_mfma_f32_16x16x32_bf16 v[52:55], v[140:143], v[24:27], v[52:55]\n"
    "s_waitcnt lgkmcnt(2)\n"
    "v_mfma_f32_16x16x32_bf16 v[64:67], v[144:147], v[24:27], v[64:67]\n"
    "ds_read_b128 v[24:27], v0 offset:26624\n"
    "v_mfma_f32_16x16x32_bf16 v[48:51], v[148:151], v[28:31], v[48:51]\n"
    "v_mfma_f32_16x16x32_bf16 v[52:55], v[152:155], v[28:31], v[52:55]\n"
    "s_waitcnt lgkmcnt(2)\n"
    "v_mfma_f32_16x16x32_bf16 v[64:67], v[156:159], v[28:31], v[64:67]\n"
    "ds_read_b128 v[28:31], v0 offset:27648\n"
    "v_mfma_f32_16x16x32_bf16 v[48:51], v[160:163], v[16:19], v[48:51]\n"
    "v_mfma_f32_16x16x32_bf16 v[52:55], v[164:167], v[16:19], v[52:55]\n"
    "s_waitcnt lgkmcnt(2)\n"
    "v_mfma_f32_16x16x32_bf16 v[64:67], v[168:171], v[16:19], v[64:67]\n"
    "ds_read_b128 v[16:19], v0 offset:28672\n"
    "v_mfma_f32_16x16x32_bf16 v[48:51], v[172:175], v[20:23], v[48:51]\n"
    "v_mfma_f32_16x16x32_bf16 v[52:55], v[176:179], v[20:23], v[52:55]\n"
    "s_waitcnt lgkmcnt(2)\n"
    "v_mfma_f32_16x16x32_bf16 v[64:67], v[180:183], v[20:23], v[64:67]\n"
    "ds_read_b128 v[20:23], v0 offset:29696\n"
    "v_mfma_f32_16x16x32_bf16 v[48:51], v[184:187], v[24:27], v[48:51]\n"
    "v_mfma_f32_16x16x32_bf16 v[52:55], v[188:191], v[24:27], v[52:55]\n"
    "s_waitcnt lgkmcnt(2)\n"
    "v_mfma_f32_16x16x32_bf16 v[64:67], v[192:195], v[24:27], v[64:67]\n"
    "ds_read_b128 v[24:27], v0 offset:30720\n"
    "v_mfma_f32_16x16x32_bf16 v[48:51], v[196:199], v[28:31], v[48:51]\n"
    "v_mfma_f32_16x16x32_bf16 v[52:55], v[200:203], v[28:31], v[52:55]\n"
    "s_waitcnt lgkmcnt(2)\n"
    "v_mfma_f32_16x16x32_bf16 v[64:67], v[204:207], v[28:31], v[64:67]\n"
    "ds_read_b128 v[28:31], v0 offset:31744\n"
    "v_mfma_f32_16x16x32_bf16 v[48:51], v[208:211], v[16:19], v[48:51]\n"
    "v_mfma_f32_16x16x32_bf16 v[52:55], v[212:215], v[16:19], v[52:55]\n"
    "s_waitcnt lgkmcnt(2)\n"
    "v_mfma_f32_16x16x32_bf16 v[64:67], v[216:219], v[16:19], v[64:67]\n"
    "v_mfma_f32_16x16x32_bf16 v[48:51], v[220:223], v[20:23], v[48:51]\n"
    "v_mfma_f32_16x16x32_bf16 v[52:55], v[224:227], v[20:23], v[52:55]\n"
    "s_waitcnt lgkmcnt(1)\n"
    "v_mfma_f32_16x16x32_bf16 v[64:67], v[228:231], v[20:23], v[64:67]\n"
    "v_mfma_f32_16x16x32_bf16 v[48:51], v[232:235], v[24:27], v[48:51]\n"
    "v_mfma_f32_16x16x32_bf16 v[52:55], v[236:239], v[24:27], v[52:55]\n"
    "s_waitcnt lgkmcnt(0)\n"
    "v_mfma_f32_16x16x32_bf16 v[64:67], v[240:243], v[24:27], v[64:67]\n"
    "v_mfma_f32_16x16x32_bf16 v[48:51], v[244:247], v[28:31], v[48:51]\n"
    "v_mfma_f32_16x16x32_bf16 v[52:55], v[248:251], v[28:31], v[52:55]\n"
    "v_mfma_f32_16x16x32_bf16 v[64:67], v[252:255], v[28:31], v[64:67]\n"
    "v_cvt_pk_bf16_f32 v48, v48, v49\n"
    "v_cvt_pk_bf16_f32 v49, v50, v51\n"
    "s_nop 1\n"
    "v_cvt_pk_bf16_f32 v50, v52, v53\n"
    "v_cvt_pk_bf16_f32 v51, v54, v55\n"
    "buffer_store_dwordx4 v[48:51], v15, s[36:39], s73 offen sc1\n"
    "s_nop 0\n"
    "v_cvt_pk_bf16_f32 v64, v64, v65\n"
    "v_cvt_pk_bf16_f32 v65, v66, v67\n"
    "s_nop 0\n"
    "v_permlane16_swap_b32 v64, v66\n"
    "v_permlane16_swap_b32 v65, v67\n"
    "buffer_store_dwordx4 v[64:67], v4, s[36:39], s73 offen sc1\n"
    "v_mov_b32 v12, s68\n"
    "s_mov_b32 s73, 0x80000000\n"
    "s_mov_b32 s68, s71\n"
    "s_mov_b32 s69, 0\n"
    "s_mov_b32 s70, 0x80000000\n"
    "v_mov_b32 v7, v12\n"
    "s_waitcnt vmcnt(12)\n"
    "s_add_u32 s71, s68, 1\n"
    "v_add_u32 v13, v11, v3\n"
    "v_cmp_gt_i32 vcc, s71, v13\n"
    "s_nop 0\n"
    "s_or_b64 exec, vcc, s[62:63]\n"
    "buffer_atomic_smax v11, v2, s[36:39], 0 offen sc0 sc1\n"
    "s_mov_b64 exec, -1\n"
    "s_waitcnt vmcnt(3)\n"
    "s_branch .LgWO_bar1_%=\n"
    ".LgWO_dma0_54_%=:\n"
    "v_mfma_f32_16x16x32_bf16 v[48:51], a[216:219], v[24:27], v[48:51]\n"
    "s_add_u32 m0, s59, 32768\n"
    "v_mfma_f32_16x16x32_bf16 v[52:55], a[220:223], v[24:27], v[52:55]\n"
    "s_waitcnt lgkmcnt(2)\n"
    "s_waitcnt vmcnt(1)\n"
    "buffer_atomic_smax v12, v10, s[36:39], s70 offen sc1\n"
    "v_mfma_f32_16x16x32_bf16 v[64:67], a[224:227], v[24:27], v[64:67]\n"
    "ds_read_b128 v[24:27], v0 offset:22528\n"
    "buffer_load_dwordx4 v1, s[36:39], s76 offen sc1 lds\n"
    "v_mfma_f32_16x16x32_bf16 v[48:51], a[228:231], v[28:31], v[48:51]\n"
    "s_add_u32 s76, s76, 16384\n"
    "s_add_u32 m0, s59, 36864\n"
    "v_mfma_f32_16x16x32_bf16 v[52:55], a[232:235], v[28:31], v[52:55]\n"
    "s_waitcnt lgkmcnt(2)\n"
    "buffer_load_dwordx4 v1, s[36:39], s76 offen sc1 lds\n"
    "v_mfma_f32_16x16x32_bf16 v[64:67], a[236:239], v[28:31], v[64:67]\n"
    "ds_read_b128 v[28:31], v0 offset:23552\n"
    "v_mfma_f32_16x16x32_bf16 v[48:51], a[240:243], v[16:19], v[48:51]\n"
    "s_add_u32 s76, s76, 16384\n"
    "s_add_u32 m0, s59, 40960\n"
    "v_mfma_f32_16x16x32_bf16 v[52:55], a[244:247], v[16:19], v[52:55]\n"
    "s_waitcnt lgkmcnt(2)\n"
    "buffer_load_dwordx4 v1, s[36:39], s76 offen sc1 lds\n"
    "v_mfma_f32_16x16x32_bf16 v[64:67], a[248:251], v[16:19], v[64:67]\n"
    "ds_read_b128 v[16:19], v0 offset:24576\n"
    "v_mfma_f32_16x16x32_bf16 v[48:51], a[252:255], v[20:23], v[48:51]\n"
    "v_mfma_f32_16x16x32_bf16 v[52:55], v[128:131], v[20:23], v[52:55]\n"
    "s_waitcnt lgkmcnt(2)\n"
    "s_add_u32 s76, s76, 16384\n"
    "s_add_u32 m0, s59, 45056\n"
    "v_mfma_f32_16x16x32_bf16 v[64:67], v[132:135], v[20:23], v[64:67]\n"
    "ds_read_b128 v[20:23], v0 offset:25600\n"
    "buffer_load_dwordx4 v1, s[36:39], s76 offen sc1 lds\n"
    "v_mfma_f32_16x16x32_bf16 v[48:51], v[136:139], v[24:27], v[48:51]\n"
    "v_mfma_f32_16x16x32_bf16 v[52:55], v[140:143], v[24:27], v[52:55]\n"
    "s_waitcnt lgkmcnt(2)\n"
    "v_mfma_f32_16x16x32_bf16 v[64:67], v[144:147], v[24:27], v[64:67]\n"
    "ds_read_b128 v[24:27], v0 offset:26624\n"
    "s_add_u32 s76, s76, 16384\n"
    "v_mfma_f32_16x16x32_bf16 v[48:51], v[148:151], v[28:31], v[48:51]\n"
    "s_add_u32 m0, s59, 49152\n"
    "v_mfma_f32_16x16x32_bf16 v[52:55], v[152:155], v[28:31], v[52:55]\n"
    "s_waitcnt lgkmcnt(2)\n"
    "buffer_load_dwordx4 v1, s[36:39], s76 offen sc1 lds\n"
    "v_mfma_f32_16x16x32_bf16 v[64:67], v[156:159], v[28:31], v[64:67]\n"
    "ds_read_b128 v[28:31], v0 offset:27648\n"
    "v_mfma_f32_16x16x32_bf16 v[48:51], v[160:163], v[16:19], v[48:51]\n"
    "s_add_u32 s76, s76, 16384\n"
    "s_add_u32 m0, s59, 53248\n"
    "v_mfma_f32_16x16x32_bf16 v[52:55], v[164:167], v[16:19], v[52:55]\n"
    "s_waitcnt lgkmcnt(2)\n"
    "buffer_load_dwordx4 v1, s[36:39], s76 offen sc1 lds\n"
    "v_mfma_f32_16x16x32_bf16 v[64:67], v[168:171], v[16:19], v[64:67]\n"
    "ds_read_b128 v[16:19], v0 offset:28672\n"
    "v_mfma_f32_16x16x32_bf16 v[48:51], v[172:175], v[20:23], v[48:51]\n"
    "v_mfma_f32_16x16x32_bf16 v[52:55], v[176:179], v[20:23], v[52:55]\n"
    "s_waitcnt lgkmcnt(2)\n"
    "s_add_u32 s76, s76, 16384\n"
    "s_add_u32 m0, s59, 57344\n"
    "v_mfma_f32_16x16x32_bf16 v[64:67], v[180:183], v[20:23], v[64:67]\n"
    "ds_read_b128 v[20:23], v0 offset:29696\n"
    "buffer_load_dwordx4 v1, s[36:39], s76 offen sc1 lds\n"
    "v_mfma_f32_16x16x32_bf16 v[48:51], v[184:187], v[24:27], v[48:51]\n"
    "v_mfma_f32_16x16x32_bf16 v[52:55], v[188:191], v[24:27], v[52:55]\n"
    "s_waitcnt lgkmcnt(2)\n"
    "v_mfma_f32_16x16x32_bf16 v[64:67], v[192:195], v[24:27], v[64:67]\n"
    "ds_read_b128 v[24:27], v0 offset:30720\n"
    "s_add_u32 s76, s76, 16384\n"
    "v_mfma_f32_16x16x32_bf16 v[48:51], v[196:199], v[28:31], v[48:51]\n"
    "s_add_u32 m0, s59, 61440\n"
    "v_mfma_f32_16x16x32_bf16 v[52:55], v[200:203], v[28:31], v[52:55]\n"
    "s_waitcnt lgkmcnt(2)\n"
    "buffer_load_dwordx4 v1, s[36:39], s76 offen sc1 lds\n"
    "v_mfma_f32_16x16x32_bf16 v[64:67], v[204:207], v[28:31], v[64:67]\n"
    "ds_read_b128 v[28:31], v0 offset:31744\n"
    "v_mfma_f32_16x16x32_bf16 v[48:51], v[208:211], v[16:19], v[48:51]\n"
    "v_mfma_f32_16x16x32_bf16 v[52:55], v[212:215], v[16:19], v[52:55]\n"
    "s_waitcnt lgkmcnt(2)\n"
    "v_mfma_f32_16x16x32_bf16 v[64:67], v[216:219], v[16:19], v[64:67]\n"
    "v_mfma_f32_16x16x32_bf16 v[48:51], v[220:223], v[20:23], v[48:51]\n"
    "v_mfma_f32_16x16x32_bf16 v[52:55], v[224:227], v[20:23], v[52:55]\n"
    "s_waitcnt lgkmcnt(1)\n"
    "v_mfma_f32_16x16x32_bf16 v[64:67], v[228:231], v[20:23], v[64:67]\n"
    "v_mfma_f32_16x16x32_bf16 v[48:51], v[232:235], v[24:27], v[48:51]\n"
    "v_mfma_f32_16x16x32_bf16 v[52:55], v[236:239], v[24:27], v[52:55]\n"
    "s_waitcnt lgkmcnt(0)\n"
    "v_mfma_f32_16x16x32_bf16 v[64:67], v[240:243], v[24:27], v[64:67]\n"
    "v_mfma_f32_16x16x32_bf16 v[48:51], v[244:247], v[28:31], v[48:51]\n"
    "v_mfma_f32_16x16x32_bf16 v[52:55], v[248:251], v[28:31], v[52:55]\n"
    "v_mfma_f32_16x16x32_bf16 v[64:67], v[252:255], v[28:31], v[64:67]\n"
    "v_cvt_pk_bf16_f32 v48, v48, v49\n"
    "v_cvt_pk_bf16_f32 v49, v50, v51\n"
    "s_nop 1\n"
    "v_cvt_pk_bf16_f32 v50, v52, v53\n"
    "v_cvt_pk_bf16_f32 v51, v54, v55\n"
    "buffer_store_dwordx4 v[48:51], v15, s[36:39], s73 offen sc1\n"
    "s_nop 0\n"
    "v_cvt_pk_bf16_f32 v64, v64, v65\n"
    "v_cvt_pk_bf16_f32 v65, v66, v67\n"
    "s_nop 0\n"
    "v_permlane16_swap_b32 v64, v66\n"
    "v_permlane16_swap_b32 v65, v67\n"
    "buffer_store_dwordx4 v[64:67], v4, s[36:39], s73 offen sc1\n"
    "v_mov_b32 v12, s68\n"
    "s_mov_b32 s73, 0x80000000\n"
    "s_mov_b32 s68, s71\n"
    "s_mov_b32 s69, 0\n"
    "s_mov_b32 s70, 0x80000000\n"
    "v_mov_b32 v7, v12\n"
    "s_waitcnt vmcnt(11)\n"
    "s_add_u32 s71, s68, 1\n"
    "v_add_u32 v13, v11, v3\n"
    "v_cmp_gt_i32 vcc, s71, v13\n"
    "s_nop 0\n"
    "s_or_b64 exec, vcc, s[62:63]\n"
    "buffer_atomic_smax v11, v2, s[36:39], 0 offen sc0 sc1\n"
    "s_mov_b64 exec, -1\n"
    "s_waitcnt vmcnt(3)\n"
    "s_branch .LgWO_bar1_%=\n"
    ".LgWO_dma0_75_%=:\n"
    "v_mfma_f32_16x16x32_bf16 v[48:51], v[172:175], v[20:23], v[48:51]\n"
    "s_add_u32 m0, s59, 32768\n"
    "v_mfma_f32_16x16x32_bf16 v[52:55], v[176:179], v[20:23], v[52:55]\n"
    "s_waitcnt lgkmcnt(2)\n"
    "buffer_load_dwordx4 v1, s[36:39], s76 offen sc1 lds\n"
    "v_mfma_f32_16x16x32_bf16 v[64:67], v[180:183], v[20:23], v[64:67]\n"
    "ds_read_b128 v[20:23], v0 offset:29696\n"
    "s_add_u32 s76, s76, 16384\n"
    "v_mfma_f32_16x16x32_bf16 v[48:51], v[184:187], v[24:27], v[48:51]\n"
    "s_add_u32 m0, s59, 36864\n"
    "v_mfma_f32_16x16x32_bf16 v[52:55], v[188:191], v[24:27], v[52:55]\n"
    "s_waitcnt lgkmcnt(2)\n"
    "buffer_load_dwordx4 v1, s[36:39], s76 offen sc1 lds\n"
    "v_mfma_f32_16x16x32_bf16 v[64:67], v[192:195], v[24:27], v[64:67]\n"
    "ds_read_b128 v[24:27], v0 offset:30720\n"
    "v_mfma_f32_16x16x32_bf16 v[48:51], v[196:199], v[28:31], v[48:51]\n"
    "s_add_u32 s76, s76, 16384\n"
    "s_add_u32 m0, s59, 40960\n"
    "v_mfma_f32_16x16x32_bf16 v[52:55], v[200:203], v[28:31], v[52:55]\n"
    "s_waitcnt lgkmcnt(2)\n"
    "buffer_load_dwordx4 v1, s[36:39], s76 offen sc1 lds\n"
    "v_mfma_f32_16x16x32_bf16 v[64:67], v[204:207], v[28:31], v[64:67]\n"
    "ds_read_b128 v[28:31], v0 offset:31744\n"
    "v_mfma_f32_16x16x32_bf16 v[48:51], v[208:211], v[16:19], v[48:51]\n"
    "v_mfma_f32_16x16x32_bf16 v[52:55], v[212:215], v[16:19], v[52:55]\n"
    "s_waitcnt lgkmcnt(2)\n"
    "s_add_u32 s76, s76, 16384\n"
    "s_add_u32 m0, s59, 45056\n"
    "v_mfma_f32_16x16x32_bf16 v[64:67], v[216:219], v[16:19], v[64:67]\n"
    "buffer_load_dwordx4 v1, s[36:39], s76 offen sc1 lds\n"
    "v_mfma_f32_16x16x32_bf16 v[48:51], v[220:223], v[20:23], v[48:51]\n"
    "v_mfma_f32_16x16x32_bf16 v[52:55], v[224:227], v[20:23], v[52:55]\n"
    "s_waitcnt lgkmcnt(1)\n"
    "v_mfma_f32_16x16x32_bf16 v[64:67], v[228:231], v[20:23], v[64:67]\n"
    "s_add_u32 s76, s76, 16384\n"
    "v_mfma_f32_16x16x32_bf16 v[48:51], v[232:235], v[24:27], v[48:51]\n"
    "s_add_u32 m0, s59, 49152\n"
    "v_mfma_f32_16x16x32_bf16 v[52:55], v[236:239], v[24:27], v[52:55]\n"
    "s_waitcnt lgkmcnt(0)\n"
    "buffer_load_dwordx4 v1, s[36:39], s76 offen sc1 lds\n"
    "v_mfma_f32_16x16x32_bf16 v[64:67], v[240:243], v[24:27], v[64:67]\n"
    "v_mfma_f32_16x16x32_bf16 v[48:51], v[244:247], v[28:31], v[48:51]\n"
    "s_add_u32 s76, s76, 16384\n"
    "s_add_u32 m0, s59, 53248\n"
    "v_mfma_f32_16x16x32_bf16 v[52:55], v[248:251], v[28:31], v[52:55]\n"
    "buffer_load_dwordx4 v1, s[36:39], s76 offen sc1 lds\n"
    "v_mfma_f32_16x16x32_bf16 v[64:67], v[252:255], v[28:31], v[64:67]\n"
    "v_cvt_pk_bf16_f32 v48, v48, v49\n"
    "v_cvt_pk_bf16_f32 v49, v50, v51\n"
    "s_nop 1\n"
    "v_cvt_pk_bf16_f32 v50, v52, v53\n"
    "v_cvt_pk_bf16_f32 v51, v54, v55\n"
    "buffer_store_dwordx4 v[48:51], v15, s[36:39], s73 offen sc1\n"
    "s_nop 0\n"
    "v_cvt_pk_bf16_f32 v64, v64, v65\n"
    "v_cvt_pk_bf16_f32 v65, v66, v67\n"
    "s_nop 0\n"
    "v_permlane16_swap_b32 v64, v66\n"
    "v_permlane16_swap_b32 v65, v67\n"
    "buffer_store_dwordx4 v[64:67], v4, s[36:39], s73 offen sc1\n"
    "v_mov_b32 v12, s68\n"
    "s_mov_b32 s73, 0x80000000\n"
    "s_mov_b32 s68, s71\n"
    "s_add_u32 m0, s59, 57344\n"
    "s_add_u32 s76, s76, 16384\n"
    "buffer_load_dwordx4 v1, s[36:39], s76 offen sc1 lds\n"
    "s_add_u32 m0, s59, 61440\n"
    "s_add_u32 s76, s76, 16384\n"
    "buffer_load_dwordx4 v1, s[36:39], s76 offen sc1 lds\n"
    "s_waitcnt vmcnt(2)\n"
    "buffer_atomic_smax v12, v14, s[36:39], 0 offen sc1\n"
    "s_mov_b32 s69, 0x80000000\n"
    "s_mov_b32 s70, 0x80000000\n"
    "v_mov_b32 v7, v12\n"
    "s_waitcnt vmcnt(12)\n"
    "s_add_u32 s71, s68, 1\n"
    "v_add_u32 v13, v11, v3\n"
    "v_cmp_gt_i32 vcc, s71, v13\n"
    "s_nop 0\n"
    "s_or_b64 exec, vcc, s[62:63]\n"
    "buffer_atomic_smax v11, v2, s[36:39], 0 offen sc0 sc1\n"
    "s_mov_b64 exec, -1\n"
    "s_waitcnt vmcnt(2)\n"
    "s_branch .LgWO_bar1_%=\n"
    ".LgWO_idle0_%=:\n"
    "s_or_b64 exec, vcc, s[62:63]\n"
    "buffer_atomic_smax v11, v2, s[36:39], 0 offen sc0 sc1\n"
    "s_mov_b64 exec, -1\n"
    "s_waitcnt vmcnt(1)\n"
    "buffer_atomic_smax v12, v14, s[36:39], 0 offen sc1\n"
    "s_branch .LgWO_block0_0_%=\n"
    ".LgWO_block0_%=:\n"
    "v_add_u32 v13, v11, v3\n"
    "v_cmp_gt_i32 vcc, s71, v13\n"
    "s_nop 0\n"
    "s_or_b64 exec, vcc, s[62:63]\n"
    "buffer_atomic_smax v11, v2, s[36:39], 0 offen sc0 sc1\n"
    "s_mov_b64 exec, -1\n"
    "s_sleep 3\n"
    "s_waitcnt vmcnt(1)\n"
    ".LgWO_block0_0_%=:\n"
    "v_add_u32 v13, v5, v3\n"
    "v_cmp_gt_i32 vcc, s71, v13\n"
    "s_cbranch_vccz .LgWO_go0_%=\n"
    "v_add_u32 v13, v5, v3\n"
    "v_cmp_gt_i32 vcc, s71, v13\n"
    "s_nop 0\n"
    "s_or_b64 exec, vcc, s[62:63]\n"
    "buffer_atomic_smax v5, v2, s[36:39], 0 offen sc0 sc1\n"
    "s_mov_b64 exec, -1\n"
    "s_sleep 3\n"
    "s_waitcnt vmcnt(1)\n"
    ".LgWO_block0_1_%=:\n"
    "v_add_u32 v13, v11, v3\n"
    "v_cmp_gt_i32 vcc, s71, v13\n"
    "s_cbranch_vccz .LgWO_go0_%=\n"
    "s_sub_u32 s72, s72, 1\n"
    "s_cbranch_scc0 .LgWO_block0_%=\n"
    "s_mov_b32 s72, 0\n"
    ".LgWO_go0_%=:\n"
    "s_lshr_b32 s76, s71, 1\n"
    "s_and_b32 s76, s76, s54\n"
    "s_mul_i32 s76, s76, s53\n"
    "s_add_u32 s76, s76, s52\n"
    "s_add_u32 s76, s76, 1024\n"
    "s_add_u32 m0, s59, 32768\n"
    "s_nop 0\n"
    "buffer_load_dwordx4 v1, s[36:39], s76 offen sc1 lds\n"
    "s_add_u32 m0, s59, 36864\n"
    "s_add_u32 s76, s76, 16384\n"
    "buffer_load_dwordx4 v1, s[36:39], s76 offen sc1 lds\n"
    "s_add_u32 m0, s59, 40960\n"
    "s_add_u32 s76, s76, 16384\n"
    "buffer_load_dwordx4 v1, s[36:39], s76 offen sc1 lds\n"
    "s_add_u32 m0, s59, 45056\n"
    "s_add_u32 s76, s76, 16384\n"
    "buffer_load_dwordx4 v1, s[36:39], s76 offen sc1 lds\n"
    "s_add_u32 m0, s59, 49152\n"
    "s_add_u32 s76, s76, 16384\n"
    "buffer_load_dwordx4 v1, s[36:39], s76 offen sc1 lds\n"
    "s_add_u32 m0, s59, 53248\n"
    "s_add_u32 s76, s76, 16384\n"
    "buffer_load_dwordx4 v1, s[36:39], s76 offen sc1 lds\n"
    "s_add_u32 m0, s59, 57344\n"
    "s_add_u32 s76, s76, 16384\n"
    "buffer_load_dwordx4 v1, s[36:39], s76 offen sc1 lds\n"
    "s_add_u32 m0, s59, 61440\n"
    "s_add_u32 s76, s76, 16384\n"
    "buffer_load_dwordx4 v1, s[36:39], s76 offen sc1 lds\n"
    "s_mov_b32 s69, 0x80000000\n"
    "s_mov_b32 s70, 0x80000000\n"
    "v_mov_b32 v7, v12\n"
    "s_waitcnt vmcnt(8)\n"
    "s_add_u32 s71, s68, 1\n"
    "v_add_u32 v13, v11, v3\n"
    "v_cmp_gt_i32 vcc, s71, v13\n"
    "s_nop 0\n"
    "s_or_b64 exec, vcc, s[62:63]\n"
    "buffer_atomic_smax v11, v2, s[36:39], 0 offen sc0 sc1\n"
    "s_mov_b64 exec, -1\n"
    "s_waitcnt vmcnt(1)\n"
    "s_branch .LgWO_bar1_%=\n"
    ".LgWO_bar0_%=:\n"
    "s_barrier\n"
    "ds_read_b128 v[16:19], v0 offset:0\n"
    "ds_read_b128 v[20:23], v0 offset:1024\n"
    "ds_read_b128 v[24:27], v0 offset:2048\n"
    "ds_read_b128 v[28:31], v0 offset:3072\n"
    "v_add_u32 v13, v5, v3\n"
    "v_cmp_gt_i32 vcc, s71, v13\n"
    "s_nop 0\n"
    "s_or_b64 exec, vcc, s[62:63]\n"
    "buffer_atomic_smax v5, v2, s[36:39], 0 offen sc0 sc1\n"
    "s_mov_b64 exec, -1\n"
    "s_lshr_b32 s76, s71, 1\n"
    "s_and_b32 s76, s76, s54\n"
    "s_mul_i32 s76, s76, s53\n"
    "s_add_u32 s76, s76, s52\n"
    "s_add_u32 s76, s76, 1024\n"
    "s_waitcnt lgkmcnt(3)\n"
    "s_branch .LgWO_head0_%=\n"
    ".LgWO_poll1_%=:\n"
    "v_mfma_f32_16x16x32_bf16 v[56:59], a[48:51], v[16:19], v[56:59]\n"
    "s_or_b64 exec, vcc, s[62:63]\n"
    "buffer_atomic_smax v11, v2, s[36:39], 0 offen sc0 sc1\n"
    "s_mov_b64 exec, -1\n"
    "v_mfma_f32_16x16x32_bf16 v[60:63], a[52:55], v[16:19], v[60:63]\n"
    "s_waitcnt lgkmcnt(2)\n"
    "v_mfma_f32_16x16x32_bf16 v[68:71], a[56:59], v[16:19], v[68:71]\n"
    "ds_read_b128 v[16:19], v9 offset:8192\n"
    "v_mfma_f32_16x16x32_bf16 v[56:59], a[60:63], v[20:23], v[56:59]\n"
    "buffer_store_dwordx4 v[64:67], v4, s[36:39], s73 offen sc1\n"
    "v_mfma_f32_16x16x32_bf16 v[60:63], a[64:67], v[20:23], v[60:63]\n"
    "s_waitcnt lgkmcnt(2)\n"
    "s_lshr_b32 s73, s68, 1\n"
    "v_mfma_f32_16x16x32_bf16 v[68:71], a[68:71], v[20:23], v[68:71]\n"
    "ds_read_b128 v[20:23], v9 offset:9216\n"
    "s_and_b32 s73, s73, s57\n"
    "v_mfma_f32_16x16x32_bf16 v[56:59], a[72:75], v[24:27], v[56:59]\n"
    "s_mul_i32 s73, s73, s56\n"
    "v_mfma_f32_16x16x32_bf16 v[60:63], a[76:79], v[24:27], v[60:63]\n"
    "s_waitcnt lgkmcnt(2)\n"
    "s_add_u32 s73, s73, s55\n"
    "v_mfma_f32_16x16x32_bf16 v[68:71], a[80:83], v[24:27], v[68:71]\n"
    "ds_read_b128 v[24:27], v9 offset:10240\n"
    "v_mfma_f32_16x16x32_bf16 v[56:59], a[84:87], v[28:31], v[56:59]\n"
    "v_mfma_f32_16x16x32_bf16 v[60:63], a[88:91], v[28:31], v[60:63]\n"
    "s_waitcnt lgkmcnt(2)\n"
    "v_mfma_f32_16x16x32_bf16 v[68:71], a[92:95], v[28:31], v[68:71]\n"
    "ds_read_b128 v[28:31], v9 offset:11264\n"
    "v_mfma_f32_16x16x32_bf16 v[56:59], a[96:99], v[16:19], v[56:59]\n"
    "v_mfma_f32_16x16x32_bf16 v[60:63], a[100:103], v[16:19], v[60:63]\n"
    "s_waitcnt lgkmcnt(2)\n"
    "v_mfma_f32_16x16x32_bf16 v[68:71], a[104:107], v[16:19], v[68:71]\n"
    "ds_read_b128 v[16:19], v9 offset:12288\n"
    "v_mfma_f32_16x16x32_bf16 v[56:59], a[108:111], v[20:23], v[56:59]\n"
    "v_mfma_f32_16x16x32_bf16 v[60:63], a[112:115], v[20:23], v[60:63]\n"
    "s_waitcnt lgkmcnt(2)\n"
    "s_waitcnt vmcnt(4)\n"
    "v_add_u32 v13, v5, v3\n"
    "v_mfma_f32_16x16x32_bf16 v[68:71], a[116:119], v[20:23], v[68:71]\n"
    "ds_read_b128 v[20:23], v9 offset:13312\n"
    "v_cmp_gt_i32 vcc, s71, v13\n"
    "v_mfma_f32_16x16x32_bf16 v[56:59], a[120:123], v[24:27], v[56:59]\n"
    "v_mfma_f32_16x16x32_bf16 v[60:63], a[124:127], v[24:27], v[60:63]\n"
    "s_waitcnt lgkmcnt(2)\n"
    "v_mfma_f32_16x16x32_bf16 v[68:71], a[128:131], v[24:27], v[68:71]\n"
    "ds_read_b128 v[24:27], v9 offset:14336\n"
    "s_cbranch_vccz .LgWO_dma1_33_%=\n"
    "v_mfma_f32_16x16x32_bf16 v[56:59], a[132:135], v[28:31], v[56:59]\n"
    "s_or_b64 exec, vcc, s[62:63]\n"
    "buffer_atomic_smax v5, v2, s[36:39], 0 offen sc0 sc1\n"
    "s_mov_b64 exec, -1\n"
    "v_mfma_f32_16x16x32_bf16 v[60:63], a[136:139], v[28:31], v[60:63]\n"
    "s_waitcnt lgkmcnt(2)\n"
    "v_mfma_f32_16x16x32_bf16 v[68:71], a[140:143], v[28:31], v[68:71]\n"
    "ds_read_b128 v[28:31], v9 offset:15360\n"
    "v_mfma_f32_16x16x32_bf16 v[56:59], a[144:147], v[16:19], v[56:59]\n"
    "v_mfma_f32_16x16x32_bf16 v[60:63], a[148:151], v[16:19], v[60:63]\n"
    "s_waitcnt lgkmcnt(2)\n"
    "v_mfma_f32_16x16x32_bf16 v[68:71], a[152:155], v[16:19], v[68:71]\n"
    "ds_read_b128 v[16:19], v9 offset:16384\n"
    "v_mfma_f32_16x16x32_bf16 v[56:59], a[156:159], v[20:23], v[56:59]\n"
    "v_mfma_f32_16x16x32_bf16 v[60:63], a[160:163], v[20:23], v[60:63]\n"
    "s_waitcnt lgkmcnt(2)\n"
    "v_mfma_f32_16x16x32_bf16 v[68:71], a[164:167], v[20:23], v[68:71]\n"
    "ds_read_b128 v[20:23], v9 offset:17408\n"
    "v_mfma_f32_16x16x32_bf16 v[56:59], a[168:171], v[24:27], v[56:59]\n"
    "v_mfma_f32_16x16x32_bf16 v[60:63], a[172:175], v[24:27], v[60:63]\n"
    "s_waitcnt lgkmcnt(2)\n"
    "v_mfma_f32_16x16x32_bf16 v[68:71], a[176:179], v[24:27], v[68:71]\n"
    "ds_read_b128 v[24:27], v9 offset:18432\n"
    "v_mfma_f32_16x16x32_bf16 v[56:59], a[180:183], v[28:31], v[56:59]\n"
    "v_mfma_f32_16x16x32_bf16 v[60:63], a[184:187], v[28:31], v[60:63]\n"
    "s_waitcnt lgkmcnt(2)\n"
    "v_mfma_f32_16x16x32_bf16 v[68:71], a[188:191], v[28:31], v[68:71]\n"
    "ds_read_b128 v[28:31], v9 offset:19456\n"
    "v_mfma_f32_16x16x32_bf16 v[56:59], a[192:195], v[16:19], v[56:59]\n"
    "v_mfma_f32_16x16x32_bf16 v[60:63], a[196:199], v[16:19], v[60:63]\n"
    "s_waitcnt lgkmcnt(2)\n"
    "s_waitcnt vmcnt(2)\n"
    "v_add_u32 v13, v11, v3\n"
    "v_mfma_f32_16x16x32_bf16 v[68:71], a[200:203], v[16:19], v[68:71]\n"
    "ds_read_b128 v[16:19], v9 offset:20480\n"
    "v_cmp_gt_i32 vcc, s71, v13\n"
    "v_mfma_f32_16x16x32_bf16 v[56:59], a[204:207], v[20:23], v[56:59]\n"
    "v_mfma_f32_16x16x32_bf16 v[60:63], a[208:211], v[20:23], v[60:63]\n"
    "s_waitcnt lgkmcnt(2)\n"
    "v_mfma_f32_16x16x32_bf16 v[68:71], a[212:215], v[20:23], v[68:71]\n"
    "ds_read_b128 v[20:23], v9 offset:21504\n"
    "s_cbranch_vccz .LgWO_dma1_54_%=\n"
    "v_mfma_f32_16x16x32_bf16 v[56:59], a[216:219], v[24:27], v[56:59]\n"
    "s_or_b64 exec, vcc, s[62:63]\n"
    "buffer_atomic_smax v11, v2, s[36:39], 0 offen sc0 sc1\n"
    "s_mov_b64 exec, -1\n"
    "v_mfma_f32_16x16x32_bf16 v[60:63], a[220:223], v[24:27], v[60:63]\n"
    "s_waitcnt lgkmcnt(2)\n"
    "s_waitcnt vmcnt(2)\n"
    "buffer_atomic_smax v12, v14, s[36:39], s70 offen sc1\n"
    "v_mfma_f32_16x16x32_bf16 v[68:71], a[224:227], v[24:27], v[68:71]\n"
    "ds_read_b128 v[24:27], v9 offset:22528\n"
    "v_mfma_f32_16x16x32_bf16 v[56:59], a[228:231], v[28:31], v[56:59]\n"
    "v_mfma_f32_16x16x32_bf16 v[60:63], a[232:235], v[28:31], v[60:63]\n"
    "s_waitcnt lgkmcnt(2)\n"
    "v_mfma_f32_16x16x32_bf16 v[68:71], a[236:239], v[28:31], v[68:71]\n"
    "ds_read_b128 v[28:31], v9 offset:23552\n"
    "v_mfma_f32_16x16x32_bf16 v[56:59], a[240:243], v[16:19], v[56:59]\n"
    "v_mfma_f32_16x16x32_bf16 v[60:63], a[244:247], v[16:19], v[60:63]\n"
    "s_waitcnt lgkmcnt(2)\n"
    "v_mfma_f32_16x16x32_bf16 v[68:71], a[248:251], v[16:19], v[68:71]\n"
    "ds_read_b128 v[16:19], v9 offset:24576\n"
    "v_mfma_f32_16x16x32_bf16 v[56:59], a[252:255], v[20:23], v[56:59]\n"
    "v_mfma_f32_16x16x32_bf16 v[60:63], v[128:131], v[20:23], v[60:63]\n"
    "s_waitcnt lgkmcnt(2)\n"
    "v_mfma_f32_16x16x32_bf16 v[68:71], v[132:135], v[20:23], v[68:71]\n"
    "ds_read_b128 v[20:23], v9 offset:25600\n"
    "v_mfma_f32_16x16x32_bf16 v[56:59], v[136:139], v[24:27], v[56:59]\n"
    "v_mfma_f32_16x16x32_bf16 v[60:63], v[140:143], v[24:27], v[60:63]\n"
    "s_waitcnt lgkmcnt(2)\n"
    "v_mfma_f32_16x16x32_bf16 v[68:71], v[144:147], v[24:27], v[68:71]\n"
    "ds_read_b128 v[24:27], v9 offset:26624\n"
    "v_mfma_f32_16x16x32_bf16 v[56:59], v[148:151], v[28:31], v[56:59]\n"
    "v_mfma_f32_16x16x32_bf16 v[60:63], v[152:155], v[28:31], v[60:63]\n"
    "s_waitcnt lgkmcnt(2)\n"
    "s_waitcnt vmcnt(2)\n"
    "v_add_u32 v13, v5, v3\n"
    "v_mfma_f32_16x16x32_bf16 v[68:71], v[156:159], v[28:31], v[68:71]\n"
    "ds_read_b128 v[28:31], v9 offset:27648\n"
    "v_cmp_gt_i32 vcc, s71, v13\n"
    "v_mfma_f32_16x16x32_bf16 v[56:59], v[160:163], v[16:19], v[56:59]\n"
    "v_mfma_f32_16x16x32_bf16 v[60:63], v[164:167], v[16:19], v[60:63]\n"
    "s_waitcnt lgkmcnt(2)\n"
    "v_mfma_f32_16x16x32_bf16 v[68:71], v[168:171], v[16:19], v[68:71]\n"
    "ds_read_b128 v[16:19], v9 offset:28672\n"
    "s_cbranch_vccz .LgWO_dma1_75_%=\n"
    "v_mfma_f32_16x16x32_bf16 v[56:59], v[172:175], v[20:23], v[56:59]\n"
    "s_or_b64 exec, vcc, s[62:63]\n"
    "buffer_atomic_smax v5, v2, s[36:39], 0 offen sc0 sc1\n"
    "s_mov_b64 exec, -1\n"
    "v_mfma_f32_16x16x32_bf16 v[60:63], v[176:179], v[20:23], v[60:63]\n"
    "s_waitcnt lgkmcnt(2)\n"
    "v_mfma_f32_16x16x32_bf16 v[68:71], v[180:183], v[20:23], v[68:71]\n"
    "ds_read_b128 v[20:23], v9 offset:29696\n"
    "v_mfma_f32_16x16x32_bf16 v[56:59], v[184:187], v[24:27], v[56:59]\n"
    "v_mfma_f32_16x16x32_bf16 v[60:63], v[188:191], v[24:27], v[60:63]\n"
    "s_waitcnt lgkmcnt(2)\n"
    "v_mfma_f32_16x16x32_bf16 v[68:71], v[192:195], v[24:27], v[68:71]\n"
    "ds_read_b128 v[24:27], v9 offset:30720\n"
    "v_mfma_f32_16x16x32_bf16 v[56:59], v[196:199], v[28:31], v[56:59]\n"
    "v_mfma_f32_16x16x32_bf16 v[60:63], v[200:203], v[28:31], v[60:63]\n"
    "s_waitcnt lgkmcnt(2)\n"
    "v_mfma_f32_16x16x32_bf16 v[68:71], v[204:207], v[28:31], v[68:71]\n"
    "ds_read_b128 v[28:31], v9 offset:31744\n"
    "v_mfma_f32_16x16x32_bf16 v[56:59], v[208:211], v[16:19], v[56:59]\n"
    "v_mfma_f32_16x16x32_bf16 v[60:63], v[212:215], v[16:19], v[60:63]\n"
    "s_waitcnt lgkmcnt(2)\n"
    "v_mfma_f32_16x16x32_bf16 v[68:71], v[216:219], v[16:19], v[68:71]\n"
    "v_mfma_f32_16x16x32_bf16 v[56:59], v[220:223], v[20:23], v[56:59]\n"
    "v_mfma_f32_16x16x32_bf16 v[60:63], v[224:227], v[20:23], v[60:63]\n"
    "s_waitcnt lgkmcnt(1)\n"
    "v_mfma_f32_16x16x32_bf16 v[68:71], v[228:231], v[20:23], v[68:71]\n"
    "v_mfma_f32_16x16x32_bf16 v[56:59], v[232:235], v[24:27], v[56:59]\n"
    "v_mfma_f32_16x16x32_bf16 v[60:63], v[236:239], v[24:27], v[60:63]\n"
    "s_waitcnt lgkmcnt(0)\n"
    "v_mfma_f32_16x16x32_bf16 v[68:71], v[240:243], v[24:27], v[68:71]\n"
    "v_mfma_f32_16x16x32_bf16 v[56:59], v[244:247], v[28:31], v[56:59]\n"
    "v_mfma_f32_16x16x32_bf16 v[60:63], v[248:251], v[28:31], v[60:63]\n"
    "v_mfma_f32_16x16x32_bf16 v[68:71], v[252:255], v[28:31], v[68:71]\n"
    "v_cvt_pk_bf16_f32 v56, v56, v57\n"
    "v_cvt_pk_bf16_f32 v57, v58, v59\n"
    "s_nop 1\n"
    "v_cvt_pk_bf16_f32 v58, v60, v61\n"
    "v_cvt_pk_bf16_f32 v59, v62, v63\n"
    "buffer_store_dwordx4 v[56:59], v15, s[36:39], s73 offen offset:1024 sc1\n"
    "s_nop 0\n"
    "v_cvt_pk_bf16_f32 v68, v68, v69\n"
    "v_cvt_pk_bf16_f32 v69, v70, v71\n"
    "s_nop 0\n"
    "v_permlane16_swap_b32 v68, v70\n"
    "v_permlane16_swap_b32 v69, v71\n"
    "buffer_store_dwordx4 v[68:71], v4, s[36:39], s73 offen offset:1024 sc1\n"
    "v_mov_b32 v12, s68\n"
    "s_mov_b32 s73, 0x80000000\n"
    "s_mov_b32 s68, s71\n"
    "s_cmp_eq_u32 s68, 128\n"
    "s_cbranch_scc1 .LgWO_done_%=\n"
    "s_waitcnt vmcnt(4)\n"
    "v_add_u32 v13, v11, v3\n"
    "v_cmp_gt_i32 vcc, s71, v13\n"
    "s_cbranch_vccnz .LgWO_idle1_%=\n"
    "s_add_u32 m0, s59, 0\n"
    "s_nop 0\n"
    "buffer_load_dwordx4 v1, s[36:39], s76 offen sc1 lds\n"
    "s_add_u32 m0, s59, 4096\n"
    "s_add_u32 s76, s76, 16384\n"
    "buffer_load_dwordx4 v1, s[36:39], s76 offen sc1 lds\n"
    "s_add_u32 m0, s59, 8192\n"
    "s_add_u32 s76, s76, 16384\n"
    "buffer_load_dwordx4 v1, s[36:39], s76 offen sc1 lds\n"
    "s_add_u32 m0, s59, 12288\n"
    "s_add_u32 s76, s76, 16384\n"
    "buffer_load_dwordx4 v1, s[36:39], s76 offen sc1 lds\n"
    "s_add_u32 m0, s59, 16384\n"
    "s_add_u32 s76, s76, 16384\n"
    "buffer_load_dwordx4 v1, s[36:39], s76 offen sc1 lds\n"
    "s_add_u32 m0, s59, 20480\n"
    "s_add_u32 s76, s76, 16384\n"
    "buffer_load_dwordx4 v1, s[36:39], s76 offen sc1 lds\n"
    "s_add_u32 m0, s59, 24576\n"
    "s_add_u32 s76, s76, 16384\n"
    "buffer_load_dwordx4 v1, s[36:39], s76 offen sc1 lds\n"
    "s_add_u32 m0, s59, 28672\n"
    "s_add_u32 s76, s76, 16384\n"
    "buffer_load_dwordx4 v1, s[36:39], s76 offen sc1 lds\n"
    "s_waitcnt vmcnt(8)\n"
    "buffer_atomic_smax v12, v10, s[36:39], 0 offen sc1\n"
    "s_mov_b32 s69, 0x80000000\n"
    "s_mov_b32 s70, 0x80000000\n"
    "v_mov_b32 v7, v12\n"
    "s_waitcnt vmcnt(11)\n"
    "s_add_u32 s71, s68, 1\n"
    "v_add_u32 v13, v11, v3\n"
    "v_cmp_gt_i32 vcc, s71, v13\n"
    "s_nop 0\n"
    "s_or_b64 exec, vcc, s[62:63]\n"
    "buffer_atomic_smax v11, v2, s[36:39], 0 offen sc0 sc1\n"
    "s_mov_b64 exec, -1\n"
    "s_waitcnt vmcnt(2)\n"
    "s_branch .LgWO_bar0_%=\n"
    ".LgWO_dma1_33_%=:\n"
    "v_mfma_f32_16x16x32_bf16 v[56:59], a[132:135], v[28:31], v[56:59]\n"
    "s_add_u32 m0, s59, 0\n"
    "v_mfma_f32_16x16x32_bf16 v[60:63], a[136:139], v[28:31], v[60:63]\n"
    "s_waitcnt lgkmcnt(2)\n"
    "buffer_load_dwordx4 v1, s[36:39], s76 offen sc1 lds\n"
    "v_mfma_f32_16x16x32_bf16 v[68:71], a[140:143], v[28:31], v[68:71]\n"
    "ds_read_b128 v[28:31], v9 offset:15360\n"
    "s_add_u32 s76, s76, 16384\n"
    "v_mfma_f32_16x16x32_bf16 v[56:59], a[144:147], v[16:19], v[56:59]\n"
    "s_add_u32 m0, s59, 4096\n"
    "v_mfma_f32_16x16x32_bf16 v[60:63], a[148:151], v[16:19], v[60:63]\n"
    "s_waitcnt lgkmcnt(2)\n"
    "buffer_load_dwordx4 v1, s[36:39], s76 offen sc1 lds\n"
    "v_mfma_f32_16x16x32_bf16 v[68:71], a[152:155], v[16:19], v[68:71]\n"
    "ds_read_b128 v[16:19], v9 offset:16384\n"
    "v_mfma_f32_16x16x32_bf16 v[56:59], a[156:159], v[20:23], v[56:59]\n"
    "s_add_u32 s76, s76, 16384\n"
    "s_add_u32 m0, s59, 8192\n"
    "v_mfma_f32_16x16x32_bf16 v[60:63], a[160:163], v[20:23], v[60:63]\n"
    "s_waitcnt lgkmcnt(2)\n"
    "buffer_load_dwordx4 v1, s[36:39], s76 offen sc1 lds\n"
    "v_mfma_f32_16x16x32_bf16 v[68:71], a[164:167], v[20:23], v[68:71]\n"
    "ds_read_b128 v[20:23], v9 offset:17408\n"
    "v_mfma_f32_16x16x32_bf16 v[56:59], a[168:171], v[24:27], v[56:59]\n"
    "v_mfma_f32_16x16x32_bf16 v[60:63], a[172:175], v[24:27], v[60:63]\n"
    "s_waitcnt lgkmcnt(2)\n"
    "s_add_u32 s76, s76, 16384\n"
    "s_add_u32 m0, s59, 12288\n"
    "v_mfma_f32_16x16x32_bf16 v[68:71], a[176:179], v[24:27], v[68:71]\n"
    "ds_read_b128 v[24:27], v9 offset:18432\n"
    "buffer_load_dwordx4 v1, s[36:39], s76 offen sc1 lds\n"
    "v_mfma_f32_16x16x32_bf16 v[56:59], a[180:183], v[28:31], v[56:59]\n"
    "v_mfma_f32_16x16x32_bf16 v[60:63], a[184:187], v[28:31], v[60:63]\n"
    "s_waitcnt lgkmcnt(2)\n"
    "v_mfma_f32_16x16x32_bf16 v[68:71], a[188:191], v[28:31], v[68:71]\n"
    "ds_read_b128 v[28:31], v9 offset:19456\n"
    "s_add_u32 s76, s76, 16384\n"
    "v_mfma_f32_16x16x32_bf16 v[56:59], a[192:195], v[16:19], v[56:59]\n"
    "s_add_u32 m0, s59, 16384\n"
    "v_mfma_f32_16x16x32_bf16 v[60:63], a[196:199], v[16:19], v[60:63]\n"
    "s_waitcnt lgkmcnt(2)\n"
    "buffer_load_dwordx4 v1, s[36:39], s76 offen sc1 lds\n"
    "v_mfma_f32_16x16x32_bf16 v[68:71], a[200:203], v[16:19], v[68:71]\n"
    "ds_read_b128 v[16:19], v9 offset:20480\n"
    "v_mfma_f32_16x16x32_bf16 v[56:59], a[204:207], v[20:23], v[56:59]\n"
    "s_add_u32 s76, s76, 16384\n"
    "s_add_u32 m0, s59, 20480\n"
    "v_mfma_f32_16x16x32_bf16 v[60:63], a[208:211], v[20:23], v[60:63]\n"
    "s_waitcnt lgkmcnt(2)\n"
    "buffer_load_dwordx4 v1, s[36:39], s76 offen sc1 lds\n"
    "v_mfma_f32_16x16x32_bf16 v[68:71], a[212:215], v[20:23], v[68:71]\n"
    "ds_read_b128 v[20:23], v9 offset:21504\n"
    "v_mfma_f32_16x16x32_bf16 v[56:59], a[216:219], v[24:27], v[56:59]\n"
    "v_mfma_f32_16x16x32_bf16 v[60:63], a[220:223], v[24:27], v[60:63]\n"
    "s_waitcnt lgkmcnt(2)\n"
    "s_waitcnt vmcnt(6)\n"
    "buffer_atomic_smax v12, v14, s[36:39], s70 offen sc1\n"
    "v_mfma_f32_16x16x32_bf16 v[68:71], a[224:227], v[24:27], v[68:71]\n"
    "ds_read_b128 v[24:27], v9 offset:22528\n"
    "s_add_u32 s76, s76, 16384\n"
    "v_mfma_f32_16x16x32_bf16 v[56:59], a[228:231], v[28:31], v[56:59]\n"
    "s_add_u32 m0, s59, 24576\n"
    "v_mfma_f32_16x16x32_bf16 v[60:63], a[232:235], v[28:31], v[60:63]\n"
    "s_waitcnt lgkmcnt(2)\n"
    "buffer_load_dwordx4 v1, s[36:39], s76 offen sc1 lds\n"
    "v_mfma_f32_16x16x32_bf16 v[68:71], a[236:239], v[28:31], v[68:71]\n"
    "ds_read_b128 v[28:31], v9 offset:23552\n"
    "s_add_u32 s76, s76, 16384\n"
    "v_mfma_f32_16x16x32_bf16 v[56:59], a[240:243], v[16:19], v[56:59]\n"
    "s_add_u32 m0, s59, 28672\n"
    "v_mfma_f32_16x16x32_bf16 v[60:63], a[244:247], v[16:19], v[60:63]\n"
    "s_waitcnt lgkmcnt(2)\n"
    "buffer_load_dwordx4 v1, s[36:39], s76 offen sc1 lds\n"
    "v_mfma_f32_16x16x32_bf16 v[68:71], a[248:251], v[16:19], v[68:71]\n"
    "ds_read_b128 v[16:19], v9 offset:24576\n"
    "v_mfma_f32_16x16x32_bf16 v[56:59], a[252:255], v[20:23], v[56:59]\n"
    "v_mfma_f32_16x16x32_bf16 v[60:63], v[128:131], v[20:23], v[60:63]\n"
    "s_waitcnt lgkmcnt(2)\n"
    "v_mfma_f32_16x16x32_bf16 v[68:71], v[132:135], v[20:23], v[68:71]\n"
    "ds_read_b128 v[20:23], v9 offset:25600\n"
    "v_mfma_f32_16x16x32_bf16 v[56:59], v[136:139], v[24:27], v[56:59]\n"
    "v_mfma_f32_16x16x32_bf16 v[60:63], v[140:143], v[24:27], v[60:63]\n"
    "s_waitcnt lgkmcnt(2)\n"
    "v_mfma_f32_16x16x32_bf16 v[68:71], v[144:147], v[24:27], v[68:71]\n"
    "ds_read_b128 v[24:27], v9 offset:26624\n"
    "v_mfma_f32_16x16x32_bf16 v[56:59], v[148:151], v[28:31], v[56:59]\n"
    "v_mfma_f32_16x16x32_bf16 v[60:63], v[152:155], v[28:31], v[60:63]\n"
    "s_waitcnt lgkmcnt(2)\n"
    "v_mfma_f32_16x16x32_bf16 v[68:71], v[156:159], v[28:31], v[68:71]\n"
    "ds_read_b128 v[28:31], v9 offset:27648\n"
    "v_mfma_f32_16x16x32_bf16 v[56:59], v[160:163], v[16:19], v[56:59]\n"
    "v_mfma_f32_16x16x32_bf16 v[60:63], v[164:167], v[16:19], v[60:63]\n"
    "s_waitcnt lgkmcnt(2)\n"
    "v_mfma_f32_16x16x32_bf16 v[68:71], v[168:171], v[16:19], v[68:71]\n"
    "ds_read_b128 v[16:19], v9 offset:28672\n"
    "v_mfma_f32_16x16x32_bf16 v[56:59], v[172:175], v[20:23], v[56:59]\n"
    "v_mfma_f32_16x16x32_bf16 v[60:63], v[176:179], v[20:23], v[60:63]\n"
    "s_waitcnt lgkmcnt(2)\n"
    "v_mfma_f32_16x16x32_bf16 v[68:71], v[180:183], v[20:23], v[68:71]\n"
    "ds_read_b128 v[20:23], v9 offset:29696\n"
    "v_mfma_f32_16x16x32_bf16 v[56:59], v[184:187], v[24:27], v[56:59]\n"
    "v_mfma_f32_16x16x32_bf16 v[60:63], v[188:191], v[24:27], v[60:63]\n"
    "s_waitcnt lgkmcnt(2)\n"
    "v_mfma_f32_16x16x32_bf16 v[68:71], v[192:195], v[24:27], v[68:71]\n"
    "ds_read_b128 v[24:27], v9 offset:30720\n"
    "v_mfma_f32_16x16x32_bf16 v[56:59], v[196:199], v[28:31], v[56:59]\n"
    "v_mfma_f32_16x16x32_bf16 v[60:63], v[200:203], v[28:31], v[60:63]\n"
    "s_waitcnt lgkmcnt(2)\n"
    "v_mfma_f32_16x16x32_bf16 v[68:71], v[204:207], v[28:31], v[68:71]\n"
    "ds_read_b128 v[28:31], v9 offset:31744\n"
    "v_mfma_f32_16x16x32_bf16 v[56:59], v[208:211], v[16:19], v[56:59]\n"
    "v_mfma_f32_16x16x32_bf16 v[60:63], v[212:215], v[16:19], v[60:63]\n"
    "s_waitcnt lgkmcnt(2)\n"
    "v_mfma_f32_16x16x32_bf16 v[68:71], v[216:219], v[16:19], v[68:71]\n"
    "v_mfma_f32_16x16x32_bf16 v[56:59], v[220:223], v[20:23], v[56:59]\n"
    "v_mfma_f32_16x16x32_bf16 v[60:63], v[224:227], v[20:23], v[60:63]\n"
    "s_waitcnt lgkmcnt(1)\n"
    "v_mfma_f32_16x16x32_bf16 v[68:71], v[228:231], v[20:23], v[68:71]\n"
    "v_mfma_f32_16x16x32_bf16 v[56:59], v[232:235], v[24:27], v[56:59]\n"
    "v_mfma_f32_16x16x32_bf16 v[60:63], v[236:239], v[24:27], v[60:63]\n"
    "s_waitcnt lgkmcnt(0)\n"
    "v_mfma_f32_16x16x32_bf16 v[68:71], v[240:243], v[24:27], v[68:71]\n"
    "v_mfma_f32_16x16x32_bf16 v[56:59], v[244:247], v[28:31], v[56:59]\n"
    "v_mfma_f32_16x16x32_bf16 v[60:63], v[248:251], v[28:31], v[60:63]\n"
    "v_mfma_f32_16x16x32_bf16 v[68:71], v[252:255], v[28:31], v[68:71]\n"
    "v_cvt_pk_bf16_f32 v56, v56, v57\n"
    "v_cvt_pk_bf16_f32 v57, v58, v59\n"
    "s_nop 1\n"
    "v_cvt_pk_bf16_f32 v58, v60, v61\n"
    "v_cvt_pk_bf16_f32 v59, v62, v63\n"
    "buffer_store_dwordx4 v[56:59], v15, s[36:39], s73 offen offset:1024 sc1\n"
    "s_nop 0\n"
    "v_cvt_pk_bf16_f32 v68, v68, v69\n"
    "v_cvt_pk_bf16_f32 v69, v70, v71\n"
    "s_nop 0\n"
    "v_permlane16_swap_b32 v68, v70\n"
    "v_permlane16_swap_b32 v69, v71\n"
    "buffer_store_dwordx4 v[68:71], v4, s[36:39], s73 offen offset:1024 sc1\n"
    "v_mov_b32 v12, s68\n"
    "s_mov_b32 s73, 0x80000000\n"
    "s_mov_b32 s68, s71\n"
    "s_mov_b32 s69, 0\n"
    "s_mov_b32 s70, 0x80000000\n"
    "v_mov_b32 v7, v12\n"
    "s_waitcnt vmcnt(12)\n"
    "s_add_u32 s71, s68, 1\n"
    "v_add_u32 v13, v11, v3\n"
    "v_cmp_gt_i32 vcc, s71, v13\n"
    "s_nop 0\n"
    "s_or_b64 exec, vcc, s[62:63]\n"
    "buffer_atomic_smax v11, v2, s[36:39], 0 offen sc0 sc1\n"
    "s_mov_b64 exec, -1\n"
    "s_waitcnt vmcnt(3)\n"
    "s_branch .LgWO_bar0_%=\n"
    ".LgWO_dma1_54_%=:\n"
    "v_mfma_f32_16x16x32_bf16 v[56:59], a[216:219], v[24:27], v[56:59]\n"
    "s_add_u32 m0, s59, 0\n"
    "v_mfma_f32_16x16x32_bf16 v[60:63], a[220:223], v[24:27], v[60:63]\n"
    "s_waitcnt lgkmcnt(2)\n"
    "s_waitcnt vmcnt(1)\n"
    "buffer_atomic_smax v12, v14, s[36:39], s70 offen sc1\n"
    "v_mfma_f32_16x16x32_bf16 v[68:71], a[224:227], v[24:27], v[68:71]\n"
    "ds_read_b128 v[24:27], v9 offset:22528\n"
    "buffer_load_dwordx4 v1, s[36:39], s76 offen sc1 lds\n"
    "v_mfma_f32_16x16x32_bf16 v[56:59], a[228:231], v[28:31], v[56:59]\n"
    "s_add_u32 s76, s76, 16384\n"
    "s_add_u32 m0, s59, 4096\n"
    "v_mfma_f32_16x16x32_bf16 v[60:63], a[232:235], v[28:31], v[60:63]\n"
    "s_waitcnt lgkmcnt(2)\n"
    "buffer_load_dwordx4 v1, s[36:39], s76 offen sc1 lds\n"
    "v_mfma_f32_16x16x32_bf16 v[68:71], a[236:239], v[28:31], v[68:71]\n"
    "ds_read_b128 v[28:31], v9 offset:23552\n"
    "v_mfma_f32_16x16x32_bf16 v[56:59], a[240:243], v[16:19], v[56:59]\n"
    "s_add_u32 s76, s76, 16384\n"
    "s_add_u32 m0, s59, 8192\n"
    "v_mfma_f32_16x16x32_bf16 v[60:63], a[244:247], v[16:19], v[60:63]\n"
    "s_waitcnt lgkmcnt(2)\n"
    "buffer_load_dwordx4 v1, s[36:39], s76 offen sc1 lds\n"
    "v_mfma_f32_16x16x32_bf16 v[68:71], a[248:251], v[16:19], v[68:71]\n"
    "ds_read_b128 v[16:19], v9 offset:24576\n"
    "v_mfma_f32_16x16x32_bf16 v[56:59], a[252:255], v[20:23], v[56:59]\n"
    "v_mfma_f32_16x16x32_bf16 v[60:63], v[128:131], v[20:23], v[60:63]\n"
    "s_waitcnt lgkmcnt(2)\n"
    "s_add_u32 s76, s76, 16384\n"
    "s_add_u32 m0, s59, 12288\n"
    "v_mfma_f32_16x16x32_bf16 v[68:71], v[132:135], v[20:23], v[68:71]\n"
    "ds_read_b128 v[20:23], v9 offset:25600\n"
    "buffer_load_dwordx4 v1, s[36:39], s76 offen sc1 lds\n"
    "v_mfma_f32_16x16x32_bf16 v[56:59], v[136:139], v[24:27], v[56:59]\n"
    "v_mfma_f32_16x16x32_bf16 v[60:63], v[140:143], v[24:27], v[60:63]\n"
    "s_waitcnt lgkmcnt(2)\n"
    "v_mfma_f32_16x16x32_bf16 v[68:71], v[144:147], v[24:27], v[68:71]\n"
    "ds_read_b128 v[24:27], v9 offset:26624\n"
    "s_add_u32 s76, s76, 16384\n"
    "v_mfma_f32_16x16x32_bf16 v[56:59], v[148:151], v[28:31], v[56:59]\n"
    "s_add_u32 m0, s59, 16384\n"
    "v_mfma_f32_16x16x32_bf16 v[60:63], v[152:155], v[28:31], v[60:63]\n"
    "s_waitcnt lgkmcnt(2)\n"
    "buffer_load_dwordx4 v1, s[36:39], s76 offen sc1 lds\n"
    "v_mfma_f32_16x16x32_bf16 v[68:71], v[156:159], v[28:31], v[68:71]\n"
    "ds_read_b128 v[28:31], v9 offset:27648\n"
    "v_mfma_f32_16x16x32_bf16 v[56:59], v[160:163], v[16:19], v[56:59]\n"
    "s_add_u32 s76, s76, 16384\n"
    "s_add_u32 m0, s59, 20480\n"
    "v_mfma_f32_16x16x32_bf16 v[60:63], v[164:167], v[16:19], v[60:63]\n"
    "s_waitcnt lgkmcnt(2)\n"
    "buffer_load_dwordx4 v1, s[36:39], s76 offen sc1 lds\n"
    "v_mfma_f32_16x16x32_bf16 v[68:71], v[168:171], v[16:19], v[68:71]\n"
    "ds_read_b128 v[16:19], v9 offset:28672\n"
    "v_mfma_f32_16x16x32_bf16 v[56:59], v[172:175], v[20:23], v[56:59]\n"
    "v_mfma_f32_16x16x32_bf16 v[60:63], v[176:179], v[20:23], v[60:63]\n"
    "s_waitcnt lgkmcnt(2)\n"
    "s_add_u32 s76, s76, 16384\n"
    "s_add_u32 m0, s59, 24576\n"
    "v_mfma_f32_16x16x32_bf16 v[68:71], v[180:183], v[20:23], v[68:71]\n"
    "ds_read_b128 v[20:23], v9 offset:29696\n"
    "buffer_load_dwordx4 v1, s[36:39], s76 offen sc1 lds\n"
    "v_mfma_f32_16x16x32_bf16 v[56:59], v[184:187], v[24:27], v[56:59]\n"
    "v_mfma_f32_16x16x32_bf16 v[60:63], v[188:191], v[24:27], v[60:63]\n"
    "s_waitcnt lgkmcnt(2)\n"
    "v_mfma_f32_16x16x32_bf16 v[68:71], v[192:195], v[24:27], v[68:71]\n"
    "ds_read_b128 v[24:27], v9 offset:30720\n"
    "s_add_u32 s76, s76, 16384\n"
    "v_mfma_f32_16x16x32_bf16 v[56:59], v[196:199], v[28:31], v[56:59]\n"
    "s_add_u32 m0, s59, 28672\n"
    "v_mfma_f32_16x16x32_bf16 v[60:63], v[200:203], v[28:31], v[60:63]\n"
    "s_waitcnt lgkmcnt(2)\n"
    "buffer_load_dwordx4 v1, s[36:39], s76 offen sc1 lds\n"
    "v_mfma_f32_16x16x32_bf16 v[68:71], v[204:207], v[28:31], v[68:71]\n"
    "ds_read_b128 v[28:31], v9 offset:31744\n"
    "v_mfma_f32_16x16x32_bf16 v[56:59], v[208:211], v[16:19], v[56:59]\n"
    "v_mfma_f32_16x16x32_bf16 v[60:63], v[212:215], v[16:19], v[60:63]\n"
    "s_waitcnt lgkmcnt(2)\n"
    "v_mfma_f32_16x16x32_bf16 v[68:71], v[216:219], v[16:19], v[68:71]\n"
    "v_mfma_f32_16x16x32_bf16 v[56:59], v[220:223], v[20:23], v[56:59]\n"
    "v_mfma_f32_16x16x32_bf16 v[60:63], v[224:227], v[20:23], v[60:63]\n"
    "s_waitcnt lgkmcnt(1)\n"
    "v_mfma_f32_16x16x32_bf16 v[68:71], v[228:231], v[20:23], v[68:71]\n"
    "v_mfma_f32_16x16x32_bf16 v[56:59], v[232:235], v[24:27], v[56:59]\n"
    "v_mfma_f32_16x16x32_bf16 v[60:63], v[236:239], v[24:27], v[60:63]\n"
    "s_waitcnt lgkmcnt(0)\n"
    "v_mfma_f32_16x16x32_bf16 v[68:71], v[240:243], v[24:27], v[68:71]\n"
    "v_mfma_f32_16x16x32_bf16 v[56:59], v[244:247], v[28:31], v[56:59]\n"
    "v_mfma_f32_16x16x32_bf16 v[60:63], v[248:251], v[28:31], v[60:63]\n"
    "v_mfma_f32_16x16x32_bf16 v[68:71], v[252:255], v[28:31], v[68:71]\n"
    "v_cvt_pk_bf16_f32 v56, v56, v57\n"
    "v_cvt_pk_bf16_f32 v57, v58, v59\n"
    "s_nop 1\n"
    "v_cvt_pk_bf16_f32 v58, v60, v61\n"
    "v_cvt_pk_bf16_f32 v59, v62, v63\n"
    "buffer_store_dwordx4 v[56:59], v15, s[36:39], s73 offen offset:1024 sc1\n"
    "s_nop 0\n"
    "v_cvt_pk_bf16_f32 v68, v68, v69\n"
    "v_cvt_pk_bf16_f32 v69, v70, v71\n"
    "s_nop 0\n"
    "v_permlane16_swap_b32 v68, v70\n"
    "v_permlane16_swap_b32 v69, v71\n"
    "buffer_store_dwordx4 v[68:71], v4, s[36:39], s73 offen offset:1024 sc1\n"
    "v_mov_b32 v12, s68\n"
    "s_mov_b32 s73, 0x80000000\n"
    "s_mov_b32 s68, s71\n"
    "s_mov_b32 s69, 0\n"
    "s_mov_b32 s70, 0x80000000\n"
    "v_mov_b32 v7, v12\n"
    "s_waitcnt vmcnt(11)\n"
    "s_add_u32 s71, s68, 1\n"
    "v_add_u32 v13, v11, v3\n"
    "v_cmp_gt_i32 vcc, s71, v13\n"
    "s_nop 0\n"
    "s_or_b64 exec, vcc, s[62:63]\n"
    "buffer_atomic_smax v11, v2, s[36:39], 0 offen sc0 sc1\n"
    "s_mov_b64 exec, -1\n"
    "s_waitcnt vmcnt(3)\n"
    "s_branch .LgWO_bar0_%=\n"
    ".LgWO_dma1_75_%=:\n"
    "v_mfma_f32_16x16x32_bf16 v[56:59], v[172:175], v[20:23], v[56:59]\n"
    "s_add_u32 m0, s59, 0\n"
    "v_mfma_f32_16x16x32_bf16 v[60:63], v[176:179], v[20:23], v[60:63]\n"
    "s_waitcnt lgkmcnt(2)\n"
    "buffer_load_dwordx4 v1, s[36:39], s76 offen sc1 lds\n"
    "v_mfma_f32_16x16x32_bf16 v[68:71], v[180:183], v[20:23], v[68:71]\n"
    "ds_read_b128 v[20:23], v9 offset:29696\n"
    "s_add_u32 s76, s76, 16384\n"
    "v_mfma_f32_16x16x32_bf16 v[56:59], v[184:187], v[24:27], v[56:59]\n"
    "s_add_u32 m0, s59, 4096\n"
    "v_mfma_f32_16x16x32_bf16 v[60:63], v[188:191], v[24:27], v[60:63]\n"
    "s_waitcnt lgkmcnt(2)\n"
    "buffer_load_dwordx4 v1, s[36:39], s76 offen sc1 lds\n"
    "v_mfma_f32_16x16x32_bf16 v[68:71], v[192:195], v[24:27], v[68:71]\n"
    "ds_read_b128 v[24:27], v9 offset:30720\n"
    "v_mfma_f32_16x16x32_bf16 v[56:59], v[196:199], v[28:31], v[56:59]\n"
    "s_add_u32 s76, s76, 16384\n"
    "s_add_u32 m0, s59, 8192\n"
    "v_mfma_f32_16x16x32_bf16 v[60:63], v[200:203], v[28:31], v[60:63]\n"
    "s_waitcnt lgkmcnt(2)\n"
    "buffer_load_dwordx4 v1, s[36:39], s76 offen sc1 lds\n"
    "v_mfma_f32_16x16x32_bf16 v[68:71], v[204:207], v[28:31], v[68:71]\n"
    "ds_read_b128 v[28:31], v9 offset:31744\n"
    "v_mfma_f32_16x16x32_bf16 v[56:59], v[208:211], v[16:19], v[56:59]\n"
    "v_mfma_f32_16x16x32_bf16 v[60:63], v[212:215], v[16:19], v[60:63]\n"
    "s_waitcnt lgkmcnt(2)\n"
    "s_add_u32 s76, s76, 16384\n"
    "s_add_u32 m0, s59, 12288\n"
    "v_mfma_f32_16x16x32_bf16 v[68:71], v[216:219], v[16:19], v[68:71]\n"
    "buffer_load_dwordx4 v1, s[36:39], s76 offen sc1 lds\n"
    "v_mfma_f32_16x16x32_bf16 v[56:59], v[220:223], v[20:23], v[56:59]\n"
    "v_mfma_f32_16x16x32_bf16 v[60:63], v[224:227], v[20:23], v[60:63]\n"
    "s_waitcnt lgkmcnt(1)\n"
    "v_mfma_f32_16x16x32_bf16 v[68:71], v[228:231], v[20:23], v[68:71]\n"
    "s_add_u32 s76, s76, 16384\n"
    "v_mfma_f32_16x16x32_bf16 v[56:59], v[232:235], v[24:27], v[56:59]\n"
    "s_add_u32 m0, s59, 16384\n"
    "v_mfma_f32_16x16x32_bf16 v[60:63], v[236:239], v[24:27], v[60:63]\n"
    "s_waitcnt lgkmcnt(0)\n"
    "buffer_load_dwordx4 v1, s[36:39], s76 offen sc1 lds\n"
    "v_mfma_f32_16x16x32_bf16 v[68:71], v[240:243], v[24:27], v[68:71]\n"
    "v_mfma_f32_16x16x32_bf16 v[56:59], v[244:247], v[28:31], v[56:59]\n"
    "s_add_u32 s76, s76, 16384\n"
    "s_add_u32 m0, s59, 20480\n"
    "v_mfma_f32_16x16x32_bf16 v[60:63], v[248:251], v[28:31], v[60:63]\n"
    "buffer_load_dwordx4 v1, s[36:39], s76 offen sc1 lds\n"
    "v_mfma_f32_16x16x32_bf16 v[68:71], v[252:255], v[28:31], v[68:71]\n"
    "v_cvt_pk_bf16_f32 v56, v56, v57\n"
    "v_cvt_pk_bf16_f32 v57, v58, v59\n"
    "s_nop 1\n"
    "v_cvt_pk_bf16_f32 v58, v60, v61\n"
    "v_cvt_pk_bf16_f32 v59, v62, v63\n"
    "buffer_store_dwordx4 v[56:59], v15, s[36:39], s73 offen offset:1024 sc1\n"
    "s_nop 0\n"
    "v_cvt_pk_bf16_f32 v68, v68, v69\n"
    "v_cvt_pk_bf16_f32 v69, v70, v71\n"
    "s_nop 0\n"
    "v_permlane16_swap_b32 v68, v70\n"
    "v_permlane16_swap_b32 v69, v71\n"
    "buffer_store_dwordx4 v[68:71], v4, s[36:39], s73 offen offset:1024 sc1\n"
    "v_mov_b32 v12, s68\n"
    "s_mov_b32 s73, 0x80000000\n"
    "s_mov_b32 s68, s71\n"
    "s_add_u32 m0, s59, 24576\n"
    "s_add_u32 s76, s76, 16384\n"
    "buffer_load_dwordx4 v1, s[36:39], s76 offen sc1 lds\n"
    "s_add_u32 m0, s59, 28672\n"
    "s_add_u32 s76, s76, 16384\n"
    "buffer_load_dwordx4 v1, s[36:39], s76 offen sc1 lds\n"
    "s_waitcnt vmcnt(2)\n"
    "buffer_atomic_smax v12, v10, s[36:39], 0 offen sc1\n"
    "s_mov_b32 s69, 0x80000000\n"
    "s_mov_b32 s70, 0x80000000\n"
    "v_mov_b32 v7, v12\n"
    "s_waitcnt vmcnt(12)\n"
    "s_add_u32 s71, s68, 1\n"
    "v_add_u32 v13, v11, v3\n"
    "v_cmp_gt_i32 vcc, s71, v13\n"
    "s_nop 0\n"
    "s_or_b64 exec, vcc, s[62:63]\n"
    "buffer_atomic_smax v11, v2, s[36:39], 0 offen sc0 sc1\n"
    "s_mov_b64 exec, -1\n"
    "s_waitcnt vmcnt(2)\n"
    "s_branch .LgWO_bar0_%=\n"
    ".LgWO_idle1_%=:\n"
    "s_or_b64 exec, vcc, s[62:63]\n"
    "buffer_atomic_smax v11, v2, s[36:39], 0 offen sc0 sc1\n"
    "s_mov_b64 exec, -1\n"
    "s_waitcnt vmcnt(1)\n"
    "buffer_atomic_smax v12, v10, s[36:39], 0 offen sc1\n"
    "s_branch .LgWO_block1_0_%=\n"
    ".LgWO_block1_%=:\n"
    "v_add_u32 v13, v11, v3\n"
    "v_cmp_gt_i32 vcc, s71, v13\n"
    "s_nop 0\n"
    "s_or_b64 exec, vcc, s[62:63]\n"
    "buffer_atomic_smax v11, v2, s[36:39], 0 offen sc0 sc1\n"
    "s_mov_b64 exec, -1\n"
    "s_sleep 3\n"
    "s_waitcnt vmcnt(1)\n"
    ".LgWO_block1_0_%=:\n"
    "v_add_u32 v13, v5, v3\n"
    "v_cmp_gt_i32 vcc, s71, v13\n"
    "s_cbranch_vccz .LgWO_go1_%=\n"
    "v_add_u32 v13, v5, v3\n"
    "v_cmp_gt_i32 vcc, s71, v13\n"
    "s_nop 0\n"
    "s_or_b64 exec, vcc, s[62:63]\n"
    "buffer_atomic_smax v5, v2, s[36:39], 0 offen sc0 sc1\n"
    "s_mov_b64 exec, -1\n"
    "s_sleep 3\n"
    "s_waitcnt vmcnt(1)\n"
    ".LgWO_block1_1_%=:\n"
    "v_add_u32 v13, v11, v3\n"
    "v_cmp_gt_i32 vcc, s71, v13\n"
    "s_cbranch_vccz .LgWO_go1_%=\n"
    "s_sub_u32 s72, s72, 1\n"
    "s_cbranch_scc0 .LgWO_block1_%=\n"
    "s_mov_b32 s72, 0\n"
    ".LgWO_go1_%=:\n"
    "s_lshr_b32 s76, s71, 1\n"
    "s_and_b32 s76, s76, s54\n"
    "s_mul_i32 s76, s76, s53\n"
    "s_add_u32 s76, s76, s52\n"
    "s_add_u32 m0, s59, 0\n"
    "s_nop 0\n"
    "buffer_load_dwordx4 v1, s[36:39], s76 offen sc1 lds\n"
    "s_add_u32 m0, s59, 4096\n"
    "s_add_u32 s76, s76, 16384\n"
    "buffer_load_dwordx4 v1, s[36:39], s76 offen sc1 lds\n"
    "s_add_u32 m0, s59, 8192\n"
    "s_add_u32 s76, s76, 16384\n"
    "buffer_load_dwordx4 v1, s[36:39], s76 offen sc1 lds\n"
    "s_add_u32 m0, s59, 12288\n"
    "s_add_u32 s76, s76, 16384\n"
    "buffer_load_dwordx4 v1, s[36:39], s76 offen sc1 lds\n"
    "s_add_u32 m0, s59, 16384\n"
    "s_add_u32 s76, s76, 16384\n"
    "buffer_load_dwordx4 v1, s[36:39], s76 offen sc1 lds\n"
    "s_add_u32 m0, s59, 20480\n"
    "s_add_u32 s76, s76, 16384\n"
    "buffer_load_dwordx4 v1, s[36:39], s76 offen sc1 lds\n"
    "s_add_u32 m0, s59, 24576\n"
    "s_add_u32 s76, s76, 16384\n"
    "buffer_load_dwordx4 v1, s[36:39], s76 offen sc1 lds\n"
    "s_add_u32 m0, s59, 28672\n"
    "s_add_u32 s76, s76, 16384\n"
    "buffer_load_dwordx4 v1, s[36:39], s76 offen sc1 lds\n"
    "s_mov_b32 s69, 0x80000000\n"
    "s_mov_b32 s70, 0x80000000\n"
    "v_mov_b32 v7, v12\n"
    "s_waitcnt vmcnt(8)\n"
    "s_add_u32 s71, s68, 1\n"
    "v_add_u32 v13, v11, v3\n"
    "v_cmp_gt_i32 vcc, s71, v13\n"
    "s_nop 0\n"
    "s_or_b64 exec, vcc, s[62:63]\n"
    "buffer_atomic_smax v11, v2, s[36:39], 0 offen sc0 sc1\n"
    "s_mov_b64 exec, -1\n"
    "s_waitcnt vmcnt(1)\n"
    "s_branch .LgWO_bar0_%=\n"
    ".LgWO_bar1_%=:\n"
    "s_barrier\n"
    "ds_read_b128 v[16:19], v9 offset:0\n"
    "ds_read_b128 v[20:23], v9 offset:1024\n"
    "ds_read_b128 v[24:27], v9 offset:2048\n"
    "ds_read_b128 v[28:31], v9 offset:3072\n"
    "v_add_u32 v13, v5, v3\n"
    "v_cmp_gt_i32 vcc, s71, v13\n"
    "s_nop 0\n"
    "s_or_b64 exec, vcc, s[62:63]\n"
    "buffer_atomic_smax v5, v2, s[36:39], 0 offen sc0 sc1\n"
    "s_mov_b64 exec, -1\n"
    "s_lshr_b32 s76, s71, 1\n"
    "s_and_b32 s76, s76, s54\n"
    "s_mul_i32 s76, s76, s53\n"
    "s_add_u32 s76, s76, s52\n"
    "s_waitcnt lgkmcnt(3)\n"
    "s_branch .LgWO_head1_%=\n"
    ".LgWO_FIN_%=:\n"
  );
}
DEVI void role_down(const Params& p, int L, int j, int i, u32 dbg, unsigned long long tag) {
  const int lane = threadIdx.x & 63, w = threadIdx.x >> 6, e0 = j * 1152 * 576 + i * 192;
  GEMM_DECL;
  g_w0 = make_srd(p.w2 + e0, 4608u * 576 * 2 - e0 * 2); g_w1 = g_w0; g_w2 = g_w0;
  g_ldw = 1152;
  g_in_base = pgb(L, B_H + 9 * j); g_in_stride = SLOT; g_in_mask = RH - 1;
  g_out_base = pgb(L, B_PF + 5 * j); g_out_kb = 12 * i; g_out_mask = RP - 1;
  g_flag = lane < 4 ? mbox(L, lane, SL_REDF, 12 * j + 4 * i + w) : ((lane < 10 && w == 0) ? mbox(L, j, SL_UP + lane - 4, 16 + i) : OOB);
  g_paddr = mbox(L, j, SL_DOWN + i, lane);
  g_pbias = lane < 24 ? 0 : (lane < 40 ? 2 * RP : 1000);
  v16u sa, sb, va;
  gemm_args(GEMM_ARGS, p, dbg, tag, sa, sb, va);
  RUN_ASM(
    "s_nop 4\n"
    "s_getpc_b64 s[84:85]\n"
    ".LgDOWN_PC_%=:\n"
    "s_and_b32 s85, s85, 0xffff\n"
    "s_mov_b32 s86, .LgDOWN_FIN_%=-.LgDOWN_PC_%=\n"
    "s_mov_b32 s87, 0x20000\n"
    "v_mbcnt_lo_u32_b32 v40, -1, 0\n"
    "v_mbcnt_hi_u32_b32 v40, -1, v40\n"
    "v_lshlrev_b32 v40, 7, v40\n"
    "buffer_load_dword v41, v40, s[84:87], 0 offen\n"
    "v_add_u32 v40, 8192, v40\n"
    "buffer_load_dword v41, v40, s[84:87], 0 offen\n"
    "v_add_u32 v40, 8192, v40\n"
    "buffer_load_dword v41, v40, s[84:87], 0 offen\n"
    "s_mov_b32 s68, 0\n"
    "s_mov_b32 s71, 0\n"
    "v_add_u32 v9, 36864, v0\n"
    "s_mov_b32 s64, 0xbfb8aa3b\n"
    "s_mov_b32 s72, 262144\n"
    "s_add_u32 m0, s59, 0\n"
    "s_mov_b32 s74, 0\n"
    "buffer_load_dwordx4 v5, s[40:43], s74 offen lds\n"
    "s_add_u32 m0, s59, 4096\n"
    "s_mov_b32 s74, 128\n"
    "buffer_load_dwordx4 v5, s[40:43], s74 offen lds\n"
    "s_add_u32 m0, s59, 8192\n"
    "s_mov_b32 s74, 256\n"
    "buffer_load_dwordx4 v5, s[40:43], s74 offen lds\n"
    "s_add_u32 m0, s59, 12288\n"
    "s_mov_b32 s74, 36864\n"
    "buffer_load_dwordx4 v5, s[40:43], s74 offen lds\n"
    "s_add_u32 m0, s59, 16384\n"
    "s_mov_b32 s74, 36992\n"
    "buffer_load_dwordx4 v5, s[40:43], s74 offen lds\n"
    "s_add_u32 m0, s59, 20480\n"
    "s_mov_b32 s74, 37120\n"
    "buffer_load_dwordx4 v5, s[40:43], s74 offen lds\n"
    "s_add_u32 m0, s59, 24576\n"
    "s_mov_b32 s74, 73728\n"
    "buffer_load_dwordx4 v5, s[40:43], s74 offen lds\n"
    "s_add_u32 m0, s59, 28672\n"
    "s_mov_b32 s74, 73856\n"
    "buffer_load_dwordx4 v5, s[40:43], s74 offen lds\n"
    "s_add_u32 m0, s59, 32768\n"
    "s_mov_b32 s74, 73984\n"
    "buffer_load_dwordx4 v5, s[40:43], s74 offen lds\n"
    "s_add_u32 m0, s59, 36864\n"
    "s_mov_b32 s74, 110592\n"
    "buffer_load_dwordx4 v5, s[40:43], s74 offen lds\n"
    "s_add_u32 m0, s59, 40960\n"
    "s_mov_b32 s74, 110720\n"
    "buffer_load_dwordx4 v5, s[40:43], s74 offen lds\n"
    "s_add_u32 m0, s59, 45056\n"
    "s_mov_b32 s74, 110848\n"
    "buffer_load_dwordx4 v5, s[40:43], s74 offen lds\n"
    "s_add_u32 m0, s59, 49152\n"
    "s_mov_b32 s74, 147456\n"
    "buffer_load_dwordx4 v5, s[40:43], s74 offen lds\n"
    "s_add_u32 m0, s59, 53248\n"
    "s_mov_b32 s74, 147584\n"
    "buffer_load_dwordx4 v5, s[40:43], s74 offen lds\n"
    "s_add_u32 m0, s59, 57344\n"
    "s_mov_b32 s74, 147712\n"
    "buffer_load_dwordx4 v5, s[40:43], s74 offen lds\n"
    "s_add_u32 m0, s59, 61440\n"
    "s_mov_b32 s74, 184320\n"
    "buffer_load_dwordx4 v5, s[40:43], s74 offen lds\n"
    "s_add_u32 m0, s59, 65536\n"
    "s_mov_b32 s74, 184448\n"
    "buffer_load_dwordx4 v5, s[40:43], s74 offen lds\n"
    "s_add_u32 m0, s59, 69632\n"
    "s_mov_b32 s74, 184576\n"
    "buffer_load_dwordx4 v5, s[40:43], s74 offen lds\n"
    "s_add_u32 m0, s59, 73728\n"
    "s_mov_b32 s74, 221184\n"
    "buffer_load_dwordx4 v5, s[40:43], s74 offen lds\n"
    "s_add_u32 m0, s59, 77824\n"
    "s_mov_b32 s74, 221312\n"
    "buffer_load_dwordx4 v5, s[40:43], s74 offen lds\n"
    "s_add_u32 m0, s59, 81920\n"
    "s_mov_b32 s74, 221440\n"
    "buffer_load_dwordx4 v5, s[40:43], s74 offen lds\n"
    "s_add_u32 m0, s59, 86016\n"
    "s_mov_b32 s74, 258048\n"
    "buffer_load_dwordx4 v5, s[40:43], s74 offen lds\n"
    "s_add_u32 m0, s59, 90112\n"
    "s_mov_b32 s74, 258176\n"
    "buffer_load_dwordx4 v5, s[40:43], s74 offen lds\n"
    "s_add_u32 m0, s59, 94208\n"
    "s_mov_b32 s74, 258304\n"
    "buffer_load_dwordx4 v5, s[40:43], s74 offen lds\n"
    "s_add_u32 m0, s59, 98304\n"
    "s_mov_b32 s74, 294912\n"
    "buffer_load_dwordx4 v5, s[40:43], s74 offen lds\n"
    "s_add_u32 m0, s59, 102400\n"
    "s_mov_b32 s74, 295040\n"
    "buffer_load_dwordx4 v5, s[40:43], s74 offen lds\n"
    "s_add_u32 m0, s59, 106496\n"
    "s_mov_b32 s74, 295168\n"
    "buffer_load_dwordx4 v5, s[40:43], s74 offen lds\n"
    "s_add_u32 m0, s59, 110592\n"
    "s_mov_b32 s74, 331776\n"
    "buffer_load_dwordx4 v5, s[40:43], s74 offen lds\n"
    "s_add_u32 m0, s59, 114688\n"
    "s_mov_b32 s74, 331904\n"
    "buffer_load_dwordx4 v5, s[40:43], s74 offen lds\n"
    "s_add_u32 m0, s59, 118784\n"
    "s_mov_b32 s74, 332032\n"
    "buffer_load_dwordx4 v5, s[40:43], s74 offen lds\n"
    "v_add_u32 v16, 49152, v6\n"
    "v_add_u32 v17, 98304, v6\n"
    "v_add_u32 v18, 49152, v7\n"
    "v_add_u32 v19, 98304, v7\n"
    "s_waitcnt vmcnt(24) lgkmcnt(0)\n"
    "s_barrier\n"
    "s_add_u32 m0, s59, 122880\n"
    "s_mov_b32 s74, 368640\n"
    "buffer_load_dwordx4 v5, s[40:43], s74 offen lds\n"
    "s_add_u32 m0, s59, 126976\n"
    "s_mov_b32 s74, 368768\n"
    "buffer_load_dwordx4 v5, s[40:43], s74 offen lds\n"
    "s_add_u32 m0, s59, 131072\n"
    "s_mov_b32 s74, 368896\n"
    "buffer_load_dwordx4 v5, s[40:43], s74 offen lds\n"
    "s_add_u32 m0, s59, 135168\n"
    "s_mov_b32 s74, 405504\n"
    "buffer_load_dwordx4 v5, s[40:43], s74 offen lds\n"
    "s_add_u32 m0, s59, 139264\n"
    "s_mov_b32 s74, 405632\n"
    "buffer_load_dwordx4 v5, s[40:43], s74 offen lds\n"
    "s_add_u32 m0, s59, 143360\n"
    "s_mov_b32 s74, 405760\n"
    "buffer_load_dwordx4 v5, s[40:43], s74 offen lds\n"
    "ds_read_b64_tr_b16 a[0:1], v6 offset:0\n"
    "ds_read_b64_tr_b16 a[2:3], v6 offset:512\n"
    "ds_read_b64_tr_b16 a[4:5], v6 offset:8\n"
    "ds_read_b64_tr_b16 a[6:7], v6 offset:520\n"
    "ds_read_b64_tr_b16 a[8:9], v7 offset:0\n"
    "ds_read_b64_tr_b16 a[10:11], v7 offset:512\n"
    "ds_read_b64_tr_b16 a[12:13], v6 offset:12288\n"
    "ds_read_b64_tr_b16 a[14:15], v6 offset:12800\n"
    "ds_read_b64_tr_b16 a[16:17], v6 offset:12296\n"
    "ds_read_b64_tr_b16 a[18:19], v6 offset:12808\n"
    "ds_read_b64_tr_b16 a[20:21], v7 offset:12288\n"
    "ds_read_b64_tr_b16 a[22:23], v7 offset:12800\n"
    "s_waitcnt vmcnt(24) lgkmcnt(0)\n"
    "s_barrier\n"
    "s_add_u32 m0, s59, 0\n"
    "s_mov_b32 s74, 442368\n"
    "buffer_load_dwordx4 v5, s[40:43], s74 offen lds\n"
    "s_add_u32 m0, s59, 4096\n"
    "s_mov_b32 s74, 442496\n"
    "buffer_load_dwordx4 v5, s[40:43], s74 offen lds\n"
    "s_add_u32 m0, s59, 8192\n"
    "s_mov_b32 s74, 442624\n"
    "buffer_load_dwordx4 v5, s[40:43], s74 offen lds\n"
    "s_add_u32 m0, s59, 12288\n"
    "s_mov_b32 s74, 479232\n"
    "buffer_load_dwordx4 v5, s[40:43], s74 offen lds\n"
    "s_add_u32 m0, s59, 16384\n"
    "s_mov_b32 s74, 479360\n"
    "buffer_load_dwordx4 v5, s[40:43], s74 offen lds\n"
    "s_add_u32 m0, s59, 20480\n"
    "s_mov_b32 s74, 479488\n"
    "buffer_load_dwordx4 v5, s[40:43], s74 offen lds\n"
    "ds_read_b64_tr_b16 a[24:25], v6 offset:24576\n"
    "ds_read_b64_tr_b16 a[26:27], v6 offset:25088\n"
    "ds_read_b64_tr_b16 a[28:29], v6 offset:24584\n"
    "ds_read_b64_tr_b16 a[30:31], v6 offset:25096\n"
    "ds_read_b64_tr_b16 a[32:33], v7 offset:24576\n"
    "ds_read_b64_tr_b16 a[34:35], v7 offset:25088\n"
    "ds_read_b64_tr_b16 a[36:37], v6 offset:36864\n"
    "ds_read_b64_tr_b16 a[38:39], v6 offset:37376\n"
    "ds_read_b64_tr_b16 a[40:41], v6 offset:36872\n"
    "ds_read_b64_tr_b16 a[42:43], v6 offset:37384\n"
    "ds_read_b64_tr_b16 a[44:45], v7 offset:36864\n"
    "ds_read_b64_tr_b16 a[46:47], v7 offset:37376\n"
    "s_waitcnt vmcnt(24) lgkmcnt(0)\n"
    "s_barrier\n"
    "s_add_u32 m0, s59, 24576\n"
    "s_mov_b32 s74, 516096\n"
    "buffer_load_dwordx4 v5, s[40:43], s74 offen lds\n"
    "s_add_u32 m0, s59, 28672\n"
    "s_mov_b32 s74, 516224\n"
    "buffer_load_dwordx4 v5, s[40:43], s74 offen lds\n"
    "s_add_u32 m0, s59, 32768\n"
    "s_mov_b32 s74, 516352\n"
    "buffer_load_dwordx4 v5, s[40:43], s74 offen lds\n"
    "s_add_u32 m0, s59, 36864\n"
    "s_mov_b32 s74, 552960\n"
    "buffer_load_dwordx4 v5, s[40:43], s74 offen lds\n"
    "s_add_u32 m0, s59, 40960\n"
    "s_mov_b32 s74, 553088\n"
    "buffer_load_dwordx4 v5, s[40:43], s74 offen lds\n"
    "s_add_u32 m0, s59, 45056\n"
    "s_mov_b32 s74, 553216\n"
    "buffer_load_dwordx4 v5, s[40:43], s74 offen lds\n"
    "ds_read_b64_tr_b16 a[48:49], v16 offset:0\n"
    "ds_read_b64_tr_b16 a[50:51], v16 offset:512\n"
    "ds_read_b64_tr_b16 a[52:53], v16 offset:8\n"
    "ds_read_b64_tr_b16 a[54:55], v16 offset:520\n"
    "ds_read_b64_tr_b16 a[56:57], v18 offset:0\n"
    "ds_read_b64_tr_b16 a[58:59], v18 offset:512\n"
    "ds_read_b64_tr_b16 a[60:61], v16 offset:12288\n"
    "ds_read_b64_tr_b16 a[62:63], v16 offset:12800\n"
    "ds_read_b64_tr_b16 a[64:65], v16 offset:12296\n"
    "ds_read_b64_tr_b16 a[66:67], v16 offset:12808\n"
    "ds_read_b64_tr_b16 a[68:69], v18 offset:12288\n"
    "ds_read_b64_tr_b16 a[70:71], v18 offset:12800\n"
    "s_waitcnt vmcnt(24) lgkmcnt(0)\n"
    "s_barrier\n"
    "s_add_u32 m0, s59, 49152\n"
    "s_mov_b32 s74, 589824\n"
    "buffer_load_dwordx4 v5, s[40:43], s74 offen lds\n"
    "s_add_u32 m0, s59, 53248\n"
    "s_mov_b32 s74, 589952\n"
    "buffer_load_dwordx4 v5, s[40:43], s74 offen lds\n"
    "s_add_u32 m0, s59, 57344\n"
    "s_mov_b32 s74, 590080\n"
    "buffer_load_dwordx4 v5, s[40:43], s74 offen lds\n"
    "s_add_u32 m0, s59, 61440\n"
    "s_mov_b32 s74, 626688\n"
    "buffer_load_dwordx4 v5, s[40:43], s74 offen lds\n"
    "s_add_u32 m0, s59, 65536\n"
    "s_mov_b32 s74, 626816\n"
    "buffer_load_dwordx4 v5, s[40:43], s74 offen lds\n"
    "s_add_u32 m0, s59, 69632\n"
    "s_mov_b32 s74, 626944\n"
    "buffer_load_dwordx4 v5, s[40:43], s74 offen lds\n"
    "ds_read_b64_tr_b16 a[72:73], v16 offset:24576\n"
    "ds_read_b64_tr_b16 a[74:75], v16 offset:25088\n"
    "ds_read_b64_tr_b16 a[76:77], v16 offset:24584\n"
    "ds_read_b64_tr_b16 a[78:79], v16 offset:25096\n"
    "ds_read_b64_tr_b16 a[80:81], v18 offset:24576\n"
    "ds_read_b64_tr_b16 a[82:83], v18 offset:25088\n"
    "ds_read_b64_tr_b16 a[84:85], v16 offset:36864\n"
    "ds_read_b64_tr_b16 a[86:87], v16 offset:37376\n"
    "ds_read_b64_tr_b16 a[88:89], v16 offset:36872\n"
    "ds_read_b64_tr_b16 a[90:91], v16 offset:37384\n"
    "ds_read_b64_tr_b16 a[92:93], v18 offset:36864\n"
    "ds_read_b64_tr_b16 a[94:95], v18 offset:37376\n"
    "s_waitcnt vmcnt(24) lgkmcnt(0)\n"
    "s_barrier\n"
    "s_add_u32 m0, s59, 73728\n"
    "s_mov_b32 s74, 663552\n"
    "buffer_load_dwordx4 v5, s[40:43], s74 offen lds\n"
    "s_add_u32 m0, s59, 77824\n"
    "s_mov_b32 s74, 663680\n"
    "buffer_load_dwordx4 v5, s[40:43], s74 offen lds\n"
    "s_add_u32 m0, s59, 81920\n"
    "s_mov_b32 s74, 663808\n"
    "buffer_load_dwordx4 v5, s[40:43], s74 offen lds\n"
    "s_add_u32 m0, s59, 86016\n"
    "s_mov_b32 s74, 700416\n"
    "buffer_load_dwordx4 v5, s[40:43], s74 offen lds\n"
    "s_add_u32 m0, s59, 90112\n"
    "s_mov_b32 s74, 700544\n"
    "buffer_load_dwordx4 v5, s[40:43], s74 offen lds\n"
    "s_add_u32 m0, s59, 94208\n"
    "s_mov_b32 s74, 700672\n"
    "buffer_load_dwordx4 v5, s[40:43], s74 offen lds\n"
    "ds_read_b64_tr_b16 a[96:97], v17 offset:0\n"
    "ds_read_b64_tr_b16 a[98:99], v17 offset:512\n"
    "ds_read_b64_tr_b16 a[100:101], v17 offset:8\n"
    "ds_read_b64_tr_b16 a[102:103], v17 offset:520\n"
    "ds_read_b64_tr_b16 a[104:105], v19 offset:0\n"
    "ds_read_b64_tr_b16 a[106:107], v19 offset:512\n"
    "ds_read_b64_tr_b16 a[108:109], v17 offset:12288\n"
    "ds_read_b64_tr_b16 a[110:111], v17 offset:12800\n"
    "ds_read_b64_tr_b16 a[112:113], v17 offset:12296\n"
    "ds_read_b64_tr_b16 a[114:115], v17 offset:12808\n"
    "ds_read_b64_tr_b16 a[116:117], v19 offset:12288\n"
    "ds_read_b64_tr_b16 a[118:119], v19 offset:12800\n"
    "s_waitcnt vmcnt(24) lgkmcnt(0)\n"
    "s_barrier\n"
    "s_add_u32 m0, s59, 98304\n"
    "s_mov_b32 s74, 737280\n"
    "buffer_load_dwordx4 v5, s[40:43], s74 offen lds\n"
    "s_add_u32 m0, s59, 102400\n"
    "s_mov_b32 s74, 737408\n"
    "buffer_load_dwordx4 v5, s[40:43], s74 offen lds\n"
    "s_add_u32 m0, s59, 106496\n"
    "s_mov_b32 s74, 737536\n"
    "buffer_load_dwordx4 v5, s[40:43], s74 offen lds\n"
    "s_add_u32 m0, s59, 110592\n"
    "s_mov_b32 s74, 774144\n"
    "buffer_load_dwordx4 v5, s[40:43], s74 offen lds\n"
    "s_add_u32 m0, s59, 114688\n"
    "s_mov_b32 s74, 774272\n"
    "buffer_load_dwordx4 v5, s[40:43], s74 offen lds\n"
    "s_add_u32 m0, s59, 118784\n"
    "s_mov_b32 s74, 774400\n"
    "buffer_load_dwordx4 v5, s[40:43], s74 offen lds\n"
    "ds_read_b64_tr_b16 a[120:121], v17 offset:24576\n"
    "ds_read_b64_tr_b16 a[122:123], v17 offset:25088\n"
    "ds_read_b64_tr_b16 a[124:125], v17 offset:24584\n"
    "ds_read_b64_tr_b16 a[126:127], v17 offset:25096\n"
    "ds_read_b64_tr_b16 a[128:129], v19 offset:24576\n"
    "ds_read_b64_tr_b16 a[130:131], v19 offset:25088\n"
    "ds_read_b64_tr_b16 a[132:133], v17 offset:36864\n"
    "ds_read_b64_tr_b16 a[134:135], v17 offset:37376\n"
    "ds_read_b64_tr_b16 a[136:137], v17 offset:36872\n"
    "ds_read_b64_tr_b16 a[138:139], v17 offset:37384\n"
    "ds_read_b64_tr_b16 a[140:141], v19 offset:36864\n"
    "ds_read_b64_tr_b16 a[142:143], v19 offset:37376\n"
    "s_waitcnt vmcnt(24) lgkmcnt(0)\n"
    "s_barrier\n"
    "s_add_u32 m0, s59, 122880\n"
    "s_mov_b32 s74, 811008\n"
    "buffer_load_dwordx4 v5, s[40:43], s74 offen lds\n"
    "s_add_u32 m0, s59, 126976\n"
    "s_mov_b32 s74, 811136\n"
    "buffer_load_dwordx4 v5, s[40:43], s74 offen lds\n"
    "s_add_u32 m0, s59, 131072\n"
    "s_mov_b32 s74, 811264\n"
    "buffer_load_dwordx4 v5, s[40:43], s74 offen lds\n"
    "s_add_u32 m0, s59, 135168\n"
    "s_mov_b32 s74, 847872\n"
    "buffer_load_dwordx4 v5, s[40:43], s74 offen lds\n"
    "s_add_u32 m0, s59, 139264\n"
    "s_mov_b32 s74, 848000\n"
    "buffer_load_dwordx4 v5, s[40:43], s74 offen lds\n"
    "s_add_u32 m0, s59, 143360\n"
    "s_mov_b32 s74, 848128\n"
    "buffer_load_dwordx4 v5, s[40:43], s74 offen lds\n"
    "ds_read_b64_tr_b16 a[144:145], v6 offset:0\n"
    "ds_read_b64_tr_b16 a[146:147], v6 offset:512\n"
    "ds_read_b64_tr_b16 a[148:149], v6 offset:8\n"
    "ds_read_b64_tr_b16 a[150:151], v6 offset:520\n"
    "ds_read_b64_tr_b16 a[152:153], v7 offset:0\n"
    "ds_read_b64_tr_b16 a[154:155], v7 offset:512\n"
    "ds_read_b64_tr_b16 a[156:157], v6 offset:12288\n"
    "ds_read_b64_tr_b16 a[158:159], v6 offset:12800\n"
    "ds_read_b64_tr_b16 a[160:161], v6 offset:12296\n"
    "ds_read_b64_tr_b16 a[162:163], v6 offset:12808\n"
    "ds_read_b64_tr_b16 a[164:165], v7 offset:12288\n"
    "ds_read_b64_tr_b16 a[166:167], v7 offset:12800\n"
    "s_waitcnt vmcnt(24) lgkmcnt(0)\n"
    "s_barrier\n"
    "s_add_u32 m0, s59, 0\n"
    "s_mov_b32 s74, 884736\n"
    "buffer_load_dwordx4 v5, s[40:43], s74 offen lds\n"
    "s_add_u32 m0, s59, 4096\n"
    "s_mov_b32 s74, 884864\n"
    "buffer_load_dwordx4 v5, s[40:43], s74 offen lds\n"
    "s_add_u32 m0, s59, 8192\n"
    "s_mov_b32 s74, 884992\n"
    "buffer_load_dwordx4 v5, s[40:43], s74 offen lds\n"
    "s_add_u32 m0, s59, 12288\n"
    "s_mov_b32 s74, 921600\n"
    "buffer_load_dwordx4 v5, s[40:43], s74 offen lds\n"
    "s_add_u32 m0, s59, 16384\n"
    "s_mov_b32 s74, 921728\n"
    "buffer_load_dwordx4 v5, s[40:43], s74 offen lds\n"
    "s_add_u32 m0, s59, 20480\n"
    "s_mov_b32 s74, 921856\n"
    "buffer_load_dwordx4 v5, s[40:43], s74 offen lds\n"
    "ds_read_b64_tr_b16 a[168:169], v6 offset:24576\n"
    "ds_read_b64_tr_b16 a[170:171], v6 offset:25088\n"
    "ds_read_b64_tr_b16 a[172:173], v6 offset:24584\n"
    "ds_read_b64_tr_b16 a[174:175], v6 offset:25096\n"
    "ds_read_b64_tr_b16 a[176:177], v7 offset:24576\n"
    "ds_read_b64_tr_b16 a[178:179], v7 offset:25088\n"
    "ds_read_b64_tr_b16 a[180:181], v6 offset:36864\n"
    "ds_read_b64_tr_b16 a[182:183], v6 offset:37376\n"
    "ds_read_b64_tr_b16 a[184:185], v6 offset:36872\n"
    "ds_read_b64_tr_b16 a[186:187], v6 offset:37384\n"
    "ds_read_b64_tr_b16 a[188:189], v7 offset:36864\n"
    "ds_read_b64_tr_b16 a[190:191], v7 offset:37376\n"
    "s_waitcnt vmcnt(24) lgkmcnt(0)\n"
    "s_barrier\n"
    "s_add_u32 m0, s59, 24576\n"
    "s_mov_b32 s74, 958464\n"
    "buffer_load_dwordx4 v5, s[40:43], s74 offen lds\n"
    "s_add_u32 m0, s59, 28672\n"
    "s_mov_b32 s74, 958592\n"
    "buffer_load_dwordx4 v5, s[40:43], s74 offen lds\n"
    "s_add_u32 m0, s59, 32768\n"
    "s_mov_b32 s74, 958720\n"
    "buffer_load_dwordx4 v5, s[40:43], s74 offen lds\n"
    "s_add_u32 m0, s59, 36864\n"
    "s_mov_b32 s74, 995328\n"
    "buffer_load_dwordx4 v5, s[40:43], s74 offen lds\n"
    "s_add_u32 m0, s59, 40960\n"
    "s_mov_b32 s74, 995456\n"
    "buffer_load_dwordx4 v5, s[40:43], s74 offen lds\n"
    "s_add_u32 m0, s59, 45056\n"
    "s_mov_b32 s74, 995584\n"
    "buffer_load_dwordx4 v5, s[40:43], s74 offen lds\n"
    "ds_read_b64_tr_b16 a[192:193], v16 offset:0\n"
    "ds_read_b64_tr_b16 a[194:195], v16 offset:512\n"
    "ds_read_b64_tr_b16 a[196:197], v16 offset:8\n"
    "ds_read_b64_tr_b16 a[198:199], v16 offset:520\n"
    "ds_read_b64_tr_b16 a[200:201], v18 offset:0\n"
    "ds_read_b64_tr_b16 a[202:203], v18 offset:512\n"
    "ds_read_b64_tr_b16 a[204:205], v16 offset:12288\n"
    "ds_read_b64_tr_b16 a[206:207], v16 offset:12800\n"
    "ds_read_b64_tr_b16 a[208:209], v16 offset:12296\n"
    "ds_read_b64_tr_b16 a[210:211], v16 offset:12808\n"
    "ds_read_b64_tr_b16 a[212:213], v18 offset:12288\n"
    "ds_read_b64_tr_b16 a[214:215], v18 offset:12800\n"
    "s_waitcnt vmcnt(24) lgkmcnt(0)\n"
    "s_barrier\n"
    "s_add_u32 m0, s59, 49152\n"
    "s_mov_b32 s74, 1032192\n"
    "buffer_load_dwordx4 v5, s[40:43], s74 offen lds\n"
    "s_add_u32 m0, s59, 53248\n"
    "s_mov_b32 s74, 1032320\n"
    "buffer_load_dwordx4 v5, s[40:43], s74 offen lds\n"
    "s_add_u32 m0, s59, 57344\n"
    "s_mov_b32 s74, 1032448\n"
    "buffer_load_dwordx4 v5, s[40:43], s74 offen lds\n"
    "s_add_u32 m0, s59, 61440\n"
    "s_mov_b32 s74, 1069056\n"
    "buffer_load_dwordx4 v5, s[40:43], s74 offen lds\n"
    "s_add_u32 m0, s59, 65536\n"
    "s_mov_b32 s74, 1069184\n"
    "buffer_load_dwordx4 v5, s[40:43], s74 offen lds\n"
    "s_add_u32 m0, s59, 69632\n"
    "s_mov_b32 s74, 1069312\n"
    "buffer_load_dwordx4 v5, s[40:43], s74 offen lds\n"
    "ds_read_b64_tr_b16 a[216:217], v16 offset:24576\n"
    "ds_read_b64_tr_b16 a[218:219], v16 offset:25088\n"
    "ds_read_b64_tr_b16 a[220:221], v16 offset:24584\n"
    "ds_read_b64_tr_b16 a[222:223], v16 offset:25096\n"
    "ds_read_b64_tr_b16 a[224:225], v18 offset:24576\n"
    "ds_read_b64_tr_b16 a[226:227], v18 offset:25088\n"
    "ds_read_b64_tr_b16 a[228:229], v16 offset:36864\n"
    "ds_read_b64_tr_b16 a[230:231], v16 offset:37376\n"
    "ds_read_b64_tr_b16 a[232:233], v16 offset:36872\n"
    "ds_read_b64_tr_b16 a[234:235], v16 offset:37384\n"
    "ds_read_b64_tr_b16 a[236:237], v18 offset:36864\n"
    "ds_read_b64_tr_b16 a[238:239], v18 offset:37376\n"
    "s_waitcnt vmcnt(24) lgkmcnt(0)\n"
    "s_barrier\n"
    "s_add_u32 m0, s59, 73728\n"
    "s_mov_b32 s74, 1105920\n"
    "buffer_load_dwordx4 v5, s[40:43], s74 offen lds\n"
    "s_add_u32 m0, s59, 77824\n"
    "s_mov_b32 s74, 1106048\n"
    "buffer_load_dwordx4 v5, s[40:43], s74 offen lds\n"
    "s_add_u32 m0, s59, 81920\n"
    "s_mov_b32 s74, 1106176\n"
    "buffer_load_dwordx4 v5, s[40:43], s74 offen lds\n"
    "s_add_u32 m0, s59, 86016\n"
    "s_mov_b32 s74, 1142784\n"
    "buffer_load_dwordx4 v5, s[40:43], s74 offen lds\n"
    "s_add_u32 m0, s59, 90112\n"
    "s_mov_b32 s74, 1142912\n"
    "buffer_load_dwordx4 v5, s[40:43], s74 offen lds\n"
    "s_add_u32 m0, s59, 94208\n"
    "s_mov_b32 s74, 1143040\n"
    "buffer_load_dwordx4 v5, s[40:43], s74 offen lds\n"
    "ds_read_b64_tr_b16 a[240:241], v17 offset:0\n"
    "ds_read_b64_tr_b16 a[242:243], v17 offset:512\n"
    "ds_read_b64_tr_b16 a[244:245], v17 offset:8\n"
    "ds_read_b64_tr_b16 a[246:247], v17 offset:520\n"
    "ds_read_b64_tr_b16 a[248:249], v19 offset:0\n"
    "ds_read_b64_tr_b16 a[250:251], v19 offset:512\n"
    "ds_read_b64_tr_b16 a[252:253], v17 offset:12288\n"
    "ds_read_b64_tr_b16 a[254:255], v17 offset:12800\n"
    "ds_read_b64_tr_b16 v[80:81], v17 offset:12296\n"
    "ds_read_b64_tr_b16 v[82:83], v17 offset:12808\n"
    "ds_read_b64_tr_b16 v[84:85], v19 offset:12288\n"
    "ds_read_b64_tr_b16 v[86:87], v19 offset:12800\n"
    "s_waitcnt vmcnt(24) lgkmcnt(0)\n"
    "s_barrier\n"
    "s_add_u32 m0, s59, 98304\n"
    "s_mov_b32 s74, 1179648\n"
    "buffer_load_dwordx4 v5, s[40:43], s74 offen lds\n"
    "s_add_u32 m0, s59, 102400\n"
    "s_mov_b32 s74, 1179776\n"
    "buffer_load_dwordx4 v5, s[40:43], s74 offen lds\n"
    "s_add_u32 m0, s59, 106496\n"
    "s_mov_b32 s74, 1179904\n"
    "buffer_load_dwordx4 v5, s[40:43], s74 offen lds\n"
    "s_add_u32 m0, s59, 110592\n"
    "s_mov_b32 s74, 1216512\n"
    "buffer_load_dwordx4 v5, s[40:43], s74 offen lds\n"
    "s_add_u32 m0, s59, 114688\n"
    "s_mov_b32 s74, 1216640\n"
    "buffer_load_dwordx4 v5, s[40:43], s74 offen lds\n"
    "s_add_u32 m0, s59, 118784\n"
    "s_mov_b32 s74, 1216768\n"
    "buffer_load_dwordx4 v5, s[40:43], s74 offen lds\n"
    "ds_read_b64_tr_b16 v[88:89], v17 offset:24576\n"
    "ds_read_b64_tr_b16 v[90:91], v17 offset:25088\n"
    "ds_read_b64_tr_b16 v[92:93], v17 offset:24584\n"
    "ds_read_b64_tr_b16 v[94:95], v17 offset:25096\n"
    "ds_read_b64_tr_b16 v[96:97], v19 offset:24576\n"
    "ds_read_b64_tr_b16 v[98:99], v19 offset:25088\n"
    "ds_read_b64_tr_b16 v[100:101], v17 offset:36864\n"
    "ds_read_b64_tr_b16 v[102:103], v17 offset:37376\n"
    "ds_read_b64_tr_b16 v[104:105], v17 offset:36872\n"
    "ds_read_b64_tr_b16 v[106:107], v17 offset:37384\n"
    "ds_read_b64_tr_b16 v[108:109], v19 offset:36864\n"
    "ds_read_b64_tr_b16 v[110:111], v19 offset:37376\n"
    "s_waitcnt vmcnt(24) lgkmcnt(0)\n"
    "s_barrier\n"
    "s_add_u32 m0, s59, 122880\n"
    "s_mov_b32 s74, 1253376\n"
    "buffer_load_dwordx4 v5, s[40:43], s74 offen lds\n"
    "s_add_u32 m0, s59, 126976\n"
    "s_mov_b32 s74, 1253504\n"
    "buffer_load_dwordx4 v5, s[40:43], s74 offen lds\n"
    "s_add_u32 m0, s59, 131072\n"
    "s_mov_b32 s74, 1253632\n"
    "buffer_load_dwordx4 v5, s[40:43], s74 offen lds\n"
    "s_add_u32 m0, s59, 135168\n"
    "s_mov_b32 s74, 1290240\n"
    "buffer_load_dwordx4 v5, s[40:43], s74 offen lds\n"
    "s_add_u32 m0, s59, 139264\n"
    "s_mov_b32 s74, 1290368\n"
    "buffer_load_dwordx4 v5, s[40:43], s74 offen lds\n"
    "s_add_u32 m0, s59, 143360\n"
    "s_mov_b32 s74, 1290496\n"
    "buffer_load_dwordx4 v5, s[40:43], s74 offen lds\n"
    "buffer_load_dwordx2 v[12:13], off, s[36:39], 0 sc1\n"
    "ds_read_b64_tr_b16 v[112:113], v6 offset:0\n"
    "ds_read_b64_tr_b16 v[114:115], v6 offset:512\n"
    "ds_read_b64_tr_b16 v[116:117], v6 offset:8\n"
    "ds_read_b64_tr_b16 v[118:119], v6 offset:520\n"
    "ds_read_b64_tr_b16 v[120:121], v7 offset:0\n"
    "ds_read_b64_tr_b16 v[122:123], v7 offset:512\n"
    "ds_read_b64_tr_b16 v[124:125], v6 offset:12288\n"
    "ds_read_b64_tr_b16 v[126:127], v6 offset:12800\n"
    "ds_read_b64_tr_b16 v[128:129], v6 offset:12296\n"
    "ds_read_b64_tr_b16 v[130:131], v6 offset:12808\n"
    "ds_read_b64_tr_b16 v[132:133], v7 offset:12288\n"
    "ds_read_b64_tr_b16 v[134:135], v7 offset:12800\n"
    "s_waitcnt vmcnt(25) lgkmcnt(0)\n"
    "s_barrier\n"
    "ds_read_b64_tr_b16 v[136:137], v6 offset:24576\n"
    "ds_read_b64_tr_b16 v[138:139], v6 offset:25088\n"
    "ds_read_b64_tr_b16 v[140:141], v6 offset:24584\n"
    "ds_read_b64_tr_b16 v[142:143], v6 offset:25096\n"
    "ds_read_b64_tr_b16 v[144:145], v7 offset:24576\n"
    "ds_read_b64_tr_b16 v[146:147], v7 offset:25088\n"
    "ds_read_b64_tr_b16 v[148:149], v6 offset:36864\n"
    "ds_read_b64_tr_b16 v[150:151], v6 offset:37376\n"
    "ds_read_b64_tr_b16 v[152:153], v6 offset:36872\n"
    "ds_read_b64_tr_b16 v[154:155], v6 offset:37384\n"
    "ds_read_b64_tr_b16 v[156:157], v7 offset:36864\n"
    "ds_read_b64_tr_b16 v[158:159], v7 offset:37376\n"
    "s_waitcnt vmcnt(19) lgkmcnt(0)\n"
    "s_barrier\n"
    "ds_read_b64_tr_b16 v[160:161], v16 offset:0\n"
    "ds_read_b64_tr_b16 v[162:163], v16 offset:512\n"
    "ds_read_b64_tr_b16 v[164:165], v16 offset:8\n"
    "ds_read_b64_tr_b16 v[166:167], v16 offset:520\n"
    "ds_read_b64_tr_b16 v[168:169], v18 offset:0\n"
    "ds_read_b64_tr_b16 v[170:171], v18 offset:512\n"
    "ds_read_b64_tr_b16 v[172:173], v16 offset:12288\n"
    "ds_read_b64_tr_b16 v[174:175], v16 offset:12800\n"
    "ds_read_b64_tr_b16 v[176:177], v16 offset:12296\n"
    "ds_read_b64_tr_b16 v[178:179], v16 offset:12808\n"
    "ds_read_b64_tr_b16 v[180:181], v18 offset:12288\n"
    "ds_read_b64_tr_b16 v[182:183], v18 offset:12800\n"
    "s_waitcnt vmcnt(13) lgkmcnt(0)\n"
    "s_barrier\n"
    "ds_read_b64_tr_b16 v[184:185], v16 offset:24576\n"
    "ds_read_b64_tr_b16 v[186:187], v16 offset:25088\n"
    "ds_read_b64_tr_b16 v[188:189], v16 offset:24584\n"
    "ds_read_b64_tr_b16 v[190:191], v16 offset:25096\n"
    "ds_read_b64_tr_b16 v[192:193], v18 offset:24576\n"
    "ds_read_b64_tr_b16 v[194:195], v18 offset:25088\n"
    "ds_read_b64_tr_b16 v[196:197], v16 offset:36864\n"
    "ds_read_b64_tr_b16 v[198:199], v16 offset:37376\n"
    "ds_read_b64_tr_b16 v[200:201], v16 offset:36872\n"
    "ds_read_b64_tr_b16 v[202:203], v16 offset:37384\n"
    "ds_read_b64_tr_b16 v[204:205], v18 offset:36864\n"
    "ds_read_b64_tr_b16 v[206:207], v18 offset:37376\n"
    "s_waitcnt vmcnt(7) lgkmcnt(0)\n"
    "s_barrier\n"
    "ds_read_b64_tr_b16 v[208:209], v17 offset:0\n"
    "ds_read_b64_tr_b16 v[210:211], v17 offset:512\n"
    "ds_read_b64_tr_b16 v[212:213], v17 offset:8\n"
    "ds_read_b64_tr_b16 v[214:215], v17 offset:520\n"
    "ds_read_b64_tr_b16 v[216:217], v19 offset:0\n"
    "ds_read_b64_tr_b16 v[218:219], v19 offset:512\n"
    "ds_read_b64_tr_b16 v[220:221], v17 offset:12288\n"
    "ds_read_b64_tr_b16 v[222:223], v17 offset:12800\n"
    "ds_read_b64_tr_b16 v[224:225], v17 offset:12296\n"
    "ds_read_b64_tr_b16 v[226:227], v17 offset:12808\n"
    "ds_read_b64_tr_b16 v[228:229], v19 offset:12288\n"
    "ds_read_b64_tr_b16 v[230:231], v19 offset:12800\n"
    "s_waitcnt vmcnt(0) lgkmcnt(0)\n"
    "s_barrier\n"
    ".LgDOWN_par_%=:\n"
    "v_readfirstlane_b32 s74, v12\n"
    "v_readfirstlane_b32 s75, v13\n"
    "s_xor_b32 s74, s74, s66\n"
    "s_xor_b32 s75, s75, s67\n"
    "s_cselect_b32 s75, 2, 0\n"
    "s_or_b32 s75, s75, s74\n"
    "s_cmp_lt_u32 s75, 2\n"
    "s_cbranch_scc1 .LgDOWN_par1_%=\n"
    "s_sleep 4\n"
    "buffer_load_dwordx2 v[12:13], off, s[36:39], 0 sc1\n"
    "s_waitcnt vmcnt(0)\n"
    "s_sub_u32 s72, s72, 1\n"
    "s_cbranch_scc0 .LgDOWN_par_%=\n"
    "s_mov_b32 s72, 0\n"
    ".LgDOWN_par1_%=:\n"
    "s_and_b32 s74, s74, 1\n"
    "s_lshl_b32 s74, s74, 13\n"
    "s_add_u32 s36, s36, s74\n"
    "s_addc_u32 s37, s37, 0\n"
    "v_add_u32 v13, v11, v3\n"
    "v_cmp_gt_i32 vcc, s71, v13\n"
    "s_nop 0\n"
    "s_or_b64 exec, vcc, s[62:63]\n"
    "buffer_atomic_smax v11, v2, s[36:39], 0 offen sc0 sc1\n"
    "s_mov_b64 exec, -1\n"
    "s_getpc_b64 s[84:85]\n"
    ".LgDOWN_PC2_%=:\n"
    "s_and_b32 s85, s85, 0xffff\n"
    "s_mov_b32 s86, .LgDOWN_FIN_%=-.LgDOWN_PC2_%=\n"
    "s_mov_b32 s87, 0x20000\n"
    "v_mbcnt_lo_u32_b32 v42, -1, 0\n"
    "v_mbcnt_hi_u32_b32 v42, -1, v42\n"
    "v_lshlrev_b32 v42, 7, v42\n"
    "buffer_load_dword v43, v42, s[84:87], 0 offen\n"
    "v_add_u32 v42, 8192, v42\n"
    "buffer_load_dword v43, v42, s[84:87], 0 offen\n"
    "v_add_u32 v42, 8192, v42\n"
    "buffer_load_dword v43, v42, s[84:87], 0 offen\n"
    "ds_read_b64_tr_b16 v[232:233], v17 offset:24576\n"
    "ds_read_b64_tr_b16 v[234:235], v17 offset:25088\n"
    "ds_read_b64_tr_b16 v[236:237], v17 offset:24584\n"
    "ds_read_b64_tr_b16 v[238:239], v17 offset:25096\n"
    "ds_read_b64_tr_b16 v[240:241], v19 offset:24576\n"
    "ds_read_b64_tr_b16 v[242:243], v19 offset:25088\n"
    "ds_read_b64_tr_b16 v[244:245], v17 offset:36864\n"
    "ds_read_b64_tr_b16 v[246:247], v17 offset:37376\n"
    "ds_read_b64_tr_b16 v[248:249], v17 offset:36872\n"
    "ds_read_b64_tr_b16 v[250:251], v17 offset:37384\n"
    "ds_read_b64_tr_b16 v[252:253], v19 offset:36864\n"
    "ds_read_b64_tr_b16 v[254:255], v19 offset:37376\n"
    "s_waitcnt lgkmcnt(0)\n"
    "s_barrier\n"
    "s_waitcnt vmcnt(0)\n"
    "v_mov_b32 v5, v11\n"
    "v_mov_b32 v12, -1\n"
    "s_mov_b32 s73, 0x80000000\n"
    "s_branch .LgDOWN_block1_1_%=\n"
    ".LgDOWN_LOOPB_%=:\n"
    ".LgDOWN_head0_%=:\n"
    "v_mfma_f32_16x16x32_bf16 v[48:51], a[0:3], v[16:19], 0\n"
    "v_mfma_f32_16x16x32_bf16 v[52:55], a[4:7], v[16:19], 0\n"
    "s_waitcnt lgkmcnt(2)\n"
    "v_cvt_pk_bf16_f32 v56, v56, v57\n"
    "v_cvt_pk_bf16_f32 v57, v58, v59\n"
    "v_mfma_f32_16x16x32_bf16 v[64:67], a[8:11], v[16:19], 0\n"
    "ds_read_b128 v[16:19], v0 offset:4096\n"
    "v_cvt_pk_bf16_f32 v58, v60, v61\n"
    "v_mfma_f32_16x16x32_bf16 v[48:51], a[12:15], v[20:23], v[48:51]\n"
    "v_cvt_pk_bf16_f32 v59, v62, v63\n"
    "v_mfma_f32_16x16x32_bf16 v[52:55], a[16:19], v[20:23], v[52:55]\n"
    "s_waitcnt lgkmcnt(2)\n"
    "v_cvt_pk_bf16_f32 v68, v68, v69\n"
    "v_cvt_pk_bf16_f32 v69, v70, v71\n"
    "v_mfma_f32_16x16x32_bf16 v[64:67], a[20:23], v[20:23], v[64:67]\n"
    "ds_read_b128 v[20:23], v0 offset:5120\n"
    "s_nop 0\n"
    "v_mfma_f32_16x16x32_bf16 v[48:51], a[24:27], v[24:27], v[48:51]\n"
    "buffer_store_dwordx4 v[56:59], v15, s[36:39], s73 offen offset:1024 sc1\n"
    "v_mfma_f32_16x16x32_bf16 v[52:55], a[28:31], v[24:27], v[52:55]\n"
    "s_waitcnt lgkmcnt(2)\n"
    "s_waitcnt vmcnt(2)\n"
    "v_add_u32 v13, v11, v3\n"
    "v_mfma_f32_16x16x32_bf16 v[64:67], a[32:35], v[24:27], v[64:67]\n"
    "ds_read_b128 v[24:27], v0 offset:6144\n"
    "v_cmp_gt_i32 vcc, s71, v13\n"
    "v_mfma_f32_16x16x32_bf16 v[48:51], a[36:39], v[28:31], v[48:51]\n"
    "buffer_atomic_smax v7, v10, s[36:39], s69 offen sc1\n"
    "v_permlane16_swap_b32 v68, v70\n"
    "v_mfma_f32_16x16x32_bf16 v[52:55], a[40:43], v[28:31], v[52:55]\n"
    "s_waitcnt lgkmcnt(2)\n"
    "v_permlane16_swap_b32 v69, v71\n"
    "v_mfma_f32_16x16x32_bf16 v[64:67], a[44:47], v[28:31], v[64:67]\n"
    "ds_read_b128 v[28:31], v0 offset:7168\n"
    "s_cbranch_vccnz .LgDOWN_poll0_%=\n"
    "v_mfma_f32_16x16x32_bf16 v[48:51], a[48:51], v[16:19], v[48:51]\n"
    "s_add_u32 m0, s59, 36864\n"
    "v_mfma_f32_16x16x32_bf16 v[52:55], a[52:55], v[16:19], v[52:55]\n"
    "s_waitcnt lgkmcnt(2)\n"
    "buffer_load_dwordx4 v1, s[36:39], s76 offen sc1 lds\n"
    "v_mfma_f32_16x16x32_bf16 v[64:67], a[56:59], v[16:19], v[64:67]\n"
    "ds_read_b128 v[16:19], v0 offset:8192\n"
    "v_mfma_f32_16x16x32_bf16 v[48:51], a[60:63], v[20:23], v[48:51]\n"
    "buffer_store_dwordx4 v[68:71], v4, s[36:39], s73 offen offset:1024 sc1\n"
    "v_mfma_f32_16x16x32_bf16 v[52:55], a[64:67], v[20:23], v[52:55]\n"
    "s_waitcnt lgkmcnt(2)\n"
    "s_add_u32 s76, s76, 16384\n"
    "s_lshr_b32 s73, s68, 1\n"
    "v_mfma_f32_16x16x32_bf16 v[64:67], a[68:71], v[20:23], v[64:67]\n"
    "ds_read_b128 v[20:23], v0 offset:9216\n"
    "s_and_b32 s73, s73, s57\n"
    "v_mfma_f32_16x16x32_bf16 v[48:51], a[72:75], v[24:27], v[48:51]\n"
    "s_add_u32 m0, s59, 40960\n"
    "s_mul_i32 s73, s73, s56\n"
    "v_mfma_f32_16x16x32_bf16 v[52:55], a[76:79], v[24:27], v[52:55]\n"
    "s_waitcnt lgkmcnt(2)\n"
    "buffer_load_dwordx4 v1, s[36:39], s76 offen sc1 lds\n"
    "s_add_u32 s73, s73, s55\n"
    "v_mfma_f32_16x16x32_bf16 v[64:67], a[80:83], v[24:27], v[64:67]\n"
    "ds_read_b128 v[24:27], v0 offset:10240\n"
    "v_mfma_f32_16x16x32_bf16 v[48:51], a[84:87], v[28:31], v[48:51]\n"
    "v_mfma_f32_16x16x32_bf16 v[52:55], a[88:91], v[28:31], v[52:55]\n"
    "s_waitcnt lgkmcnt(2)\n"
    "s_add_u32 s76, s76, 16384\n"
    "s_add_u32 m0, s59, 45056\n"
    "v_mfma_f32_16x16x32_bf16 v[64:67], a[92:95], v[28:31], v[64:67]\n"
    "ds_read_b128 v[28:31], v0 offset:11264\n"
    "buffer_load_dwordx4 v1, s[36:39], s76 offen sc1 lds\n"
    "v_mfma_f32_16x16x32_bf16 v[48:51], a[96:99], v[16:19], v[48:51]\n"
    "v_mfma_f32_16x16x32_bf16 v[52:55], a[100:103], v[16:19], v[52:55]\n"
    "s_waitcnt lgkmcnt(2)\n"
    "v_mfma_f32_16x16x32_bf16 v[64:67], a[104:107], v[16:19], v[64:67]\n"
    "ds_read_b128 v[16:19], v0 offset:12288\n"
    "v_mfma_f32_16x16x32_bf16 v[48:51], a[108:111], v[20:23], v[48:51]\n"
    "v_mfma_f32_16x16x32_bf16 v[52:55], a[112:115], v[20:23], v[52:55]\n"
    "s_waitcnt lgkmcnt(2)\n"
    "v_mfma_f32_16x16x32_bf16 v[64:67], a[116:119], v[20:23], v[64:67]\n"
    "ds_read_b128 v[20:23], v0 offset:13312\n"
    "s_add_u32 s76, s76, 16384\n"
    "v_mfma_f32_16x16x32_bf16 v[48:51], a[120:123], v[24:27], v[48:51]\n"
    "s_add_u32 m0, s59, 49152\n"
    "v_mfma_f32_16x16x32_bf16 v[52:55], a[124:127], v[24:27], v[52:55]\n"
    "s_waitcnt lgkmcnt(2)\n"
    "buffer_load_dwordx4 v1, s[36:39], s76 offen sc1 lds\n"
    "v_mfma_f32_16x16x32_bf16 v[64:67], a[128:131], v[24:27], v[64:67]\n"
    "ds_read_b128 v[24:27], v0 offset:14336\n"
    "v_mfma_f32_16x16x32_bf16 v[48:51], a[132:135], v[28:31], v[48:51]\n"
    "v_mfma_f32_16x16x32_bf16 v[52:55], a[136:139], v[28:31], v[52:55]\n"
    "s_waitcnt lgkmcnt(2)\n"
    "v_mfma_f32_16x16x32_bf16 v[64:67], a[140:143], v[28:31], v[64:67]\n"
    "ds_read_b128 v[28:31], v0 offset:15360\n"
    "s_add_u32 s76, s76, 16384\n"
    "v_mfma_f32_16x16x32_bf16 v[48:51], a[144:147], v[16:19], v[48:51]\n"
    "s_add_u32 m0, s59, 53248\n"
    "v_mfma_f32_16x16x32_bf16 v[52:55], a[148:151], v[16:19], v[52:55]\n"
    "s_waitcnt lgkmcnt(2)\n"
    "buffer_load_dwordx4 v1, s[36:39], s76 offen sc1 lds\n"
    "v_mfma_f32_16x16x32_bf16 v[64:67], a[152:155], v[16:19], v[64:67]\n"
    "ds_read_b128 v[16:19], v0 offset:16384\n"
    "v_mfma_f32_16x16x32_bf16 v[48:51], a[156:159], v[20:23], v[48:51]\n"
    "v_mfma_f32_16x16x32_bf16 v[52:55], a[160:163], v[20:23], v[52:55]\n"
    "s_waitcnt lgkmcnt(2)\n"
    "v_mfma_f32_16x16x32_bf16 v[64:67], a[164:167], v[20:23], v[64:67]\n"
    "ds_read_b128 v[20:23], v0 offset:17408\n"
    "s_add_u32 s76, s76, 16384\n"
    "v_mfma_f32_16x16x32_bf16 v[48:51], a[168:171], v[24:27], v[48:51]\n"
    "s_add_u32 m0, s59, 57344\n"
    "v_mfma_f32_16x16x32_bf16 v[52:55], a[172:175], v[24:27], v[52:55]\n"
    "s_waitcnt lgkmcnt(2)\n"
    "buffer_load_dwordx4 v1, s[36:39], s76 offen sc1 lds\n"
    "v_mfma_f32_16x16x32_bf16 v[64:67], a[176:179], v[24:27], v[64:67]\n"
    "ds_read_b128 v[24:27], v0 offset:18432\n"
    "v_mfma_f32_16x16x32_bf16 v[48:51], a[180:183], v[28:31], v[48:51]\n"
    "v_mfma_f32_16x16x32_bf16 v[52:55], a[184:187], v[28:31], v[52:55]\n"
    "s_waitcnt lgkmcnt(2)\n"
    "v_mfma_f32_16x16x32_bf16 v[64:67], a[188:191], v[28:31], v[64:67]\n"
    "ds_read_b128 v[28:31], v0 offset:19456\n"
    "v_mfma_f32_16x16x32_bf16 v[48:51], a[192:195], v[16:19], v[48:51]\n"
    "s_add_u32 s76, s76, 16384\n"
    "s_add_u32 m0, s59, 61440\n"
    "v_mfma_f32_16x16x32_bf16 v[52:55], a[196:199], v[16:19], v[52:55]\n"
    "s_waitcnt lgkmcnt(2)\n"
    "buffer_load_dwordx4 v1, s[36:39], s76 offen sc1 lds\n"
    "v_mfma_f32_16x16x32_bf16 v[64:67], a[200:203], v[16:19], v[64:67]\n"
    "ds_read_b128 v[16:19], v0 offset:20480\n"
    "v_mfma_f32_16x16x32_bf16 v[48:51], a[204:207], v[20:23], v[48:51]\n"
    "v_mfma_f32_16x16x32_bf16 v[52:55], a[208:211], v[20:23], v[52:55]\n"
    "s_waitcnt lgkmcnt(2)\n"
    "v_mfma_f32_16x16x32_bf16 v[64:67], a[212:215], v[20:23], v[64:67]\n"
    "ds_read_b128 v[20:23], v0 offset:21504\n"
    "v_mfma_f32_16x16x32_bf16 v[48:51], a[216:219], v[24:27], v[48:51]\n"
    "s_add_u32 s76, s76, 16384\n"
    "s_add_u32 m0, s59, 65536\n"
    "v_mfma_f32_16x16x32_bf16 v[52:55], a[220:223], v[24:27], v[52:55]\n"
    "s_waitcnt lgkmcnt(2)\n"
    "s_waitcnt vmcnt(6)\n"
    "buffer_atomic_smax v12, v10, s[36:39], s70 offen sc1\n"
    "v_mfma_f32_16x16x32_bf16 v[64:67], a[224:227], v[24:27], v[64:67]\n"
    "ds_read_b128 v[24:27], v0 offset:22528\n"
    "buffer_load_dwordx4 v1, s[36:39], s76 offen sc1 lds\n"
    "v_mfma_f32_16x16x32_bf16 v[48:51], a[228:231], v[28:31], v[48:51]\n"
    "v_mfma_f32_16x16x32_bf16 v[52:55], a[232:235], v[28:31], v[52:55]\n"
    "s_waitcnt lgkmcnt(2)\n"
    "v_mfma_f32_16x16x32_bf16 v[64:67], a[236:239], v[28:31], v[64:67]\n"
    "ds_read_b128 v[28:31], v0 offset:23552\n"
    "v_mfma_f32_16x16x32_bf16 v[48:51], a[240:243], v[16:19], v[48:51]\n"
    "s_add_u32 s76, s76, 16384\n"
    "s_add_u32 m0, s59, 69632\n"
    "v_mfma_f32_16x16x32_bf16 v[52:55], a[244:247], v[16:19], v[52:55]\n"
    "s_waitcnt lgkmcnt(2)\n"
    "buffer_load_dwordx4 v1, s[36:39], s76 offen sc1 lds\n"
    "v_mfma_f32_16x16x32_bf16 v[64:67], a[248:251], v[16:19], v[64:67]\n"
    "ds_read_b128 v[16:19], v0 offset:24576\n"
    "v_mfma_f32_16x16x32_bf16 v[48:51], a[252:255], v[20:23], v[48:51]\n"
    "v_mfma_f32_16x16x32_bf16 v[52:55], v[80:83], v[20:23], v[52:55]\n"
    "s_waitcnt lgkmcnt(2)\n"
    "v_mfma_f32_16x16x32_bf16 v[64:67], v[84:87], v[20:23], v[64:67]\n"
    "ds_read_b128 v[20:23], v0 offset:25600\n"
    "v_mfma_f32_16x16x32_bf16 v[48:51], v[88:91], v[24:27], v[48:51]\n"
    "v_mfma_f32_16x16x32_bf16 v[52:55], v[92:95], v[24:27], v[52:55]\n"
    "s_waitcnt lgkmcnt(2)\n"
    "v_mfma_f32_16x16x32_bf16 v[64:67], v[96:99], v[24:27], v[64:67]\n"
    "ds_read_b128 v[24:27], v0 offset:26624\n"
    "v_mfma_f32_16x16x32_bf16 v[48:51], v[100:103], v[28:31], v[48:51]\n"
    "v_mfma_f32_16x16x32_bf16 v[52:55], v[104:107], v[28:31], v[52:55]\n"
    "s_waitcnt lgkmcnt(2)\n"
    "v_mfma_f32_16x16x32_bf16 v[64:67], v[108:111], v[28:31], v[64:67]\n"
    "ds_read_b128 v[28:31], v0 offset:27648\n"
    ".LgDOWN_join0_%=:\n"
    "v_mfma_f32_16x16x32_bf16 v[48:51], v[112:115], v[16:19], v[48:51]\n"
    "s_add_u32 s71, s68, 2\n"
    "s_waitcnt vmcnt(9)\n"
    "v_mfma_f32_16x16x32_bf16 v[52:55], v[116:119], v[16:19], v[52:55]\n"
    "s_waitcnt lgkmcnt(2)\n"
    "v_add_u32 v13, v11, v3\n"
    "v_mfma_f32_16x16x32_bf16 v[64:67], v[120:123], v[16:19], v[64:67]\n"
    "ds_read_b128 v[16:19], v0 offset:28672\n"
    "v_cmp_gt_i32 vcc, s71, v13\n"
    "v_mfma_f32_16x16x32_bf16 v[48:51], v[124:127], v[20:23], v[48:51]\n"
    "v_mfma_f32_16x16x32_bf16 v[52:55], v[128:131], v[20:23], v[52:55]\n"
    "s_waitcnt lgkmcnt(2)\n"
    "v_mfma_f32_16x16x32_bf16 v[64:67], v[132:135], v[20:23], v[64:67]\n"
    "ds_read_b128 v[20:23], v0 offset:29696\n"
    "v_mfma_f32_16x16x32_bf16 v[48:51], v[136:139], v[24:27], v[48:51]\n"
    "s_or_b64 exec, vcc, s[62:63]\n"
    "buffer_atomic_smax v11, v2, s[36:39], 0 offen sc0 sc1\n"
    "s_mov_b64 exec, -1\n"
    "v_mfma_f32_16x16x32_bf16 v[52:55], v[140:143], v[24:27], v[52:55]\n"
    "s_waitcnt lgkmcnt(2)\n"
    "s_lshr_b32 s76, s71, 1\n"
    "v_mfma_f32_16x16x32_bf16 v[64:67], v[144:147], v[24:27], v[64:67]\n"
    "ds_read_b128 v[24:27], v0 offset:30720\n"
    "s_and_b32 s76, s76, s54\n"
    "v_mfma_f32_16x16x32_bf16 v[48:51], v[148:151], v[28:31], v[48:51]\n"
    "s_mul_i32 s76, s76, s53\n"
    "v_mfma_f32_16x16x32_bf16 v[52:55], v[152:155], v[28:31], v[52:55]\n"
    "s_waitcnt lgkmcnt(2)\n"
    "s_add_u32 s76, s76, s52\n"
    "v_mfma_f32_16x16x32_bf16 v[64:67], v[156:159], v[28:31], v[64:67]\n"
    "ds_read_b128 v[28:31], v0 offset:31744\n"
    "v_mfma_f32_16x16x32_bf16 v[48:51], v[160:163], v[16:19], v[48:51]\n"
    "v_mfma_f32_16x16x32_bf16 v[52:55], v[164:167], v[16:19], v[52:55]\n"
    "s_waitcnt lgkmcnt(2)\n"
    "v_mfma_f32_16x16x32_bf16 v[64:67], v[168:171], v[16:19], v[64:67]\n"
    "ds_read_b128 v[16:19], v0 offset:32768\n"
    "v_mfma_f32_16x16x32_bf16 v[48:51], v[172:175], v[20:23], v[48:51]\n"
    "v_mfma_f32_16x16x32_bf16 v[52:55], v[176:179], v[20:23], v[52:55]\n"
    "s_waitcnt lgkmcnt(2)\n"
    "v_mfma_f32_16x16x32_bf16 v[64:67], v[180:183], v[20:23], v[64:67]\n"
    "ds_read_b128 v[20:23], v0 offset:33792\n"
    "v_mfma_f32_16x16x32_bf16 v[48:51], v[184:187], v[24:27], v[48:51]\n"
    "v_mfma_f32_16x16x32_bf16 v[52:55], v[188:191], v[24:27], v[52:55]\n"
    "s_waitcnt lgkmcnt(2)\n"
    "v_mfma_f32_16x16x32_bf16 v[64:67], v[192:195], v[24:27], v[64:67]\n"
    "ds_read_b128 v[24:27], v0 offset:34816\n"
    "v_mfma_f32_16x16x32_bf16 v[48:51], v[196:199], v[28:31], v[48:51]\n"
    "v_mfma_f32_16x16x32_bf16 v[52:55], v[200:203], v[28:31], v[52:55]\n"
    "s_waitcnt lgkmcnt(2)\n"
    "v_add_u32 v13, v5, v3\n"
    "v_mfma_f32_16x16x32_bf16 v[64:67], v[204:207], v[28:31], v[64:67]\n"
    "ds_read_b128 v[28:31], v0 offset:35840\n"
    "v_cmp_gt_i32 vcc, s71, v13\n"
    "v_mfma_f32_16x16x32_bf16 v[48:51], v[208:211], v[16:19], v[48:51]\n"
    "v_mfma_f32_16x16x32_bf16 v[52:55], v[212:215], v[16:19], v[52:55]\n"
    "s_waitcnt lgkmcnt(2)\n"
    "v_mfma_f32_16x16x32_bf16 v[64:67], v[216:219], v[16:19], v[64:67]\n"
    "v_mfma_f32_16x16x32_bf16 v[48:51], v[220:223], v[20:23], v[48:51]\n"
    "s_waitcnt vmcnt(1) lgkmcnt(0)\n"
    "s_barrier\n"
    "ds_read_b128 v[16:19], v9 offset:0\n"
    "s_or_b64 exec, vcc, s[62:63]\n"
    "buffer_atomic_smax v5, v2, s[36:39], 0 offen sc0 sc1\n"
    "s_mov_b64 exec, -1\n"
    "v_mfma_f32_16x16x32_bf16 v[52:55], v[224:227], v[20:23], v[52:55]\n"
    "v_mfma_f32_16x16x32_bf16 v[64:67], v[228:231], v[20:23], v[64:67]\n"
    "ds_read_b128 v[20:23], v9 offset:1024\n"
    "v_mfma_f32_16x16x32_bf16 v[48:51], v[232:235], v[24:27], v[48:51]\n"
    "v_mfma_f32_16x16x32_bf16 v[52:55], v[236:239], v[24:27], v[52:55]\n"
    "v_mfma_f32_16x16x32_bf16 v[64:67], v[240:243], v[24:27], v[64:67]\n"
    "ds_read_b128 v[24:27], v9 offset:2048\n"
    "v_mfma_f32_16x16x32_bf16 v[48:51], v[244:247], v[28:31], v[48:51]\n"
    "s_mov_b32 s69, 0x80000000\n"
    "s_mov_b32 s70, 0\n"
    "v_mov_b32 v12, s68\n"
    "v_mfma_f32_16x16x32_bf16 v[52:55], v[248:251], v[28:31], v[52:55]\n"
    "s_waitcnt lgkmcnt(2)\n"
    "s_add_u32 s68, s68, 1\n"
    "v_mfma_f32_16x16x32_bf16 v[64:67], v[252:255], v[28:31], v[64:67]\n"
    "ds_read_b128 v[28:31], v9 offset:3072\n"
    ".LgDOWN_head1_%=:\n"
    "v_mfma_f32_16x16x32_bf16 v[56:59], a[0:3], v[16:19], 0\n"
    "v_mfma_f32_16x16x32_bf16 v[60:63], a[4:7], v[16:19], 0\n"
    "s_waitcnt lgkmcnt(2)\n"
    "v_cvt_pk_bf16_f32 v48, v48, v49\n"
    "v_cvt_pk_bf16_f32 v49, v50, v51\n"
    "v_mfma_f32_16x16x32_bf16 v[68:71], a[8:11], v[16:19], 0\n"
    "ds_read_b128 v[16:19], v9 offset:4096\n"
    "v_cvt_pk_bf16_f32 v50, v52, v53\n"
    "v_mfma_f32_16x16x32_bf16 v[56:59], a[12:15], v[20:23], v[56:59]\n"
    "v_cvt_pk_bf16_f32 v51, v54, v55\n"
    "v_mfma_f32_16x16x32_bf16 v[60:63], a[16:19], v[20:23], v[60:63]\n"
    "s_waitcnt lgkmcnt(2)\n"
    "v_cvt_pk_bf16_f32 v64, v64, v65\n"
    "v_cvt_pk_bf16_f32 v65, v66, v67\n"
    "v_mfma_f32_16x16x32_bf16 v[68:71], a[20:23], v[20:23], v[68:71]\n"
    "ds_read_b128 v[20:23], v9 offset:5120\n"
    "s_nop 0\n"
    "v_mfma_f32_16x16x32_bf16 v[56:59], a[24:27], v[24:27], v[56:59]\n"
    "buffer_store_dwordx4 v[48:51], v15, s[36:39], s73 offen sc1\n"
    "v_mfma_f32_16x16x32_bf16 v[60:63], a[28:31], v[24:27], v[60:63]\n"
    "s_waitcnt lgkmcnt(2)\n"
    "s_waitcnt vmcnt(2)\n"
    "v_add_u32 v13, v11, v3\n"
    "v_mfma_f32_16x16x32_bf16 v[68:71], a[32:35], v[24:27], v[68:71]\n"
    "ds_read_b128 v[24:27], v9 offset:6144\n"
    "v_cmp_gt_i32 vcc, s71, v13\n"
    "v_mfma_f32_16x16x32_bf16 v[56:59], a[36:39], v[28:31], v[56:59]\n"
    "buffer_atomic_smax v7, v14, s[36:39], s69 offen sc1\n"
    "v_permlane16_swap_b32 v64, v66\n"
    "v_mfma_f32_16x16x32_bf16 v[60:63], a[40:43], v[28:31], v[60:63]\n"
    "s_waitcnt lgkmcnt(2)\n"
    "v_permlane16_swap_b32 v65, v67\n"
    "v_mfma_f32_16x16x32_bf16 v[68:71], a[44:47], v[28:31], v[68:71]\n"
    "ds_read_b128 v[28:31], v9 offset:7168\n"
    "s_cbranch_vccnz .LgDOWN_poll1_%=\n"
    "v_mfma_f32_16x16x32_bf16 v[56:59], a[48:51], v[16:19], v[56:59]\n"
    "s_add_u32 m0, s59, 0\n"
    "v_mfma_f32_16x16x32_bf16 v[60:63], a[52:55], v[16:19], v[60:63]\n"
    "s_waitcnt lgkmcnt(2)\n"
    "buffer_load_dwordx4 v1, s[36:39], s76 offen sc1 lds\n"
    "v_mfma_f32_16x16x32_bf16 v[68:71], a[56:59], v[16:19], v[68:71]\n"
    "ds_read_b128 v[16:19], v9 offset:8192\n"
    "v_mfma_f32_16x16x32_bf16 v[56:59], a[60:63], v[20:23], v[56:59]\n"
    "buffer_store_dwordx4 v[64:67], v4, s[36:39], s73 offen sc1\n"
    "v_mfma_f32_16x16x32_bf16 v[60:63], a[64:67], v[20:23], v[60:63]\n"
    "s_waitcnt lgkmcnt(2)\n"
    "s_add_u32 s76, s76, 16384\n"
    "s_lshr_b32 s73, s68, 1\n"
    "v_mfma_f32_16x16x32_bf16 v[68:71], a[68:71], v[20:23], v[68:71]\n"
    "ds_read_b128 v[20:23], v9 offset:9216\n"
    "s_and_b32 s73, s73, s57\n"
    "v_mfma_f32_16x16x32_bf16 v[56:59], a[72:75], v[24:27], v[56:59]\n"
    "s_add_u32 m0, s59, 4096\n"
    "s_mul_i32 s73, s73, s56\n"
    "v_mfma_f32_16x16x32_bf16 v[60:63], a[76:79], v[24:27], v[60:63]\n"
    "s_waitcnt lgkmcnt(2)\n"
    "buffer_load_dwordx4 v1, s[36:39], s76 offen sc1 lds\n"
    "s_add_u32 s73, s73, s55\n"
    "v_mfma_f32_16x16x32_bf16 v[68:71], a[80:83], v[24:27], v[68:71]\n"
    "ds_read_b128 v[24:27], v9 offset:10240\n"
    "v_mfma_f32_16x16x32_bf16 v[56:59], a[84:87], v[28:31], v[56:59]\n"
    "v_mfma_f32_16x16x32_bf16 v[60:63], a[88:91], v[28:31], v[60:63]\n"
    "s_waitcnt lgkmcnt(2)\n"
    "s_add_u32 s76, s76, 16384\n"
    "s_add_u32 m0, s59, 8192\n"
    "v_mfma_f32_16x16x32_bf16 v[68:71], a[92:95], v[28:31], v[68:71]\n"
    "ds_read_b128 v[28:31], v9 offset:11264\n"
    "buffer_load_dwordx4 v1, s[36:39], s76 offen sc1 lds\n"
    "v_mfma_f32_16x16x32_bf16 v[56:59], a[96:99], v[16:19], v[56:59]\n"
    "v_mfma_f32_16x16x32_bf16 v[60:63], a[100:103], v[16:19], v[60:63]\n"
    "s_waitcnt lgkmcnt(2)\n"
    "v_mfma_f32_16x16x32_bf16 v[68:71], a[104:107], v[16:19], v[68:71]\n"
    "ds_read_b128 v[16:19], v9 offset:12288\n"
    "v_mfma_f32_16x16x32_bf16 v[56:59], a[108:111], v[20:23], v[56:59]\n"
    "v_mfma_f32_16x16x32_bf16 v[60:63], a[112:115], v[20:23], v[60:63]\n"
    "s_waitcnt lgkmcnt(2)\n"
    "v_mfma_f32_16x16x32_bf16 v[68:71], a[116:119], v[20:23], v[68:71]\n"
    "ds_read_b128 v[20:23], v9 offset:13312\n"
    "s_add_u32 s76, s76, 16384\n"
    "v_mfma_f32_16x16x32_bf16 v[56:59], a[120:123], v[24:27], v[56:59]\n"
    "s_add_u32 m0, s59, 12288\n"
    "v_mfma_f32_16x16x32_bf16 v[60:63], a[124:127], v[24:27], v[60:63]\n"
    "s_waitcnt lgkmcnt(2)\n"
    "buffer_load_dwordx4 v1, s[36:39], s76 offen sc1 lds\n"
    "v_mfma_f32_16x16x32_bf16 v[68:71], a[128:131], v[24:27], v[68:71]\n"
    "ds_read_b128 v[24:27], v9 offset:14336\n"
    "v_mfma_f32_16x16x32_bf16 v[56:59], a[132:135], v[28:31], v[56:59]\n"
    "v_mfma_f32_16x16x32_bf16 v[60:63], a[136:139], v[28:31], v[60:63]\n"
    "s_waitcnt lgkmcnt(2)\n"
    "v_mfma_f32_16x16x32_bf16 v[68:71], a[140:143], v[28:31], v[68:71]\n"
    "ds_read_b128 v[28:31], v9 offset:15360\n"
    "s_add_u32 s76, s76, 16384\n"
    "v_mfma_f32_16x16x32_bf16 v[56:59], a[144:147], v[16:19], v[56:59]\n"
    "s_add_u32 m0, s59, 16384\n"
    "v_mfma_f32_16x16x32_bf16 v[60:63], a[148:151], v[16:19], v[60:63]\n"
    "s_waitcnt lgkmcnt(2)\n"
    "buffer_load_dwordx4 v1, s[36:39], s76 offen sc1 lds\n"
    "v_mfma_f32_16x16x32_bf16 v[68:71], a[152:155], v[16:19], v[68:71]\n"
    "ds_read_b128 v[16:19], v9 offset:16384\n"
    "v_mfma_f32_16x16x32_bf16 v[56:59], a[156:159], v[20:23], v[56:59]\n"
    "v_mfma_f32_16x16x32_bf16 v[60:63], a[160:163], v[20:23], v[60:63]\n"
    "s_waitcnt lgkmcnt(2)\n"
    "v_mfma_f32_16x16x32_bf16 v[68:71], a[164:167], v[20:23], v[68:71]\n"
    "ds_read_b128 v[20:23], v9 offset:17408\n"
    "s_add_u32 s76, s76, 16384\n"
    "v_mfma_f32_16x16x32_bf16 v[56:59], a[168:171], v[24:27], v[56:59]\n"
    "s_add_u32 m0, s59, 20480\n"
    "v_mfma_f32_16x16x32_bf16 v[60:63], a[172:175], v[24:27], v[60:63]\n"
    "s_waitcnt lgkmcnt(2)\n"
    "buffer_load_dwordx4 v1, s[36:39], s76 offen sc1 lds\n"
    "v_mfma_f32_16x16x32_bf16 v[68:71], a[176:179], v[24:27], v[68:71]\n"
    "ds_read_b128 v[24:27], v9 offset:18432\n"
    "v_mfma_f32_16x16x32_bf16 v[56:59], a[180:183], v[28:31], v[56:59]\n"
    "v_mfma_f32_16x16x32_bf16 v[60:63], a[184:187], v[28:31], v[60:63]\n"
    "s_waitcnt lgkmcnt(2)\n"
    "v_mfma_f32_16x16x32_bf16 v[68:71], a[188:191], v[28:31], v[68:71]\n"
    "ds_read_b128 v[28:31], v9 offset:19456\n"
    "v_mfma_f32_16x16x32_bf16 v[56:59], a[192:195], v[16:19], v[56:59]\n"
    "s_add_u32 s76, s76, 16384\n"
    "s_add_u32 m0, s59, 24576\n"
    "v_mfma_f32_16x16x32_bf16 v[60:63], a[196:199], v[16:19], v[60:63]\n"
    "s_waitcnt lgkmcnt(2)\n"
    "buffer_load_dwordx4 v1, s[36:39], s76 offen sc1 lds\n"
    "v_mfma_f32_16x16x32_bf16 v[68:71], a[200:203], v[16:19], v[68:71]\n"
    "ds_read_b128 v[16:19], v9 offset:20480\n"
    "v_mfma_f32_16x16x32_bf16 v[56:59], a[204:207], v[20:23], v[56:59]\n"
    "v_mfma_f32_16x16x32_bf16 v[60:63], a[208:211], v[20:23], v[60:63]\n"
    "s_waitcnt lgkmcnt(2)\n"
    "v_mfma_f32_16x16x32_bf16 v[68:71], a[212:215], v[20:23], v[68:71]\n"
    "ds_read_b128 v[20:23], v9 offset:21504\n"
    "v_mfma_f32_16x16x32_bf16 v[56:59], a[216:219], v[24:27], v[56:59]\n"
    "s_add_u32 s76, s76, 16384\n"
    "s_add_u32 m0, s59, 28672\n"
    "v_mfma_f32_16x16x32_bf16 v[60:63], a[220:223], v[24:27], v[60:63]\n"
    "s_waitcnt lgkmcnt(2)\n"
    "s_waitcnt vmcnt(6)\n"
    "buffer_atomic_smax v12, v14, s[36:39], s70 offen sc1\n"
    "v_mfma_f32_16x16x32_bf16 v[68:71], a[224:227], v[24:27], v[68:71]\n"
    "ds_read_b128 v[24:27], v9 offset:22528\n"
    "buffer_load_dwordx4 v1, s[36:39], s76 offen sc1 lds\n"
    "v_mfma_f32_16x16x32_bf16 v[56:59], a[228:231], v[28:31], v[56:59]\n"
    "v_mfma_f32_16x16x32_bf16 v[60:63], a[232:235], v[28:31], v[60:63]\n"
    "s_waitcnt lgkmcnt(2)\n"
    "v_mfma_f32_16x16x32_bf16 v[68:71], a[236:239], v[28:31], v[68:71]\n"
    "ds_read_b128 v[28:31], v9 offset:23552\n"
    "v_mfma_f32_16x16x32_bf16 v[56:59], a[240:243], v[16:19], v[56:59]\n"
    "s_add_u32 s76, s76, 16384\n"
    "s_add_u32 m0, s59, 32768\n"
    "v_mfma_f32_16x16x32_bf16 v[60:63], a[244:247], v[16:19], v[60:63]\n"
    "s_waitcnt lgkmcnt(2)\n"
    "buffer_load_dwordx4 v1, s[36:39], s76 offen sc1 lds\n"
    "v_mfma_f32_16x16x32_bf16 v[68:71], a[248:251], v[16:19], v[68:71]\n"
    "ds_read_b128 v[16:19], v9 offset:24576\n"
    "v_mfma_f32_16x16x32_bf16 v[56:59], a[252:255], v[20:23], v[56:59]\n"
    "v_mfma_f32_16x16x32_bf16 v[60:63], v[80:83], v[20:23], v[60:63]\n"
    "s_waitcnt lgkmcnt(2)\n"
    "v_mfma_f32_16x16x32_bf16 v[68:71], v[84:87], v[20:23], v[68:71]\n"
    "ds_read_b128 v[20:23], v9 offset:25600\n"
    "v_mfma_f32_16x16x32_bf16 v[56:59], v[88:91], v[24:27], v[56:59]\n"
    "v_mfma_f32_16x16x32_bf16 v[60:63], v[92:95], v[24:27], v[60:63]\n"
    "s_waitcnt lgkmcnt(2)\n"
    "v_mfma_f32_16x16x32_bf16 v[68:71], v[96:99], v[24:27], v[68:71]\n"
    "ds_read_b128 v[24:27], v9 offset:26624\n"
    "v_mfma_f32_16x16x32_bf16 v[56:59], v[100:103], v[28:31], v[56:59]\n"
    "v_mfma_f32_16x16x32_bf16 v[60:63], v[104:107], v[28:31], v[60:63]\n"
    "s_waitcnt lgkmcnt(2)\n"
    "v_mfma_f32_16x16x32_bf16 v[68:71], v[108:111], v[28:31], v[68:71]\n"
    "ds_read_b128 v[28:31], v9 offset:27648\n"
    ".LgDOWN_join1_%=:\n"
    "v_mfma_f32_16x16x32_bf16 v[56:59], v[112:115], v[16:19], v[56:59]\n"
    "s_add_u32 s71, s68, 2\n"
    "s_waitcnt vmcnt(9)\n"
    "v_mfma_f32_16x16x32_bf16 v[60:63], v[116:119], v[16:19], v[60:63]\n"
    "s_waitcnt lgkmcnt(2)\n"
    "v_add_u32 v13, v11, v3\n"
    "v_mfma_f32_16x16x32_bf16 v[68:71], v[120:123], v[16:19], v[68:71]\n"
    "ds_read_b128 v[16:19], v9 offset:28672\n"
    "v_cmp_gt_i32 vcc, s71, v13\n"
    "v_mfma_f32_16x16x32_bf16 v[56:59], v[124:127], v[20:23], v[56:59]\n"
    "v_mfma_f32_16x16x32_bf16 v[60:63], v[128:131], v[20:23], v[60:63]\n"
    "s_waitcnt lgkmcnt(2)\n"
    "v_mfma_f32_16x16x32_bf16 v[68:71], v[132:135], v[20:23], v[68:71]\n"
    "ds_read_b128 v[20:23], v9 offset:29696\n"
    "v_mfma_f32_16x16x32_bf16 v[56:59], v[136:139], v[24:27], v[56:59]\n"
    "s_or_b64 exec, vcc, s[62:63]\n"
    "buffer_atomic_smax v11, v2, s[36:39], 0 offen sc0 sc1\n"
    "s_mov_b64 exec, -1\n"
    "v_mfma_f32_16x16x32_bf16 v[60:63], v[140:143], v[24:27], v[60:63]\n"
    "s_waitcnt lgkmcnt(2)\n"
    "s_lshr_b32 s76, s71, 1\n"
    "v_mfma_f32_16x16x32_bf16 v[68:71], v[144:147], v[24:27], v[68:71]\n"
    "ds_read_b128 v[24:27], v9 offset:30720\n"
    "s_and_b32 s76, s76, s54\n"
    "v_mfma_f32_16x16x32_bf16 v[56:59], v[148:151], v[28:31], v[56:59]\n"
    "s_mul_i32 s76, s76, s53\n"
    "v_mfma_f32_16x16x32_bf16 v[60:63], v[152:155], v[28:31], v[60:63]\n"
    "s_waitcnt lgkmcnt(2)\n"
    "s_add_u32 s76, s76, s52\n"
    "v_mfma_f32_16x16x32_bf16 v[68:71], v[156:159], v[28:31], v[68:71]\n"
    "ds_read_b128 v[28:31], v9 offset:31744\n"
    "s_add_u32 s76, s76, 1024\n"
    "v_mfma_f32_16x16x32_bf16 v[56:59], v[160:163], v[16:19], v[56:59]\n"
    "v_mfma_f32_16x16x32_bf16 v[60:63], v[164:167], v[16:19], v[60:63]\n"
    "s_waitcnt lgkmcnt(2)\n"
    "v_mfma_f32_16x16x32_bf16 v[68:71], v[168:171], v[16:19], v[68:71]\n"
    "ds_read_b128 v[16:19], v9 offset:32768\n"
    "v_mfma_f32_16x16x32_bf16 v[56:59], v[172:175], v[20:23], v[56:59]\n"
    "v_mfma_f32_16x16x32_bf16 v[60:63], v[176:179], v[20:23], v[60:63]\n"
    "s_waitcnt lgkmcnt(2)\n"
    "v_mfma_f32_16x16x32_bf16 v[68:71], v[180:183], v[20:23], v[68:71]\n"
    "ds_read_b128 v[20:23], v9 offset:33792\n"
    "v_mfma_f32_16x16x32_bf16 v[56:59], v[184:187], v[24:27], v[56:59]\n"
    "v_mfma_f32_16x16x32_bf16 v[60:63], v[188:191], v[24:27], v[60:63]\n"
    "s_waitcnt lgkmcnt(2)\n"
    "v_mfma_f32_16x16x32_bf16 v[68:71], v[192:195], v[24:27], v[68:71]\n"
    "ds_read_b128 v[24:27], v9 offset:34816\n"
    "v_mfma_f32_16x16x32_bf16 v[56:59], v[196:199], v[28:31], v[56:59]\n"
    "v_mfma_f32_16x16x32_bf16 v[60:63], v[200:203], v[28:31], v[60:63]\n"
    "s_waitcnt lgkmcnt(2)\n"
    "v_add_u32 v13, v5, v3\n"
    "v_mfma_f32_16x16x32_bf16 v[68:71], v[204:207], v[28:31], v[68:71]\n"
    "ds_read_b128 v[28:31], v9 offset:35840\n"
    "v_cmp_gt_i32 vcc, s71, v13\n"
    "v_mfma_f32_16x16x32_bf16 v[56:59], v[208:211], v[16:19], v[56:59]\n"
    "v_mfma_f32_16x16x32_bf16 v[60:63], v[212:215], v[16:19], v[60:63]\n"
    "s_waitcnt lgkmcnt(2)\n"
    "v_mfma_f32_16x16x32_bf16 v[68:71], v[216:219], v[16:19], v[68:71]\n"
    "v_mfma_f32_16x16x32_bf16 v[56:59], v[220:223], v[20:23], v[56:59]\n"
    "s_waitcnt vmcnt(1) lgkmcnt(0)\n"
    "s_barrier\n"
    "ds_read_b128 v[16:19], v0 offset:0\n"
    "s_or_b64 exec, vcc, s[62:63]\n"
    "buffer_atomic_smax v5, v2, s[36:39], 0 offen sc0 sc1\n"
    "s_mov_b64 exec, -1\n"
    "v_mfma_f32_16x16x32_bf16 v[60:63], v[224:227], v[20:23], v[60:63]\n"
    "v_mfma_f32_16x16x32_bf16 v[68:71], v[228:231], v[20:23], v[68:71]\n"
    "ds_read_b128 v[20:23], v0 offset:1024\n"
    "v_mfma_f32_16x16x32_bf16 v[56:59], v[232:235], v[24:27], v[56:59]\n"
    "v_mfma_f32_16x16x32_bf16 v[60:63], v[236:239], v[24:27], v[60:63]\n"
    "v_mfma_f32_16x16x32_bf16 v[68:71], v[240:243], v[24:27], v[68:71]\n"
    "ds_read_b128 v[24:27], v0 offset:2048\n"
    "v_mfma_f32_16x16x32_bf16 v[56:59], v[244:247], v[28:31], v[56:59]\n"
    "s_mov_b32 s69, 0x80000000\n"
    "s_mov_b32 s70, 0\n"
    "v_mov_b32 v12, s68\n"
    "v_mfma_f32_16x16x32_bf16 v[60:63], v[248:251], v[28:31], v[60:63]\n"
    "s_waitcnt lgkmcnt(2)\n"
    "s_add_u32 s68, s68, 1\n"
    "v_mfma_f32_16x16x32_bf16 v[68:71], v[252:255], v[28:31], v[68:71]\n"
    "ds_read_b128 v[28:31], v0 offset:3072\n"
    "s_branch .LgDOWN_head0_%=\n"
    ".LgDOWN_LOOPE_%=:\n"
    ".LgDOWN_done_%=:\n"
    "s_waitcnt vmcnt(0)\n"
    "buffer_atomic_smax v12, v10, s[36:39], 0 offen sc1\n"
    "s_waitcnt vmcnt(0)\n"
    "s_branch .LgDOWN_FIN_%=\n"
    ".LgDOWN_poll0_%=:\n"
    "v_mfma_f32_16x16x32_bf16 v[48:51], a[48:51], v[16:19], v[48:51]\n"
    "s_or_b64 exec, vcc, s[62:63]\n"
    "buffer_atomic_smax v11, v2, s[36:39], 0 offen sc0 sc1\n"
    "s_mov_b64 exec, -1\n"
    "v_mfma_f32_16x16x32_bf16 v[52:55], a[52:55], v[16:19], v[52:55]\n"
    "s_waitcnt lgkmcnt(2)\n"
    "v_mfma_f32_16x16x32_bf16 v[64:67], a[56:59], v[16:19], v[64:67]\n"
    "ds_read_b128 v[16:19], v0 offset:8192\n"
    "v_mfma_f32_16x16x32_bf16 v[48:51], a[60:63], v[20:23], v[48:51]\n"
    "buffer_store_dwordx4 v[68:71], v4, s[36:39], s73 offen offset:1024 sc1\n"
    "v_mfma_f32_16x16x32_bf16 v[52:55], a[64:67], v[20:23], v[52:55]\n"
    "s_waitcnt lgkmcnt(2)\n"
    "s_lshr_b32 s73, s68, 1\n"
    "v_mfma_f32_16x16x32_bf16 v[64:67], a[68:71], v[20:23], v[64:67]\n"
    "ds_read_b128 v[20:23], v0 offset:9216\n"
    "s_and_b32 s73, s73, s57\n"
    "v_mfma_f32_16x16x32_bf16 v[48:51], a[72:75], v[24:27], v[48:51]\n"
    "s_mul_i32 s73, s73, s56\n"
    "v_mfma_f32_16x16x32_bf16 v[52:55], a[76:79], v[24:27], v[52:55]\n"
    "s_waitcnt lgkmcnt(2)\n"
    "s_add_u32 s73, s73, s55\n"
    "v_mfma_f32_16x16x32_bf16 v[64:67], a[80:83], v[24:27], v[64:67]\n"
    "ds_read_b128 v[24:27], v0 offset:10240\n"
    "v_mfma_f32_16x16x32_bf16 v[48:51], a[84:87], v[28:31], v[48:51]\n"
    "v_mfma_f32_16x16x32_bf16 v[52:55], a[88:91], v[28:31], v[52:55]\n"
    "s_waitcnt lgkmcnt(2)\n"
    "v_mfma_f32_16x16x32_bf16 v[64:67], a[92:95], v[28:31], v[64:67]\n"
    "ds_read_b128 v[28:31], v0 offset:11264\n"
    "v_mfma_f32_16x16x32_bf16 v[48:51], a[96:99], v[16:19], v[48:51]\n"
    "v_mfma_f32_16x16x32_bf16 v[52:55], a[100:103], v[16:19], v[52:55]\n"
    "s_waitcnt lgkmcnt(2)\n"
    "v_mfma_f32_16x16x32_bf16 v[64:67], a[104:107], v[16:19], v[64:67]\n"
    "ds_read_b128 v[16:19], v0 offset:12288\n"
    "v_mfma_f32_16x16x32_bf16 v[48:51], a[108:111], v[20:23], v[48:51]\n"
    "v_mfma_f32_16x16x32_bf16 v[52:55], a[112:115], v[20:23], v[52:55]\n"
    "s_waitcnt lgkmcnt(2)\n"
    "s_waitcnt vmcnt(4)\n"
    "v_add_u32 v13, v5, v3\n"
    "v_mfma_f32_16x16x32_bf16 v[64:67], a[116:119], v[20:23], v[64:67]\n"
    "ds_read_b128 v[20:23], v0 offset:13312\n"
    "v_cmp_gt_i32 vcc, s71, v13\n"
    "v_mfma_f32_16x16x32_bf16 v[48:51], a[120:123], v[24:27], v[48:51]\n"
    "v_mfma_f32_16x16x32_bf16 v[52:55], a[124:127], v[24:27], v[52:55]\n"
    "s_waitcnt lgkmcnt(2)\n"
    "v_mfma_f32_16x16x32_bf16 v[64:67], a[128:131], v[24:27], v[64:67]\n"
    "ds_read_b128 v[24:27], v0 offset:14336\n"
    "s_cbranch_vccz .LgDOWN_dma0_33_%=\n"
    "v_mfma_f32_16x16x32_bf16 v[48:51], a[132:135], v[28:31], v[48:51]\n"
    "s_or_b64 exec, vcc, s[62:63]\n"
    "buffer_atomic_smax v5, v2, s[36:39], 0 offen sc0 sc1\n"
    "s_mov_b64 exec, -1\n"
    "v_mfma_f32_16x16x32_bf16 v[52:55], a[136:139], v[28:31], v[52:55]\n"
    "s_waitcnt lgkmcnt(2)\n"
    "v_mfma_f32_16x16x32_bf16 v[64:67], a[140:143], v[28:31], v[64:67]\n"
    "ds_read_b128 v[28:31], v0 offset:15360\n"
    "v_mfma_f32_16x16x32_bf16 v[48:51], a[144:147], v[16:19], v[48:51]\n"
    "v_mfma_f32_16x16x32_bf16 v[52:55], a[148:151], v[16:19], v[52:55]\n"
    "s_waitcnt lgkmcnt(2)\n"
    "v_mfma_f32_16x16x32_bf16 v[64:67], a[152:155], v[16:19], v[64:67]\n"
    "ds_read_b128 v[16:19], v0 offset:16384\n"
    "v_mfma_f32_16x16x32_bf16 v[48:51], a[156:159], v[20:23], v[48:51]\n"
    "v_mfma_f32_16x16x32_bf16 v[52:55], a[160:163], v[20:23], v[52:55]\n"
    "s_waitcnt lgkmcnt(2)\n"
    "v_mfma_f32_16x16x32_bf16 v[64:67], a[164:167], v[20:23], v[64:67]\n"
    "ds_read_b128 v[20:23], v0 offset:17408\n"
    "v_mfma_f32_16x16x32_bf16 v[48:51], a[168:171], v[24:27], v[48:51]\n"
    "v_mfma_f32_16x16x32_bf16 v[52:55], a[172:175], v[24:27], v[52:55]\n"
    "s_waitcnt lgkmcnt(2)\n"
    "v_mfma_f32_16x16x32_bf16 v[64:67], a[176:179], v[24:27], v[64:67]\n"
    "ds_read_b128 v[24:27], v0 offset:18432\n"
    "v_mfma_f32_16x16x32_bf16 v[48:51], a[180:183], v[28:31], v[48:51]\n"
    "v_mfma_f32_16x16x32_bf16 v[52:55], a[184:187], v[28:31], v[52:55]\n"
    "s_waitcnt lgkmcnt(2)\n"
    "v_mfma_f32_16x16x32_bf16 v[64:67], a[188:191], v[28:31], v[64:67]\n"
    "ds_read_b128 v[28:31], v0 offset:19456\n"
    "v_mfma_f32_16x16x32_bf16 v[48:51], a[192:195], v[16:19], v[48:51]\n"
    "v_mfma_f32_16x16x32_bf16 v[52:55], a[196:199], v[16:19], v[52:55]\n"
    "s_waitcnt lgkmcnt(2)\n"
    "s_waitcnt vmcnt(2)\n"
    "v_add_u32 v13, v11, v3\n"
    "v_mfma_f32_16x16x32_bf16 v[64:67], a[200:203], v[16:19], v[64:67]\n"
    "ds_read_b128 v[16:19], v0 offset:20480\n"
    "v_cmp_gt_i32 vcc, s71, v13\n"
    "v_mfma_f32_16x16x32_bf16 v[48:51], a[204:207], v[20:23], v[48:51]\n"
    "v_mfma_f32_16x16x32_bf16 v[52:55], a[208:211], v[20:23], v[52:55]\n"
    "s_waitcnt lgkmcnt(2)\n"
    "v_mfma_f32_16x16x32_bf16 v[64:67], a[212:215], v[20:23], v[64:67]\n"
    "ds_read_b128 v[20:23], v0 offset:21504\n"
    "s_cbranch_vccz .LgDOWN_dma0_54_%=\n"
    "v_mfma_f32_16x16x32_bf16 v[48:51], a[216:219], v[24:27], v[48:51]\n"
    "s_or_b64 exec, vcc, s[62:63]\n"
    "buffer_atomic_smax v11, v2, s[36:39], 0 offen sc0 sc1\n"
    "s_mov_b64 exec, -1\n"
    "v_mfma_f32_16x16x32_bf16 v[52:55], a[220:223], v[24:27], v[52:55]\n"
    "s_waitcnt lgkmcnt(2)\n"
    "s_waitcnt vmcnt(2)\n"
    "buffer_atomic_smax v12, v10, s[36:39], s70 offen sc1\n"
    "v_mfma_f32_16x16x32_bf16 v[64:67], a[224:227], v[24:27], v[64:67]\n"
    "ds_read_b128 v[24:27], v0 offset:22528\n"
    "v_mfma_f32_16x16x32_bf16 v[48:51], a[228:231], v[28:31], v[48:51]\n"
    "v_mfma_f32_16x16x32_bf16 v[52:55], a[232:235], v[28:31], v[52:55]\n"
    "s_waitcnt lgkmcnt(2)\n"
    "v_mfma_f32_16x16x32_bf16 v[64:67], a[236:239], v[28:31], v[64:67]\n"
    "ds_read_b128 v[28:31], v0 offset:23552\n"
    "v_mfma_f32_16x16x32_bf16 v[48:51], a[240:243], v[16:19], v[48:51]\n"
    "v_mfma_f32_16x16x32_bf16 v[52:55], a[244:247], v[16:19], v[52:55]\n"
    "s_waitcnt lgkmcnt(2)\n"
    "v_mfma_f32_16x16x32_bf16 v[64:67], a[248:251], v[16:19], v[64:67]\n"
    "ds_read_b128 v[16:19], v0 offset:24576\n"
    "v_mfma_f32_16x16x32_bf16 v[48:51], a[252:255], v[20:23], v[48:51]\n"
    "v_mfma_f32_16x16x32_bf16 v[52:55], v[80:83], v[20:23], v[52:55]\n"
    "s_waitcnt lgkmcnt(2)\n"
    "v_mfma_f32_16x16x32_bf16 v[64:67], v[84:87], v[20:23], v[64:67]\n"
    "ds_read_b128 v[20:23], v0 offset:25600\n"
    "v_mfma_f32_16x16x32_bf16 v[48:51], v[88:91], v[24:27], v[48:51]\n"
    "v_mfma_f32_16x16x32_bf16 v[52:55], v[92:95], v[24:27], v[52:55]\n"
    "s_waitcnt lgkmcnt(2)\n"
    "v_mfma_f32_16x16x32_bf16 v[64:67], v[96:99], v[24:27], v[64:67]\n"
    "ds_read_b128 v[24:27], v0 offset:26624\n"
    "v_mfma_f32_16x16x32_bf16 v[48:51], v[100:103], v[28:31], v[48:51]\n"
    "v_mfma_f32_16x16x32_bf16 v[52:55], v[104:107], v[28:31], v[52:55]\n"
    "s_waitcnt lgkmcnt(2)\n"
    "s_waitcnt vmcnt(2)\n"
    "v_add_u32 v13, v5, v3\n"
    "v_mfma_f32_16x16x32_bf16 v[64:67], v[108:111], v[28:31], v[64:67]\n"
    "ds_read_b128 v[28:31], v0 offset:27648\n"
    "v_cmp_gt_i32 vcc, s71, v13\n"
    "v_mfma_f32_16x16x32_bf16 v[48:51], v[112:115], v[16:19], v[48:51]\n"
    "v_mfma_f32_16x16x32_bf16 v[52:55], v[116:119], v[16:19], v[52:55]\n"
    "s_waitcnt lgkmcnt(2)\n"
    "v_mfma_f32_16x16x32_bf16 v[64:67], v[120:123], v[16:19], v[64:67]\n"
    "ds_read_b128 v[16:19], v0 offset:28672\n"
    "s_cbranch_vccz .LgDOWN_dma0_75_%=\n"
    "v_mfma_f32_16x16x32_bf16 v[48:51], v[124:127], v[20:23], v[48:51]\n"
    "s_or_b64 exec, vcc, s[62:63]\n"
    "buffer_atomic_smax v5, v2, s[36:39], 0 offen sc0 sc1\n"
    "s_mov_b64 exec, -1\n"
    "v_mfma_f32_16x16x32_bf16 v[52:55], v[128:131], v[20:23], v[52:55]\n"
    "s_waitcnt lgkmcnt(2)\n"
    "v_mfma_f32_16x16x32_bf16 v[64:67], v[132:135], v[20:23], v[64:67]\n"
    "ds_read_b128 v[20:23], v0 offset:29696\n"
    "v_mfma_f32_16x16x32_bf16 v[48:51], v[136:139], v[24:27], v[48:51]\n"
    "v_mfma_f32_16x16x32_bf16 v[52:55], v[140:143], v[24:27], v[52:55]\n"
    "s_waitcnt lgkmcnt(2)\n"
    "v_mfma_f32_16x16x32_bf16 v[64:67], v[144:147], v[24:27], v[64:67]\n"
    "ds_read_b128 v[24:27], v0 offset:30720\n"
    "v_mfma_f32_16x16x32_bf16 v[48:51], v[148:151], v[28:31], v[48:51]\n"
    "v_mfma_f32_16x16x32_bf16 v[52:55], v[152:155], v[28:31], v[52:55]\n"
    "s_waitcnt lgkmcnt(2)\n"
    "v_mfma_f32_16x16x32_bf16 v[64:67], v[156:159], v[28:31], v[64:67]\n"
    "ds_read_b128 v[28:31], v0 offset:31744\n"
    "v_mfma_f32_16x16x32_bf16 v[48:51], v[160:163], v[16:19], v[48:51]\n"
    "v_mfma_f32_16x16x32_bf16 v[52:55], v[164:167], v[16:19], v[52:55]\n"
    "s_waitcnt lgkmcnt(2)\n"
    "v_mfma_f32_16x16x32_bf16 v[64:67], v[168:171], v[16:19], v[64:67]\n"
    "ds_read_b128 v[16:19], v0 offset:32768\n"
    "v_mfma_f32_16x16x32_bf16 v[48:51], v[172:175], v[20:23], v[48:51]\n"
    "v_mfma_f32_16x16x32_bf16 v[52:55], v[176:179], v[20:23], v[52:55]\n"
    "s_waitcnt lgkmcnt(2)\n"
    "v_mfma_f32_16x16x32_bf16 v[64:67], v[180:183], v[20:23], v[64:67]\n"
    "ds_read_b128 v[20:23], v0 offset:33792\n"
    "v_mfma_f32_16x16x32_bf16 v[48:51], v[184:187], v[24:27], v[48:51]\n"
    "v_mfma_f32_16x16x32_bf16 v[52:55], v[188:191], v[24:27], v[52:55]\n"
    "s_waitcnt lgkmcnt(2)\n"
    "s_waitcnt vmcnt(2)\n"
    "v_add_u32 v13, v11, v3\n"
    "v_mfma_f32_16x16x32_bf16 v[64:67], v[192:195], v[24:27], v[64:67]\n"
    "ds_read_b128 v[24:27], v0 offset:34816\n"
    "v_cmp_gt_i32 vcc, s71, v13\n"
    "v_mfma_f32_16x16x32_bf16 v[48:51], v[196:199], v[28:31], v[48:51]\n"
    "v_mfma_f32_16x16x32_bf16 v[52:55], v[200:203], v[28:31], v[52:55]\n"
    "s_waitcnt lgkmcnt(2)\n"
    "v_mfma_f32_16x16x32_bf16 v[64:67], v[204:207], v[28:31], v[64:67]\n"
    "ds_read_b128 v[28:31], v0 offset:35840\n"
    "s_cbranch_vccz .LgDOWN_dma0_96_%=\n"
    "v_mfma_f32_16x16x32_bf16 v[48:51], v[208:211], v[16:19], v[48:51]\n"
    "s_or_b64 exec, vcc, s[62:63]\n"
    "buffer_atomic_smax v11, v2, s[36:39], 0 offen sc0 sc1\n"
    "s_mov_b64 exec, -1\n"
    "v_mfma_f32_16x16x32_bf16 v[52:55], v[212:215], v[16:19], v[52:55]\n"
    "s_waitcnt lgkmcnt(2)\n"
    "v_mfma_f32_16x16x32_bf16 v[64:67], v[216:219], v[16:19], v[64:67]\n"
    "v_mfma_f32_16x16x32_bf16 v[48:51], v[220:223], v[20:23], v[48:51]\n"
    "v_mfma_f32_16x16x32_bf16 v[52:55], v[224:227], v[20:23], v[52:55]\n"
    "s_waitcnt lgkmcnt(1)\n"
    "v_mfma_f32_16x16x32_bf16 v[64:67], v[228:231], v[20:23], v[64:67]\n"
    "v_mfma_f32_16x16x32_bf16 v[48:51], v[232:235], v[24:27], v[48:51]\n"
    "v_mfma_f32_16x16x32_bf16 v[52:55], v[236:239], v[24:27], v[52:55]\n"
    "s_waitcnt lgkmcnt(0)\n"
    "v_mfma_f32_16x16x32_bf16 v[64:67], v[240:243], v[24:27], v[64:67]\n"
    "v_mfma_f32_16x16x32_bf16 v[48:51], v[244:247], v[28:31], v[48:51]\n"
    "v_mfma_f32_16x16x32_bf16 v[52:55], v[248:251], v[28:31], v[52:55]\n"
    "v_mfma_f32_16x16x32_bf16 v[64:67], v[252:255], v[28:31], v[64:67]\n"
    "v_cvt_pk_bf16_f32 v48, v48, v49\n"
    "v_cvt_pk_bf16_f32 v49, v50, v51\n"
    "s_nop 1\n"
    "v_cvt_pk_bf16_f32 v50, v52, v53\n"
    "v_cvt_pk_bf16_f32 v51, v54, v55\n"
    "buffer_store_dwordx4 v[48:51], v15, s[36:39], s73 offen sc1\n"
    "s_nop 0\n"
    "v_cvt_pk_bf16_f32 v64, v64, v65\n"
    "v_cvt_pk_bf16_f32 v65, v66, v67\n"
    "s_nop 0\n"
    "v_permlane16_swap_b32 v64, v66\n"
    "v_permlane16_swap_b32 v65, v67\n"
    "buffer_store_dwordx4 v[64:67], v4, s[36:39], s73 offen sc1\n"
    "v_mov_b32 v12, s68\n"
    "s_mov_b32 s73, 0x80000000\n"
    "s_mov_b32 s68, s71\n"
    "s_waitcnt vmcnt(3)\n"
    "v_add_u32 v13, v5, v3\n"
    "v_cmp_gt_i32 vcc, s71, v13\n"
    "s_cbranch_vccnz .LgDOWN_idle0_%=\n"
    "s_add_u32 m0, s59, 36864\n"
    "s_nop 0\n"
    "buffer_load_dwordx4 v1, s[36:39], s76 offen sc1 lds\n"
    "s_add_u32 m0, s59, 40960\n"
    "s_add_u32 s76, s76, 16384\n"
    "buffer_load_dwordx4 v1, s[36:39], s76 offen sc1 lds\n"
    "s_add_u32 m0, s59, 45056\n"
    "s_add_u32 s76, s76, 16384\n"
    "buffer_load_dwordx4 v1, s[36:39], s76 offen sc1 lds\n"
    "s_add_u32 m0, s59, 49152\n"
    "s_add_u32 s76, s76, 16384\n"
    "buffer_load_dwordx4 v1, s[36:39], s76 offen sc1 lds\n"
    "s_add_u32 m0, s59, 53248\n"
    "s_add_u32 s76, s76, 16384\n"
    "buffer_load_dwordx4 v1, s[36:39], s76 offen sc1 lds\n"
    "s_add_u32 m0, s59, 57344\n"
    "s_add_u32 s76, s76, 16384\n"
    "buffer_load_dwordx4 v1, s[36:39], s76 offen sc1 lds\n"
    "s_add_u32 m0, s59, 61440\n"
    "s_add_u32 s76, s76, 16384\n"
    "buffer_load_dwordx4 v1, s[36:39], s76 offen sc1 lds\n"
    "s_add_u32 m0, s59, 65536\n"
    "s_add_u32 s76, s76, 16384\n"
    "buffer_load_dwordx4 v1, s[36:39], s76 offen sc1 lds\n"
    "s_add_u32 m0, s59, 69632\n"
    "s_add_u32 s76, s76, 16384\n"
    "buffer_load_dwordx4 v1, s[36:39], s76 offen sc1 lds\n"
    "s_waitcnt vmcnt(9)\n"
    "buffer_atomic_smax v12, v14, s[36:39], 0 offen sc1\n"
    "s_mov_b32 s69, 0x80000000\n"
    "s_mov_b32 s70, 0x80000000\n"
    "v_mov_b32 v7, v12\n"
    "s_waitcnt vmcnt(12)\n"
    "s_add_u32 s71, s68, 1\n"
    "v_add_u32 v13, v11, v3\n"
    "v_cmp_gt_i32 vcc, s71, v13\n"
    "s_nop 0\n"
    "s_or_b64 exec, vcc, s[62:63]\n"
    "buffer_atomic_smax v11, v2, s[36:39], 0 offen sc0 sc1\n"
    "s_mov_b64 exec, -1\n"
    "s_waitcnt vmcnt(2)\n"
    "s_branch .LgDOWN_bar1_%=\n"
    ".LgDOWN_dma0_33_%=:\n"
    "v_mfma_f32_16x16x32_bf16 v[48:51], a[132:135], v[28:31], v[48:51]\n"
    "s_add_u32 m0, s59, 36864\n"
    "v_mfma_f32_16x16x32_bf16 v[52:55], a[136:139], v[28:31], v[52:55]\n"
    "s_waitcnt lgkmcnt(2)\n"
    "buffer_load_dwordx4 v1, s[36:39], s76 offen sc1 lds\n"
    "v_mfma_f32_16x16x32_bf16 v[64:67], a[140:143], v[28:31], v[64:67]\n"
    "ds_read_b128 v[28:31], v0 offset:15360\n"
    "s_add_u32 s76, s76, 16384\n"
    "v_mfma_f32_16x16x32_bf16 v[48:51], a[144:147], v[16:19], v[48:51]\n"
    "s_add_u32 m0, s59, 40960\n"
    "v_mfma_f32_16x16x32_bf16 v[52:55], a[148:151], v[16:19], v[52:55]\n"
    "s_waitcnt lgkmcnt(2)\n"
    "buffer_load_dwordx4 v1, s[36:39], s76 offen sc1 lds\n"
    "v_mfma_f32_16x16x32_bf16 v[64:67], a[152:155], v[16:19], v[64:67]\n"
    "ds_read_b128 v[16:19], v0 offset:16384\n"
    "v_mfma_f32_16x16x32_bf16 v[48:51], a[156:159], v[20:23], v[48:51]\n"
    "s_add_u32 s76, s76, 16384\n"
    "s_add_u32 m0, s59, 45056\n"
    "v_mfma_f32_16x16x32_bf16 v[52:55], a[160:163], v[20:23], v[52:55]\n"
    "s_waitcnt lgkmcnt(2)\n"
    "buffer_load_dwordx4 v1, s[36:39], s76 offen sc1 lds\n"
    "v_mfma_f32_16x16x32_bf16 v[64:67], a[164:167], v[20:23], v[64:67]\n"
    "ds_read_b128 v[20:23], v0 offset:17408\n"
    "v_mfma_f32_16x16x32_bf16 v[48:51], a[168:171], v[24:27], v[48:51]\n"
    "v_mfma_f32_16x16x32_bf16 v[52:55], a[172:175], v[24:27], v[52:55]\n"
    "s_waitcnt lgkmcnt(2)\n"
    "s_add_u32 s76, s76, 16384\n"
    "s_add_u32 m0, s59, 49152\n"
    "v_mfma_f32_16x16x32_bf16 v[64:67], a[176:179], v[24:27], v[64:67]\n"
    "ds_read_b128 v[24:27], v0 offset:18432\n"
    "buffer_load_dwordx4 v1, s[36:39], s76 offen sc1 lds\n"
    "v_mfma_f32_16x16x32_bf16 v[48:51], a[180:183], v[28:31], v[48:51]\n"
    "v_mfma_f32_16x16x32_bf16 v[52:55], a[184:187], v[28:31], v[52:55]\n"
    "s_waitcnt lgkmcnt(2)\n"
    "v_mfma_f32_16x16x32_bf16 v[64:67], a[188:191], v[28:31], v[64:67]\n"
    "ds_read_b128 v[28:31], v0 offset:19456\n"
    "s_add_u32 s76, s76, 16384\n"
    "v_mfma_f32_16x16x32_bf16 v[48:51], a[192:195], v[16:19], v[48:51]\n"
    "s_add_u32 m0, s59, 53248\n"
    "v_mfma_f32_16x16x32_bf16 v[52:55], a[196:199], v[16:19], v[52:55]\n"
    "s_waitcnt lgkmcnt(2)\n"
    "buffer_load_dwordx4 v1, s[36:39], s76 offen sc1 lds\n"
    "v_mfma_f32_16x16x32_bf16 v[64:67], a[200:203], v[16:19], v[64:67]\n"
    "ds_read_b128 v[16:19], v0 offset:20480\n"
    "v_mfma_f32_16x16x32_bf16 v[48:51], a[204:207], v[20:23], v[48:51]\n"
    "s_add_u32 s76, s76, 16384\n"
    "s_add_u32 m0, s59, 57344\n"
    "v_mfma_f32_16x16x32_bf16 v[52:55], a[208:211], v[20:23], v[52:55]\n"
    "s_waitcnt lgkmcnt(2)\n"
    "buffer_load_dwordx4 v1, s[36:39], s76 offen sc1 lds\n"
    "v_mfma_f32_16x16x32_bf16 v[64:67], a[212:215], v[20:23], v[64:67]\n"
    "ds_read_b128 v[20:23], v0 offset:21504\n"
    "v_mfma_f32_16x16x32_bf16 v[48:51], a[216:219], v[24:27], v[48:51]\n"
    "v_mfma_f32_16x16x32_bf16 v[52:55], a[220:223], v[24:27], v[52:55]\n"
    "s_waitcnt lgkmcnt(2)\n"
    "s_waitcnt vmcnt(6)\n"
    "buffer_atomic_smax v12, v10, s[36:39], s70 offen sc1\n"
    "v_mfma_f32_16x16x32_bf16 v[64:67], a[224:227], v[24:27], v[64:67]\n"
    "ds_read_b128 v[24:27], v0 offset:22528\n"
    "s_add_u32 s76, s76, 16384\n"
    "v_mfma_f32_16x16x32_bf16 v[48:51], a[228:231], v[28:31], v[48:51]\n"
    "s_add_u32 m0, s59, 61440\n"
    "v_mfma_f32_16x16x32_bf16 v[52:55], a[232:235], v[28:31], v[52:55]\n"
    "s_waitcnt lgkmcnt(2)\n"
    "buffer_load_dwordx4 v1, s[36:39], s76 offen sc1 lds\n"
    "v_mfma_f32_16x16x32_bf16 v[64:67], a[236:239], v[28:31], v[64:67]\n"
    "ds_read_b128 v[28:31], v0 offset:23552\n"
    "s_add_u32 s76, s76, 16384\n"
    "v_mfma_f32_16x16x32_bf16 v[48:51], a[240:243], v[16:19], v[48:51]\n"
    "s_add_u32 m0, s59, 65536\n"
    "v_mfma_f32_16x16x32_bf16 v[52:55], a[244:247], v[16:19], v[52:55]\n"
    "s_waitcnt lgkmcnt(2)\n"
    "buffer_load_dwordx4 v1, s[36:39], s76 offen sc1 lds\n"
    "v_mfma_f32_16x16x32_bf16 v[64:67], a[248:251], v[16:19], v[64:67]\n"
    "ds_read_b128 v[16:19], v0 offset:24576\n"
    "v_mfma_f32_16x16x32_bf16 v[48:51], a[252:255], v[20:23], v[48:51]\n"
    "s_add_u32 s76, s76, 16384\n"
    "s_add_u32 m0, s59, 69632\n"
    "v_mfma_f32_16x16x32_bf16 v[52:55], v[80:83], v[20:23], v[52:55]\n"
    "s_waitcnt lgkmcnt(2)\n"
    "buffer_load_dwordx4 v1, s[36:39], s76 offen sc1 lds\n"
    "v_mfma_f32_16x16x32_bf16 v[64:67], v[84:87], v[20:23], v[64:67]\n"
    "ds_read_b128 v[20:23], v0 offset:25600\n"
    "v_mfma_f32_16x16x32_bf16 v[48:51], v[88:91], v[24:27], v[48:51]\n"
    "v_mfma_f32_16x16x32_bf16 v[52:55], v[92:95], v[24:27], v[52:55]\n"
    "s_waitcnt lgkmcnt(2)\n"
    "v_mfma_f32_16x16x32_bf16 v[64:67], v[96:99], v[24:27], v[64:67]\n"
    "ds_read_b128 v[24:27], v0 offset:26624\n"
    "v_mfma_f32_16x16x32_bf16 v[48:51], v[100:103], v[28:31], v[48:51]\n"
    "v_mfma_f32_16x16x32_bf16 v[52:55], v[104:107], v[28:31], v[52:55]\n"
    "s_waitcnt lgkmcnt(2)\n"
    "v_mfma_f32_16x16x32_bf16 v[64:67], v[108:111], v[28:31], v[64:67]\n"
    "ds_read_b128 v[28:31], v0 offset:27648\n"
    "s_branch .LgDOWN_join0_%=\n"
    ".LgDOWN_dma0_54_%=:\n"
    "v_mfma_f32_16x16x32_bf16 v[48:51], a[216:219], v[24:27], v[48:51]\n"
    "s_add_u32 m0, s59, 36864\n"
    "v_mfma_f32_16x16x32_bf16 v[52:55], a[220:223], v[24:27], v[52:55]\n"
    "s_waitcnt lgkmcnt(2)\n"
    "s_waitcnt vmcnt(1)\n"
    "buffer_atomic_smax v12, v10, s[36:39], s70 offen sc1\n"
    "v_mfma_f32_16x16x32_bf16 v[64:67], a[224:227], v[24:27], v[64:67]\n"
    "ds_read_b128 v[24:27], v0 offset:22528\n"
    "buffer_load_dwordx4 v1, s[36:39], s76 offen sc1 lds\n"
    "v_mfma_f32_16x16x32_bf16 v[48:51], a[228:231], v[28:31], v[48:51]\n"
    "s_add_u32 s76, s76, 16384\n"
    "s_add_u32 m0, s59, 40960\n"
    "v_mfma_f32_16x16x32_bf16 v[52:55], a[232:235], v[28:31], v[52:55]\n"
    "s_waitcnt lgkmcnt(2)\n"
    "buffer_load_dwordx4 v1, s[36:39], s76 offen sc1 lds\n"
    "v_mfma_f32_16x16x32_bf16 v[64:67], a[236:239], v[28:31], v[64:67]\n"
    "ds_read_b128 v[28:31], v0 offset:23552\n"
    "v_mfma_f32_16x16x32_bf16 v[48:51], a[240:243], v[16:19], v[48:51]\n"
    "s_add_u32 s76, s76, 16384\n"
    "s_add_u32 m0, s59, 45056\n"
    "v_mfma_f32_16x16x32_bf16 v[52:55], a[244:247], v[16:19], v[52:55]\n"
    "s_waitcnt lgkmcnt(2)\n"
    "buffer_load_dwordx4 v1, s[36:39], s76 offen sc1 lds\n"
    "v_mfma_f32_16x16x32_bf16 v[64:67], a[248:251], v[16:19], v[64:67]\n"
    "ds_read_b128 v[16:19], v0 offset:24576\n"
    "v_mfma_f32_16x16x32_bf16 v[48:51], a[252:255], v[20:23], v[48:51]\n"
    "v_mfma_f32_16x16x32_bf16 v[52:55], v[80:83], v[20:23], v[52:55]\n"
    "s_waitcnt lgkmcnt(2)\n"
    "s_add_u32 s76, s76, 16384\n"
    "s_add_u32 m0, s59, 49152\n"
    "v_mfma_f32_16x16x32_bf16 v[64:67], v[84:87], v[20:23], v[64:67]\n"
    "ds_read_b128 v[20:23], v0 offset:25600\n"
    "buffer_load_dwordx4 v1, s[36:39], s76 offen sc1 lds\n"
    "v_mfma_f32_16x16x32_bf16 v[48:51], v[88:91], v[24:27], v[48:51]\n"
    "v_mfma_f32_16x16x32_bf16 v[52:55], v[92:95], v[24:27], v[52:55]\n"
    "s_waitcnt lgkmcnt(2)\n"
    "v_mfma_f32_16x16x32_bf16 v[64:67], v[96:99], v[24:27], v[64:67]\n"
    "ds_read_b128 v[24:27], v0 offset:26624\n"
    "s_add_u32 s76, s76, 16384\n"
    "v_mfma_f32_16x16x32_bf16 v[48:51], v[100:103], v[28:31], v[48:51]\n"
    "s_add_u32 m0, s59, 53248\n"
    "v_mfma_f32_16x16x32_bf16 v[52:55], v[104:107], v[28:31], v[52:55]\n"
    "s_waitcnt lgkmcnt(2)\n"
    "buffer_load_dwordx4 v1, s[36:39], s76 offen sc1 lds\n"
    "v_mfma_f32_16x16x32_bf16 v[64:67], v[108:111], v[28:31], v[64:67]\n"
    "ds_read_b128 v[28:31], v0 offset:27648\n"
    "v_mfma_f32_16x16x32_bf16 v[48:51], v[112:115], v[16:19], v[48:51]\n"
    "s_add_u32 s76, s76, 16384\n"
    "s_add_u32 m0, s59, 57344\n"
    "v_mfma_f32_16x16x32_bf16 v[52:55], v[116:119], v[16:19], v[52:55]\n"
    "s_waitcnt lgkmcnt(2)\n"
    "buffer_load_dwordx4 v1, s[36:39], s76 offen sc1 lds\n"
    "v_mfma_f32_16x16x32_bf16 v[64:67], v[120:123], v[16:19], v[64:67]\n"
    "ds_read_b128 v[16:19], v0 offset:28672\n"
    "v_mfma_f32_16x16x32_bf16 v[48:51], v[124:127], v[20:23], v[48:51]\n"
    "v_mfma_f32_16x16x32_bf16 v[52:55], v[128:131], v[20:23], v[52:55]\n"
    "s_waitcnt lgkmcnt(2)\n"
    "s_add_u32 s76, s76, 16384\n"
    "s_add_u32 m0, s59, 61440\n"
    "v_mfma_f32_16x16x32_bf16 v[64:67], v[132:135], v[20:23], v[64:67]\n"
    "ds_read_b128 v[20:23], v0 offset:29696\n"
    "buffer_load_dwordx4 v1, s[36:39], s76 offen sc1 lds\n"
    "v_mfma_f32_16x16x32_bf16 v[48:51], v[136:139], v[24:27], v[48:51]\n"
    "v_mfma_f32_16x16x32_bf16 v[52:55], v[140:143], v[24:27], v[52:55]\n"
    "s_waitcnt lgkmcnt(2)\n"
    "v_mfma_f32_16x16x32_bf16 v[64:67], v[144:147], v[24:27], v[64:67]\n"
    "ds_read_b128 v[24:27], v0 offset:30720\n"
    "s_add_u32 s76, s76, 16384\n"
    "v_mfma_f32_16x16x32_bf16 v[48:51], v[148:151], v[28:31], v[48:51]\n"
    "s_add_u32 m0, s59, 65536\n"
    "v_mfma_f32_16x16x32_bf16 v[52:55], v[152:155], v[28:31], v[52:55]\n"
    "s_waitcnt lgkmcnt(2)\n"
    "buffer_load_dwordx4 v1, s[36:39], s76 offen sc1 lds\n"
    "v_mfma_f32_16x16x32_bf16 v[64:67], v[156:159], v[28:31], v[64:67]\n"
    "ds_read_b128 v[28:31], v0 offset:31744\n"
    "v_mfma_f32_16x16x32_bf16 v[48:51], v[160:163], v[16:19], v[48:51]\n"
    "s_add_u32 s76, s76, 16384\n"
    "s_add_u32 m0, s59, 69632\n"
    "v_mfma_f32_16x16x32_bf16 v[52:55], v[164:167], v[16:19], v[52:55]\n"
    "s_waitcnt lgkmcnt(2)\n"
    "buffer_load_dwordx4 v1, s[36:39], s76 offen sc1 lds\n"
    "v_mfma_f32_16x16x32_bf16 v[64:67], v[168:171], v[16:19], v[64:67]\n"
    "ds_read_b128 v[16:19], v0 offset:32768\n"
    "v_mfma_f32_16x16x32_bf16 v[48:51], v[172:175], v[20:23], v[48:51]\n"
    "v_mfma_f32_16x16x32_bf16 v[52:55], v[176:179], v[20:23], v[52:55]\n"
    "s_waitcnt lgkmcnt(2)\n"
    "v_mfma_f32_16x16x32_bf16 v[64:67], v[180:183], v[20:23], v[64:67]\n"
    "ds_read_b128 v[20:23], v0 offset:33792\n"
    "v_mfma_f32_16x16x32_bf16 v[48:51], v[184:187], v[24:27], v[48:51]\n"
    "v_mfma_f32_16x16x32_bf16 v[52:55], v[188:191], v[24:27], v[52:55]\n"
    "s_waitcnt lgkmcnt(2)\n"
    "v_mfma_f32_16x16x32_bf16 v[64:67], v[192:195], v[24:27], v[64:67]\n"
    "ds_read_b128 v[24:27], v0 offset:34816\n"
    "v_mfma_f32_16x16x32_bf16 v[48:51], v[196:199], v[28:31], v[48:51]\n"
    "v_mfma_f32_16x16x32_bf16 v[52:55], v[200:203], v[28:31], v[52:55]\n"
    "s_waitcnt lgkmcnt(2)\n"
    "v_mfma_f32_16x16x32_bf16 v[64:67], v[204:207], v[28:31], v[64:67]\n"
    "ds_read_b128 v[28:31], v0 offset:35840\n"
    "v_mfma_f32_16x16x32_bf16 v[48:51], v[208:211], v[16:19], v[48:51]\n"
    "v_mfma_f32_16x16x32_bf16 v[52:55], v[212:215], v[16:19], v[52:55]\n"
    "s_waitcnt lgkmcnt(2)\n"
    "v_mfma_f32_16x16x32_bf16 v[64:67], v[216:219], v[16:19], v[64:67]\n"
    "v_mfma_f32_16x16x32_bf16 v[48:51], v[220:223], v[20:23], v[48:51]\n"
    "v_mfma_f32_16x16x32_bf16 v[52:55], v[224:227], v[20:23], v[52:55]\n"
    "s_waitcnt lgkmcnt(1)\n"
    "v_mfma_f32_16x16x32_bf16 v[64:67], v[228:231], v[20:23], v[64:67]\n"
    "v_mfma_f32_16x16x32_bf16 v[48:51], v[232:235], v[24:27], v[48:51]\n"
    "v_mfma_f32_16x16x32_bf16 v[52:55], v[236:239], v[24:27], v[52:55]\n"
    "s_waitcnt lgkmcnt(0)\n"
    "v_mfma_f32_16x16x32_bf16 v[64:67], v[240:243], v[24:27], v[64:67]\n"
    "v_mfma_f32_16x16x32_bf16 v[48:51], v[244:247], v[28:31], v[48:51]\n"
    "v_mfma_f32_16x16x32_bf16 v[52:55], v[248:251], v[28:31], v[52:55]\n"
    "v_mfma_f32_16x16x32_bf16 v[64:67], v[252:255], v[28:31], v[64:67]\n"
    "v_cvt_pk_bf16_f32 v48, v48, v49\n"
    "v_cvt_pk_bf16_f32 v49, v50, v51\n"
    "s_nop 1\n"
    "v_cvt_pk_bf16_f32 v50, v52, v53\n"
    "v_cvt_pk_bf16_f32 v51, v54, v55\n"
    "buffer_store_dwordx4 v[48:51], v15, s[36:39], s73 offen sc1\n"
    "s_nop 0\n"
    "v_cvt_pk_bf16_f32 v64, v64, v65\n"
    "v_cvt_pk_bf16_f32 v65, v66, v67\n"
    "s_nop 0\n"
    "v_permlane16_swap_b32 v64, v66\n"
    "v_permlane16_swap_b32 v65, v67\n"
    "buffer_store_dwordx4 v[64:67], v4, s[36:39], s73 offen sc1\n"
    "v_mov_b32 v12, s68\n"
    "s_mov_b32 s73, 0x80000000\n"
    "s_mov_b32 s68, s71\n"
    "s_mov_b32 s69, 0\n"
    "s_mov_b32 s70, 0x80000000\n"
    "v_mov_b32 v7, v12\n"
    "s_waitcnt vmcnt(12)\n"
    "s_add_u32 s71, s68, 1\n"
    "v_add_u32 v13, v11, v3\n"
    "v_cmp_gt_i32 vcc, s71, v13\n"
    "s_nop 0\n"
    "s_or_b64 exec, vcc, s[62:63]\n"
    "buffer_atomic_smax v11, v2, s[36:39], 0 offen sc0 sc1\n"
    "s_mov_b64 exec, -1\n"
    "s_waitcnt vmcnt(3)\n"
    "s_branch .LgDOWN_bar1_%=\n"
    ".LgDOWN_dma0_75_%=:\n"
    "v_mfma_f32_16x16x32_bf16 v[48:51], v[124:127], v[20:23], v[48:51]\n"
    "s_add_u32 m0, s59, 36864\n"
    "v_mfma_f32_16x16x32_bf16 v[52:55], v[128:131], v[20:23], v[52:55]\n"
    "s_waitcnt lgkmcnt(2)\n"
    "buffer_load_dwordx4 v1, s[36:39], s76 offen sc1 lds\n"
    "v_mfma_f32_16x16x32_bf16 v[64:67], v[132:135], v[20:23], v[64:67]\n"
    "ds_read_b128 v[20:23], v0 offset:29696\n"
    "s_add_u32 s76, s76, 16384\n"
    "v_mfma_f32_16x16x32_bf16 v[48:51], v[136:139], v[24:27], v[48:51]\n"
    "s_add_u32 m0, s59, 40960\n"
    "v_mfma_f32_16x16x32_bf16 v[52:55], v[140:143], v[24:27], v[52:55]\n"
    "s_waitcnt lgkmcnt(2)\n"
    "buffer_load_dwordx4 v1, s[36:39], s76 offen sc1 lds\n"
    "v_mfma_f32_16x16x32_bf16 v[64:67], v[144:147], v[24:27], v[64:67]\n"
    "ds_read_b128 v[24:27], v0 offset:30720\n"
    "v_mfma_f32_16x16x32_bf16 v[48:51], v[148:151], v[28:31], v[48:51]\n"
    "s_add_u32 s76, s76, 16384\n"
    "s_add_u32 m0, s59, 45056\n"
    "v_mfma_f32_16x16x32_bf16 v[52:55], v[152:155], v[28:31], v[52:55]\n"
    "s_waitcnt lgkmcnt(2)\n"
    "buffer_load_dwordx4 v1, s[36:39], s76 offen sc1 lds\n"
    "v_mfma_f32_16x16x32_bf16 v[64:67], v[156:159], v[28:31], v[64:67]\n"
    "ds_read_b128 v[28:31], v0 offset:31744\n"
    "v_mfma_f32_16x16x32_bf16 v[48:51], v[160:163], v[16:19], v[48:51]\n"
    "v_mfma_f32_16x16x32_bf16 v[52:55], v[164:167], v[16:19], v[52:55]\n"
    "s_waitcnt lgkmcnt(2)\n"
    "s_add_u32 s76, s76, 16384\n"
    "s_add_u32 m0, s59, 49152\n"
    "v_mfma_f32_16x16x32_bf16 v[64:67], v[168:171], v[16:19], v[64:67]\n"
    "ds_read_b128 v[16:19], v0 offset:32768\n"
    "buffer_load_dwordx4 v1, s[36:39], s76 offen sc1 lds\n"
    "v_mfma_f32_16x16x32_bf16 v[48:51], v[172:175], v[20:23], v[48:51]\n"
    "v_mfma_f32_16x16x32_bf16 v[52:55], v[176:179], v[20:23], v[52:55]\n"
    "s_waitcnt lgkmcnt(2)\n"
    "v_mfma_f32_16x16x32_bf16 v[64:67], v[180:183], v[20:23], v[64:67]\n"
    "ds_read_b128 v[20:23], v0 offset:33792\n"
    "s_add_u32 s76, s76, 16384\n"
    "v_mfma_f32_16x16x32_bf16 v[48:51], v[184:187], v[24:27], v[48:51]\n"
    "s_add_u32 m0, s59, 53248\n"
    "v_mfma_f32_16x16x32_bf16 v[52:55], v[188:191], v[24:27], v[52:55]\n"
    "s_waitcnt lgkmcnt(2)\n"
    "buffer_load_dwordx4 v1, s[36:39], s76 offen sc1 lds\n"
    "v_mfma_f32_16x16x32_bf16 v[64:67], v[192:195], v[24:27], v[64:67]\n"
    "ds_read_b128 v[24:27], v0 offset:34816\n"
    "v_mfma_f32_16x16x32_bf16 v[48:51], v[196:199], v[28:31], v[48:51]\n"
    "s_add_u32 s76, s76, 16384\n"
    "s_add_u32 m0, s59, 57344\n"
    "v_mfma_f32_16x16x32_bf16 v[52:55], v[200:203], v[28:31], v[52:55]\n"
    "s_waitcnt lgkmcnt(2)\n"
    "buffer_load_dwordx4 v1, s[36:39], s76 offen sc1 lds\n"
    "v_mfma_f32_16x16x32_bf16 v[64:67], v[204:207], v[28:31], v[64:67]\n"
    "ds_read_b128 v[28:31], v0 offset:35840\n"
    "v_mfma_f32_16x16x32_bf16 v[48:51], v[208:211], v[16:19], v[48:51]\n"
    "v_mfma_f32_16x16x32_bf16 v[52:55], v[212:215], v[16:19], v[52:55]\n"
    "s_waitcnt lgkmcnt(2)\n"
    "s_add_u32 s76, s76, 16384\n"
    "s_add_u32 m0, s59, 61440\n"
    "v_mfma_f32_16x16x32_bf16 v[64:67], v[216:219], v[16:19], v[64:67]\n"
    "buffer_load_dwordx4 v1, s[36:39], s76 offen sc1 lds\n"
    "v_mfma_f32_16x16x32_bf16 v[48:51], v[220:223], v[20:23], v[48:51]\n"
    "v_mfma_f32_16x16x32_bf16 v[52:55], v[224:227], v[20:23], v[52:55]\n"
    "s_waitcnt lgkmcnt(1)\n"
    "v_mfma_f32_16x16x32_bf16 v[64:67], v[228:231], v[20:23], v[64:67]\n"
    "s_add_u32 s76, s76, 16384\n"
    "v_mfma_f32_16x16x32_bf16 v[48:51], v[232:235], v[24:27], v[48:51]\n"
    "s_add_u32 m0, s59, 65536\n"
    "v_mfma_f32_16x16x32_bf16 v[52:55], v[236:239], v[24:27], v[52:55]\n"
    "s_waitcnt lgkmcnt(0)\n"
    "buffer_load_dwordx4 v1, s[36:39], s76 offen sc1 lds\n"
    "v_mfma_f32_16x16x32_bf16 v[64:67], v[240:243], v[24:27], v[64:67]\n"
    "v_mfma_f32_16x16x32_bf16 v[48:51], v[244:247], v[28:31], v[48:51]\n"
    "s_add_u32 s76, s76, 16384\n"
    "s_add_u32 m0, s59, 69632\n"
    "v_mfma_f32_16x16x32_bf16 v[52:55], v[248:251], v[28:31], v[52:55]\n"
    "buffer_load_dwordx4 v1, s[36:39], s76 offen sc1 lds\n"
    "v_mfma_f32_16x16x32_bf16 v[64:67], v[252:255], v[28:31], v[64:67]\n"
    "v_cvt_pk_bf16_f32 v48, v48, v49\n"
    "v_cvt_pk_bf16_f32 v49, v50, v51\n"
    "s_nop 1\n"
    "v_cvt_pk_bf16_f32 v50, v52, v53\n"
    "v_cvt_pk_bf16_f32 v51, v54, v55\n"
    "buffer_store_dwordx4 v[48:51], v15, s[36:39], s73 offen sc1\n"
    "s_nop 0\n"
    "v_cvt_pk_bf16_f32 v64, v64, v65\n"
    "v_cvt_pk_bf16_f32 v65, v66, v67\n"
    "s_nop 0\n"
    "v_permlane16_swap_b32 v64, v66\n"
    "v_permlane16_swap_b32 v65, v67\n"
    "buffer_store_dwordx4 v[64:67], v4, s[36:39], s73 offen sc1\n"
    "v_mov_b32 v12, s68\n"
    "s_mov_b32 s73, 0x80000000\n"
    "s_mov_b32 s68, s71\n"
    "s_mov_b32 s69, 0\n"
    "s_mov_b32 s70, 0x80000000\n"
    "v_mov_b32 v7, v12\n"
    "s_waitcnt vmcnt(12)\n"
    "s_add_u32 s71, s68, 1\n"
    "v_add_u32 v13, v11, v3\n"
    "v_cmp_gt_i32 vcc, s71, v13\n"
    "s_nop 0\n"
    "s_or_b64 exec, vcc, s[62:63]\n"
    "buffer_atomic_smax v11, v2, s[36:39], 0 offen sc0 sc1\n"
    "s_mov_b64 exec, -1\n"
    "s_waitcnt vmcnt(3)\n"
    "s_branch .LgDOWN_bar1_%=\n"
    ".LgDOWN_dma0_96_%=:\n"
    "v_mfma_f32_16x16x32_bf16 v[48:51], v[208:211], v[16:19], v[48:51]\n"
    "s_add_u32 m0, s59, 36864\n"
    "v_mfma_f32_16x16x32_bf16 v[52:55], v[212:215], v[16:19], v[52:55]\n"
    "s_waitcnt lgkmcnt(2)\n"
    "buffer_load_dwordx4 v1, s[36:39], s76 offen sc1 lds\n"
    "v_mfma_f32_16x16x32_bf16 v[64:67], v[216:219], v[16:19], v[64:67]\n"
    "s_add_u32 s76, s76, 16384\n"
    "s_add_u32 m0, s59, 40960\n"
    "v_mfma_f32_16x16x32_bf16 v[48:51], v[220:223], v[20:23], v[48:51]\n"
    "buffer_load_dwordx4 v1, s[36:39], s76 offen sc1 lds\n"
    "v_mfma_f32_16x16x32_bf16 v[52:55], v[224:227], v[20:23], v[52:55]\n"
    "s_waitcnt lgkmcnt(1)\n"
    "v_mfma_f32_16x16x32_bf16 v[64:67], v[228:231], v[20:23], v[64:67]\n"
    "v_mfma_f32_16x16x32_bf16 v[48:51], v[232:235], v[24:27], v[48:51]\n"
    "s_add_u32 s76, s76, 16384\n"
    "s_add_u32 m0, s59, 45056\n"
    "v_mfma_f32_16x16x32_bf16 v[52:55], v[236:239], v[24:27], v[52:55]\n"
    "s_waitcnt lgkmcnt(0)\n"
    "buffer_load_dwordx4 v1, s[36:39], s76 offen sc1 lds\n"
    "v_mfma_f32_16x16x32_bf16 v[64:67], v[240:243], v[24:27], v[64:67]\n"
    "v_mfma_f32_16x16x32_bf16 v[48:51], v[244:247], v[28:31], v[48:51]\n"
    "v_mfma_f32_16x16x32_bf16 v[52:55], v[248:251], v[28:31], v[52:55]\n"
    "s_add_u32 s76, s76, 16384\n"
    "s_add_u32 m0, s59, 49152\n"
    "v_mfma_f32_16x16x32_bf16 v[64:67], v[252:255], v[28:31], v[64:67]\n"
    "buffer_load_dwordx4 v1, s[36:39], s76 offen sc1 lds\n"
    "v_cvt_pk_bf16_f32 v48, v48, v49\n"
    "v_cvt_pk_bf16_f32 v49, v50, v51\n"
    "s_nop 0\n"
    "v_cvt_pk_bf16_f32 v50, v52, v53\n"
    "v_cvt_pk_bf16_f32 v51, v54, v55\n"
    "buffer_store_dwordx4 v[48:51], v15, s[36:39], s73 offen sc1\n"
    "s_nop 0\n"
    "v_cvt_pk_bf16_f32 v64, v64, v65\n"
    "v_cvt_pk_bf16_f32 v65, v66, v67\n"
    "s_nop 0\n"
    "v_permlane16_swap_b32 v64, v66\n"
    "v_permlane16_swap_b32 v65, v67\n"
    "buffer_store_dwordx4 v[64:67], v4, s[36:39], s73 offen sc1\n"
    "v_mov_b32 v12, s68\n"
    "s_mov_b32 s73, 0x80000000\n"
    "s_mov_b32 s68, s71\n"
    "s_add_u32 m0, s59, 53248\n"
    "s_add_u32 s76, s76, 16384\n"
    "buffer_load_dwordx4 v1, s[36:39], s76 offen sc1 lds\n"
    "s_add_u32 m0, s59, 57344\n"
    "s_add_u32 s76, s76, 16384\n"
    "buffer_load_dwordx4 v1, s[36:39], s76 offen sc1 lds\n"
    "s_add_u32 m0, s59, 61440\n"
    "s_add_u32 s76, s76, 16384\n"
    "buffer_load_dwordx4 v1, s[36:39], s76 offen sc1 lds\n"
    "s_add_u32 m0, s59, 65536\n"
    "s_add_u32 s76, s76, 16384\n"
    "buffer_load_dwordx4 v1, s[36:39], s76 offen sc1 lds\n"
    "s_add_u32 m0, s59, 69632\n"
    "s_add_u32 s76, s76, 16384\n"
    "buffer_load_dwordx4 v1, s[36:39], s76 offen sc1 lds\n"
    "s_waitcnt vmcnt(5)\n"
    "buffer_atomic_smax v12, v14, s[36:39], 0 offen sc1\n"
    "s_mov_b32 s69, 0x80000000\n"
    "s_mov_b32 s70, 0x80000000\n"
    "v_mov_b32 v7, v12\n"
    "s_waitcnt vmcnt(12)\n"
    "s_add_u32 s71, s68, 1\n"
    "v_add_u32 v13, v11, v3\n"
    "v_cmp_gt_i32 vcc, s71, v13\n"
    "s_nop 0\n"
    "s_or_b64 exec, vcc, s[62:63]\n"
    "buffer_atomic_smax v11, v2, s[36:39], 0 offen sc0 sc1\n"
    "s_mov_b64 exec, -1\n"
    "s_waitcnt vmcnt(2)\n"
    "s_branch .LgDOWN_bar1_%=\n"
    ".LgDOWN_idle0_%=:\n"
    "s_or_b64 exec, vcc, s[62:63]\n"
    "buffer_atomic_smax v5, v2, s[36:39], 0 offen sc0 sc1\n"
    "s_mov_b64 exec, -1\n"
    "s_waitcnt vmcnt(1)\n"
    "buffer_atomic_smax v12, v14, s[36:39], 0 offen sc1\n"
    "s_branch .LgDOWN_block0_1_%=\n"
    ".LgDOWN_block0_%=:\n"
    "v_add_u32 v13, v11, v3\n"
    "v_cmp_gt_i32 vcc, s71, v13\n"
    "s_nop 0\n"
    "s_or_b64 exec, vcc, s[62:63]\n"
    "buffer_atomic_smax v11, v2, s[36:39], 0 offen sc0 sc1\n"
    "s_mov_b64 exec, -1\n"
    "s_sleep 3\n"
    "s_waitcnt vmcnt(1)\n"
    ".LgDOWN_block0_0_%=:\n"
    "v_add_u32 v13, v5, v3\n"
    "v_cmp_gt_i32 vcc, s71, v13\n"
    "s_cbranch_vccz .LgDOWN_go0_%=\n"
    "v_add_u32 v13, v5, v3\n"
    "v_cmp_gt_i32 vcc, s71, v13\n"
    "s_nop 0\n"
    "s_or_b64 exec, vcc, s[62:63]\n"
    "buffer_atomic_smax v5, v2, s[36:39], 0 offen sc0 sc1\n"
    "s_mov_b64 exec, -1\n"
    "s_sleep 3\n"
    "s_waitcnt vmcnt(1)\n"
    ".LgDOWN_block0_1_%=:\n"
    "v_add_u32 v13, v11, v3\n"
    "v_cmp_gt_i32 vcc, s71, v13\n"
    "s_cbranch_vccz .LgDOWN_go0_%=\n"
    "s_sub_u32 s72, s72, 1\n"
    "s_cbranch_scc0 .LgDOWN_block0_%=\n"
    "s_mov_b32 s72, 0\n"
    ".LgDOWN_go0_%=:\n"
    "s_lshr_b32 s76, s71, 1\n"
    "s_and_b32 s76, s76, s54\n"
    "s_mul_i32 s76, s76, s53\n"
    "s_add_u32 s76, s76, s52\n"
    "s_add_u32 s76, s76, 1024\n"
    "s_add_u32 m0, s59, 36864\n"
    "s_nop 0\n"
    "buffer_load_dwordx4 v1, s[36:39], s76 offen sc1 lds\n"
    "s_add_u32 m0, s59, 40960\n"
    "s_add_u32 s76, s76, 16384\n"
    "buffer_load_dwordx4 v1, s[36:39], s76 offen sc1 lds\n"
    "s_add_u32 m0, s59, 45056\n"
    "s_add_u32 s76, s76, 16384\n"
    "buffer_load_dwordx4 v1, s[36:39], s76 offen sc1 lds\n"
    "s_add_u32 m0, s59, 49152\n"
    "s_add_u32 s76, s76, 16384\n"
    "buffer_load_dwordx4 v1, s[36:39], s76 offen sc1 lds\n"
    "s_add_u32 m0, s59, 53248\n"
    "s_add_u32 s76, s76, 16384\n"
    "buffer_load_dwordx4 v1, s[36:39], s76 offen sc1 lds\n"
    "s_add_u32 m0, s59, 57344\n"
    "s_add_u32 s76, s76, 16384\n"
    "buffer_load_dwordx4 v1, s[36:39], s76 offen sc1 lds\n"
    "s_add_u32 m0, s59, 61440\n"
    "s_add_u32 s76, s76, 16384\n"
    "buffer_load_dwordx4 v1, s[36:39], s76 offen sc1 lds\n"
    "s_add_u32 m0, s59, 65536\n"
    "s_add_u32 s76, s76, 16384\n"
    "buffer_load_dwordx4 v1, s[36:39], s76 offen sc1 lds\n"
    "s_add_u32 m0, s59, 69632\n"
    "s_add_u32 s76, s76, 16384\n"
    "buffer_load_dwordx4 v1, s[36:39], s76 offen sc1 lds\n"
    "s_mov_b32 s69, 0x80000000\n"
    "s_mov_b32 s70, 0x80000000\n"
    "v_mov_b32 v7, v12\n"
    "s_waitcnt vmcnt(9)\n"
    "s_add_u32 s71, s68, 1\n"
    "v_add_u32 v13, v11, v3\n"
    "v_cmp_gt_i32 vcc, s71, v13\n"
    "s_nop 0\n"
    "s_or_b64 exec, vcc, s[62:63]\n"
    "buffer_atomic_smax v11, v2, s[36:39], 0 offen sc0 sc1\n"
    "s_mov_b64 exec, -1\n"
    "s_waitcnt vmcnt(1)\n"
    "s_branch .LgDOWN_bar1_%=\n"
    ".LgDOWN_bar0_%=:\n"
    "s_barrier\n"
    "ds_read_b128 v[16:19], v0 offset:0\n"
    "ds_read_b128 v[20:23], v0 offset:1024\n"
    "ds_read_b128 v[24:27], v0 offset:2048\n"
    "ds_read_b128 v[28:31], v0 offset:3072\n"
    "v_add_u32 v13, v5, v3\n"
    "v_cmp_gt_i32 vcc, s71, v13\n"
    "s_nop 0\n"
    "s_or_b64 exec, vcc, s[62:63]\n"
    "buffer_atomic_smax v5, v2, s[36:39], 0 offen sc0 sc1\n"
    "s_mov_b64 exec, -1\n"
    "s_lshr_b32 s76, s71, 1\n"
    "s_and_b32 s76, s76, s54\n"
    "s_mul_i32 s76, s76, s53\n"
    "s_add_u32 s76, s76, s52\n"
    "s_add_u32 s76, s76, 1024\n"
    "s_waitcnt lgkmcnt(3)\n"
    "s_branch .LgDOWN_head0_%=\n"
    ".LgDOWN_poll1_%=:\n"
    "v_mfma_f32_16x16x32_bf16 v[56:59], a[48:51], v[16:19], v[56:59]\n"
    "s_or_b64 exec, vcc, s[62:63]\n"
    "buffer_atomic_smax v11, v2, s[36:39], 0 offen sc0 sc1\n"
    "s_mov_b64 exec, -1\n"
    "v_mfma_f32_16x16x32_bf16 v[60:63], a[52:55], v[16:19], v[60:63]\n"
    "s_waitcnt lgkmcnt(2)\n"
    "v_mfma_f32_16x16x32_bf16 v[68:71], a[56:59], v[16:19], v[68:71]\n"
    "ds_read_b128 v[16:19], v9 offset:8192\n"
    "v_mfma_f32_16x16x32_bf16 v[56:59], a[60:63], v[20:23], v[56:59]\n"
    "buffer_store_dwordx4 v[64:67], v4, s[36:39], s73 offen sc1\n"
    "v_mfma_f32_16x16x32_bf16 v[60:63], a[64:67], v[20:23], v[60:63]\n"
    "s_waitcnt lgkmcnt(2)\n"
    "s_lshr_b32 s73, s68, 1\n"
    "v_mfma_f32_16x16x32_bf16 v[68:71], a[68:71], v[20:23], v[68:71]\n"
    "ds_read_b128 v[20:23], v9 offset:9216\n"
    "s_and_b32 s73, s73, s57\n"
    "v_mfma_f32_16x16x32_bf16 v[56:59], a[72:75], v[24:27], v[56:59]\n"
    "s_mul_i32 s73, s73, s56\n"
    "v_mfma_f32_16x16x32_bf16 v[60:63], a[76:79], v[24:27], v[60:63]\n"
    "s_waitcnt lgkmcnt(2)\n"
    "s_add_u32 s73, s73, s55\n"
    "v_mfma_f32_16x16x32_bf16 v[68:71], a[80:83], v[24:27], v[68:71]\n"
    "ds_read_b128 v[24:27], v9 offset:10240\n"
    "v_mfma_f32_16x16x32_bf16 v[56:59], a[84:87], v[28:31], v[56:59]\n"
    "v_mfma_f32_16x16x32_bf16 v[60:63], a[88:91], v[28:31], v[60:63]\n"
    "s_waitcnt lgkmcnt(2)\n"
    "v_mfma_f32_16x16x32_bf16 v[68:71], a[92:95], v[28:31], v[68:71]\n"
    "ds_read_b128 v[28:31], v9 offset:11264\n"
    "v_mfma_f32_16x16x32_bf16 v[56:59], a[96:99], v[16:19], v[56:59]\n"
    "v_mfma_f32_16x16x32_bf16 v[60:63], a[100:103], v[16:19], v[60:63]\n"
    "s_waitcnt lgkmcnt(2)\n"
    "v_mfma_f32_16x16x32_bf16 v[68:71], a[104:107], v[16:19], v[68:71]\n"
    "ds_read_b128 v[16:19], v9 offset:12288\n"
    "v_mfma_f32_16x16x32_bf16 v[56:59], a[108:111], v[20:23], v[56:59]\n"
    "v_mfma_f32_16x16x32_bf16 v[60:63], a[112:115], v[20:23], v[60:63]\n"
    "s_waitcnt lgkmcnt(2)\n"
    "s_waitcnt vmcnt(4)\n"
    "v_add_u32 v13, v5, v3\n"
    "v_mfma_f32_16x16x32_bf16 v[68:71], a[116:119], v[20:23], v[68:71]\n"
    "ds_read_b128 v[20:23], v9 offset:13312\n"
    "v_cmp_gt_i32 vcc, s71, v13\n"
    "v_mfma_f32_16x16x32_bf16 v[56:59], a[120:123], v[24:27], v[56:59]\n"
    "v_mfma_f32_16x16x32_bf16 v[60:63], a[124:127], v[24:27], v[60:63]\n"
    "s_waitcnt lgkmcnt(2)\n"
    "v_mfma_f32_16x16x32_bf16 v[68:71], a[128:131], v[24:27], v[68:71]\n"
    "ds_read_b128 v[24:27], v9 offset:14336\n"
    "s_cbranch_vccz .LgDOWN_dma1_33_%=\n"
    "v_mfma_f32_16x16x32_bf16 v[56:59], a[132:135], v[28:31], v[56:59]\n"
    "s_or_b64 exec, vcc, s[62:63]\n"
    "buffer_atomic_smax v5, v2, s[36:39], 0 offen sc0 sc1\n"
    "s_mov_b64 exec, -1\n"
    "v_mfma_f32_16x16x32_bf16 v[60:63], a[136:139], v[28:31], v[60:63]\n"
    "s_waitcnt lgkmcnt(2)\n"
    "v_mfma_f32_16x16x32_bf16 v[68:71], a[140:143], v[28:31], v[68:71]\n"
    "ds_read_b128 v[28:31], v9 offset:15360\n"
    "v_mfma_f32_16x16x32_bf16 v[56:59], a[144:147], v[16:19], v[56:59]\n"
    "v_mfma_f32_16x16x32_bf16 v[60:63], a[148:151], v[16:19], v[60:63]\n"
    "s_waitcnt lgkmcnt(2)\n"
    "v_mfma_f32_16x16x32_bf16 v[68:71], a[152:155], v[16:19], v[68:71]\n"
    "ds_read_b128 v[16:19], v9 offset:16384\n"
    "v_mfma_f32_16x16x32_bf16 v[56:59], a[156:159], v[20:23], v[56:59]\n"
    "v_mfma_f32_16x16x32_bf16 v[60:63], a[160:163], v[20:23], v[60:63]\n"
    "s_waitcnt lgkmcnt(2)\n"
    "v_mfma_f32_16x16x32_bf16 v[68:71], a[164:167], v[20:23], v[68:71]\n"
    "ds_read_b128 v[20:23], v9 offset:17408\n"
    "v_mfma_f32_16x16x32_bf16 v[56:59], a[168:171], v[24:27], v[56:59]\n"
    "v_mfma_f32_16x16x32_bf16 v[60:63], a[172:175], v[24:27], v[60:63]\n"
    "s_waitcnt lgkmcnt(2)\n"
    "v_mfma_f32_16x16x32_bf16 v[68:71], a[176:179], v[24:27], v[68:71]\n"
    "ds_read_b128 v[24:27], v9 offset:18432\n"
    "v_mfma_f32_16x16x32_bf16 v[56:59], a[180:183], v[28:31], v[56:59]\n"
    "v_mfma_f32_16x16x32_bf16 v[60:63], a[184:187], v[28:31], v[60:63]\n"
    "s_waitcnt lgkmcnt(2)\n"
    "v_mfma_f32_16x16x32_bf16 v[68:71], a[188:191], v[28:31], v[68:71]\n"
    "ds_read_b128 v[28:31], v9 offset:19456\n"
    "v_mfma_f32_16x16x32_bf16 v[56:59], a[192:195], v[16:19], v[56:59]\n"
    "v_mfma_f32_16x16x32_bf16 v[60:63], a[196:199], v[16:19], v[60:63]\n"
    "s_waitcnt lgkmcnt(2)\n"
    "s_waitcnt vmcnt(2)\n"
    "v_add_u32 v13, v11, v3\n"
    "v_mfma_f32_16x16x32_bf16 v[68:71], a[200:203], v[16:19], v[68:71]\n"
    "ds_read_b128 v[16:19], v9 offset:20480\n"
    "v_cmp_gt_i32 vcc, s71, v13\n"
    "v_mfma_f32_16x16x32_bf16 v[56:59], a[204:207], v[20:23], v[56:59]\n"
    "v_mfma_f32_16x16x32_bf16 v[60:63], a[208:211], v[20:23], v[60:63]\n"
    "s_waitcnt lgkmcnt(2)\n"
    "v_mfma_f32_16x16x32_bf16 v[68:71], a[212:215], v[20:23], v[68:71]\n"
    "ds_read_b128 v[20:23], v9 offset:21504\n"
    "s_cbranch_vccz .LgDOWN_dma1_54_%=\n"
    "v_mfma_f32_16x16x32_bf16 v[56:59], a[216:219], v[24:27], v[56:59]\n"
    "s_or_b64 exec, vcc, s[62:63]\n"
    "buffer_atomic_smax v11, v2, s[36:39], 0 offen sc0 sc1\n"
    "s_mov_b64 exec, -1\n"
    "v_mfma_f32_16x16x32_bf16 v[60:63], a[220:223], v[24:27], v[60:63]\n"
    "s_waitcnt lgkmcnt(2)\n"
    "s_waitcnt vmcnt(2)\n"
    "buffer_atomic_smax v12, v14, s[36:39], s70 offen sc1\n"
    "v_mfma_f32_16x16x32_bf16 v[68:71], a[224:227], v[24:27], v[68:71]\n"
    "ds_read_b128 v[24:27], v9 offset:22528\n"
    "v_mfma_f32_16x16x32_bf16 v[56:59], a[228:231], v[28:31], v[56:59]\n"
    "v_mfma_f32_16x16x32_bf16 v[60:63], a[232:235], v[28:31], v[60:63]\n"
    "s_waitcnt lgkmcnt(2)\n"
    "v_mfma_f32_16x16x32_bf16 v[68:71], a[236:239], v[28:31], v[68:71]\n"
    "ds_read_b128 v[28:31], v9 offset:23552\n"
    "v_mfma_f32_16x16x32_bf16 v[56:59], a[240:243], v[16:19], v[56:59]\n"
    "v_mfma_f32_16x16x32_bf16 v[60:63], a[244:247], v[16:19], v[60:63]\n"
    "s_waitcnt lgkmcnt(2)\n"
    "v_mfma_f32_16x16x32_bf16 v[68:71], a[248:251], v[16:19], v[68:71]\n"
    "ds_read_b128 v[16:19], v9 offset:24576\n"
    "v_mfma_f32_16x16x32_bf16 v[56:59], a[252:255], v[20:23], v[56:59]\n"
    "v_mfma_f32_16x16x32_bf16 v[60:63], v[80:83], v[20:23], v[60:63]\n"
    "s_waitcnt lgkmcnt(2)\n"
    "v_mfma_f32_16x16x32_bf16 v[68:71], v[84:87], v[20:23], v[68:71]\n"
    "ds_read_b128 v[20:23], v9 offset:25600\n"
    "v_mfma_f32_16x16x32_bf16 v[56:59], v[88:91], v[24:27], v[56:59]\n"
    "v_mfma_f32_16x16x32_bf16 v[60:63], v[92:95], v[24:27], v[60:63]\n"
    "s_waitcnt lgkmcnt(2)\n"
    "v_mfma_f32_16x16x32_bf16 v[68:71], v[96:99], v[24:27], v[68:71]\n"
    "ds_read_b128 v[24:27], v9 offset:26624\n"
    "v_mfma_f32_16x16x32_bf16 v[56:59], v[100:103], v[28:31], v[56:59]\n"
    "v_mfma_f32_16x16x32_bf16 v[60:63], v[104:107], v[28:31], v[60:63]\n"
    "s_waitcnt lgkmcnt(2)\n"
    "s_waitcnt vmcnt(2)\n"
    "v_add_u32 v13, v5, v3\n"
    "v_mfma_f32_16x16x32_bf16 v[68:71], v[108:111], v[28:31], v[68:71]\n"
    "ds_read_b128 v[28:31], v9 offset:27648\n"
    "v_cmp_gt_i32 vcc, s71, v13\n"
    "v_mfma_f32_16x16x32_bf16 v[56:59], v[112:115], v[16:19], v[56:59]\n"
    "v_mfma_f32_16x16x32_bf16 v[60:63], v[116:119], v[16:19], v[60:63]\n"
    "s_waitcnt lgkmcnt(2)\n"
    "v_mfma_f32_16x16x32_bf16 v[68:71], v[120:123], v[16:19], v[68:71]\n"
    "ds_read_b128 v[16:19], v9 offset:28672\n"
    "s_cbranch_vccz .LgDOWN_dma1_75_%=\n"
    "v_mfma_f32_16x16x32_bf16 v[56:59], v[124:127], v[20:23], v[56:59]\n"
    "s_or_b64 exec, vcc, s[62:63]\n"
    "buffer_atomic_smax v5, v2, s[36:39], 0 offen sc0 sc1\n"
    "s_mov_b64 exec, -1\n"
    "v_mfma_f32_16x16x32_bf16 v[60:63], v[128:131], v[20:23], v[60:63]\n"
    "s_waitcnt lgkmcnt(2)\n"
    "v_mfma_f32_16x16x32_bf16 v[68:71], v[132:135], v[20:23], v[68:71]\n"
    "ds_read_b128 v[20:23], v9 offset:29696\n"
    "v_mfma_f32_16x16x32_bf16 v[56:59], v[136:139], v[24:27], v[56:59]\n"
    "v_mfma_f32_16x16x32_bf16 v[60:63], v[140:143], v[24:27], v[60:63]\n"
    "s_waitcnt lgkmcnt(2)\n"
    "v_mfma_f32_16x16x32_bf16 v[68:71], v[144:147], v[24:27], v[68:71]\n"
    "ds_read_b128 v[24:27], v9 offset:30720\n"
    "v_mfma_f32_16x16x32_bf16 v[56:59], v[148:151], v[28:31], v[56:59]\n"
    "v_mfma_f32_16x16x32_bf16 v[60:63], v[152:155], v[28:31], v[60:63]\n"
    "s_waitcnt lgkmcnt(2)\n"
    "v_mfma_f32_16x16x32_bf16 v[68:71], v[156:159], v[28:31], v[68:71]\n"
    "ds_read_b128 v[28:31], v9 offset:31744\n"
    "v_mfma_f32_16x16x32_bf16 v[56:59], v[160:163], v[16:19], v[56:59]\n"
    "v_mfma_f32_16x16x32_bf16 v[60:63], v[164:167], v[16:19], v[60:63]\n"
    "s_waitcnt lgkmcnt(2)\n"
    "v_mfma_f32_16x16x32_bf16 v[68:71], v[168:171], v[16:19], v[68:71]\n"
    "ds_read_b128 v[16:19], v9 offset:32768\n"
    "v_mfma_f32_16x16x32_bf16 v[56:59], v[172:175], v[20:23], v[56:59]\n"
    "v_mfma_f32_16x16x32_bf16 v[60:63], v[176:179], v[20:23], v[60:63]\n"
    "s_waitcnt lgkmcnt(2)\n"
    "v_mfma_f32_16x16x32_bf16 v[68:71], v[180:183], v[20:23], v[68:71]\n"
    "ds_read_b128 v[20:23], v9 offset:33792\n"
    "v_mfma_f32_16x16x32_bf16 v[56:59], v[184:187], v[24:27], v[56:59]\n"
    "v_mfma_f32_16x16x32_bf16 v[60:63], v[188:191], v[24:27], v[60:63]\n"
    "s_waitcnt lgkmcnt(2)\n"
    "s_waitcnt vmcnt(2)\n"
    "v_add_u32 v13, v11, v3\n"
    "v_mfma_f32_16x16x32_bf16 v[68:71], v[192:195], v[24:27], v[68:71]\n"
    "ds_read_b128 v[24:27], v9 offset:34816\n"
    "v_cmp_gt_i32 vcc, s71, v13\n"
    "v_mfma_f32_16x16x32_bf16 v[56:59], v[196:199], v[28:31], v[56:59]\n"
    "v_mfma_f32_16x16x32_bf16 v[60:63], v[200:203], v[28:31], v[60:63]\n"
    "s_waitcnt lgkmcnt(2)\n"
    "v_mfma_f32_16x16x32_bf16 v[68:71], v[204:207], v[28:31], v[68:71]\n"
    "ds_read_b128 v[28:31], v9 offset:35840\n"
    "s_cbranch_vccz .LgDOWN_dma1_96_%=\n"
    "v_mfma_f32_16x16x32_bf16 v[56:59], v[208:211], v[16:19], v[56:59]\n"
    "s_or_b64 exec, vcc, s[62:63]\n"
    "buffer_atomic_smax v11, v2, s[36:39], 0 offen sc0 sc1\n"
    "s_mov_b64 exec, -1\n"
    "v_mfma_f32_16x16x32_bf16 v[60:63], v[212:215], v[16:19], v[60:63]\n"
    "s_waitcnt lgkmcnt(2)\n"
    "v_mfma_f32_16x16x32_bf16 v[68:71], v[216:219], v[16:19], v[68:71]\n"
    "v_mfma_f32_16x16x32_bf16 v[56:59], v[220:223], v[20:23], v[56:59]\n"
    "v_mfma_f32_16x16x32_bf16 v[60:63], v[224:227], v[20:23], v[60:63]\n"
    "s_waitcnt lgkmcnt(1)\n"
    "v_mfma_f32_16x16x32_bf16 v[68:71], v[228:231], v[20:23], v[68:71]\n"
    "v_mfma_f32_16x16x32_bf16 v[56:59], v[232:235], v[24:27], v[56:59]\n"
    "v_mfma_f32_16x16x32_bf16 v[60:63], v[236:239], v[24:27], v[60:63]\n"
    "s_waitcnt lgkmcnt(0)\n"
    "v_mfma_f32_16x16x32_bf16 v[68:71], v[240:243], v[24:27], v[68:71]\n"
    "v_mfma_f32_16x16x32_bf16 v[56:59], v[244:247], v[28:31], v[56:59]\n"
    "v_mfma_f32_16x16x32_bf16 v[60:63], v[248:251], v[28:31], v[60:63]\n"
    "v_mfma_f32_16x16x32_bf16 v[68:71], v[252:255], v[28:31], v[68:71]\n"
    "v_cvt_pk_bf16_f32 v56, v56, v57\n"
    "v_cvt_pk_bf16_f32 v57, v58, v59\n"
    "s_nop 1\n"
    "v_cvt_pk_bf16_f32 v58, v60, v61\n"
    "v_cvt_pk_bf16_f32 v59, v62, v63\n"
    "buffer_store_dwordx4 v[56:59], v15, s[36:39], s73 offen offset:1024 sc1\n"
    "s_nop 0\n"
    "v_cvt_pk_bf16_f32 v68, v68, v69\n"
    "v_cvt_pk_bf16_f32 v69, v70, v71\n"
    "s_nop 0\n"
    "v_permlane16_swap_b32 v68, v70\n"
    "v_permlane16_swap_b32 v69, v71\n"
    "buffer_store_dwordx4 v[68:71], v4, s[36:39], s73 offen offset:1024 sc1\n"
    "v_mov_b32 v12, s68\n"
    "s_mov_b32 s73, 0x80000000\n"
    "s_mov_b32 s68, s71\n"
    "s_cmp_eq_u32 s68, 128\n"
    "s_cbranch_scc1 .LgDOWN_done_%=\n"
    "s_waitcnt vmcnt(3)\n"
    "v_add_u32 v13, v5, v3\n"
    "v_cmp_gt_i32 vcc, s71, v13\n"
    "s_cbranch_vccnz .LgDOWN_idle1_%=\n"
    "s_add_u32 m0, s59, 0\n"
    "s_nop 0\n"
    "buffer_load_dwordx4 v1, s[36:39], s76 offen sc1 lds\n"
    "s_add_u32 m0, s59, 4096\n"
    "s_add_u32 s76, s76, 16384\n"
    "buffer_load_dwordx4 v1, s[36:39], s76 offen sc1 lds\n"
    "s_add_u32 m0, s59, 8192\n"
    "s_add_u32 s76, s76, 16384\n"
    "buffer_load_dwordx4 v1, s[36:39], s76 offen sc1 lds\n"
    "s_add_u32 m0, s59, 12288\n"
    "s_add_u32 s76, s76, 16384\n"
    "buffer_load_dwordx4 v1, s[36:39], s76 offen sc1 lds\n"
    "s_add_u32 m0, s59, 16384\n"
    "s_add_u32 s76, s76, 16384\n"
    "buffer_load_dwordx4 v1, s[36:39], s76 offen sc1 lds\n"
    "s_add_u32 m0, s59, 20480\n"
    "s_add_u32 s76, s76, 16384\n"
    "buffer_load_dwordx4 v1, s[36:39], s76 offen sc1 lds\n"
    "s_add_u32 m0, s59, 24576\n"
    "s_add_u32 s76, s76, 16384\n"
    "buffer_load_dwordx4 v1, s[36:39], s76 offen sc1 lds\n"
    "s_add_u32 m0, s59, 28672\n"
    "s_add_u32 s76, s76, 16384\n"
    "buffer_load_dwordx4 v1, s[36:39], s76 offen sc1 lds\n"
    "s_add_u32 m0, s59, 32768\n"
    "s_add_u32 s76, s76, 16384\n"
    "buffer_load_dwordx4 v1, s[36:39], s76 offen sc1 lds\n"
    "s_waitcnt vmcnt(9)\n"
    "buffer_atomic_smax v12, v10, s[36:39], 0 offen sc1\n"
    "s_mov_b32 s69, 0x80000000\n"
    "s_mov_b32 s70, 0x80000000\n"
    "v_mov_b32 v7, v12\n"
    "s_waitcnt vmcnt(12)\n"
    "s_add_u32 s71, s68, 1\n"
    "v_add_u32 v13, v11, v3\n"
    "v_cmp_gt_i32 vcc, s71, v13\n"
    "s_nop 0\n"
    "s_or_b64 exec, vcc, s[62:63]\n"
    "buffer_atomic_smax v11, v2, s[36:39], 0 offen sc0 sc1\n"
    "s_mov_b64 exec, -1\n"
    "s_waitcnt vmcnt(2)\n"
    "s_branch .LgDOWN_bar0_%=\n"
    ".LgDOWN_dma1_33_%=:\n"
    "v_mfma_f32_16x16x32_bf16 v[56:59], a[132:135], v[28:31], v[56:59]\n"
    "s_add_u32 m0, s59, 0\n"
    "v_mfma_f32_16x16x32_bf16 v[60:63], a[136:139], v[28:31], v[60:63]\n"
    "s_waitcnt lgkmcnt(2)\n"
    "buffer_load_dwordx4 v1, s[36:39], s76 offen sc1 lds\n"
    "v_mfma_f32_16x16x32_bf16 v[68:71], a[140:143], v[28:31], v[68:71]\n"
    "ds_read_b128 v[28:31], v9 offset:15360\n"
    "s_add_u32 s76, s76, 16384\n"
    "v_mfma_f32_16x16x32_bf16 v[56:59], a[144:147], v[16:19], v[56:59]\n"
    "s_add_u32 m0, s59, 4096\n"
    "v_mfma_f32_16x16x32_bf16 v[60:63], a[148:151], v[16:19], v[60:63]\n"
    "s_waitcnt lgkmcnt(2)\n"
    "buffer_load_dwordx4 v1, s[36:39], s76 offen sc1 lds\n"
    "v_mfma_f32_16x16x32_bf16 v[68:71], a[152:155], v[16:19], v[68:71]\n"
    "ds_read_b128 v[16:19], v9 offset:16384\n"
    "v_mfma_f32_16x16x32_bf16 v[56:59], a[156:159], v[20:23], v[56:59]\n"
    "s_add_u32 s76, s76, 16384\n"
    "s_add_u32 m0, s59, 8192\n"
    "v_mfma_f32_16x16x32_bf16 v[60:63], a[160:163], v[20:23], v[60:63]\n"
    "s_waitcnt lgkmcnt(2)\n"
    "buffer_load_dwordx4 v1, s[36:39], s76 offen sc1 lds\n"
    "v_mfma_f32_16x16x32_bf16 v[68:71], a[164:167], v[20:23], v[68:71]\n"
    "ds_read_b128 v[20:23], v9 offset:17408\n"
    "v_mfma_f32_16x16x32_bf16 v[56:59], a[168:171], v[24:27], v[56:59]\n"
    "v_mfma_f32_16x16x32_bf16 v[60:63], a[172:175], v[24:27], v[60:63]\n"
    "s_waitcnt lgkmcnt(2)\n"
    "s_add_u32 s76, s76, 16384\n"
    "s_add_u32 m0, s59, 12288\n"
    "v_mfma_f32_16x16x32_bf16 v[68:71], a[176:179], v[24:27], v[68:71]\n"
    "ds_read_b128 v[24:27], v9 offset:18432\n"
    "buffer_load_dwordx4 v1, s[36:39], s76 offen sc1 lds\n"
    "v_mfma_f32_16x16x32_bf16 v[56:59], a[180:183], v[28:31], v[56:59]\n"
    "v_mfma_f32_16x16x32_bf16 v[60:63], a[184:187], v[28:31], v[60:63]\n"
    "s_waitcnt lgkmcnt(2)\n"
    "v_mfma_f32_16x16x32_bf16 v[68:71], a[188:191], v[28:31], v[68:71]\n"
    "ds_read_b128 v[28:31], v9 offset:19456\n"
    "s_add_u32 s76, s76, 16384\n"
    "v_mfma_f32_16x16x32_bf16 v[56:59], a[192:195], v[16:19], v[56:59]\n"
    "s_add_u32 m0, s59, 16384\n"
    "v_mfma_f32_16x16x32_bf16 v[60:63], a[196:199], v[16:19], v[60:63]\n"
    "s_waitcnt lgkmcnt(2)\n"
    "buffer_load_dwordx4 v1, s[36:39], s76 offen sc1 lds\n"
    "v_mfma_f32_16x16x32_bf16 v[68:71], a[200:203], v[16:19], v[68:71]\n"
    "ds_read_b128 v[16:19], v9 offset:20480\n"
    "v_mfma_f32_16x16x32_bf16 v[56:59], a[204:207], v[20:23], v[56:59]\n"
    "s_add_u32 s76, s76, 16384\n"
    "s_add_u32 m0, s59, 20480\n"
    "v_mfma_f32_16x16x32_bf16 v[60:63], a[208:211], v[20:23], v[60:63]\n"
    "s_waitcnt lgkmcnt(2)\n"
    "buffer_load_dwordx4 v1, s[36:39], s76 offen sc1 lds\n"
    "v_mfma_f32_16x16x32_bf16 v[68:71], a[212:215], v[20:23], v[68:71]\n"
    "ds_read_b128 v[20:23], v9 offset:21504\n"
    "v_mfma_f32_16x16x32_bf16 v[56:59], a[216:219], v[24:27], v[56:59]\n"
    "v_mfma_f32_16x16x32_bf16 v[60:63], a[220:223], v[24:27], v[60:63]\n"
    "s_waitcnt lgkmcnt(2)\n"
    "s_waitcnt vmcnt(6)\n"
    "buffer_atomic_smax v12, v14, s[36:39], s70 offen sc1\n"
    "v_mfma_f32_16x16x32_bf16 v[68:71], a[224:227], v[24:27], v[68:71]\n"
    "ds_read_b128 v[24:27], v9 offset:22528\n"
    "s_add_u32 s76, s76, 16384\n"
    "v_mfma_f32_16x16x32_bf16 v[56:59], a[228:231], v[28:31], v[56:59]\n"
    "s_add_u32 m0, s59, 24576\n"
    "v_mfma_f32_16x16x32_bf16 v[60:63], a[232:235], v[28:31], v[60:63]\n"
    "s_waitcnt lgkmcnt(2)\n"
    "buffer_load_dwordx4 v1, s[36:39], s76 offen sc1 lds\n"
    "v_mfma_f32_16x16x32_bf16 v[68:71], a[236:239], v[28:31], v[68:71]\n"
    "ds_read_b128 v[28:31], v9 offset:23552\n"
    "s_add_u32 s76, s76, 16384\n"
    "v_mfma_f32_16x16x32_bf16 v[56:59], a[240:243], v[16:19], v[56:59]\n"
    "s_add_u32 m0, s59, 28672\n"
    "v_mfma_f32_16x16x32_bf16 v[60:63], a[244:247], v[16:19], v[60:63]\n"
    "s_waitcnt lgkmcnt(2)\n"
    "buffer_load_dwordx4 v1, s[36:39], s76 offen sc1 lds\n"
    "v_mfma_f32_16x16x32_bf16 v[68:71], a[248:251], v[16:19], v[68:71]\n"
    "ds_read_b128 v[16:19], v9 offset:24576\n"
    "v_mfma_f32_16x16x32_bf16 v[56:59], a[252:255], v[20:23], v[56:59]\n"
    "s_add_u32 s76, s76, 16384\n"
    "s_add_u32 m0, s59, 32768\n"
    "v_mfma_f32_16x16x32_bf16 v[60:63], v[80:83], v[20:23], v[60:63]\n"
    "s_waitcnt lgkmcnt(2)\n"
    "buffer_load_dwordx4 v1, s[36:39], s76 offen sc1 lds\n"
    "v_mfma_f32_16x16x32_bf16 v[68:71], v[84:87], v[20:23], v[68:71]\n"
    "ds_read_b128 v[20:23], v9 offset:25600\n"
    "v_mfma_f32_16x16x32_bf16 v[56:59], v[88:91], v[24:27], v[56:59]\n"
    "v_mfma_f32_16x16x32_bf16 v[60:63], v[92:95], v[24:27], v[60:63]\n"
    "s_waitcnt lgkmcnt(2)\n"
    "v_mfma_f32_16x16x32_bf16 v[68:71], v[96:99], v[24:27], v[68:71]\n"
    "ds_read_b128 v[24:27], v9 offset:26624\n"
    "v_mfma_f32_16x16x32_bf16 v[56:59], v[100:103], v[28:31], v[56:59]\n"
    "v_mfma_f32_16x16x32_bf16 v[60:63], v[104:107], v[28:31], v[60:63]\n"
    "s_waitcnt lgkmcnt(2)\n"
    "v_mfma_f32_16x16x32_bf16 v[68:71], v[108:111], v[28:31], v[68:71]\n"
    "ds_read_b128 v[28:31], v9 offset:27648\n"
    "s_branch .LgDOWN_join1_%=\n"
    ".LgDOWN_dma1_54_%=:\n"
    "v_mfma_f32_16x16x32_bf16 v[56:59], a[216:219], v[24:27], v[56:59]\n"
    "s_add_u32 m0, s59, 0\n"
    "v_mfma_f32_16x16x32_bf16 v[60:63], a[220:223], v[24:27], v[60:63]\n"
    "s_waitcnt lgkmcnt(2)\n"
    "s_waitcnt vmcnt(1)\n"
    "buffer_atomic_smax v12, v14, s[36:39], s70 offen sc1\n"
    "v_mfma_f32_16x16x32_bf16 v[68:71], a[224:227], v[24:27], v[68:71]\n"
    "ds_read_b128 v[24:27], v9 offset:22528\n"
    "buffer_load_dwordx4 v1, s[36:39], s76 offen sc1 lds\n"
    "v_mfma_f32_16x16x32_bf16 v[56:59], a[228:231], v[28:31], v[56:59]\n"
    "s_add_u32 s76, s76, 16384\n"
    "s_add_u32 m0, s59, 4096\n"
    "v_mfma_f32_16x16x32_bf16 v[60:63], a[232:235], v[28:31], v[60:63]\n"
    "s_waitcnt lgkmcnt(2)\n"
    "buffer_load_dwordx4 v1, s[36:39], s76 offen sc1 lds\n"
    "v_mfma_f32_16x16x32_bf16 v[68:71], a[236:239], v[28:31], v[68:71]\n"
    "ds_read_b128 v[28:31], v9 offset:23552\n"
    "v_mfma_f32_16x16x32_bf16 v[56:59], a[240:243], v[16:19], v[56:59]\n"
    "s_add_u32 s76, s76, 16384\n"
    "s_add_u32 m0, s59, 8192\n"
    "v_mfma_f32_16x16x32_bf16 v[60:63], a[244:247], v[16:19], v[60:63]\n"
    "s_waitcnt lgkmcnt(2)\n"
    "buffer_load_dwordx4 v1, s[36:39], s76 offen sc1 lds\n"
    "v_mfma_f32_16x16x32_bf16 v[68:71], a[248:251], v[16:19], v[68:71]\n"
    "ds_read_b128 v[16:19], v9 offset:24576\n"
    "v_mfma_f32_16x16x32_bf16 v[56:59], a[252:255], v[20:23], v[56:59]\n"
    "v_mfma_f32_16x16x32_bf16 v[60:63], v[80:83], v[20:23], v[60:63]\n"
    "s_waitcnt lgkmcnt(2)\n"
    "s_add_u32 s76, s76, 16384\n"
    "s_add_u32 m0, s59, 12288\n"
    "v_mfma_f32_16x16x32_bf16 v[68:71], v[84:87], v[20:23], v[68:71]\n"
    "ds_read_b128 v[20:23], v9 offset:25600\n"
    "buffer_load_dwordx4 v1, s[36:39], s76 offen sc1 lds\n"
    "v_mfma_f32_16x16x32_bf16 v[56:59], v[88:91], v[24:27], v[56:59]\n"
    "v_mfma_f32_16x16x32_bf16 v[60:63], v[92:95], v[24:27], v[60:63]\n"
    "s_waitcnt lgkmcnt(2)\n"
    "v_mfma_f32_16x16x32_bf16 v[68:71], v[96:99], v[24:27], v[68:71]\n"
    "ds_read_b128 v[24:27], v9 offset:26624\n"
    "s_add_u32 s76, s76, 16384\n"
    "v_mfma_f32_16x16x32_bf16 v[56:59], v[100:103], v[28:31], v[56:59]\n"
    "s_add_u32 m0, s59, 16384\n"
    "v_mfma_f32_16x16x32_bf16 v[60:63], v[104:107], v[28:31], v[60:63]\n"
    "s_waitcnt lgkmcnt(2)\n"
    "buffer_load_dwordx4 v1, s[36:39], s76 offen sc1 lds\n"
    "v_mfma_f32_16x16x32_bf16 v[68:71], v[108:111], v[28:31], v[68:71]\n"
    "ds_read_b128 v[28:31], v9 offset:27648\n"
    "v_mfma_f32_16x16x32_bf16 v[56:59], v[112:115], v[16:19], v[56:59]\n"
    "s_add_u32 s76, s76, 16384\n"
    "s_add_u32 m0, s59, 20480\n"
    "v_mfma_f32_16x16x32_bf16 v[60:63], v[116:119], v[16:19], v[60:63]\n"
    "s_waitcnt lgkmcnt(2)\n"
    "buffer_load_dwordx4 v1, s[36:39], s76 offen sc1 lds\n"
    "v_mfma_f32_16x16x32_bf16 v[68:71], v[120:123], v[16:19], v[68:71]\n"
    "ds_read_b128 v[16:19], v9 offset:28672\n"
    "v_mfma_f32_16x16x32_bf16 v[56:59], v[124:127], v[20:23], v[56:59]\n"
    "v_mfma_f32_16x16x32_bf16 v[60:63], v[128:131], v[20:23], v[60:63]\n"
    "s_waitcnt lgkmcnt(2)\n"
    "s_add_u32 s76, s76, 16384\n"
    "s_add_u32 m0, s59, 24576\n"
    "v_mfma_f32_16x16x32_bf16 v[68:71], v[132:135], v[20:23], v[68:71]\n"
    "ds_read_b128 v[20:23], v9 offset:29696\n"
    "buffer_load_dwordx4 v1, s[36:39], s76 offen sc1 lds\n"
    "v_mfma_f32_16x16x32_bf16 v[56:59], v[136:139], v[24:27], v[56:59]\n"
    "v_mfma_f32_16x16x32_bf16 v[60:63], v[140:143], v[24:27], v[60:63]\n"
    "s_waitcnt lgkmcnt(2)\n"
    "v_mfma_f32_16x16x32_bf16 v[68:71], v[144:147], v[24:27], v[68:71]\n"
    "ds_read_b128 v[24:27], v9 offset:30720\n"
    "s_add_u32 s76, s76, 16384\n"
    "v_mfma_f32_16x16x32_bf16 v[56:59], v[148:151], v[28:31], v[56:59]\n"
    "s_add_u32 m0, s59, 28672\n"
    "v_mfma_f32_16x16x32_bf16 v[60:63], v[152:155], v[28:31], v[60:63]\n"
    "s_waitcnt lgkmcnt(2)\n"
    "buffer_load_dwordx4 v1, s[36:39], s76 offen sc1 lds\n"
    "v_mfma_f32_16x16x32_bf16 v[68:71], v[156:159], v[28:31], v[68:71]\n"
    "ds_read_b128 v[28:31], v9 offset:31744\n"
    "v_mfma_f32_16x16x32_bf16 v[56:59], v[160:163], v[16:19], v[56:59]\n"
    "s_add_u32 s76, s76, 16384\n"
    "s_add_u32 m0, s59, 32768\n"
    "v_mfma_f32_16x16x32_bf16 v[60:63], v[164:167], v[16:19], v[60:63]\n"
    "s_waitcnt lgkmcnt(2)\n"
    "buffer_load_dwordx4 v1, s[36:39], s76 offen sc1 lds\n"
    "v_mfma_f32_16x16x32_bf16 v[68:71], v[168:171], v[16:19], v[68:71]\n"
    "ds_read_b128 v[16:19], v9 offset:32768\n"
    "v_mfma_f32_16x16x32_bf16 v[56:59], v[172:175], v[20:23], v[56:59]\n"
    "v_mfma_f32_16x16x32_bf16 v[60:63], v[176:179], v[20:23], v[60:63]\n"
    "s_waitcnt lgkmcnt(2)\n"
    "v_mfma_f32_16x16x32_bf16 v[68:71], v[180:183], v[20:23], v[68:71]\n"
    "ds_read_b128 v[20:23], v9 offset:33792\n"
    "v_mfma_f32_16x16x32_bf16 v[56:59], v[184:187], v[24:27], v[56:59]\n"
    "v_mfma_f32_16x16x32_bf16 v[60:63], v[188:191], v[24:27], v[60:63]\n"
    "s_waitcnt lgkmcnt(2)\n"
    "v_mfma_f32_16x16x32_bf16 v[68:71], v[192:195], v[24:27], v[68:71]\n"
    "ds_read_b128 v[24:27], v9 offset:34816\n"
    "v_mfma_f32_16x16x32_bf16 v[56:59], v[196:199], v[28:31], v[56:59]\n"
    "v_mfma_f32_16x16x32_bf16 v[60:63], v[200:203], v[28:31], v[60:63]\n"
    "s_waitcnt lgkmcnt(2)\n"
    "v_mfma_f32_16x16x32_bf16 v[68:71], v[204:207], v[28:31], v[68:71]\n"
    "ds_read_b128 v[28:31], v9 offset:35840\n"
    "v_mfma_f32_16x16x32_bf16 v[56:59], v[208:211], v[16:19], v[56:59]\n"
    "v_mfma_f32_16x16x32_bf16 v[60:63], v[212:215], v[16:19], v[60:63]\n"
    "s_waitcnt lgkmcnt(2)\n"
    "v_mfma_f32_16x16x32_bf16 v[68:71], v[216:219], v[16:19], v[68:71]\n"
    "v_mfma_f32_16x16x32_bf16 v[56:59], v[220:223], v[20:23], v[56:59]\n"
    "v_mfma_f32_16x16x32_bf16 v[60:63], v[224:227], v[20:23], v[60:63]\n"
    "s_waitcnt lgkmcnt(1)\n"
    "v_mfma_f32_16x16x32_bf16 v[68:71], v[228:231], v[20:23], v[68:71]\n"
    "v_mfma_f32_16x16x32_bf16 v[56:59], v[232:235], v[24:27], v[56:59]\n"
    "v_mfma_f32_16x16x32_bf16 v[60:63], v[236:239], v[24:27], v[60:63]\n"
    "s_waitcnt lgkmcnt(0)\n"
    "v_mfma_f32_16x16x32_bf16 v[68:71], v[240:243], v[24:27], v[68:71]\n"
    "v_mfma_f32_16x16x32_bf16 v[56:59], v[244:247], v[28:31], v[56:59]\n"
    "v_mfma_f32_16x16x32_bf16 v[60:63], v[248:251], v[28:31], v[60:63]\n"
    "v_mfma_f32_16x16x32_bf16 v[68:71], v[252:255], v[28:31], v[68:71]\n"
    "v_cvt_pk_bf16_f32 v56, v56, v57\n"
    "v_cvt_pk_bf16_f32 v57, v58, v59\n"
    "s_nop 1\n"
    "v_cvt_pk_bf16_f32 v58, v60, v61\n"
    "v_cvt_pk_bf16_f32 v59, v62, v63\n"
    "buffer_store_dwordx4 v[56:59], v15, s[36:39], s73 offen offset:1024 sc1\n"
    "s_nop 0\n"
    "v_cvt_pk_bf16_f32 v68, v68, v69\n"
    "v_cvt_pk_bf16_f32 v69, v70, v71\n"
    "s_nop 0\n"
    "v_permlane16_swap_b32 v68, v70\n"
    "v_permlane16_swap_b32 v69, v71\n"
    "buffer_store_dwordx4 v[68:71], v4, s[36:39], s73 offen offset:1024 sc1\n"
    "v_mov_b32 v12, s68\n"
    "s_mov_b32 s73, 0x80000000\n"
    "s_mov_b32 s68, s71\n"
    "s_mov_b32 s69, 0\n"
    "s_mov_b32 s70, 0x80000000\n"
    "v_mov_b32 v7, v12\n"
    "s_waitcnt vmcnt(12)\n"
    "s_add_u32 s71, s68, 1\n"
    "v_add_u32 v13, v11, v3\n"
    "v_cmp_gt_i32 vcc, s71, v13\n"
    "s_nop 0\n"
    "s_or_b64 exec, vcc, s[62:63]\n"
    "buffer_atomic_smax v11, v2, s[36:39], 0 offen sc0 sc1\n"
    "s_mov_b64 exec, -1\n"
    "s_waitcnt vmcnt(3)\n"
    "s_branch .LgDOWN_bar0_%=\n"
    ".LgDOWN_dma1_75_%=:\n"
    "v_mfma_f32_16x16x32_bf16 v[56:59], v[124:127], v[20:23], v[56:59]\n"
    "s_add_u32 m0, s59, 0\n"
    "v_mfma_f32_16x16x32_bf16 v[60:63], v[128:131], v[20:23], v[60:63]\n"
    "s_waitcnt lgkmcnt(2)\n"
    "buffer_load_dwordx4 v1, s[36:39], s76 offen sc1 lds\n"
    "v_mfma_f32_16x16x32_bf16 v[68:71], v[132:135], v[20:23], v[68:71]\n"
    "ds_read_b128 v[20:23], v9 offset:29696\n"
    "s_add_u32 s76, s76, 16384\n"
    "v_mfma_f32_16x16x32_bf16 v[56:59], v[136:139], v[24:27], v[56:59]\n"
    "s_add_u32 m0, s59, 4096\n"
    "v_mfma_f32_16x16x32_bf16 v[60:63], v[140:143], v[24:27], v[60:63]\n"
    "s_waitcnt lgkmcnt(2)\n"
    "buffer_load_dwordx4 v1, s[36:39], s76 offen sc1 lds\n"
    "v_mfma_f32_16x16x32_bf16 v[68:71], v[144:147], v[24:27], v[68:71]\n"
    "ds_read_b128 v[24:27], v9 offset:30720\n"
    "v_mfma_f32_16x16x32_bf16 v[56:59], v[148:151], v[28:31], v[56:59]\n"
    "s_add_u32 s76, s76, 16384\n"
    "s_add_u32 m0, s59, 8192\n"
    "v_mfma_f32_16x16x32_bf16 v[60:63], v[152:155], v[28:31], v[60:63]\n"
    "s_waitcnt lgkmcnt(2)\n"
    "buffer_load_dwordx4 v1, s[36:39], s76 offen sc1 lds\n"
    "v_mfma_f32_16x16x32_bf16 v[68:71], v[156:159], v[28:31], v[68:71]\n"
    "ds_read_b128 v[28:31], v9 offset:31744\n"
    "v_mfma_f32_16x16x32_bf16 v[56:59], v[160:163], v[16:19], v[56:59]\n"
    "v_mfma_f32_16x16x32_bf16 v[60:63], v[164:167], v[16:19], v[60:63]\n"
    "s_waitcnt lgkmcnt(2)\n"
    "s_add_u32 s76, s76, 16384\n"
    "s_add_u32 m0, s59, 12288\n"
    "v_mfma_f32_16x16x32_bf16 v[68:71], v[168:171], v[16:19], v[68:71]\n"
    "ds_read_b128 v[16:19], v9 offset:32768\n"
    "buffer_load_dwordx4 v1, s[36:39], s76 offen sc1 lds\n"
    "v_mfma_f32_16x16x32_bf16 v[56:59], v[172:175], v[20:23], v[56:59]\n"
    "v_mfma_f32_16x16x32_bf16 v[60:63], v[176:179], v[20:23], v[60:63]\n"
    "s_waitcnt lgkmcnt(2)\n"
    "v_mfma_f32_16x16x32_bf16 v[68:71], v[180:183], v[20:23], v[68:71]\n"
    "ds_read_b128 v[20:23], v9 offset:33792\n"
    "s_add_u32 s76, s76, 16384\n"
    "v_mfma_f32_16x16x32_bf16 v[56:59], v[184:187], v[24:27], v[56:59]\n"
    "s_add_u32 m0, s59, 16384\n"
    "v_mfma_f32_16x16x32_bf16 v[60:63], v[188:191], v[24:27], v[60:63]\n"
    "s_waitcnt lgkmcnt(2)\n"
    "buffer_load_dwordx4 v1, s[36:39], s76 offen sc1 lds\n"
    "v_mfma_f32_16x16x32_bf16 v[68:71], v[192:195], v[24:27], v[68:71]\n"
    "ds_read_b128 v[24:27], v9 offset:34816\n"
    "v_mfma_f32_16x16x32_bf16 v[56:59], v[196:199], v[28:31], v[56:59]\n"
    "s_add_u32 s76, s76, 16384\n"
    "s_add_u32 m0, s59, 20480\n"
    "v_mfma_f32_16x16x32_bf16 v[60:63], v[200:203], v[28:31], v[60:63]\n"
    "s_waitcnt lgkmcnt(2)\n"
    "buffer_load_dwordx4 v1, s[36:39], s76 offen sc1 lds\n"
    "v_mfma_f32_16x16x32_bf16 v[68:71], v[204:207], v[28:31], v[68:71]\n"
    "ds_read_b128 v[28:31], v9 offset:35840\n"
    "v_mfma_f32_16x16x32_bf16 v[56:59], v[208:211], v[16:19], v[56:59]\n"
    "v_mfma_f32_16x16x32_bf16 v[60:63], v[212:215], v[16:19], v[60:63]\n"
    "s_waitcnt lgkmcnt(2)\n"
    "s_add_u32 s76, s76, 16384\n"
    "s_add_u32 m0, s59, 24576\n"
    "v_mfma_f32_16x16x32_bf16 v[68:71], v[216:219], v[16:19], v[68:71]\n"
    "buffer_load_dwordx4 v1, s[36:39], s76 offen sc1 lds\n"
    "v_mfma_f32_16x16x32_bf16 v[56:59], v[220:223], v[20:23], v[56:59]\n"
    "v_mfma_f32_16x16x32_bf16 v[60:63], v[224:227], v[20:23], v[60:63]\n"
    "s_waitcnt lgkmcnt(1)\n"
    "v_mfma_f32_16x16x32_bf16 v[68:71], v[228:231], v[20:23], v[68:71]\n"
    "s_add_u32 s76, s76, 16384\n"
    "v_mfma_f32_16x16x32_bf16 v[56:59], v[232:235], v[24:27], v[56:59]\n"
    "s_add_u32 m0, s59, 28672\n"
    "v_mfma_f32_16x16x32_bf16 v[60:63], v[236:239], v[24:27], v[60:63]\n"
    "s_waitcnt lgkmcnt(0)\n"
    "buffer_load_dwordx4 v1, s[36:39], s76 offen sc1 lds\n"
    "v_mfma_f32_16x16x32_bf16 v[68:71], v[240:243], v[24:27], v[68:71]\n"
    "v_mfma_f32_16x16x32_bf16 v[56:59], v[244:247], v[28:31], v[56:59]\n"
    "s_add_u32 s76, s76, 16384\n"
    "s_add_u32 m0, s59, 32768\n"
    "v_mfma_f32_16x16x32_bf16 v[60:63], v[248:251], v[28:31], v[60:63]\n"
    "buffer_load_dwordx4 v1, s[36:39], s76 offen sc1 lds\n"
    "v_mfma_f32_16x16x32_bf16 v[68:71], v[252:255], v[28:31], v[68:71]\n"
    "v_cvt_pk_bf16_f32 v56, v56, v57\n"
    "v_cvt_pk_bf16_f32 v57, v58, v59\n"
    "s_nop 1\n"
    "v_cvt_pk_bf16_f32 v58, v60, v61\n"
    "v_cvt_pk_bf16_f32 v59, v62, v63\n"
    "buffer_store_dwordx4 v[56:59], v15, s[36:39], s73 offen offset:1024 sc1\n"
    "s_nop 0\n"
    "v_cvt_pk_bf16_f32 v68, v68, v69\n"
    "v_cvt_pk_bf16_f32 v69, v70, v71\n"
    "s_nop 0\n"
    "v_permlane16_swap_b32 v68, v70\n"
    "v_permlane16_swap_b32 v69, v71\n"
    "buffer_store_dwordx4 v[68:71], v4, s[36:39], s73 offen offset:1024 sc1\n"
    "v_mov_b32 v12, s68\n"
    "s_mov_b32 s73, 0x80000000\n"
    "s_mov_b32 s68, s71\n"
    "s_mov_b32 s69, 0\n"
    "s_mov_b32 s70, 0x80000000\n"
    "v_mov_b32 v7, v12\n"
    "s_waitcnt vmcnt(12)\n"
    "s_add_u32 s71, s68, 1\n"
    "v_add_u32 v13, v11, v3\n"
    "v_cmp_gt_i32 vcc, s71, v13\n"
    "s_nop 0\n"
    "s_or_b64 exec, vcc, s[62:63]\n"
    "buffer_atomic_smax v11, v2, s[36:39], 0 offen sc0 sc1\n"
    "s_mov_b64 exec, -1\n"
    "s_waitcnt vmcnt(3)\n"
    "s_branch .LgDOWN_bar0_%=\n"
    ".LgDOWN_dma1_96_%=:\n"
    "v_mfma_f32_16x16x32_bf16 v[56:59], v[208:211], v[16:19], v[56:59]\n"
    "s_add_u32 m0, s59, 0\n"
    "v_mfma_f32_16x16x32_bf16 v[60:63], v[212:215], v[16:19], v[60:63]\n"
    "s_waitcnt lgkmcnt(2)\n"
    "buffer_load_dwordx4 v1, s[36:39], s76 offen sc1 lds\n"
    "v_mfma_f32_16x16x32_bf16 v[68:71], v[216:219], v[16:19], v[68:71]\n"
    "s_add_u32 s76, s76, 16384\n"
    "s_add_u32 m0, s59, 4096\n"
    "v_mfma_f32_16x16x32_bf16 v[56:59], v[220:223], v[20:23], v[56:59]\n"
    "buffer_load_dwordx4 v1, s[36:39], s76 offen sc1 lds\n"
    "v_mfma_f32_16x16x32_bf16 v[60:63], v[224:227], v[20:23], v[60:63]\n"
    "s_waitcnt lgkmcnt(1)\n"
    "v_mfma_f32_16x16x32_bf16 v[68:71], v[228:231], v[20:23], v[68:71]\n"
    "v_mfma_f32_16x16x32_bf16 v[56:59], v[232:235], v[24:27], v[56:59]\n"
    "s_add_u32 s76, s76, 16384\n"
    "s_add_u32 m0, s59, 8192\n"
    "v_mfma_f32_16x16x32_bf16 v[60:63], v[236:239], v[24:27], v[60:63]\n"
    "s_waitcnt lgkmcnt(0)\n"
    "buffer_load_dwordx4 v1, s[36:39], s76 offen sc1 lds\n"
    "v_mfma_f32_16x16x32_bf16 v[68:71], v[240:243], v[24:27], v[68:71]\n"
    "v_mfma_f32_16x16x32_bf16 v[56:59], v[244:247], v[28:31], v[56:59]\n"
    "v_mfma_f32_16x16x32_bf16 v[60:63], v[248:251], v[28:31], v[60:63]\n"
    "s_add_u32 s76, s76, 16384\n"
    "s_add_u32 m0, s59, 12288\n"
    "v_mfma_f32_16x16x32_bf16 v[68:71], v[252:255], v[28:31], v[68:71]\n"
    "buffer_load_dwordx4 v1, s[36:39], s76 offen sc1 lds\n"
    "v_cvt_pk_bf16_f32 v56, v56, v57\n"
    "v_cvt_pk_bf16_f32 v57, v58, v59\n"
    "s_nop 0\n"
    "v_cvt_pk_bf16_f32 v58, v60, v61\n"
    "v_cvt_pk_bf16_f32 v59, v62, v63\n"
    "buffer_store_dwordx4 v[56:59], v15, s[36:39], s73 offen offset:1024 sc1\n"
    "s_nop 0\n"
    "v_cvt_pk_bf16_f32 v68, v68, v69\n"
    "v_cvt_pk_bf16_f32 v69, v70, v71\n"
    "s_nop 0\n"
    "v_permlane16_swap_b32 v68, v70\n"
    "v_permlane16_swap_b32 v69, v71\n"
    "buffer_store_dwordx4 v[68:71], v4, s[36:39], s73 offen offset:1024 sc1\n"
    "v_mov_b32 v12, s68\n"
    "s_mov_b32 s73, 0x80000000\n"
    "s_mov_b32 s68, s71\n"
    "s_add_u32 m0, s59, 16384\n"
    "s_add_u32 s76, s76, 16384\n"
    "buffer_load_dwordx4 v1, s[36:39], s76 offen sc1 lds\n"
    "s_add_u32 m0, s59, 20480\n"
    "s_add_u32 s76, s76, 16384\n"
    "buffer_load_dwordx4 v1, s[36:39], s76 offen sc1 lds\n"
    "s_add_u32 m0, s59, 24576\n"
    "s_add_u32 s76, s76, 16384\n"
    "buffer_load_dwordx4 v1, s[36:39], s76 offen sc1 lds\n"
    "s_add_u32 m0, s59, 28672\n"
    "s_add_u32 s76, s76, 16384\n"
    "buffer_load_dwordx4 v1, s[36:39], s76 offen sc1 lds\n"
    "s_add_u32 m0, s59, 32768\n"
    "s_add_u32 s76, s76, 16384\n"
    "buffer_load_dwordx4 v1, s[36:39], s76 offen sc1 lds\n"
    "s_waitcnt vmcnt(5)\n"
    "buffer_atomic_smax v12, v10, s[36:39], 0 offen sc1\n"
    "s_mov_b32 s69, 0x80000000\n"
    "s_mov_b32 s70, 0x80000000\n"
    "v_mov_b32 v7, v12\n"
    "s_waitcnt vmcnt(12)\n"
    "s_add_u32 s71, s68, 1\n"
    "v_add_u32 v13, v11, v3\n"
    "v_cmp_gt_i32 vcc, s71, v13\n"
    "s_nop 0\n"
    "s_or_b64 exec, vcc, s[62:63]\n"
    "buffer_atomic_smax v11, v2, s[36:39], 0 offen sc0 sc1\n"
    "s_mov_b64 exec, -1\n"
    "s_waitcnt vmcnt(2)\n"
    "s_branch .LgDOWN_bar0_%=\n"
    ".LgDOWN_idle1_%=:\n"
    "s_or_b64 exec, vcc, s[62:63]\n"
    "buffer_atomic_smax v5, v2, s[36:39], 0 offen sc0 sc1\n"
    "s_mov_b64 exec, -1\n"
    "s_waitcnt vmcnt(1)\n"
    "buffer_atomic_smax v12, v10, s[36:39], 0 offen sc1\n"
    "s_branch .LgDOWN_block1_1_%=\n"
    ".LgDOWN_block1_%=:\n"
    "v_add_u32 v13, v11, v3\n"
    "v_cmp_gt_i32 vcc, s71, v13\n"
    "s_nop 0\n"
    "s_or_b64 exec, vcc, s[62:63]\n"
    "buffer_atomic_smax v11, v2, s[36:39], 0 offen sc0 sc1\n"
    "s_mov_b64 exec, -1\n"
    "s_sleep 3\n"
    "s_waitcnt vmcnt(1)\n"
    ".LgDOWN_block1_0_%=:\n"
    "v_add_u32 v13, v5, v3\n"
    "v_cmp_gt_i32 vcc, s71, v13\n"
    "s_cbranch_vccz .LgDOWN_go1_%=\n"
    "v_add_u32 v13, v5, v3\n"
    "v_cmp_gt_i32 vcc, s71, v13\n"
    "s_nop 0\n"
    "s_or_b64 exec, vcc, s[62:63]\n"
    "buffer_atomic_smax v5, v2, s[36:39], 0 offen sc0 sc1\n"
    "s_mov_b64 exec, -1\n"
    "s_sleep 3\n"
    "s_waitcnt vmcnt(1)\n"
    ".LgDOWN_block1_1_%=:\n"
    "v_add_u32 v13, v11, v3\n"
    "v_cmp_gt_i32 vcc, s71, v13\n"
    "s_cbranch_vccz .LgDOWN_go1_%=\n"
    "s_sub_u32 s72, s72, 1\n"
    "s_cbranch_scc0 .LgDOWN_block1_%=\n"
    "s_mov_b32 s72, 0\n"
    ".LgDOWN_go1_%=:\n"
    "s_lshr_b32 s76, s71, 1\n"
    "s_and_b32 s76, s76, s54\n"
    "s_mul_i32 s76, s76, s53\n"
    "s_add_u32 s76, s76, s52\n"
    "s_add_u32 m0, s59, 0\n"
    "s_nop 0\n"
    "buffer_load_dwordx4 v1, s[36:39], s76 offen sc1 lds\n"
    "s_add_u32 m0, s59, 4096\n"
    "s_add_u32 s76, s76, 16384\n"
    "buffer_load_dwordx4 v1, s[36:39], s76 offen sc1 lds\n"
    "s_add_u32 m0, s59, 8192\n"
    "s_add_u32 s76, s76, 16384\n"
    "buffer_load_dwordx4 v1, s[36:39], s76 offen sc1 lds\n"
    "s_add_u32 m0, s59, 12288\n"
    "s_add_u32 s76, s76, 16384\n"
    "buffer_load_dwordx4 v1, s[36:39], s76 offen sc1 lds\n"
    "s_add_u32 m0, s59, 16384\n"
    "s_add_u32 s76, s76, 16384\n"
    "buffer_load_dwordx4 v1, s[36:39], s76 offen sc1 lds\n"
    "s_add_u32 m0, s59, 20480\n"
    "s_add_u32 s76, s76, 16384\n"
    "buffer_load_dwordx4 v1, s[36:39], s76 offen sc1 lds\n"
    "s_add_u32 m0, s59, 24576\n"
    "s_add_u32 s76, s76, 16384\n"
    "buffer_load_dwordx4 v1, s[36:39], s76 offen sc1 lds\n"
    "s_add_u32 m0, s59, 28672\n"
    "s_add_u32 s76, s76, 16384\n"
    "buffer_load_dwordx4 v1, s[36:39], s76 offen sc1 lds\n"
    "s_add_u32 m0, s59, 32768\n"
    "s_add_u32 s76, s76, 16384\n"
    "buffer_load_dwordx4 v1, s[36:39], s76 offen sc1 lds\n"
    "s_mov_b32 s69, 0x80000000\n"
    "s_mov_b32 s70, 0x80000000\n"
    "v_mov_b32 v7, v12\n"
    "s_waitcnt vmcnt(9)\n"
    "s_add_u32 s71, s68, 1\n"
    "v_add_u32 v13, v11, v3\n"
    "v_cmp_gt_i32 vcc, s71, v13\n"
    "s_nop 0\n"
    "s_or_b64 exec, vcc, s[62:63]\n"
    "buffer_atomic_smax v11, v2, s[36:39], 0 offen sc0 sc1\n"
    "s_mov_b64 exec, -1\n"
    "s_waitcnt vmcnt(1)\n"
    "s_branch .LgDOWN_bar0_%=\n"
    ".LgDOWN_bar1_%=:\n"
    "s_barrier\n"
    "ds_read_b128 v[16:19], v9 offset:0\n"
    "ds_read_b128 v[20:23], v9 offset:1024\n"
    "ds_read_b128 v[24:27], v9 offset:2048\n"
    "ds_read_b128 v[28:31], v9 offset:3072\n"
    "v_add_u32 v13, v5, v3\n"
    "v_cmp_gt_i32 vcc, s71, v13\n"
    "s_nop 0\n"
    "s_or_b64 exec, vcc, s[62:63]\n"
    "buffer_atomic_smax v5, v2, s[36:39], 0 offen sc0 sc1\n"
    "s_mov_b64 exec, -1\n"
    "s_lshr_b32 s76, s71, 1\n"
    "s_and_b32 s76, s76, s54\n"
    "s_mul_i32 s76, s76, s53\n"
    "s_add_u32 s76, s76, s52\n"
    "s_waitcnt lgkmcnt(3)\n"
    "s_branch .LgDOWN_head1_%=\n"
    ".LgDOWN_FIN_%=:\n"
  );
}

DEVI void role_rec(const Params& p, int L, int j, int i, u32 dbg, int& rid, unsigned long long& tag, bool& sure, unsigned char* smem) {
  const int tid = threadIdx.x, lane = tid & 63, w = (int)rfl(tid >> 6), h = 8 * j + i;
  const int c = lane & 15, g = lane >> 4, pp = lane & 3;
  {
    v4u t;
    const int q0 = w == 0 ? 4 * (c >> 3) + (c & 3) : (c & 7);
    const u32 one = (w == 0 && ((c >> 2) & 1) != (g & 1)) ? 0u : 0x3F80u << (16 * (q0 & 1));
    const v4u t1 = {(u32)(((g & 1) * 16 + c) * 8), (u32)(g * 128 + (c & 7) * 16 + (c >> 3) * 8), c < 8 ? (u32)(g * 128 + c * 16) : OOB, (u32)((lane & 31) * 64 + (lane >> 5) * 8)};
    const v4u t2 = {(u32)(g * 16384 + c * 16), g == 0 ? (u32)(c * 16) : OOB,
                    (u32)((g >> 1) * 128 + (c & 7) * 16 + (2 * (g & 1) + (c >> 3)) * 32768), t1.w};
#pragma unroll
    for (int d = 0; d < 4; ++d) t[d] = w == 1 ? t1[d] : (w == 2 ? t2[d] : ((q0 >> 1) == d ? one : 0u));
    *(v4u*)(smem + 12288 + tid * 16) = t;
    asm volatile("s_waitcnt lgkmcnt(0)" ::: "memory");
  }
  const u32 wb = 576u * 4096 * 2 - h * 256;
  v16u sa = cat4(make_srd(p.ws, WS_END), make_srd(p.Wq + h * 128, wb), make_srd(p.Wk + h * 128, wb), make_srd(p.Wv + h * 128, wb));
  const v4u so = make_srd(p.Wo + h * 128 * 576, 128u * 1152);
  const int mine = 4 * i + j, unit = w == 0 ? mine : ((w == 1 && i == 3) ? 32 + j : -1);
  const u32 hot = HOT + L * 8192, hot2 = HOT2 + (1 - L) * 8192, img = hot2 + j * 2 * CHUNK;
  v16u sb = {so.x, so.y, so.z, so.w,
             pga(L, B_QKV + 3 * h + 1) + w * 2048, pga(L, B_KVM + h) + w * 1536, mbox_att(L, j, i, w), mbox(L, j, SL_REC + i, w),
             (u32)w * 1024, dbg + w * 64, INITF + L * 8192, hot + 4 * w * CHUNK + h * 256,
             unit < 0 ? OOB : hot + unit * CHUNK, unit < 0 ? OOB : hot2 + (unit >> 5) * CHUNK + (unit & 31) * 256, img, hot + (32 + w) * CHUNK + h * 256};
  const int jr = lane >> 3;
  const u32 row = (u32)(16 * (w >> 1) + 8 * ((jr >> 1) & 1) + 4 * (w & 1) + 2 * (jr >> 2) + (jr & 1));
  const u32 ch = (((u32)lane & 7) ^ wswz(jr, w)) << 4;
  v16u va = {0, 0, 0, 0, 0, 0, 0, 0, 0, 0, 0, 0, 0, 0, 0, 0};
  va[0] = lane * 16;
  va[1] = 16384 + w * 2048 + lane * 16;
  va[2] = (u32)((c >> 3) * 0xAAAA) | 0x4444u;
  const int qq = (lane >> 2) & 3;
  va[3] = (u32)((g >> 1) * 4096 + (g & 1) * 2048 + (4 * (qq >> 1) + (qq & 1)) * 128 + ((2 * w + (pp >> 1)) << 4) + 8 * (pp & 1));
  va[4] = row * 8192 + ch;
  va[5] = row * 1152 + ch;
  va[6] = (u32)(w >> 1) * 4096 + tr_base(lane, 4 * (w & 1) + pp);
  va[7] = (u32)w * 4096 + tr_base(lane, 4 * (qq >> 1) + (pp >> 1)) + 8 * (pp & 1);
  va[8] = hot2 + 32 * CHUNK + ((w == 0 && i < 4) ? 0 : 1);
  va[9] = (u32)(h * 64 + 4 * w);
  va[10] = hot + mine * CHUNK + w * 2048;
  va[11] = w == 3 ? hot + (32 + j) * CHUNK + i * 1024 : (w == 2 ? img + i * 1024 : ((w == 1 && i == 1) ? img + CHUNK : OOB));
  va[12] = w == 3 ? ~0u : 0u;
  va[13] = w == 3 ? 0u : ~0u;
  asm volatile("" : "+v"(va));
  if (!confirm(rid, tag, sure, smem)) return;
  va[14] = (u32)(2 * tag); va[15] = (u32)((2 * tag) >> 32);
  RUN_ASM(
    "s_nop 4\n"
    "s_getpc_b64 s[88:89]\n"
    ".Lrec_PC_%=:\n"
    "s_and_b32 s89, s89, 0xffff\n"
    "s_mov_b32 s90, .Lrec_FIN_%=-.Lrec_PC_%=\n"
    "s_mov_b32 s91, 0x20000\n"
    "v_mbcnt_lo_u32_b32 v78, -1, 0\n"
    "v_mbcnt_hi_u32_b32 v78, -1, v78\n"
    "v_lshlrev_b32 v78, 7, v78\n"
    "buffer_load_dword v79, v78, s[88:91], 0 offen\n"
    "v_add_u32 v78, 8192, v78\n"
    "buffer_load_dword v79, v78, s[88:91], 0 offen\n"
    "v_readfirstlane_b32 s76, v8\n"
    "v_readfirstlane_b32 s77, v9\n"
    "v_readfirstlane_b32 s78, v10\n"
    "v_readfirstlane_b32 s79, v11\n"
    "v_readfirstlane_b32 s80, v12\n"
    "v_readfirstlane_b32 s81, v13\n"
    "v_readfirstlane_b32 s30, v14\n"
    "v_readfirstlane_b32 s31, v15\n"
    "s_memtime s[34:35]\n"
    "s_memrealtime s[92:93]\n"
    "s_mov_b32 s69, 262144\n"
    "s_add_u32 m0, s60, 16384\n"
    "s_mov_b32 s73, 0\n"
    "buffer_load_dwordx4 v4, s[40:43], s73 offen lds\n"
    "s_add_u32 m0, s60, 20480\n"
    "s_mov_b32 s73, 128\n"
    "buffer_load_dwordx4 v4, s[40:43], s73 offen lds\n"
    "s_add_u32 m0, s60, 24576\n"
    "s_mov_b32 s73, 0\n"
    "buffer_load_dwordx4 v4, s[44:47], s73 offen lds\n"
    "s_add_u32 m0, s60, 28672\n"
    "s_mov_b32 s73, 128\n"
    "buffer_load_dwordx4 v4, s[44:47], s73 offen lds\n"
    "s_add_u32 m0, s60, 32768\n"
    "s_mov_b32 s73, 0\n"
    "buffer_load_dwordx4 v4, s[48:51], s73 offen lds\n"
    "s_add_u32 m0, s60, 36864\n"
    "s_mov_b32 s73, 128\n"
    "buffer_load_dwordx4 v4, s[48:51], s73 offen lds\n"
    "s_add_u32 m0, s60, 40960\n"
    "s_mov_b32 s73, 262144\n"
    "buffer_load_dwordx4 v4, s[40:43], s73 offen lds\n"
    "s_add_u32 m0, s60, 45056\n"
    "s_mov_b32 s73, 262272\n"
    "buffer_load_dwordx4 v4, s[40:43], s73 offen lds\n"
    "s_add_u32 m0, s60, 49152\n"
    "s_mov_b32 s73, 262144\n"
    "buffer_load_dwordx4 v4, s[44:47], s73 offen lds\n"
    "s_add_u32 m0, s60, 53248\n"
    "s_mov_b32 s73, 262272\n"
    "buffer_load_dwordx4 v4, s[44:47], s73 offen lds\n"
    "s_add_u32 m0, s60, 57344\n"
    "s_mov_b32 s73, 262144\n"
    "buffer_load_dwordx4 v4, s[48:51], s73 offen lds\n"
    "s_add_u32 m0, s60, 61440\n"
    "s_mov_b32 s73, 262272\n"
    "buffer_load_dwordx4 v4, s[48:51], s73 offen lds\n"
    "s_add_u32 m0, s60, 65536\n"
    "s_mov_b32 s73, 524288\n"
    "buffer_load_dwordx4 v4, s[40:43], s73 offen lds\n"
    "s_add_u32 m0, s60, 69632\n"
    "s_mov_b32 s73, 524416\n"
    "buffer_load_dwordx4 v4, s[40:43], s73 offen lds\n"
    "s_add_u32 m0, s60, 73728\n"
    "s_mov_b32 s73, 524288\n"
    "buffer_load_dwordx4 v4, s[44:47], s73 offen lds\n"
    "s_add_u32 m0, s60, 77824\n"
    "s_mov_b32 s73, 524416\n"
    "buffer_load_dwordx4 v4, s[44:47], s73 offen lds\n"
    "s_add_u32 m0, s60, 81920\n"
    "s_mov_b32 s73, 524288\n"
    "buffer_load_dwordx4 v4, s[48:51], s73 offen lds\n"
    "s_add_u32 m0, s60, 86016\n"
    "s_mov_b32 s73, 524416\n"
    "buffer_load_dwordx4 v4, s[48:51], s73 offen lds\n"
    "s_add_u32 m0, s60, 90112\n"
    "s_mov_b32 s73, 786432\n"
    "buffer_load_dwordx4 v4, s[40:43], s73 offen lds\n"
    "s_add_u32 m0, s60, 94208\n"
    "s_mov_b32 s73, 786560\n"
    "buffer_load_dwordx4 v4, s[40:43], s73 offen lds\n"
    "s_add_u32 m0, s60, 98304\n"
    "s_mov_b32 s73, 786432\n"
    "buffer_load_dwordx4 v4, s[44:47], s73 offen lds\n"
    "s_add_u32 m0, s60, 102400\n"
    "s_mov_b32 s73, 786560\n"
    "buffer_load_dwordx4 v4, s[44:47], s73 offen lds\n"
    "s_add_u32 m0, s60, 106496\n"
    "s_mov_b32 s73, 786432\n"
    "buffer_load_dwordx4 v4, s[48:51], s73 offen lds\n"
    "s_add_u32 m0, s60, 110592\n"
    "s_mov_b32 s73, 786560\n"
    "buffer_load_dwordx4 v4, s[48:51], s73 offen lds\n"
    "s_add_u32 m0, s60, 114688\n"
    "s_mov_b32 s73, 1048576\n"
    "buffer_load_dwordx4 v4, s[40:43], s73 offen lds\n"
    "s_add_u32 m0, s60, 118784\n"
    "s_mov_b32 s73, 1048704\n"
    "buffer_load_dwordx4 v4, s[40:43], s73 offen lds\n"
    "s_add_u32 m0, s60, 122880\n"
    "s_mov_b32 s73, 1048576\n"
    "buffer_load_dwordx4 v4, s[44:47], s73 offen lds\n"
    "s_add_u32 m0, s60, 126976\n"
    "s_mov_b32 s73, 1048704\n"
    "buffer_load_dwordx4 v4, s[44:47], s73 offen lds\n"
    "s_add_u32 m0, s60, 131072\n"
    "s_mov_b32 s73, 1048576\n"
    "buffer_load_dwordx4 v4, s[48:51], s73 offen lds\n"
    "s_add_u32 m0, s60, 135168\n"
    "s_mov_b32 s73, 1048704\n"
    "buffer_load_dwordx4 v4, s[48:51], s73 offen lds\n"
    "s_add_u32 m0, s60, 139264\n"
    "s_mov_b32 s73, 1310720\n"
    "buffer_load_dwordx4 v4, s[40:43], s73 offen lds\n"
    "s_add_u32 m0, s60, 143360\n"
    "s_mov_b32 s73, 1310848\n"
    "buffer_load_dwordx4 v4, s[40:43], s73 offen lds\n"
    "s_add_u32 m0, s60, 147456\n"
    "s_mov_b32 s73, 1310720\n"
    "buffer_load_dwordx4 v4, s[44:47], s73 offen lds\n"
    "s_add_u32 m0, s60, 151552\n"
    "s_mov_b32 s73, 1310848\n"
    "buffer_load_dwordx4 v4, s[44:47], s73 offen lds\n"
    "s_add_u32 m0, s60, 155648\n"
    "s_mov_b32 s73, 1310720\n"
    "buffer_load_dwordx4 v4, s[48:51], s73 offen lds\n"
    "s_add_u32 m0, s60, 159744\n"
    "s_mov_b32 s73, 1310848\n"
    "buffer_load_dwordx4 v4, s[48:51], s73 offen lds\n"
    "s_waitcnt vmcnt(30) lgkmcnt(0)\n"
    "s_barrier\n"
    "v_add_u32 v8, 16384, v6\n"
    "ds_read_b64_tr_b16 a[0:1], v8 offset:0\n"
    "ds_read_b64_tr_b16 a[2:3], v8 offset:512\n"
    "ds_read_b64_tr_b16 a[8:9], v8 offset:8\n"
    "ds_read_b64_tr_b16 a[10:11], v8 offset:520\n"
    "ds_read_b64_tr_b16 a[144:145], v8 offset:8192\n"
    "ds_read_b64_tr_b16 a[146:147], v8 offset:8704\n"
    "ds_read_b64_tr_b16 a[152:153], v8 offset:8200\n"
    "ds_read_b64_tr_b16 a[154:155], v8 offset:8712\n"
    "ds_read_b64_tr_b16 v[112:113], v8 offset:16384\n"
    "ds_read_b64_tr_b16 v[114:115], v8 offset:16896\n"
    "ds_read_b64_tr_b16 v[224:225], v8 offset:16392\n"
    "ds_read_b64_tr_b16 v[226:227], v8 offset:16904\n"
    "s_waitcnt vmcnt(24) lgkmcnt(0)\n"
    "s_barrier\n"
    "s_add_u32 m0, s60, 16384\n"
    "s_mov_b32 s73, 1572864\n"
    "buffer_load_dwordx4 v4, s[40:43], s73 offen lds\n"
    "s_add_u32 m0, s60, 20480\n"
    "s_mov_b32 s73, 1572992\n"
    "buffer_load_dwordx4 v4, s[40:43], s73 offen lds\n"
    "s_add_u32 m0, s60, 24576\n"
    "s_mov_b32 s73, 1572864\n"
    "buffer_load_dwordx4 v4, s[44:47], s73 offen lds\n"
    "s_add_u32 m0, s60, 28672\n"
    "s_mov_b32 s73, 1572992\n"
    "buffer_load_dwordx4 v4, s[44:47], s73 offen lds\n"
    "s_add_u32 m0, s60, 32768\n"
    "s_mov_b32 s73, 1572864\n"
    "buffer_load_dwordx4 v4, s[48:51], s73 offen lds\n"
    "s_add_u32 m0, s60, 36864\n"
    "s_mov_b32 s73, 1572992\n"
    "buffer_load_dwordx4 v4, s[48:51], s73 offen lds\n"
    "v_add_u32 v8, 40960, v6\n"
    "ds_read_b64_tr_b16 a[4:5], v8 offset:0\n"
    "ds_read_b64_tr_b16 a[6:7], v8 offset:512\n"
    "ds_read_b64_tr_b16 a[12:13], v8 offset:8\n"
    "ds_read_b64_tr_b16 a[14:15], v8 offset:520\n"
    "ds_read_b64_tr_b16 a[148:149], v8 offset:8192\n"
    "ds_read_b64_tr_b16 a[150:151], v8 offset:8704\n"
    "ds_read_b64_tr_b16 a[156:157], v8 offset:8200\n"
    "ds_read_b64_tr_b16 a[158:159], v8 offset:8712\n"
    "ds_read_b64_tr_b16 v[116:117], v8 offset:16384\n"
    "ds_read_b64_tr_b16 v[118:119], v8 offset:16896\n"
    "ds_read_b64_tr_b16 v[228:229], v8 offset:16392\n"
    "ds_read_b64_tr_b16 v[230:231], v8 offset:16904\n"
    "s_waitcnt vmcnt(24) lgkmcnt(0)\n"
    "s_barrier\n"
    "s_add_u32 m0, s60, 40960\n"
    "s_mov_b32 s73, 1835008\n"
    "buffer_load_dwordx4 v4, s[40:43], s73 offen lds\n"
    "s_add_u32 m0, s60, 45056\n"
    "s_mov_b32 s73, 1835136\n"
    "buffer_load_dwordx4 v4, s[40:43], s73 offen lds\n"
    "s_add_u32 m0, s60, 49152\n"
    "s_mov_b32 s73, 1835008\n"
    "buffer_load_dwordx4 v4, s[44:47], s73 offen lds\n"
    "s_add_u32 m0, s60, 53248\n"
    "s_mov_b32 s73, 1835136\n"
    "buffer_load_dwordx4 v4, s[44:47], s73 offen lds\n"
    "s_add_u32 m0, s60, 57344\n"
    "s_mov_b32 s73, 1835008\n"
    "buffer_load_dwordx4 v4, s[48:51], s73 offen lds\n"
    "s_add_u32 m0, s60, 61440\n"
    "s_mov_b32 s73, 1835136\n"
    "buffer_load_dwordx4 v4, s[48:51], s73 offen lds\n"
    "v_add_u32 v8, 65536, v6\n"
    "ds_read_b64_tr_b16 a[16:17], v8 offset:0\n"
    "ds_read_b64_tr_b16 a[18:19], v8 offset:512\n"
    "ds_read_b64_tr_b16 a[24:25], v8 offset:8\n"
    "ds_read_b64_tr_b16 a[26:27], v8 offset:520\n"
    "ds_read_b64_tr_b16 a[160:161], v8 offset:8192\n"
    "ds_read_b64_tr_b16 a[162:163], v8 offset:8704\n"
    "ds_read_b64_tr_b16 a[168:169], v8 offset:8200\n"
    "ds_read_b64_tr_b16 a[170:171], v8 offset:8712\n"
    "ds_read_b64_tr_b16 v[120:121], v8 offset:16384\n"
    "ds_read_b64_tr_b16 v[122:123], v8 offset:16896\n"
    "ds_read_b64_tr_b16 v[232:233], v8 offset:16392\n"
    "ds_read_b64_tr_b16 v[234:235], v8 offset:16904\n"
    "s_waitcnt vmcnt(24) lgkmcnt(0)\n"
    "s_barrier\n"
    "s_add_u32 m0, s60, 65536\n"
    "s_mov_b32 s73, 2097152\n"
    "buffer_load_dwordx4 v4, s[40:43], s73 offen lds\n"
    "s_add_u32 m0, s60, 69632\n"
    "s_mov_b32 s73, 2097280\n"
    "buffer_load_dwordx4 v4, s[40:43], s73 offen lds\n"
    "s_add_u32 m0, s60, 73728\n"
    "s_mov_b32 s73, 2097152\n"
    "buffer_load_dwordx4 v4, s[44:47], s73 offen lds\n"
    "s_add_u32 m0, s60, 77824\n"
    "s_mov_b32 s73, 2097280\n"
    "buffer_load_dwordx4 v4, s[44:47], s73 offen lds\n"
    "s_add_u32 m0, s60, 81920\n"
    "s_mov_b32 s73, 2097152\n"
    "buffer_load_dwordx4 v4, s[48:51], s73 offen lds\n"
    "s_add_u32 m0, s60, 86016\n"
    "s_mov_b32 s73, 2097280\n"
    "buffer_load_dwordx4 v4, s[48:51], s73 offen lds\n"
    "v_add_u32 v8, 90112, v6\n"
    "ds_read_b64_tr_b16 a[20:21], v8 offset:0\n"
    "ds_read_b64_tr_b16 a[22:23], v8 offset:512\n"
    "ds_read_b64_tr_b16 a[28:29], v8 offset:8\n"
    "ds_read_b64_tr_b16 a[30:31], v8 offset:520\n"
    "ds_read_b64_tr_b16 a[164:165], v8 offset:8192\n"
    "ds_read_b64_tr_b16 a[166:167], v8 offset:8704\n"
    "ds_read_b64_tr_b16 a[172:173], v8 offset:8200\n"
    "ds_read_b64_tr_b16 a[174:175], v8 offset:8712\n"
    "ds_read_b64_tr_b16 v[124:125], v8 offset:16384\n"
    "ds_read_b64_tr_b16 v[126:127], v8 offset:16896\n"
    "ds_read_b64_tr_b16 v[236:237], v8 offset:16392\n"
    "ds_read_b64_tr_b16 v[238:239], v8 offset:16904\n"
    "s_waitcnt vmcnt(24) lgkmcnt(0)\n"
    "s_barrier\n"
    "s_add_u32 m0, s60, 90112\n"
    "s_mov_b32 s73, 2359296\n"
    "buffer_load_dwordx4 v4, s[40:43], s73 offen lds\n"
    "s_add_u32 m0, s60, 94208\n"
    "s_mov_b32 s73, 2359424\n"
    "buffer_load_dwordx4 v4, s[40:43], s73 offen lds\n"
    "s_add_u32 m0, s60, 98304\n"
    "s_mov_b32 s73, 2359296\n"
    "buffer_load_dwordx4 v4, s[44:47], s73 offen lds\n"
    "s_add_u32 m0, s60, 102400\n"
    "s_mov_b32 s73, 2359424\n"
    "buffer_load_dwordx4 v4, s[44:47], s73 offen lds\n"
    "s_add_u32 m0, s60, 106496\n"
    "s_mov_b32 s73, 2359296\n"
    "buffer_load_dwordx4 v4, s[48:51], s73 offen lds\n"
    "s_add_u32 m0, s60, 110592\n"
    "s_mov_b32 s73, 2359424\n"
    "buffer_load_dwordx4 v4, s[48:51], s73 offen lds\n"
    "v_add_u32 v8, 114688, v6\n"
    "ds_read_b64_tr_b16 a[32:33], v8 offset:0\n"
    "ds_read_b64_tr_b16 a[34:35], v8 offset:512\n"
    "ds_read_b64_tr_b16 a[40:41], v8 offset:8\n"
    "ds_read_b64_tr_b16 a[42:43], v8 offset:520\n"
    "ds_read_b64_tr_b16 a[176:177], v8 offset:8192\n"
    "ds_read_b64_tr_b16 a[178:179], v8 offset:8704\n"
    "ds_read_b64_tr_b16 a[184:185], v8 offset:8200\n"
    "ds_read_b64_tr_b16 a[186:187], v8 offset:8712\n"
    "ds_read_b64_tr_b16 v[128:129], v8 offset:16384\n"
    "ds_read_b64_tr_b16 v[130:131], v8 offset:16896\n"
    "ds_read_b64_tr_b16 v[240:241], v8 offset:16392\n"
    "ds_read_b64_tr_b16 v[242:243], v8 offset:16904\n"
    "s_waitcnt vmcnt(24) lgkmcnt(0)\n"
    "s_barrier\n"
    "s_add_u32 m0, s60, 114688\n"
    "s_mov_b32 s73, 2621440\n"
    "buffer_load_dwordx4 v4, s[40:43], s73 offen lds\n"
    "s_add_u32 m0, s60, 118784\n"
    "s_mov_b32 s73, 2621568\n"
    "buffer_load_dwordx4 v4, s[40:43], s73 offen lds\n"
    "s_add_u32 m0, s60, 122880\n"
    "s_mov_b32 s73, 2621440\n"
    "buffer_load_dwordx4 v4, s[44:47], s73 offen lds\n"
    "s_add_u32 m0, s60, 126976\n"
    "s_mov_b32 s73, 2621568\n"
    "buffer_load_dwordx4 v4, s[44:47], s73 offen lds\n"
    "s_add_u32 m0, s60, 131072\n"
    "s_mov_b32 s73, 2621440\n"
    "buffer_load_dwordx4 v4, s[48:51], s73 offen lds\n"
    "s_add_u32 m0, s60, 135168\n"
    "s_mov_b32 s73, 2621568\n"
    "buffer_load_dwordx4 v4, s[48:51], s73 offen lds\n"
    "v_add_u32 v8, 139264, v6\n"
    "ds_read_b64_tr_b16 a[36:37], v8 offset:0\n"
    "ds_read_b64_tr_b16 a[38:39], v8 offset:512\n"
    "ds_read_b64_tr_b16 a[44:45], v8 offset:8\n"
    "ds_read_b64_tr_b16 a[46:47], v8 offset:520\n"
    "ds_read_b64_tr_b16 a[180:181], v8 offset:8192\n"
    "ds_read_b64_tr_b16 a[182:183], v8 offset:8704\n"
    "ds_read_b64_tr_b16 a[188:189], v8 offset:8200\n"
    "ds_read_b64_tr_b16 a[190:191], v8 offset:8712\n"
    "ds_read_b64_tr_b16 v[132:133], v8 offset:16384\n"
    "ds_read_b64_tr_b16 v[134:135], v8 offset:16896\n"
    "ds_read_b64_tr_b16 v[244:245], v8 offset:16392\n"
    "ds_read_b64_tr_b16 v[246:247], v8 offset:16904\n"
    "s_waitcnt vmcnt(24) lgkmcnt(0)\n"
    "s_barrier\n"
    "s_add_u32 m0, s60, 139264\n"
    "s_mov_b32 s73, 2883584\n"
    "buffer_load_dwordx4 v4, s[40:43], s73 offen lds\n"
    "s_add_u32 m0, s60, 143360\n"
    "s_mov_b32 s73, 2883712\n"
    "buffer_load_dwordx4 v4, s[40:43], s73 offen lds\n"
    "s_add_u32 m0, s60, 147456\n"
    "s_mov_b32 s73, 2883584\n"
    "buffer_load_dwordx4 v4, s[44:47], s73 offen lds\n"
    "s_add_u32 m0, s60, 151552\n"
    "s_mov_b32 s73, 2883712\n"
    "buffer_load_dwordx4 v4, s[44:47], s73 offen lds\n"
    "s_add_u32 m0, s60, 155648\n"
    "s_mov_b32 s73, 2883584\n"
    "buffer_load_dwordx4 v4, s[48:51], s73 offen lds\n"
    "s_add_u32 m0, s60, 159744\n"
    "s_mov_b32 s73, 2883712\n"
    "buffer_load_dwordx4 v4, s[48:51], s73 offen lds\n"
    "v_add_u32 v8, 16384, v6\n"
    "ds_read_b64_tr_b16 a[48:49], v8 offset:0\n"
    "ds_read_b64_tr_b16 a[50:51], v8 offset:512\n"
    "ds_read_b64_tr_b16 a[56:57], v8 offset:8\n"
    "ds_read_b64_tr_b16 a[58:59], v8 offset:520\n"
    "ds_read_b64_tr_b16 a[192:193], v8 offset:8192\n"
    "ds_read_b64_tr_b16 a[194:195], v8 offset:8704\n"
    "ds_read_b64_tr_b16 a[200:201], v8 offset:8200\n"
    "ds_read_b64_tr_b16 a[202:203], v8 offset:8712\n"
    "ds_read_b64_tr_b16 v[136:137], v8 offset:16384\n"
    "ds_read_b64_tr_b16 v[138:139], v8 offset:16896\n"
    "ds_read_b64_tr_b16 v[248:249], v8 offset:16392\n"
    "ds_read_b64_tr_b16 v[250:251], v8 offset:16904\n"
    "s_waitcnt vmcnt(24) lgkmcnt(0)\n"
    "s_barrier\n"
    "s_add_u32 m0, s60, 16384\n"
    "s_mov_b32 s73, 3145728\n"
    "buffer_load_dwordx4 v4, s[40:43], s73 offen lds\n"
    "s_add_u32 m0, s60, 20480\n"
    "s_mov_b32 s73, 3145856\n"
    "buffer_load_dwordx4 v4, s[40:43], s73 offen lds\n"
    "s_add_u32 m0, s60, 24576\n"
    "s_mov_b32 s73, 3145728\n"
    "buffer_load_dwordx4 v4, s[44:47], s73 offen lds\n"
    "s_add_u32 m0, s60, 28672\n"
    "s_mov_b32 s73, 3145856\n"
    "buffer_load_dwordx4 v4, s[44:47], s73 offen lds\n"
    "s_add_u32 m0, s60, 32768\n"
    "s_mov_b32 s73, 3145728\n"
    "buffer_load_dwordx4 v4, s[48:51], s73 offen lds\n"
    "s_add_u32 m0, s60, 36864\n"
    "s_mov_b32 s73, 3145856\n"
    "buffer_load_dwordx4 v4, s[48:51], s73 offen lds\n"
    "v_add_u32 v8, 40960, v6\n"
    "ds_read_b64_tr_b16 a[52:53], v8 offset:0\n"
    "ds_read_b64_tr_b16 a[54:55], v8 offset:512\n"
    "ds_read_b64_tr_b16 a[60:61], v8 offset:8\n"
    "ds_read_b64_tr_b16 a[62:63], v8 offset:520\n"
    "ds_read_b64_tr_b16 a[196:197], v8 offset:8192\n"
    "ds_read_b64_tr_b16 a[198:199], v8 offset:8704\n"
    "ds_read_b64_tr_b16 a[204:205], v8 offset:8200\n"
    "ds_read_b64_tr_b16 a[206:207], v8 offset:8712\n"
    "ds_read_b64_tr_b16 v[140:141], v8 offset:16384\n"
    "ds_read_b64_tr_b16 v[142:143], v8 offset:16896\n"
    "ds_read_b64_tr_b16 v[252:253], v8 offset:16392\n"
    "ds_read_b64_tr_b16 v[254:255], v8 offset:16904\n"
    "s_waitcnt vmcnt(24) lgkmcnt(0)\n"
    "s_barrier\n"
    "s_add_u32 m0, s60, 40960\n"
    "s_mov_b32 s73, 3407872\n"
    "buffer_load_dwordx4 v4, s[40:43], s73 offen lds\n"
    "s_add_u32 m0, s60, 45056\n"
    "s_mov_b32 s73, 3408000\n"
    "buffer_load_dwordx4 v4, s[40:43], s73 offen lds\n"
    "s_add_u32 m0, s60, 49152\n"
    "s_mov_b32 s73, 3407872\n"
    "buffer_load_dwordx4 v4, s[44:47], s73 offen lds\n"
    "s_add_u32 m0, s60, 53248\n"
    "s_mov_b32 s73, 3408000\n"
    "buffer_load_dwordx4 v4, s[44:47], s73 offen lds\n"
    "s_add_u32 m0, s60, 57344\n"
    "s_mov_b32 s73, 3407872\n"
    "buffer_load_dwordx4 v4, s[48:51], s73 offen lds\n"
    "s_add_u32 m0, s60, 61440\n"
    "s_mov_b32 s73, 3408000\n"
    "buffer_load_dwordx4 v4, s[48:51], s73 offen lds\n"
    "v_add_u32 v8, 65536, v6\n"
    "ds_read_b64_tr_b16 a[64:65], v8 offset:0\n"
    "ds_read_b64_tr_b16 a[66:67], v8 offset:512\n"
    "ds_read_b64_tr_b16 a[72:73], v8 offset:8\n"
    "ds_read_b64_tr_b16 a[74:75], v8 offset:520\n"
    "ds_read_b64_tr_b16 a[208:209], v8 offset:8192\n"
    "ds_read_b64_tr_b16 a[210:211], v8 offset:8704\n"
    "ds_read_b64_tr_b16 a[216:217], v8 offset:8200\n"
    "ds_read_b64_tr_b16 a[218:219], v8 offset:8712\n"
    "ds_read_b64_tr_b16 v[144:145], v8 offset:16384\n"
    "ds_read_b64_tr_b16 v[146:147], v8 offset:16896\n"
    "ds_read_b64_tr_b16 v[208:209], v8 offset:16392\n"
    "ds_read_b64_tr_b16 v[210:211], v8 offset:16904\n"
    "s_waitcnt vmcnt(24) lgkmcnt(0)\n"
    "s_barrier\n"
    "s_add_u32 m0, s60, 65536\n"
    "s_mov_b32 s73, 3670016\n"
    "buffer_load_dwordx4 v4, s[40:43], s73 offen lds\n"
    "s_add_u32 m0, s60, 69632\n"
    "s_mov_b32 s73, 3670144\n"
    "buffer_load_dwordx4 v4, s[40:43], s73 offen lds\n"
    "s_add_u32 m0, s60, 73728\n"
    "s_mov_b32 s73, 3670016\n"
    "buffer_load_dwordx4 v4, s[44:47], s73 offen lds\n"
    "s_add_u32 m0, s60, 77824\n"
    "s_mov_b32 s73, 3670144\n"
    "buffer_load_dwordx4 v4, s[44:47], s73 offen lds\n"
    "s_add_u32 m0, s60, 81920\n"
    "s_mov_b32 s73, 3670016\n"
    "buffer_load_dwordx4 v4, s[48:51], s73 offen lds\n"
    "s_add_u32 m0, s60, 86016\n"
    "s_mov_b32 s73, 3670144\n"
    "buffer_load_dwordx4 v4, s[48:51], s73 offen lds\n"
    "v_add_u32 v8, 90112, v6\n"
    "ds_read_b64_tr_b16 a[68:69], v8 offset:0\n"
    "ds_read_b64_tr_b16 a[70:71], v8 offset:512\n"
    "ds_read_b64_tr_b16 a[76:77], v8 offset:8\n"
    "ds_read_b64_tr_b16 a[78:79], v8 offset:520\n"
    "ds_read_b64_tr_b16 a[212:213], v8 offset:8192\n"
    "ds_read_b64_tr_b16 a[214:215], v8 offset:8704\n"
    "ds_read_b64_tr_b16 a[220:221], v8 offset:8200\n"
    "ds_read_b64_tr_b16 a[222:223], v8 offset:8712\n"
    "ds_read_b64_tr_b16 v[148:149], v8 offset:16384\n"
    "ds_read_b64_tr_b16 v[150:151], v8 offset:16896\n"
    "ds_read_b64_tr_b16 v[212:213], v8 offset:16392\n"
    "ds_read_b64_tr_b16 v[214:215], v8 offset:16904\n"
    "s_waitcnt vmcnt(24) lgkmcnt(0)\n"
    "s_barrier\n"
    "s_add_u32 m0, s60, 90112\n"
    "s_mov_b32 s73, 3932160\n"
    "buffer_load_dwordx4 v4, s[40:43], s73 offen lds\n"
    "s_add_u32 m0, s60, 94208\n"
    "s_mov_b32 s73, 3932288\n"
    "buffer_load_dwordx4 v4, s[40:43], s73 offen lds\n"
    "s_add_u32 m0, s60, 98304\n"
    "s_mov_b32 s73, 3932160\n"
    "buffer_load_dwordx4 v4, s[44:47], s73 offen lds\n"
    "s_add_u32 m0, s60, 102400\n"
    "s_mov_b32 s73, 3932288\n"
    "buffer_load_dwordx4 v4, s[44:47], s73 offen lds\n"
    "s_add_u32 m0, s60, 106496\n"
    "s_mov_b32 s73, 3932160\n"
    "buffer_load_dwordx4 v4, s[48:51], s73 offen lds\n"
    "s_add_u32 m0, s60, 110592\n"
    "s_mov_b32 s73, 3932288\n"
    "buffer_load_dwordx4 v4, s[48:51], s73 offen lds\n"
    "v_add_u32 v8, 114688, v6\n"
    "ds_read_b64_tr_b16 a[80:81], v8 offset:0\n"
    "ds_read_b64_tr_b16 a[82:83], v8 offset:512\n"
    "ds_read_b64_tr_b16 a[88:89], v8 offset:8\n"
    "ds_read_b64_tr_b16 a[90:91], v8 offset:520\n"
    "ds_read_b64_tr_b16 a[224:225], v8 offset:8192\n"
    "ds_read_b64_tr_b16 a[226:227], v8 offset:8704\n"
    "ds_read_b64_tr_b16 a[232:233], v8 offset:8200\n"
    "ds_read_b64_tr_b16 a[234:235], v8 offset:8712\n"
    "ds_read_b64_tr_b16 v[152:153], v8 offset:16384\n"
    "ds_read_b64_tr_b16 v[154:155], v8 offset:16896\n"
    "ds_read_b64_tr_b16 v[216:217], v8 offset:16392\n"
    "ds_read_b64_tr_b16 v[218:219], v8 offset:16904\n"
    "s_waitcnt vmcnt(24) lgkmcnt(0)\n"
    "s_barrier\n"
    "s_add_u32 m0, s60, 114688\n"
    "s_mov_b32 s73, 4194304\n"
    "buffer_load_dwordx4 v4, s[40:43], s73 offen lds\n"
    "s_add_u32 m0, s60, 118784\n"
    "s_mov_b32 s73, 4194432\n"
    "buffer_load_dwordx4 v4, s[40:43], s73 offen lds\n"
    "s_add_u32 m0, s60, 122880\n"
    "s_mov_b32 s73, 4194304\n"
    "buffer_load_dwordx4 v4, s[44:47], s73 offen lds\n"
    "s_add_u32 m0, s60, 126976\n"
    "s_mov_b32 s73, 4194432\n"
    "buffer_load_dwordx4 v4, s[44:47], s73 offen lds\n"
    "s_add_u32 m0, s60, 131072\n"
    "s_mov_b32 s73, 4194304\n"
    "buffer_load_dwordx4 v4, s[48:51], s73 offen lds\n"
    "s_add_u32 m0, s60, 135168\n"
    "s_mov_b32 s73, 4194432\n"
    "buffer_load_dwordx4 v4, s[48:51], s73 offen lds\n"
    "v_add_u32 v8, 139264, v6\n"
    "ds_read_b64_tr_b16 a[84:85], v8 offset:0\n"
    "ds_read_b64_tr_b16 a[86:87], v8 offset:512\n"
    "ds_read_b64_tr_b16 a[92:93], v8 offset:8\n"
    "ds_read_b64_tr_b16 a[94:95], v8 offset:520\n"
    "ds_read_b64_tr_b16 a[228:229], v8 offset:8192\n"
    "ds_read_b64_tr_b16 a[230:231], v8 offset:8704\n"
    "ds_read_b64_tr_b16 a[236:237], v8 offset:8200\n"
    "ds_read_b64_tr_b16 a[238:239], v8 offset:8712\n"
    "ds_read_b64_tr_b16 v[156:157], v8 offset:16384\n"
    "ds_read_b64_tr_b16 v[158:159], v8 offset:16896\n"
    "ds_read_b64_tr_b16 v[220:221], v8 offset:16392\n"
    "ds_read_b64_tr_b16 v[222:223], v8 offset:16904\n"
    "s_waitcnt vmcnt(24) lgkmcnt(0)\n"
    "s_barrier\n"
    "s_add_u32 m0, s60, 139264\n"
    "s_mov_b32 s73, 4456448\n"
    "buffer_load_dwordx4 v4, s[40:43], s73 offen lds\n"
    "s_add_u32 m0, s60, 143360\n"
    "s_mov_b32 s73, 4456576\n"
    "buffer_load_dwordx4 v4, s[40:43], s73 offen lds\n"
    "s_add_u32 m0, s60, 147456\n"
    "s_mov_b32 s73, 4456448\n"
    "buffer_load_dwordx4 v4, s[44:47], s73 offen lds\n"
    "s_add_u32 m0, s60, 151552\n"
    "s_mov_b32 s73, 4456576\n"
    "buffer_load_dwordx4 v4, s[44:47], s73 offen lds\n"
    "s_add_u32 m0, s60, 155648\n"
    "s_mov_b32 s73, 4456448\n"
    "buffer_load_dwordx4 v4, s[48:51], s73 offen lds\n"
    "s_add_u32 m0, s60, 159744\n"
    "s_mov_b32 s73, 4456576\n"
    "buffer_load_dwordx4 v4, s[48:51], s73 offen lds\n"
    "v_add_u32 v8, 16384, v6\n"
    "ds_read_b64_tr_b16 a[96:97], v8 offset:0\n"
    "ds_read_b64_tr_b16 a[98:99], v8 offset:512\n"
    "ds_read_b64_tr_b16 a[104:105], v8 offset:8\n"
    "ds_read_b64_tr_b16 a[106:107], v8 offset:520\n"
    "ds_read_b64_tr_b16 a[240:241], v8 offset:8192\n"
    "ds_read_b64_tr_b16 a[242:243], v8 offset:8704\n"
    "ds_read_b64_tr_b16 a[248:249], v8 offset:8200\n"
    "ds_read_b64_tr_b16 a[250:251], v8 offset:8712\n"
    "ds_read_b64_tr_b16 v[160:161], v8 offset:16384\n"
    "ds_read_b64_tr_b16 v[162:163], v8 offset:16896\n"
    "ds_read_b64_tr_b16 v[168:169], v8 offset:16392\n"
    "ds_read_b64_tr_b16 v[170:171], v8 offset:16904\n"
    "s_waitcnt vmcnt(24) lgkmcnt(0)\n"
    "s_barrier\n"
    "v_add_u32 v8, 40960, v6\n"
    "ds_read_b64_tr_b16 a[100:101], v8 offset:0\n"
    "ds_read_b64_tr_b16 a[102:103], v8 offset:512\n"
    "ds_read_b64_tr_b16 a[108:109], v8 offset:8\n"
    "ds_read_b64_tr_b16 a[110:111], v8 offset:520\n"
    "ds_read_b64_tr_b16 a[244:245], v8 offset:8192\n"
    "ds_read_b64_tr_b16 a[246:247], v8 offset:8704\n"
    "ds_read_b64_tr_b16 a[252:253], v8 offset:8200\n"
    "ds_read_b64_tr_b16 a[254:255], v8 offset:8712\n"
    "ds_read_b64_tr_b16 v[164:165], v8 offset:16384\n"
    "ds_read_b64_tr_b16 v[166:167], v8 offset:16896\n"
    "ds_read_b64_tr_b16 v[172:173], v8 offset:16392\n"
    "ds_read_b64_tr_b16 v[174:175], v8 offset:16904\n"
    "s_waitcnt vmcnt(18) lgkmcnt(0)\n"
    "s_barrier\n"
    "s_add_u32 m0, s60, 16384\n"
    "s_mov_b32 s73, 74240\n"
    "buffer_load_dwordx4 v5, s[52:55], s73 offen lds\n"
    "s_add_u32 m0, s60, 20480\n"
    "s_mov_b32 s73, 74368\n"
    "buffer_load_dwordx4 v5, s[52:55], s73 offen lds\n"
    "s_add_u32 m0, s60, 24576\n"
    "s_mov_b32 s73, 74496\n"
    "buffer_load_dwordx4 v5, s[52:55], s73 offen lds\n"
    "s_add_u32 m0, s60, 28672\n"
    "s_mov_b32 s73, 74624\n"
    "buffer_load_dwordx4 v5, s[52:55], s73 offen lds\n"
    "s_add_u32 m0, s60, 32768\n"
    "s_mov_b32 s73, 111104\n"
    "buffer_load_dwordx4 v5, s[52:55], s73 offen lds\n"
    "s_add_u32 m0, s60, 36864\n"
    "s_mov_b32 s73, 111232\n"
    "buffer_load_dwordx4 v5, s[52:55], s73 offen lds\n"
    "s_add_u32 m0, s60, 40960\n"
    "s_mov_b32 s73, 111360\n"
    "buffer_load_dwordx4 v5, s[52:55], s73 offen lds\n"
    "s_add_u32 m0, s60, 45056\n"
    "s_mov_b32 s73, 111488\n"
    "buffer_load_dwordx4 v5, s[52:55], s73 offen lds\n"
    "v_add_u32 v8, 65536, v6\n"
    "ds_read_b64_tr_b16 a[112:113], v8 offset:0\n"
    "ds_read_b64_tr_b16 a[114:115], v8 offset:512\n"
    "ds_read_b64_tr_b16 a[120:121], v8 offset:8\n"
    "ds_read_b64_tr_b16 a[122:123], v8 offset:520\n"
    "ds_read_b64_tr_b16 v[80:81], v8 offset:8192\n"
    "ds_read_b64_tr_b16 v[82:83], v8 offset:8704\n"
    "ds_read_b64_tr_b16 v[88:89], v8 offset:8200\n"
    "ds_read_b64_tr_b16 v[90:91], v8 offset:8712\n"
    "ds_read_b64_tr_b16 v[176:177], v8 offset:16384\n"
    "ds_read_b64_tr_b16 v[178:179], v8 offset:16896\n"
    "ds_read_b64_tr_b16 v[184:185], v8 offset:16392\n"
    "ds_read_b64_tr_b16 v[186:187], v8 offset:16904\n"
    "s_waitcnt vmcnt(20) lgkmcnt(0)\n"
    "s_barrier\n"
    "s_add_u32 m0, s60, 49152\n"
    "s_mov_b32 s73, 73728\n"
    "buffer_load_dwordx4 v5, s[52:55], s73 offen lds\n"
    "s_add_u32 m0, s60, 53248\n"
    "s_mov_b32 s73, 73856\n"
    "buffer_load_dwordx4 v5, s[52:55], s73 offen lds\n"
    "s_add_u32 m0, s60, 57344\n"
    "s_mov_b32 s73, 73984\n"
    "buffer_load_dwordx4 v5, s[52:55], s73 offen lds\n"
    "s_add_u32 m0, s60, 61440\n"
    "s_mov_b32 s73, 74112\n"
    "buffer_load_dwordx4 v5, s[52:55], s73 offen lds\n"
    "s_add_u32 m0, s60, 65536\n"
    "s_mov_b32 s73, 110592\n"
    "buffer_load_dwordx4 v5, s[52:55], s73 offen lds\n"
    "s_add_u32 m0, s60, 69632\n"
    "s_mov_b32 s73, 110720\n"
    "buffer_load_dwordx4 v5, s[52:55], s73 offen lds\n"
    "s_add_u32 m0, s60, 73728\n"
    "s_mov_b32 s73, 110848\n"
    "buffer_load_dwordx4 v5, s[52:55], s73 offen lds\n"
    "s_add_u32 m0, s60, 77824\n"
    "s_mov_b32 s73, 110976\n"
    "buffer_load_dwordx4 v5, s[52:55], s73 offen lds\n"
    "v_add_u32 v8, 90112, v6\n"
    "ds_read_b64_tr_b16 a[116:117], v8 offset:0\n"
    "ds_read_b64_tr_b16 a[118:119], v8 offset:512\n"
    "ds_read_b64_tr_b16 a[124:125], v8 offset:8\n"
    "ds_read_b64_tr_b16 a[126:127], v8 offset:520\n"
    "ds_read_b64_tr_b16 v[84:85], v8 offset:8192\n"
    "ds_read_b64_tr_b16 v[86:87], v8 offset:8704\n"
    "ds_read_b64_tr_b16 v[92:93], v8 offset:8200\n"
    "ds_read_b64_tr_b16 v[94:95], v8 offset:8712\n"
    "ds_read_b64_tr_b16 v[180:181], v8 offset:16384\n"
    "ds_read_b64_tr_b16 v[182:183], v8 offset:16896\n"
    "ds_read_b64_tr_b16 v[188:189], v8 offset:16392\n"
    "ds_read_b64_tr_b16 v[190:191], v8 offset:16904\n"
    "s_waitcnt vmcnt(22) lgkmcnt(0)\n"
    "s_barrier\n"
    "s_add_u32 m0, s60, 81920\n"
    "s_mov_b32 s73, 0\n"
    "buffer_load_dwordx4 v5, s[52:55], s73 offen lds\n"
    "s_add_u32 m0, s60, 86016\n"
    "s_mov_b32 s73, 128\n"
    "buffer_load_dwordx4 v5, s[52:55], s73 offen lds\n"
    "s_add_u32 m0, s60, 90112\n"
    "s_mov_b32 s73, 256\n"
    "buffer_load_dwordx4 v5, s[52:55], s73 offen lds\n"
    "s_add_u32 m0, s60, 94208\n"
    "s_mov_b32 s73, 384\n"
    "buffer_load_dwordx4 v5, s[52:55], s73 offen lds\n"
    "s_add_u32 m0, s60, 98304\n"
    "s_mov_b32 s73, 36864\n"
    "buffer_load_dwordx4 v5, s[52:55], s73 offen lds\n"
    "s_add_u32 m0, s60, 102400\n"
    "s_mov_b32 s73, 36992\n"
    "buffer_load_dwordx4 v5, s[52:55], s73 offen lds\n"
    "s_add_u32 m0, s60, 106496\n"
    "s_mov_b32 s73, 37120\n"
    "buffer_load_dwordx4 v5, s[52:55], s73 offen lds\n"
    "s_add_u32 m0, s60, 110592\n"
    "s_mov_b32 s73, 37248\n"
    "buffer_load_dwordx4 v5, s[52:55], s73 offen lds\n"
    "v_add_u32 v8, 114688, v6\n"
    "ds_read_b64_tr_b16 a[128:129], v8 offset:0\n"
    "ds_read_b64_tr_b16 a[130:131], v8 offset:512\n"
    "ds_read_b64_tr_b16 a[136:137], v8 offset:8\n"
    "ds_read_b64_tr_b16 a[138:139], v8 offset:520\n"
    "ds_read_b64_tr_b16 v[96:97], v8 offset:8192\n"
    "ds_read_b64_tr_b16 v[98:99], v8 offset:8704\n"
    "ds_read_b64_tr_b16 v[104:105], v8 offset:8200\n"
    "ds_read_b64_tr_b16 v[106:107], v8 offset:8712\n"
    "ds_read_b64_tr_b16 v[192:193], v8 offset:16384\n"
    "ds_read_b64_tr_b16 v[194:195], v8 offset:16896\n"
    "ds_read_b64_tr_b16 v[200:201], v8 offset:16392\n"
    "ds_read_b64_tr_b16 v[202:203], v8 offset:16904\n"
    "s_waitcnt vmcnt(24) lgkmcnt(0)\n"
    "s_barrier\n"
    "v_add_u32 v8, 139264, v6\n"
    "ds_read_b64_tr_b16 a[132:133], v8 offset:0\n"
    "ds_read_b64_tr_b16 a[134:135], v8 offset:512\n"
    "ds_read_b64_tr_b16 a[140:141], v8 offset:8\n"
    "ds_read_b64_tr_b16 a[142:143], v8 offset:520\n"
    "ds_read_b64_tr_b16 v[100:101], v8 offset:8192\n"
    "ds_read_b64_tr_b16 v[102:103], v8 offset:8704\n"
    "ds_read_b64_tr_b16 v[108:109], v8 offset:8200\n"
    "ds_read_b64_tr_b16 v[110:111], v8 offset:8712\n"
    "ds_read_b64_tr_b16 v[196:197], v8 offset:16384\n"
    "ds_read_b64_tr_b16 v[198:199], v8 offset:16896\n"
    "ds_read_b64_tr_b16 v[204:205], v8 offset:16392\n"
    "ds_read_b64_tr_b16 v[206:207], v8 offset:16904\n"
    "s_waitcnt vmcnt(16) lgkmcnt(0)\n"
    "s_barrier\n"
    "s_add_u32 m0, s60, 114688\n"
    "s_mov_b32 s73, 512\n"
    "buffer_load_dwordx4 v5, s[52:55], s73 offen lds\n"
    "s_add_u32 m0, s60, 118784\n"
    "s_mov_b32 s73, 640\n"
    "buffer_load_dwordx4 v5, s[52:55], s73 offen lds\n"
    "s_add_u32 m0, s60, 122880\n"
    "s_mov_b32 s73, 768\n"
    "buffer_load_dwordx4 v5, s[52:55], s73 offen lds\n"
    "s_add_u32 m0, s60, 126976\n"
    "s_mov_b32 s73, 896\n"
    "buffer_load_dwordx4 v5, s[52:55], s73 offen lds\n"
    "s_add_u32 m0, s60, 131072\n"
    "s_mov_b32 s73, 37376\n"
    "buffer_load_dwordx4 v5, s[52:55], s73 offen lds\n"
    "s_add_u32 m0, s60, 135168\n"
    "s_mov_b32 s73, 37504\n"
    "buffer_load_dwordx4 v5, s[52:55], s73 offen lds\n"
    "s_add_u32 m0, s60, 139264\n"
    "s_mov_b32 s73, 37632\n"
    "buffer_load_dwordx4 v5, s[52:55], s73 offen lds\n"
    "s_add_u32 m0, s60, 143360\n"
    "s_mov_b32 s73, 37760\n"
    "buffer_load_dwordx4 v5, s[52:55], s73 offen lds\n"
    "s_add_u32 m0, s60, 147456\n"
    "s_mov_b32 s73, 1024\n"
    "buffer_load_dwordx4 v5, s[52:55], s73 offen lds\n"
    "s_add_u32 m0, s60, 151552\n"
    "s_mov_b32 s73, 37888\n"
    "buffer_load_dwordx4 v5, s[52:55], s73 offen lds\n"
    "s_add_u32 m0, s60, 155648\n"
    "s_mov_b32 s73, 74752\n"
    "buffer_load_dwordx4 v5, s[52:55], s73 offen lds\n"
    "s_add_u32 m0, s60, 159744\n"
    "s_mov_b32 s73, 111616\n"
    "buffer_load_dwordx4 v5, s[52:55], s73 offen lds\n"
    "buffer_load_dwordx2 v[12:13], off, s[36:39], 0 sc1\n"
    "v_add_u32 v8, 16384, v7\n"
    "v_xor_b32 v9, 32, v8\n"
    "v_xor_b32 v10, 320, v8\n"
    "v_xor_b32 v11, 32, v10\n"
    "ds_read_b64_tr_b16 v[16:17], v8 offset:0\n"
    "ds_read_b64_tr_b16 v[18:19], v8 offset:512\n"
    "ds_read_b64_tr_b16 v[20:21], v8 offset:16384\n"
    "ds_read_b64_tr_b16 v[22:23], v8 offset:16896\n"
    "ds_read_b64_tr_b16 v[24:25], v9 offset:0\n"
    "ds_read_b64_tr_b16 v[26:27], v9 offset:512\n"
    "ds_read_b64_tr_b16 v[28:29], v9 offset:16384\n"
    "ds_read_b64_tr_b16 v[30:31], v9 offset:16896\n"
    "s_waitcnt lgkmcnt(4)\n"
    "ds_read_b64_tr_b16 v[32:33], v10 offset:0\n"
    "ds_read_b64_tr_b16 v[34:35], v10 offset:512\n"
    "ds_read_b64_tr_b16 v[36:37], v10 offset:16384\n"
    "ds_read_b64_tr_b16 v[38:39], v10 offset:16896\n"
    "ds_read_b64_tr_b16 v[40:41], v11 offset:0\n"
    "ds_read_b64_tr_b16 v[42:43], v11 offset:512\n"
    "ds_read_b64_tr_b16 v[44:45], v11 offset:16384\n"
    "ds_read_b64_tr_b16 v[46:47], v11 offset:16896\n"
    "s_waitcnt vmcnt(21) lgkmcnt(0)\n"
    "s_barrier\n"
    "v_add_u32 v14, 0, v1\n"
    "ds_write_b128 v14, v[224:227] offset:0\n"
    "ds_write_b128 v14, v[228:231] offset:1024\n"
    "ds_write_b128 v14, v[232:235] offset:8192\n"
    "ds_write_b128 v14, v[236:239] offset:9216\n"
    "ds_write_b128 v14, v[240:243] offset:16384\n"
    "ds_write_b128 v14, v[244:247] offset:17408\n"
    "ds_write_b128 v14, v[248:251] offset:24576\n"
    "ds_write_b128 v14, v[252:255] offset:25600\n"
    "s_waitcnt lgkmcnt(4)\n"
    "v_add_u32 v8, 49152, v7\n"
    "v_xor_b32 v9, 32, v8\n"
    "v_xor_b32 v10, 320, v8\n"
    "v_xor_b32 v11, 32, v10\n"
    "ds_read_b64_tr_b16 v[48:49], v10 offset:0\n"
    "ds_read_b64_tr_b16 v[50:51], v10 offset:512\n"
    "ds_read_b64_tr_b16 v[52:53], v10 offset:16384\n"
    "ds_read_b64_tr_b16 v[54:55], v10 offset:16896\n"
    "ds_read_b64_tr_b16 v[56:57], v11 offset:0\n"
    "ds_read_b64_tr_b16 v[58:59], v11 offset:512\n"
    "ds_read_b64_tr_b16 v[60:61], v11 offset:16384\n"
    "ds_read_b64_tr_b16 v[62:63], v11 offset:16896\n"
    "s_waitcnt lgkmcnt(4)\n"
    "ds_read_b64_tr_b16 v[64:65], v8 offset:0\n"
    "ds_read_b64_tr_b16 v[66:67], v8 offset:512\n"
    "ds_read_b64_tr_b16 v[68:69], v8 offset:16384\n"
    "ds_read_b64_tr_b16 v[70:71], v8 offset:16896\n"
    "ds_read_b64_tr_b16 v[72:73], v9 offset:0\n"
    "ds_read_b64_tr_b16 v[74:75], v9 offset:512\n"
    "ds_read_b64_tr_b16 v[76:77], v9 offset:16384\n"
    "ds_read_b64_tr_b16 v[78:79], v9 offset:16896\n"
    "s_waitcnt vmcnt(13) lgkmcnt(0)\n"
    "s_barrier\n"
    "v_mov_b64 v[224:225], v[16:17]\n"
    "v_mov_b64 v[226:227], v[18:19]\n"
    "v_mov_b64 v[228:229], v[20:21]\n"
    "v_mov_b64 v[230:231], v[22:23]\n"
    "v_mov_b64 v[232:233], v[24:25]\n"
    "v_mov_b64 v[234:235], v[26:27]\n"
    "v_mov_b64 v[236:237], v[28:29]\n"
    "v_mov_b64 v[238:239], v[30:31]\n"
    "v_mov_b64 v[240:241], v[32:33]\n"
    "v_mov_b64 v[242:243], v[34:35]\n"
    "v_mov_b64 v[244:245], v[36:37]\n"
    "v_mov_b64 v[246:247], v[38:39]\n"
    "v_mov_b64 v[248:249], v[40:41]\n"
    "v_mov_b64 v[250:251], v[42:43]\n"
    "v_mov_b64 v[252:253], v[44:45]\n"
    "v_mov_b64 v[254:255], v[46:47]\n"
    "v_add_u32 v14, 32768, v1\n"
    "ds_write_b128 v14, v[208:211] offset:0\n"
    "ds_write_b128 v14, v[212:215] offset:1024\n"
    "ds_write_b128 v14, v[216:219] offset:8192\n"
    "ds_write_b128 v14, v[220:223] offset:9216\n"
    "ds_write_b128 v14, v[64:67] offset:16384\n"
    "ds_write_b128 v14, v[68:71] offset:17408\n"
    "ds_write_b128 v14, v[72:75] offset:24576\n"
    "ds_write_b128 v14, v[76:79] offset:25600\n"
    "s_waitcnt lgkmcnt(4)\n"
    "v_add_u32 v8, 81920, v7\n"
    "v_xor_b32 v9, 32, v8\n"
    "v_xor_b32 v10, 320, v8\n"
    "v_xor_b32 v11, 32, v10\n"
    "ds_read_b64_tr_b16 v[16:17], v8 offset:0\n"
    "ds_read_b64_tr_b16 v[18:19], v8 offset:512\n"
    "ds_read_b64_tr_b16 v[20:21], v8 offset:16384\n"
    "ds_read_b64_tr_b16 v[22:23], v8 offset:16896\n"
    "ds_read_b64_tr_b16 v[24:25], v9 offset:0\n"
    "ds_read_b64_tr_b16 v[26:27], v9 offset:512\n"
    "ds_read_b64_tr_b16 v[28:29], v9 offset:16384\n"
    "ds_read_b64_tr_b16 v[30:31], v9 offset:16896\n"
    "s_waitcnt lgkmcnt(4)\n"
    "ds_read_b64_tr_b16 v[32:33], v10 offset:0\n"
    "ds_read_b64_tr_b16 v[34:35], v10 offset:512\n"
    "ds_read_b64_tr_b16 v[36:37], v10 offset:16384\n"
    "ds_read_b64_tr_b16 v[38:39], v10 offset:16896\n"
    "ds_read_b64_tr_b16 v[40:41], v11 offset:0\n"
    "ds_read_b64_tr_b16 v[42:43], v11 offset:512\n"
    "ds_read_b64_tr_b16 v[44:45], v11 offset:16384\n"
    "ds_read_b64_tr_b16 v[46:47], v11 offset:16896\n"
    "s_waitcnt vmcnt(5) lgkmcnt(0)\n"
    "s_barrier\n"
    "v_mov_b64 v[208:209], v[48:49]\n"
    "v_mov_b64 v[210:211], v[50:51]\n"
    "v_mov_b64 v[212:213], v[52:53]\n"
    "v_mov_b64 v[214:215], v[54:55]\n"
    "v_mov_b64 v[216:217], v[56:57]\n"
    "v_mov_b64 v[218:219], v[58:59]\n"
    "v_mov_b64 v[220:221], v[60:61]\n"
    "v_mov_b64 v[222:223], v[62:63]\n"
    "v_add_u32 v14, 65536, v1\n"
    "ds_write_b128 v14, v[16:19] offset:0\n"
    "ds_write_b128 v14, v[20:23] offset:1024\n"
    "ds_write_b128 v14, v[24:27] offset:8192\n"
    "ds_write_b128 v14, v[28:31] offset:9216\n"
    "ds_write_b128 v14, v[32:35] offset:16384\n"
    "ds_write_b128 v14, v[36:39] offset:17408\n"
    "ds_write_b128 v14, v[40:43] offset:24576\n"
    "ds_write_b128 v14, v[44:47] offset:25600\n"
    "s_waitcnt lgkmcnt(4)\n"
    "v_add_u32 v8, 114688, v7\n"
    "v_xor_b32 v9, 32, v8\n"
    "v_xor_b32 v10, 320, v8\n"
    "v_xor_b32 v11, 32, v10\n"
    "ds_read_b64_tr_b16 v[48:49], v8 offset:0\n"
    "ds_read_b64_tr_b16 v[50:51], v8 offset:512\n"
    "ds_read_b64_tr_b16 v[52:53], v8 offset:16384\n"
    "ds_read_b64_tr_b16 v[54:55], v8 offset:16896\n"
    "ds_read_b64_tr_b16 v[56:57], v9 offset:0\n"
    "ds_read_b64_tr_b16 v[58:59], v9 offset:512\n"
    "ds_read_b64_tr_b16 v[60:61], v9 offset:16384\n"
    "ds_read_b64_tr_b16 v[62:63], v9 offset:16896\n"
    "s_waitcnt lgkmcnt(4)\n"
    "ds_read_b64_tr_b16 v[64:65], v10 offset:0\n"
    "ds_read_b64_tr_b16 v[66:67], v10 offset:512\n"
    "ds_read_b64_tr_b16 v[68:69], v10 offset:16384\n"
    "ds_read_b64_tr_b16 v[70:71], v10 offset:16896\n"
    "ds_read_b64_tr_b16 v[72:73], v11 offset:0\n"
    "ds_read_b64_tr_b16 v[74:75], v11 offset:512\n"
    "ds_read_b64_tr_b16 v[76:77], v11 offset:16384\n"
    "ds_read_b64_tr_b16 v[78:79], v11 offset:16896\n"
    "s_waitcnt vmcnt(0) lgkmcnt(0)\n"
    "s_barrier\n"
    ".Lrec_par_%=:\n"
    "v_readfirstlane_b32 s73, v12\n"
    "v_readfirstlane_b32 s74, v13\n"
    "s_xor_b32 s73, s73, s30\n"
    "s_xor_b32 s74, s74, s31\n"
    "s_cselect_b32 s74, 2, 0\n"
    "s_or_b32 s74, s74, s73\n"
    "s_cmp_lt_u32 s74, 2\n"
    "s_cbranch_scc1 .Lrec_par1_%=\n"
    "s_sleep 4\n"
    "buffer_load_dwordx2 v[12:13], off, s[36:39], 0 sc1\n"
    "s_waitcnt vmcnt(0)\n"
    "s_sub_u32 s69, s69, 1\n"
    "s_cbranch_scc0 .Lrec_par_%=\n"
    "s_mov_b32 s69, 0\n"
    ".Lrec_par1_%=:\n"
    "s_and_b32 s73, s73, 1\n"
    "s_lshl_b32 s73, s73, 13\n"
    "s_add_u32 s36, s36, s73\n"
    "s_addc_u32 s37, s37, 0\n"
    "v_lshlrev_b32 v44, 3, v0\n"
    "v_mov_b32 v45, 0x80000000\n"
    "v_cmp_gt_u32 vcc, 192, v0\n"
    "s_nop 1\n"
    "v_cndmask_b32 v44, v45, v44, vcc\n"
    "v_mov_b32 v46, -1\n"
    "buffer_load_dword v45, v44, s[36:39], s62 offen sc1\n"
    "s_mov_b64 exec, 1\n"
    "buffer_atomic_smax v46, off, s[36:39], s59 sc0 sc1\n"
    "s_mov_b64 exec, -1\n"
    "v_add_u32 v14, 98304, v1\n"
    "ds_write_b128 v14, v[48:51] offset:0\n"
    "ds_write_b128 v14, v[52:55] offset:1024\n"
    "ds_write_b128 v14, v[56:59] offset:8192\n"
    "ds_write_b128 v14, v[60:63] offset:9216\n"
    "ds_write_b128 v14, v[64:67] offset:16384\n"
    "ds_write_b128 v14, v[68:71] offset:17408\n"
    "ds_write_b128 v14, v[72:75] offset:24576\n"
    "ds_write_b128 v14, v[76:79] offset:25600\n"
    "s_waitcnt lgkmcnt(4)\n"
    "v_add_u32 v8, 147456, v3\n"
    "v_xor_b32 v9, 32, v8\n"
    "v_xor_b32 v10, 320, v8\n"
    "v_xor_b32 v11, 32, v10\n"
    "ds_read_b64_tr_b16 v[16:17], v8 offset:0\n"
    "ds_read_b64_tr_b16 v[18:19], v9 offset:1024\n"
    "ds_read_b64_tr_b16 v[20:21], v10 offset:0\n"
    "ds_read_b64_tr_b16 v[22:23], v11 offset:1024\n"
    "ds_read_b64_tr_b16 v[24:25], v8 offset:8192\n"
    "ds_read_b64_tr_b16 v[26:27], v9 offset:9216\n"
    "ds_read_b64_tr_b16 v[28:29], v10 offset:8192\n"
    "ds_read_b64_tr_b16 v[30:31], v11 offset:9216\n"
    "s_waitcnt lgkmcnt(4)\n"
    "s_waitcnt lgkmcnt(0)\n"
    "s_barrier\n"
    "v_add_u32 v14, 131072, v1\n"
    "ds_write_b128 v14, v[16:19] offset:0\n"
    "ds_write_b128 v14, v[20:23] offset:1024\n"
    "ds_write_b128 v14, v[24:27] offset:8192\n"
    "ds_write_b128 v14, v[28:31] offset:9216\n"
    "s_memtime s[32:33]\n"
    "s_memrealtime s[28:29]\n"
    "s_mov_b32 s84, 0x3e0293ee\n"
    "s_waitcnt vmcnt(0)\n"
    ".Lrec_init_%=:\n"
    "v_cmp_gt_i32 vcc, 0, v45\n"
    "s_cbranch_vccz .Lrec_init1_%=\n"
    "s_sleep 2\n"
    "buffer_load_dword v45, v44, s[36:39], s62 offen sc1\n"
    "s_waitcnt vmcnt(0)\n"
    "s_sub_u32 s69, s69, 1\n"
    "s_cbranch_scc0 .Lrec_init_%=\n"
    "s_mov_b32 s69, 0\n"
    ".Lrec_init1_%=:\n"
    "v_mov_b32 v3, v46\n"
    "s_mov_b32 s40, s76\n"
    "s_mov_b32 s41, s77\n"
    "s_mov_b32 s42, s78\n"
    "s_mov_b32 s43, s79\n"
    "s_mov_b32 s44, s80\n"
    "s_mov_b32 s45, s81\n"
    "s_mov_b32 s50, 0\n"
    "s_waitcnt lgkmcnt(0)\n"
    "s_mov_b32 s46, s34\n"
    "s_lshl_b32 s47, s92, 2\n"
    "s_mov_b32 s33, s32\n"
    "s_lshl_b32 s28, s28, 2\n"
    "s_mov_b32 s52, 900\n"
    "s_and_b32 s35, s40, 1\n"
    "s_and_b32 s40, s40, -2\n"
    "s_mul_i32 s35, s35, 20\n"
    "s_add_u32 s34, s35, 38\n"
    "s_lshl_b32 s35, s35, 2\n"
    "s_mov_b32 s53, 0\n"
    "s_mov_b32 s54, 0x7fffffff\n"
    "s_mov_b32 s55, 0\n"
    "s_mov_b32 s68, -1\n"
    "s_add_u32 s68, s68, 1\n"
    "s_and_b32 s70, s68, 3\n"
    "s_lshl_b32 s70, s70, 17\n"
    "s_add_u32 s70, s70, s66\n"
    "s_add_u32 s71, s70, 4096\n"
    "s_add_u32 s72, s70, 16384\n"
    "s_sub_u32 s46, s33, s46\n"
    "s_sub_u32 s47, s28, s47\n"
    "v_cvt_f32_u32 v40, s46\n"
    "v_cvt_f32_u32 v41, s47\n"
    "v_rcp_f32 v41, v41\n"
    "s_mov_b32 s46, s33\n"
    "s_mov_b32 s47, s28\n"
    "v_mul_f32 v40, v40, v41\n"
    "v_mov_b32 v42, 0x40c80000\n"
    "v_med3_f32 v40, v40, 4.0, v42\n"
    "v_rcp_f32 v41, v40\n"
    "v_mul_f32 v40, 0x43800000, v40\n"
    "v_mul_f32 v41, 0x44b54000, v41\n"
    "v_cvt_u32_f32 v40, v40\n"
    "v_cvt_u32_f32 v41, v41\n"
    "v_readfirstlane_b32 s48, v40\n"
    "v_readfirstlane_b32 s49, v41\n"
    "v_mov_b64 v[40:41], 0\n"
    "v_mov_b64 v[42:43], 0\n"
    "v_mov_b64 v[44:45], 0\n"
    "v_mov_b64 v[46:47], 0\n"
    "v_mov_b64 v[48:49], 0\n"
    "v_mov_b64 v[50:51], 0\n"
    "v_mov_b64 v[52:53], 0\n"
    "v_mov_b64 v[54:55], 0\n"
    "v_mov_b64 v[56:57], 0\n"
    "v_mov_b64 v[58:59], 0\n"
    "v_mov_b64 v[60:61], 0\n"
    "v_mov_b64 v[62:63], 0\n"
    "s_waitcnt lgkmcnt(0)\n"
    "s_barrier\n"
    "s_branch .Lrec_entry_%=\n"
    ".Lrec_LOOPB_%=:\n"
    ".Lrec_top_%=:\n"
    "buffer_load_dwordx4 v[4:7], v0, s[36:39], s70 offen sc1\n"
    "s_mov_b32 s82, s50\n"
    "s_add_u32 s50, s50, 589824\n"
    "s_cmp_eq_u32 s50, 1769472\n"
    "s_cselect_b32 s50, 0, s50\n"
    "s_add_u32 s80, s68, 2\n"
    "s_and_b32 s80, s80, 3\n"
    "s_lshl_b32 s80, s80, 17\n"
    "buffer_load_dwordx4 v[8:11], v0, s[36:39], s70 offen offset:1024 sc1\n"
    "s_and_b32 s80, s80, s45\n"
    "s_and_b32 s91, s50, s44\n"
    "s_add_u32 s80, s80, s91\n"
    "s_add_u32 s80, s80, s43\n"
    "s_add_u32 s89, s42, s50\n"
    "s_lshr_b32 s90, s68, 2\n"
    "s_lshl_b32 s90, s90, 14\n"
    "buffer_load_dwordx4 v[12:15], v0, s[36:39], s70 offen offset:2048 sc1\n"
    "s_and_b32 s91, s68, 3\n"
    "s_lshl_b32 s91, s91, 11\n"
    "s_add_u32 s90, s90, s91\n"
    "s_add_u32 s90, s90, s40\n"
    "s_add_u32 s51, s90, s41\n"
    "s_and_b32 s76, s68, 7\n"
    "s_mul_i32 s76, s76, 4194304\n"
    "buffer_load_dwordx4 v[16:19], v0, s[36:39], s70 offen offset:3072 sc1\n"
    "s_add_u32 s75, s76, s56\n"
    "s_add_u32 s83, s75, 16384\n"
    "s_add_u32 s76, s76, s57\n"
    "s_add_u32 s77, s63, s82\n"
    "s_add_u32 s86, s77, 262144\n"
    "s_add_u32 s87, s67, s82\n"
    "s_add_u32 s91, s68, 1\n"
    "buffer_load_dwordx4 v[20:23], v0, s[36:39], s71 offen sc1\n"
    "s_and_b32 s79, s91, 3\n"
    "s_lshl_b32 s79, s79, 17\n"
    "s_add_u32 s79, s79, s65\n"
    "s_add_u32 s78, s64, s82\n"
    "s_add_u32 s88, s78, 4096\n"
    "s_nop 1\n"
    "buffer_load_dwordx4 v[24:27], v0, s[36:39], s71 offen offset:1024 sc1\n"
    "s_nop 6\n"
    "buffer_load_dwordx4 v[28:31], v0, s[36:39], s71 offen offset:2048 sc1\n"
    "s_nop 6\n"
    "buffer_load_dwordx4 v[32:35], v0, s[36:39], s71 offen offset:3072 sc1\n"
    "s_waitcnt vmcnt(7)\n"
    ".Lrec_start_%=:\n"
    "v_smfmac_f32_16x16x64_bf16 v[40:43], v[4:7], a[0:7], v2\n"
    "v_max3_u32 v3, v4, v5, v6\n"
    "v_smfmac_f32_16x16x64_bf16 v[44:47], v[4:7], a[8:15], v2\n"
    "v_max_u32 v3, v3, v7\n"
    "v_smfmac_f32_16x16x64_bf16 v[48:51], v[4:7], a[144:151], v2\n"
    "v_cmp_eq_u32 vcc, -1, v3\n"
    "v_smfmac_f32_16x16x64_bf16 v[52:55], v[4:7], a[152:159], v2\n"
    "buffer_load_dwordx4 v[36:39], v0, s[36:39], s72 offen sc1\n"
    "v_smfmac_f32_16x16x64_bf16 v[56:59], v[4:7], v[112:119], v2\n"
    "s_cbranch_vccnz .Lrec_retry0_%=\n"
    ".Lrec_back0_%=:\n"
    "s_waitcnt lgkmcnt(0)\n"
    "v_smfmac_f32_16x16x64_bf16 v[60:63], v[4:7], v[72:79], v2\n"
    "ds_read_b128 v[72:75], v1 offset:8192\n"
    "s_waitcnt vmcnt(7)\n"
    "v_smfmac_f32_16x16x64_bf16 v[40:43], v[8:11], a[16:23], v2\n"
    "ds_read_b128 v[76:79], v1 offset:9216\n"
    "v_max3_u32 v3, v3, v8, v9\n"
    "v_smfmac_f32_16x16x64_bf16 v[44:47], v[8:11], a[24:31], v2\n"
    "buffer_load_dwordx4 v[68:71], v0, s[36:39], s75 offen offset:1024 sc1\n"
    "v_smfmac_f32_16x16x64_bf16 v[48:51], v[8:11], a[160:167], v2\n"
    "v_max3_u32 v3, v3, v10, v11\n"
    "v_smfmac_f32_16x16x64_bf16 v[52:55], v[8:11], a[168:175], v2\n"
    "buffer_load_dwordx4 v[64:67], v0, s[36:39], s75 offen sc1\n"
    "v_smfmac_f32_16x16x64_bf16 v[56:59], v[8:11], v[120:127], v2\n"
    "s_waitcnt vmcnt(8) lgkmcnt(0)\n"
    "v_smfmac_f32_16x16x64_bf16 v[60:63], v[8:11], v[72:79], v2\n"
    "ds_read_b128 v[72:75], v1 offset:16384\n"
    "v_smfmac_f32_16x16x64_bf16 v[40:43], v[12:15], a[32:39], v2\n"
    "ds_read_b128 v[76:79], v1 offset:17408\n"
    "v_max3_u32 v3, v3, v12, v13\n"
    "v_smfmac_f32_16x16x64_bf16 v[44:47], v[12:15], a[40:47], v2\n"
    "v_max3_u32 v3, v3, v14, v15\n"
    "v_smfmac_f32_16x16x64_bf16 v[48:51], v[12:15], a[176:183], v2\n"
    "buffer_load_dwordx4 v[4:7], v0, s[36:39], s83 offen sc1\n"
    "v_smfmac_f32_16x16x64_bf16 v[52:55], v[12:15], a[184:191], v2\n"
    "buffer_load_dwordx4 v[8:11], v0, s[36:39], s83 offen offset:1024 sc1\n"
    "v_smfmac_f32_16x16x64_bf16 v[56:59], v[12:15], v[128:135], v2\n"
    "s_waitcnt vmcnt(9) lgkmcnt(0)\n"
    "v_max3_u32 v3, v3, v16, v17\n"
    "v_smfmac_f32_16x16x64_bf16 v[60:63], v[12:15], v[72:79], v2\n"
    "ds_read_b128 v[72:75], v1 offset:24576\n"
    "v_max3_u32 v3, v3, v18, v19\n"
    "v_smfmac_f32_16x16x64_bf16 v[40:43], v[16:19], a[48:55], v2\n"
    "ds_read_b128 v[76:79], v1 offset:25600\n"
    "v_cmp_eq_u32 vcc, -1, v3\n"
    "v_smfmac_f32_16x16x64_bf16 v[44:47], v[16:19], a[56:63], v2\n"
    "v_mov_b64 v[12:13], -1\n"
    "v_smfmac_f32_16x16x64_bf16 v[48:51], v[16:19], a[192:199], v2\n"
    "s_cbranch_vccnz .Lrec_retry1_%=\n"
    ".Lrec_back1_%=:\n"
    "v_mov_b64 v[14:15], -1\n"
    "v_smfmac_f32_16x16x64_bf16 v[52:55], v[16:19], a[200:207], v2\n"
    "s_waitcnt vmcnt(8) lgkmcnt(0)\n"
    "v_max3_u32 v3, v3, v20, v21\n"
    "v_smfmac_f32_16x16x64_bf16 v[60:63], v[16:19], v[72:79], v2\n"
    "ds_read_b128 v[72:75], v1 offset:32768\n"
    "v_max3_u32 v3, v3, v22, v23\n"
    "v_smfmac_f32_16x16x64_bf16 v[40:43], v[20:23], a[64:71], v2\n"
    "ds_read_b128 v[76:79], v1 offset:33792\n"
    "v_smfmac_f32_16x16x64_bf16 v[44:47], v[20:23], a[72:79], v2\n"
    "buffer_store_dwordx4 v[12:15], v0, s[36:39], s89 offen sc1\n"
    "v_smfmac_f32_16x16x64_bf16 v[48:51], v[20:23], a[208:215], v2\n"
    "v_smfmac_f32_16x16x64_bf16 v[52:55], v[20:23], a[216:223], v2\n"
    "s_waitcnt vmcnt(7) lgkmcnt(0)\n"
    "v_max3_u32 v3, v3, v24, v25\n"
    "v_smfmac_f32_16x16x64_bf16 v[60:63], v[20:23], v[72:79], v2\n"
    "ds_read_b128 v[72:75], v1 offset:40960\n"
    "v_max3_u32 v3, v3, v26, v27\n"
    "v_smfmac_f32_16x16x64_bf16 v[40:43], v[24:27], a[80:87], v2\n"
    "ds_read_b128 v[76:79], v1 offset:41984\n"
    "v_max3_u32 v3, v3, v28, v29\n"
    "v_smfmac_f32_16x16x64_bf16 v[44:47], v[24:27], a[88:95], v2\n"
    "v_max3_u32 v3, v3, v30, v31\n"
    "v_smfmac_f32_16x16x64_bf16 v[48:51], v[24:27], a[224:231], v2\n"
    "buffer_store_dwordx4 v[12:15], v0, s[36:39], s89 offen offset:1024 sc1\n"
    "v_smfmac_f32_16x16x64_bf16 v[52:55], v[24:27], a[232:239], v2\n"
    "s_waitcnt vmcnt(7) lgkmcnt(0)\n"
    "v_max3_u32 v3, v3, v32, v33\n"
    "v_smfmac_f32_16x16x64_bf16 v[60:63], v[24:27], v[72:79], v2\n"
    "ds_read_b128 v[72:75], v0 offset:13312\n"
    "v_max3_u32 v3, v3, v34, v35\n"
    "v_smfmac_f32_16x16x64_bf16 v[40:43], v[28:31], a[96:103], v2\n"
    "s_waitcnt vmcnt(6)\n"
    "buffer_store_dwordx4 v[12:15], v0, s[36:39], s80 offen sc1\n"
    "v_max3_u32 v3, v3, v36, v37\n"
    "v_smfmac_f32_16x16x64_bf16 v[44:47], v[28:31], a[104:111], v2\n"
    "ds_read_b128 v[12:15], v0 offset:12288\n"
    "v_max3_u32 v3, v3, v38, v39\n"
    "v_smfmac_f32_16x16x64_bf16 v[40:43], v[32:35], a[112:119], v2\n"
    "s_lshr_b32 s73, s60, 1\n"
    "v_cmp_eq_u32 vcc, -1, v3\n"
    "v_smfmac_f32_16x16x64_bf16 v[44:47], v[32:35], a[120:127], v2\n"
    "v_add_u32 v79, s60, v0\n"
    "v_smfmac_f32_16x16x64_bf16 v[48:51], v[28:31], a[240:247], v2\n"
    "s_cbranch_vccnz .Lrec_retry2_%=\n"
    ".Lrec_back2_%=:\n"
    "v_xor_b32 v76, 1024, v79\n"
    "v_smfmac_f32_16x16x64_bf16 v[40:43], v[36:39], a[128:135], v2\n"
    "s_waitcnt lgkmcnt(0)\n"
    "s_memrealtime s[28:29]\n"
    "v_xor_b32 v77, 2048, v79\n"
    "v_smfmac_f32_16x16x64_bf16 v[44:47], v[36:39], a[136:143], v2\n"
    "s_memrealtime s[30:31]\n"
    "v_xor_b32 v78, 3072, v79\n"
    "v_smfmac_f32_16x16x64_bf16 v[52:55], v[28:31], a[248:255], v2\n"
    "s_memtime s[32:33]\n"
    "v_cvt_pk_bf16_f32 v40, v40, v41\n"
    "v_smfmac_f32_16x16x64_bf16 v[48:51], v[32:35], v[80:87], v2\n"
    "s_memrealtime s[96:97]\n"
    "v_cvt_pk_bf16_f32 v41, v42, v43\n"
    "v_smfmac_f32_16x16x64_bf16 v[52:55], v[32:35], v[88:95], v2\n"
    "s_memrealtime s[98:99]\n"
    "v_cvt_pk_bf16_f32 v42, v44, v45\n"
    "v_smfmac_f32_16x16x64_bf16 v[48:51], v[36:39], v[96:103], v2\n"
    "v_cvt_pk_bf16_f32 v43, v46, v47\n"
    "v_smfmac_f32_16x16x64_bf16 v[52:55], v[36:39], v[104:111], v2\n"
    "v_add_u32 v1, 49152, v1\n"
    "v_mfma_f32_16x16x32_bf16 v[44:47], v[40:43], v[12:15], 0\n"
    "s_waitcnt lgkmcnt(0)\n"
    "v_cvt_pk_bf16_f32 v48, v48, v49\n"
    "v_cvt_pk_bf16_f32 v49, v50, v51\n"
    "v_lshrrev_b32 v3, 1, v79\n"
    "v_cvt_pk_bf16_f32 v50, v52, v53\n"
    "v_cvt_pk_bf16_f32 v51, v54, v55\n"
    "s_add_u32 s28, s28, s30\n"
    "s_add_u32 s96, s96, s98\n"
    "v_mfma_f32_16x16x32_bf16 v[52:55], v[48:51], v[12:15], 0\n"
    "s_add_u32 s28, s28, s96\n"
    "v_cvt_pk_bf16_f32 v40, v44, v45\n"
    "v_cvt_pk_bf16_f32 v41, v46, v47\n"
    "v_mov_b32 v44, s28\n"
    "v_mov_b32_dpp v42, v40 row_ror:8 row_mask:0xf bank_mask:0xf\n"
    "v_mov_b32_dpp v43, v41 row_ror:8 row_mask:0xf bank_mask:0xf\n"
    "v_add_u32 v44, s49, v44\n"
    "buffer_store_dword v44, off, s[36:39], s51 sc1\n"
    "s_waitcnt vmcnt(6)\n"
    "v_mfma_f32_16x16x32_bf16 v[44:47], v[68:71], v[40:43], 0\n"
    "v_cvt_pk_bf16_f32 v48, v52, v53\n"
    "v_cvt_pk_bf16_f32 v49, v54, v55\n"
    "v_mfma_f32_16x16x32_bf16 v[68:71], v[64:67], v[40:43], 0\n"
    "v_mov_b32_dpp v50, v48 row_ror:8 row_mask:0xf bank_mask:0xf\n"
    "v_mov_b32_dpp v51, v49 row_ror:8 row_mask:0xf bank_mask:0xf\n"
    "s_nop 1\n"
    "v_mfma_f32_16x16x32_bf16 v[52:55], v[48:51], v[40:43], 0\n"
    "s_nop 2\n"
    "v_mov_b32_dpp v68, v44 row_ror:8 row_mask:0xf bank_mask:0xc\n"
    "v_mov_b32_dpp v69, v45 row_ror:8 row_mask:0xf bank_mask:0xc\n"
    "v_mov_b32_dpp v70, v46 row_ror:8 row_mask:0xf bank_mask:0xc\n"
    "v_mov_b32_dpp v71, v47 row_ror:8 row_mask:0xf bank_mask:0xc\n"
    "ds_write_b128 v79, v[68:71] offset:0\n"
    "v_mov_b32_dpp v52, v54 row_ror:8 row_mask:0xf bank_mask:0xc\n"
    "v_mov_b32_dpp v53, v55 row_ror:8 row_mask:0xf bank_mask:0xc\n"
    "ds_write_b64 v3, v[52:53] offset:4096\n"
    "v_smfmac_f32_16x16x64_bf16 v[56:59], v[16:19], v[136:143], v2\n"
    "v_smfmac_f32_16x16x64_bf16 v[56:59], v[20:23], v[144:151], v2\n"
    "ds_read_b128 v[16:19], v1 offset:16384\n"
    "v_smfmac_f32_16x16x64_bf16 v[56:59], v[24:27], v[152:159], v2\n"
    "ds_read_b128 v[20:23], v1 offset:17408\n"
    "v_smfmac_f32_16x16x64_bf16 v[56:59], v[28:31], v[160:167], v2\n"
    "s_waitcnt lgkmcnt(2)\n"
    "s_barrier\n"
    "ds_read_b128 v[64:67], v76 offset:0\n"
    "v_smfmac_f32_16x16x64_bf16 v[60:63], v[28:31], v[168:175], v2\n"
    "ds_read_b128 v[40:43], v77 offset:0\n"
    "v_smfmac_f32_16x16x64_bf16 v[56:59], v[32:35], v[176:183], v2\n"
    "ds_read_b128 v[44:47], v78 offset:0\n"
    "v_smfmac_f32_16x16x64_bf16 v[60:63], v[32:35], v[184:191], v2\n"
    "ds_read_b64 v[12:13], v72 offset:4096\n"
    "ds_read_b64 v[14:15], v72 offset:4608\n"
    "v_smfmac_f32_16x16x64_bf16 v[56:59], v[36:39], v[192:199], v2\n"
    "ds_read_b64 v[52:53], v72 offset:5120\n"
    "ds_read_b64 v[54:55], v72 offset:5632\n"
    "v_smfmac_f32_16x16x64_bf16 v[60:63], v[36:39], v[200:207], v2\n"
    "buffer_store_dwordx4 v[48:51], v74, s[36:39], s76 offen sc1\n"
    "s_cmp_eq_u32 s68, 63\n"
    "s_waitcnt lgkmcnt(4)\n"
    "v_add_f32 v68, v68, v64\n"
    "v_add_f32 v69, v69, v65\n"
    "v_add_f32 v70, v70, v66\n"
    "v_add_f32 v71, v71, v67\n"
    "v_pk_add_f32 v[40:41], v[40:41], v[44:45]\n"
    "v_pk_add_f32 v[42:43], v[42:43], v[46:47]\n"
    "s_waitcnt lgkmcnt(0)\n"
    "v_pk_add_f32 v[12:13], v[12:13], v[14:15]\n"
    "v_pk_add_f32 v[52:53], v[52:53], v[54:55]\n"
    "v_pk_add_f32 v[68:69], v[68:69], v[40:41]\n"
    "v_pk_add_f32 v[70:71], v[70:71], v[42:43]\n"
    "v_pk_add_f32 v[12:13], v[12:13], v[52:53]\n"
    "v_max3_f32 v46, v68, v69, v70\n"
    "v_max3_f32 v47, v71, v12, v13\n"
    "v_max_f32 v3, v46, v47\n"
    "v_cvt_pk_bf16_f32 v56, v56, v57\n"
    "v_cvt_pk_bf16_f32 v57, v58, v59\n"
    "v_max_f32_dpp v46, v3, v3 row_ror:8 row_mask:0xf bank_mask:0xf\n"
    "v_max_f32_dpp v47, v3, v3 row_ror:8 row_mask:0xf bank_mask:0xf\n"
    "v_cvt_pk_bf16_f32 v58, v60, v61\n"
    "v_cvt_pk_bf16_f32 v59, v62, v63\n"
    "v_permlane16_swap_b32 v46, v47\n"
    "v_max_f32 v3, v46, v47\n"
    "v_max_f32 v46, v46, v47\n"
    "v_add_u32 v14, s73, v73\n"
    "v_xor_b32 v15, 128, v0\n"
    "v_permlane32_swap_b32 v3, v46\n"
    "v_max_f32 v3, v3, v46\n"
    "v_mul_f32 v46, 0xbe0293ee, v3\n"
    "v_pk_fma_f32 v[68:69], v[68:69], s[84:85], v[46:47] op_sel_hi:[1,0,0]\n"
    "v_pk_fma_f32 v[70:71], v[70:71], s[84:85], v[46:47] op_sel_hi:[1,0,0]\n"
    "v_pk_fma_f32 v[12:13], v[12:13], s[84:85], v[46:47] op_sel_hi:[1,0,0]\n"
    "v_exp_f32 v68, v68\n"
    "v_exp_f32 v69, v69\n"
    "v_exp_f32 v70, v70\n"
    "v_exp_f32 v71, v71\n"
    "v_exp_f32 v12, v12\n"
    "v_cvt_pk_bf16_f32 v40, v68, v69\n"
    "v_cvt_pk_bf16_f32 v41, v70, v71\n"
    "v_exp_f32 v13, v13\n"
    "v_mov_b32_dpp v42, v40 row_ror:8 row_mask:0xf bank_mask:0xf\n"
    "v_mov_b32_dpp v43, v41 row_ror:8 row_mask:0xf bank_mask:0xf\n"
    "v_cvt_pk_bf16_f32 v44, v12, v13\n"
    "v_pk_add_f32 v[68:69], v[68:69], v[70:71]\n"
    "s_waitcnt vmcnt(5)\n"
    "v_mfma_f32_16x16x32_bf16 v[48:51], v[4:7], v[40:43], 0\n"
    "ds_read_b128 v[28:31], v1 offset:1024\n"
    "v_mov_b32_dpp v45, v44 row_ror:8 row_mask:0xf bank_mask:0xf\n"
    "v_add_f32 v3, v12, v13\n"
    "v_mfma_f32_16x16x32_bf16 v[52:55], v[8:11], v[40:43], 0\n"
    "ds_read_b128 v[24:27], v1 offset:0\n"
    "v_add_f32 v46, v68, v69\n"
    "v_fma_f32 v3, v3, 0.5, v46\n"
    "v_mfma_f32_16x16x16_bf16 v[48:51], v[56:57], v[44:45], v[48:51]\n"
    "ds_read_b128 v[32:35], v1 offset:24576\n"
    "v_add_f32_dpp v46, v3, v3 row_ror:8 row_mask:0xf bank_mask:0xf\n"
    "v_add_f32_dpp v47, v3, v3 row_ror:8 row_mask:0xf bank_mask:0xf\n"
    "v_mfma_f32_16x16x16_bf16 v[52:55], v[58:59], v[44:45], v[52:55]\n"
    "buffer_load_dwordx2 v[12:13], v75, s[36:39], s90 offen sc1\n"
    "v_permlane16_swap_b32 v46, v47\n"
    "v_add_f32 v3, v46, v47\n"
    "v_add_f32 v46, v46, v47\n"
    "v_mov_b64 v[64:65], 0\n"
    "v_mov_b64 v[66:67], 0\n"
    "v_permlane32_swap_b32 v3, v46\n"
    "v_add_f32 v3, v3, v46\n"
    "v_rcp_f32 v46, v3\n"
    "v_mov_b32_dpp v48, v52 row_ror:8 row_mask:0xf bank_mask:0xc\n"
    "v_mov_b32_dpp v49, v53 row_ror:8 row_mask:0xf bank_mask:0xc\n"
    "v_mov_b32_dpp v50, v54 row_ror:8 row_mask:0xf bank_mask:0xc\n"
    "v_mov_b32_dpp v51, v55 row_ror:8 row_mask:0xf bank_mask:0xc\n"
    "v_pk_mul_f32 v[48:49], v[48:49], v[46:47] op_sel_hi:[1,0]\n"
    "v_pk_mul_f32 v[50:51], v[50:51], v[46:47] op_sel_hi:[1,0]\n"
    "v_cvt_pk_bf16_f32 v48, v48, v49\n"
    "v_cvt_pk_bf16_f32 v49, v50, v51\n"
    "ds_write_b64 v14, v[48:49] offset:8192\n"
    "s_cbranch_scc1 .Lrec_fin_%=\n"
    "ds_read_b128 v[36:39], v1 offset:25600\n"
    "ds_read_b128 v[72:75], v1 offset:8192\n"
    "ds_read_b128 v[76:79], v1 offset:9216\n"
    "ds_read_b128 v[4:7], v1 offset:32768\n"
    "ds_read_b128 v[8:11], v1 offset:33792\n"
    "v_mov_b64 v[68:69], 0\n"
    "v_mov_b64 v[70:71], 0\n"
    "s_waitcnt vmcnt(3) lgkmcnt(5)\n"
    "s_barrier\n"
    "ds_read_b128 v[40:43], v0 offset:8192\n"
    "ds_read_b128 v[44:47], v0 offset:9216\n"
    "ds_read_b128 v[48:51], v15 offset:8192\n"
    "ds_read_b128 v[52:55], v15 offset:9216\n"
    "buffer_store_dwordx4 v[56:59], v0, s[36:39], s76 offen offset:512 sc1\n"
    "s_waitcnt lgkmcnt(3)\n"
    "v_smfmac_f32_16x16x64_bf16 v[64:67], v[40:43], v[16:23], v2\n"
    "ds_read_b128 v[16:19], v1 offset:40960\n"
    "s_waitcnt lgkmcnt(3)\n"
    "v_smfmac_f32_16x16x64_bf16 v[64:67], v[44:47], v[24:31], v2\n"
    "ds_read_b128 v[20:23], v1 offset:41984\n"
    "v_smfmac_f32_16x16x64_bf16 v[68:71], v[40:43], v[32:39], v2\n"
    "ds_read_b128 v[24:27], v1 offset:49152\n"
    "v_mov_b64 v[56:57], 0\n"
    "v_smfmac_f32_16x16x64_bf16 v[68:71], v[44:47], v[72:79], v2\n"
    "ds_read_b128 v[28:31], v1 offset:50176\n"
    "v_mov_b64 v[58:59], 0\n"
    "s_waitcnt lgkmcnt(5)\n"
    "v_smfmac_f32_16x16x64_bf16 v[64:67], v[48:51], v[4:11], v2\n"
    "ds_read_b128 v[32:35], v1 offset:57344\n"
    "v_mov_b64 v[60:61], 0\n"
    "s_waitcnt lgkmcnt(5)\n"
    "v_smfmac_f32_16x16x64_bf16 v[64:67], v[52:55], v[208:215], v2\n"
    "ds_read_b128 v[36:39], v1 offset:58368\n"
    "ds_read_b128 v[8:11], v0 offset:14336\n"
    "s_waitcnt lgkmcnt(5)\n"
    "v_smfmac_f32_16x16x64_bf16 v[68:71], v[48:51], v[16:23], v2\n"
    "v_add_u32 v1, 32768, v1\n"
    "ds_read_b128 v[72:75], v1 offset:32768\n"
    "v_mov_b64 v[62:63], 0\n"
    "v_smfmac_f32_16x16x64_bf16 v[68:71], v[52:55], v[216:223], v2\n"
    "ds_read_b128 v[76:79], v1 offset:33792\n"
    "v_cvt_pk_bf16_f32 v64, v64, v65\n"
    "s_waitcnt lgkmcnt(5)\n"
    "v_smfmac_f32_16x16x64_bf16 v[56:59], v[40:43], v[24:31], v2\n"
    "ds_read_b128 v[16:19], v1 offset:40960\n"
    "v_cvt_pk_bf16_f32 v65, v66, v67\n"
    "v_smfmac_f32_16x16x64_bf16 v[56:59], v[44:47], v[224:231], v2\n"
    "ds_read_b128 v[20:23], v1 offset:41984\n"
    "v_cvt_pk_bf16_f32 v66, v68, v69\n"
    "v_cvt_pk_bf16_f32 v67, v70, v71\n"
    "s_waitcnt lgkmcnt(5)\n"
    "v_smfmac_f32_16x16x64_bf16 v[60:63], v[40:43], v[32:39], v2\n"
    "ds_read_b128 v[24:27], v1 offset:49152\n"
    "v_permlane16_swap_b32 v64, v66\n"
    "v_smfmac_f32_16x16x64_bf16 v[60:63], v[44:47], v[232:239], v2\n"
    "ds_read_b128 v[28:31], v1 offset:50176\n"
    "v_permlane16_swap_b32 v65, v67\n"
    "s_waitcnt lgkmcnt(4)\n"
    "v_smfmac_f32_16x16x64_bf16 v[56:59], v[48:51], v[72:79], v2\n"
    "ds_read_b128 v[32:35], v1 offset:57344\n"
    "buffer_store_dwordx4 v[64:67], v8, s[36:39], s77 offen sc1\n"
    "v_smfmac_f32_16x16x64_bf16 v[56:59], v[52:55], v[240:247], v2\n"
    "ds_read_b128 v[36:39], v1 offset:58368\n"
    "s_waitcnt lgkmcnt(4)\n"
    "v_smfmac_f32_16x16x64_bf16 v[60:63], v[48:51], v[16:23], v2\n"
    "v_smfmac_f32_16x16x64_bf16 v[60:63], v[52:55], v[248:255], v2\n"
    "v_cvt_pk_bf16_f32 v56, v56, v57\n"
    "s_waitcnt lgkmcnt(2)\n"
    "v_mfma_f32_16x16x32_bf16 v[4:7], v[40:43], v[24:27], 0\n"
    "v_cvt_pk_bf16_f32 v57, v58, v59\n"
    "v_mfma_f32_16x16x32_bf16 v[4:7], v[48:51], v[28:31], v[4:7]\n"
    "v_cvt_pk_bf16_f32 v58, v60, v61\n"
    "v_cvt_pk_bf16_f32 v59, v62, v63\n"
    "s_waitcnt lgkmcnt(0)\n"
    "v_mfma_f32_16x16x32_bf16 v[4:7], v[44:47], v[32:35], v[4:7]\n"
    "v_permlane16_swap_b32 v56, v58\n"
    "v_mfma_f32_16x16x32_bf16 v[4:7], v[52:55], v[36:39], v[4:7]\n"
    "v_permlane16_swap_b32 v57, v59\n"
    "buffer_store_dwordx4 v[56:59], v8, s[36:39], s86 offen sc1\n"
    "s_waitcnt vmcnt(3)\n"
    "v_max_u32 v12, v12, v13\n"
    "s_mov_b32 s33, s32\n"
    "s_nop 0\n"
    "v_max_u32_dpp v12, v12, v12 row_ror:8 row_mask:0xf bank_mask:0xf\n"
    "s_nop 1\n"
    "v_max_u32_dpp v12, v12, v12 row_ror:4 row_mask:0xf bank_mask:0xf\n"
    "v_cvt_pk_bf16_f32 v4, v4, v5\n"
    "v_cvt_pk_bf16_f32 v5, v6, v7\n"
    "v_max_u32_dpp v12, v12, v12 row_ror:2 row_mask:0xf bank_mask:0xf\n"
    "s_nop 0\n"
    "v_permlane16_swap_b32 v4, v6\n"
    "v_max_u32_dpp v12, v12, v12 row_ror:1 row_mask:0xf bank_mask:0xf\n"
    "v_permlane16_swap_b32 v5, v7\n"
    "buffer_store_dwordx4 v[4:7], v9, s[36:39], s87 offen sc1\n"
    "v_readlane_b32 s95, v12, 0\n"
    "v_readlane_b32 s99, v12, 16\n"
    "v_readlane_b32 s30, v12, 32\n"
    "v_readlane_b32 s31, v12, 48\n"
    "s_max_u32 s95, s95, s99\n"
    "s_max_u32 s30, s30, s31\n"
    "s_max_u32 s95, s95, s30\n"
    "s_cmp_eq_u32 s95, -1\n"
    "s_cbranch_scc1 .Lrec_tsretry_%=\n"
    ".Lrec_tsgo_%=:\n"
    "s_memtime s[96:97]\n"
    "v_mov_b32 v76, v10\n"
    "s_mov_b32 s31, s95\n"
    "s_sub_u32 s95, s95, s28\n"
    "s_min_u32 s95, s95, 4000\n"
    "s_add_u32 s95, s95, s34\n"
    "s_mul_i32 s95, s95, s48\n"
    "s_lshr_b32 s95, s95, 8\n"
    "s_add_u32 s32, s32, s95\n"
    "s_waitcnt lgkmcnt(0)\n"
    "s_sub_u32 s95, s32, s96\n"
    "s_mul_i32 s95, s95, 43\n"
    "s_ashr_i32 s95, s95, 10\n"
    "s_max_i32 s95, s95, 0\n"
    "s_min_i32 s95, s95, 100\n"
    ".Lrec_tsloop_%=:\n"
    "s_sub_u32 s95, s95, 1\n"
    "s_cbranch_scc0 .Lrec_tsloop_%=\n"
    "s_cmp_lg_u32 s60, 0\n"
    "s_cbranch_scc1 .Lrec_nored_%=\n"
    ".Lrec_red_%=:\n"
    "buffer_load_dwordx4 v[4:7], v0, s[36:39], s78 offen sc1\n"
    "buffer_load_dwordx4 v[8:11], v0, s[36:39], s78 offen offset:1024 sc1\n"
    "buffer_load_dwordx4 v[12:15], v0, s[36:39], s78 offen offset:2048 sc1\n"
    "buffer_load_dwordx4 v[16:19], v0, s[36:39], s78 offen offset:3072 sc1\n"
    "buffer_load_dwordx4 v[20:23], v0, s[36:39], s88 offen sc1\n"
    "buffer_load_dwordx4 v[24:27], v0, s[36:39], s88 offen offset:1024 sc1\n"
    "buffer_load_dwordx4 v[28:31], v0, s[36:39], s88 offen offset:2048 sc1\n"
    "buffer_load_dwordx4 v[32:35], v0, s[36:39], s88 offen offset:3072 sc1\n"
    "ds_read_b128 v[72:75], v0 offset:15360\n"
    "v_lshl_or_b32 v77, s68, 1, 1\n"
    "s_waitcnt vmcnt(11)\n"
    "s_mov_b64 exec, 1\n"
    "buffer_atomic_smax v77, off, s[36:39], s58 sc1\n"
    "s_mov_b64 exec, -1\n"
    "v_mov_b32 v3, -1\n"
    "v_subrev_u32 v1, 81920, v1\n"
    "s_sub_u32 s95, s52, s35\n"
    "s_mul_i32 s95, s48, s95\n"
    "s_lshr_b32 s95, s95, 10\n"
    "s_add_u32 s32, s32, s95\n"
    "s_add_u32 s68, s68, 1\n"
    "s_and_b32 s70, s68, 3\n"
    "s_lshl_b32 s70, s70, 17\n"
    "s_add_u32 s70, s70, s66\n"
    "s_add_u32 s71, s70, 4096\n"
    "s_add_u32 s72, s70, 16384\n"
    "s_sub_u32 s46, s33, s46\n"
    "s_sub_u32 s47, s28, s47\n"
    "v_cvt_f32_u32 v40, s46\n"
    "v_cvt_f32_u32 v41, s47\n"
    "v_rcp_f32 v41, v41\n"
    "s_mov_b32 s46, s33\n"
    "s_mov_b32 s47, s28\n"
    "v_mul_f32 v40, v40, v41\n"
    "v_mov_b32 v42, 0x40c80000\n"
    "v_med3_f32 v40, v40, 4.0, v42\n"
    "v_rcp_f32 v41, v40\n"
    "v_mul_f32 v40, 0x43800000, v40\n"
    "v_mul_f32 v41, 0x44b54000, v41\n"
    "v_cvt_u32_f32 v40, v40\n"
    "v_cvt_u32_f32 v41, v41\n"
    "v_readfirstlane_b32 s48, v40\n"
    "v_readfirstlane_b32 s49, v41\n"
    "v_mov_b64 v[40:41], 0\n"
    "v_mov_b64 v[42:43], 0\n"
    "v_mov_b64 v[44:45], 0\n"
    "v_mov_b64 v[46:47], 0\n"
    "v_mov_b64 v[48:49], 0\n"
    "v_mov_b64 v[50:51], 0\n"
    "v_mov_b64 v[52:53], 0\n"
    "v_mov_b64 v[54:55], 0\n"
    "v_mov_b64 v[56:57], 0\n"
    "v_mov_b64 v[58:59], 0\n"
    "v_mov_b64 v[60:61], 0\n"
    "v_mov_b64 v[62:63], 0\n"
    "s_mov_b64 exec, 1\n"
    "buffer_atomic_smax v3, off, s[36:39], s59 sc0 sc1\n"
    "s_mov_b64 exec, -1\n"
    "s_waitcnt lgkmcnt(0)\n"
    ".Lrec_rsum_%=:\n"
    "s_waitcnt vmcnt(9)\n"
    "v_mfma_f32_16x16x32_bf16 v[36:39], v[4:7], v[72:75], 0\n"
    "v_max3_u32 v78, v4, v5, v6\n"
    "v_max_u32 v78, v78, v7\n"
    "s_waitcnt vmcnt(8)\n"
    "v_mfma_f32_16x16x32_bf16 v[36:39], v[8:11], v[72:75], v[36:39]\n"
    "v_max3_u32 v78, v78, v8, v9\n"
    "v_max3_u32 v78, v78, v10, v11\n"
    "s_waitcnt vmcnt(7)\n"
    "v_mfma_f32_16x16x32_bf16 v[36:39], v[12:15], v[72:75], v[36:39]\n"
    "v_max3_u32 v78, v78, v12, v13\n"
    "v_max3_u32 v78, v78, v14, v15\n"
    "s_waitcnt vmcnt(6)\n"
    "v_mfma_f32_16x16x32_bf16 v[36:39], v[16:19], v[72:75], v[36:39]\n"
    "v_max3_u32 v78, v78, v16, v17\n"
    "v_max3_u32 v78, v78, v18, v19\n"
    "s_waitcnt vmcnt(5)\n"
    "v_mfma_f32_16x16x32_bf16 v[36:39], v[20:23], v[72:75], v[36:39]\n"
    "v_max3_u32 v78, v78, v20, v21\n"
    "v_max3_u32 v78, v78, v22, v23\n"
    "s_waitcnt vmcnt(4)\n"
    "v_mfma_f32_16x16x32_bf16 v[36:39], v[24:27], v[72:75], v[36:39]\n"
    "v_max3_u32 v78, v78, v24, v25\n"
    "v_max3_u32 v78, v78, v26, v27\n"
    "s_waitcnt vmcnt(3)\n"
    "v_mfma_f32_16x16x32_bf16 v[36:39], v[28:31], v[72:75], v[36:39]\n"
    "v_max3_u32 v78, v78, v28, v29\n"
    "v_max3_u32 v78, v78, v30, v31\n"
    "s_waitcnt vmcnt(2)\n"
    "v_mfma_f32_16x16x32_bf16 v[36:39], v[32:35], v[72:75], v[36:39]\n"
    "v_max3_u32 v78, v78, v32, v33\n"
    "v_max3_u32 v78, v78, v34, v35\n"
    "v_cmp_eq_u32 vcc, -1, v78\n"
    "s_nop 4\n"
    "v_cvt_pk_bf16_f32 v4, v36, v37\n"
    "v_cvt_pk_bf16_f32 v6, v36, v37\n"
    "v_cvt_pk_bf16_f32 v5, v38, v39\n"
    "v_cvt_pk_bf16_f32 v7, v38, v39\n"
    "s_cbranch_vccnz .Lrec_rretry_%=\n"
    ".Lrec_rgo_%=:\n"
    "v_permlane16_swap_b32 v4, v6\n"
    "v_permlane16_swap_b32 v5, v7\n"
    "buffer_store_dwordx4 v[4:7], v76, s[36:39], s79 offen sc1\n"
    "s_waitcnt vmcnt(1)\n"
    ".Lrec_b3_%=:\n"
    ".Lrec_entry_%=:\n"
    "s_memtime s[96:97]\n"
    "ds_read_b128 v[72:75], v1 offset:0\n"
    "ds_read_b128 v[76:79], v1 offset:1024\n"
    "v_readfirstlane_b32 s81, v3\n"
    "s_lshl_b32 s73, s68, 1\n"
    "s_cmp_le_i32 s81, s73\n"
    "s_cbranch_scc1 .Lrec_waitp_%=\n"
    "s_waitcnt lgkmcnt(0)\n"
    "s_sub_u32 s95, s32, s96\n"
    "s_mul_i32 s95, s95, 43\n"
    "s_ashr_i32 s95, s95, 10\n"
    "s_max_i32 s95, s95, 0\n"
    "s_min_i32 s95, s95, 100\n"
    ".Lrec_gloop_%=:\n"
    "s_sub_u32 s95, s95, 1\n"
    "s_cbranch_scc0 .Lrec_gloop_%=\n"
    "s_branch .Lrec_top_%=\n"
    ".Lrec_LOOPE_%=:\n"
    ".Lrec_fin_%=:\n"
    "buffer_store_dwordx4 v[56:59], v0, s[36:39], s76 offen offset:512 sc1\n"
    "s_waitcnt vmcnt(0) lgkmcnt(0)\n"
    "v_lshl_or_b32 v46, s68, 1, 1\n"
    "s_mov_b64 exec, 1\n"
    "buffer_atomic_smax v46, off, s[36:39], s58 sc1\n"
    "s_mov_b64 exec, -1\n"
    "s_waitcnt vmcnt(0)\n"
    "s_branch .Lrec_FIN_%=\n"
    ".Lrec_retry0_%=:\n"
    "s_cmp_eq_u32 s69, 0\n"
    "s_cbranch_scc1 .Lrec_back0_%=\n"
    "s_waitcnt lgkmcnt(0)\n"
    "ds_read_b128 v[72:75], v1 offset:0\n"
    "ds_read_b128 v[76:79], v1 offset:1024\n"
    ".Lrec_again0_%=:\n"
    "buffer_load_dwordx4 v[4:7], v0, s[36:39], s70 offen sc1\n"
    "s_nop 7\n"
    "v_mov_b64 v[40:41], 0\n"
    "v_mov_b64 v[42:43], 0\n"
    "v_mov_b64 v[44:45], 0\n"
    "v_mov_b64 v[46:47], 0\n"
    "v_mov_b64 v[48:49], 0\n"
    "v_mov_b64 v[50:51], 0\n"
    "v_mov_b64 v[52:53], 0\n"
    "v_mov_b64 v[54:55], 0\n"
    "v_mov_b64 v[56:57], 0\n"
    "v_mov_b64 v[58:59], 0\n"
    "v_mov_b64 v[60:61], 0\n"
    "v_mov_b64 v[62:63], 0\n"
    "s_waitcnt vmcnt(0)\n"
    "v_max3_u32 v3, v4, v5, v6\n"
    "v_max_u32 v3, v3, v7\n"
    "v_cmp_eq_u32 vcc, -1, v3\n"
    "s_cbranch_vccz .Lrec_start_%=\n"
    "s_sub_u32 s69, s69, 1\n"
    "s_cbranch_scc0 .Lrec_again0_%=\n"
    "s_mov_b32 s69, 0\n"
    "s_branch .Lrec_start_%=\n"
    ".Lrec_retry1_%=:\n"
    "s_cmp_eq_u32 s69, 0\n"
    "s_cbranch_scc1 .Lrec_back1_%=\n"
    "s_waitcnt lgkmcnt(0)\n"
    "ds_read_b128 v[72:75], v1 offset:0\n"
    "ds_read_b128 v[76:79], v1 offset:1024\n"
    ".Lrec_again1_%=:\n"
    "buffer_load_dwordx4 v[4:7], v0, s[36:39], s70 offen sc1\n"
    "buffer_load_dwordx4 v[8:11], v0, s[36:39], s70 offen offset:1024 sc1\n"
    "buffer_load_dwordx4 v[12:15], v0, s[36:39], s70 offen offset:2048 sc1\n"
    "buffer_load_dwordx4 v[16:19], v0, s[36:39], s70 offen offset:3072 sc1\n"
    "s_nop 7\n"
    "v_mov_b64 v[40:41], 0\n"
    "v_mov_b64 v[42:43], 0\n"
    "v_mov_b64 v[44:45], 0\n"
    "v_mov_b64 v[46:47], 0\n"
    "v_mov_b64 v[48:49], 0\n"
    "v_mov_b64 v[50:51], 0\n"
    "v_mov_b64 v[52:53], 0\n"
    "v_mov_b64 v[54:55], 0\n"
    "v_mov_b64 v[56:57], 0\n"
    "v_mov_b64 v[58:59], 0\n"
    "v_mov_b64 v[60:61], 0\n"
    "v_mov_b64 v[62:63], 0\n"
    "s_waitcnt vmcnt(0)\n"
    "v_max3_u32 v3, v4, v5, v6\n"
    "v_max3_u32 v3, v3, v7, v8\n"
    "v_max3_u32 v3, v3, v9, v10\n"
    "v_max3_u32 v3, v3, v11, v12\n"
    "v_max3_u32 v3, v3, v13, v14\n"
    "v_max3_u32 v3, v3, v15, v16\n"
    "v_max3_u32 v3, v3, v17, v18\n"
    "v_max_u32 v3, v3, v19\n"
    "v_cmp_eq_u32 vcc, -1, v3\n"
    "s_cbranch_vccz .Lrec_start_%=\n"
    "s_sub_u32 s69, s69, 1\n"
    "s_cbranch_scc0 .Lrec_again1_%=\n"
    "s_mov_b32 s69, 0\n"
    "s_branch .Lrec_start_%=\n"
    ".Lrec_retry2_%=:\n"
    "s_cmp_eq_u32 s69, 0\n"
    "s_cbranch_scc1 .Lrec_back2_%=\n"
    "s_waitcnt lgkmcnt(0)\n"
    "ds_read_b128 v[72:75], v1 offset:0\n"
    "ds_read_b128 v[76:79], v1 offset:1024\n"
    ".Lrec_again2_%=:\n"
    "buffer_load_dwordx4 v[4:7], v0, s[36:39], s70 offen sc1\n"
    "buffer_load_dwordx4 v[8:11], v0, s[36:39], s70 offen offset:1024 sc1\n"
    "buffer_load_dwordx4 v[12:15], v0, s[36:39], s70 offen offset:2048 sc1\n"
    "buffer_load_dwordx4 v[16:19], v0, s[36:39], s70 offen offset:3072 sc1\n"
    "buffer_load_dwordx4 v[20:23], v0, s[36:39], s71 offen sc1\n"
    "buffer_load_dwordx4 v[24:27], v0, s[36:39], s71 offen offset:1024 sc1\n"
    "buffer_load_dwordx4 v[28:31], v0, s[36:39], s71 offen offset:2048 sc1\n"
    "buffer_load_dwordx4 v[32:35], v0, s[36:39], s71 offen offset:3072 sc1\n"
    "buffer_load_dwordx4 v[36:39], v0, s[36:39], s72 offen sc1\n"
    "s_nop 7\n"
    "v_mov_b64 v[40:41], 0\n"
    "v_mov_b64 v[42:43], 0\n"
    "v_mov_b64 v[44:45], 0\n"
    "v_mov_b64 v[46:47], 0\n"
    "v_mov_b64 v[48:49], 0\n"
    "v_mov_b64 v[50:51], 0\n"
    "v_mov_b64 v[52:53], 0\n"
    "v_mov_b64 v[54:55], 0\n"
    "v_mov_b64 v[56:57], 0\n"
    "v_mov_b64 v[58:59], 0\n"
    "v_mov_b64 v[60:61], 0\n"
    "v_mov_b64 v[62:63], 0\n"
    "s_waitcnt vmcnt(0)\n"
    "v_max3_u32 v3, v4, v5, v6\n"
    "v_max3_u32 v3, v3, v7, v8\n"
    "v_max3_u32 v3, v3, v9, v10\n"
    "v_max3_u32 v3, v3, v11, v12\n"
    "v_max3_u32 v3, v3, v13, v14\n"
    "v_max3_u32 v3, v3, v15, v16\n"
    "v_max3_u32 v3, v3, v17, v18\n"
    "v_max3_u32 v3, v3, v19, v20\n"
    "v_max3_u32 v3, v3, v21, v22\n"
    "v_max3_u32 v3, v3, v23, v24\n"
    "v_max3_u32 v3, v3, v25, v26\n"
    "v_max3_u32 v3, v3, v27, v28\n"
    "v_max3_u32 v3, v3, v29, v30\n"
    "v_max3_u32 v3, v3, v31, v32\n"
    "v_max3_u32 v3, v3, v33, v34\n"
    "v_max3_u32 v3, v3, v35, v36\n"
    "v_max3_u32 v3, v3, v37, v38\n"
    "v_max_u32 v3, v3, v39\n"
    "v_cmp_eq_u32 vcc, -1, v3\n"
    "s_cbranch_vccz .Lrec_start_%=\n"
    "s_sub_u32 s69, s69, 1\n"
    "s_cbranch_scc0 .Lrec_again2_%=\n"
    "s_mov_b32 s69, 0\n"
    "s_branch .Lrec_start_%=\n"
    ".Lrec_tsretry_%=:\n"
    "s_mov_b32 s98, 24\n"
    ".Lrec_tsretry1_%=:\n"
    "s_mov_b32 s95, s28\n"
    "s_sub_u32 s98, s98, 1\n"
    "s_cbranch_scc1 .Lrec_tsgo_%=\n"
    "s_sleep 2\n"
    "buffer_load_dwordx2 v[12:13], v11, s[36:39], s90 offen sc1\n"
    "s_waitcnt vmcnt(0)\n"
    "v_max_u32 v12, v12, v13\n"
    "s_nop 1\n"
    "v_max_u32_dpp v12, v12, v12 row_ror:8 row_mask:0xf bank_mask:0xf\n"
    "s_nop 1\n"
    "v_max_u32_dpp v12, v12, v12 row_ror:4 row_mask:0xf bank_mask:0xf\n"
    "s_nop 1\n"
    "v_max_u32_dpp v12, v12, v12 row_ror:2 row_mask:0xf bank_mask:0xf\n"
    "s_nop 1\n"
    "v_max_u32_dpp v12, v12, v12 row_ror:1 row_mask:0xf bank_mask:0xf\n"
    "s_nop 0\n"
    "v_readlane_b32 s95, v12, 0\n"
    "v_readlane_b32 s99, v12, 16\n"
    "v_readlane_b32 s30, v12, 32\n"
    "v_readlane_b32 s31, v12, 48\n"
    "s_max_u32 s95, s95, s99\n"
    "s_max_u32 s30, s30, s31\n"
    "s_max_u32 s95, s95, s30\n"
    "s_cmp_eq_u32 s95, -1\n"
    "s_cbranch_scc1 .Lrec_tsretry1_%=\n"
    "s_branch .Lrec_tsgo_%=\n"
    ".Lrec_rretry_%=:\n"
    "s_sub_u32 s69, s69, 1\n"
    "s_cbranch_scc1 .Lrec_rdead_%=\n"
    "s_nop 7\n"
    "buffer_load_dwordx4 v[4:7], v0, s[36:39], s78 offen sc1\n"
    "buffer_load_dwordx4 v[8:11], v0, s[36:39], s78 offen offset:1024 sc1\n"
    "buffer_load_dwordx4 v[12:15], v0, s[36:39], s78 offen offset:2048 sc1\n"
    "buffer_load_dwordx4 v[16:19], v0, s[36:39], s78 offen offset:3072 sc1\n"
    "buffer_load_dwordx4 v[20:23], v0, s[36:39], s88 offen sc1\n"
    "buffer_load_dwordx4 v[24:27], v0, s[36:39], s88 offen offset:1024 sc1\n"
    "buffer_load_dwordx4 v[28:31], v0, s[36:39], s88 offen offset:2048 sc1\n"
    "buffer_load_dwordx4 v[32:35], v0, s[36:39], s88 offen offset:3072 sc1\n"
    "s_waitcnt vmcnt(0)\n"
    "s_branch .Lrec_rsum_%=\n"
    ".Lrec_rdead_%=:\n"
    "s_mov_b32 s69, 0\n"
    "s_nop 7\n"
    "s_branch .Lrec_rgo_%=\n"
    ".Lrec_nored_%=:\n"
    "s_nop 15\n"
    "s_cmp_lt_i32 s64, 0\n"
    "s_cbranch_scc0 .Lrec_red_%=\n"
    "v_lshl_or_b32 v77, s68, 1, 1\n"
    "s_waitcnt vmcnt(3)\n"
    "s_mov_b64 exec, 1\n"
    "buffer_atomic_smax v77, off, s[36:39], s58 sc1\n"
    "s_mov_b64 exec, -1\n"
    "v_mov_b32 v3, -1\n"
    "v_subrev_u32 v1, 81920, v1\n"
    "s_sub_u32 s95, s52, s35\n"
    "s_mul_i32 s95, s48, s95\n"
    "s_lshr_b32 s95, s95, 10\n"
    "s_add_u32 s32, s32, s95\n"
    "s_add_u32 s68, s68, 1\n"
    "s_and_b32 s70, s68, 3\n"
    "s_lshl_b32 s70, s70, 17\n"
    "s_add_u32 s70, s70, s66\n"
    "s_add_u32 s71, s70, 4096\n"
    "s_add_u32 s72, s70, 16384\n"
    "s_sub_u32 s46, s33, s46\n"
    "s_sub_u32 s47, s28, s47\n"
    "v_cvt_f32_u32 v40, s46\n"
    "v_cvt_f32_u32 v41, s47\n"
    "v_rcp_f32 v41, v41\n"
    "s_mov_b32 s46, s33\n"
    "s_mov_b32 s47, s28\n"
    "v_mul_f32 v40, v40, v41\n"
    "v_mov_b32 v42, 0x40c80000\n"
    "v_med3_f32 v40, v40, 4.0, v42\n"
    "v_rcp_f32 v41, v40\n"
    "v_mul_f32 v40, 0x43800000, v40\n"
    "v_mul_f32 v41, 0x44b54000, v41\n"
    "v_cvt_u32_f32 v40, v40\n"
    "v_cvt_u32_f32 v41, v41\n"
    "v_readfirstlane_b32 s48, v40\n"
    "v_readfirstlane_b32 s49, v41\n"
    "v_mov_b64 v[40:41], 0\n"
    "v_mov_b64 v[42:43], 0\n"
    "v_mov_b64 v[44:45], 0\n"
    "v_mov_b64 v[46:47], 0\n"
    "v_mov_b64 v[48:49], 0\n"
    "v_mov_b64 v[50:51], 0\n"
    "v_mov_b64 v[52:53], 0\n"
    "v_mov_b64 v[54:55], 0\n"
    "v_mov_b64 v[56:57], 0\n"
    "v_mov_b64 v[58:59], 0\n"
    "v_mov_b64 v[60:61], 0\n"
    "v_mov_b64 v[62:63], 0\n"
    "s_mov_b64 exec, 1\n"
    "buffer_atomic_smax v3, off, s[36:39], s59 sc0 sc1\n"
    "s_mov_b64 exec, -1\n"
    "s_waitcnt vmcnt(0)\n"
    "s_branch .Lrec_b3_%=\n"
    ".Lrec_waitp_%=:\n"
    "v_mov_b32 v3, -1\n"
    "s_mov_b64 exec, 1\n"
    "buffer_atomic_smax v3, off, s[36:39], s59 sc0 sc1\n"
    "s_mov_b64 exec, -1\n"
    "s_waitcnt vmcnt(0)\n"
    "v_readfirstlane_b32 s81, v3\n"
    "s_nop 0\n"
    "s_cmp_gt_i32 s81, s73\n"
    "s_cbranch_scc1 .Lrec_waitp1_%=\n"
    "s_sleep 1\n"
    "s_sub_u32 s69, s69, 1\n"
    "s_cbranch_scc0 .Lrec_waitp_%=\n"
    "s_mov_b32 s69, 0\n"
    ".Lrec_waitp1_%=:\n"
    "s_memtime s[96:97]\n"
    "s_waitcnt lgkmcnt(0)\n"
    "s_sub_u32 s95, s32, s96\n"
    "s_mul_i32 s95, s95, 43\n"
    "s_ashr_i32 s95, s95, 10\n"
    "s_max_i32 s95, s95, 0\n"
    "s_min_i32 s95, s95, 100\n"
    ".Lrec_gloop2_%=:\n"
    "s_sub_u32 s95, s95, 1\n"
    "s_cbranch_scc0 .Lrec_gloop2_%=\n"
    "s_branch .Lrec_top_%=\n"
    ".Lrec_FIN_%=:\n"
  );
}

typedef unsigned long long u64;
DEVI u64 ld8u(rsrc_t r, u32 voff) { v2u v = ld8(r, voff, 0); asm volatile("" : "+v"(v)); return (u64)v.x | ((u64)v.y << 32); }
DEVI u32 find_shift(rsrc_t rw, bool decide, u64 tag) {
  const int lane = threadIdx.x & 63;
  unsigned budget = SPIN_LIMIT;
  if (decide) {
    u32 best[2] = {~0u, ~0u};
    const u32 probe = 126 * CHUNK + 8192;
#pragma unroll 1
    for (int r = 0; r < 5; ++r) {
      const u32 a = lane == 0 ? probe + (u32)(r & 1) * 8192 : OOB;
      u32 v = 0;
      const unsigned long long t0 = __builtin_amdgcn_s_memtime();
      asm volatile("s_waitcnt lgkmcnt(0)\n\tbuffer_atomic_or %0, %1, %2, 0 offen sc0 sc1\n\ts_waitcnt vmcnt(0)" : "+v"(v) : "v"(a), "s"(rw) : "memory");
      const u32 dt = (u32)(__builtin_amdgcn_s_memtime() - t0);
      if (r & 1) best[1] = min(best[1], dt); else best[0] = min(best[0], dt);
    }
    for (;;) {
      v2u v[4];
      const u32 a = READY + (u32)lane * 8;
#pragma unroll
      for (int k = 0; k < 4; ++k) v[k] = (v2u){0u, 0u};
      asm volatile("buffer_atomic_or_x2 %0, %4, %5, 0 offen sc0 sc1\n\tbuffer_atomic_or_x2 %1, %4, %5, 0 offen offset:512 sc0 sc1\n\t"
                   "buffer_atomic_or_x2 %2, %4, %5, 0 offen offset:1024 sc0 sc1\n\tbuffer_atomic_or_x2 %3, %4, %5, 0 offen offset:1536 sc0 sc1\n\ts_waitcnt vmcnt(0)"
                   : "+v"(v[0]), "+v"(v[1]), "+v"(v[2]), "+v"(v[3]) : "v"(a), "s"(rw) : "memory");
      bool ok = true;
#pragma unroll
      for (int k = 0; k < 4; ++k) ok = ok & (v[k].x == (u32)tag) & (v[k].y == (u32)(tag >> 32));
      if (__all(ok) || budget == 0) break;
      --budget;
    }
    const u64 v = lane == 0 ? 2 * tag + (best[0] < best[1] ? 0u : 1u) : tag;
    st8(rw, (v2u){(u32)v, (u32)(v >> 32)}, lane == 0 ? PARW : (lane == 1 ? EPOCH : OOB), 0);
  }
  u64 v;
  for (;;) {
    cbar();
    v = ld8u(rw, PARW) ^ (2 * tag);
    if (__all(v < 2) || budget == 0) break;
    --budget;
    __builtin_amdgcn_s_sleep(4);
  }
  cbar();
  return (rfl((u32)v) & 1u) * 8192;
}
DEVI void hot_init(const Params& p, rsrc_t rw, int L, int n, u32 shift) {
  const int tid = threadIdx.x, w = (int)rfl(tid >> 6);
  const u32 hot = HOT + L * 8192 + shift, hot2 = HOT2 + (1 - L) * 8192 + shift;
  const v4u ones = {~0u, ~0u, ~0u, ~0u};
#pragma unroll
  for (int k = 0; k < 9; ++k) {
    st16(rw, ones, tid * 16, hot + (u32)(9 * n + k) * CHUNK);
    st16(rw, ones, tid * 16 + 4096, hot + (u32)(9 * n + k) * CHUNK);
  }
  {
    const u32 b = hot2 + (u32)(4 + n) * 2 * CHUNK;
    st16(rw, ones, tid * 16, b); st16(rw, ones, tid * 16 + 4096, b);
    st16(rw, ones, w == 0 ? (u32)tid * 16 : OOB, b + CHUNK);
  }
  if (n < 8) {
#pragma unroll
    for (int k = 0; k < 2; ++k) {
      st16(rw, ones, tid * 16, hot2 + (u32)(32 + 2 * n + k) * CHUNK);
      st16(rw, ones, tid * 16 + 4096, hot2 + (u32)(32 + 2 * n + k) * CHUNK);
    }
  }
  if (n >= 8) {
    const u32 b = hot2 + (u32)(n - 8) * 2 * CHUNK;
#pragma unroll
    for (int k = 0; k < 3; ++k) {
      const int pc = tid + 256 * k;
      if (pc < 576) {
        v4u v = *(const v4u*)(p.mem0 + (pc & 7) * 576 + (pc >> 3) * 8);
#pragma unroll
        for (int d = 0; d < 4; ++d) v[d] = min(v[d], 0xFFFFFFFEu);
        st16(rw, v, pc < 512 ? (u32)pc * 16 : CHUNK + (u32)(pc - 512) * 16, b);
      }
    }
  }
  drain();
  __syncthreads();
  if (tid == 0) st4(rw, 0u, INITF + L * 8192 + n * 128 + shift, 0);
}

DEVI void role_att(const Params& p, int L, int j, int a, u32 dbg, u32 shift) {
  const int tid = threadIdx.x, lane = tid & 63, w = (int)rfl(tid >> 6);
  const int c = lane & 15, g = lane >> 4;
  const int hl = 4 * a + w, h = 8 * j + hl;
  const v4u ws = make_srd(p.ws + shift, WS_END);
  v16u sa = cat4(ws, ws, ws, ws);
  v16u sb = {pga(L, B_QKV + 3 * h), pga(L, B_KVM + h), pgb(L, B_O + h), 0, dbg + w * 64,
             0, 0, 0, 0, 0, 0, 0, 0, 0, 0, 0};
  v16u va = {0, 0, 0, 0, 0, 0, 0, 0, 0, 0, 0, 0, 0, 0, 0, 0};
  va[0] = lane * 16;
  va[1] = (u32)(g * 128 + (c & 7) * 16);
  va[2] = lane < 3 ? mbox(L, j, SL_WO + lane, hl) : mbox(L, j, SL_P + hl, 48);
  va[4] = lane < 16 ? mbox_att(L, j, hl, lane) : OOB;
  va[5] = (u32)(lane < 4 ? 0 : (lane < 7 ? 2 * RO : ((lane >> 2) == 2 ? -2 : 1000)));
  va[6] = (u32)(c - 4 * g);
  va[7] = 0;
  RUN_ASM(
    "s_nop 4\n"
    "s_mov_b32 s20, 0\n"
    "s_mov_b32 s21, 0x80000000\n"
    "s_getpc_b64 s[84:85]\n"
    ".Latt_PC_%=:\n"
    "s_and_b32 s85, s85, 0xffff\n"
    "s_mov_b32 s86, .Latt_FIN_%=-.Latt_PC_%=\n"
    "s_mov_b32 s87, 0x20000\n"
    "v_mbcnt_lo_u32_b32 v240, -1, 0\n"
    "v_mbcnt_hi_u32_b32 v240, -1, v240\n"
    "v_lshlrev_b32 v240, 7, v240\n"
    "buffer_load_dword v241, v240, s[84:87], 0 offen\n"
    "v_add_u32 v240, 8192, v240\n"
    "buffer_load_dword v241, v240, s[84:87], 0 offen\n"
    "v_cmp_gt_i32 s[10:11], 0, v6\n"
    "v_cmp_gt_i32 s[12:13], 1, v6\n"
    "v_cmp_gt_i32 s[14:15], 2, v6\n"
    "v_cmp_gt_i32 s[16:17], 3, v6\n"
    "v_mov_b32 v10, 0xff61b1e6\n"
    "s_mov_b32 s18, 0x3e0293ee\n"
    "s_mov_b32 s68, 0\n"
    "s_mov_b32 s77, 1\n"
    "v_mov_b32 v8, -1\n"
    "v_mov_b32 v12, -1\n"
    "s_mov_b32 s69, 262144\n"
    "s_branch .Latt_waitp_%=\n"
    ".Latt_LOOPB_%=:\n"
    ".Latt_top_%=:\n"
    "s_waitcnt vmcnt(15)\n"
    "v_mfma_f32_16x16x32_bf16 v[160:163], v[16:19], v[48:51], 0\n"
    "v_mfma_f32_16x16x32_bf16 v[164:167], v[16:19], v[52:55], 0\n"
    "v_mfma_f32_16x16x32_bf16 v[168:171], v[20:23], v[52:55], 0\n"
    "s_waitcnt vmcnt(12)\n"
    "v_mfma_f32_16x16x32_bf16 v[160:163], v[24:27], v[56:59], v[160:163]\n"
    "v_mfma_f32_16x16x32_bf16 v[164:167], v[24:27], v[60:63], v[164:167]\n"
    "v_mfma_f32_16x16x32_bf16 v[168:171], v[28:31], v[60:63], v[168:171]\n"
    "s_waitcnt vmcnt(10)\n"
    "v_mfma_f32_16x16x32_bf16 v[160:163], v[32:35], v[64:67], v[160:163]\n"
    "v_mfma_f32_16x16x32_bf16 v[164:167], v[32:35], v[68:71], v[164:167]\n"
    "v_mfma_f32_16x16x32_bf16 v[168:171], v[36:39], v[68:71], v[168:171]\n"
    "s_waitcnt vmcnt(8)\n"
    "v_mfma_f32_16x16x32_bf16 v[160:163], v[40:43], v[72:75], v[160:163]\n"
    "v_mfma_f32_16x16x32_bf16 v[164:167], v[40:43], v[76:79], v[164:167]\n"
    "v_mfma_f32_16x16x32_bf16 v[168:171], v[44:47], v[76:79], v[168:171]\n"
    "v_add_u32 v11, v8, v5\n"
    "v_cmp_gt_i32 vcc, s77, v11\n"
    "s_nop 0\n"
    "s_or_b64 exec, vcc, s[20:21]\n"
    "buffer_atomic_smax v8, v4, s[36:39], 0 offen sc0 sc1\n"
    "s_mov_b64 exec, -1\n"
    "s_add_u32 s72, s68, 1\n"
    "s_and_b32 s4, s72, 7\n"
    "s_mul_i32 s4, s4, 4194304\n"
    "s_add_u32 s4, s4, s52\n"
    "s_add_u32 s5, s4, 4096\n"
    "s_add_u32 s6, s4, 16384\n"
    "s_add_u32 s7, s4, 20480\n"
    "s_add_u32 s8, s4, 32768\n"
    "s_add_u32 s9, s4, 36864\n"
    "s_and_b32 s73, s68, 7\n"
    "s_mul_i32 s73, s73, 4194304\n"
    "s_add_u32 s73, s73, s53\n"
    "s_add_u32 s74, s73, 3072\n"
    "s_and_b32 s75, s68, 7\n"
    "s_lshl_b32 s75, s75, 22\n"
    "s_add_u32 s75, s75, s54\n"
    "s_add_u32 s76, s75, 4096\n"
    "v_cndmask_b32 v160, v160, v10, s[10:11]\n"
    "v_cndmask_b32 v161, v161, v10, s[12:13]\n"
    "v_cndmask_b32 v162, v162, v10, s[14:15]\n"
    "v_cndmask_b32 v163, v163, v10, s[16:17]\n"
    "v_cndmask_b32 v168, v168, v10, s[10:11]\n"
    "v_cndmask_b32 v169, v169, v10, s[12:13]\n"
    "v_cndmask_b32 v170, v170, v10, s[14:15]\n"
    "v_cndmask_b32 v171, v171, v10, s[16:17]\n"
    "v_max3_f32 v224, v160, v161, v162\n"
    "v_max3_f32 v226, v164, v165, v166\n"
    "v_max_f32 v224, v224, v163\n"
    "v_max3_f32 v226, v226, v167, v168\n"
    "v_max3_f32 v226, v226, v169, v170\n"
    "v_max_f32 v226, v226, v171\n"
    "v_mov_b32 v238, v224\n"
    "v_mov_b32 v239, v226\n"
    "s_nop 0\n"
    "v_permlane16_swap_b32 v224, v238\n"
    "v_permlane16_swap_b32 v226, v239\n"
    "v_max_f32 v224, v224, v238\n"
    "v_max_f32 v226, v226, v239\n"
    "v_mov_b32 v238, v224\n"
    "v_mov_b32 v239, v226\n"
    "s_nop 0\n"
    "v_permlane32_swap_b32 v224, v238\n"
    "v_permlane32_swap_b32 v226, v239\n"
    "v_max_f32 v224, v224, v238\n"
    "v_max_f32 v226, v226, v239\n"
    "v_mul_f32 v230, 0xbe0293ee, v224\n"
    "v_mul_f32 v232, 0xbe0293ee, v226\n"
    "v_pk_fma_f32 v[160:161], v[160:161], s[18:19], v[230:231] op_sel_hi:[1,0,0]\n"
    "v_pk_fma_f32 v[162:163], v[162:163], s[18:19], v[230:231] op_sel_hi:[1,0,0]\n"
    "v_pk_fma_f32 v[164:165], v[164:165], s[18:19], v[232:233] op_sel_hi:[1,0,0]\n"
    "v_pk_fma_f32 v[166:167], v[166:167], s[18:19], v[232:233] op_sel_hi:[1,0,0]\n"
    "v_pk_fma_f32 v[168:169], v[168:169], s[18:19], v[232:233] op_sel_hi:[1,0,0]\n"
    "v_pk_fma_f32 v[170:171], v[170:171], s[18:19], v[232:233] op_sel_hi:[1,0,0]\n"
    "v_exp_f32 v160, v160\n"
    "v_exp_f32 v161, v161\n"
    "v_exp_f32 v162, v162\n"
    "v_exp_f32 v163, v163\n"
    "v_exp_f32 v164, v164\n"
    "v_exp_f32 v165, v165\n"
    "v_exp_f32 v166, v166\n"
    "v_exp_f32 v167, v167\n"
    "v_exp_f32 v168, v168\n"
    "v_exp_f32 v169, v169\n"
    "v_exp_f32 v170, v170\n"
    "v_exp_f32 v171, v171\n"
    "v_pk_add_f32 v[230:231], v[160:161], v[162:163]\n"
    "v_pk_add_f32 v[232:233], v[164:165], v[166:167]\n"
    "v_pk_add_f32 v[234:235], v[168:169], v[170:171]\n"
    "v_pk_add_f32 v[232:233], v[232:233], v[234:235]\n"
    "v_add_f32 v228, v230, v231\n"
    "v_add_f32 v229, v232, v233\n"
    "v_mov_b32 v238, v228\n"
    "v_mov_b32 v239, v229\n"
    "s_nop 0\n"
    "v_permlane16_swap_b32 v228, v238\n"
    "v_permlane16_swap_b32 v229, v239\n"
    "v_add_f32 v228, v228, v238\n"
    "v_add_f32 v229, v229, v239\n"
    "v_mov_b32 v238, v228\n"
    "v_mov_b32 v239, v229\n"
    "s_nop 0\n"
    "v_permlane32_swap_b32 v228, v238\n"
    "v_permlane32_swap_b32 v229, v239\n"
    "v_add_f32 v228, v228, v238\n"
    "v_add_f32 v229, v229, v239\n"
    "v_add_u32 v11, v12, v5\n"
    "v_cmp_gt_i32 vcc, s77, v11\n"
    "s_nop 0\n"
    "s_or_b64 exec, vcc, s[20:21]\n"
    "buffer_atomic_smax v12, v4, s[36:39], 0 offen sc0 sc1\n"
    "s_mov_b64 exec, -1\n"
    "s_waitcnt vmcnt(1)\n"
    "v_add_u32 v11, v8, v5\n"
    "v_cmp_gt_i32 vcc, s77, v11\n"
    "s_nop 0\n"
    "s_and_b32 s71, vcc_lo, 0xff\n"
    "s_cbranch_scc1 .Latt_waitr_%=\n"
    ".Latt_gor_%=:\n"
    "buffer_load_dwordx4 v[128:131], v1, s[36:39], s73 offen sc1\n"
    "buffer_load_dwordx4 v[132:135], v1, s[36:39], s73 offen offset:1536 sc1\n"
    "buffer_load_dwordx4 v[136:139], v1, s[36:39], s74 offen sc1\n"
    "buffer_load_dwordx4 v[140:143], v1, s[36:39], s74 offen offset:1536 sc1\n"
    "buffer_load_dwordx4 v[144:147], v0, s[36:39], s73 offen offset:512 sc1\n"
    "buffer_load_dwordx4 v[148:151], v0, s[36:39], s73 offen offset:2048 sc1\n"
    "buffer_load_dwordx4 v[152:155], v0, s[36:39], s74 offen offset:512 sc1\n"
    "buffer_load_dwordx4 v[156:159], v0, s[36:39], s74 offen offset:2048 sc1\n"
    "s_and_b32 s71, vcc_lo, 0xf00\n"
    "s_cselect_b32 s70, 0, 1\n"
    "s_cselect_b32 s71, 0x80000000, 0\n"
    "v_or_b32 v7, s71, v0\n"
    "s_nop 15\n"
    "buffer_load_dwordx4 v[16:19], v7, s[36:39], s6 offen sc1\n"
    "s_nop 15\n"
    "buffer_load_dwordx4 v[20:23], v7, s[36:39], s6 offen offset:1024 sc1\n"
    "s_waitcnt vmcnt(9)\n"
    "v_mfma_f32_16x16x32_bf16 v[172:175], v[128:131], v[48:51], 0\n"
    "v_mfma_f32_16x16x32_bf16 v[176:179], v[128:131], v[52:55], 0\n"
    "s_waitcnt vmcnt(8)\n"
    "v_mfma_f32_16x16x32_bf16 v[172:175], v[132:135], v[56:59], v[172:175]\n"
    "v_mfma_f32_16x16x32_bf16 v[176:179], v[132:135], v[60:63], v[176:179]\n"
    "s_waitcnt vmcnt(7)\n"
    "v_mfma_f32_16x16x32_bf16 v[172:175], v[136:139], v[64:67], v[172:175]\n"
    "v_mfma_f32_16x16x32_bf16 v[176:179], v[136:139], v[68:71], v[176:179]\n"
    "s_waitcnt vmcnt(6)\n"
    "v_mfma_f32_16x16x32_bf16 v[172:175], v[140:143], v[72:75], v[172:175]\n"
    "v_mfma_f32_16x16x32_bf16 v[176:179], v[140:143], v[76:79], v[176:179]\n"
    "s_waitcnt vmcnt(2)\n"
    "v_mov_b64 v[80:81], v[144:145]\n"
    "v_mov_b64 v[86:87], v[146:147]\n"
    "v_mov_b64 v[92:93], v[148:149]\n"
    "v_mov_b64 v[98:99], v[150:151]\n"
    "v_mov_b64 v[104:105], v[152:153]\n"
    "v_mov_b64 v[110:111], v[154:155]\n"
    "v_mov_b64 v[116:117], v[156:157]\n"
    "v_mov_b64 v[122:123], v[158:159]\n"
    "v_max3_f32 v230, v172, v173, v174\n"
    "v_max3_f32 v232, v176, v177, v178\n"
    "v_max3_f32 v230, v230, v175, v224\n"
    "v_max3_f32 v232, v232, v179, v226\n"
    "v_mov_b32 v238, v230\n"
    "v_mov_b32 v239, v232\n"
    "s_nop 0\n"
    "v_permlane16_swap_b32 v230, v238\n"
    "v_permlane16_swap_b32 v232, v239\n"
    "v_max_f32 v230, v230, v238\n"
    "v_max_f32 v232, v232, v239\n"
    "v_mul_f32 v230, 0xbe0293ee, v230\n"
    "v_mul_f32 v232, 0xbe0293ee, v232\n"
    "v_pk_fma_f32 v[172:173], v[172:173], s[18:19], v[230:231] op_sel_hi:[1,0,0]\n"
    "v_pk_fma_f32 v[174:175], v[174:175], s[18:19], v[230:231] op_sel_hi:[1,0,0]\n"
    "v_pk_fma_f32 v[176:177], v[176:177], s[18:19], v[232:233] op_sel_hi:[1,0,0]\n"
    "v_pk_fma_f32 v[178:179], v[178:179], s[18:19], v[232:233] op_sel_hi:[1,0,0]\n"
    "v_fma_f32 v224, v224, s18, v230\n"
    "v_fma_f32 v226, v226, s18, v232\n"
    "v_exp_f32 v172, v172\n"
    "v_exp_f32 v173, v173\n"
    "v_exp_f32 v174, v174\n"
    "v_exp_f32 v175, v175\n"
    "buffer_load_dwordx4 v[24:27], v7, s[36:39], s6 offen offset:2048 sc1\n"
    "v_exp_f32 v176, v176\n"
    "v_exp_f32 v177, v177\n"
    "v_exp_f32 v178, v178\n"
    "v_exp_f32 v179, v179\n"
    "buffer_load_dwordx4 v[28:31], v7, s[36:39], s6 offen offset:3072 sc1\n"
    "v_exp_f32 v224, v224\n"
    "v_exp_f32 v226, v226\n"
    "v_pk_add_f32 v[230:231], v[172:173], v[174:175]\n"
    "v_pk_add_f32 v[232:233], v[176:177], v[178:179]\n"
    "v_add_f32 v230, v230, v231\n"
    "v_add_f32 v232, v232, v233\n"
    "v_mov_b32 v238, v230\n"
    "v_mov_b32 v239, v232\n"
    "s_nop 0\n"
    "v_permlane16_swap_b32 v230, v238\n"
    "v_permlane16_swap_b32 v232, v239\n"
    "v_add_f32 v230, v230, v238\n"
    "v_add_f32 v232, v232, v239\n"
    "v_fmac_f32 v230, v228, v224\n"
    "v_fmac_f32 v232, v229, v226\n"
    "v_rcp_f32 v230, v230\n"
    "v_rcp_f32 v232, v232\n"
    "buffer_load_dwordx4 v[32:35], v7, s[36:39], s7 offen sc1\n"
    "v_mul_f32 v224, v224, v230\n"
    "v_mul_f32 v226, v226, v232\n"
    "v_pk_mul_f32 v[164:165], v[164:165], v[226:227] op_sel_hi:[1,0]\n"
    "v_pk_mul_f32 v[166:167], v[166:167], v[226:227] op_sel_hi:[1,0]\n"
    "v_pk_mul_f32 v[168:169], v[168:169], v[226:227] op_sel_hi:[1,0]\n"
    "v_pk_mul_f32 v[170:171], v[170:171], v[226:227] op_sel_hi:[1,0]\n"
    "v_pk_mul_f32 v[176:177], v[176:177], v[232:233] op_sel_hi:[1,0]\n"
    "v_pk_mul_f32 v[178:179], v[178:179], v[232:233] op_sel_hi:[1,0]\n"
    "v_pk_mul_f32 v[172:173], v[172:173], v[230:231] op_sel_hi:[1,0]\n"
    "v_pk_mul_f32 v[174:175], v[174:175], v[230:231] op_sel_hi:[1,0]\n"
    "v_pk_mul_f32 v[160:161], v[160:161], v[224:225] op_sel_hi:[1,0]\n"
    "v_pk_mul_f32 v[162:163], v[162:163], v[224:225] op_sel_hi:[1,0]\n"
    "v_cvt_pk_bf16_f32 v216, v164, v165\n"
    "v_cvt_pk_bf16_f32 v217, v166, v167\n"
    "v_cvt_pk_bf16_f32 v218, v168, v169\n"
    "v_cvt_pk_bf16_f32 v219, v170, v171\n"
    "v_cvt_pk_bf16_f32 v220, v176, v177\n"
    "v_cvt_pk_bf16_f32 v221, v178, v179\n"
    "v_cvt_pk_bf16_f32 v212, v172, v173\n"
    "v_cvt_pk_bf16_f32 v213, v174, v175\n"
    "v_cvt_pk_bf16_f32 v214, v160, v161\n"
    "v_cvt_pk_bf16_f32 v215, v162, v163\n"
    "v_mfma_f32_16x16x32_bf16 v[188:191], v[82:85], v[216:219], 0\n"
    "v_mfma_f32_16x16x32_bf16 v[192:195], v[88:91], v[216:219], 0\n"
    "v_mfma_f32_16x16x32_bf16 v[180:183], v[80:83], v[212:215], 0\n"
    "v_mfma_f32_16x16x32_bf16 v[184:187], v[86:89], v[212:215], 0\n"
    "v_mfma_f32_16x16x16_bf16 v[188:191], v[80:81], v[220:221], v[188:191]\n"
    "v_mfma_f32_16x16x16_bf16 v[192:195], v[86:87], v[220:221], v[192:195]\n"
    "v_mfma_f32_16x16x32_bf16 v[204:207], v[94:97], v[216:219], 0\n"
    "v_mfma_f32_16x16x32_bf16 v[208:211], v[100:103], v[216:219], 0\n"
    "v_cvt_pk_bf16_f32 v180, v180, v181\n"
    "v_cvt_pk_bf16_f32 v181, v182, v183\n"
    "v_mfma_f32_16x16x32_bf16 v[196:199], v[92:95], v[212:215], 0\n"
    "v_cvt_pk_bf16_f32 v182, v184, v185\n"
    "v_cvt_pk_bf16_f32 v183, v186, v187\n"
    "v_mfma_f32_16x16x32_bf16 v[200:203], v[98:101], v[212:215], 0\n"
    "buffer_store_dwordx4 v[180:183], v0, s[36:39], s75 offen sc1\n"
    "v_cvt_pk_bf16_f32 v188, v188, v189\n"
    "v_mfma_f32_16x16x16_bf16 v[204:207], v[92:93], v[220:221], v[204:207]\n"
    "v_cvt_pk_bf16_f32 v189, v190, v191\n"
    "v_cvt_pk_bf16_f32 v190, v192, v193\n"
    "v_mfma_f32_16x16x16_bf16 v[208:211], v[98:99], v[220:221], v[208:211]\n"
    "v_cvt_pk_bf16_f32 v191, v194, v195\n"
    "buffer_store_dwordx4 v[188:191], v0, s[36:39], s75 offen offset:1024 sc1\n"
    "s_nop 1\n"
    "v_mfma_f32_16x16x32_bf16 v[188:191], v[106:109], v[216:219], 0\n"
    "v_mfma_f32_16x16x32_bf16 v[192:195], v[112:115], v[216:219], 0\n"
    "v_cvt_pk_bf16_f32 v196, v196, v197\n"
    "v_cvt_pk_bf16_f32 v197, v198, v199\n"
    "v_mfma_f32_16x16x32_bf16 v[180:183], v[104:107], v[212:215], 0\n"
    "v_cvt_pk_bf16_f32 v198, v200, v201\n"
    "v_cvt_pk_bf16_f32 v199, v202, v203\n"
    "v_mfma_f32_16x16x32_bf16 v[184:187], v[110:113], v[212:215], 0\n"
    "buffer_store_dwordx4 v[196:199], v0, s[36:39], s75 offen offset:2048 sc1\n"
    "v_cvt_pk_bf16_f32 v204, v204, v205\n"
    "v_mfma_f32_16x16x16_bf16 v[188:191], v[104:105], v[220:221], v[188:191]\n"
    "v_cvt_pk_bf16_f32 v205, v206, v207\n"
    "v_cvt_pk_bf16_f32 v206, v208, v209\n"
    "v_mfma_f32_16x16x16_bf16 v[192:195], v[110:111], v[220:221], v[192:195]\n"
    "v_cvt_pk_bf16_f32 v207, v210, v211\n"
    "buffer_store_dwordx4 v[204:207], v0, s[36:39], s75 offen offset:3072 sc1\n"
    "s_nop 1\n"
    "v_mfma_f32_16x16x32_bf16 v[204:207], v[118:121], v[216:219], 0\n"
    "v_mfma_f32_16x16x32_bf16 v[208:211], v[124:127], v[216:219], 0\n"
    "v_cvt_pk_bf16_f32 v180, v180, v181\n"
    "v_cvt_pk_bf16_f32 v181, v182, v183\n"
    "v_mfma_f32_16x16x32_bf16 v[196:199], v[116:119], v[212:215], 0\n"
    "v_cvt_pk_bf16_f32 v182, v184, v185\n"
    "v_cvt_pk_bf16_f32 v183, v186, v187\n"
    "v_mfma_f32_16x16x32_bf16 v[200:203], v[122:125], v[212:215], 0\n"
    "buffer_store_dwordx4 v[180:183], v0, s[36:39], s76 offen sc1\n"
    "v_cvt_pk_bf16_f32 v188, v188, v189\n"
    "v_mfma_f32_16x16x16_bf16 v[204:207], v[116:117], v[220:221], v[204:207]\n"
    "v_cvt_pk_bf16_f32 v189, v190, v191\n"
    "v_cvt_pk_bf16_f32 v190, v192, v193\n"
    "v_mfma_f32_16x16x16_bf16 v[208:211], v[122:123], v[220:221], v[208:211]\n"
    "v_cvt_pk_bf16_f32 v191, v194, v195\n"
    "buffer_store_dwordx4 v[188:191], v0, s[36:39], s76 offen offset:1024 sc1\n"
    "v_cvt_pk_bf16_f32 v196, v196, v197\n"
    "v_cvt_pk_bf16_f32 v197, v198, v199\n"
    "v_cvt_pk_bf16_f32 v198, v200, v201\n"
    "v_cvt_pk_bf16_f32 v199, v202, v203\n"
    "buffer_store_dwordx4 v[196:199], v0, s[36:39], s76 offen offset:2048 sc1\n"
    "v_cvt_pk_bf16_f32 v204, v204, v205\n"
    "v_cvt_pk_bf16_f32 v205, v206, v207\n"
    "v_cvt_pk_bf16_f32 v206, v208, v209\n"
    "v_cvt_pk_bf16_f32 v207, v210, v211\n"
    "buffer_store_dwordx4 v[204:207], v0, s[36:39], s76 offen offset:3072 sc1\n"
    "s_nop 13\n"
    "buffer_load_dwordx4 v[36:39], v7, s[36:39], s7 offen offset:1024 sc1\n"
    "s_nop 13\n"
    "buffer_load_dwordx4 v[40:43], v7, s[36:39], s7 offen offset:2048 sc1\n"
    "s_nop 13\n"
    "buffer_load_dwordx4 v[44:47], v7, s[36:39], s7 offen offset:3072 sc1\n"
    "s_nop 13\n"
    "buffer_load_dwordx4 v[48:51], v7, s[36:39], s4 offen sc1\n"
    "s_nop 13\n"
    "buffer_load_dwordx4 v[52:55], v7, s[36:39], s4 offen offset:1024 sc1\n"
    "v_mov_b32 v9, s77\n"
    "s_waitcnt vmcnt(5)\n"
    "s_mov_b64 exec, 15\n"
    "buffer_atomic_smax v9, v2, s[36:39], 0 offen sc1\n"
    "s_mov_b64 exec, -1\n"
    "buffer_load_dwordx4 v[56:59], v7, s[36:39], s4 offen offset:2048 sc1\n"
    "buffer_load_dwordx4 v[60:63], v7, s[36:39], s4 offen offset:3072 sc1\n"
    "buffer_load_dwordx4 v[64:67], v7, s[36:39], s5 offen sc1\n"
    "buffer_load_dwordx4 v[68:71], v7, s[36:39], s5 offen offset:1024 sc1\n"
    "buffer_load_dwordx4 v[72:75], v7, s[36:39], s5 offen offset:2048 sc1\n"
    "buffer_load_dwordx4 v[76:79], v7, s[36:39], s5 offen offset:3072 sc1\n"
    "buffer_load_dwordx4 v[82:85], v7, s[36:39], s8 offen sc1\n"
    "buffer_load_dwordx4 v[88:91], v7, s[36:39], s8 offen offset:1024 sc1\n"
    "buffer_load_dwordx4 v[94:97], v7, s[36:39], s8 offen offset:2048 sc1\n"
    "buffer_load_dwordx4 v[100:103], v7, s[36:39], s8 offen offset:3072 sc1\n"
    "buffer_load_dwordx4 v[106:109], v7, s[36:39], s9 offen sc1\n"
    "buffer_load_dwordx4 v[112:115], v7, s[36:39], s9 offen offset:1024 sc1\n"
    "buffer_load_dwordx4 v[118:121], v7, s[36:39], s9 offen offset:2048 sc1\n"
    "buffer_load_dwordx4 v[124:127], v7, s[36:39], s9 offen offset:3072 sc1\n"
    "s_add_u32 s68, s68, 1\n"
    "s_add_u32 s77, s77, 2\n"
    "s_cmp_lg_u32 s70, 0\n"
    "s_cbranch_scc1 .Latt_top_%=\n"
    ".Latt_LOOPE_%=:\n"
    "s_cmp_lg_u32 s68, 64\n"
    "s_cbranch_scc1 .Latt_waitp_%=\n"
    "s_waitcnt vmcnt(0)\n"
    "s_branch .Latt_FIN_%=\n"
    ".Latt_waitp_%=:\n"
    "s_sub_u32 s72, s77, 2\n"
    "v_add_u32 v11, v8, v5\n"
    "v_cmp_gt_i32 vcc, s72, v11\n"
    "s_nop 0\n"
    "s_or_b64 exec, vcc, s[20:21]\n"
    "buffer_atomic_smax v8, v4, s[36:39], 0 offen sc0 sc1\n"
    "s_mov_b64 exec, -1\n"
    "s_waitcnt vmcnt(0)\n"
    "v_add_u32 v11, v8, v5\n"
    "v_cmp_gt_i32 vcc, s72, v11\n"
    "s_cbranch_vccz .Latt_gop_%=\n"
    "s_sleep 3\n"
    "s_sub_u32 s69, s69, 1\n"
    "s_cbranch_scc0 .Latt_waitp_%=\n"
    "s_mov_b32 s69, 0\n"
    ".Latt_gop_%=:\n"
    "s_and_b32 s4, s68, 7\n"
    "s_mul_i32 s4, s4, 4194304\n"
    "s_add_u32 s4, s4, s52\n"
    "s_add_u32 s5, s4, 4096\n"
    "s_add_u32 s6, s4, 16384\n"
    "s_add_u32 s7, s4, 20480\n"
    "s_add_u32 s8, s4, 32768\n"
    "s_add_u32 s9, s4, 36864\n"
    "buffer_load_dwordx4 v[16:19], v0, s[36:39], s6 offen sc1\n"
    "buffer_load_dwordx4 v[20:23], v0, s[36:39], s6 offen offset:1024 sc1\n"
    "buffer_load_dwordx4 v[24:27], v0, s[36:39], s6 offen offset:2048 sc1\n"
    "buffer_load_dwordx4 v[28:31], v0, s[36:39], s6 offen offset:3072 sc1\n"
    "buffer_load_dwordx4 v[32:35], v0, s[36:39], s7 offen sc1\n"
    "buffer_load_dwordx4 v[36:39], v0, s[36:39], s7 offen offset:1024 sc1\n"
    "buffer_load_dwordx4 v[40:43], v0, s[36:39], s7 offen offset:2048 sc1\n"
    "buffer_load_dwordx4 v[44:47], v0, s[36:39], s7 offen offset:3072 sc1\n"
    "buffer_load_dwordx4 v[48:51], v0, s[36:39], s4 offen sc1\n"
    "buffer_load_dwordx4 v[52:55], v0, s[36:39], s4 offen offset:1024 sc1\n"
    "buffer_load_dwordx4 v[56:59], v0, s[36:39], s4 offen offset:2048 sc1\n"
    "buffer_load_dwordx4 v[60:63], v0, s[36:39], s4 offen offset:3072 sc1\n"
    "buffer_load_dwordx4 v[64:67], v0, s[36:39], s5 offen sc1\n"
    "buffer_load_dwordx4 v[68:71], v0, s[36:39], s5 offen offset:1024 sc1\n"
    "buffer_load_dwordx4 v[72:75], v0, s[36:39], s5 offen offset:2048 sc1\n"
    "buffer_load_dwordx4 v[76:79], v0, s[36:39], s5 offen offset:3072 sc1\n"
    "buffer_load_dwordx4 v[82:85], v0, s[36:39], s8 offen sc1\n"
    "buffer_load_dwordx4 v[88:91], v0, s[36:39], s8 offen offset:1024 sc1\n"
    "buffer_load_dwordx4 v[94:97], v0, s[36:39], s8 offen offset:2048 sc1\n"
    "buffer_load_dwordx4 v[100:103], v0, s[36:39], s8 offen offset:3072 sc1\n"
    "buffer_load_dwordx4 v[106:109], v0, s[36:39], s9 offen sc1\n"
    "buffer_load_dwordx4 v[112:115], v0, s[36:39], s9 offen offset:1024 sc1\n"
    "buffer_load_dwordx4 v[118:121], v0, s[36:39], s9 offen offset:2048 sc1\n"
    "buffer_load_dwordx4 v[124:127], v0, s[36:39], s9 offen offset:3072 sc1\n"
    "s_waitcnt vmcnt(8)\n"
    "s_branch .Latt_top_%=\n"
    ".Latt_waitr_%=:\n"
    "s_or_b64 exec, vcc, s[20:21]\n"
    "buffer_atomic_smax v8, v4, s[36:39], 0 offen sc0 sc1\n"
    "s_mov_b64 exec, -1\n"
    "s_sleep 3\n"
    "s_waitcnt vmcnt(1)\n"
    "v_add_u32 v11, v12, v5\n"
    "v_cmp_gt_i32 vcc, s77, v11\n"
    "s_nop 0\n"
    "s_and_b32 s71, vcc_lo, 0xff\n"
    "s_cbranch_scc0 .Latt_gor_%=\n"
    "s_or_b64 exec, vcc, s[20:21]\n"
    "buffer_atomic_smax v12, v4, s[36:39], 0 offen sc0 sc1\n"
    "s_mov_b64 exec, -1\n"
    "s_sleep 3\n"
    "s_waitcnt vmcnt(1)\n"
    "v_add_u32 v11, v8, v5\n"
    "v_cmp_gt_i32 vcc, s77, v11\n"
    "s_nop 0\n"
    "s_and_b32 s71, vcc_lo, 0xff\n"
    "s_cbranch_scc0 .Latt_gor_%=\n"
    "s_sub_u32 s69, s69, 1\n"
    "s_cbranch_scc0 .Latt_waitr_%=\n"
    ".Latt_deadr_%=:\n"
    "s_mov_b32 s69, 0\n"
    "s_branch .Latt_gor_%=\n"
    ".Latt_FIN_%=:\n"
  );
}

template <int MODE>
DEVI void red_args(const Params& p, int L, int q, u32 dbg, u64 tag, v16u& sa, v16u& sb, v16u& va, v16u& vb) {
  const int tid = threadIdx.x, lane = tid & 63, w = (int)rfl(tid >> 6);
  const int c = lane & 15, g = lane >> 4;
  const bool rowres = MODE == 0 || (MODE == 1 && L == 0);
  const bool fin = MODE == 2 && L == 1;
  const v4u ws = make_srd(p.ws, WS_END), rx = make_srd(p.x, 2048 * 1152), ro = make_srd(p.out, 2048 * 1152);
  u32 ft = OOB;
  if (lane < 32) {
    if (MODE == 1) { if (lane < 24) ft = mbox(L, lane / 6, SL_UP + lane % 6, 4 * q + w); }
    else if (!fin) ft = mbox(MODE == 0 ? 0 : 1, lane >> 3, SL_P + (lane & 7), (MODE == 0 ? 16 * L : 0) + 4 * q + w);
  } else if (MODE != 0) {
    if (lane < 44) ft = mbox(L, (lane - 32) / 3, (MODE == 1 ? SL_WO : SL_DOWN) + (lane - 32) % 3, (MODE == 1 ? 8 : 24) + 4 * q + w);
    else if (lane == 44 && !rowres) ft = MODE == 1 ? mbox(0, q, SL_REDF, 48 + w) : mbox(L, q, SL_REDA, 48 + w);
  }
  const unsigned long long fm = __ballot(ft != OOB);
  sa = cat4(ws, rowres ? rx : ws, fin ? ro : ws, (v4u){(u32)(2 * tag), (u32)((2 * tag) >> 32), 0u, 0u});
  sb = (v16u){pgb(L, MODE == 1 ? B_PA : B_PF),
              rowres ? 0u : (MODE == 1 ? pgb(1, B_X) : pgb(L, B_X1)), rowres ? 36864u : SLOT, (u32)(rowres ? 63 : (MODE == 1 ? RX2 - 1 : RX - 1)), rowres ? 128u : 1024u,
              MODE == 1 ? pgb(L, B_X1) : pgb(1, B_X), (u32)((MODE == 0 || fin) ? 63 : (MODE == 1 ? RX - 1 : RX2 - 1)),
              fin ? 0u : (MODE == 0 ? OFF_XN0 : (MODE == 1 ? pgb(L, B_XN2) : pgb(1, B_XN))), 4096u,
              rowres ? 0u : ~0u, dbg + w * 64, fin ? 0u : ~0u, (u32)fm, (u32)(fm >> 32), (u32)(L == 0 ? 0 : (L == 1 ? LN_B : LN_C)), (u32)(L == 0 ? LN_B - 1 : (L == 1 ? LN_C - 1 : 63))};
  const int need = w == 0 ? 0x10F : (w == 1 ? 0x23F : (w == 2 ? 0x4F0 : 0xF00));
  const bool in = MODE != 0 && lane < 48 && ((need >> (lane % 12)) & 1);
  const bool back = MODE != 0 && lane >= 48 && lane < 52 && !fin;
  auto frag = [&](int I) { return kb_off(2 * (I >> 1)) + (u32)((I & 1) * 512 + (q >> 1) * 1024 + (c >> 3) * 256 + (8 * (q & 1) + (c & 7)) * 16); };
  auto rowm = [&](int I) { return (u32)((8 * q + (c & 7)) * 1152 + (2 * I + (c >> 3)) * 16); };
  const int I0 = 8 * w + 2 * (g & 1) + (g >> 1);
  const u32 pv = (u32)(q * CHUNK + w * 2048 + lane * 16), pv8 = (u32)(4 * CHUNK + (4 * q + w) * 256 + lane * 16);
  va[0] = lane == 63 ? OOB : mbox(L, q, MODE == 1 ? SL_REDA : SL_REDF, lane);
  va[1] = (u32)(in ? -(q >> 1) : (back ? 2 * (MODE == 1 ? RX : RX2) : 1000));
  va[2] = (u32)g * 5 * CHUNK + frag(8 * w);
  va[3] = (u32)g * 5 * CHUNK + frag(32 + w);
  va[4] = fin ? (u32)(q * 9216 + tid * 16) : frag(I0);
  va[5] = fin ? (w == 0 ? (u32)(q * 9216 + 8192 + tid * 16) : OOB) : (g == 0 ? frag(32 + w) : OOB);
  va[6] = rowres ? rowm(I0) : pv;
  va[7] = g == 0 ? (rowres ? rowm(32 + w) : pv8) : OOB;
  va[8] = pv;
  va[9] = g == 0 ? pv8 : OOB;
  va[10] = fin ? rowm(I0) - q * 9216 : (u32)(((c & 7) * 4 + w) * 8);
  va[11] = fin ? (u32)tid * 16 : (u32)((c & 7) * 32);
  va[12] = fin ? (g == 0 ? rowm(32 + w) - q * 9216 : (u32)(12288 + tid * 16)) : (g < 2 ? 0x3F800000u : 0u);
  va[13] = ft; va[14] = 0; va[15] = 0;
  const u32 one = 0x3F80u << (16 * (c & 1));
#pragma unroll
  for (int d = 0; d < 4; ++d) {
    const bool on = ((c & 7) >> 1) == d;
    vb[d] = (on && c < 8) ? one : 0u;
    vb[4 + d] = (on && c >= 8) ? one : 0u;
    vb[8 + d] = (on && g == 2 * (c >> 3)) ? one : 0u;
    vb[12 + d] = (on && g == 2 * (c >> 3) + 1) ? one : 0u;
  }
#pragma unroll
  for (int i = 0; i < 16; ++i) { sa[i] = rfl(sa[i]); sb[i] = rfl(sb[i]); }
}
#define RUN_RED(TEXT) asm volatile(TEXT : "+{s[36:51]}"(sa), "+{s[52:67]}"(sb), "+{v[0:15]}"(va), "+{v[16:31]}"(vb) :: CLOB_RED)
#define CLOB_RED \
  "v32", "v33", "v34", "v35", "v36", "v37", "v38", "v39", "v40", "v41", "v42", "v43", "v44", "v45", "v46", "v47", "v48", "v49", "v50", "v51", "v52", "v53", "v54", "v55", \
  "v56", "v57", "v58", "v59", "v60", "v61", "v62", "v63", "v64", "v65", "v66", "v67", "v68", "v69", "v70", "v71", "v72", "v73", "v74", "v75", "v76", "v77", "v78", "v79", \
  "v80", "v81", "v82", "v83", "v84", "v85", "v86", "v87", "v88", "v89", "v90", "v91", "v92", "v93", "v94", "v95", "v96", "v97", "v98", "v99", "v100", "v101", "v102", "v103", \
  "v104", "v105", "v106", "v107", "v108", "v109", "v110", "v111", "v112", "v113", "v114", "v115", "v116", "v117", "v118", "v119", "v120", "v121", "v122", "v123", "v124", "v125", "v126", "v127", \
  "v128", "v129", "v130", "v131", "v132", "v133", "v134", "v135", "s68", "s69", "s70", "s71", "s72", "s73", "s74", "s75", "s76", "s77", "s78", "s79", "s80", "s81", "s82", "s83", \
  "s84", "s85", "s86", "s87", "s88", "s89", "s90", "s91", "s92", "s93", "s94", "s95", "s96", "s97", "s98", "s99", "vcc", "scc", "memory"
DEVI void role_ln0(const Params& p, int grp, int q, u32 dbg, u64 tag) {
  v16u sa, sb, va, vb;
  red_args<0>(p, grp, q, dbg, tag, sa, sb, va, vb);
  RUN_RED(
    "s_nop 4\n"
    "s_mov_b32 s50, 0\n"
    "s_mov_b32 s51, 0x80000000\n"
    "s_getpc_b64 s[84:85]\n"
    ".LrLN0_PC_%=:\n"
    "s_and_b32 s85, s85, 0xffff\n"
    "s_mov_b32 s86, .LrLN0_FIN_%=-.LrLN0_PC_%=\n"
    "s_mov_b32 s87, 0x20000\n"
    "v_mbcnt_lo_u32_b32 v134, -1, 0\n"
    "v_mbcnt_hi_u32_b32 v134, -1, v134\n"
    "v_lshlrev_b32 v134, 7, v134\n"
    "buffer_load_dword v135, v134, s[84:87], 0 offen\n"
    "v_add_u32 v134, 8192, v134\n"
    "buffer_load_dword v135, v134, s[84:87], 0 offen\n"
    "s_mov_b32 s69, 0\n"
    "v_mov_b32 v122, -1\n"
    "v_mov_b32 v123, -1\n"
    "s_mov_b32 s70, 262144\n"
    "s_add_u32 s69, s66, 0\n"
    "s_and_b32 s73, s69, s55\n"
    "s_mul_i32 s73, s73, s54\n"
    "s_add_u32 s73, s73, s53\n"
    "s_add_u32 s74, s73, s56\n"
    "buffer_load_dwordx4 v[40:43], v6, s[40:43], s73 offen sc1\n"
    "buffer_load_dwordx4 v[44:47], v6, s[40:43], s74 offen sc1\n"
    "buffer_load_dwordx4 v[48:51], v7, s[40:43], s73 offen sc1\n"
    "s_add_u32 s69, s66, 1\n"
    "s_and_b32 s73, s69, s55\n"
    "s_mul_i32 s73, s73, s54\n"
    "s_add_u32 s73, s73, s53\n"
    "s_add_u32 s74, s73, s56\n"
    "buffer_load_dwordx4 v[52:55], v6, s[40:43], s73 offen sc1\n"
    "buffer_load_dwordx4 v[56:59], v6, s[40:43], s74 offen sc1\n"
    "buffer_load_dwordx4 v[60:63], v7, s[40:43], s73 offen sc1\n"
    "s_add_u32 s69, s66, 2\n"
    "s_and_b32 s73, s69, s55\n"
    "s_mul_i32 s73, s73, s54\n"
    "s_add_u32 s73, s73, s53\n"
    "s_add_u32 s74, s73, s56\n"
    "buffer_load_dwordx4 v[68:71], v6, s[40:43], s73 offen sc1\n"
    "buffer_load_dwordx4 v[72:75], v6, s[40:43], s74 offen sc1\n"
    "buffer_load_dwordx4 v[76:79], v7, s[40:43], s73 offen sc1\n"
    "s_add_u32 s69, s66, 3\n"
    "s_and_b32 s73, s69, s55\n"
    "s_mul_i32 s73, s73, s54\n"
    "s_add_u32 s73, s73, s53\n"
    "s_add_u32 s74, s73, s56\n"
    "buffer_load_dwordx4 v[124:127], v6, s[40:43], s73 offen sc1\n"
    "buffer_load_dwordx4 v[128:131], v6, s[40:43], s74 offen sc1\n"
    "buffer_load_dwordx4 v[132:135], v7, s[40:43], s73 offen sc1\n"
    "s_sub_u32 s68, s66, 1\n"
    ".LrLN0_par_%=:\n"
    "buffer_load_dwordx2 v[14:15], off, s[36:39], 0 sc1\n"
    "s_waitcnt vmcnt(0)\n"
    "v_readfirstlane_b32 s75, v14\n"
    "v_readfirstlane_b32 s76, v15\n"
    "s_xor_b32 s75, s75, s48\n"
    "s_xor_b32 s76, s76, s49\n"
    "s_cselect_b32 s76, 2, 0\n"
    "s_or_b32 s76, s76, s75\n"
    "s_cmp_lt_u32 s76, 2\n"
    "s_cbranch_scc1 .LrLN0_par1_%=\n"
    "s_sleep 4\n"
    "s_sub_u32 s70, s70, 1\n"
    "s_cbranch_scc0 .LrLN0_par_%=\n"
    "s_mov_b32 s70, 0\n"
    ".LrLN0_par1_%=:\n"
    "s_and_b32 s75, s75, 1\n"
    "s_lshl_b32 s75, s75, 13\n"
    "s_add_u32 s36, s36, s75\n"
    "s_addc_u32 s37, s37, 0\n"
    "s_and_b32 s76, s75, s61\n"
    "s_add_u32 s40, s40, s76\n"
    "s_addc_u32 s41, s41, 0\n"
    "s_and_b32 s76, s75, s63\n"
    "s_add_u32 s44, s44, s76\n"
    "s_addc_u32 s45, s45, 0\n"
    ".LrLN0_LOOPB_%=:\n"
    ".LrLN0_top_%=:\n"
    "s_add_u32 s68, s68, 1\n"
    "s_add_u32 s69, s68, 4\n"
    "s_and_b32 s75, s68, s58\n"
    "s_mul_i32 s77, s75, 81920\n"
    "s_add_u32 s77, s77, s59\n"
    "s_add_u32 s78, s77, s60\n"
    "s_and_b32 s73, s69, s55\n"
    "s_mul_i32 s73, s73, s54\n"
    "s_add_u32 s73, s73, s53\n"
    "s_add_u32 s74, s73, s56\n"
    "s_waitcnt vmcnt(27)\n"
    "v_mfma_f32_16x16x32_bf16 v[80:83], v[24:27], v[40:43], 0\n"
    "v_mfma_f32_16x16x32_bf16 v[84:87], v[28:31], v[40:43], 0\n"
    "s_waitcnt vmcnt(26)\n"
    "v_mfma_f32_16x16x32_bf16 v[88:91], v[24:27], v[44:47], 0\n"
    "v_mfma_f32_16x16x32_bf16 v[92:95], v[28:31], v[44:47], 0\n"
    "s_waitcnt vmcnt(25)\n"
    "v_mfma_f32_16x16x32_bf16 v[96:99], v[24:27], v[48:51], 0\n"
    "buffer_load_dwordx4 v[40:43], v6, s[40:43], s73 offen sc1\n"
    "buffer_load_dwordx4 v[44:47], v6, s[40:43], s74 offen sc1\n"
    "buffer_load_dwordx4 v[48:51], v7, s[40:43], s73 offen sc1\n"
    "v_pk_add_f32 v[112:113], v[80:81], v[82:83]\n"
    "v_pk_add_f32 v[114:115], v[84:85], v[86:87]\n"
    "v_pk_add_f32 v[112:113], v[112:113], v[88:89]\n"
    "v_pk_add_f32 v[114:115], v[114:115], v[90:91]\n"
    "v_pk_add_f32 v[112:113], v[112:113], v[92:93]\n"
    "v_pk_add_f32 v[114:115], v[114:115], v[94:95]\n"
    "v_pk_add_f32 v[112:113], v[112:113], v[96:97]\n"
    "v_pk_add_f32 v[114:115], v[114:115], v[98:99]\n"
    "v_pk_add_f32 v[112:113], v[112:113], v[114:115]\n"
    "v_add_f32 v112, v112, v113\n"
    "s_nop 1\n"
    "v_add_f32_dpp v14, v112, v112 row_ror:8 row_mask:0xf bank_mask:0xf\n"
    "v_add_f32_dpp v15, v112, v112 row_ror:8 row_mask:0xf bank_mask:0xf\n"
    "s_nop 1\n"
    "v_permlane16_swap_b32 v14, v15\n"
    "v_add_f32 v112, v14, v15\n"
    "v_add_f32 v14, v14, v15\n"
    "s_nop 1\n"
    "v_permlane32_swap_b32 v112, v14\n"
    "v_add_f32 v112, v112, v14\n"
    "v_mul_f32 v112, 0x3be38e39, v112\n"
    "v_mul_f32 v114, v112, v12\n"
    "v_pk_add_f32 v[80:81], v[80:81], v[112:113] op_sel_hi:[1,0] neg_lo:[0,1] neg_hi:[0,1]\n"
    "v_pk_add_f32 v[82:83], v[82:83], v[112:113] op_sel_hi:[1,0] neg_lo:[0,1] neg_hi:[0,1]\n"
    "v_pk_add_f32 v[84:85], v[84:85], v[112:113] op_sel_hi:[1,0] neg_lo:[0,1] neg_hi:[0,1]\n"
    "v_pk_add_f32 v[86:87], v[86:87], v[112:113] op_sel_hi:[1,0] neg_lo:[0,1] neg_hi:[0,1]\n"
    "v_pk_add_f32 v[88:89], v[88:89], v[112:113] op_sel_hi:[1,0] neg_lo:[0,1] neg_hi:[0,1]\n"
    "v_pk_add_f32 v[90:91], v[90:91], v[112:113] op_sel_hi:[1,0] neg_lo:[0,1] neg_hi:[0,1]\n"
    "v_pk_add_f32 v[92:93], v[92:93], v[112:113] op_sel_hi:[1,0] neg_lo:[0,1] neg_hi:[0,1]\n"
    "v_pk_add_f32 v[94:95], v[94:95], v[112:113] op_sel_hi:[1,0] neg_lo:[0,1] neg_hi:[0,1]\n"
    "v_pk_add_f32 v[96:97], v[96:97], v[114:115] op_sel_hi:[1,0] neg_lo:[0,1] neg_hi:[0,1]\n"
    "v_pk_add_f32 v[98:99], v[98:99], v[114:115] op_sel_hi:[1,0] neg_lo:[0,1] neg_hi:[0,1]\n"
    "v_pk_mul_f32 v[116:117], v[80:81], v[80:81]\n"
    "v_pk_mul_f32 v[118:119], v[82:83], v[82:83]\n"
    "v_pk_fma_f32 v[116:117], v[84:85], v[84:85], v[116:117]\n"
    "v_pk_fma_f32 v[118:119], v[86:87], v[86:87], v[118:119]\n"
    "v_pk_fma_f32 v[116:117], v[88:89], v[88:89], v[116:117]\n"
    "v_pk_fma_f32 v[118:119], v[90:91], v[90:91], v[118:119]\n"
    "v_pk_fma_f32 v[116:117], v[92:93], v[92:93], v[116:117]\n"
    "v_pk_fma_f32 v[118:119], v[94:95], v[94:95], v[118:119]\n"
    "v_pk_fma_f32 v[116:117], v[96:97], v[96:97], v[116:117]\n"
    "v_pk_fma_f32 v[118:119], v[98:99], v[98:99], v[118:119]\n"
    "v_pk_add_f32 v[116:117], v[116:117], v[118:119]\n"
    "v_add_f32 v113, v116, v117\n"
    "s_nop 1\n"
    "v_add_f32_dpp v14, v113, v113 row_ror:8 row_mask:0xf bank_mask:0xf\n"
    "v_add_f32_dpp v15, v113, v113 row_ror:8 row_mask:0xf bank_mask:0xf\n"
    "s_nop 1\n"
    "v_permlane16_swap_b32 v14, v15\n"
    "v_add_f32 v113, v14, v15\n"
    "v_add_f32 v14, v14, v15\n"
    "s_nop 1\n"
    "v_permlane32_swap_b32 v113, v14\n"
    "v_add_f32 v113, v113, v14\n"
    "ds_write_b64 v10, v[112:113]\n"
    "s_waitcnt vmcnt(17)\n"
    "s_add_u32 s75, s66, 1\n"
    "s_min_i32 s75, s75, s68\n"
    "s_sub_u32 s76, s68, 2\n"
    "s_max_i32 s75, s75, s76\n"
    "s_sub_u32 s75, s75, 1\n"
    "v_lshl_or_b32 v14, s75, 1, 1\n"
    "s_mov_b64 exec, s[64:65]\n"
    "buffer_atomic_smax v14, v13, s[36:39], 0 offen sc1\n"
    "s_mov_b64 exec, -1\n"
    "s_waitcnt lgkmcnt(0)\n"
    "s_barrier\n"
    "ds_read_b128 v[32:35], v11\n"
    "ds_read_b128 v[36:39], v11 offset:16\n"
    "v_xor_b32 v10, 256, v10\n"
    "v_xor_b32 v11, 256, v11\n"
    "s_waitcnt lgkmcnt(0)\n"
    "v_pk_add_f32 v[114:115], v[32:33], v[34:35]\n"
    "v_pk_add_f32 v[116:117], v[36:37], v[38:39]\n"
    "v_pk_add_f32 v[114:115], v[114:115], v[116:117]\n"
    "v_fmac_f32 v32, 0xbe800000, v114\n"
    "v_fmac_f32 v34, 0xbe800000, v114\n"
    "v_fmac_f32 v36, 0xbe800000, v114\n"
    "v_fmac_f32 v38, 0xbe800000, v114\n"
    "v_fmac_f32 v112, 0xbe800000, v114\n"
    "v_mul_f32 v116, v32, v32\n"
    "v_fmac_f32 v116, v34, v34\n"
    "v_fmac_f32 v116, v36, v36\n"
    "v_fmac_f32 v116, v38, v38\n"
    "v_fmac_f32 v115, 0x43100000, v116\n"
    "v_mov_b32 v116, 0x3727c5ac\n"
    "v_fmac_f32 v116, 0x3ae38e39, v115\n"
    "v_rsq_f32 v116, v116\n"
    "s_nop 0\n"
    "v_mul_f32 v114, v112, v116\n"
    "v_pk_fma_f32 v[80:81], v[80:81], v[116:117], v[114:115] op_sel_hi:[1,0,0]\n"
    "v_pk_fma_f32 v[82:83], v[82:83], v[116:117], v[114:115] op_sel_hi:[1,0,0]\n"
    "v_pk_fma_f32 v[84:85], v[84:85], v[116:117], v[114:115] op_sel_hi:[1,0,0]\n"
    "v_pk_fma_f32 v[86:87], v[86:87], v[116:117], v[114:115] op_sel_hi:[1,0,0]\n"
    "v_pk_fma_f32 v[88:89], v[88:89], v[116:117], v[114:115] op_sel_hi:[1,0,0]\n"
    "v_pk_fma_f32 v[90:91], v[90:91], v[116:117], v[114:115] op_sel_hi:[1,0,0]\n"
    "v_pk_fma_f32 v[92:93], v[92:93], v[116:117], v[114:115] op_sel_hi:[1,0,0]\n"
    "v_pk_fma_f32 v[94:95], v[94:95], v[116:117], v[114:115] op_sel_hi:[1,0,0]\n"
    "v_pk_fma_f32 v[96:97], v[96:97], v[116:117], v[114:115] op_sel_hi:[1,0,0]\n"
    "v_pk_fma_f32 v[98:99], v[98:99], v[116:117], v[114:115] op_sel_hi:[1,0,0]\n"
    "v_cvt_pk_bf16_f32 v100, v80, v81\n"
    "v_cvt_pk_bf16_f32 v101, v82, v83\n"
    "v_cvt_pk_bf16_f32 v102, v84, v85\n"
    "v_cvt_pk_bf16_f32 v103, v86, v87\n"
    "v_cvt_pk_bf16_f32 v104, v88, v89\n"
    "v_cvt_pk_bf16_f32 v105, v90, v91\n"
    "v_cvt_pk_bf16_f32 v106, v92, v93\n"
    "v_cvt_pk_bf16_f32 v107, v94, v95\n"
    "v_cvt_pk_bf16_f32 v108, v96, v97\n"
    "v_cvt_pk_bf16_f32 v109, v98, v99\n"
    "v_permlane16_swap_b32 v100, v102\n"
    "v_permlane16_swap_b32 v101, v103\n"
    "buffer_store_dwordx4 v[100:103], v4, s[44:47], s77 offen sc1\n"
    "v_permlane16_swap_b32 v104, v106\n"
    "v_permlane16_swap_b32 v105, v107\n"
    "buffer_store_dwordx4 v[104:107], v4, s[44:47], s78 offen sc1\n"
    "v_permlane16_swap_b32 v108, v110\n"
    "v_permlane16_swap_b32 v109, v111\n"
    "buffer_store_dwordx4 v[108:111], v5, s[44:47], s77 offen sc1\n"
    "s_cmp_eq_u32 s68, s66\n"
    "s_cbranch_scc1 .LrLN0_first_%=\n"
    ".LrLN0_first1_%=:\n"
    "s_add_u32 s68, s68, 1\n"
    "s_add_u32 s69, s68, 4\n"
    "s_and_b32 s75, s68, s58\n"
    "s_mul_i32 s77, s75, 81920\n"
    "s_add_u32 s77, s77, s59\n"
    "s_add_u32 s78, s77, s60\n"
    "s_and_b32 s73, s69, s55\n"
    "s_mul_i32 s73, s73, s54\n"
    "s_add_u32 s73, s73, s53\n"
    "s_add_u32 s74, s73, s56\n"
    "s_waitcnt vmcnt(27)\n"
    "v_mfma_f32_16x16x32_bf16 v[80:83], v[24:27], v[52:55], 0\n"
    "v_mfma_f32_16x16x32_bf16 v[84:87], v[28:31], v[52:55], 0\n"
    "s_waitcnt vmcnt(26)\n"
    "v_mfma_f32_16x16x32_bf16 v[88:91], v[24:27], v[56:59], 0\n"
    "v_mfma_f32_16x16x32_bf16 v[92:95], v[28:31], v[56:59], 0\n"
    "s_waitcnt vmcnt(25)\n"
    "v_mfma_f32_16x16x32_bf16 v[96:99], v[24:27], v[60:63], 0\n"
    "buffer_load_dwordx4 v[52:55], v6, s[40:43], s73 offen sc1\n"
    "buffer_load_dwordx4 v[56:59], v6, s[40:43], s74 offen sc1\n"
    "buffer_load_dwordx4 v[60:63], v7, s[40:43], s73 offen sc1\n"
    "v_pk_add_f32 v[112:113], v[80:81], v[82:83]\n"
    "v_pk_add_f32 v[114:115], v[84:85], v[86:87]\n"
    "v_pk_add_f32 v[112:113], v[112:113], v[88:89]\n"
    "v_pk_add_f32 v[114:115], v[114:115], v[90:91]\n"
    "v_pk_add_f32 v[112:113], v[112:113], v[92:93]\n"
    "v_pk_add_f32 v[114:115], v[114:115], v[94:95]\n"
    "v_pk_add_f32 v[112:113], v[112:113], v[96:97]\n"
    "v_pk_add_f32 v[114:115], v[114:115], v[98:99]\n"
    "v_pk_add_f32 v[112:113], v[112:113], v[114:115]\n"
    "v_add_f32 v112, v112, v113\n"
    "s_nop 1\n"
    "v_add_f32_dpp v14, v112, v112 row_ror:8 row_mask:0xf bank_mask:0xf\n"
    "v_add_f32_dpp v15, v112, v112 row_ror:8 row_mask:0xf bank_mask:0xf\n"
    "s_nop 1\n"
    "v_permlane16_swap_b32 v14, v15\n"
    "v_add_f32 v112, v14, v15\n"
    "v_add_f32 v14, v14, v15\n"
    "s_nop 1\n"
    "v_permlane32_swap_b32 v112, v14\n"
    "v_add_f32 v112, v112, v14\n"
    "v_mul_f32 v112, 0x3be38e39, v112\n"
    "v_mul_f32 v114, v112, v12\n"
    "v_pk_add_f32 v[80:81], v[80:81], v[112:113] op_sel_hi:[1,0] neg_lo:[0,1] neg_hi:[0,1]\n"
    "v_pk_add_f32 v[82:83], v[82:83], v[112:113] op_sel_hi:[1,0] neg_lo:[0,1] neg_hi:[0,1]\n"
    "v_pk_add_f32 v[84:85], v[84:85], v[112:113] op_sel_hi:[1,0] neg_lo:[0,1] neg_hi:[0,1]\n"
    "v_pk_add_f32 v[86:87], v[86:87], v[112:113] op_sel_hi:[1,0] neg_lo:[0,1] neg_hi:[0,1]\n"
    "v_pk_add_f32 v[88:89], v[88:89], v[112:113] op_sel_hi:[1,0] neg_lo:[0,1] neg_hi:[0,1]\n"
    "v_pk_add_f32 v[90:91], v[90:91], v[112:113] op_sel_hi:[1,0] neg_lo:[0,1] neg_hi:[0,1]\n"
    "v_pk_add_f32 v[92:93], v[92:93], v[112:113] op_sel_hi:[1,0] neg_lo:[0,1] neg_hi:[0,1]\n"
    "v_pk_add_f32 v[94:95], v[94:95], v[112:113] op_sel_hi:[1,0] neg_lo:[0,1] neg_hi:[0,1]\n"
    "v_pk_add_f32 v[96:97], v[96:97], v[114:115] op_sel_hi:[1,0] neg_lo:[0,1] neg_hi:[0,1]\n"
    "v_pk_add_f32 v[98:99], v[98:99], v[114:115] op_sel_hi:[1,0] neg_lo:[0,1] neg_hi:[0,1]\n"
    "v_pk_mul_f32 v[116:117], v[80:81], v[80:81]\n"
    "v_pk_mul_f32 v[118:119], v[82:83], v[82:83]\n"
    "v_pk_fma_f32 v[116:117], v[84:85], v[84:85], v[116:117]\n"
    "v_pk_fma_f32 v[118:119], v[86:87], v[86:87], v[118:119]\n"
    "v_pk_fma_f32 v[116:117], v[88:89], v[88:89], v[116:117]\n"
    "v_pk_fma_f32 v[118:119], v[90:91], v[90:91], v[118:119]\n"
    "v_pk_fma_f32 v[116:117], v[92:93], v[92:93], v[116:117]\n"
    "v_pk_fma_f32 v[118:119], v[94:95], v[94:95], v[118:119]\n"
    "v_pk_fma_f32 v[116:117], v[96:97], v[96:97], v[116:117]\n"
    "v_pk_fma_f32 v[118:119], v[98:99], v[98:99], v[118:119]\n"
    "v_pk_add_f32 v[116:117], v[116:117], v[118:119]\n"
    "v_add_f32 v113, v116, v117\n"
    "s_nop 1\n"
    "v_add_f32_dpp v14, v113, v113 row_ror:8 row_mask:0xf bank_mask:0xf\n"
    "v_add_f32_dpp v15, v113, v113 row_ror:8 row_mask:0xf bank_mask:0xf\n"
    "s_nop 1\n"
    "v_permlane16_swap_b32 v14, v15\n"
    "v_add_f32 v113, v14, v15\n"
    "v_add_f32 v14, v14, v15\n"
    "s_nop 1\n"
    "v_permlane32_swap_b32 v113, v14\n"
    "v_add_f32 v113, v113, v14\n"
    "ds_write_b64 v10, v[112:113]\n"
    "s_waitcnt vmcnt(17)\n"
    "s_add_u32 s75, s66, 1\n"
    "s_min_i32 s75, s75, s68\n"
    "s_sub_u32 s76, s68, 2\n"
    "s_max_i32 s75, s75, s76\n"
    "s_sub_u32 s75, s75, 1\n"
    "v_lshl_or_b32 v14, s75, 1, 1\n"
    "s_mov_b64 exec, s[64:65]\n"
    "buffer_atomic_smax v14, v13, s[36:39], 0 offen sc1\n"
    "s_mov_b64 exec, -1\n"
    "s_waitcnt lgkmcnt(0)\n"
    "s_barrier\n"
    "ds_read_b128 v[32:35], v11\n"
    "ds_read_b128 v[36:39], v11 offset:16\n"
    "v_xor_b32 v10, 256, v10\n"
    "v_xor_b32 v11, 256, v11\n"
    "s_waitcnt lgkmcnt(0)\n"
    "v_pk_add_f32 v[114:115], v[32:33], v[34:35]\n"
    "v_pk_add_f32 v[116:117], v[36:37], v[38:39]\n"
    "v_pk_add_f32 v[114:115], v[114:115], v[116:117]\n"
    "v_fmac_f32 v32, 0xbe800000, v114\n"
    "v_fmac_f32 v34, 0xbe800000, v114\n"
    "v_fmac_f32 v36, 0xbe800000, v114\n"
    "v_fmac_f32 v38, 0xbe800000, v114\n"
    "v_fmac_f32 v112, 0xbe800000, v114\n"
    "v_mul_f32 v116, v32, v32\n"
    "v_fmac_f32 v116, v34, v34\n"
    "v_fmac_f32 v116, v36, v36\n"
    "v_fmac_f32 v116, v38, v38\n"
    "v_fmac_f32 v115, 0x43100000, v116\n"
    "v_mov_b32 v116, 0x3727c5ac\n"
    "v_fmac_f32 v116, 0x3ae38e39, v115\n"
    "v_rsq_f32 v116, v116\n"
    "s_nop 0\n"
    "v_mul_f32 v114, v112, v116\n"
    "v_pk_fma_f32 v[80:81], v[80:81], v[116:117], v[114:115] op_sel_hi:[1,0,0]\n"
    "v_pk_fma_f32 v[82:83], v[82:83], v[116:117], v[114:115] op_sel_hi:[1,0,0]\n"
    "v_pk_fma_f32 v[84:85], v[84:85], v[116:117], v[114:115] op_sel_hi:[1,0,0]\n"
    "v_pk_fma_f32 v[86:87], v[86:87], v[116:117], v[114:115] op_sel_hi:[1,0,0]\n"
    "v_pk_fma_f32 v[88:89], v[88:89], v[116:117], v[114:115] op_sel_hi:[1,0,0]\n"
    "v_pk_fma_f32 v[90:91], v[90:91], v[116:117], v[114:115] op_sel_hi:[1,0,0]\n"
    "v_pk_fma_f32 v[92:93], v[92:93], v[116:117], v[114:115] op_sel_hi:[1,0,0]\n"
    "v_pk_fma_f32 v[94:95], v[94:95], v[116:117], v[114:115] op_sel_hi:[1,0,0]\n"
    "v_pk_fma_f32 v[96:97], v[96:97], v[116:117], v[114:115] op_sel_hi:[1,0,0]\n"
    "v_pk_fma_f32 v[98:99], v[98:99], v[116:117], v[114:115] op_sel_hi:[1,0,0]\n"
    "v_cvt_pk_bf16_f32 v100, v80, v81\n"
    "v_cvt_pk_bf16_f32 v101, v82, v83\n"
    "v_cvt_pk_bf16_f32 v102, v84, v85\n"
    "v_cvt_pk_bf16_f32 v103, v86, v87\n"
    "v_cvt_pk_bf16_f32 v104, v88, v89\n"
    "v_cvt_pk_bf16_f32 v105, v90, v91\n"
    "v_cvt_pk_bf16_f32 v106, v92, v93\n"
    "v_cvt_pk_bf16_f32 v107, v94, v95\n"
    "v_cvt_pk_bf16_f32 v108, v96, v97\n"
    "v_cvt_pk_bf16_f32 v109, v98, v99\n"
    "v_permlane16_swap_b32 v100, v102\n"
    "v_permlane16_swap_b32 v101, v103\n"
    "buffer_store_dwordx4 v[100:103], v4, s[44:47], s77 offen sc1\n"
    "v_permlane16_swap_b32 v104, v106\n"
    "v_permlane16_swap_b32 v105, v107\n"
    "buffer_store_dwordx4 v[104:107], v4, s[44:47], s78 offen sc1\n"
    "v_permlane16_swap_b32 v108, v110\n"
    "v_permlane16_swap_b32 v109, v111\n"
    "buffer_store_dwordx4 v[108:111], v5, s[44:47], s77 offen sc1\n"
    "s_add_u32 s68, s68, 1\n"
    "s_add_u32 s69, s68, 4\n"
    "s_and_b32 s75, s68, s58\n"
    "s_mul_i32 s77, s75, 81920\n"
    "s_add_u32 s77, s77, s59\n"
    "s_add_u32 s78, s77, s60\n"
    "s_and_b32 s73, s69, s55\n"
    "s_mul_i32 s73, s73, s54\n"
    "s_add_u32 s73, s73, s53\n"
    "s_add_u32 s74, s73, s56\n"
    "s_waitcnt vmcnt(27)\n"
    "v_mfma_f32_16x16x32_bf16 v[80:83], v[24:27], v[68:71], 0\n"
    "v_mfma_f32_16x16x32_bf16 v[84:87], v[28:31], v[68:71], 0\n"
    "s_waitcnt vmcnt(26)\n"
    "v_mfma_f32_16x16x32_bf16 v[88:91], v[24:27], v[72:75], 0\n"
    "v_mfma_f32_16x16x32_bf16 v[92:95], v[28:31], v[72:75], 0\n"
    "s_waitcnt vmcnt(25)\n"
    "v_mfma_f32_16x16x32_bf16 v[96:99], v[24:27], v[76:79], 0\n"
    "buffer_load_dwordx4 v[68:71], v6, s[40:43], s73 offen sc1\n"
    "buffer_load_dwordx4 v[72:75], v6, s[40:43], s74 offen sc1\n"
    "buffer_load_dwordx4 v[76:79], v7, s[40:43], s73 offen sc1\n"
    "v_pk_add_f32 v[112:113], v[80:81], v[82:83]\n"
    "v_pk_add_f32 v[114:115], v[84:85], v[86:87]\n"
    "v_pk_add_f32 v[112:113], v[112:113], v[88:89]\n"
    "v_pk_add_f32 v[114:115], v[114:115], v[90:91]\n"
    "v_pk_add_f32 v[112:113], v[112:113], v[92:93]\n"
    "v_pk_add_f32 v[114:115], v[114:115], v[94:95]\n"
    "v_pk_add_f32 v[112:113], v[112:113], v[96:97]\n"
    "v_pk_add_f32 v[114:115], v[114:115], v[98:99]\n"
    "v_pk_add_f32 v[112:113], v[112:113], v[114:115]\n"
    "v_add_f32 v112, v112, v113\n"
    "s_nop 1\n"
    "v_add_f32_dpp v14, v112, v112 row_ror:8 row_mask:0xf bank_mask:0xf\n"
    "v_add_f32_dpp v15, v112, v112 row_ror:8 row_mask:0xf bank_mask:0xf\n"
    "s_nop 1\n"
    "v_permlane16_swap_b32 v14, v15\n"
    "v_add_f32 v112, v14, v15\n"
    "v_add_f32 v14, v14, v15\n"
    "s_nop 1\n"
    "v_permlane32_swap_b32 v112, v14\n"
    "v_add_f32 v112, v112, v14\n"
    "v_mul_f32 v112, 0x3be38e39, v112\n"
    "v_mul_f32 v114, v112, v12\n"
    "v_pk_add_f32 v[80:81], v[80:81], v[112:113] op_sel_hi:[1,0] neg_lo:[0,1] neg_hi:[0,1]\n"
    "v_pk_add_f32 v[82:83], v[82:83], v[112:113] op_sel_hi:[1,0] neg_lo:[0,1] neg_hi:[0,1]\n"
    "v_pk_add_f32 v[84:85], v[84:85], v[112:113] op_sel_hi:[1,0] neg_lo:[0,1] neg_hi:[0,1]\n"
    "v_pk_add_f32 v[86:87], v[86:87], v[112:113] op_sel_hi:[1,0] neg_lo:[0,1] neg_hi:[0,1]\n"
    "v_pk_add_f32 v[88:89], v[88:89], v[112:113] op_sel_hi:[1,0] neg_lo:[0,1] neg_hi:[0,1]\n"
    "v_pk_add_f32 v[90:91], v[90:91], v[112:113] op_sel_hi:[1,0] neg_lo:[0,1] neg_hi:[0,1]\n"
    "v_pk_add_f32 v[92:93], v[92:93], v[112:113] op_sel_hi:[1,0] neg_lo:[0,1] neg_hi:[0,1]\n"
    "v_pk_add_f32 v[94:95], v[94:95], v[112:113] op_sel_hi:[1,0] neg_lo:[0,1] neg_hi:[0,1]\n"
    "v_pk_add_f32 v[96:97], v[96:97], v[114:115] op_sel_hi:[1,0] neg_lo:[0,1] neg_hi:[0,1]\n"
    "v_pk_add_f32 v[98:99], v[98:99], v[114:115] op_sel_hi:[1,0] neg_lo:[0,1] neg_hi:[0,1]\n"
    "v_pk_mul_f32 v[116:117], v[80:81], v[80:81]\n"
    "v_pk_mul_f32 v[118:119], v[82:83], v[82:83]\n"
    "v_pk_fma_f32 v[116:117], v[84:85], v[84:85], v[116:117]\n"
    "v_pk_fma_f32 v[118:119], v[86:87], v[86:87], v[118:119]\n"
    "v_pk_fma_f32 v[116:117], v[88:89], v[88:89], v[116:117]\n"
    "v_pk_fma_f32 v[118:119], v[90:91], v[90:91], v[118:119]\n"
    "v_pk_fma_f32 v[116:117], v[92:93], v[92:93], v[116:117]\n"
    "v_pk_fma_f32 v[118:119], v[94:95], v[94:95], v[118:119]\n"
    "v_pk_fma_f32 v[116:117], v[96:97], v[96:97], v[116:117]\n"
    "v_pk_fma_f32 v[118:119], v[98:99], v[98:99], v[118:119]\n"
    "v_pk_add_f32 v[116:117], v[116:117], v[118:119]\n"
    "v_add_f32 v113, v116, v117\n"
    "s_nop 1\n"
    "v_add_f32_dpp v14, v113, v113 row_ror:8 row_mask:0xf bank_mask:0xf\n"
    "v_add_f32_dpp v15, v113, v113 row_ror:8 row_mask:0xf bank_mask:0xf\n"
    "s_nop 1\n"
    "v_permlane16_swap_b32 v14, v15\n"
    "v_add_f32 v113, v14, v15\n"
    "v_add_f32 v14, v14, v15\n"
    "s_nop 1\n"
    "v_permlane32_swap_b32 v113, v14\n"
    "v_add_f32 v113, v113, v14\n"
    "ds_write_b64 v10, v[112:113]\n"
    "s_waitcnt vmcnt(17)\n"
    "s_add_u32 s75, s66, 1\n"
    "s_min_i32 s75, s75, s68\n"
    "s_sub_u32 s76, s68, 2\n"
    "s_max_i32 s75, s75, s76\n"
    "s_sub_u32 s75, s75, 1\n"
    "v_lshl_or_b32 v14, s75, 1, 1\n"
    "s_mov_b64 exec, s[64:65]\n"
    "buffer_atomic_smax v14, v13, s[36:39], 0 offen sc1\n"
    "s_mov_b64 exec, -1\n"
    "s_waitcnt lgkmcnt(0)\n"
    "s_barrier\n"
    "ds_read_b128 v[32:35], v11\n"
    "ds_read_b128 v[36:39], v11 offset:16\n"
    "v_xor_b32 v10, 256, v10\n"
    "v_xor_b32 v11, 256, v11\n"
    "s_waitcnt lgkmcnt(0)\n"
    "v_pk_add_f32 v[114:115], v[32:33], v[34:35]\n"
    "v_pk_add_f32 v[116:117], v[36:37], v[38:39]\n"
    "v_pk_add_f32 v[114:115], v[114:115], v[116:117]\n"
    "v_fmac_f32 v32, 0xbe800000, v114\n"
    "v_fmac_f32 v34, 0xbe800000, v114\n"
    "v_fmac_f32 v36, 0xbe800000, v114\n"
    "v_fmac_f32 v38, 0xbe800000, v114\n"
    "v_fmac_f32 v112, 0xbe800000, v114\n"
    "v_mul_f32 v116, v32, v32\n"
    "v_fmac_f32 v116, v34, v34\n"
    "v_fmac_f32 v116, v36, v36\n"
    "v_fmac_f32 v116, v38, v38\n"
    "v_fmac_f32 v115, 0x43100000, v116\n"
    "v_mov_b32 v116, 0x3727c5ac\n"
    "v_fmac_f32 v116, 0x3ae38e39, v115\n"
    "v_rsq_f32 v116, v116\n"
    "s_nop 0\n"
    "v_mul_f32 v114, v112, v116\n"
    "v_pk_fma_f32 v[80:81], v[80:81], v[116:117], v[114:115] op_sel_hi:[1,0,0]\n"
    "v_pk_fma_f32 v[82:83], v[82:83], v[116:117], v[114:115] op_sel_hi:[1,0,0]\n"
    "v_pk_fma_f32 v[84:85], v[84:85], v[116:117], v[114:115] op_sel_hi:[1,0,0]\n"
    "v_pk_fma_f32 v[86:87], v[86:87], v[116:117], v[114:115] op_sel_hi:[1,0,0]\n"
    "v_pk_fma_f32 v[88:89], v[88:89], v[116:117], v[114:115] op_sel_hi:[1,0,0]\n"
    "v_pk_fma_f32 v[90:91], v[90:91], v[116:117], v[114:115] op_sel_hi:[1,0,0]\n"
    "v_pk_fma_f32 v[92:93], v[92:93], v[116:117], v[114:115] op_sel_hi:[1,0,0]\n"
    "v_pk_fma_f32 v[94:95], v[94:95], v[116:117], v[114:115] op_sel_hi:[1,0,0]\n"
    "v_pk_fma_f32 v[96:97], v[96:97], v[116:117], v[114:115] op_sel_hi:[1,0,0]\n"
    "v_pk_fma_f32 v[98:99], v[98:99], v[116:117], v[114:115] op_sel_hi:[1,0,0]\n"
    "v_cvt_pk_bf16_f32 v100, v80, v81\n"
    "v_cvt_pk_bf16_f32 v101, v82, v83\n"
    "v_cvt_pk_bf16_f32 v102, v84, v85\n"
    "v_cvt_pk_bf16_f32 v103, v86, v87\n"
    "v_cvt_pk_bf16_f32 v104, v88, v89\n"
    "v_cvt_pk_bf16_f32 v105, v90, v91\n"
    "v_cvt_pk_bf16_f32 v106, v92, v93\n"
    "v_cvt_pk_bf16_f32 v107, v94, v95\n"
    "v_cvt_pk_bf16_f32 v108, v96, v97\n"
    "v_cvt_pk_bf16_f32 v109, v98, v99\n"
    "v_permlane16_swap_b32 v100, v102\n"
    "v_permlane16_swap_b32 v101, v103\n"
    "buffer_store_dwordx4 v[100:103], v4, s[44:47], s77 offen sc1\n"
    "v_permlane16_swap_b32 v104, v106\n"
    "v_permlane16_swap_b32 v105, v107\n"
    "buffer_store_dwordx4 v[104:107], v4, s[44:47], s78 offen sc1\n"
    "v_permlane16_swap_b32 v108, v110\n"
    "v_permlane16_swap_b32 v109, v111\n"
    "buffer_store_dwordx4 v[108:111], v5, s[44:47], s77 offen sc1\n"
    "s_add_u32 s68, s68, 1\n"
    "s_add_u32 s69, s68, 4\n"
    "s_and_b32 s75, s68, s58\n"
    "s_mul_i32 s77, s75, 81920\n"
    "s_add_u32 s77, s77, s59\n"
    "s_add_u32 s78, s77, s60\n"
    "s_and_b32 s73, s69, s55\n"
    "s_mul_i32 s73, s73, s54\n"
    "s_add_u32 s73, s73, s53\n"
    "s_add_u32 s74, s73, s56\n"
    "s_waitcnt vmcnt(27)\n"
    "v_mfma_f32_16x16x32_bf16 v[80:83], v[24:27], v[124:127], 0\n"
    "v_mfma_f32_16x16x32_bf16 v[84:87], v[28:31], v[124:127], 0\n"
    "s_waitcnt vmcnt(26)\n"
    "v_mfma_f32_16x16x32_bf16 v[88:91], v[24:27], v[128:131], 0\n"
    "v_mfma_f32_16x16x32_bf16 v[92:95], v[28:31], v[128:131], 0\n"
    "s_waitcnt vmcnt(25)\n"
    "v_mfma_f32_16x16x32_bf16 v[96:99], v[24:27], v[132:135], 0\n"
    "buffer_load_dwordx4 v[124:127], v6, s[40:43], s73 offen sc1\n"
    "buffer_load_dwordx4 v[128:131], v6, s[40:43], s74 offen sc1\n"
    "buffer_load_dwordx4 v[132:135], v7, s[40:43], s73 offen sc1\n"
    "v_pk_add_f32 v[112:113], v[80:81], v[82:83]\n"
    "v_pk_add_f32 v[114:115], v[84:85], v[86:87]\n"
    "v_pk_add_f32 v[112:113], v[112:113], v[88:89]\n"
    "v_pk_add_f32 v[114:115], v[114:115], v[90:91]\n"
    "v_pk_add_f32 v[112:113], v[112:113], v[92:93]\n"
    "v_pk_add_f32 v[114:115], v[114:115], v[94:95]\n"
    "v_pk_add_f32 v[112:113], v[112:113], v[96:97]\n"
    "v_pk_add_f32 v[114:115], v[114:115], v[98:99]\n"
    "v_pk_add_f32 v[112:113], v[112:113], v[114:115]\n"
    "v_add_f32 v112, v112, v113\n"
    "s_nop 1\n"
    "v_add_f32_dpp v14, v112, v112 row_ror:8 row_mask:0xf bank_mask:0xf\n"
    "v_add_f32_dpp v15, v112, v112 row_ror:8 row_mask:0xf bank_mask:0xf\n"
    "s_nop 1\n"
    "v_permlane16_swap_b32 v14, v15\n"
    "v_add_f32 v112, v14, v15\n"
    "v_add_f32 v14, v14, v15\n"
    "s_nop 1\n"
    "v_permlane32_swap_b32 v112, v14\n"
    "v_add_f32 v112, v112, v14\n"
    "v_mul_f32 v112, 0x3be38e39, v112\n"
    "v_mul_f32 v114, v112, v12\n"
    "v_pk_add_f32 v[80:81], v[80:81], v[112:113] op_sel_hi:[1,0] neg_lo:[0,1] neg_hi:[0,1]\n"
    "v_pk_add_f32 v[82:83], v[82:83], v[112:113] op_sel_hi:[1,0] neg_lo:[0,1] neg_hi:[0,1]\n"
    "v_pk_add_f32 v[84:85], v[84:85], v[112:113] op_sel_hi:[1,0] neg_lo:[0,1] neg_hi:[0,1]\n"
    "v_pk_add_f32 v[86:87], v[86:87], v[112:113] op_sel_hi:[1,0] neg_lo:[0,1] neg_hi:[0,1]\n"
    "v_pk_add_f32 v[88:89], v[88:89], v[112:113] op_sel_hi:[1,0] neg_lo:[0,1] neg_hi:[0,1]\n"
    "v_pk_add_f32 v[90:91], v[90:91], v[112:113] op_sel_hi:[1,0] neg_lo:[0,1] neg_hi:[0,1]\n"
    "v_pk_add_f32 v[92:93], v[92:93], v[112:113] op_sel_hi:[1,0] neg_lo:[0,1] neg_hi:[0,1]\n"
    "v_pk_add_f32 v[94:95], v[94:95], v[112:113] op_sel_hi:[1,0] neg_lo:[0,1] neg_hi:[0,1]\n"
    "v_pk_add_f32 v[96:97], v[96:97], v[114:115] op_sel_hi:[1,0] neg_lo:[0,1] neg_hi:[0,1]\n"
    "v_pk_add_f32 v[98:99], v[98:99], v[114:115] op_sel_hi:[1,0] neg_lo:[0,1] neg_hi:[0,1]\n"
    "v_pk_mul_f32 v[116:117], v[80:81], v[80:81]\n"
    "v_pk_mul_f32 v[118:119], v[82:83], v[82:83]\n"
    "v_pk_fma_f32 v[116:117], v[84:85], v[84:85], v[116:117]\n"
    "v_pk_fma_f32 v[118:119], v[86:87], v[86:87], v[118:119]\n"
    "v_pk_fma_f32 v[116:117], v[88:89], v[88:89], v[116:117]\n"
    "v_pk_fma_f32 v[118:119], v[90:91], v[90:91], v[118:119]\n"
    "v_pk_fma_f32 v[116:117], v[92:93], v[92:93], v[116:117]\n"
    "v_pk_fma_f32 v[118:119], v[94:95], v[94:95], v[118:119]\n"
    "v_pk_fma_f32 v[116:117], v[96:97], v[96:97], v[116:117]\n"
    "v_pk_fma_f32 v[118:119], v[98:99], v[98:99], v[118:119]\n"
    "v_pk_add_f32 v[116:117], v[116:117], v[118:119]\n"
    "v_add_f32 v113, v116, v117\n"
    "s_nop 1\n"
    "v_add_f32_dpp v14, v113, v113 row_ror:8 row_mask:0xf bank_mask:0xf\n"
    "v_add_f32_dpp v15, v113, v113 row_ror:8 row_mask:0xf bank_mask:0xf\n"
    "s_nop 1\n"
    "v_permlane16_swap_b32 v14, v15\n"
    "v_add_f32 v113, v14, v15\n"
    "v_add_f32 v14, v14, v15\n"
    "s_nop 1\n"
    "v_permlane32_swap_b32 v113, v14\n"
    "v_add_f32 v113, v113, v14\n"
    "ds_write_b64 v10, v[112:113]\n"
    "s_waitcnt vmcnt(17)\n"
    "s_add_u32 s75, s66, 1\n"
    "s_min_i32 s75, s75, s68\n"
    "s_sub_u32 s76, s68, 2\n"
    "s_max_i32 s75, s75, s76\n"
    "s_sub_u32 s75, s75, 1\n"
    "v_lshl_or_b32 v14, s75, 1, 1\n"
    "s_mov_b64 exec, s[64:65]\n"
    "buffer_atomic_smax v14, v13, s[36:39], 0 offen sc1\n"
    "s_mov_b64 exec, -1\n"
    "s_waitcnt lgkmcnt(0)\n"
    "s_barrier\n"
    "ds_read_b128 v[32:35], v11\n"
    "ds_read_b128 v[36:39], v11 offset:16\n"
    "v_xor_b32 v10, 256, v10\n"
    "v_xor_b32 v11, 256, v11\n"
    "s_waitcnt lgkmcnt(0)\n"
    "v_pk_add_f32 v[114:115], v[32:33], v[34:35]\n"
    "v_pk_add_f32 v[116:117], v[36:37], v[38:39]\n"
    "v_pk_add_f32 v[114:115], v[114:115], v[116:117]\n"
    "v_fmac_f32 v32, 0xbe800000, v114\n"
    "v_fmac_f32 v34, 0xbe800000, v114\n"
    "v_fmac_f32 v36, 0xbe800000, v114\n"
    "v_fmac_f32 v38, 0xbe800000, v114\n"
    "v_fmac_f32 v112, 0xbe800000, v114\n"
    "v_mul_f32 v116, v32, v32\n"
    "v_fmac_f32 v116, v34, v34\n"
    "v_fmac_f32 v116, v36, v36\n"
    "v_fmac_f32 v116, v38, v38\n"
    "v_fmac_f32 v115, 0x43100000, v116\n"
    "v_mov_b32 v116, 0x3727c5ac\n"
    "v_fmac_f32 v116, 0x3ae38e39, v115\n"
    "v_rsq_f32 v116, v116\n"
    "s_nop 0\n"
    "v_mul_f32 v114, v112, v116\n"
    "v_pk_fma_f32 v[80:81], v[80:81], v[116:117], v[114:115] op_sel_hi:[1,0,0]\n"
    "v_pk_fma_f32 v[82:83], v[82:83], v[116:117], v[114:115] op_sel_hi:[1,0,0]\n"
    "v_pk_fma_f32 v[84:85], v[84:85], v[116:117], v[114:115] op_sel_hi:[1,0,0]\n"
    "v_pk_fma_f32 v[86:87], v[86:87], v[116:117], v[114:115] op_sel_hi:[1,0,0]\n"
    "v_pk_fma_f32 v[88:89], v[88:89], v[116:117], v[114:115] op_sel_hi:[1,0,0]\n"
    "v_pk_fma_f32 v[90:91], v[90:91], v[116:117], v[114:115] op_sel_hi:[1,0,0]\n"
    "v_pk_fma_f32 v[92:93], v[92:93], v[116:117], v[114:115] op_sel_hi:[1,0,0]\n"
    "v_pk_fma_f32 v[94:95], v[94:95], v[116:117], v[114:115] op_sel_hi:[1,0,0]\n"
    "v_pk_fma_f32 v[96:97], v[96:97], v[116:117], v[114:115] op_sel_hi:[1,0,0]\n"
    "v_pk_fma_f32 v[98:99], v[98:99], v[116:117], v[114:115] op_sel_hi:[1,0,0]\n"
    "v_cvt_pk_bf16_f32 v100, v80, v81\n"
    "v_cvt_pk_bf16_f32 v101, v82, v83\n"
    "v_cvt_pk_bf16_f32 v102, v84, v85\n"
    "v_cvt_pk_bf16_f32 v103, v86, v87\n"
    "v_cvt_pk_bf16_f32 v104, v88, v89\n"
    "v_cvt_pk_bf16_f32 v105, v90, v91\n"
    "v_cvt_pk_bf16_f32 v106, v92, v93\n"
    "v_cvt_pk_bf16_f32 v107, v94, v95\n"
    "v_cvt_pk_bf16_f32 v108, v96, v97\n"
    "v_cvt_pk_bf16_f32 v109, v98, v99\n"
    "v_permlane16_swap_b32 v100, v102\n"
    "v_permlane16_swap_b32 v101, v103\n"
    "buffer_store_dwordx4 v[100:103], v4, s[44:47], s77 offen sc1\n"
    "v_permlane16_swap_b32 v104, v106\n"
    "v_permlane16_swap_b32 v105, v107\n"
    "buffer_store_dwordx4 v[104:107], v4, s[44:47], s78 offen sc1\n"
    "v_permlane16_swap_b32 v108, v110\n"
    "v_permlane16_swap_b32 v109, v111\n"
    "buffer_store_dwordx4 v[108:111], v5, s[44:47], s77 offen sc1\n"
    "s_cmp_eq_u32 s68, s67\n"
    "s_cbranch_scc1 .LrLN0_fin_%=\n"
    "s_branch .LrLN0_top_%=\n"
    ".LrLN0_LOOPE_%=:\n"
    ".LrLN0_fin_%=:\n"
    "s_waitcnt vmcnt(0)\n"
    "s_mov_b32 s68, 63\n"
    "v_lshl_or_b32 v14, s68, 1, 1\n"
    "s_mov_b64 exec, s[64:65]\n"
    "buffer_atomic_smax v14, v13, s[36:39], 0 offen sc1\n"
    "s_mov_b64 exec, -1\n"
    "s_waitcnt vmcnt(0)\n"
    "s_barrier\n"
    "s_branch .LrLN0_FIN_%=\n"
    ".LrLN0_first_%=:\n"
    "s_waitcnt vmcnt(0)\n"
    "v_lshl_or_b32 v14, s68, 1, 1\n"
    "s_mov_b64 exec, s[64:65]\n"
    "buffer_atomic_smax v14, v13, s[36:39], 0 offen sc1\n"
    "s_mov_b64 exec, -1\n"
    "s_branch .LrLN0_first1_%=\n"
    ".LrLN0_FIN_%=:\n"
  );
}
DEVI void role_mid(v16u sa, v16u sb, v16u va, v16u vb) {
  RUN_RED(
    "s_nop 4\n"
    "s_mov_b32 s50, 0\n"
    "s_mov_b32 s51, 0x80000000\n"
    "s_getpc_b64 s[84:85]\n"
    ".LrMID_PC_%=:\n"
    "s_and_b32 s85, s85, 0xffff\n"
    "s_mov_b32 s86, .LrMID_FIN_%=-.LrMID_PC_%=\n"
    "s_mov_b32 s87, 0x20000\n"
    "v_mbcnt_lo_u32_b32 v134, -1, 0\n"
    "v_mbcnt_hi_u32_b32 v134, -1, v134\n"
    "v_lshlrev_b32 v134, 7, v134\n"
    "buffer_load_dword v135, v134, s[84:87], 0 offen\n"
    "v_add_u32 v134, 8192, v134\n"
    "buffer_load_dword v135, v134, s[84:87], 0 offen\n"
    "s_mov_b32 s69, 0\n"
    "v_mov_b32 v122, -1\n"
    "v_mov_b32 v123, -1\n"
    "s_mov_b32 s70, 262144\n"
    ".LrMID_par_%=:\n"
    "buffer_load_dwordx2 v[14:15], off, s[36:39], 0 sc1\n"
    "s_waitcnt vmcnt(0)\n"
    "v_readfirstlane_b32 s75, v14\n"
    "v_readfirstlane_b32 s76, v15\n"
    "s_xor_b32 s75, s75, s48\n"
    "s_xor_b32 s76, s76, s49\n"
    "s_cselect_b32 s76, 2, 0\n"
    "s_or_b32 s76, s76, s75\n"
    "s_cmp_lt_u32 s76, 2\n"
    "s_cbranch_scc1 .LrMID_par1_%=\n"
    "s_sleep 4\n"
    "s_sub_u32 s70, s70, 1\n"
    "s_cbranch_scc0 .LrMID_par_%=\n"
    "s_mov_b32 s70, 0\n"
    ".LrMID_par1_%=:\n"
    "s_and_b32 s75, s75, 1\n"
    "s_lshl_b32 s75, s75, 13\n"
    "s_add_u32 s36, s36, s75\n"
    "s_addc_u32 s37, s37, 0\n"
    "s_and_b32 s76, s75, s61\n"
    "s_add_u32 s40, s40, s76\n"
    "s_addc_u32 s41, s41, 0\n"
    "s_and_b32 s76, s75, s63\n"
    "s_add_u32 s44, s44, s76\n"
    "s_addc_u32 s45, s45, 0\n"
    "s_branch .LrMID_block_%=\n"
    ".LrMID_LOOPB_%=:\n"
    ".LrMID_top_%=:\n"
    "s_mov_b32 s68, s69\n"
    "s_add_u32 s69, s68, 1\n"
    "s_and_b32 s75, s68, s58\n"
    "s_mul_i32 s77, s75, 4194304\n"
    "s_add_u32 s77, s77, s59\n"
    "s_add_u32 s78, s77, s60\n"
    "s_and_b32 s71, s69, 7\n"
    "s_mul_i32 s71, s71, 4194304\n"
    "s_add_u32 s71, s71, s52\n"
    "s_add_u32 s72, s71, 4096\n"
    "s_and_b32 s73, s69, s55\n"
    "s_mul_i32 s73, s73, s54\n"
    "s_add_u32 s73, s73, s53\n"
    "s_add_u32 s74, s73, s56\n"
    "s_mul_i32 s75, s75, 4194304\n"
    "s_add_u32 s75, s75, s57\n"
    "s_add_u32 s76, s75, 1024\n"
    "s_waitcnt vmcnt(12)\n"
    "v_mfma_f32_16x16x32_bf16 v[80:83], v[16:19], v[32:35], 0\n"
    "s_waitcnt vmcnt(11)\n"
    "v_mfma_f32_16x16x32_bf16 v[80:83], v[20:23], v[36:39], v[80:83]\n"
    "s_waitcnt vmcnt(10)\n"
    "v_mfma_f32_16x16x32_bf16 v[84:87], v[16:19], v[40:43], 0\n"
    "s_waitcnt vmcnt(9)\n"
    "v_mfma_f32_16x16x32_bf16 v[84:87], v[20:23], v[44:47], v[84:87]\n"
    "s_waitcnt vmcnt(8)\n"
    "v_mfma_f32_16x16x32_bf16 v[88:91], v[16:19], v[48:51], 0\n"
    "s_waitcnt vmcnt(7)\n"
    "v_mfma_f32_16x16x32_bf16 v[88:91], v[20:23], v[52:55], v[88:91]\n"
    "s_waitcnt vmcnt(6)\n"
    "v_mfma_f32_16x16x32_bf16 v[92:95], v[16:19], v[56:59], 0\n"
    "s_waitcnt vmcnt(5)\n"
    "v_mfma_f32_16x16x32_bf16 v[92:95], v[20:23], v[60:63], v[92:95]\n"
    "s_waitcnt vmcnt(4)\n"
    "v_mfma_f32_16x16x32_bf16 v[96:99], v[16:19], v[64:67], 0\n"
    "s_waitcnt vmcnt(3)\n"
    "v_mfma_f32_16x16x32_bf16 v[80:83], v[24:27], v[68:71], v[80:83]\n"
    "v_mfma_f32_16x16x32_bf16 v[84:87], v[28:31], v[68:71], v[84:87]\n"
    "s_waitcnt vmcnt(2)\n"
    "v_mfma_f32_16x16x32_bf16 v[88:91], v[24:27], v[72:75], v[88:91]\n"
    "v_mfma_f32_16x16x32_bf16 v[92:95], v[28:31], v[72:75], v[92:95]\n"
    "s_waitcnt vmcnt(1)\n"
    "v_mfma_f32_16x16x32_bf16 v[96:99], v[24:27], v[76:79], v[96:99]\n"
    "v_pk_add_f32 v[112:113], v[80:81], v[82:83]\n"
    "v_pk_add_f32 v[114:115], v[84:85], v[86:87]\n"
    "v_pk_add_f32 v[112:113], v[112:113], v[88:89]\n"
    "v_pk_add_f32 v[114:115], v[114:115], v[90:91]\n"
    "v_pk_add_f32 v[112:113], v[112:113], v[92:93]\n"
    "v_pk_add_f32 v[114:115], v[114:115], v[94:95]\n"
    "s_nop 1\n"
    "v_pk_add_f32 v[112:113], v[112:113], v[96:97]\n"
    "v_pk_add_f32 v[114:115], v[114:115], v[98:99]\n"
    "v_pk_add_f32 v[112:113], v[112:113], v[114:115]\n"
    "v_add_f32 v112, v112, v113\n"
    "v_cvt_pk_bf16_f32 v100, v80, v81\n"
    "v_cvt_pk_bf16_f32 v101, v82, v83\n"
    "v_cvt_pk_bf16_f32 v102, v84, v85\n"
    "v_cvt_pk_bf16_f32 v103, v86, v87\n"
    "v_cvt_pk_bf16_f32 v104, v88, v89\n"
    "v_cvt_pk_bf16_f32 v105, v90, v91\n"
    "v_cvt_pk_bf16_f32 v106, v92, v93\n"
    "v_cvt_pk_bf16_f32 v107, v94, v95\n"
    "v_cvt_pk_bf16_f32 v108, v96, v97\n"
    "v_cvt_pk_bf16_f32 v109, v98, v99\n"
    "v_add_f32_dpp v14, v112, v112 row_ror:8 row_mask:0xf bank_mask:0xf\n"
    "v_add_f32_dpp v15, v112, v112 row_ror:8 row_mask:0xf bank_mask:0xf\n"
    "s_nop 1\n"
    "v_permlane16_swap_b32 v14, v15\n"
    "v_add_f32 v112, v14, v15\n"
    "v_add_f32 v14, v14, v15\n"
    "s_nop 1\n"
    "v_permlane32_swap_b32 v112, v14\n"
    "v_add_f32 v112, v112, v14\n"
    "v_mul_f32 v112, 0x3be38e39, v112\n"
    "v_mul_f32 v114, v112, v12\n"
    "v_pk_add_f32 v[80:81], v[80:81], v[112:113] op_sel_hi:[1,0] neg_lo:[0,1] neg_hi:[0,1]\n"
    "v_pk_add_f32 v[82:83], v[82:83], v[112:113] op_sel_hi:[1,0] neg_lo:[0,1] neg_hi:[0,1]\n"
    "v_pk_add_f32 v[84:85], v[84:85], v[112:113] op_sel_hi:[1,0] neg_lo:[0,1] neg_hi:[0,1]\n"
    "v_pk_add_f32 v[86:87], v[86:87], v[112:113] op_sel_hi:[1,0] neg_lo:[0,1] neg_hi:[0,1]\n"
    "v_pk_add_f32 v[88:89], v[88:89], v[112:113] op_sel_hi:[1,0] neg_lo:[0,1] neg_hi:[0,1]\n"
    "v_pk_add_f32 v[90:91], v[90:91], v[112:113] op_sel_hi:[1,0] neg_lo:[0,1] neg_hi:[0,1]\n"
    "v_pk_add_f32 v[92:93], v[92:93], v[112:113] op_sel_hi:[1,0] neg_lo:[0,1] neg_hi:[0,1]\n"
    "v_pk_add_f32 v[94:95], v[94:95], v[112:113] op_sel_hi:[1,0] neg_lo:[0,1] neg_hi:[0,1]\n"
    "v_pk_add_f32 v[96:97], v[96:97], v[114:115] op_sel_hi:[1,0] neg_lo:[0,1] neg_hi:[0,1]\n"
    "v_pk_add_f32 v[98:99], v[98:99], v[114:115] op_sel_hi:[1,0] neg_lo:[0,1] neg_hi:[0,1]\n"
    "v_pk_mul_f32 v[116:117], v[80:81], v[80:81]\n"
    "v_pk_mul_f32 v[118:119], v[82:83], v[82:83]\n"
    "v_pk_fma_f32 v[116:117], v[84:85], v[84:85], v[116:117]\n"
    "v_pk_fma_f32 v[118:119], v[86:87], v[86:87], v[118:119]\n"
    "v_pk_fma_f32 v[116:117], v[88:89], v[88:89], v[116:117]\n"
    "v_pk_fma_f32 v[118:119], v[90:91], v[90:91], v[118:119]\n"
    "v_pk_fma_f32 v[116:117], v[92:93], v[92:93], v[116:117]\n"
    "v_pk_fma_f32 v[118:119], v[94:95], v[94:95], v[118:119]\n"
    "v_pk_fma_f32 v[116:117], v[96:97], v[96:97], v[116:117]\n"
    "v_pk_fma_f32 v[118:119], v[98:99], v[98:99], v[118:119]\n"
    "v_pk_add_f32 v[116:117], v[116:117], v[118:119]\n"
    "v_add_f32 v113, v116, v117\n"
    "s_nop 1\n"
    "v_add_f32_dpp v14, v113, v113 row_ror:8 row_mask:0xf bank_mask:0xf\n"
    "v_add_f32_dpp v15, v113, v113 row_ror:8 row_mask:0xf bank_mask:0xf\n"
    "s_nop 1\n"
    "v_permlane16_swap_b32 v14, v15\n"
    "v_add_f32 v113, v14, v15\n"
    "v_add_f32 v14, v14, v15\n"
    "s_nop 1\n"
    "v_permlane32_swap_b32 v113, v14\n"
    "v_add_f32 v113, v113, v14\n"
    "ds_write_b64 v10, v[112:113]\n"
    "v_add_u32 v120, v122, v1\n"
    "v_ashrrev_i32 v120, 1, v120\n"
    "v_cmp_gt_i32 vcc, s69, v120\n"
    "s_nop 0\n"
    "s_or_b64 exec, vcc, s[50:51]\n"
    "buffer_atomic_smax v122, v0, s[36:39], 0 offen sc0 sc1\n"
    "s_mov_b64 exec, -1\n"
    "s_nop 0\n"
    "v_permlane16_swap_b32 v100, v102\n"
    "v_permlane16_swap_b32 v101, v103\n"
    "v_permlane16_swap_b32 v104, v106\n"
    "v_permlane16_swap_b32 v105, v107\n"
    "v_permlane16_swap_b32 v108, v110\n"
    "v_permlane16_swap_b32 v109, v111\n"
    "s_waitcnt lgkmcnt(0)\n"
    "s_barrier\n"
    "ds_read_b128 v[32:35], v11\n"
    "ds_read_b128 v[36:39], v11 offset:16\n"
    "buffer_store_dwordx4 v[100:103], v8, s[36:39], s75 offen sc1\n"
    "buffer_store_dwordx4 v[104:107], v8, s[36:39], s76 offen sc1\n"
    "buffer_store_dwordx4 v[108:111], v9, s[36:39], s75 offen sc1\n"
    "v_xor_b32 v10, 256, v10\n"
    "v_xor_b32 v11, 256, v11\n"
    "s_waitcnt lgkmcnt(0)\n"
    "v_pk_add_f32 v[114:115], v[32:33], v[34:35]\n"
    "v_pk_add_f32 v[116:117], v[36:37], v[38:39]\n"
    "v_pk_add_f32 v[114:115], v[114:115], v[116:117]\n"
    "v_fmac_f32 v32, 0xbe800000, v114\n"
    "v_fmac_f32 v34, 0xbe800000, v114\n"
    "v_fmac_f32 v36, 0xbe800000, v114\n"
    "v_fmac_f32 v38, 0xbe800000, v114\n"
    "v_fmac_f32 v112, 0xbe800000, v114\n"
    "v_mul_f32 v116, v32, v32\n"
    "v_fmac_f32 v116, v34, v34\n"
    "v_fmac_f32 v116, v36, v36\n"
    "v_fmac_f32 v116, v38, v38\n"
    "v_fmac_f32 v115, 0x43100000, v116\n"
    "v_mov_b32 v116, 0x3727c5ac\n"
    "v_fmac_f32 v116, 0x3ae38e39, v115\n"
    "v_rsq_f32 v116, v116\n"
    "s_nop 0\n"
    "v_mul_f32 v114, v112, v116\n"
    "v_pk_fma_f32 v[80:81], v[80:81], v[116:117], v[114:115] op_sel_hi:[1,0,0]\n"
    "v_pk_fma_f32 v[82:83], v[82:83], v[116:117], v[114:115] op_sel_hi:[1,0,0]\n"
    "v_pk_fma_f32 v[84:85], v[84:85], v[116:117], v[114:115] op_sel_hi:[1,0,0]\n"
    "v_pk_fma_f32 v[86:87], v[86:87], v[116:117], v[114:115] op_sel_hi:[1,0,0]\n"
    "v_pk_fma_f32 v[88:89], v[88:89], v[116:117], v[114:115] op_sel_hi:[1,0,0]\n"
    "v_pk_fma_f32 v[90:91], v[90:91], v[116:117], v[114:115] op_sel_hi:[1,0,0]\n"
    "v_pk_fma_f32 v[92:93], v[92:93], v[116:117], v[114:115] op_sel_hi:[1,0,0]\n"
    "v_pk_fma_f32 v[94:95], v[94:95], v[116:117], v[114:115] op_sel_hi:[1,0,0]\n"
    "v_pk_fma_f32 v[96:97], v[96:97], v[116:117], v[114:115] op_sel_hi:[1,0,0]\n"
    "v_pk_fma_f32 v[98:99], v[98:99], v[116:117], v[114:115] op_sel_hi:[1,0,0]\n"
    "v_cvt_pk_bf16_f32 v100, v80, v81\n"
    "v_cvt_pk_bf16_f32 v101, v82, v83\n"
    "v_cvt_pk_bf16_f32 v102, v84, v85\n"
    "v_cvt_pk_bf16_f32 v103, v86, v87\n"
    "v_cvt_pk_bf16_f32 v104, v88, v89\n"
    "v_cvt_pk_bf16_f32 v105, v90, v91\n"
    "v_cvt_pk_bf16_f32 v106, v92, v93\n"
    "v_cvt_pk_bf16_f32 v107, v94, v95\n"
    "v_cvt_pk_bf16_f32 v108, v96, v97\n"
    "v_cvt_pk_bf16_f32 v109, v98, v99\n"
    "v_permlane16_swap_b32 v100, v102\n"
    "v_permlane16_swap_b32 v101, v103\n"
    "buffer_store_dwordx4 v[100:103], v4, s[44:47], s77 offen sc1\n"
    "v_permlane16_swap_b32 v104, v106\n"
    "v_permlane16_swap_b32 v105, v107\n"
    "buffer_store_dwordx4 v[104:107], v4, s[44:47], s78 offen sc1\n"
    "v_permlane16_swap_b32 v108, v110\n"
    "v_permlane16_swap_b32 v109, v111\n"
    "buffer_store_dwordx4 v[108:111], v5, s[44:47], s77 offen sc1\n"
    "s_cmp_eq_u32 s69, 64\n"
    "s_cbranch_scc1 .LrMID_fin_%=\n"
    "s_waitcnt vmcnt(6)\n"
    "v_add_u32 v120, v122, v1\n"
    "v_ashrrev_i32 v120, 1, v120\n"
    "v_cmp_gt_i32 vcc, s69, v120\n"
    "s_cbranch_vccnz .LrMID_idle_%=\n"
    "buffer_load_dwordx4 v[32:35], v2, s[36:39], s71 offen sc1\n"
    "buffer_load_dwordx4 v[36:39], v2, s[36:39], s71 offen offset:512 sc1\n"
    "buffer_load_dwordx4 v[40:43], v2, s[36:39], s71 offen offset:2048 sc1\n"
    "buffer_load_dwordx4 v[44:47], v2, s[36:39], s71 offen offset:2560 sc1\n"
    "buffer_load_dwordx4 v[48:51], v2, s[36:39], s72 offen sc1\n"
    "buffer_load_dwordx4 v[52:55], v2, s[36:39], s72 offen offset:512 sc1\n"
    "buffer_load_dwordx4 v[56:59], v2, s[36:39], s72 offen offset:2048 sc1\n"
    "buffer_load_dwordx4 v[60:63], v2, s[36:39], s72 offen offset:2560 sc1\n"
    "buffer_load_dwordx4 v[64:67], v3, s[36:39], s71 offen sc1\n"
    "buffer_load_dwordx4 v[68:71], v6, s[40:43], s73 offen sc1\n"
    "buffer_load_dwordx4 v[72:75], v6, s[40:43], s74 offen sc1\n"
    "buffer_load_dwordx4 v[76:79], v7, s[40:43], s73 offen sc1\n"
    "s_waitcnt vmcnt(12)\n"
    "v_lshl_or_b32 v14, s68, 1, 1\n"
    "s_mov_b64 exec, s[64:65]\n"
    "buffer_atomic_smax v14, v13, s[36:39], 0 offen sc1\n"
    "s_mov_b64 exec, -1\n"
    "s_branch .LrMID_top_%=\n"
    ".LrMID_LOOPE_%=:\n"
    ".LrMID_fin_%=:\n"
    "s_waitcnt vmcnt(0)\n"
    "s_mov_b32 s68, 63\n"
    "v_lshl_or_b32 v14, s68, 1, 1\n"
    "s_mov_b64 exec, s[64:65]\n"
    "buffer_atomic_smax v14, v13, s[36:39], 0 offen sc1\n"
    "s_mov_b64 exec, -1\n"
    "s_waitcnt vmcnt(0)\n"
    "s_barrier\n"
    "s_branch .LrMID_FIN_%=\n"
    ".LrMID_idle_%=:\n"
    "v_add_u32 v120, v123, v1\n"
    "v_ashrrev_i32 v120, 1, v120\n"
    "v_cmp_gt_i32 vcc, s69, v120\n"
    "s_nop 0\n"
    "s_or_b64 exec, vcc, s[50:51]\n"
    "buffer_atomic_smax v123, v0, s[36:39], 0 offen sc0 sc1\n"
    "s_mov_b64 exec, -1\n"
    "s_waitcnt vmcnt(1)\n"
    "v_lshl_or_b32 v14, s68, 1, 1\n"
    "s_mov_b64 exec, s[64:65]\n"
    "buffer_atomic_smax v14, v13, s[36:39], 0 offen sc1\n"
    "s_mov_b64 exec, -1\n"
    ".LrMID_block_%=:\n"
    "v_add_u32 v120, v122, v1\n"
    "v_ashrrev_i32 v120, 1, v120\n"
    "v_cmp_gt_i32 vcc, s69, v120\n"
    "s_nop 0\n"
    "s_or_b64 exec, vcc, s[50:51]\n"
    "buffer_atomic_smax v122, v0, s[36:39], 0 offen sc0 sc1\n"
    "s_mov_b64 exec, -1\n"
    "s_sleep 3\n"
    "s_waitcnt vmcnt(1)\n"
    "v_add_u32 v120, v123, v1\n"
    "v_ashrrev_i32 v120, 1, v120\n"
    "v_cmp_gt_i32 vcc, s69, v120\n"
    "s_cbranch_vccz .LrMID_go_%=\n"
    "v_add_u32 v120, v123, v1\n"
    "v_ashrrev_i32 v120, 1, v120\n"
    "v_cmp_gt_i32 vcc, s69, v120\n"
    "s_nop 0\n"
    "s_or_b64 exec, vcc, s[50:51]\n"
    "buffer_atomic_smax v123, v0, s[36:39], 0 offen sc0 sc1\n"
    "s_mov_b64 exec, -1\n"
    "s_sleep 3\n"
    "s_waitcnt vmcnt(1)\n"
    "v_add_u32 v120, v122, v1\n"
    "v_ashrrev_i32 v120, 1, v120\n"
    "v_cmp_gt_i32 vcc, s69, v120\n"
    "s_cbranch_vccz .LrMID_go_%=\n"
    "s_sub_u32 s70, s70, 1\n"
    "s_cbranch_scc0 .LrMID_block_%=\n"
    "s_mov_b32 s70, 0\n"
    ".LrMID_go_%=:\n"
    "s_and_b32 s71, s69, 7\n"
    "s_mul_i32 s71, s71, 4194304\n"
    "s_add_u32 s71, s71, s52\n"
    "s_add_u32 s72, s71, 4096\n"
    "s_and_b32 s73, s69, s55\n"
    "s_mul_i32 s73, s73, s54\n"
    "s_add_u32 s73, s73, s53\n"
    "s_add_u32 s74, s73, s56\n"
    "buffer_load_dwordx4 v[32:35], v2, s[36:39], s71 offen sc1\n"
    "buffer_load_dwordx4 v[36:39], v2, s[36:39], s71 offen offset:512 sc1\n"
    "buffer_load_dwordx4 v[40:43], v2, s[36:39], s71 offen offset:2048 sc1\n"
    "buffer_load_dwordx4 v[44:47], v2, s[36:39], s71 offen offset:2560 sc1\n"
    "buffer_load_dwordx4 v[48:51], v2, s[36:39], s72 offen sc1\n"
    "buffer_load_dwordx4 v[52:55], v2, s[36:39], s72 offen offset:512 sc1\n"
    "buffer_load_dwordx4 v[56:59], v2, s[36:39], s72 offen offset:2048 sc1\n"
    "buffer_load_dwordx4 v[60:63], v2, s[36:39], s72 offen offset:2560 sc1\n"
    "buffer_load_dwordx4 v[64:67], v3, s[36:39], s71 offen sc1\n"
    "buffer_load_dwordx4 v[68:71], v6, s[40:43], s73 offen sc1\n"
    "buffer_load_dwordx4 v[72:75], v6, s[40:43], s74 offen sc1\n"
    "buffer_load_dwordx4 v[76:79], v7, s[40:43], s73 offen sc1\n"
    "buffer_load_dword v121, v7, s[40:43], s73 offen\n"
    "s_branch .LrMID_top_%=\n"
    ".LrMID_FIN_%=:\n"
  );
}
DEVI void role_fin(const Params& p, int q, u32 dbg, u64 tag) {
  v16u sa, sb, va, vb;
  red_args<2>(p, 1, q, dbg, tag, sa, sb, va, vb);
  RUN_RED(
    "s_nop 4\n"
    "s_mov_b32 s50, 0\n"
    "s_mov_b32 s51, 0x80000000\n"
    "s_getpc_b64 s[84:85]\n"
    ".LrFIN_PC_%=:\n"
    "s_and_b32 s85, s85, 0xffff\n"
    "s_mov_b32 s86, .LrFIN_FIN_%=-.LrFIN_PC_%=\n"
    "s_mov_b32 s87, 0x20000\n"
    "v_mbcnt_lo_u32_b32 v134, -1, 0\n"
    "v_mbcnt_hi_u32_b32 v134, -1, v134\n"
    "v_lshlrev_b32 v134, 7, v134\n"
    "buffer_load_dword v135, v134, s[84:87], 0 offen\n"
    "v_add_u32 v134, 8192, v134\n"
    "buffer_load_dword v135, v134, s[84:87], 0 offen\n"
    "s_mov_b32 s69, 0\n"
    "v_mov_b32 v122, -1\n"
    "v_mov_b32 v123, -1\n"
    "s_mov_b32 s70, 262144\n"
    ".LrFIN_par_%=:\n"
    "buffer_load_dwordx2 v[14:15], off, s[36:39], 0 sc1\n"
    "s_waitcnt vmcnt(0)\n"
    "v_readfirstlane_b32 s75, v14\n"
    "v_readfirstlane_b32 s76, v15\n"
    "s_xor_b32 s75, s75, s48\n"
    "s_xor_b32 s76, s76, s49\n"
    "s_cselect_b32 s76, 2, 0\n"
    "s_or_b32 s76, s76, s75\n"
    "s_cmp_lt_u32 s76, 2\n"
    "s_cbranch_scc1 .LrFIN_par1_%=\n"
    "s_sleep 4\n"
    "s_sub_u32 s70, s70, 1\n"
    "s_cbranch_scc0 .LrFIN_par_%=\n"
    "s_mov_b32 s70, 0\n"
    ".LrFIN_par1_%=:\n"
    "s_and_b32 s75, s75, 1\n"
    "s_lshl_b32 s75, s75, 13\n"
    "s_add_u32 s36, s36, s75\n"
    "s_addc_u32 s37, s37, 0\n"
    "s_and_b32 s76, s75, s61\n"
    "s_add_u32 s40, s40, s76\n"
    "s_addc_u32 s41, s41, 0\n"
    "s_and_b32 s76, s75, s63\n"
    "s_add_u32 s44, s44, s76\n"
    "s_addc_u32 s45, s45, 0\n"
    "s_branch .LrFIN_block_%=\n"
    ".LrFIN_LOOPB_%=:\n"
    ".LrFIN_top_%=:\n"
    "s_mov_b32 s68, s69\n"
    "s_add_u32 s69, s68, 1\n"
    "s_and_b32 s75, s68, s58\n"
    "s_mul_i32 s77, s75, 36864\n"
    "s_add_u32 s77, s77, s59\n"
    "s_add_u32 s78, s77, s60\n"
    "s_and_b32 s71, s69, 7\n"
    "s_mul_i32 s71, s71, 4194304\n"
    "s_add_u32 s71, s71, s52\n"
    "s_add_u32 s72, s71, 4096\n"
    "s_and_b32 s73, s69, s55\n"
    "s_mul_i32 s73, s73, s54\n"
    "s_add_u32 s73, s73, s53\n"
    "s_add_u32 s74, s73, s56\n"
    "s_waitcnt vmcnt(11)\n"
    "v_mfma_f32_16x16x32_bf16 v[80:83], v[16:19], v[32:35], 0\n"
    "s_waitcnt vmcnt(10)\n"
    "v_mfma_f32_16x16x32_bf16 v[80:83], v[20:23], v[36:39], v[80:83]\n"
    "s_waitcnt vmcnt(9)\n"
    "v_mfma_f32_16x16x32_bf16 v[84:87], v[16:19], v[40:43], 0\n"
    "s_waitcnt vmcnt(8)\n"
    "v_mfma_f32_16x16x32_bf16 v[84:87], v[20:23], v[44:47], v[84:87]\n"
    "s_waitcnt vmcnt(7)\n"
    "v_mfma_f32_16x16x32_bf16 v[88:91], v[16:19], v[48:51], 0\n"
    "s_waitcnt vmcnt(6)\n"
    "v_mfma_f32_16x16x32_bf16 v[88:91], v[20:23], v[52:55], v[88:91]\n"
    "s_waitcnt vmcnt(5)\n"
    "v_mfma_f32_16x16x32_bf16 v[92:95], v[16:19], v[56:59], 0\n"
    "s_waitcnt vmcnt(4)\n"
    "v_mfma_f32_16x16x32_bf16 v[92:95], v[20:23], v[60:63], v[92:95]\n"
    "s_waitcnt vmcnt(3)\n"
    "v_mfma_f32_16x16x32_bf16 v[96:99], v[16:19], v[64:67], 0\n"
    "s_waitcnt vmcnt(2)\n"
    "v_mfma_f32_16x16x32_bf16 v[80:83], v[24:27], v[68:71], v[80:83]\n"
    "v_mfma_f32_16x16x32_bf16 v[84:87], v[28:31], v[68:71], v[84:87]\n"
    "s_waitcnt vmcnt(1)\n"
    "v_mfma_f32_16x16x32_bf16 v[88:91], v[24:27], v[72:75], v[88:91]\n"
    "v_mfma_f32_16x16x32_bf16 v[92:95], v[28:31], v[72:75], v[92:95]\n"
    "s_waitcnt vmcnt(0)\n"
    "v_mfma_f32_16x16x32_bf16 v[96:99], v[24:27], v[76:79], v[96:99]\n"
    "v_add_u32 v120, v122, v1\n"
    "v_ashrrev_i32 v120, 1, v120\n"
    "v_cmp_gt_i32 vcc, s69, v120\n"
    "s_nop 0\n"
    "s_or_b64 exec, vcc, s[50:51]\n"
    "buffer_atomic_smax v122, v0, s[36:39], 0 offen sc0 sc1\n"
    "s_mov_b64 exec, -1\n"
    "v_lshl_or_b32 v14, s68, 1, 1\n"
    "s_mov_b64 exec, s[64:65]\n"
    "buffer_atomic_smax v14, v13, s[36:39], 0 offen sc1\n"
    "s_mov_b64 exec, -1\n"
    "v_cvt_pk_bf16_f32 v100, v80, v81\n"
    "v_cvt_pk_bf16_f32 v101, v82, v83\n"
    "v_cvt_pk_bf16_f32 v102, v84, v85\n"
    "v_cvt_pk_bf16_f32 v103, v86, v87\n"
    "v_cvt_pk_bf16_f32 v104, v88, v89\n"
    "v_cvt_pk_bf16_f32 v105, v90, v91\n"
    "v_cvt_pk_bf16_f32 v106, v92, v93\n"
    "v_cvt_pk_bf16_f32 v107, v94, v95\n"
    "v_cvt_pk_bf16_f32 v108, v96, v97\n"
    "v_cvt_pk_bf16_f32 v109, v98, v99\n"
    "v_permlane16_swap_b32 v100, v102\n"
    "v_permlane16_swap_b32 v101, v103\n"
    "ds_write_b128 v10, v[100:103]\n"
    "v_permlane16_swap_b32 v104, v106\n"
    "v_permlane16_swap_b32 v105, v107\n"
    "ds_write_b128 v10, v[104:107] offset:128\n"
    "v_permlane16_swap_b32 v108, v110\n"
    "v_permlane16_swap_b32 v109, v111\n"
    "ds_write_b128 v12, v[108:111]\n"
    "v_xor_b32 v10, 16384, v10\n"
    "v_xor_b32 v12, 16384, v12\n"
    "s_waitcnt lgkmcnt(0)\n"
    "s_barrier\n"
    "ds_read_b128 v[100:103], v11 offset:0\n"
    "ds_read_b128 v[104:107], v11 offset:4096\n"
    "ds_read_b128 v[108:111], v11 offset:8192\n"
    "v_xor_b32 v11, 16384, v11\n"
    "s_waitcnt lgkmcnt(2)\n"
    "buffer_store_dwordx4 v[100:103], v4, s[44:47], s77 offen sc1\n"
    "s_waitcnt lgkmcnt(1)\n"
    "buffer_store_dwordx4 v[104:107], v4, s[44:47], s78 offen sc1\n"
    "s_waitcnt lgkmcnt(0)\n"
    "buffer_store_dwordx4 v[108:111], v5, s[44:47], s77 offen sc1\n"
    "s_cmp_eq_u32 s69, 64\n"
    "s_cbranch_scc1 .LrFIN_fin_%=\n"
    "s_waitcnt vmcnt(4)\n"
    "v_add_u32 v120, v122, v1\n"
    "v_ashrrev_i32 v120, 1, v120\n"
    "v_cmp_gt_i32 vcc, s69, v120\n"
    "s_cbranch_vccnz .LrFIN_idle_%=\n"
    "buffer_load_dwordx4 v[32:35], v2, s[36:39], s71 offen sc1\n"
    "buffer_load_dwordx4 v[36:39], v2, s[36:39], s71 offen offset:512 sc1\n"
    "buffer_load_dwordx4 v[40:43], v2, s[36:39], s71 offen offset:2048 sc1\n"
    "buffer_load_dwordx4 v[44:47], v2, s[36:39], s71 offen offset:2560 sc1\n"
    "buffer_load_dwordx4 v[48:51], v2, s[36:39], s72 offen sc1\n"
    "buffer_load_dwordx4 v[52:55], v2, s[36:39], s72 offen offset:512 sc1\n"
    "buffer_load_dwordx4 v[56:59], v2, s[36:39], s72 offen offset:2048 sc1\n"
    "buffer_load_dwordx4 v[60:63], v2, s[36:39], s72 offen offset:2560 sc1\n"
    "buffer_load_dwordx4 v[64:67], v3, s[36:39], s71 offen sc1\n"
    "buffer_load_dwordx4 v[68:71], v6, s[40:43], s73 offen sc1\n"
    "buffer_load_dwordx4 v[72:75], v6, s[40:43], s74 offen sc1\n"
    "buffer_load_dwordx4 v[76:79], v7, s[40:43], s73 offen sc1\n"
    "s_branch .LrFIN_top_%=\n"
    ".LrFIN_LOOPE_%=:\n"
    ".LrFIN_fin_%=:\n"
    "s_waitcnt vmcnt(0)\n"
    "s_branch .LrFIN_FIN_%=\n"
    ".LrFIN_idle_%=:\n"
    ".LrFIN_block_%=:\n"
    "v_add_u32 v120, v122, v1\n"
    "v_ashrrev_i32 v120, 1, v120\n"
    "v_cmp_gt_i32 vcc, s69, v120\n"
    "s_nop 0\n"
    "s_or_b64 exec, vcc, s[50:51]\n"
    "buffer_atomic_smax v122, v0, s[36:39], 0 offen sc0 sc1\n"
    "s_mov_b64 exec, -1\n"
    "s_sleep 3\n"
    "s_waitcnt vmcnt(1)\n"
    "v_add_u32 v120, v123, v1\n"
    "v_ashrrev_i32 v120, 1, v120\n"
    "v_cmp_gt_i32 vcc, s69, v120\n"
    "s_cbranch_vccz .LrFIN_go_%=\n"
    "v_add_u32 v120, v123, v1\n"
    "v_ashrrev_i32 v120, 1, v120\n"
    "v_cmp_gt_i32 vcc, s69, v120\n"
    "s_nop 0\n"
    "s_or_b64 exec, vcc, s[50:51]\n"
    "buffer_atomic_smax v123, v0, s[36:39], 0 offen sc0 sc1\n"
    "s_mov_b64 exec, -1\n"
    "s_sleep 3\n"
    "s_waitcnt vmcnt(1)\n"
    "v_add_u32 v120, v122, v1\n"
    "v_ashrrev_i32 v120, 1, v120\n"
    "v_cmp_gt_i32 vcc, s69, v120\n"
    "s_cbranch_vccz .LrFIN_go_%=\n"
    "s_sub_u32 s70, s70, 1\n"
    "s_cbranch_scc0 .LrFIN_block_%=\n"
    "s_mov_b32 s70, 0\n"
    ".LrFIN_go_%=:\n"
    "s_and_b32 s71, s69, 7\n"
    "s_mul_i32 s71, s71, 4194304\n"
    "s_add_u32 s71, s71, s52\n"
    "s_add_u32 s72, s71, 4096\n"
    "s_and_b32 s73, s69, s55\n"
    "s_mul_i32 s73, s73, s54\n"
    "s_add_u32 s73, s73, s53\n"
    "s_add_u32 s74, s73, s56\n"
    "buffer_load_dwordx4 v[32:35], v2, s[36:39], s71 offen sc1\n"
    "buffer_load_dwordx4 v[36:39], v2, s[36:39], s71 offen offset:512 sc1\n"
    "buffer_load_dwordx4 v[40:43], v2, s[36:39], s71 offen offset:2048 sc1\n"
    "buffer_load_dwordx4 v[44:47], v2, s[36:39], s71 offen offset:2560 sc1\n"
    "buffer_load_dwordx4 v[48:51], v2, s[36:39], s72 offen sc1\n"
    "buffer_load_dwordx4 v[52:55], v2, s[36:39], s72 offen offset:512 sc1\n"
    "buffer_load_dwordx4 v[56:59], v2, s[36:39], s72 offen offset:2048 sc1\n"
    "buffer_load_dwordx4 v[60:63], v2, s[36:39], s72 offen offset:2560 sc1\n"
    "buffer_load_dwordx4 v[64:67], v3, s[36:39], s71 offen sc1\n"
    "buffer_load_dwordx4 v[68:71], v6, s[40:43], s73 offen sc1\n"
    "buffer_load_dwordx4 v[72:75], v6, s[40:43], s74 offen sc1\n"
    "buffer_load_dwordx4 v[76:79], v7, s[40:43], s73 offen sc1\n"
    "s_branch .LrFIN_top_%=\n"
    ".LrFIN_FIN_%=:\n"
  );
}

__global__ void __launch_bounds__(256, 1) mega(Params p) {
  extern __shared__ __attribute__((aligned(16))) unsigned char smem[];
  const rsrc_t rw = __builtin_amdgcn_make_buffer_rsrc(p.ws, 0, WS_END + 8192, 0x00020000);
  const unsigned long long t00 = __builtin_amdgcn_s_memrealtime();
  if (threadIdx.x < 64) {
    const int lane = threadIdx.x, b = blockIdx.x;
    if (p.role_mask & 0x40000) st16(rw, (v4u){~0u, ~0u, ~0u, ~0u}, lane < 22 ? CHUNK + (u32)b * 352 + lane * 16 : OOB, 0);
    const u64 tg = ld8u(rw, EPOCH) + 1;
    drain();
    if (lane == 0) {
      {
        const v2u tv = {(u32)tg, (u32)(tg >> 32)};
        const u32 a = READY + (u32)b * 8;
        asm volatile("buffer_atomic_swap_x2 %0, %1, %2, 0 offen sc1" :: "v"(tv), "v"(a), "s"(rw) : "memory");
      }
      int rid = (int)(__builtin_amdgcn_s_getreg(20 | (3 << 11)) & 7u) * 32 + (b >> 3);
#pragma unroll 1
      for (int k = 0; k < 256; ++k) {
        if (__hip_atomic_exchange((u64*)(p.ws + CLAIM) + ((rid & 31) * 8 + (rid >> 5)), tg, __ATOMIC_RELAXED, __HIP_MEMORY_SCOPE_AGENT) != tg) break;
        rid = (rid + 1) & 255;
      }
      *(volatile v4u*)smem = (v4u){(u32)rid, (u32)tg, (u32)(tg >> 32), 0u};
    }
  }
  int rid = (int)(__builtin_amdgcn_s_getreg(20 | (3 << 11)) & 7u) * 32 + (int)(blockIdx.x >> 3);
  u64 tag = 0;
  bool sure = false;
  for (;;) {
  if (!sure && (rid >= 128 || (rid & 31) >= SL_ATT)) { confirm(rid, tag, sure, smem); continue; }
  const int L = rid >> 7, j = (rid >> 5) & 3, sl = rid & 31;
  const u32 dbg = OFF_DBG + (u32)rid * 66 * 256;
  auto on = [&](int r) { return ((p.role_mask >> (2 + 8 * L + r)) & 1) != 0; };
  STAMP(65, 8);
  STAMP(65 + 32, 9);
  if (((PULL >> L) & 1) && sl >= SL_WO && sl < SL_REDA) pull_weights(p, j, sl - SL_WO);
  if (sl < SL_P) { delay_start(L ? DL_R1 : 0, t00); if (on(1)) { role_rec(p, L, j, sl, dbg, rid, tag, sure, smem); continue; } if (!confirm(rid, tag, sure, smem)) continue; }
  else if (sl < SL_ATT) { delay_start(L ? DL_R1 : 0, t00); if (on(0)) { role_p(p, L, j, sl - SL_P, dbg, rid, tag, sure, smem); continue; } if (!confirm(rid, tag, sure, smem)) continue; }
  else if (sl < SL_WO) {
    const u32 shift = find_shift(rw, rid == 128 + SL_ATT && threadIdx.x < 64, tag);
    if (p.role_mask & 2) hot_init(p, rw, L, 2 * j + sl - SL_ATT, shift);
    if (on(2)) role_att(p, L, j, sl - SL_ATT, dbg, shift);
  }
  else if (sl < SL_UP) { delay_start(L ? DL_W1 : DL_W0, t00); if (on(3)) role_wo(p, L, j, sl - SL_WO, dbg, tag); }
  else if (sl < SL_DOWN) { delay_start(L ? DL_U1 : DL_U0, t00); if (on(5)) role_up(p, L, j, sl - SL_UP, dbg, tag); }
  else if (sl < SL_REDA) { delay_start(L ? DL_D1 : DL_D0, t00); if (on(6)) role_down(p, L, j, sl - SL_DOWN, dbg, tag); }
  else {
    if (sl == SL_REDA && (p.role_mask & 2)) hot_init(p, rw, L, 8 + j, find_shift(rw, false, tag));
    if ((p.role_mask & 1) && (L == 1 || sl == SL_REDF)) role_ln0(p, L == 0 ? 2 : (sl == SL_REDA ? 1 : 0), j, dbg, tag);
    if (sl == SL_REDF && L == 1) {
      if (on(7)) role_fin(p, j, dbg, tag);
    } else {
      v16u sa, sb, va, vb;
      if (sl == SL_REDA) red_args<1>(p, L, j, dbg, tag, sa, sb, va, vb);
      else red_args<2>(p, 0, j, dbg, tag, sa, sb, va, vb);
      if (on(sl == SL_REDA ? 4 : 7)) role_mid(sa, sb, va, vb);
    }
  }
  break;
  }
}

static void fill_params(Params& p, void* const* d_in, void* d_out, void* d_ws) {
  p.x = (const u16*)d_in[0]; p.mem0 = (const u16*)d_in[1];
  p.Wq = (const u16*)d_in[2]; p.Wk = (const u16*)d_in[3]; p.Wv = (const u16*)d_in[4]; p.Wo = (const u16*)d_in[5];
  p.w1 = (const u16*)d_in[8]; p.w3 = (const u16*)d_in[9]; p.w2 = (const u16*)d_in[10];
  p.out = (u16*)d_out; p.ws = (unsigned char*)d_ws;
}

extern "C" void kernel_launch(void* const* d_in, const int* in_sizes, int n_in, void* d_out, int out_size, void* d_ws,
                              size_t ws_size, hipStream_t stream) {
  Params p;
  fill_params(p, d_in, d_out, d_ws);
  (void)hipFuncSetAttribute((const void*)mega, hipFuncAttributeMaxDynamicSharedMemorySize, LDS_BYTES);
  p.role_mask = 0x7FFFF; p.unused = 0;
  mega<<<256, 256, LDS_BYTES, stream>>>(p);
}
```

```cpp
#include <hip/hip_runtime.h>
#include <stdint.h>
#include <type_traits>
#include <utility>

typedef unsigned short u16;
typedef unsigned int u32;
typedef short bf16x8 __attribute__((ext_vector_type(8)));
typedef short bf16x4 __attribute__((ext_vector_type(4)));
typedef float f32x4 __attribute__((ext_vector_type(4)));
typedef float f32x2 __attribute__((ext_vector_type(2)));
typedef __bf16 bf2_t __attribute__((ext_vector_type(2)));
typedef unsigned v16u __attribute__((ext_vector_type(16)));
typedef unsigned v8u __attribute__((ext_vector_type(8)));
typedef unsigned v4u __attribute__((ext_vector_type(4)));
typedef unsigned v2u __attribute__((ext_vector_type(2)));
typedef __amdgpu_buffer_rsrc_t rsrc_t;
#define DEVI __device__ __forceinline__

static constexpr int NSEG = 64;
static constexpr int WARM = 1, PSLOW = 1, PSKIP = 2, PULL = 1, RQ = 8, RO = 8, RP = 8, RH = 8, RX = 8, RX2 = 16, LN_B = 28, LN_C = 52, PS_ATT = 3, PS_RED = 3, DL_R1 = 12, DL_W0 = 5, DL_W1 = 14, DL_U0 = 8, DL_U1 = 3, DL_D0 = 11, DL_D1 = 5;

static constexpr u32 PARW = 0, EPOCH = 128, READY = 2048, CLAIM = 4096;
static constexpr u32 CHUNK = 16384;
static constexpr u32 MBOX = CHUNK;
static constexpr u32 INITF = 5 * CHUNK;
static constexpr u32 CTRL_BYTES = 6 * CHUNK + 8192;
static constexpr u32 HOT = 6 * CHUNK, HOT2 = 2u << 20;
static constexpr u32 SLOT = 4u << 20;
static constexpr u32 PGA = SLOT, PGB = SLOT + (2u << 20);
static constexpr int cmax(int a, int b) { return a > b ? a : b; }
static constexpr int NSLOT = cmax(cmax(cmax(RQ, RO), cmax(RP, RH)), cmax(RX, RX2));
DEVI u32 pga(int L, int blk) { return PGA + (u32)blk * CHUNK + (u32)L * 8192; }
DEVI u32 pgb(int L, int blk) { return PGB + (u32)blk * CHUNK + (u32)(1 - L) * 8192; }
static constexpr int B_QKV = 0, B_KVM = 96;
static constexpr int B_O = 0, B_H = 32, B_PA = 68, B_PF = 88, B_XN2 = 108, B_X1 = 113, B_XN = 118, B_X = 123;
static constexpr u32 OFF_XN0 = SLOT * (1 + NSLOT);
static constexpr u32 OFF_DBG = OFF_XN0 + 320 * CHUNK;
static constexpr u32 WS_END = OFF_DBG + 256u * 66 * 256;
DEVI u32 kb_off(int kb) { return (u32)((kb >> 3) * CHUNK + (kb & 7) * 1024); }
static constexpr u32 OOB = 0x80000000u;
static constexpr int LDS_BYTES = 163840;
static constexpr unsigned SPIN_LIMIT = 1u << 18;
static constexpr int SL_REC = 0, SL_P = 8, SL_ATT = 16, SL_WO = 18, SL_UP = 21, SL_DOWN = 27, SL_REDA = 30, SL_REDF = 31;
DEVI u32 mbox(int L, int j, int role, int slot) { return MBOX + (u32)(j * CHUNK + L * 8192 + role * 256 + 4 * slot); }
DEVI u32 mbox_att(int L, int j, int hl, int slot) { return mbox(L, j, SL_ATT + (hl >> 2), 16 * (hl & 3) + slot); }

struct Params {
  const u16* x; const u16* mem0; const u16* Wq; const u16* Wk; const u16* Wv; const u16* Wo;
  const u16* w1; const u16* w3; const u16* w2; u16* out; unsigned char* ws;
  int role_mask; int unused;
};

template <class F, int... I> DEVI void static_for_impl(F&& f, std::integer_sequence<int, I...>) { (f(std::integral_constant<int, I>{}), ...); }
template <int N, class F> DEVI void static_for(F&& f) { static_for_impl(f, std::make_integer_sequence<int, N>{}); }
DEVI float bflo(u32 d) { return __uint_as_float(d << 16); }
DEVI float bfhi(u32 d) { return __uint_as_float(d & 0xffff0000u); }
DEVI u32 pk2(float a, float b) {
  f32x2 f = {a, b};
  bf2_t r = __builtin_convertvector(f, bf2_t);
  return __builtin_bit_cast(u32, r);
}
DEVI bf16x8 as_frag(v4u v) { return __builtin_bit_cast(bf16x8, v); }
DEVI bf16x4 as_frag4(v2u v) { return __builtin_bit_cast(bf16x4, v); }
DEVI f32x4 mfma32(v4u a, v4u b, f32x4 c) { return __builtin_amdgcn_mfma_f32_16x16x32_bf16(as_frag(a), as_frag(b), c, 0, 0, 0); }
DEVI f32x4 mfma16(v2u a, v2u b, f32x4 c) { return __builtin_amdgcn_mfma_f32_16x16x16bf16_1k(as_frag4(a), as_frag4(b), c, 0, 0, 0); }
DEVI v4u ld16(rsrc_t r, u32 voff, u32 soff) { return __builtin_amdgcn_raw_buffer_load_b128(r, voff, soff, 16); }
DEVI v2u ld8(rsrc_t r, u32 voff, u32 soff) { return __builtin_amdgcn_raw_buffer_load_b64(r, voff, soff, 16); }
DEVI u32 ld4(rsrc_t r, u32 voff, u32 soff) { return __builtin_amdgcn_raw_buffer_load_b32(r, voff, soff, 16); }
DEVI void st16(rsrc_t r, v4u v, u32 voff, u32 soff) { __builtin_amdgcn_raw_buffer_store_b128(v, r, voff, soff, 16); }
DEVI void st8(rsrc_t r, v2u v, u32 voff, u32 soff) { __builtin_amdgcn_raw_buffer_store_b64(v, r, voff, soff, 16); }
DEVI void st4(rsrc_t r, u32 v, u32 voff, u32 soff) { __builtin_amdgcn_raw_buffer_store_b32(v, r, voff, soff, 16); }
DEVI void drain() { asm volatile("s_waitcnt vmcnt(0)" ::: "memory"); }
DEVI void cbar() { asm volatile("" ::: "memory"); }
DEVI void sbar() { __builtin_amdgcn_sched_barrier(0); }
DEVI u32 rfl(u32 v) { return (u32)__builtin_amdgcn_readfirstlane((int)v); }
DEVI float xmax16_32(float x) {
  u32 a = __float_as_uint(x);
  auto r = __builtin_amdgcn_permlane16_swap(a, a, false, false);
  x = fmaxf(__uint_as_float(r[0]), __uint_as_float(r[1]));
  a = __float_as_uint(x);
  auto q = __builtin_amdgcn_permlane32_swap(a, a, false, false);
  return fmaxf(__uint_as_float(q[0]), __uint_as_float(q[1]));
}
DEVI float xsum16_32(float x) {
  u32 a = __float_as_uint(x);
  auto r = __builtin_amdgcn_permlane16_swap(a, a, false, false);
  x = __uint_as_float(r[0]) + __uint_as_float(r[1]);
  a = __float_as_uint(x);
  auto q = __builtin_amdgcn_permlane32_swap(a, a, false, false);
  return __uint_as_float(q[0]) + __uint_as_float(q[1]);
}
DEVI float ror8(float x) { return __int_as_float(__builtin_amdgcn_update_dpp(0, __float_as_int(x), 0x128, 0xf, 0xf, false)); }
DEVI v4u swap16(v2u a, v2u b) {
  auto s0 = __builtin_amdgcn_permlane16_swap(a.x, b.x, false, false);
  auto s1 = __builtin_amdgcn_permlane16_swap(a.y, b.y, false, false);
  return (v4u){s0[0], s1[0], s0[1], s1[1]};
}

#define STAMP(row, slot) do { } while (0)

DEVI bool flags_ok(int v, int bias, int s) {
  asm volatile("" : "+v"(v));
  return __all(v + bias >= s);
}
template <int SLP> DEVI void wait_flags(rsrc_t r, u32 addr, int bias, int s, unsigned& budget) {
  for (;;) {
    cbar();
    const int v = (int)ld4(r, addr, 0);
    if (flags_ok(v, bias, s) || budget == 0) break;
    --budget;
    __builtin_amdgcn_s_sleep(SLP);
  }
  cbar();
}
DEVI void publish(rsrc_t r, u32 flag, int s, int lane) {
  drain();
  if (lane == 0) st4(r, (u32)s, flag, 0);
  cbar();
}
DEVI void delay_start(int us, unsigned long long t0) {
  if (us == 0) return;
  while (__builtin_amdgcn_s_memrealtime() - t0 < (unsigned long long)us * 100u) __builtin_amdgcn_s_sleep(16);
}
DEVI void pull_weights(const Params& p, int j, int n) {
  const int tid = threadIdx.x;
  u32 acc = 0;
  {
    const u16* w = n % 3 == 0 ? p.Wq : (n % 3 == 1 ? p.Wk : p.Wv);
    const rsrc_t r = __builtin_amdgcn_make_buffer_rsrc((void*)(w + j * 1024), 0, 576 * 8192 - j * 2048, 0x00020000);
    if (PSLOW) {
#pragma unroll
      for (int k = 0; k < 3; ++k) {
        const int row = (k * 4 + n / 3) * 64 + (tid >> 2);
        acc ^= __builtin_amdgcn_raw_buffer_load_b32(r, row < 576 ? (u32)(row * 8192 + 256 + (tid & 1) * 128 + (tid & 2) * 512) : OOB, 0, 0);
      }
    }
#pragma unroll
    for (int k = PSKIP; k < 9; ++k) {
      const int row = (k * 4 + n / 3) * 16 + (tid >> 4);
      acc ^= __builtin_amdgcn_raw_buffer_load_b32(r, (u32)(row * 8192 + (tid & 15) * 128), 0, 0);
    }
  }
  {
    const rsrc_t r = __builtin_amdgcn_make_buffer_rsrc((void*)(p.Wo + (size_t)j * 1024 * 576), 0, 1024 * 1152, 0x00020000);
#pragma unroll
    for (int k = 0; k < 3; ++k) acc ^= __builtin_amdgcn_raw_buffer_load_b32(r, (u32)((k * 12 + n) * 256 + tid) * 128, 0, 0);
  }
  asm volatile("" :: "v"(acc));
}
DEVI v4u make_srd(const void* p, u32 bytes) {
  const unsigned long long a = (unsigned long long)p;
  return (v4u){rfl((u32)a), rfl((u32)(a >> 32) & 0xffffu), bytes, 0x00020000u};
}
DEVI v16u cat4(v4u a, v4u b, v4u c, v4u d) {
  return (v16u){a.x, a.y, a.z, a.w, b.x, b.y, b.z, b.w, c.x, c.y, c.z, c.w, d.x, d.y, d.z, d.w};
}
DEVI u32 wswz(int j, int g) { return (u32)(4 * ((j >> 1) & 1) ^ 2 * (g & 1)); }
DEVI u32 tr_base(int lane, int chunk) {
  const int g = lane >> 4, q = (lane >> 2) & 3;
  return (u32)(g * 1024 + q * 128) + (((u32)chunk ^ wswz(q, g)) << 4);
}

#define RUN_ASM(TEXT) do { for (int i_ = 0; i_ < 16; ++i_) { sa[i_] = rfl(sa[i_]); sb[i_] = rfl(sb[i_]); } \
    asm volatile(TEXT "s_endpgm\n" :: "{s[36:51]}"(sa), "{s[52:67]}"(sb), "{v[0:15]}"(va) : "v255", "a255", "s99", "vcc", "memory"); \
    __builtin_unreachable(); } while (0)

#define GEMM_DECL v4u g_w0, g_w1, g_w2; u32 g_ldw, g_in_base, g_in_stride, g_in_mask, g_out_base, g_out_kb, g_out_mask, g_flag, g_paddr; int g_pbias, g_pinit = -1, g_n0 = 2, g_n1 = 2
#define GEMM_ARGS g_w0, g_w1, g_w2, g_ldw, g_in_base, g_in_stride, g_in_mask, g_out_base, g_out_kb, g_out_mask, g_flag, g_paddr, g_pbias, g_pinit, g_n0, g_n1
DEVI void gemm_args(v4u g_w0, v4u g_w1, v4u g_w2, u32 g_ldw, u32 g_in_base, u32 g_in_stride, u32 g_in_mask, u32 g_out_base, u32 g_out_kb,
                    u32 g_out_mask, u32 g_flag, u32 g_paddr, int g_pbias, int g_pinit, int g_n0, int g_n1, const Params& p, u32 dbg, unsigned long long tag, v16u& sa, v16u& sb, v16u& va) {
  const int tid = threadIdx.x, lane = tid & 63, w = (int)rfl(tid >> 6);
  const int c = lane & 15, gg = lane >> 4, pp = lane & 3;
  sa = cat4(make_srd(p.ws, WS_END), g_w0, g_w1, g_w2);
  sb = (v16u){g_in_base, g_in_stride, g_in_mask, g_out_base, SLOT, g_out_mask, 0, (u32)w * 1024, dbg + w * 64, 0, 0, 0x80000000u, 0, 0, (u32)(2 * tag), (u32)((2 * tag) >> 32)};
  va[0] = lane * 16;
  va[1] = lane * 16 + w * 2048;
  va[2] = lane == 63 ? OOB : g_paddr;
  va[3] = (u32)g_pbias;
  va[4] = (gg & 1) ? OOB : kb_off((int)g_out_kb + 8 + 2 * (w >> 1)) + (u32)(((2 * (w & 1) + (gg >> 1)) * 16 + c) * 16);
  va[5] = (u32)(8 * w + (lane >> 3)) * g_ldw + ((((u32)lane & 7) ^ wswz(lane >> 3, w)) << 4);
  va[6] = (u32)(w >> 1) * 4096 + tr_base(lane, 4 * (w & 1) + pp);
  va[7] = 2 * 4096 + tr_base(lane, 2 * w + (pp >> 1)) + 8 * (pp & 1);
  for (int i = 8; i < 16; ++i) va[i] = 0;
  va[8] = w < 2 ? va[1] : OOB;
  va[11] = (u32)g_pinit;
  va[14] = lane < g_n0 ? g_flag : OOB;
  va[10] = lane < g_n1 ? OOB : g_flag;
  va[15] = kb_off((int)g_out_kb + 2 * w) + lane * 16;
}

DEVI bool confirm(int& rid, unsigned long long& tag, bool& sure, unsigned char* smem) {
  if (sure) return true;
  asm volatile("s_waitcnt lgkmcnt(0)\n\ts_barrier" ::: "memory");
  const v4u boot = *(volatile v4u*)smem;
  const int r = (int)rfl(boot.x);
  tag = (unsigned long long)rfl(boot.y) | ((unsigned long long)rfl(boot.z) << 32);
  asm volatile("s_waitcnt lgkmcnt(0)\n\ts_barrier" ::: "memory");
  const bool ok = r == rid;
  rid = r; sure = true;
  return ok;
}
DEVI void role_p(const Params& p, int L, int j, int i, u32 dbg, int& rid, unsigned long long& tag, bool& sure, unsigned char* smem) {
  const int lane = threadIdx.x & 63, w = threadIdx.x >> 6, h = 8 * j + i;
  const u32 wb = 576u * 4096 * 2 - h * 256;
  GEMM_DECL;
  g_w0 = make_srd(p.Wq + h * 128, wb); g_w1 = make_srd(p.Wk + h * 128, wb); g_w2 = make_srd(p.Wv + h * 128, wb);
  g_ldw = 8192;
  g_in_base = L ? pgb(1, B_XN) : OFF_XN0; g_in_stride = L ? SLOT : 5 * CHUNK; g_in_mask = L ? RX2 - 1 : 63;
  g_out_base = pga(L, B_QKV + 3 * h); g_out_kb = 0; g_out_mask = RQ - 1;
  g_flag = lane == 0 ? mbox(L, j, SL_REC + i, w) : (lane == 1 ? mbox_att(L, j, i, 8 + w) : OOB);
  g_n0 = 0; g_n1 = 0;
  g_paddr = mbox(L, j, SL_P + i, lane);
  g_pbias = lane < (L ? 16 : 48) ? (lane >> 3) & 1 : (lane == 48 ? 2 * RQ : 1000);
  g_pinit = lane < 16 || lane >= 48 ? -1 : (lane < 32 ? 2 * LN_B - 1 : 2 * LN_C - 1);
  v16u sa, sb, va;
  gemm_args(GEMM_ARGS, p, dbg, tag, sa, sb, va);
  asm volatile("" : "+v"(va));
  if (!confirm(rid, tag, sure, smem)) return;
  sb[14] = (u32)(2 * tag); sb[15] = (u32)((2 * tag) >> 32);
  RUN_ASM(
    "s_nop 4\n"
    "s_getpc_b64 s[84:85]\n"
    ".LgP_PC_%=:\n"
    "s_and_b32 s85, s85, 0xffff\n"
    "s_mov_b32 s86, .LgP_FIN_%=-.LgP_PC_%=\n"
    "s_mov_b32 s87, 0x20000\n"
    "v_mbcnt_lo_u32_b32 v40, -1, 0\n"
    "v_mbcnt_hi_u32_b32 v40, -1, v40\n"
    "v_lshlrev_b32 v40, 7, v40\n"
    "buffer_load_dword v41, v40, s[84:87], 0 offen\n"
    "v_add_u32 v40, 8192, v40\n"
    "buffer_load_dword v41, v40, s[84:87], 0 offen\n"
    "v_add_u32 v40, 8192, v40\n"
    "buffer_load_dword v41, v40, s[84:87], 0 offen\n"
    "s_mov_b32 s68, 0\n"
    "s_mov_b32 s71, 0\n"
    "v_add_u32 v9, 20480, v0\n"
    "s_mov_b32 s64, 0xbfb8aa3b\n"
    "s_mov_b32 s72, 262144\n"
    "s_add_u32 m0, s59, 0\n"
    "s_mov_b32 s74, 0\n"
    "buffer_load_dwordx4 v5, s[40:43], s74 offen lds\n"
    "s_add_u32 m0, s59, 4096\n"
    "s_mov_b32 s74, 128\n"
    "buffer_load_dwordx4 v5, s[40:43], s74 offen lds\n"
    "s_add_u32 m0, s59, 8192\n"
    "s_mov_b32 s74, 0\n"
    "buffer_load_dwordx4 v5, s[44:47], s74 offen lds\n"
    "s_add_u32 m0, s59, 12288\n"
    "s_mov_b32 s74, 128\n"
    "buffer_load_dwordx4 v5, s[44:47], s74 offen lds\n"
    "s_add_u32 m0, s59, 16384\n"
    "s_mov_b32 s74, 0\n"
    "buffer_load_dwordx4 v5, s[48:51], s74 offen lds\n"
    "s_add_u32 m0, s59, 20480\n"
    "s_mov_b32 s74, 128\n"
    "buffer_load_dwordx4 v5, s[48:51], s74 offen lds\n"
    "s_add_u32 m0, s59, 24576\n"
    "s_mov_b32 s74, 262144\n"
    "buffer_load_dwordx4 v5, s[40:43], s74 offen lds\n"
    "s_add_u32 m0, s59, 28672\n"
    "s_mov_b32 s74, 262272\n"
    "buffer_load_dwordx4 v5, s[40:43], s74 offen lds\n"
    "s_add_u32 m0, s59, 32768\n"
    "s_mov_b32 s74, 262144\n"
    "buffer_load_dwordx4 v5, s[44:47], s74 offen lds\n"
    "s_add_u32 m0, s59, 36864\n"
    "s_mov_b32 s74, 262272\n"
    "buffer_load_dwordx4 v5, s[44:47], s74 offen lds\n"
    "s_add_u32 m0, s59, 40960\n"
    "s_mov_b32 s74, 262144\n"
    "buffer_load_dwordx4 v5, s[48:51], s74 offen lds\n"
    "s_add_u32 m0, s59, 45056\n"
    "s_mov_b32 s74, 262272\n"
    "buffer_load_dwordx4 v5, s[48:51], s74 offen lds\n"
    "s_add_u32 m0, s59, 49152\n"
    "s_mov_b32 s74, 524288\n"
    "buffer_load_dwordx4 v5, s[40:43], s74 offen lds\n"
    "s_add_u32 m0, s59, 53248\n"
    "s_mov_b32 s74, 524416\n"
    "buffer_load_dwordx4 v5, s[40:43], s74 offen lds\n"
    "s_add_u32 m0, s59, 57344\n"
    "s_mov_b32 s74, 524288\n"
    "buffer_load_dwordx4 v5, s[44:47], s74 offen lds\n"
    "s_add_u32 m0, s59, 61440\n"
    "s_mov_b32 s74, 524416\n"
    "buffer_load_dwordx4 v5, s[44:47], s74 offen lds\n"
    "s_add_u32 m0, s59, 65536\n"
    "s_mov_b32 s74, 524288\n"
    "buffer_load_dwordx4 v5, s[48:51], s74 offen lds\n"
    "s_add_u32 m0, s59, 69632\n"
    "s_mov_b32 s74, 524416\n"
    "buffer_load_dwordx4 v5, s[48:51], s74 offen lds\n"
    "s_add_u32 m0, s59, 73728\n"
    "s_mov_b32 s74, 786432\n"
    "buffer_load_dwordx4 v5, s[40:43], s74 offen lds\n"
    "s_add_u32 m0, s59, 77824\n"
    "s_mov_b32 s74, 786560\n"
    "buffer_load_dwordx4 v5, s[40:43], s74 offen lds\n"
    "s_add_u32 m0, s59, 81920\n"
    "s_mov_b32 s74, 786432\n"
    "buffer_load_dwordx4 v5, s[44:47], s74 offen lds\n"
    "s_add_u32 m0, s59, 86016\n"
    "s_mov_b32 s74, 786560\n"
    "buffer_load_dwordx4 v5, s[44:47], s74 offen lds\n"
    "s_add_u32 m0, s59, 90112\n"
    "s_mov_b32 s74, 786432\n"
    "buffer_load_dwordx4 v5, s[48:51], s74 offen lds\n"
    "s_add_u32 m0, s59, 94208\n"
    "s_mov_b32 s74, 786560\n"
    "buffer_load_dwordx4 v5, s[48:51], s74 offen lds\n"
    "s_add_u32 m0, s59, 98304\n"
    "s_mov_b32 s74, 1048576\n"
    "buffer_load_dwordx4 v5, s[40:43], s74 offen lds\n"
    "s_add_u32 m0, s59, 102400\n"
    "s_mov_b32 s74, 1048704\n"
    "buffer_load_dwordx4 v5, s[40:43], s74 offen lds\n"
    "s_add_u32 m0, s59, 106496\n"
    "s_mov_b32 s74, 1048576\n"
    "buffer_load_dwordx4 v5, s[44:47], s74 offen lds\n"
    "s_add_u32 m0, s59, 110592\n"
    "s_mov_b32 s74, 1048704\n"
    "buffer_load_dwordx4 v5, s[44:47], s74 offen lds\n"
    "s_add_u32 m0, s59, 114688\n"
    "s_mov_b32 s74, 1048576\n"
    "buffer_load_dwordx4 v5, s[48:51], s74 offen lds\n"
    "s_add_u32 m0, s59, 118784\n"
    "s_mov_b32 s74, 1048704\n"
    "buffer_load_dwordx4 v5, s[48:51], s74 offen lds\n"
    "v_add_u32 v16, 49152, v6\n"
    "v_add_u32 v17, 98304, v6\n"
    "v_add_u32 v18, 49152, v7\n"
    "v_add_u32 v19, 98304, v7\n"
    "s_waitcnt vmcnt(24) lgkmcnt(0)\n"
    "s_barrier\n"
    "s_add_u32 m0, s59, 122880\n"
    "s_mov_b32 s74, 1310720\n"
    "buffer_load_dwordx4 v5, s[40:43], s74 offen lds\n"
    "s_add_u32 m0, s59, 126976\n"
    "s_mov_b32 s74, 1310848\n"
    "buffer_load_dwordx4 v5, s[40:43], s74 offen lds\n"
    "s_add_u32 m0, s59, 131072\n"
    "s_mov_b32 s74, 1310720\n"
    "buffer_load_dwordx4 v5, s[44:47], s74 offen lds\n"
    "s_add_u32 m0, s59, 135168\n"
    "s_mov_b32 s74, 1310848\n"
    "buffer_load_dwordx4 v5, s[44:47], s74 offen lds\n"
    "s_add_u32 m0, s59, 139264\n"
    "s_mov_b32 s74, 1310720\n"
    "buffer_load_dwordx4 v5, s[48:51], s74 offen lds\n"
    "s_add_u32 m0, s59, 143360\n"
    "s_mov_b32 s74, 1310848\n"
    "buffer_load_dwordx4 v5, s[48:51], s74 offen lds\n"
    "ds_read_b64_tr_b16 a[0:1], v6 offset:8192\n"
    "ds_read_b64_tr_b16 a[2:3], v6 offset:8704\n"
    "ds_read_b64_tr_b16 a[4:5], v6 offset:8200\n"
    "ds_read_b64_tr_b16 a[6:7], v6 offset:8712\n"
    "ds_read_b64_tr_b16 a[8:9], v6 offset:16384\n"
    "ds_read_b64_tr_b16 a[10:11], v6 offset:16896\n"
    "ds_read_b64_tr_b16 a[12:13], v6 offset:16392\n"
    "ds_read_b64_tr_b16 a[14:15], v6 offset:16904\n"
    "ds_read_b64_tr_b16 a[16:17], v6 offset:0\n"
    "ds_read_b64_tr_b16 a[18:19], v6 offset:512\n"
    "ds_read_b64_tr_b16 a[20:21], v6 offset:8\n"
    "ds_read_b64_tr_b16 a[22:23], v6 offset:520\n"
    "s_waitcnt vmcnt(24) lgkmcnt(0)\n"
    "s_barrier\n"
    "s_add_u32 m0, s59, 0\n"
    "s_mov_b32 s74, 1572864\n"
    "buffer_load_dwordx4 v5, s[40:43], s74 offen lds\n"
    "s_add_u32 m0, s59, 4096\n"
    "s_mov_b32 s74, 1572992\n"
    "buffer_load_dwordx4 v5, s[40:43], s74 offen lds\n"
    "s_add_u32 m0, s59, 8192\n"
    "s_mov_b32 s74, 1572864\n"
    "buffer_load_dwordx4 v5, s[44:47], s74 offen lds\n"
    "s_add_u32 m0, s59, 12288\n"
    "s_mov_b32 s74, 1572992\n"
    "buffer_load_dwordx4 v5, s[44:47], s74 offen lds\n"
    "s_add_u32 m0, s59, 16384\n"
    "s_mov_b32 s74, 1572864\n"
    "buffer_load_dwordx4 v5, s[48:51], s74 offen lds\n"
    "s_add_u32 m0, s59, 20480\n"
    "s_mov_b32 s74, 1572992\n"
    "buffer_load_dwordx4 v5, s[48:51], s74 offen lds\n"
    "ds_read_b64_tr_b16 a[24:25], v6 offset:32768\n"
    "ds_read_b64_tr_b16 a[26:27], v6 offset:33280\n"
    "ds_read_b64_tr_b16 a[28:29], v6 offset:32776\n"
    "ds_read_b64_tr_b16 a[30:31], v6 offset:33288\n"
    "ds_read_b64_tr_b16 a[32:33], v6 offset:40960\n"
    "ds_read_b64_tr_b16 a[34:35], v6 offset:41472\n"
    "ds_read_b64_tr_b16 a[36:37], v6 offset:40968\n"
    "ds_read_b64_tr_b16 a[38:39], v6 offset:41480\n"
    "ds_read_b64_tr_b16 a[40:41], v6 offset:24576\n"
    "ds_read_b64_tr_b16 a[42:43], v6 offset:25088\n"
    "ds_read_b64_tr_b16 a[44:45], v6 offset:24584\n"
    "ds_read_b64_tr_b16 a[46:47], v6 offset:25096\n"
    "s_waitcnt vmcnt(24) lgkmcnt(0)\n"
    "s_barrier\n"
    "s_add_u32 m0, s59, 24576\n"
    "s_mov_b32 s74, 1835008\n"
    "buffer_load_dwordx4 v5, s[40:43], s74 offen lds\n"
    "s_add_u32 m0, s59, 28672\n"
    "s_mov_b32 s74, 1835136\n"
    "buffer_load_dwordx4 v5, s[40:43], s74 offen lds\n"
    "s_add_u32 m0, s59, 32768\n"
    "s_mov_b32 s74, 1835008\n"
    "buffer_load_dwordx4 v5, s[44:47], s74 offen lds\n"
    "s_add_u32 m0, s59, 36864\n"
    "s_mov_b32 s74, 1835136\n"
    "buffer_load_dwordx4 v5, s[44:47], s74 offen lds\n"
    "s_add_u32 m0, s59, 40960\n"
    "s_mov_b32 s74, 1835008\n"
    "buffer_load_dwordx4 v5, s[48:51], s74 offen lds\n"
    "s_add_u32 m0, s59, 45056\n"
    "s_mov_b32 s74, 1835136\n"
    "buffer_load_dwordx4 v5, s[48:51], s74 offen lds\n"
    "ds_read_b64_tr_b16 a[48:49], v16 offset:8192\n"
    "ds_read_b64_tr_b16 a[50:51], v16 offset:8704\n"
    "ds_read_b64_tr_b16 a[52:53], v16 offset:8200\n"
    "ds_read_b64_tr_b16 a[54:55], v16 offset:8712\n"
    "ds_read_b64_tr_b16 a[56:57], v16 offset:16384\n"
    "ds_read_b64_tr_b16 a[58:59], v16 offset:16896\n"
    "ds_read_b64_tr_b16 a[60:61], v16 offset:16392\n"
    "ds_read_b64_tr_b16 a[62:63], v16 offset:16904\n"
    "ds_read_b64_tr_b16 a[64:65], v16 offset:0\n"
    "ds_read_b64_tr_b16 a[66:67], v16 offset:512\n"
    "ds_read_b64_tr_b16 a[68:69], v16 offset:8\n"
    "ds_read_b64_tr_b16 a[70:71], v16 offset:520\n"
    "s_waitcnt vmcnt(24) lgkmcnt(0)\n"
    "s_barrier\n"
    "s_add_u32 m0, s59, 49152\n"
    "s_mov_b32 s74, 2097152\n"
    "buffer_load_dwordx4 v5, s[40:43], s74 offen lds\n"
    "s_add_u32 m0, s59, 53248\n"
    "s_mov_b32 s74, 2097280\n"
    "buffer_load_dwordx4 v5, s[40:43], s74 offen lds\n"
    "s_add_u32 m0, s59, 57344\n"
    "s_mov_b32 s74, 2097152\n"
    "buffer_load_dwordx4 v5, s[44:47], s74 offen lds\n"
    "s_add_u32 m0, s59, 61440\n"
    "s_mov_b32 s74, 2097280\n"
    "buffer_load_dwordx4 v5, s[44:47], s74 offen lds\n"
    "s_add_u32 m0, s59, 65536\n"
    "s_mov_b32 s74, 2097152\n"
    "buffer_load_dwordx4 v5, s[48:51], s74 offen lds\n"
    "s_add_u32 m0, s59, 69632\n"
    "s_mov_b32 s74, 2097280\n"
    "buffer_load_dwordx4 v5, s[48:51], s74 offen lds\n"
    "ds_read_b64_tr_b16 a[72:73], v16 offset:32768\n"
    "ds_read_b64_tr_b16 a[74:75], v16 offset:33280\n"
    "ds_read_b64_tr_b16 a[76:77], v16 offset:32776\n"
    "ds_read_b64_tr_b16 a[78:79], v16 offset:33288\n"
    "ds_read_b64_tr_b16 a[80:81], v16 offset:40960\n"
    "ds_read_b64_tr_b16 a[82:83], v16 offset:41472\n"
    "ds_read_b64_tr_b16 a[84:85], v16 offset:40968\n"
    "ds_read_b64_tr_b16 a[86:87], v16 offset:41480\n"
    "ds_read_b64_tr_b16 a[88:89], v16 offset:24576\n"
    "ds_read_b64_tr_b16 a[90:91], v16 offset:25088\n"
    "ds_read_b64_tr_b16 a[92:93], v16 offset:24584\n"
    "ds_read_b64_tr_b16 a[94:95], v16 offset:25096\n"
    "s_waitcnt vmcnt(24) lgkmcnt(0)\n"
    "s_barrier\n"
    "s_add_u32 m0, s59, 73728\n"
    "s_mov_b32 s74, 2359296\n"
    "buffer_load_dwordx4 v5, s[40:43], s74 offen lds\n"
    "s_add_u32 m0, s59, 77824\n"
    "s_mov_b32 s74, 2359424\n"
    "buffer_load_dwordx4 v5, s[40:43], s74 offen lds\n"
    "s_add_u32 m0, s59, 81920\n"
    "s_mov_b32 s74, 2359296\n"
    "buffer_load_dwordx4 v5, s[44:47], s74 offen lds\n"
    "s_add_u32 m0, s59, 86016\n"
    "s_mov_b32 s74, 2359424\n"
    "buffer_load_dwordx4 v5, s[44:47], s74 offen lds\n"
    "s_add_u32 m0, s59, 90112\n"
    "s_mov_b32 s74, 2359296\n"
    "buffer_load_dwordx4 v5, s[48:51], s74 offen lds\n"
    "s_add_u32 m0, s59, 94208\n"
    "s_mov_b32 s74, 2359424\n"
    "buffer_load_dwordx4 v5, s[48:51], s74 offen lds\n"
    "ds_read_b64_tr_b16 a[96:97], v17 offset:8192\n"
    "ds_read_b64_tr_b16 a[98:99], v17 offset:8704\n"
    "ds_read_b64_tr_b16 a[100:101], v17 offset:8200\n"
    "ds_read_b64_tr_b16 a[102:103], v17 offset:8712\n"
    "ds_read_b64_tr_b16 a[104:105], v17 offset:16384\n"
    "ds_read_b64_tr_b16 a[106:107], v17 offset:16896\n"
    "ds_read_b64_tr_b16 a[108:109], v17 offset:16392\n"
    "ds_read_b64_tr_b16 a[110:111], v17 offset:16904\n"
    "ds_read_b64_tr_b16 a[112:113], v17 offset:0\n"
    "ds_read_b64_tr_b16 a[114:115], v17 offset:512\n"
    "ds_read_b64_tr_b16 a[116:117], v17 offset:8\n"
    "ds_read_b64_tr_b16 a[118:119], v17 offset:520\n"
    "s_waitcnt vmcnt(24) lgkmcnt(0)\n"
    "s_barrier\n"
    "s_add_u32 m0, s59, 98304\n"
    "s_mov_b32 s74, 2621440\n"
    "buffer_load_dwordx4 v5, s[40:43], s74 offen lds\n"
    "s_add_u32 m0, s59, 102400\n"
    "s_mov_b32 s74, 2621568\n"
    "buffer_load_dwordx4 v5, s[40:43], s74 offen lds\n"
    "s_add_u32 m0, s59, 106496\n"
    "s_mov_b32 s74, 2621440\n"
    "buffer_load_dwordx4 v5, s[44:47], s74 offen lds\n"
    "s_add_u32 m0, s59, 110592\n"
    "s_mov_b32 s74, 2621568\n"
    "buffer_load_dwordx4 v5, s[44:47], s74 offen lds\n"
    "s_add_u32 m0, s59, 114688\n"
    "s_mov_b32 s74, 2621440\n"
    "buffer_load_dwordx4 v5, s[48:51], s74 offen lds\n"
    "s_add_u32 m0, s59, 118784\n"
    "s_mov_b32 s74, 2621568\n"
    "buffer_load_dwordx4 v5, s[48:51], s74 offen lds\n"
    "ds_read_b64_tr_b16 a[120:121], v17 offset:32768\n"
    "ds_read_b64_tr_b16 a[122:123], v17 offset:33280\n"
    "ds_read_b64_tr_b16 a[124:125], v17 offset:32776\n"
    "ds_read_b64_tr_b16 a[126:127], v17 offset:33288\n"
    "ds_read_b64_tr_b16 a[128:129], v17 offset:40960\n"
    "ds_read_b64_tr_b16 a[130:131], v17 offset:41472\n"
    "ds_read_b64_tr_b16 a[132:133], v17 offset:40968\n"
    "ds_read_b64_tr_b16 a[134:135], v17 offset:41480\n"
    "ds_read_b64_tr_b16 a[136:137], v17 offset:24576\n"
    "ds_read_b64_tr_b16 a[138:139], v17 offset:25088\n"
    "ds_read_b64_tr_b16 a[140:141], v17 offset:24584\n"
    "ds_read_b64_tr_b16 a[142:143], v17 offset:25096\n"
    "s_waitcnt vmcnt(24) lgkmcnt(0)\n"
    "s_barrier\n"
    "s_add_u32 m0, s59, 122880\n"
    "s_mov_b32 s74, 2883584\n"
    "buffer_load_dwordx4 v5, s[40:43], s74 offen lds\n"
    "s_add_u32 m0, s59, 126976\n"
    "s_mov_b32 s74, 2883712\n"
    "buffer_load_dwordx4 v5, s[40:43], s74 offen lds\n"
    "s_add_u32 m0, s59, 131072\n"
    "s_mov_b32 s74, 2883584\n"
    "buffer_load_dwordx4 v5, s[44:47], s74 offen lds\n"
    "s_add_u32 m0, s59, 135168\n"
    "s_mov_b32 s74, 2883712\n"
    "buffer_load_dwordx4 v5, s[44:47], s74 offen lds\n"
    "s_add_u32 m0, s59, 139264\n"
    "s_mov_b32 s74, 2883584\n"
    "buffer_load_dwordx4 v5, s[48:51], s74 offen lds\n"
    "s_add_u32 m0, s59, 143360\n"
    "s_mov_b32 s74, 2883712\n"
    "buffer_load_dwordx4 v5, s[48:51], s74 offen lds\n"
    "ds_read_b64_tr_b16 a[144:145], v6 offset:8192\n"
    "ds_read_b64_tr_b16 a[146:147], v6 offset:8704\n"
    "ds_read_b64_tr_b16 a[148:149], v6 offset:8200\n"
    "ds_read_b64_tr_b16 a[150:151], v6 offset:8712\n"
    "ds_read_b64_tr_b16 a[152:153], v6 offset:16384\n"
    "ds_read_b64_tr_b16 a[154:155], v6 offset:16896\n"
    "ds_read_b64_tr_b16 a[156:157], v6 offset:16392\n"
    "ds_read_b64_tr_b16 a[158:159], v6 offset:16904\n"
    "ds_read_b64_tr_b16 a[160:161], v6 offset:0\n"
    "ds_read_b64_tr_b16 a[162:163], v6 offset:512\n"
    "ds_read_b64_tr_b16 a[164:165], v6 offset:8\n"
    "ds_read_b64_tr_b16 a[166:167], v6 offset:520\n"
    "s_waitcnt vmcnt(24) lgkmcnt(0)\n"
    "s_barrier\n"
    "s_add_u32 m0, s59, 0\n"
    "s_mov_b32 s74, 3145728\n"
    "buffer_load_dwordx4 v5, s[40:43], s74 offen lds\n"
    "s_add_u32 m0, s59, 4096\n"
    "s_mov_b32 s74, 3145856\n"
    "buffer_load_dwordx4 v5, s[40:43], s74 offen lds\n"
    "s_add_u32 m0, s59, 8192\n"
    "s_mov_b32 s74, 3145728\n"
    "buffer_load_dwordx4 v5, s[44:47], s74 offen lds\n"
    "s_add_u32 m0, s59, 12288\n"
    "s_mov_b32 s74, 3145856\n"
    "buffer_load_dwordx4 v5, s[44:47], s74 offen lds\n"
    "s_add_u32 m0, s59, 16384\n"
    "s_mov_b32 s74, 3145728\n"
    "buffer_load_dwordx4 v5, s[48:51], s74 offen lds\n"
    "s_add_u32 m0, s59, 20480\n"
    "s_mov_b32 s74, 3145856\n"
    "buffer_load_dwordx4 v5, s[48:51], s74 offen lds\n"
    "ds_read_b64_tr_b16 a[168:169], v6 offset:32768\n"
    "ds_read_b64_tr_b16 a[170:171], v6 offset:33280\n"
    "ds_read_b64_tr_b16 a[172:173], v6 offset:32776\n"
    "ds_read_b64_tr_b16 a[174:175], v6 offset:33288\n"
    "ds_read_b64_tr_b16 a[176:177], v6 offset:40960\n"
    "ds_read_b64_tr_b16 a[178:179], v6 offset:41472\n"
    "ds_read_b64_tr_b16 a[180:181], v6 offset:40968\n"
    "ds_read_b64_tr_b16 a[182:183], v6 offset:41480\n"
    "ds_read_b64_tr_b16 a[184:185], v6 offset:24576\n"
    "ds_read_b64_tr_b16 a[186:187], v6 offset:25088\n"
    "ds_read_b64_tr_b16 a[188:189], v6 offset:24584\n"
    "ds_read_b64_tr_b16 a[190:191], v6 offset:25096\n"
    "s_waitcnt vmcnt(24) lgkmcnt(0)\n"
    "s_barrier\n"
    "s_add_u32 m0, s59, 24576\n"
    "s_mov_b32 s74, 3407872\n"
    "buffer_load_dwordx4 v5, s[40:43], s74 offen lds\n"
    "s_add_u32 m0, s59, 28672\n"
    "s_mov_b32 s74, 3408000\n"
    "buffer_load_dwordx4 v5, s[40:43], s74 offen lds\n"
    "s_add_u32 m0, s59, 32768\n"
    "s_mov_b32 s74, 3407872\n"
    "buffer_load_dwordx4 v5, s[44:47], s74 offen lds\n"
    "s_add_u32 m0, s59, 36864\n"
    "s_mov_b32 s74, 3408000\n"
    "buffer_load_dwordx4 v5, s[44:47], s74 offen lds\n"
    "s_add_u32 m0, s59, 40960\n"
    "s_mov_b32 s74, 3407872\n"
    "buffer_load_dwordx4 v5, s[48:51], s74 offen lds\n"
    "s_add_u32 m0, s59, 45056\n"
    "s_mov_b32 s74, 3408000\n"
    "buffer_load_dwordx4 v5, s[48:51], s74 offen lds\n"
    "ds_read_b64_tr_b16 a[192:193], v16 offset:8192\n"
    "ds_read_b64_tr_b16 a[194:195], v16 offset:8704\n"
    "ds_read_b64_tr_b16 a[196:197], v16 offset:8200\n"
    "ds_read_b64_tr_b16 a[198:199], v16 offset:8712\n"
    "ds_read_b64_tr_b16 a[200:201], v16 offset:16384\n"
    "ds_read_b64_tr_b16 a[202:203], v16 offset:16896\n"
    "ds_read_b64_tr_b16 a[204:205], v16 offset:16392\n"
    "ds_read_b64_tr_b16 a[206:207], v16 offset:16904\n"
    "ds_read_b64_tr_b16 a[208:209], v16 offset:0\n"
    "ds_read_b64_tr_b16 a[210:211], v16 offset:512\n"
    "ds_read_b64_tr_b16 a[212:213], v16 offset:8\n"
    "ds_read_b64_tr_b16 a[214:215], v16 offset:520\n"
    "s_waitcnt vmcnt(24) lgkmcnt(0)\n"
    "s_barrier\n"
    "s_add_u32 m0, s59, 49152\n"
    "s_mov_b32 s74, 3670016\n"
    "buffer_load_dwordx4 v5, s[40:43], s74 offen lds\n"
    "s_add_u32 m0, s59, 53248\n"
    "s_mov_b32 s74, 3670144\n"
    "buffer_load_dwordx4 v5, s[40:43], s74 offen lds\n"
    "s_add_u32 m0, s59, 57344\n"
    "s_mov_b32 s74, 3670016\n"
    "buffer_load_dwordx4 v5, s[44:47], s74 offen lds\n"
    "s_add_u32 m0, s59, 61440\n"
    "s_mov_b32 s74, 3670144\n"
    "buffer_load_dwordx4 v5, s[44:47], s74 offen lds\n"
    "s_add_u32 m0, s59, 65536\n"
    "s_mov_b32 s74, 3670016\n"
    "buffer_load_dwordx4 v5, s[48:51], s74 offen lds\n"
    "s_add_u32 m0, s59, 69632\n"
    "s_mov_b32 s74, 3670144\n"
    "buffer_load_dwordx4 v5, s[48:51], s74 offen lds\n"
    "ds_read_b64_tr_b16 a[216:217], v16 offset:32768\n"
    "ds_read_b64_tr_b16 a[218:219], v16 offset:33280\n"
    "ds_read_b64_tr_b16 a[220:221], v16 offset:32776\n"
    "ds_read_b64_tr_b16 a[222:223], v16 offset:33288\n"
    "ds_read_b64_tr_b16 a[224:225], v16 offset:40960\n"
    "ds_read_b64_tr_b16 a[226:227], v16 offset:41472\n"
    "ds_read_b64_tr_b16 a[228:229], v16 offset:40968\n"
    "ds_read_b64_tr_b16 a[230:231], v16 offset:41480\n"
    "ds_read_b64_tr_b16 a[232:233], v16 offset:24576\n"
    "ds_read_b64_tr_b16 a[234:235], v16 offset:25088\n"
    "ds_read_b64_tr_b16 a[236:237], v16 offset:24584\n"
    "ds_read_b64_tr_b16 a[238:239], v16 offset:25096\n"
    "s_waitcnt vmcnt(24) lgkmcnt(0)\n"
    "s_barrier\n"
    "s_add_u32 m0, s59, 73728\n"
    "s_mov_b32 s74, 3932160\n"
    "buffer_load_dwordx4 v5, s[40:43], s74 offen lds\n"
    "s_add_u32 m0, s59, 77824\n"
    "s_mov_b32 s74, 3932288\n"
    "buffer_load_dwordx4 v5, s[40:43], s74 offen lds\n"
    "s_add_u32 m0, s59, 81920\n"
    "s_mov_b32 s74, 3932160\n"
    "buffer_load_dwordx4 v5, s[44:47], s74 offen lds\n"
    "s_add_u32 m0, s59, 86016\n"
    "s_mov_b32 s74, 3932288\n"
    "buffer_load_dwordx4 v5, s[44:47], s74 offen lds\n"
    "s_add_u32 m0, s59, 90112\n"
    "s_mov_b32 s74, 3932160\n"
    "buffer_load_dwordx4 v5, s[48:51], s74 offen lds\n"
    "s_add_u32 m0, s59, 94208\n"
    "s_mov_b32 s74, 3932288\n"
    "buffer_load_dwordx4 v5, s[48:51], s74 offen lds\n"
    "ds_read_b64_tr_b16 a[240:241], v17 offset:8192\n"
    "ds_read_b64_tr_b16 a[242:243], v17 offset:8704\n"
    "ds_read_b64_tr_b16 a[244:245], v17 offset:8200\n"
    "ds_read_b64_tr_b16 a[246:247], v17 offset:8712\n"
    "ds_read_b64_tr_b16 a[248:249], v17 offset:16384\n"
    "ds_read_b64_tr_b16 a[250:251], v17 offset:16896\n"
    "ds_read_b64_tr_b16 a[252:253], v17 offset:16392\n"
    "ds_read_b64_tr_b16 a[254:255], v17 offset:16904\n"
    "ds_read_b64_tr_b16 v[80:81], v17 offset:0\n"
    "ds_read_b64_tr_b16 v[82:83], v17 offset:512\n"
    "ds_read_b64_tr_b16 v[84:85], v17 offset:8\n"
    "ds_read_b64_tr_b16 v[86:87], v17 offset:520\n"
    "s_waitcnt vmcnt(24) lgkmcnt(0)\n"
    "s_barrier\n"
    "s_add_u32 m0, s59, 98304\n"
    "s_mov_b32 s74, 4194304\n"
    "buffer_load_dwordx4 v5, s[40:43], s74 offen lds\n"
    "s_add_u32 m0, s59, 102400\n"
    "s_mov_b32 s74, 4194432\n"
    "buffer_load_dwordx4 v5, s[40:43], s74 offen lds\n"
    "s_add_u32 m0, s59, 106496\n"
    "s_mov_b32 s74, 4194304\n"
    "buffer_load_dwordx4 v5, s[44:47], s74 offen lds\n"
    "s_add_u32 m0, s59, 110592\n"
    "s_mov_b32 s74, 4194432\n"
    "buffer_load_dwordx4 v5, s[44:47], s74 offen lds\n"
    "s_add_u32 m0, s59, 114688\n"
    "s_mov_b32 s74, 4194304\n"
    "buffer_load_dwordx4 v5, s[48:51], s74 offen lds\n"
    "s_add_u32 m0, s59, 118784\n"
    "s_mov_b32 s74, 4194432\n"
    "buffer_load_dwordx4 v5, s[48:51], s74 offen lds\n"
    "ds_read_b64_tr_b16 v[88:89], v17 offset:32768\n"
    "ds_read_b64_tr_b16 v[90:91], v17 offset:33280\n"
    "ds_read_b64_tr_b16 v[92:93], v17 offset:32776\n"
    "ds_read_b64_tr_b16 v[94:95], v17 offset:33288\n"
    "ds_read_b64_tr_b16 v[96:97], v17 offset:40960\n"
    "ds_read_b64_tr_b16 v[98:99], v17 offset:41472\n"
    "ds_read_b64_tr_b16 v[100:101], v17 offset:40968\n"
    "ds_read_b64_tr_b16 v[102:103], v17 offset:41480\n"
    "ds_read_b64_tr_b16 v[104:105], v17 offset:24576\n"
    "ds_read_b64_tr_b16 v[106:107], v17 offset:25088\n"
    "ds_read_b64_tr_b16 v[108:109], v17 offset:24584\n"
    "ds_read_b64_tr_b16 v[110:111], v17 offset:25096\n"
    "s_waitcnt vmcnt(24) lgkmcnt(0)\n"
    "s_barrier\n"
    "s_add_u32 m0, s59, 122880\n"
    "s_mov_b32 s74, 4456448\n"
    "buffer_load_dwordx4 v5, s[40:43], s74 offen lds\n"
    "s_add_u32 m0, s59, 126976\n"
    "s_mov_b32 s74, 4456576\n"
    "buffer_load_dwordx4 v5, s[40:43], s74 offen lds\n"
    "s_add_u32 m0, s59, 131072\n"
    "s_mov_b32 s74, 4456448\n"
    "buffer_load_dwordx4 v5, s[44:47], s74 offen lds\n"
    "s_add_u32 m0, s59, 135168\n"
    "s_mov_b32 s74, 4456576\n"
    "buffer_load_dwordx4 v5, s[44:47], s74 offen lds\n"
    "s_add_u32 m0, s59, 139264\n"
    "s_mov_b32 s74, 4456448\n"
    "buffer_load_dwordx4 v5, s[48:51], s74 offen lds\n"
    "s_add_u32 m0, s59, 143360\n"
    "s_mov_b32 s74, 4456576\n"
    "buffer_load_dwordx4 v5, s[48:51], s74 offen lds\n"
    "buffer_load_dwordx2 v[12:13], off, s[36:39], 0 sc1\n"
    "ds_read_b64_tr_b16 v[112:113], v6 offset:8192\n"
    "ds_read_b64_tr_b16 v[114:115], v6 offset:8704\n"
    "ds_read_b64_tr_b16 v[116:117], v6 offset:8200\n"
    "ds_read_b64_tr_b16 v[118:119], v6 offset:8712\n"
    "ds_read_b64_tr_b16 v[120:121], v6 offset:16384\n"
    "ds_read_b64_tr_b16 v[122:123], v6 offset:16896\n"
    "ds_read_b64_tr_b16 v[124:125], v6 offset:16392\n"
    "ds_read_b64_tr_b16 v[126:127], v6 offset:16904\n"
    "ds_read_b64_tr_b16 v[128:129], v6 offset:0\n"
    "ds_read_b64_tr_b16 v[130:131], v6 offset:512\n"
    "ds_read_b64_tr_b16 v[132:133], v6 offset:8\n"
    "ds_read_b64_tr_b16 v[134:135], v6 offset:520\n"
    "s_waitcnt vmcnt(25) lgkmcnt(0)\n"
    "s_barrier\n"
    "ds_read_b64_tr_b16 v[136:137], v6 offset:32768\n"
    "ds_read_b64_tr_b16 v[138:139], v6 offset:33280\n"
    "ds_read_b64_tr_b16 v[140:141], v6 offset:32776\n"
    "ds_read_b64_tr_b16 v[142:143], v6 offset:33288\n"
    "ds_read_b64_tr_b16 v[144:145], v6 offset:40960\n"
    "ds_read_b64_tr_b16 v[146:147], v6 offset:41472\n"
    "ds_read_b64_tr_b16 v[148:149], v6 offset:40968\n"
    "ds_read_b64_tr_b16 v[150:151], v6 offset:41480\n"
    "ds_read_b64_tr_b16 v[152:153], v6 offset:24576\n"
    "ds_read_b64_tr_b16 v[154:155], v6 offset:25088\n"
    "ds_read_b64_tr_b16 v[156:157], v6 offset:24584\n"
    "ds_read_b64_tr_b16 v[158:159], v6 offset:25096\n"
    "s_waitcnt vmcnt(19) lgkmcnt(0)\n"
    "s_barrier\n"
    "ds_read_b64_tr_b16 v[160:161], v16 offset:8192\n"
    "ds_read_b64_tr_b16 v[162:163], v16 offset:8704\n"
    "ds_read_b64_tr_b16 v[164:165], v16 offset:8200\n"
    "ds_read_b64_tr_b16 v[166:167], v16 offset:8712\n"
    "ds_read_b64_tr_b16 v[168:169], v16 offset:16384\n"
    "ds_read_b64_tr_b16 v[170:171], v16 offset:16896\n"
    "ds_read_b64_tr_b16 v[172:173], v16 offset:16392\n"
    "ds_read_b64_tr_b16 v[174:175], v16 offset:16904\n"
    "ds_read_b64_tr_b16 v[176:177], v16 offset:0\n"
    "ds_read_b64_tr_b16 v[178:179], v16 offset:512\n"
    "ds_read_b64_tr_b16 v[180:181], v16 offset:8\n"
    "ds_read_b64_tr_b16 v[182:183], v16 offset:520\n"
    "s_waitcnt vmcnt(13) lgkmcnt(0)\n"
    "s_barrier\n"
    "ds_read_b64_tr_b16 v[184:185], v16 offset:32768\n"
    "ds_read_b64_tr_b16 v[186:187], v16 offset:33280\n"
    "ds_read_b64_tr_b16 v[188:189], v16 offset:32776\n"
    "ds_read_b64_tr_b16 v[190:191], v16 offset:33288\n"
    "ds_read_b64_tr_b16 v[192:193], v16 offset:40960\n"
    "ds_read_b64_tr_b16 v[194:195], v16 offset:41472\n"
    "ds_read_b64_tr_b16 v[196:197], v16 offset:40968\n"
    "ds_read_b64_tr_b16 v[198:199], v16 offset:41480\n"
    "ds_read_b64_tr_b16 v[200:201], v16 offset:24576\n"
    "ds_read_b64_tr_b16 v[202:203], v16 offset:25088\n"
    "ds_read_b64_tr_b16 v[204:205], v16 offset:24584\n"
    "ds_read_b64_tr_b16 v[206:207], v16 offset:25096\n"
    "s_waitcnt vmcnt(7) lgkmcnt(0)\n"
    "s_barrier\n"
    "ds_read_b64_tr_b16 v[208:209], v17 offset:8192\n"
    "ds_read_b64_tr_b16 v[210:211], v17 offset:8704\n"
    "ds_read_b64_tr_b16 v[212:213], v17 offset:8200\n"
    "ds_read_b64_tr_b16 v[214:215], v17 offset:8712\n"
    "ds_read_b64_tr_b16 v[216:217], v17 offset:16384\n"
    "ds_read_b64_tr_b16 v[218:219], v17 offset:16896\n"
    "ds_read_b64_tr_b16 v[220:221], v17 offset:16392\n"
    "ds_read_b64_tr_b16 v[222:223], v17 offset:16904\n"
    "ds_read_b64_tr_b16 v[224:225], v17 offset:0\n"
    "ds_read_b64_tr_b16 v[226:227], v17 offset:512\n"
    "ds_read_b64_tr_b16 v[228:229], v17 offset:8\n"
    "ds_read_b64_tr_b16 v[230:231], v17 offset:520\n"
    "s_waitcnt vmcnt(0) lgkmcnt(0)\n"
    "s_barrier\n"
    ".LgP_par_%=:\n"
    "v_readfirstlane_b32 s74, v12\n"
    "v_readfirstlane_b32 s75, v13\n"
    "s_xor_b32 s74, s74, s66\n"
    "s_xor_b32 s75, s75, s67\n"
    "s_cselect_b32 s75, 2, 0\n"
    "s_or_b32 s75, s75, s74\n"
    "s_cmp_lt_u32 s75, 2\n"
    "s_cbranch_scc1 .LgP_par1_%=\n"
    "s_sleep 4\n"
    "buffer_load_dwordx2 v[12:13], off, s[36:39], 0 sc1\n"
    "s_waitcnt vmcnt(0)\n"
    "s_sub_u32 s72, s72, 1\n"
    "s_cbranch_scc0 .LgP_par_%=\n"
    "s_mov_b32 s72, 0\n"
    ".LgP_par1_%=:\n"
    "s_and_b32 s74, s74, 1\n"
    "s_lshl_b32 s74, s74, 13\n"
    "s_add_u32 s36, s36, s74\n"
    "s_addc_u32 s37, s37, 0\n"
    "v_add_u32 v13, v11, v3\n"
    "v_cmp_gt_i32 vcc, s71, v13\n"
    "s_nop 0\n"
    "s_or_b64 exec, vcc, s[62:63]\n"
    "buffer_atomic_smax v11, v2, s[36:39], 0 offen sc0 sc1\n"
    "s_mov_b64 exec, -1\n"
    "s_getpc_b64 s[84:85]\n"
    ".LgP_PC2_%=:\n"
    "s_and_b32 s85, s85, 0xffff\n"
    "s_mov_b32 s86, .LgP_FIN_%=-.LgP_PC2_%=\n"
    "s_mov_b32 s87, 0x20000\n"
    "v_mbcnt_lo_u32_b32 v42, -1, 0\n"
    "v_mbcnt_hi_u32_b32 v42, -1, v42\n"
    "v_lshlrev_b32 v42, 7, v42\n"
    "buffer_load_dword v43, v42, s[84:87], 0 offen\n"
    "v_add_u32 v42, 8192, v42\n"
    "buffer_load_dword v43, v42, s[84:87], 0 offen\n"
    "v_add_u32 v42, 8192, v42\n"
    "buffer_load_dword v43, v42, s[84:87], 0 offen\n"
    "ds_read_b64_tr_b16 v[232:233], v17 offset:32768\n"
    "ds_read_b64_tr_b16 v[234:235], v17 offset:33280\n"
    "ds_read_b64_tr_b16 v[236:237], v17 offset:32776\n"
    "ds_read_b64_tr_b16 v[238:239], v17 offset:33288\n"
    "ds_read_b64_tr_b16 v[240:241], v17 offset:40960\n"
    "ds_read_b64_tr_b16 v[242:243], v17 offset:41472\n"
    "ds_read_b64_tr_b16 v[244:245], v17 offset:40968\n"
    "ds_read_b64_tr_b16 v[246:247], v17 offset:41480\n"
    "ds_read_b64_tr_b16 v[248:249], v17 offset:24576\n"
    "ds_read_b64_tr_b16 v[250:251], v17 offset:25088\n"
    "ds_read_b64_tr_b16 v[252:253], v17 offset:24584\n"
    "ds_read_b64_tr_b16 v[254:255], v17 offset:25096\n"
    "s_waitcnt lgkmcnt(0)\n"
    "s_barrier\n"
    "s_waitcnt vmcnt(0)\n"
    "v_mov_b32 v5, v11\n"
    "v_mov_b32 v12, -1\n"
    "s_mov_b32 s73, 0x80000000\n"
    "s_mov_b32 s79, 0x80000000\n"
    "s_mov_b32 s80, 0x80000000\n"
    "s_branch .LgP_block1_1_%=\n"
    ".LgP_LOOPB_%=:\n"
    ".LgP_head0_%=:\n"
    "v_mfma_f32_16x16x32_bf16 v[32:35], a[0:3], v[16:19], 0\n"
    "v_mfma_f32_16x16x32_bf16 v[36:39], a[4:7], v[16:19], 0\n"
    "v_cvt_pk_bf16_f32 v40, v40, v41\n"
    "v_cvt_pk_bf16_f32 v41, v42, v43\n"
    "v_mfma_f32_16x16x32_bf16 v[48:51], v[16:19], a[8:11], 0\n"
    "v_cvt_pk_bf16_f32 v42, v44, v45\n"
    "v_cvt_pk_bf16_f32 v43, v46, v47\n"
    "v_mfma_f32_16x16x32_bf16 v[56:59], v[16:19], a[12:15], 0\n"
    "buffer_store_dwordx4 v[40:43], v1, s[36:39], s79 offen offset:1024 sc1\n"
    "v_mfma_f32_16x16x32_bf16 v[64:67], a[16:19], v[16:19], 0\n"
    "s_waitcnt lgkmcnt(1)\n"
    "v_cvt_pk_bf16_f32 v55, v54, v55\n"
    "v_cvt_pk_bf16_f32 v54, v52, v53\n"
    "v_mfma_f32_16x16x32_bf16 v[68:71], a[20:23], v[16:19], 0\n"
    "ds_read_b128 v[16:19], v0 offset:3072\n"
    "v_mov_b64 v[52:53], v[28:29]\n"
    "v_mfma_f32_16x16x32_bf16 v[32:35], a[24:27], v[20:23], v[32:35]\n"
    "buffer_store_dwordx4 v[52:55], v1, s[36:39], s80 offen sc1\n"
    "v_mfma_f32_16x16x32_bf16 v[36:39], a[28:31], v[20:23], v[36:39]\n"
    "s_waitcnt vmcnt(3)\n"
    "v_cvt_pk_bf16_f32 v63, v62, v63\n"
    "v_add_u32 v13, v11, v3\n"
    "v_mfma_f32_16x16x32_bf16 v[48:51], v[20:23], a[32:35], v[48:51]\n"
    "v_cvt_pk_bf16_f32 v62, v60, v61\n"
    "v_cmp_gt_i32 vcc, s71, v13\n"
    "v_mfma_f32_16x16x32_bf16 v[56:59], v[20:23], a[36:39], v[56:59]\n"
    "buffer_atomic_smax v7, v10, s[36:39], s69 offen sc1\n"
    "v_mov_b64 v[60:61], v[30:31]\n"
    "v_mfma_f32_16x16x32_bf16 v[64:67], a[40:43], v[20:23], v[64:67]\n"
    "s_waitcnt lgkmcnt(1)\n"
    "v_cvt_pk_bf16_f32 v72, v72, v73\n"
    "v_cvt_pk_bf16_f32 v73, v74, v75\n"
    "v_mfma_f32_16x16x32_bf16 v[68:71], a[44:47], v[20:23], v[68:71]\n"
    "ds_read_b128 v[20:23], v0 offset:4096\n"
    "s_cbranch_vccnz .LgP_poll0_%=\n"
    "v_mfma_f32_16x16x32_bf16 v[32:35], a[48:51], v[24:27], v[32:35]\n"
    "s_add_u32 m0, s59, 20480\n"
    "v_mfma_f32_16x16x32_bf16 v[36:39], a[52:55], v[24:27], v[36:39]\n"
    "buffer_store_dwordx4 v[60:63], v1, s[36:39], s80 offen offset:1024 sc1\n"
    "v_mfma_f32_16x16x32_bf16 v[48:51], v[24:27], a[56:59], v[48:51]\n"
    "buffer_load_dwordx4 v1, s[36:39], s76 offen sc1 lds\n"
    "v_cvt_pk_bf16_f32 v74, v76, v77\n"
    "v_cvt_pk_bf16_f32 v75, v78, v79\n"
    "v_mfma_f32_16x16x32_bf16 v[56:59], v[24:27], a[60:63], v[56:59]\n"
    "v_mfma_f32_16x16x32_bf16 v[64:67], a[64:67], v[24:27], v[64:67]\n"
    "s_waitcnt lgkmcnt(1)\n"
    "v_mfma_f32_16x16x32_bf16 v[68:71], a[68:71], v[24:27], v[68:71]\n"
    "ds_read_b128 v[24:27], v0 offset:5120\n"
    "v_mfma_f32_16x16x32_bf16 v[32:35], a[72:75], v[16:19], v[32:35]\n"
    "buffer_store_dwordx4 v[72:75], v1, s[36:39], s73 offen offset:1024 sc1\n"
    "v_mfma_f32_16x16x32_bf16 v[36:39], a[76:79], v[16:19], v[36:39]\n"
    "s_lshr_b32 s73, s68, 1\n"
    "v_mfma_f32_16x16x32_bf16 v[48:51], v[16:19], a[80:83], v[48:51]\n"
    "s_add_u32 s76, s76, 16384\n"
    "s_add_u32 m0, s59, 24576\n"
    "s_and_b32 s73, s73, s57\n"
    "v_mfma_f32_16x16x32_bf16 v[56:59], v[16:19], a[84:87], v[56:59]\n"
    "buffer_load_dwordx4 v1, s[36:39], s76 offen sc1 lds\n"
    "s_mul_i32 s73, s73, s56\n"
    "v_mfma_f32_16x16x32_bf16 v[64:67], a[88:91], v[16:19], v[64:67]\n"
    "s_waitcnt lgkmcnt(1)\n"
    "s_add_u32 s73, s73, s55\n"
    "v_mfma_f32_16x16x32_bf16 v[68:71], a[92:95], v[16:19], v[68:71]\n"
    "ds_read_b128 v[16:19], v0 offset:6144\n"
    "s_add_u32 s79, s73, 16384\n"
    "v_mfma_f32_16x16x32_bf16 v[32:35], a[96:99], v[20:23], v[32:35]\n"
    "s_add_u32 s80, s73, 32768\n"
    "v_mfma_f32_16x16x32_bf16 v[36:39], a[100:103], v[20:23], v[36:39]\n"
    "v_mfma_f32_16x16x32_bf16 v[48:51], v[20:23], a[104:107], v[48:51]\n"
    "v_mfma_f32_16x16x32_bf16 v[56:59], v[20:23], a[108:111], v[56:59]\n"
    "v_mfma_f32_16x16x32_bf16 v[64:67], a[112:115], v[20:23], v[64:67]\n"
    "s_waitcnt lgkmcnt(1)\n"
    "v_mfma_f32_16x16x32_bf16 v[68:71], a[116:119], v[20:23], v[68:71]\n"
    "ds_read_b128 v[20:23], v0 offset:7168\n"
    "v_mfma_f32_16x16x32_bf16 v[32:35], a[120:123], v[24:27], v[32:35]\n"
    "s_add_u32 s76, s76, 16384\n"
    "s_add_u32 m0, s59, 28672\n"
    "v_mfma_f32_16x16x32_bf16 v[36:39], a[124:127], v[24:27], v[36:39]\n"
    "buffer_load_dwordx4 v1, s[36:39], s76 offen sc1 lds\n"
    "v_mfma_f32_16x16x32_bf16 v[48:51], v[24:27], a[128:131], v[48:51]\n"
    "v_mfma_f32_16x16x32_bf16 v[56:59], v[24:27], a[132:135], v[56:59]\n"
    "v_mfma_f32_16x16x32_bf16 v[64:67], a[136:139], v[24:27], v[64:67]\n"
    "s_waitcnt lgkmcnt(1)\n"
    "v_mfma_f32_16x16x32_bf16 v[68:71], a[140:143], v[24:27], v[68:71]\n"
    "ds_read_b128 v[24:27], v0 offset:8192\n"
    "v_mfma_f32_16x16x32_bf16 v[32:35], a[144:147], v[16:19], v[32:35]\n"
    "v_mfma_f32_16x16x32_bf16 v[36:39], a[148:151], v[16:19], v[36:39]\n"
    "v_mfma_f32_16x16x32_bf16 v[48:51], v[16:19], a[152:155], v[48:51]\n"
    "v_mfma_f32_16x16x32_bf16 v[56:59], v[16:19], a[156:159], v[56:59]\n"
    "v_mfma_f32_16x16x32_bf16 v[64:67], a[160:163], v[16:19], v[64:67]\n"
    "s_waitcnt lgkmcnt(1)\n"
    "s_add_u32 s76, s76, 16384\n"
    "s_add_u32 m0, s59, 32768\n"
    "v_mfma_f32_16x16x32_bf16 v[68:71], a[164:167], v[16:19], v[68:71]\n"
    "ds_read_b128 v[16:19], v0 offset:9216\n"
    "buffer_load_dwordx4 v1, s[36:39], s76 offen sc1 lds\n"
    "v_mfma_f32_16x16x32_bf16 v[32:35], a[168:171], v[20:23], v[32:35]\n"
    "v_mfma_f32_16x16x32_bf16 v[36:39], a[172:175], v[20:23], v[36:39]\n"
    "v_mfma_f32_16x16x32_bf16 v[48:51], v[20:23], a[176:179], v[48:51]\n"
    "v_mfma_f32_16x16x32_bf16 v[56:59], v[20:23], a[180:183], v[56:59]\n"
    "v_mfma_f32_16x16x32_bf16 v[64:67], a[184:187], v[20:23], v[64:67]\n"
    "s_waitcnt lgkmcnt(1)\n"
    "v_mfma_f32_16x16x32_bf16 v[68:71], a[188:191], v[20:23], v[68:71]\n"
    "ds_read_b128 v[20:23], v0 offset:10240\n"
    "v_mfma_f32_16x16x32_bf16 v[32:35], a[192:195], v[24:27], v[32:35]\n"
    "v_mfma_f32_16x16x32_bf16 v[36:39], a[196:199], v[24:27], v[36:39]\n"
    "v_mfma_f32_16x16x32_bf16 v[48:51], v[24:27], a[200:203], v[48:51]\n"
    "s_add_u32 s76, s76, 16384\n"
    "s_add_u32 m0, s59, 36864\n"
    "v_mfma_f32_16x16x32_bf16 v[56:59], v[24:27], a[204:207], v[56:59]\n"
    "buffer_load_dwordx4 v8, s[36:39], s76 offen sc1 lds\n"
    "v_mfma_f32_16x16x32_bf16 v[64:67], a[208:211], v[24:27], v[64:67]\n"
    "s_waitcnt lgkmcnt(1)\n"
    "v_mfma_f32_16x16x32_bf16 v[68:71], a[212:215], v[24:27], v[68:71]\n"
    "ds_read_b128 v[24:27], v0 offset:11264\n"
    "v_mfma_f32_16x16x32_bf16 v[32:35], a[216:219], v[16:19], v[32:35]\n"
    "v_mfma_f32_16x16x32_bf16 v[36:39], a[220:223], v[16:19], v[36:39]\n"
    "v_mfma_f32_16x16x32_bf16 v[48:51], v[16:19], a[224:227], v[48:51]\n"
    "v_mfma_f32_16x16x32_bf16 v[56:59], v[16:19], a[228:231], v[56:59]\n"
    "v_mfma_f32_16x16x32_bf16 v[64:67], a[232:235], v[16:19], v[64:67]\n"
    "s_waitcnt lgkmcnt(1)\n"
    "s_waitcnt vmcnt(4)\n"
    "buffer_atomic_smax v12, v10, s[36:39], s70 offen sc1\n"
    "v_mfma_f32_16x16x32_bf16 v[68:71], a[236:239], v[16:19], v[68:71]\n"
    "ds_read_b128 v[16:19], v0 offset:12288\n"
    "v_mfma_f32_16x16x32_bf16 v[32:35], a[240:243], v[20:23], v[32:35]\n"
    "v_mfma_f32_16x16x32_bf16 v[36:39], a[244:247], v[20:23], v[36:39]\n"
    "v_mfma_f32_16x16x32_bf16 v[48:51], v[20:23], a[248:251], v[48:51]\n"
    "v_mfma_f32_16x16x32_bf16 v[56:59], v[20:23], a[252:255], v[56:59]\n"
    "v_mfma_f32_16x16x32_bf16 v[64:67], v[80:83], v[20:23], v[64:67]\n"
    "s_waitcnt lgkmcnt(1)\n"
    "v_mfma_f32_16x16x32_bf16 v[68:71], v[84:87], v[20:23], v[68:71]\n"
    "ds_read_b128 v[20:23], v0 offset:13312\n"
    "v_mfma_f32_16x16x32_bf16 v[32:35], v[88:91], v[24:27], v[32:35]\n"
    "v_mfma_f32_16x16x32_bf16 v[36:39], v[92:95], v[24:27], v[36:39]\n"
    "v_mfma_f32_16x16x32_bf16 v[48:51], v[24:27], v[96:99], v[48:51]\n"
    "v_mfma_f32_16x16x32_bf16 v[56:59], v[24:27], v[100:103], v[56:59]\n"
    "v_mfma_f32_16x16x32_bf16 v[64:67], v[104:107], v[24:27], v[64:67]\n"
    "s_waitcnt lgkmcnt(1)\n"
    "v_mfma_f32_16x16x32_bf16 v[68:71], v[108:111], v[24:27], v[68:71]\n"
    "ds_read_b128 v[24:27], v0 offset:14336\n"
    "v_mfma_f32_16x16x32_bf16 v[32:35], v[112:115], v[16:19], v[32:35]\n"
    "v_mfma_f32_16x16x32_bf16 v[36:39], v[116:119], v[16:19], v[36:39]\n"
    "v_mfma_f32_16x16x32_bf16 v[48:51], v[16:19], v[120:123], v[48:51]\n"
    "v_mfma_f32_16x16x32_bf16 v[56:59], v[16:19], v[124:127], v[56:59]\n"
    "v_mfma_f32_16x16x32_bf16 v[64:67], v[128:131], v[16:19], v[64:67]\n"
    "s_waitcnt lgkmcnt(1)\n"
    "v_mfma_f32_16x16x32_bf16 v[68:71], v[132:135], v[16:19], v[68:71]\n"
    "ds_read_b128 v[16:19], v0 offset:15360\n"
    ".LgP_join0_%=:\n"
    "v_mfma_f32_16x16x32_bf16 v[32:35], v[136:139], v[20:23], v[32:35]\n"
    "s_add_u32 s71, s68, 2\n"
    "s_waitcnt vmcnt(5)\n"
    "v_mfma_f32_16x16x32_bf16 v[36:39], v[140:143], v[20:23], v[36:39]\n"
    "v_add_u32 v13, v11, v3\n"
    "v_mfma_f32_16x16x32_bf16 v[48:51], v[20:23], v[144:147], v[48:51]\n"
    "v_cmp_gt_i32 vcc, s71, v13\n"
    "v_mfma_f32_16x16x32_bf16 v[56:59], v[20:23], v[148:151], v[56:59]\n"
    "v_mfma_f32_16x16x32_bf16 v[64:67], v[152:155], v[20:23], v[64:67]\n"
    "s_waitcnt lgkmcnt(1)\n"
    "v_mfma_f32_16x16x32_bf16 v[68:71], v[156:159], v[20:23], v[68:71]\n"
    "ds_read_b128 v[20:23], v0 offset:16384\n"
    "v_mfma_f32_16x16x32_bf16 v[32:35], v[160:163], v[24:27], v[32:35]\n"
    "s_or_b64 exec, vcc, s[62:63]\n"
    "buffer_atomic_smax v11, v2, s[36:39], 0 offen sc0 sc1\n"
    "s_mov_b64 exec, -1\n"
    "v_mfma_f32_16x16x32_bf16 v[36:39], v[164:167], v[24:27], v[36:39]\n"
    "s_lshr_b32 s76, s71, 1\n"
    "v_mfma_f32_16x16x32_bf16 v[48:51], v[24:27], v[168:171], v[48:51]\n"
    "s_and_b32 s76, s76, s54\n"
    "v_mfma_f32_16x16x32_bf16 v[56:59], v[24:27], v[172:175], v[56:59]\n"
    "s_mul_i32 s76, s76, s53\n"
    "v_mfma_f32_16x16x32_bf16 v[64:67], v[176:179], v[24:27], v[64:67]\n"
    "s_waitcnt lgkmcnt(1)\n"
    "s_add_u32 s76, s76, s52\n"
    "v_mfma_f32_16x16x32_bf16 v[68:71], v[180:183], v[24:27], v[68:71]\n"
    "ds_read_b128 v[24:27], v0 offset:17408\n"
    "v_mfma_f32_16x16x32_bf16 v[32:35], v[184:187], v[16:19], v[32:35]\n"
    "v_mfma_f32_16x16x32_bf16 v[36:39], v[188:191], v[16:19], v[36:39]\n"
    "v_mfma_f32_16x16x32_bf16 v[48:51], v[16:19], v[192:195], v[48:51]\n"
    "v_mfma_f32_16x16x32_bf16 v[56:59], v[16:19], v[196:199], v[56:59]\n"
    "s_waitcnt vmcnt(1) lgkmcnt(0)\n"
    "s_barrier\n"
    "v_mfma_f32_16x16x32_bf16 v[64:67], v[200:203], v[16:19], v[64:67]\n"
    "v_mfma_f32_16x16x32_bf16 v[68:71], v[204:207], v[16:19], v[68:71]\n"
    "ds_read_b128 v[16:19], v9 offset:0\n"
    "v_mfma_f32_16x16x32_bf16 v[32:35], v[208:211], v[20:23], v[32:35]\n"
    "v_mfma_f32_16x16x32_bf16 v[36:39], v[212:215], v[20:23], v[36:39]\n"
    "v_add_u32 v13, v5, v3\n"
    "v_mfma_f32_16x16x32_bf16 v[48:51], v[20:23], v[216:219], v[48:51]\n"
    "v_cmp_gt_i32 vcc, s71, v13\n"
    "v_mfma_f32_16x16x32_bf16 v[56:59], v[20:23], v[220:223], v[56:59]\n"
    "v_mfma_f32_16x16x32_bf16 v[64:67], v[224:227], v[20:23], v[64:67]\n"
    "v_mfma_f32_16x16x32_bf16 v[68:71], v[228:231], v[20:23], v[68:71]\n"
    "ds_read_b128 v[20:23], v9 offset:1024\n"
    "v_mfma_f32_16x16x32_bf16 v[32:35], v[232:235], v[24:27], v[32:35]\n"
    "s_or_b64 exec, vcc, s[62:63]\n"
    "buffer_atomic_smax v5, v2, s[36:39], 0 offen sc0 sc1\n"
    "s_mov_b64 exec, -1\n"
    "v_mfma_f32_16x16x32_bf16 v[36:39], v[236:239], v[24:27], v[36:39]\n"
    "v_mfma_f32_16x16x32_bf16 v[48:51], v[24:27], v[240:243], v[48:51]\n"
    "v_mfma_f32_16x16x32_bf16 v[56:59], v[24:27], v[244:247], v[56:59]\n"
    "s_mov_b32 s69, 0x80000000\n"
    "s_mov_b32 s70, 0\n"
    "v_mov_b32 v12, s68\n"
    "v_mfma_f32_16x16x32_bf16 v[64:67], v[248:251], v[24:27], v[64:67]\n"
    "s_waitcnt lgkmcnt(1)\n"
    "s_add_u32 s68, s68, 1\n"
    "v_mfma_f32_16x16x32_bf16 v[68:71], v[252:255], v[24:27], v[68:71]\n"
    "ds_read_b128 v[24:27], v9 offset:2048\n"
    ".LgP_head1_%=:\n"
    "v_mfma_f32_16x16x32_bf16 v[40:43], a[0:3], v[16:19], 0\n"
    "v_mfma_f32_16x16x32_bf16 v[44:47], a[4:7], v[16:19], 0\n"
    "v_cvt_pk_bf16_f32 v32, v32, v33\n"
    "v_cvt_pk_bf16_f32 v33, v34, v35\n"
    "v_mfma_f32_16x16x32_bf16 v[52:55], v[16:19], a[8:11], 0\n"
    "v_cvt_pk_bf16_f32 v34, v36, v37\n"
    "v_cvt_pk_bf16_f32 v35, v38, v39\n"
    "v_mfma_f32_16x16x32_bf16 v[60:63], v[16:19], a[12:15], 0\n"
    "buffer_store_dwordx4 v[32:35], v1, s[36:39], s79 offen sc1\n"
    "v_mfma_f32_16x16x32_bf16 v[72:75], a[16:19], v[16:19], 0\n"
    "s_waitcnt lgkmcnt(1)\n"
    "v_cvt_pk_bf16_f32 v28, v48, v49\n"
    "v_cvt_pk_bf16_f32 v29, v50, v51\n"
    "v_mfma_f32_16x16x32_bf16 v[76:79], a[20:23], v[16:19], 0\n"
    "ds_read_b128 v[16:19], v9 offset:3072\n"
    "v_cvt_pk_bf16_f32 v30, v56, v57\n"
    "v_mfma_f32_16x16x32_bf16 v[40:43], a[24:27], v[20:23], v[40:43]\n"
    "v_cvt_pk_bf16_f32 v31, v58, v59\n"
    "v_mfma_f32_16x16x32_bf16 v[44:47], a[28:31], v[20:23], v[44:47]\n"
    "s_waitcnt vmcnt(2)\n"
    "v_cvt_pk_bf16_f32 v64, v64, v65\n"
    "v_add_u32 v13, v11, v3\n"
    "v_mfma_f32_16x16x32_bf16 v[52:55], v[20:23], a[32:35], v[52:55]\n"
    "v_cvt_pk_bf16_f32 v65, v66, v67\n"
    "v_cmp_gt_i32 vcc, s71, v13\n"
    "v_mfma_f32_16x16x32_bf16 v[60:63], v[20:23], a[36:39], v[60:63]\n"
    "buffer_atomic_smax v7, v14, s[36:39], s69 offen sc1\n"
    "v_cvt_pk_bf16_f32 v66, v68, v69\n"
    "v_cvt_pk_bf16_f32 v67, v70, v71\n"
    "v_mfma_f32_16x16x32_bf16 v[72:75], a[40:43], v[20:23], v[72:75]\n"
    "s_waitcnt lgkmcnt(1)\n"
    "v_mfma_f32_16x16x32_bf16 v[76:79], a[44:47], v[20:23], v[76:79]\n"
    "ds_read_b128 v[20:23], v9 offset:4096\n"
    "s_cbranch_vccnz .LgP_poll1_%=\n"
    "v_mfma_f32_16x16x32_bf16 v[40:43], a[48:51], v[24:27], v[40:43]\n"
    "s_add_u32 m0, s59, 0\n"
    "v_mfma_f32_16x16x32_bf16 v[44:47], a[52:55], v[24:27], v[44:47]\n"
    "buffer_store_dwordx4 v[64:67], v1, s[36:39], s73 offen sc1\n"
    "v_mfma_f32_16x16x32_bf16 v[52:55], v[24:27], a[56:59], v[52:55]\n"
    "buffer_load_dwordx4 v1, s[36:39], s76 offen sc1 lds\n"
    "s_lshr_b32 s73, s68, 1\n"
    "v_mfma_f32_16x16x32_bf16 v[60:63], v[24:27], a[60:63], v[60:63]\n"
    "s_and_b32 s73, s73, s57\n"
    "v_mfma_f32_16x16x32_bf16 v[72:75], a[64:67], v[24:27], v[72:75]\n"
    "s_waitcnt lgkmcnt(1)\n"
    "s_mul_i32 s73, s73, s56\n"
    "v_mfma_f32_16x16x32_bf16 v[76:79], a[68:71], v[24:27], v[76:79]\n"
    "ds_read_b128 v[24:27], v9 offset:5120\n"
    "s_add_u32 s73, s73, s55\n"
    "v_mfma_f32_16x16x32_bf16 v[40:43], a[72:75], v[16:19], v[40:43]\n"
    "s_add_u32 s79, s73, 16384\n"
    "v_mfma_f32_16x16x32_bf16 v[44:47], a[76:79], v[16:19], v[44:47]\n"
    "s_add_u32 s80, s73, 32768\n"
    "v_mfma_f32_16x16x32_bf16 v[52:55], v[16:19], a[80:83], v[52:55]\n"
    "s_add_u32 s76, s76, 16384\n"
    "s_add_u32 m0, s59, 4096\n"
    "v_mfma_f32_16x16x32_bf16 v[60:63], v[16:19], a[84:87], v[60:63]\n"
    "buffer_load_dwordx4 v1, s[36:39], s76 offen sc1 lds\n"
    "v_mfma_f32_16x16x32_bf16 v[72:75], a[88:91], v[16:19], v[72:75]\n"
    "s_waitcnt lgkmcnt(1)\n"
    "v_mfma_f32_16x16x32_bf16 v[76:79], a[92:95], v[16:19], v[76:79]\n"
    "ds_read_b128 v[16:19], v9 offset:6144\n"
    "v_mfma_f32_16x16x32_bf16 v[40:43], a[96:99], v[20:23], v[40:43]\n"
    "v_mfma_f32_16x16x32_bf16 v[44:47], a[100:103], v[20:23], v[44:47]\n"
    "v_mfma_f32_16x16x32_bf16 v[52:55], v[20:23], a[104:107], v[52:55]\n"
    "v_mfma_f32_16x16x32_bf16 v[60:63], v[20:23], a[108:111], v[60:63]\n"
    "v_mfma_f32_16x16x32_bf16 v[72:75], a[112:115], v[20:23], v[72:75]\n"
    "s_waitcnt lgkmcnt(1)\n"
    "v_mfma_f32_16x16x32_bf16 v[76:79], a[116:119], v[20:23], v[76:79]\n"
    "ds_read_b128 v[20:23], v9 offset:7168\n"
    "v_mfma_f32_16x16x32_bf16 v[40:43], a[120:123], v[24:27], v[40:43]\n"
    "s_add_u32 s76, s76, 16384\n"
    "s_add_u32 m0, s59, 8192\n"
    "v_mfma_f32_16x16x32_bf16 v[44:47], a[124:127], v[24:27], v[44:47]\n"
    "buffer_load_dwordx4 v1, s[36:39], s76 offen sc1 lds\n"
    "v_mfma_f32_16x16x32_bf16 v[52:55], v[24:27], a[128:131], v[52:55]\n"
    "v_mfma_f32_16x16x32_bf16 v[60:63], v[24:27], a[132:135], v[60:63]\n"
    "v_mfma_f32_16x16x32_bf16 v[72:75], a[136:139], v[24:27], v[72:75]\n"
    "s_waitcnt lgkmcnt(1)\n"
    "v_mfma_f32_16x16x32_bf16 v[76:79], a[140:143], v[24:27], v[76:79]\n"
    "ds_read_b128 v[24:27], v9 offset:8192\n"
    "v_mfma_f32_16x16x32_bf16 v[40:43], a[144:147], v[16:19], v[40:43]\n"
    "v_mfma_f32_16x16x32_bf16 v[44:47], a[148:151], v[16:19], v[44:47]\n"
    "v_mfma_f32_16x16x32_bf16 v[52:55], v[16:19], a[152:155], v[52:55]\n"
    "v_mfma_f32_16x16x32_bf16 v[60:63], v[16:19], a[156:159], v[60:63]\n"
    "v_mfma_f32_16x16x32_bf16 v[72:75], a[160:163], v[16:19], v[72:75]\n"
    "s_waitcnt lgkmcnt(1)\n"
    "s_add_u32 s76, s76, 16384\n"
    "s_add_u32 m0, s59, 12288\n"
    "v_mfma_f32_16x16x32_bf16 v[76:79], a[164:167], v[16:19], v[76:79]\n"
    "ds_read_b128 v[16:19], v9 offset:9216\n"
    "buffer_load_dwordx4 v1, s[36:39], s76 offen sc1 lds\n"
    "v_mfma_f32_16x16x32_bf16 v[40:43], a[168:171], v[20:23], v[40:43]\n"
    "v_mfma_f32_16x16x32_bf16 v[44:47], a[172:175], v[20:23], v[44:47]\n"
    "v_mfma_f32_16x16x32_bf16 v[52:55], v[20:23], a[176:179], v[52:55]\n"
    "v_mfma_f32_16x16x32_bf16 v[60:63], v[20:23], a[180:183], v[60:63]\n"
    "v_mfma_f32_16x16x32_bf16 v[72:75], a[184:187], v[20:23], v[72:75]\n"
    "s_waitcnt lgkmcnt(1)\n"
    "v_mfma_f32_16x16x32_bf16 v[76:79], a[188:191], v[20:23], v[76:79]\n"
    "ds_read_b128 v[20:23], v9 offset:10240\n"
    "v_mfma_f32_16x16x32_bf16 v[40:43], a[192:195], v[24:27], v[40:43]\n"
    "v_mfma_f32_16x16x32_bf16 v[44:47], a[196:199], v[24:27], v[44:47]\n"
    "v_mfma_f32_16x16x32_bf16 v[52:55], v[24:27], a[200:203], v[52:55]\n"
    "s_add_u32 s76, s76, 16384\n"
    "s_add_u32 m0, s59, 16384\n"
    "v_mfma_f32_16x16x32_bf16 v[60:63], v[24:27], a[204:207], v[60:63]\n"
    "buffer_load_dwordx4 v8, s[36:39], s76 offen sc1 lds\n"
    "v_mfma_f32_16x16x32_bf16 v[72:75], a[208:211], v[24:27], v[72:75]\n"
    "s_waitcnt lgkmcnt(1)\n"
    "s_waitcnt vmcnt(5)\n"
    "buffer_atomic_smax v12, v14, s[36:39], s70 offen sc1\n"
    "v_mfma_f32_16x16x32_bf16 v[76:79], a[212:215], v[24:27], v[76:79]\n"
    "ds_read_b128 v[24:27], v9 offset:11264\n"
    "v_mfma_f32_16x16x32_bf16 v[40:43], a[216:219], v[16:19], v[40:43]\n"
    "v_mfma_f32_16x16x32_bf16 v[44:47], a[220:223], v[16:19], v[44:47]\n"
    "v_mfma_f32_16x16x32_bf16 v[52:55], v[16:19], a[224:227], v[52:55]\n"
    "v_mfma_f32_16x16x32_bf16 v[60:63], v[16:19], a[228:231], v[60:63]\n"
    "v_mfma_f32_16x16x32_bf16 v[72:75], a[232:235], v[16:19], v[72:75]\n"
    "s_waitcnt lgkmcnt(1)\n"
    "v_mfma_f32_16x16x32_bf16 v[76:79], a[236:239], v[16:19], v[76:79]\n"
    "ds_read_b128 v[16:19], v9 offset:12288\n"
    "v_mfma_f32_16x16x32_bf16 v[40:43], a[240:243], v[20:23], v[40:43]\n"
    "v_mfma_f32_16x16x32_bf16 v[44:47], a[244:247], v[20:23], v[44:47]\n"
    "v_mfma_f32_16x16x32_bf16 v[52:55], v[20:23], a[248:251], v[52:55]\n"
    "v_mfma_f32_16x16x32_bf16 v[60:63], v[20:23], a[252:255], v[60:63]\n"
    "v_mfma_f32_16x16x32_bf16 v[72:75], v[80:83], v[20:23], v[72:75]\n"
    "s_waitcnt lgkmcnt(1)\n"
    "v_mfma_f32_16x16x32_bf16 v[76:79], v[84:87], v[20:23], v[76:79]\n"
    "ds_read_b128 v[20:23], v9 offset:13312\n"
    "v_mfma_f32_16x16x32_bf16 v[40:43], v[88:91], v[24:27], v[40:43]\n"
    "v_mfma_f32_16x16x32_bf16 v[44:47], v[92:95], v[24:27], v[44:47]\n"
    "v_mfma_f32_16x16x32_bf16 v[52:55], v[24:27], v[96:99], v[52:55]\n"
    "v_mfma_f32_16x16x32_bf16 v[60:63], v[24:27], v[100:103], v[60:63]\n"
    "v_mfma_f32_16x16x32_bf16 v[72:75], v[104:107], v[24:27], v[72:75]\n"
    "s_waitcnt lgkmcnt(1)\n"
    "v_mfma_f32_16x16x32_bf16 v[76:79], v[108:111], v[24:27], v[76:79]\n"
    "ds_read_b128 v[24:27], v9 offset:14336\n"
    "v_mfma_f32_16x16x32_bf16 v[40:43], v[112:115], v[16:19], v[40:43]\n"
    "v_mfma_f32_16x16x32_bf16 v[44:47], v[116:119], v[16:19], v[44:47]\n"
    "v_mfma_f32_16x16x32_bf16 v[52:55], v[16:19], v[120:123], v[52:55]\n"
    "v_mfma_f32_16x16x32_bf16 v[60:63], v[16:19], v[124:127], v[60:63]\n"
    "v_mfma_f32_16x16x32_bf16 v[72:75], v[128:131], v[16:19], v[72:75]\n"
    "s_waitcnt lgkmcnt(1)\n"
    "v_mfma_f32_16x16x32_bf16 v[76:79], v[132:135], v[16:19], v[76:79]\n"
    "ds_read_b128 v[16:19], v9 offset:15360\n"
    ".LgP_join1_%=:\n"
    "v_mfma_f32_16x16x32_bf16 v[40:43], v[136:139], v[20:23], v[40:43]\n"
    "s_add_u32 s71, s68, 2\n"
    "s_waitcnt vmcnt(5)\n"
    "v_mfma_f32_16x16x32_bf16 v[44:47], v[140:143], v[20:23], v[44:47]\n"
    "v_add_u32 v13, v11, v3\n"
    "v_mfma_f32_16x16x32_bf16 v[52:55], v[20:23], v[144:147], v[52:55]\n"
    "v_cmp_gt_i32 vcc, s71, v13\n"
    "v_mfma_f32_16x16x32_bf16 v[60:63], v[20:23], v[148:151], v[60:63]\n"
    "v_mfma_f32_16x16x32_bf16 v[72:75], v[152:155], v[20:23], v[72:75]\n"
    "s_waitcnt lgkmcnt(1)\n"
    "v_mfma_f32_16x16x32_bf16 v[76:79], v[156:159], v[20:23], v[76:79]\n"
    "ds_read_b128 v[20:23], v9 offset:16384\n"
    "v_mfma_f32_16x16x32_bf16 v[40:43], v[160:163], v[24:27], v[40:43]\n"
    "s_or_b64 exec, vcc, s[62:63]\n"
    "buffer_atomic_smax v11, v2, s[36:39], 0 offen sc0 sc1\n"
    "s_mov_b64 exec, -1\n"
    "v_mfma_f32_16x16x32_bf16 v[44:47], v[164:167], v[24:27], v[44:47]\n"
    "s_lshr_b32 s76, s71, 1\n"
    "v_mfma_f32_16x16x32_bf16 v[52:55], v[24:27], v[168:171], v[52:55]\n"
    "s_and_b32 s76, s76, s54\n"
    "v_mfma_f32_16x16x32_bf16 v[60:63], v[24:27], v[172:175], v[60:63]\n"
    "s_mul_i32 s76, s76, s53\n"
    "v_mfma_f32_16x16x32_bf16 v[72:75], v[176:179], v[24:27], v[72:75]\n"
    "s_waitcnt lgkmcnt(1)\n"
    "s_add_u32 s76, s76, s52\n"
    "v_mfma_f32_16x16x32_bf16 v[76:79], v[180:183], v[24:27], v[76:79]\n"
    "ds_read_b128 v[24:27], v9 offset:17408\n"
    "s_add_u32 s76, s76, 1024\n"
    "v_mfma_f32_16x16x32_bf16 v[40:43], v[184:187], v[16:19], v[40:43]\n"
    "v_mfma_f32_16x16x32_bf16 v[44:47], v[188:191], v[16:19], v[44:47]\n"
    "v_mfma_f32_16x16x32_bf16 v[52:55], v[16:19], v[192:195], v[52:55]\n"
    "v_mfma_f32_16x16x32_bf16 v[60:63], v[16:19], v[196:199], v[60:63]\n"
    "s_waitcnt vmcnt(1) lgkmcnt(0)\n"
    "s_barrier\n"
    "v_mfma_f32_16x16x32_bf16 v[72:75], v[200:203], v[16:19], v[72:75]\n"
    "v_mfma_f32_16x16x32_bf16 v[76:79], v[204:207], v[16:19], v[76:79]\n"
    "ds_read_b128 v[16:19], v0 offset:0\n"
    "v_mfma_f32_16x16x32_bf16 v[40:43], v[208:211], v[20:23], v[40:43]\n"
    "v_mfma_f32_16x16x32_bf16 v[44:47], v[212:215], v[20:23], v[44:47]\n"
    "v_add_u32 v13, v5, v3\n"
    "v_mfma_f32_16x16x32_bf16 v[52:55], v[20:23], v[216:219], v[52:55]\n"
    "v_cmp_gt_i32 vcc, s71, v13\n"
    "v_mfma_f32_16x16x32_bf16 v[60:63], v[20:23], v[220:223], v[60:63]\n"
    "v_mfma_f32_16x16x32_bf16 v[72:75], v[224:227], v[20:23], v[72:75]\n"
    "v_mfma_f32_16x16x32_bf16 v[76:79], v[228:231], v[20:23], v[76:79]\n"
    "ds_read_b128 v[20:23], v0 offset:1024\n"
    "v_mfma_f32_16x16x32_bf16 v[40:43], v[232:235], v[24:27], v[40:43]\n"
    "s_or_b64 exec, vcc, s[62:63]\n"
    "buffer_atomic_smax v5, v2, s[36:39], 0 offen sc0 sc1\n"
    "s_mov_b64 exec, -1\n"
    "v_mfma_f32_16x16x32_bf16 v[44:47], v[236:239], v[24:27], v[44:47]\n"
    "v_mfma_f32_16x16x32_bf16 v[52:55], v[24:27], v[240:243], v[52:55]\n"
    "v_mfma_f32_16x16x32_bf16 v[60:63], v[24:27], v[244:247], v[60:63]\n"
    "s_mov_b32 s69, 0x80000000\n"
    "s_mov_b32 s70, 0\n"
    "v_mov_b32 v12, s68\n"
    "v_mfma_f32_16x16x32_bf16 v[72:75], v[248:251], v[24:27], v[72:75]\n"
    "s_waitcnt lgkmcnt(1)\n"
    "s_add_u32 s68, s68, 1\n"
    "v_mfma_f32_16x16x32_bf16 v[76:79], v[252:255], v[24:27], v[76:79]\n"
    "ds_read_b128 v[24:27], v0 offset:2048\n"
    "s_branch .LgP_head0_%=\n"
    ".LgP_LOOPE_%=:\n"
    ".LgP_done_%=:\n"
    "s_waitcnt vmcnt(0)\n"
    "buffer_atomic_smax v12, v10, s[36:39], 0 offen sc1\n"
    "s_waitcnt vmcnt(0)\n"
    "s_branch .LgP_FIN_%=\n"
    ".LgP_poll0_%=:\n"
    "v_mfma_f32_16x16x32_bf16 v[32:35], a[48:51], v[24:27], v[32:35]\n"
    "s_or_b64 exec, vcc, s[62:63]\n"
    "buffer_atomic_smax v11, v2, s[36:39], 0 offen sc0 sc1\n"
    "s_mov_b64 exec, -1\n"
    "v_mfma_f32_16x16x32_bf16 v[36:39], a[52:55], v[24:27], v[36:39]\n"
    "buffer_store_dwordx4 v[60:63], v1, s[36:39], s80 offen offset:1024 sc1\n"
    "v_mfma_f32_16x16x32_bf16 v[48:51], v[24:27], a[56:59], v[48:51]\n"
    "v_cvt_pk_bf16_f32 v74, v76, v77\n"
    "v_cvt_pk_bf16_f32 v75, v78, v79\n"
    "v_mfma_f32_16x16x32_bf16 v[56:59], v[24:27], a[60:63], v[56:59]\n"
    "v_mfma_f32_16x16x32_bf16 v[64:67], a[64:67], v[24:27], v[64:67]\n"
    "s_waitcnt lgkmcnt(1)\n"
    "v_mfma_f32_16x16x32_bf16 v[68:71], a[68:71], v[24:27], v[68:71]\n"
    "ds_read_b128 v[24:27], v0 offset:5120\n"
    "v_mfma_f32_16x16x32_bf16 v[32:35], a[72:75], v[16:19], v[32:35]\n"
    "buffer_store_dwordx4 v[72:75], v1, s[36:39], s73 offen offset:1024 sc1\n"
    "v_mfma_f32_16x16x32_bf16 v[36:39], a[76:79], v[16:19], v[36:39]\n"
    "s_lshr_b32 s73, s68, 1\n"
    "v_mfma_f32_16x16x32_bf16 v[48:51], v[16:19], a[80:83], v[48:51]\n"
    "s_and_b32 s73, s73, s57\n"
    "v_mfma_f32_16x16x32_bf16 v[56:59], v[16:19], a[84:87], v[56:59]\n"
    "s_mul_i32 s73, s73, s56\n"
    "v_mfma_f32_16x16x32_bf16 v[64:67], a[88:91], v[16:19], v[64:67]\n"
    "s_waitcnt lgkmcnt(1)\n"
    "s_add_u32 s73, s73, s55\n"
    "v_mfma_f32_16x16x32_bf16 v[68:71], a[92:95], v[16:19], v[68:71]\n"
    "ds_read_b128 v[16:19], v0 offset:6144\n"
    "s_add_u32 s79, s73, 16384\n"
    "v_mfma_f32_16x16x32_bf16 v[32:35], a[96:99], v[20:23], v[32:35]\n"
    "s_add_u32 s80, s73, 32768\n"
    "v_mfma_f32_16x16x32_bf16 v[36:39], a[100:103], v[20:23], v[36:39]\n"
    "s_waitcnt vmcnt(6)\n"
    "v_add_u32 v13, v5, v3\n"
    "v_mfma_f32_16x16x32_bf16 v[48:51], v[20:23], a[104:107], v[48:51]\n"
    "v_cmp_gt_i32 vcc, s71, v13\n"
    "v_mfma_f32_16x16x32_bf16 v[56:59], v[20:23], a[108:111], v[56:59]\n"
    "v_mfma_f32_16x16x32_bf16 v[64:67], a[112:115], v[20:23], v[64:67]\n"
    "s_waitcnt lgkmcnt(1)\n"
    "v_mfma_f32_16x16x32_bf16 v[68:71], a[116:119], v[20:23], v[68:71]\n"
    "ds_read_b128 v[20:23], v0 offset:7168\n"
    "s_cbranch_vccz .LgP_dma0_30_%=\n"
    "v_mfma_f32_16x16x32_bf16 v[32:35], a[120:123], v[24:27], v[32:35]\n"
    "s_or_b64 exec, vcc, s[62:63]\n"
    "buffer_atomic_smax v5, v2, s[36:39], 0 offen sc0 sc1\n"
    "s_mov_b64 exec, -1\n"
    "v_mfma_f32_16x16x32_bf16 v[36:39], a[124:127], v[24:27], v[36:39]\n"
    "v_mfma_f32_16x16x32_bf16 v[48:51], v[24:27], a[128:131], v[48:51]\n"
    "v_mfma_f32_16x16x32_bf16 v[56:59], v[24:27], a[132:135], v[56:59]\n"
    "v_mfma_f32_16x16x32_bf16 v[64:67], a[136:139], v[24:27], v[64:67]\n"
    "s_waitcnt lgkmcnt(1)\n"
    "v_mfma_f32_16x16x32_bf16 v[68:71], a[140:143], v[24:27], v[68:71]\n"
    "ds_read_b128 v[24:27], v0 offset:8192\n"
    "v_mfma_f32_16x16x32_bf16 v[32:35], a[144:147], v[16:19], v[32:35]\n"
    "v_mfma_f32_16x16x32_bf16 v[36:39], a[148:151], v[16:19], v[36:39]\n"
    "v_mfma_f32_16x16x32_bf16 v[48:51], v[16:19], a[152:155], v[48:51]\n"
    "v_mfma_f32_16x16x32_bf16 v[56:59], v[16:19], a[156:159], v[56:59]\n"
    "v_mfma_f32_16x16x32_bf16 v[64:67], a[160:163], v[16:19], v[64:67]\n"
    "s_waitcnt lgkmcnt(1)\n"
    "v_mfma_f32_16x16x32_bf16 v[68:71], a[164:167], v[16:19], v[68:71]\n"
    "ds_read_b128 v[16:19], v0 offset:9216\n"
    "v_mfma_f32_16x16x32_bf16 v[32:35], a[168:171], v[20:23], v[32:35]\n"
    "v_mfma_f32_16x16x32_bf16 v[36:39], a[172:175], v[20:23], v[36:39]\n"
    "s_waitcnt vmcnt(3)\n"
    "v_add_u32 v13, v11, v3\n"
    "v_mfma_f32_16x16x32_bf16 v[48:51], v[20:23], a[176:179], v[48:51]\n"
    "v_cmp_gt_i32 vcc, s71, v13\n"
    "v_mfma_f32_16x16x32_bf16 v[56:59], v[20:23], a[180:183], v[56:59]\n"
    "v_mfma_f32_16x16x32_bf16 v[64:67], a[184:187], v[20:23], v[64:67]\n"
    "s_waitcnt lgkmcnt(1)\n"
    "v_mfma_f32_16x16x32_bf16 v[68:71], a[188:191], v[20:23], v[68:71]\n"
    "ds_read_b128 v[20:23], v0 offset:10240\n"
    "s_cbranch_vccz .LgP_dma0_48_%=\n"
    "v_mfma_f32_16x16x32_bf16 v[32:35], a[192:195], v[24:27], v[32:35]\n"
    "s_or_b64 exec, vcc, s[62:63]\n"
    "buffer_atomic_smax v11, v2, s[36:39], 0 offen sc0 sc1\n"
    "s_mov_b64 exec, -1\n"
    "v_mfma_f32_16x16x32_bf16 v[36:39], a[196:199], v[24:27], v[36:39]\n"
    "v_mfma_f32_16x16x32_bf16 v[48:51], v[24:27], a[200:203], v[48:51]\n"
    "v_mfma_f32_16x16x32_bf16 v[56:59], v[24:27], a[204:207], v[56:59]\n"
    "v_mfma_f32_16x16x32_bf16 v[64:67], a[208:211], v[24:27], v[64:67]\n"
    "s_waitcnt lgkmcnt(1)\n"
    "v_mfma_f32_16x16x32_bf16 v[68:71], a[212:215], v[24:27], v[68:71]\n"
    "ds_read_b128 v[24:27], v0 offset:11264\n"
    "v_mfma_f32_16x16x32_bf16 v[32:35], a[216:219], v[16:19], v[32:35]\n"
    "v_mfma_f32_16x16x32_bf16 v[36:39], a[220:223], v[16:19], v[36:39]\n"
    "v_mfma_f32_16x16x32_bf16 v[48:51], v[16:19], a[224:227], v[48:51]\n"
    "v_mfma_f32_16x16x32_bf16 v[56:59], v[16:19], a[228:231], v[56:59]\n"
    "v_mfma_f32_16x16x32_bf16 v[64:67], a[232:235], v[16:19], v[64:67]\n"
    "s_waitcnt lgkmcnt(1)\n"
    "s_waitcnt vmcnt(2)\n"
    "buffer_atomic_smax v12, v10, s[36:39], s70 offen sc1\n"
    "v_mfma_f32_16x16x32_bf16 v[68:71], a[236:239], v[16:19], v[68:71]\n"
    "ds_read_b128 v[16:19], v0 offset:12288\n"
    "v_mfma_f32_16x16x32_bf16 v[32:35], a[240:243], v[20:23], v[32:35]\n"
    "v_mfma_f32_16x16x32_bf16 v[36:39], a[244:247], v[20:23], v[36:39]\n"
    "s_waitcnt vmcnt(2)\n"
    "v_add_u32 v13, v5, v3\n"
    "v_mfma_f32_16x16x32_bf16 v[48:51], v[20:23], a[248:251], v[48:51]\n"
    "v_cmp_gt_i32 vcc, s71, v13\n"
    "v_mfma_f32_16x16x32_bf16 v[56:59], v[20:23], a[252:255], v[56:59]\n"
    "v_mfma_f32_16x16x32_bf16 v[64:67], v[80:83], v[20:23], v[64:67]\n"
    "s_waitcnt lgkmcnt(1)\n"
    "v_mfma_f32_16x16x32_bf16 v[68:71], v[84:87], v[20:23], v[68:71]\n"
    "ds_read_b128 v[20:23], v0 offset:13312\n"
    "s_cbranch_vccz .LgP_dma0_66_%=\n"
    "v_mfma_f32_16x16x32_bf16 v[32:35], v[88:91], v[24:27], v[32:35]\n"
    "s_or_b64 exec, vcc, s[62:63]\n"
    "buffer_atomic_smax v5, v2, s[36:39], 0 offen sc0 sc1\n"
    "s_mov_b64 exec, -1\n"
    "v_mfma_f32_16x16x32_bf16 v[36:39], v[92:95], v[24:27], v[36:39]\n"
    "v_mfma_f32_16x16x32_bf16 v[48:51], v[24:27], v[96:99], v[48:51]\n"
    "v_mfma_f32_16x16x32_bf16 v[56:59], v[24:27], v[100:103], v[56:59]\n"
    "v_mfma_f32_16x16x32_bf16 v[64:67], v[104:107], v[24:27], v[64:67]\n"
    "s_waitcnt lgkmcnt(1)\n"
    "v_mfma_f32_16x16x32_bf16 v[68:71], v[108:111], v[24:27], v[68:71]\n"
    "ds_read_b128 v[24:27], v0 offset:14336\n"
    "v_mfma_f32_16x16x32_bf16 v[32:35], v[112:115], v[16:19], v[32:35]\n"
    "v_mfma_f32_16x16x32_bf16 v[36:39], v[116:119], v[16:19], v[36:39]\n"
    "v_mfma_f32_16x16x32_bf16 v[48:51], v[16:19], v[120:123], v[48:51]\n"
    "v_mfma_f32_16x16x32_bf16 v[56:59], v[16:19], v[124:127], v[56:59]\n"
    "v_mfma_f32_16x16x32_bf16 v[64:67], v[128:131], v[16:19], v[64:67]\n"
    "s_waitcnt lgkmcnt(1)\n"
    "v_mfma_f32_16x16x32_bf16 v[68:71], v[132:135], v[16:19], v[68:71]\n"
    "ds_read_b128 v[16:19], v0 offset:15360\n"
    "v_mfma_f32_16x16x32_bf16 v[32:35], v[136:139], v[20:23], v[32:35]\n"
    "v_mfma_f32_16x16x32_bf16 v[36:39], v[140:143], v[20:23], v[36:39]\n"
    "s_waitcnt vmcnt(2)\n"
    "v_add_u32 v13, v11, v3\n"
    "v_mfma_f32_16x16x32_bf16 v[48:51], v[20:23], v[144:147], v[48:51]\n"
    "v_cmp_gt_i32 vcc, s71, v13\n"
    "v_mfma_f32_16x16x32_bf16 v[56:59], v[20:23], v[148:151], v[56:59]\n"
    "v_mfma_f32_16x16x32_bf16 v[64:67], v[152:155], v[20:23], v[64:67]\n"
    "s_waitcnt lgkmcnt(1)\n"
    "v_mfma_f32_16x16x32_bf16 v[68:71], v[156:159], v[20:23], v[68:71]\n"
    "ds_read_b128 v[20:23], v0 offset:16384\n"
    "s_cbranch_vccz .LgP_dma0_84_%=\n"
    "v_mfma_f32_16x16x32_bf16 v[32:35], v[160:163], v[24:27], v[32:35]\n"
    "s_or_b64 exec, vcc, s[62:63]\n"
    "buffer_atomic_smax v11, v2, s[36:39], 0 offen sc0 sc1\n"
    "s_mov_b64 exec, -1\n"
    "v_mfma_f32_16x16x32_bf16 v[36:39], v[164:167], v[24:27], v[36:39]\n"
    "v_mfma_f32_16x16x32_bf16 v[48:51], v[24:27], v[168:171], v[48:51]\n"
    "v_mfma_f32_16x16x32_bf16 v[56:59], v[24:27], v[172:175], v[56:59]\n"
    "v_mfma_f32_16x16x32_bf16 v[64:67], v[176:179], v[24:27], v[64:67]\n"
    "s_waitcnt lgkmcnt(1)\n"
    "v_mfma_f32_16x16x32_bf16 v[68:71], v[180:183], v[24:27], v[68:71]\n"
    "ds_read_b128 v[24:27], v0 offset:17408\n"
    "v_mfma_f32_16x16x32_bf16 v[32:35], v[184:187], v[16:19], v[32:35]\n"
    "v_mfma_f32_16x16x32_bf16 v[36:39], v[188:191], v[16:19], v[36:39]\n"
    "v_mfma_f32_16x16x32_bf16 v[48:51], v[16:19], v[192:195], v[48:51]\n"
    "v_mfma_f32_16x16x32_bf16 v[56:59], v[16:19], v[196:199], v[56:59]\n"
    "v_mfma_f32_16x16x32_bf16 v[64:67], v[200:203], v[16:19], v[64:67]\n"
    "s_waitcnt lgkmcnt(1)\n"
    "v_mfma_f32_16x16x32_bf16 v[68:71], v[204:207], v[16:19], v[68:71]\n"
    "v_mfma_f32_16x16x32_bf16 v[32:35], v[208:211], v[20:23], v[32:35]\n"
    "v_mfma_f32_16x16x32_bf16 v[36:39], v[212:215], v[20:23], v[36:39]\n"
    "s_waitcnt vmcnt(1)\n"
    "v_add_u32 v13, v5, v3\n"
    "v_mfma_f32_16x16x32_bf16 v[48:51], v[20:23], v[216:219], v[48:51]\n"
    "v_cmp_gt_i32 vcc, s71, v13\n"
    "v_mfma_f32_16x16x32_bf16 v[56:59], v[20:23], v[220:223], v[56:59]\n"
    "v_mfma_f32_16x16x32_bf16 v[64:67], v[224:227], v[20:23], v[64:67]\n"
    "s_waitcnt lgkmcnt(0)\n"
    "v_mfma_f32_16x16x32_bf16 v[68:71], v[228:231], v[20:23], v[68:71]\n"
    "s_cbranch_vccz .LgP_dma0_102_%=\n"
    "v_mfma_f32_16x16x32_bf16 v[32:35], v[232:235], v[24:27], v[32:35]\n"
    "s_or_b64 exec, vcc, s[62:63]\n"
    "buffer_atomic_smax v5, v2, s[36:39], 0 offen sc0 sc1\n"
    "s_mov_b64 exec, -1\n"
    "v_mfma_f32_16x16x32_bf16 v[36:39], v[236:239], v[24:27], v[36:39]\n"
    "v_mfma_f32_16x16x32_bf16 v[48:51], v[24:27], v[240:243], v[48:51]\n"
    "v_mfma_f32_16x16x32_bf16 v[56:59], v[24:27], v[244:247], v[56:59]\n"
    "v_mfma_f32_16x16x32_bf16 v[64:67], v[248:251], v[24:27], v[64:67]\n"
    "v_mfma_f32_16x16x32_bf16 v[68:71], v[252:255], v[24:27], v[68:71]\n"
    "v_cvt_pk_bf16_f32 v32, v32, v33\n"
    "v_cvt_pk_bf16_f32 v33, v34, v35\n"
    "v_cvt_pk_bf16_f32 v34, v36, v37\n"
    "v_cvt_pk_bf16_f32 v35, v38, v39\n"
    "buffer_store_dwordx4 v[32:35], v1, s[36:39], s79 offen sc1\n"
    "v_cvt_pk_bf16_f32 v28, v48, v49\n"
    "v_cvt_pk_bf16_f32 v29, v50, v51\n"
    "v_cvt_pk_bf16_f32 v30, v56, v57\n"
    "v_cvt_pk_bf16_f32 v31, v58, v59\n"
    "v_cvt_pk_bf16_f32 v64, v64, v65\n"
    "v_cvt_pk_bf16_f32 v65, v66, v67\n"
    "v_cvt_pk_bf16_f32 v66, v68, v69\n"
    "v_cvt_pk_bf16_f32 v67, v70, v71\n"
    "buffer_store_dwordx4 v[64:67], v1, s[36:39], s73 offen sc1\n"
    "v_mov_b32 v12, s68\n"
    "s_mov_b32 s73, 0x80000000\n"
    "s_mov_b32 s79, 0x80000000\n"
    "s_mov_b32 s80, 0x80000000\n"
    "s_mov_b32 s68, s71\n"
    "s_waitcnt vmcnt(3)\n"
    "v_add_u32 v13, v11, v3\n"
    "v_cmp_gt_i32 vcc, s71, v13\n"
    "s_cbranch_vccnz .LgP_idle0_%=\n"
    "s_add_u32 m0, s59, 20480\n"
    "s_nop 0\n"
    "buffer_load_dwordx4 v1, s[36:39], s76 offen sc1 lds\n"
    "s_add_u32 m0, s59, 24576\n"
    "s_add_u32 s76, s76, 16384\n"
    "buffer_load_dwordx4 v1, s[36:39], s76 offen sc1 lds\n"
    "s_add_u32 m0, s59, 28672\n"
    "s_add_u32 s76, s76, 16384\n"
    "buffer_load_dwordx4 v1, s[36:39], s76 offen sc1 lds\n"
    "s_add_u32 m0, s59, 32768\n"
    "s_add_u32 s76, s76, 16384\n"
    "buffer_load_dwordx4 v1, s[36:39], s76 offen sc1 lds\n"
    "s_add_u32 m0, s59, 36864\n"
    "s_add_u32 s76, s76, 16384\n"
    "buffer_load_dwordx4 v8, s[36:39], s76 offen sc1 lds\n"
    "s_waitcnt vmcnt(5)\n"
    "buffer_atomic_smax v12, v14, s[36:39], 0 offen sc1\n"
    "s_mov_b32 s69, 0x80000000\n"
    "s_mov_b32 s70, 0x80000000\n"
    "v_mov_b32 v7, v12\n"
    "s_waitcnt vmcnt(8)\n"
    "s_add_u32 s71, s68, 1\n"
    "v_add_u32 v13, v11, v3\n"
    "v_cmp_gt_i32 vcc, s71, v13\n"
    "s_nop 0\n"
    "s_or_b64 exec, vcc, s[62:63]\n"
    "buffer_atomic_smax v11, v2, s[36:39], 0 offen sc0 sc1\n"
    "s_mov_b64 exec, -1\n"
    "s_waitcnt vmcnt(2)\n"
    "s_branch .LgP_bar1_%=\n"
    ".LgP_dma0_30_%=:\n"
    "v_mfma_f32_16x16x32_bf16 v[32:35], a[120:123], v[24:27], v[32:35]\n"
    "s_add_u32 m0, s59, 20480\n"
    "v_mfma_f32_16x16x32_bf16 v[36:39], a[124:127], v[24:27], v[36:39]\n"
    "buffer_load_dwordx4 v1, s[36:39], s76 offen sc1 lds\n"
    "v_mfma_f32_16x16x32_bf16 v[48:51], v[24:27], a[128:131], v[48:51]\n"
    "s_add_u32 s76, s76, 16384\n"
    "s_add_u32 m0, s59, 24576\n"
    "v_mfma_f32_16x16x32_bf16 v[56:59], v[24:27], a[132:135], v[56:59]\n"
    "buffer_load_dwordx4 v1, s[36:39], s76 offen sc1 lds\n"
    "v_mfma_f32_16x16x32_bf16 v[64:67], a[136:139], v[24:27], v[64:67]\n"
    "s_waitcnt lgkmcnt(1)\n"
    "v_mfma_f32_16x16x32_bf16 v[68:71], a[140:143], v[24:27], v[68:71]\n"
    "ds_read_b128 v[24:27], v0 offset:8192\n"
    "v_mfma_f32_16x16x32_bf16 v[32:35], a[144:147], v[16:19], v[32:35]\n"
    "s_add_u32 s76, s76, 16384\n"
    "s_add_u32 m0, s59, 28672\n"
    "v_mfma_f32_16x16x32_bf16 v[36:39], a[148:151], v[16:19], v[36:39]\n"
    "buffer_load_dwordx4 v1, s[36:39], s76 offen sc1 lds\n"
    "v_mfma_f32_16x16x32_bf16 v[48:51], v[16:19], a[152:155], v[48:51]\n"
    "v_mfma_f32_16x16x32_bf16 v[56:59], v[16:19], a[156:159], v[56:59]\n"
    "v_mfma_f32_16x16x32_bf16 v[64:67], a[160:163], v[16:19], v[64:67]\n"
    "s_waitcnt lgkmcnt(1)\n"
    "s_add_u32 s76, s76, 16384\n"
    "s_add_u32 m0, s59, 32768\n"
    "v_mfma_f32_16x16x32_bf16 v[68:71], a[164:167], v[16:19], v[68:71]\n"
    "ds_read_b128 v[16:19], v0 offset:9216\n"
    "buffer_load_dwordx4 v1, s[36:39], s76 offen sc1 lds\n"
    "v_mfma_f32_16x16x32_bf16 v[32:35], a[168:171], v[20:23], v[32:35]\n"
    "v_mfma_f32_16x16x32_bf16 v[36:39], a[172:175], v[20:23], v[36:39]\n"
    "v_mfma_f32_16x16x32_bf16 v[48:51], v[20:23], a[176:179], v[48:51]\n"
    "s_add_u32 s76, s76, 16384\n"
    "s_add_u32 m0, s59, 36864\n"
    "v_mfma_f32_16x16x32_bf16 v[56:59], v[20:23], a[180:183], v[56:59]\n"
    "buffer_load_dwordx4 v8, s[36:39], s76 offen sc1 lds\n"
    "v_mfma_f32_16x16x32_bf16 v[64:67], a[184:187], v[20:23], v[64:67]\n"
    "s_waitcnt lgkmcnt(1)\n"
    "v_mfma_f32_16x16x32_bf16 v[68:71], a[188:191], v[20:23], v[68:71]\n"
    "ds_read_b128 v[20:23], v0 offset:10240\n"
    "v_mfma_f32_16x16x32_bf16 v[32:35], a[192:195], v[24:27], v[32:35]\n"
    "v_mfma_f32_16x16x32_bf16 v[36:39], a[196:199], v[24:27], v[36:39]\n"
    "v_mfma_f32_16x16x32_bf16 v[48:51], v[24:27], a[200:203], v[48:51]\n"
    "v_mfma_f32_16x16x32_bf16 v[56:59], v[24:27], a[204:207], v[56:59]\n"
    "v_mfma_f32_16x16x32_bf16 v[64:67], a[208:211], v[24:27], v[64:67]\n"
    "s_waitcnt lgkmcnt(1)\n"
    "v_mfma_f32_16x16x32_bf16 v[68:71], a[212:215], v[24:27], v[68:71]\n"
    "ds_read_b128 v[24:27], v0 offset:11264\n"
    "v_mfma_f32_16x16x32_bf16 v[32:35], a[216:219], v[16:19], v[32:35]\n"
    "v_mfma_f32_16x16x32_bf16 v[36:39], a[220:223], v[16:19], v[36:39]\n"
    "v_mfma_f32_16x16x32_bf16 v[48:51], v[16:19], a[224:227], v[48:51]\n"
    "v_mfma_f32_16x16x32_bf16 v[56:59], v[16:19], a[228:231], v[56:59]\n"
    "v_mfma_f32_16x16x32_bf16 v[64:67], a[232:235], v[16:19], v[64:67]\n"
    "s_waitcnt lgkmcnt(1)\n"
    "s_waitcnt vmcnt(5)\n"
    "buffer_atomic_smax v12, v10, s[36:39], s70 offen sc1\n"
    "v_mfma_f32_16x16x32_bf16 v[68:71], a[236:239], v[16:19], v[68:71]\n"
    "ds_read_b128 v[16:19], v0 offset:12288\n"
    "v_mfma_f32_16x16x32_bf16 v[32:35], a[240:243], v[20:23], v[32:35]\n"
    "v_mfma_f32_16x16x32_bf16 v[36:39], a[244:247], v[20:23], v[36:39]\n"
    "v_mfma_f32_16x16x32_bf16 v[48:51], v[20:23], a[248:251], v[48:51]\n"
    "v_mfma_f32_16x16x32_bf16 v[56:59], v[20:23], a[252:255], v[56:59]\n"
    "v_mfma_f32_16x16x32_bf16 v[64:67], v[80:83], v[20:23], v[64:67]\n"
    "s_waitcnt lgkmcnt(1)\n"
    "v_mfma_f32_16x16x32_bf16 v[68:71], v[84:87], v[20:23], v[68:71]\n"
    "ds_read_b128 v[20:23], v0 offset:13312\n"
    "v_mfma_f32_16x16x32_bf16 v[32:35], v[88:91], v[24:27], v[32:35]\n"
    "v_mfma_f32_16x16x32_bf16 v[36:39], v[92:95], v[24:27], v[36:39]\n"
    "v_mfma_f32_16x16x32_bf16 v[48:51], v[24:27], v[96:99], v[48:51]\n"
    "v_mfma_f32_16x16x32_bf16 v[56:59], v[24:27], v[100:103], v[56:59]\n"
    "v_mfma_f32_16x16x32_bf16 v[64:67], v[104:107], v[24:27], v[64:67]\n"
    "s_waitcnt lgkmcnt(1)\n"
    "v_mfma_f32_16x16x32_bf16 v[68:71], v[108:111], v[24:27], v[68:71]\n"
    "ds_read_b128 v[24:27], v0 offset:14336\n"
    "v_mfma_f32_16x16x32_bf16 v[32:35], v[112:115], v[16:19], v[32:35]\n"
    "v_mfma_f32_16x16x32_bf16 v[36:39], v[116:119], v[16:19], v[36:39]\n"
    "v_mfma_f32_16x16x32_bf16 v[48:51], v[16:19], v[120:123], v[48:51]\n"
    "v_mfma_f32_16x16x32_bf16 v[56:59], v[16:19], v[124:127], v[56:59]\n"
    "v_mfma_f32_16x16x32_bf16 v[64:67], v[128:131], v[16:19], v[64:67]\n"
    "s_waitcnt lgkmcnt(1)\n"
    "v_mfma_f32_16x16x32_bf16 v[68:71], v[132:135], v[16:19], v[68:71]\n"
    "ds_read_b128 v[16:19], v0 offset:15360\n"
    "s_branch .LgP_join0_%=\n"
    ".LgP_dma0_48_%=:\n"
    "v_mfma_f32_16x16x32_bf16 v[32:35], a[192:195], v[24:27], v[32:35]\n"
    "s_add_u32 m0, s59, 20480\n"
    "v_mfma_f32_16x16x32_bf16 v[36:39], a[196:199], v[24:27], v[36:39]\n"
    "buffer_load_dwordx4 v1, s[36:39], s76 offen sc1 lds\n"
    "v_mfma_f32_16x16x32_bf16 v[48:51], v[24:27], a[200:203], v[48:51]\n"
    "s_add_u32 s76, s76, 16384\n"
    "s_add_u32 m0, s59, 24576\n"
    "v_mfma_f32_16x16x32_bf16 v[56:59], v[24:27], a[204:207], v[56:59]\n"
    "buffer_load_dwordx4 v1, s[36:39], s76 offen sc1 lds\n"
    "v_mfma_f32_16x16x32_bf16 v[64:67], a[208:211], v[24:27], v[64:67]\n"
    "s_waitcnt lgkmcnt(1)\n"
    "v_mfma_f32_16x16x32_bf16 v[68:71], a[212:215], v[24:27], v[68:71]\n"
    "ds_read_b128 v[24:27], v0 offset:11264\n"
    "v_mfma_f32_16x16x32_bf16 v[32:35], a[216:219], v[16:19], v[32:35]\n"
    "s_add_u32 s76, s76, 16384\n"
    "s_add_u32 m0, s59, 28672\n"
    "v_mfma_f32_16x16x32_bf16 v[36:39], a[220:223], v[16:19], v[36:39]\n"
    "buffer_load_dwordx4 v1, s[36:39], s76 offen sc1 lds\n"
    "v_mfma_f32_16x16x32_bf16 v[48:51], v[16:19], a[224:227], v[48:51]\n"
    "v_mfma_f32_16x16x32_bf16 v[56:59], v[16:19], a[228:231], v[56:59]\n"
    "v_mfma_f32_16x16x32_bf16 v[64:67], a[232:235], v[16:19], v[64:67]\n"
    "s_waitcnt lgkmcnt(1)\n"
    "s_waitcnt vmcnt(4)\n"
    "buffer_atomic_smax v12, v10, s[36:39], s70 offen sc1\n"
    "v_mfma_f32_16x16x32_bf16 v[68:71], a[236:239], v[16:19], v[68:71]\n"
    "ds_read_b128 v[16:19], v0 offset:12288\n"
    "s_add_u32 s76, s76, 16384\n"
    "v_mfma_f32_16x16x32_bf16 v[32:35], a[240:243], v[20:23], v[32:35]\n"
    "s_add_u32 m0, s59, 32768\n"
    "v_mfma_f32_16x16x32_bf16 v[36:39], a[244:247], v[20:23], v[36:39]\n"
    "buffer_load_dwordx4 v1, s[36:39], s76 offen sc1 lds\n"
    "v_mfma_f32_16x16x32_bf16 v[48:51], v[20:23], a[248:251], v[48:51]\n"
    "s_add_u32 s76, s76, 16384\n"
    "s_add_u32 m0, s59, 36864\n"
    "v_mfma_f32_16x16x32_bf16 v[56:59], v[20:23], a[252:255], v[56:59]\n"
    "buffer_load_dwordx4 v8, s[36:39], s76 offen sc1 lds\n"
    "v_mfma_f32_16x16x32_bf16 v[64:67], v[80:83], v[20:23], v[64:67]\n"
    "s_waitcnt lgkmcnt(1)\n"
    "v_mfma_f32_16x16x32_bf16 v[68:71], v[84:87], v[20:23], v[68:71]\n"
    "ds_read_b128 v[20:23], v0 offset:13312\n"
    "v_mfma_f32_16x16x32_bf16 v[32:35], v[88:91], v[24:27], v[32:35]\n"
    "v_mfma_f32_16x16x32_bf16 v[36:39], v[92:95], v[24:27], v[36:39]\n"
    "v_mfma_f32_16x16x32_bf16 v[48:51], v[24:27], v[96:99], v[48:51]\n"
    "v_mfma_f32_16x16x32_bf16 v[56:59], v[24:27], v[100:103], v[56:59]\n"
    "v_mfma_f32_16x16x32_bf16 v[64:67], v[104:107], v[24:27], v[64:67]\n"
    "s_waitcnt lgkmcnt(1)\n"
    "v_mfma_f32_16x16x32_bf16 v[68:71], v[108:111], v[24:27], v[68:71]\n"
    "ds_read_b128 v[24:27], v0 offset:14336\n"
    "v_mfma_f32_16x16x32_bf16 v[32:35], v[112:115], v[16:19], v[32:35]\n"
    "v_mfma_f32_16x16x32_bf16 v[36:39], v[116:119], v[16:19], v[36:39]\n"
    "v_mfma_f32_16x16x32_bf16 v[48:51], v[16:19], v[120:123], v[48:51]\n"
    "v_mfma_f32_16x16x32_bf16 v[56:59], v[16:19], v[124:127], v[56:59]\n"
    "v_mfma_f32_16x16x32_bf16 v[64:67], v[128:131], v[16:19], v[64:67]\n"
    "s_waitcnt lgkmcnt(1)\n"
    "v_mfma_f32_16x16x32_bf16 v[68:71], v[132:135], v[16:19], v[68:71]\n"
    "ds_read_b128 v[16:19], v0 offset:15360\n"
    "s_branch .LgP_join0_%=\n"
    ".LgP_dma0_66_%=:\n"
    "v_mfma_f32_16x16x32_bf16 v[32:35], v[88:91], v[24:27], v[32:35]\n"
    "s_add_u32 m0, s59, 20480\n"
    "v_mfma_f32_16x16x32_bf16 v[36:39], v[92:95], v[24:27], v[36:39]\n"
    "buffer_load_dwordx4 v1, s[36:39], s76 offen sc1 lds\n"
    "v_mfma_f32_16x16x32_bf16 v[48:51], v[24:27], v[96:99], v[48:51]\n"
    "s_add_u32 s76, s76, 16384\n"
    "s_add_u32 m0, s59, 24576\n"
    "v_mfma_f32_16x16x32_bf16 v[56:59], v[24:27], v[100:103], v[56:59]\n"
    "buffer_load_dwordx4 v1, s[36:39], s76 offen sc1 lds\n"
    "v_mfma_f32_16x16x32_bf16 v[64:67], v[104:107], v[24:27], v[64:67]\n"
    "s_waitcnt lgkmcnt(1)\n"
    "v_mfma_f32_16x16x32_bf16 v[68:71], v[108:111], v[24:27], v[68:71]\n"
    "ds_read_b128 v[24:27], v0 offset:14336\n"
    "v_mfma_f32_16x16x32_bf16 v[32:35], v[112:115], v[16:19], v[32:35]\n"
    "s_add_u32 s76, s76, 16384\n"
    "s_add_u32 m0, s59, 28672\n"
    "v_mfma_f32_16x16x32_bf16 v[36:39], v[116:119], v[16:19], v[36:39]\n"
    "buffer_load_dwordx4 v1, s[36:39], s76 offen sc1 lds\n"
    "v_mfma_f32_16x16x32_bf16 v[48:51], v[16:19], v[120:123], v[48:51]\n"
    "v_mfma_f32_16x16x32_bf16 v[56:59], v[16:19], v[124:127], v[56:59]\n"
    "v_mfma_f32_16x16x32_bf16 v[64:67], v[128:131], v[16:19], v[64:67]\n"
    "s_waitcnt lgkmcnt(1)\n"
    "s_add_u32 s76, s76, 16384\n"
    "s_add_u32 m0, s59, 32768\n"
    "v_mfma_f32_16x16x32_bf16 v[68:71], v[132:135], v[16:19], v[68:71]\n"
    "ds_read_b128 v[16:19], v0 offset:15360\n"
    "buffer_load_dwordx4 v1, s[36:39], s76 offen sc1 lds\n"
    "v_mfma_f32_16x16x32_bf16 v[32:35], v[136:139], v[20:23], v[32:35]\n"
    "v_mfma_f32_16x16x32_bf16 v[36:39], v[140:143], v[20:23], v[36:39]\n"
    "v_mfma_f32_16x16x32_bf16 v[48:51], v[20:23], v[144:147], v[48:51]\n"
    "s_add_u32 s76, s76, 16384\n"
    "s_add_u32 m0, s59, 36864\n"
    "v_mfma_f32_16x16x32_bf16 v[56:59], v[20:23], v[148:151], v[56:59]\n"
    "buffer_load_dwordx4 v8, s[36:39], s76 offen sc1 lds\n"
    "v_mfma_f32_16x16x32_bf16 v[64:67], v[152:155], v[20:23], v[64:67]\n"
    "s_waitcnt lgkmcnt(1)\n"
    "v_mfma_f32_16x16x32_bf16 v[68:71], v[156:159], v[20:23], v[68:71]\n"
    "ds_read_b128 v[20:23], v0 offset:16384\n"
    "v_mfma_f32_16x16x32_bf16 v[32:35], v[160:163], v[24:27], v[32:35]\n"
    "v_mfma_f32_16x16x32_bf16 v[36:39], v[164:167], v[24:27], v[36:39]\n"
    "v_mfma_f32_16x16x32_bf16 v[48:51], v[24:27], v[168:171], v[48:51]\n"
    "v_mfma_f32_16x16x32_bf16 v[56:59], v[24:27], v[172:175], v[56:59]\n"
    "v_mfma_f32_16x16x32_bf16 v[64:67], v[176:179], v[24:27], v[64:67]\n"
    "s_waitcnt lgkmcnt(1)\n"
    "v_mfma_f32_16x16x32_bf16 v[68:71], v[180:183], v[24:27], v[68:71]\n"
    "ds_read_b128 v[24:27], v0 offset:17408\n"
    "v_mfma_f32_16x16x32_bf16 v[32:35], v[184:187], v[16:19], v[32:35]\n"
    "v_mfma_f32_16x16x32_bf16 v[36:39], v[188:191], v[16:19], v[36:39]\n"
    "v_mfma_f32_16x16x32_bf16 v[48:51], v[16:19], v[192:195], v[48:51]\n"
    "v_mfma_f32_16x16x32_bf16 v[56:59], v[16:19], v[196:199], v[56:59]\n"
    "v_mfma_f32_16x16x32_bf16 v[64:67], v[200:203], v[16:19], v[64:67]\n"
    "s_waitcnt lgkmcnt(1)\n"
    "v_mfma_f32_16x16x32_bf16 v[68:71], v[204:207], v[16:19], v[68:71]\n"
    "v_mfma_f32_16x16x32_bf16 v[32:35], v[208:211], v[20:23], v[32:35]\n"
    "v_mfma_f32_16x16x32_bf16 v[36:39], v[212:215], v[20:23], v[36:39]\n"
    "v_mfma_f32_16x16x32_bf16 v[48:51], v[20:23], v[216:219], v[48:51]\n"
    "v_mfma_f32_16x16x32_bf16 v[56:59], v[20:23], v[220:223], v[56:59]\n"
    "v_mfma_f32_16x16x32_bf16 v[64:67], v[224:227], v[20:23], v[64:67]\n"
    "s_waitcnt lgkmcnt(0)\n"
    "v_mfma_f32_16x16x32_bf16 v[68:71], v[228:231], v[20:23], v[68:71]\n"
    "v_mfma_f32_16x16x32_bf16 v[32:35], v[232:235], v[24:27], v[32:35]\n"
    "v_mfma_f32_16x16x32_bf16 v[36:39], v[236:239], v[24:27], v[36:39]\n"
    "v_mfma_f32_16x16x32_bf16 v[48:51], v[24:27], v[240:243], v[48:51]\n"
    "v_mfma_f32_16x16x32_bf16 v[56:59], v[24:27], v[244:247], v[56:59]\n"
    "v_mfma_f32_16x16x32_bf16 v[64:67], v[248:251], v[24:27], v[64:67]\n"
    "v_mfma_f32_16x16x32_bf16 v[68:71], v[252:255], v[24:27], v[68:71]\n"
    "v_cvt_pk_bf16_f32 v32, v32, v33\n"
    "v_cvt_pk_bf16_f32 v33, v34, v35\n"
    "v_cvt_pk_bf16_f32 v34, v36, v37\n"
    "v_cvt_pk_bf16_f32 v35, v38, v39\n"
    "buffer_store_dwordx4 v[32:35], v1, s[36:39], s79 offen sc1\n"
    "v_cvt_pk_bf16_f32 v28, v48, v49\n"
    "v_cvt_pk_bf16_f32 v29, v50, v51\n"
    "v_cvt_pk_bf16_f32 v30, v56, v57\n"
    "v_cvt_pk_bf16_f32 v31, v58, v59\n"
    "v_cvt_pk_bf16_f32 v64, v64, v65\n"
    "v_cvt_pk_bf16_f32 v65, v66, v67\n"
    "v_cvt_pk_bf16_f32 v66, v68, v69\n"
    "v_cvt_pk_bf16_f32 v67, v70, v71\n"
    "buffer_store_dwordx4 v[64:67], v1, s[36:39], s73 offen sc1\n"
    "v_mov_b32 v12, s68\n"
    "s_mov_b32 s73, 0x80000000\n"
    "s_mov_b32 s79, 0x80000000\n"
    "s_mov_b32 s80, 0x80000000\n"
    "s_mov_b32 s68, s71\n"
    "s_mov_b32 s69, 0\n"
    "s_mov_b32 s70, 0x80000000\n"
    "v_mov_b32 v7, v12\n"
    "s_waitcnt vmcnt(8)\n"
    "s_add_u32 s71, s68, 1\n"
    "v_add_u32 v13, v11, v3\n"
    "v_cmp_gt_i32 vcc, s71, v13\n"
    "s_nop 0\n"
    "s_or_b64 exec, vcc, s[62:63]\n"
    "buffer_atomic_smax v11, v2, s[36:39], 0 offen sc0 sc1\n"
    "s_mov_b64 exec, -1\n"
    "s_waitcnt vmcnt(3)\n"
    "s_branch .LgP_bar1_%=\n"
    ".LgP_dma0_84_%=:\n"
    "v_mfma_f32_16x16x32_bf16 v[32:35], v[160:163], v[24:27], v[32:35]\n"
    "s_add_u32 m0, s59, 20480\n"
    "v_mfma_f32_16x16x32_bf16 v[36:39], v[164:167], v[24:27], v[36:39]\n"
    "buffer_load_dwordx4 v1, s[36:39], s76 offen sc1 lds\n"
    "v_mfma_f32_16x16x32_bf16 v[48:51], v[24:27], v[168:171], v[48:51]\n"
    "s_add_u32 s76, s76, 16384\n"
    "s_add_u32 m0, s59, 24576\n"
    "v_mfma_f32_16x16x32_bf16 v[56:59], v[24:27], v[172:175], v[56:59]\n"
    "buffer_load_dwordx4 v1, s[36:39], s76 offen sc1 lds\n"
    "v_mfma_f32_16x16x32_bf16 v[64:67], v[176:179], v[24:27], v[64:67]\n"
    "s_waitcnt lgkmcnt(1)\n"
    "v_mfma_f32_16x16x32_bf16 v[68:71], v[180:183], v[24:27], v[68:71]\n"
    "ds_read_b128 v[24:27], v0 offset:17408\n"
    "v_mfma_f32_16x16x32_bf16 v[32:35], v[184:187], v[16:19], v[32:35]\n"
    "s_add_u32 s76, s76, 16384\n"
    "s_add_u32 m0, s59, 28672\n"
    "v_mfma_f32_16x16x32_bf16 v[36:39], v[188:191], v[16:19], v[36:39]\n"
    "buffer_load_dwordx4 v1, s[36:39], s76 offen sc1 lds\n"
    "v_mfma_f32_16x16x32_bf16 v[48:51], v[16:19], v[192:195], v[48:51]\n"
    "v_mfma_f32_16x16x32_bf16 v[56:59], v[16:19], v[196:199], v[56:59]\n"
    "v_mfma_f32_16x16x32_bf16 v[64:67], v[200:203], v[16:19], v[64:67]\n"
    "s_waitcnt lgkmcnt(1)\n"
    "s_add_u32 s76, s76, 16384\n"
    "s_add_u32 m0, s59, 32768\n"
    "v_mfma_f32_16x16x32_bf16 v[68:71], v[204:207], v[16:19], v[68:71]\n"
    "buffer_load_dwordx4 v1, s[36:39], s76 offen sc1 lds\n"
    "v_mfma_f32_16x16x32_bf16 v[32:35], v[208:211], v[20:23], v[32:35]\n"
    "v_mfma_f32_16x16x32_bf16 v[36:39], v[212:215], v[20:23], v[36:39]\n"
    "v_mfma_f32_16x16x32_bf16 v[48:51], v[20:23], v[216:219], v[48:51]\n"
    "s_add_u32 s76, s76, 16384\n"
    "s_add_u32 m0, s59, 36864\n"
    "v_mfma_f32_16x16x32_bf16 v[56:59], v[20:23], v[220:223], v[56:59]\n"
    "buffer_load_dwordx4 v8, s[36:39], s76 offen sc1 lds\n"
    "v_mfma_f32_16x16x32_bf16 v[64:67], v[224:227], v[20:23], v[64:67]\n"
    "s_waitcnt lgkmcnt(0)\n"
    "v_mfma_f32_16x16x32_bf16 v[68:71], v[228:231], v[20:23], v[68:71]\n"
    "v_mfma_f32_16x16x32_bf16 v[32:35], v[232:235], v[24:27], v[32:35]\n"
    "v_mfma_f32_16x16x32_bf16 v[36:39], v[236:239], v[24:27], v[36:39]\n"
    "v_mfma_f32_16x16x32_bf16 v[48:51], v[24:27], v[240:243], v[48:51]\n"
    "v_mfma_f32_16x16x32_bf16 v[56:59], v[24:27], v[244:247], v[56:59]\n"
    "v_mfma_f32_16x16x32_bf16 v[64:67], v[248:251], v[24:27], v[64:67]\n"
    "v_mfma_f32_16x16x32_bf16 v[68:71], v[252:255], v[24:27], v[68:71]\n"
    "v_cvt_pk_bf16_f32 v32, v32, v33\n"
    "v_cvt_pk_bf16_f32 v33, v34, v35\n"
    "v_cvt_pk_bf16_f32 v34, v36, v37\n"
    "v_cvt_pk_bf16_f32 v35, v38, v39\n"
    "buffer_store_dwordx4 v[32:35], v1, s[36:39], s79 offen sc1\n"
    "v_cvt_pk_bf16_f32 v28, v48, v49\n"
    "v_cvt_pk_bf16_f32 v29, v50, v51\n"
    "v_cvt_pk_bf16_f32 v30, v56, v57\n"
    "v_cvt_pk_bf16_f32 v31, v58, v59\n"
    "v_cvt_pk_bf16_f32 v64, v64, v65\n"
    "v_cvt_pk_bf16_f32 v65, v66, v67\n"
    "v_cvt_pk_bf16_f32 v66, v68, v69\n"
    "v_cvt_pk_bf16_f32 v67, v70, v71\n"
    "buffer_store_dwordx4 v[64:67], v1, s[36:39], s73 offen sc1\n"
    "v_mov_b32 v12, s68\n"
    "s_mov_b32 s73, 0x80000000\n"
    "s_mov_b32 s79, 0x80000000\n"
    "s_mov_b32 s80, 0x80000000\n"
    "s_mov_b32 s68, s71\n"
    "s_mov_b32 s69, 0\n"
    "s_mov_b32 s70, 0x80000000\n"
    "v_mov_b32 v7, v12\n"
    "s_waitcnt vmcnt(7)\n"
    "s_add_u32 s71, s68, 1\n"
    "v_add_u32 v13, v11, v3\n"
    "v_cmp_gt_i32 vcc, s71, v13\n"
    "s_nop 0\n"
    "s_or_b64 exec, vcc, s[62:63]\n"
    "buffer_atomic_smax v11, v2, s[36:39], 0 offen sc0 sc1\n"
    "s_mov_b64 exec, -1\n"
    "s_waitcnt vmcnt(3)\n"
    "s_branch .LgP_bar1_%=\n"
    ".LgP_dma0_102_%=:\n"
    "v_mfma_f32_16x16x32_bf16 v[32:35], v[232:235], v[24:27], v[32:35]\n"
    "s_add_u32 m0, s59, 20480\n"
    "v_mfma_f32_16x16x32_bf16 v[36:39], v[236:239], v[24:27], v[36:39]\n"
    "buffer_load_dwordx4 v1, s[36:39], s76 offen sc1 lds\n"
    "v_mfma_f32_16x16x32_bf16 v[48:51], v[24:27], v[240:243], v[48:51]\n"
    "s_add_u32 s76, s76, 16384\n"
    "s_add_u32 m0, s59, 24576\n"
    "v_mfma_f32_16x16x32_bf16 v[56:59], v[24:27], v[244:247], v[56:59]\n"
    "buffer_load_dwordx4 v1, s[36:39], s76 offen sc1 lds\n"
    "v_mfma_f32_16x16x32_bf16 v[64:67], v[248:251], v[24:27], v[64:67]\n"
    "v_mfma_f32_16x16x32_bf16 v[68:71], v[252:255], v[24:27], v[68:71]\n"
    "v_cvt_pk_bf16_f32 v32, v32, v33\n"
    "v_cvt_pk_bf16_f32 v33, v34, v35\n"
    "v_cvt_pk_bf16_f32 v34, v36, v37\n"
    "v_cvt_pk_bf16_f32 v35, v38, v39\n"
    "buffer_store_dwordx4 v[32:35], v1, s[36:39], s79 offen sc1\n"
    "v_cvt_pk_bf16_f32 v28, v48, v49\n"
    "v_cvt_pk_bf16_f32 v29, v50, v51\n"
    "v_cvt_pk_bf16_f32 v30, v56, v57\n"
    "v_cvt_pk_bf16_f32 v31, v58, v59\n"
    "v_cvt_pk_bf16_f32 v64, v64, v65\n"
    "v_cvt_pk_bf16_f32 v65, v66, v67\n"
    "v_cvt_pk_bf16_f32 v66, v68, v69\n"
    "v_cvt_pk_bf16_f32 v67, v70, v71\n"
    "buffer_store_dwordx4 v[64:67], v1, s[36:39], s73 offen sc1\n"
    "v_mov_b32 v12, s68\n"
    "s_mov_b32 s73, 0x80000000\n"
    "s_mov_b32 s79, 0x80000000\n"
    "s_mov_b32 s80, 0x80000000\n"
    "s_mov_b32 s68, s71\n"
    "s_add_u32 m0, s59, 28672\n"
    "s_add_u32 s76, s76, 16384\n"
    "buffer_load_dwordx4 v1, s[36:39], s76 offen sc1 lds\n"
    "s_add_u32 m0, s59, 32768\n"
    "s_add_u32 s76, s76, 16384\n"
    "buffer_load_dwordx4 v1, s[36:39], s76 offen sc1 lds\n"
    "s_add_u32 m0, s59, 36864\n"
    "s_add_u32 s76, s76, 16384\n"
    "buffer_load_dwordx4 v8, s[36:39], s76 offen sc1 lds\n"
    "s_waitcnt vmcnt(3)\n"
    "buffer_atomic_smax v12, v14, s[36:39], 0 offen sc1\n"
    "s_mov_b32 s69, 0x80000000\n"
    "s_mov_b32 s70, 0x80000000\n"
    "v_mov_b32 v7, v12\n"
    "s_waitcnt vmcnt(8)\n"
    "s_add_u32 s71, s68, 1\n"
    "v_add_u32 v13, v11, v3\n"
    "v_cmp_gt_i32 vcc, s71, v13\n"
    "s_nop 0\n"
    "s_or_b64 exec, vcc, s[62:63]\n"
    "buffer_atomic_smax v11, v2, s[36:39], 0 offen sc0 sc1\n"
    "s_mov_b64 exec, -1\n"
    "s_waitcnt vmcnt(2)\n"
    "s_branch .LgP_bar1_%=\n"
    ".LgP_idle0_%=:\n"
    "s_or_b64 exec, vcc, s[62:63]\n"
    "buffer_atomic_smax v11, v2, s[36:39], 0 offen sc0 sc1\n"
    "s_mov_b64 exec, -1\n"
    "s_waitcnt vmcnt(1)\n"
    "buffer_atomic_smax v12, v14, s[36:39], 0 offen sc1\n"
    "s_branch .LgP_block0_0_%=\n"
    ".LgP_block0_%=:\n"
    "v_add_u32 v13, v11, v3\n"
    "v_cmp_gt_i32 vcc, s71, v13\n"
    "s_nop 0\n"
    "s_or_b64 exec, vcc, s[62:63]\n"
    "buffer_atomic_smax v11, v2, s[36:39], 0 offen sc0 sc1\n"
    "s_mov_b64 exec, -1\n"
    "s_sleep 3\n"
    "s_waitcnt vmcnt(1)\n"
    ".LgP_block0_0_%=:\n"
    "v_add_u32 v13, v5, v3\n"
    "v_cmp_gt_i32 vcc, s71, v13\n"
    "s_cbranch_vccz .LgP_go0_%=\n"
    "v_add_u32 v13, v5, v3\n"
    "v_cmp_gt_i32 vcc, s71, v13\n"
    "s_nop 0\n"
    "s_or_b64 exec, vcc, s[62:63]\n"
    "buffer_atomic_smax v5, v2, s[36:39], 0 offen sc0 sc1\n"
    "s_mov_b64 exec, -1\n"
    "s_sleep 3\n"
    "s_waitcnt vmcnt(1)\n"
    ".LgP_block0_1_%=:\n"
    "v_add_u32 v13, v11, v3\n"
    "v_cmp_gt_i32 vcc, s71, v13\n"
    "s_cbranch_vccz .LgP_go0_%=\n"
    "s_sub_u32 s72, s72, 1\n"
    "s_cbranch_scc0 .LgP_block0_%=\n"
    "s_mov_b32 s72, 0\n"
    ".LgP_go0_%=:\n"
    "s_lshr_b32 s76, s71, 1\n"
    "s_and_b32 s76, s76, s54\n"
    "s_mul_i32 s76, s76, s53\n"
    "s_add_u32 s76, s76, s52\n"
    "s_add_u32 s76, s76, 1024\n"
    "s_add_u32 m0, s59, 20480\n"
    "s_nop 0\n"
    "buffer_load_dwordx4 v1, s[36:39], s76 offen sc1 lds\n"
    "s_add_u32 m0, s59, 24576\n"
    "s_add_u32 s76, s76, 16384\n"
    "buffer_load_dwordx4 v1, s[36:39], s76 offen sc1 lds\n"
    "s_add_u32 m0, s59, 28672\n"
    "s_add_u32 s76, s76, 16384\n"
    "buffer_load_dwordx4 v1, s[36:39], s76 offen sc1 lds\n"
    "s_add_u32 m0, s59, 32768\n"
    "s_add_u32 s76, s76, 16384\n"
    "buffer_load_dwordx4 v1, s[36:39], s76 offen sc1 lds\n"
    "s_add_u32 m0, s59, 36864\n"
    "s_add_u32 s76, s76, 16384\n"
    "buffer_load_dwordx4 v8, s[36:39], s76 offen sc1 lds\n"
    "s_mov_b32 s69, 0x80000000\n"
    "s_mov_b32 s70, 0x80000000\n"
    "v_mov_b32 v7, v12\n"
    "s_waitcnt vmcnt(5)\n"
    "s_add_u32 s71, s68, 1\n"
    "v_add_u32 v13, v11, v3\n"
    "v_cmp_gt_i32 vcc, s71, v13\n"
    "s_nop 0\n"
    "s_or_b64 exec, vcc, s[62:63]\n"
    "buffer_atomic_smax v11, v2, s[36:39], 0 offen sc0 sc1\n"
    "s_mov_b64 exec, -1\n"
    "s_waitcnt vmcnt(1)\n"
    "s_branch .LgP_bar1_%=\n"
    ".LgP_bar0_%=:\n"
    "s_barrier\n"
    "ds_read_b128 v[16:19], v0 offset:0\n"
    "ds_read_b128 v[20:23], v0 offset:1024\n"
    "ds_read_b128 v[24:27], v0 offset:2048\n"
    "v_add_u32 v13, v5, v3\n"
    "v_cmp_gt_i32 vcc, s71, v13\n"
    "s_nop 0\n"
    "s_or_b64 exec, vcc, s[62:63]\n"
    "buffer_atomic_smax v5, v2, s[36:39], 0 offen sc0 sc1\n"
    "s_mov_b64 exec, -1\n"
    "s_lshr_b32 s76, s71, 1\n"
    "s_and_b32 s76, s76, s54\n"
    "s_mul_i32 s76, s76, s53\n"
    "s_add_u32 s76, s76, s52\n"
    "s_add_u32 s76, s76, 1024\n"
    "s_waitcnt lgkmcnt(2)\n"
    "s_branch .LgP_head0_%=\n"
    ".LgP_poll1_%=:\n"
    "v_mfma_f32_16x16x32_bf16 v[40:43], a[48:51], v[24:27], v[40:43]\n"
    "s_or_b64 exec, vcc, s[62:63]\n"
    "buffer_atomic_smax v11, v2, s[36:39], 0 offen sc0 sc1\n"
    "s_mov_b64 exec, -1\n"
    "v_mfma_f32_16x16x32_bf16 v[44:47], a[52:55], v[24:27], v[44:47]\n"
    "buffer_store_dwordx4 v[64:67], v1, s[36:39], s73 offen sc1\n"
    "v_mfma_f32_16x16x32_bf16 v[52:55], v[24:27], a[56:59], v[52:55]\n"
    "s_lshr_b32 s73, s68, 1\n"
    "v_mfma_f32_16x16x32_bf16 v[60:63], v[24:27], a[60:63], v[60:63]\n"
    "s_and_b32 s73, s73, s57\n"
    "v_mfma_f32_16x16x32_bf16 v[72:75], a[64:67], v[24:27], v[72:75]\n"
    "s_waitcnt lgkmcnt(1)\n"
    "s_mul_i32 s73, s73, s56\n"
    "v_mfma_f32_16x16x32_bf16 v[76:79], a[68:71], v[24:27], v[76:79]\n"
    "ds_read_b128 v[24:27], v9 offset:5120\n"
    "s_add_u32 s73, s73, s55\n"
    "v_mfma_f32_16x16x32_bf16 v[40:43], a[72:75], v[16:19], v[40:43]\n"
    "s_add_u32 s79, s73, 16384\n"
    "v_mfma_f32_16x16x32_bf16 v[44:47], a[76:79], v[16:19], v[44:47]\n"
    "s_add_u32 s80, s73, 32768\n"
    "v_mfma_f32_16x16x32_bf16 v[52:55], v[16:19], a[80:83], v[52:55]\n"
    "v_mfma_f32_16x16x32_bf16 v[60:63], v[16:19], a[84:87], v[60:63]\n"
    "v_mfma_f32_16x16x32_bf16 v[72:75], a[88:91], v[16:19], v[72:75]\n"
    "s_waitcnt lgkmcnt(1)\n"
    "v_mfma_f32_16x16x32_bf16 v[76:79], a[92:95], v[16:19], v[76:79]\n"
    "ds_read_b128 v[16:19], v9 offset:6144\n"
    "v_mfma_f32_16x16x32_bf16 v[40:43], a[96:99], v[20:23], v[40:43]\n"
    "v_mfma_f32_16x16x32_bf16 v[44:47], a[100:103], v[20:23], v[44:47]\n"
    "s_waitcnt vmcnt(4)\n"
    "v_add_u32 v13, v5, v3\n"
    "v_mfma_f32_16x16x32_bf16 v[52:55], v[20:23], a[104:107], v[52:55]\n"
    "v_cmp_gt_i32 vcc, s71, v13\n"
    "v_mfma_f32_16x16x32_bf16 v[60:63], v[20:23], a[108:111], v[60:63]\n"
    "v_mfma_f32_16x16x32_bf16 v[72:75], a[112:115], v[20:23], v[72:75]\n"
    "s_waitcnt lgkmcnt(1)\n"
    "v_mfma_f32_16x16x32_bf16 v[76:79], a[116:119], v[20:23], v[76:79]\n"
    "ds_read_b128 v[20:23], v9 offset:7168\n"
    "s_cbranch_vccz .LgP_dma1_30_%=\n"
    "v_mfma_f32_16x16x32_bf16 v[40:43], a[120:123], v[24:27], v[40:43]\n"
    "s_or_b64 exec, vcc, s[62:63]\n"
    "buffer_atomic_smax v5, v2, s[36:39], 0 offen sc0 sc1\n"
    "s_mov_b64 exec, -1\n"
    "v_mfma_f32_16x16x32_bf16 v[44:47], a[124:127], v[24:27], v[44:47]\n"
    "v_mfma_f32_16x16x32_bf16 v[52:55], v[24:27], a[128:131], v[52:55]\n"
    "v_mfma_f32_16x16x32_bf16 v[60:63], v[24:27], a[132:135], v[60:63]\n"
    "v_mfma_f32_16x16x32_bf16 v[72:75], a[136:139], v[24:27], v[72:75]\n"
    "s_waitcnt lgkmcnt(1)\n"
    "v_mfma_f32_16x16x32_bf16 v[76:79], a[140:143], v[24:27], v[76:79]\n"
    "ds_read_b128 v[24:27], v9 offset:8192\n"
    "v_mfma_f32_16x16x32_bf16 v[40:43], a[144:147], v[16:19], v[40:43]\n"
    "v_mfma_f32_16x16x32_bf16 v[44:47], a[148:151], v[16:19], v[44:47]\n"
    "v_mfma_f32_16x16x32_bf16 v[52:55], v[16:19], a[152:155], v[52:55]\n"
    "v_mfma_f32_16x16x32_bf16 v[60:63], v[16:19], a[156:159], v[60:63]\n"
    "v_mfma_f32_16x16x32_bf16 v[72:75], a[160:163], v[16:19], v[72:75]\n"
    "s_waitcnt lgkmcnt(1)\n"
    "v_mfma_f32_16x16x32_bf16 v[76:79], a[164:167], v[16:19], v[76:79]\n"
    "ds_read_b128 v[16:19], v9 offset:9216\n"
    "v_mfma_f32_16x16x32_bf16 v[40:43], a[168:171], v[20:23], v[40:43]\n"
    "v_mfma_f32_16x16x32_bf16 v[44:47], a[172:175], v[20:23], v[44:47]\n"
    "s_waitcnt vmcnt(2)\n"
    "v_add_u32 v13, v11, v3\n"
    "v_mfma_f32_16x16x32_bf16 v[52:55], v[20:23], a[176:179], v[52:55]\n"
    "v_cmp_gt_i32 vcc, s71, v13\n"
    "v_mfma_f32_16x16x32_bf16 v[60:63], v[20:23], a[180:183], v[60:63]\n"
    "v_mfma_f32_16x16x32_bf16 v[72:75], a[184:187], v[20:23], v[72:75]\n"
    "s_waitcnt lgkmcnt(1)\n"
    "v_mfma_f32_16x16x32_bf16 v[76:79], a[188:191], v[20:23], v[76:79]\n"
    "ds_read_b128 v[20:23], v9 offset:10240\n"
    "s_cbranch_vccz .LgP_dma1_48_%=\n"
    "v_mfma_f32_16x16x32_bf16 v[40:43], a[192:195], v[24:27], v[40:43]\n"
    "s_or_b64 exec, vcc, s[62:63]\n"
    "buffer_atomic_smax v11, v2, s[36:39], 0 offen sc0 sc1\n"
    "s_mov_b64 exec, -1\n"
    "v_mfma_f32_16x16x32_bf16 v[44:47], a[196:199], v[24:27], v[44:47]\n"
    "v_mfma_f32_16x16x32_bf16 v[52:55], v[24:27], a[200:203], v[52:55]\n"
    "v_mfma_f32_16x16x32_bf16 v[60:63], v[24:27], a[204:207], v[60:63]\n"
    "v_mfma_f32_16x16x32_bf16 v[72:75], a[208:211], v[24:27], v[72:75]\n"
    "s_waitcnt lgkmcnt(1)\n"
    "s_waitcnt vmcnt(2)\n"
    "buffer_atomic_smax v12, v14, s[36:39], s70 offen sc1\n"
    "v_mfma_f32_16x16x32_bf16 v[76:79], a[212:215], v[24:27], v[76:79]\n"
    "ds_read_b128 v[24:27], v9 offset:11264\n"
    "v_mfma_f32_16x16x32_bf16 v[40:43], a[216:219], v[16:19], v[40:43]\n"
    "v_mfma_f32_16x16x32_bf16 v[44:47], a[220:223], v[16:19], v[44:47]\n"
    "v_mfma_f32_16x16x32_bf16 v[52:55], v[16:19], a[224:227], v[52:55]\n"
    "v_mfma_f32_16x16x32_bf16 v[60:63], v[16:19], a[228:231], v[60:63]\n"
    "v_mfma_f32_16x16x32_bf16 v[72:75], a[232:235], v[16:19], v[72:75]\n"
    "s_waitcnt lgkmcnt(1)\n"
    "v_mfma_f32_16x16x32_bf16 v[76:79], a[236:239], v[16:19], v[76:79]\n"
    "ds_read_b128 v[16:19], v9 offset:12288\n"
    "v_mfma_f32_16x16x32_bf16 v[40:43], a[240:243], v[20:23], v[40:43]\n"
    "v_mfma_f32_16x16x32_bf16 v[44:47], a[244:247], v[20:23], v[44:47]\n"
    "s_waitcnt vmcnt(2)\n"
    "v_add_u32 v13, v5, v3\n"
    "v_mfma_f32_16x16x32_bf16 v[52:55], v[20:23], a[248:251], v[52:55]\n"
    "v_cmp_gt_i32 vcc, s71, v13\n"
    "v_mfma_f32_16x16x32_bf16 v[60:63], v[20:23], a[252:255], v[60:63]\n"
    "v_mfma_f32_16x16x32_bf16 v[72:75], v[80:83], v[20:23], v[72:75]\n"
    "s_waitcnt lgkmcnt(1)\n"
    "v_mfma_f32_16x16x32_bf16 v[76:79], v[84:87], v[20:23], v[76:79]\n"
    "ds_read_b128 v[20:23], v9 offset:13312\n"
    "s_cbranch_vccz .LgP_dma1_66_%=\n"
    "v_mfma_f32_16x16x32_bf16 v[40:43], v[88:91], v[24:27], v[40:43]\n"
    "s_or_b64 exec, vcc, s[62:63]\n"
    "buffer_atomic_smax v5, v2, s[36:39], 0 offen sc0 sc1\n"
    "s_mov_b64 exec, -1\n"
    "v_mfma_f32_16x16x32_bf16 v[44:47], v[92:95], v[24:27], v[44:47]\n"
    "v_mfma_f32_16x16x32_bf16 v[52:55], v[24:27], v[96:99], v[52:55]\n"
    "v_mfma_f32_16x16x32_bf16 v[60:63], v[24:27], v[100:103], v[60:63]\n"
    "v_mfma_f32_16x16x32_bf16 v[72:75], v[104:107], v[24:27], v[72:75]\n"
    "s_waitcnt lgkmcnt(1)\n"
    "v_mfma_f32_16x16x32_bf16 v[76:79], v[108:111], v[24:27], v[76:79]\n"
    "ds_read_b128 v[24:27], v9 offset:14336\n"
    "v_mfma_f32_16x16x32_bf16 v[40:43], v[112:115], v[16:19], v[40:43]\n"
    "v_mfma_f32_16x16x32_bf16 v[44:47], v[116:119], v[16:19], v[44:47]\n"
    "v_mfma_f32_16x16x32_bf16 v[52:55], v[16:19], v[120:123], v[52:55]\n"
    "v_mfma_f32_16x16x32_bf16 v[60:63], v[16:19], v[124:127], v[60:63]\n"
    "v_mfma_f32_16x16x32_bf16 v[72:75], v[128:131], v[16:19], v[72:75]\n"
    "s_waitcnt lgkmcnt(1)\n"
    "v_mfma_f32_16x16x32_bf16 v[76:79], v[132:135], v[16:19], v[76:79]\n"
    "ds_read_b128 v[16:19], v9 offset:15360\n"
    "v_mfma_f32_16x16x32_bf16 v[40:43], v[136:139], v[20:23], v[40:43]\n"
    "v_mfma_f32_16x16x32_bf16 v[44:47], v[140:143], v[20:23], v[44:47]\n"
    "s_waitcnt vmcnt(2)\n"
    "v_add_u32 v13, v11, v3\n"
    "v_mfma_f32_16x16x32_bf16 v[52:55], v[20:23], v[144:147], v[52:55]\n"
    "v_cmp_gt_i32 vcc, s71, v13\n"
    "v_mfma_f32_16x16x32_bf16 v[60:63], v[20:23], v[148:151], v[60:63]\n"
    "v_mfma_f32_16x16x32_bf16 v[72:75], v[152:155], v[20:23], v[72:75]\n"
    "s_waitcnt lgkmcnt(1)\n"
    "v_mfma_f32_16x16x32_bf16 v[76:79], v[156:159], v[20:23], v[76:79]\n"
    "ds_read_b128 v[20:23], v9 offset:16384\n"
    "s_cbranch_vccz .LgP_dma1_84_%=\n"
    "v_mfma_f32_16x16x32_bf16 v[40:43], v[160:163], v[24:27], v[40:43]\n"
    "s_or_b64 exec, vcc, s[62:63]\n"
    "buffer_atomic_smax v11, v2, s[36:39], 0 offen sc0 sc1\n"
    "s_mov_b64 exec, -1\n"
    "v_mfma_f32_16x16x32_bf16 v[44:47], v[164:167], v[24:27], v[44:47]\n"
    "v_mfma_f32_16x16x32_bf16 v[52:55], v[24:27], v[168:171], v[52:55]\n"
    "v_mfma_f32_16x16x32_bf16 v[60:63], v[24:27], v[172:175], v[60:63]\n"
    "v_mfma_f32_16x16x32_bf16 v[72:75], v[176:179], v[24:27], v[72:75]\n"
    "s_waitcnt lgkmcnt(1)\n"
    "v_mfma_f32_16x16x32_bf16 v[76:79], v[180:183], v[24:27], v[76:79]\n"
    "ds_read_b128 v[24:27], v9 offset:17408\n"
    "v_mfma_f32_16x16x32_bf16 v[40:43], v[184:187], v[16:19], v[40:43]\n"
    "v_mfma_f32_16x16x32_bf16 v[44:47], v[188:191], v[16:19], v[44:47]\n"
    "v_mfma_f32_16x16x32_bf16 v[52:55], v[16:19], v[192:195], v[52:55]\n"
    "v_mfma_f32_16x16x32_bf16 v[60:63], v[16:19], v[196:199], v[60:63]\n"
    "v_mfma_f32_16x16x32_bf16 v[72:75], v[200:203], v[16:19], v[72:75]\n"
    "s_waitcnt lgkmcnt(1)\n"
    "v_mfma_f32_16x16x32_bf16 v[76:79], v[204:207], v[16:19], v[76:79]\n"
    "v_mfma_f32_16x16x32_bf16 v[40:43], v[208:211], v[20:23], v[40:43]\n"
    "v_mfma_f32_16x16x32_bf16 v[44:47], v[212:215], v[20:23], v[44:47]\n"
    "s_waitcnt vmcnt(1)\n"
    "v_add_u32 v13, v5, v3\n"
    "v_mfma_f32_16x16x32_bf16 v[52:55], v[20:23], v[216:219], v[52:55]\n"
    "v_cmp_gt_i32 vcc, s71, v13\n"
    "v_mfma_f32_16x16x32_bf16 v[60:63], v[20:23], v[220:223], v[60:63]\n"
    "v_mfma_f32_16x16x32_bf16 v[72:75], v[224:227], v[20:23], v[72:75]\n"
    "s_waitcnt lgkmcnt(0)\n"
    "v_mfma_f32_16x16x32_bf16 v[76:79], v[228:231], v[20:23], v[76:79]\n"
    "s_cbranch_vccz .LgP_dma1_102_%=\n"
    "v_mfma_f32_16x16x32_bf16 v[40:43], v[232:235], v[24:27], v[40:43]\n"
    "s_or_b64 exec, vcc, s[62:63]\n"
    "buffer_atomic_smax v5, v2, s[36:39], 0 offen sc0 sc1\n"
    "s_mov_b64 exec, -1\n"
    "v_mfma_f32_16x16x32_bf16 v[44:47], v[236:239], v[24:27], v[44:47]\n"
    "v_mfma_f32_16x16x32_bf16 v[52:55], v[24:27], v[240:243], v[52:55]\n"
    "v_mfma_f32_16x16x32_bf16 v[60:63], v[24:27], v[244:247], v[60:63]\n"
    "v_mfma_f32_16x16x32_bf16 v[72:75], v[248:251], v[24:27], v[72:75]\n"
    "v_mfma_f32_16x16x32_bf16 v[76:79], v[252:255], v[24:27], v[76:79]\n"
    "v_cvt_pk_bf16_f32 v40, v40, v41\n"
    "v_cvt_pk_bf16_f32 v41, v42, v43\n"
    "v_cvt_pk_bf16_f32 v42, v44, v45\n"
    "v_cvt_pk_bf16_f32 v43, v46, v47\n"
    "buffer_store_dwordx4 v[40:43], v1, s[36:39], s79 offen offset:1024 sc1\n"
    "v_cvt_pk_bf16_f32 v55, v54, v55\n"
    "v_cvt_pk_bf16_f32 v54, v52, v53\n"
    "v_mov_b64 v[52:53], v[28:29]\n"
    "buffer_store_dwordx4 v[52:55], v1, s[36:39], s80 offen sc1\n"
    "v_cvt_pk_bf16_f32 v63, v62, v63\n"
    "v_cvt_pk_bf16_f32 v62, v60, v61\n"
    "v_mov_b64 v[60:61], v[30:31]\n"
    "buffer_store_dwordx4 v[60:63], v1, s[36:39], s80 offen offset:1024 sc1\n"
    "v_cvt_pk_bf16_f32 v72, v72, v73\n"
    "v_cvt_pk_bf16_f32 v73, v74, v75\n"
    "v_cvt_pk_bf16_f32 v74, v76, v77\n"
    "v_cvt_pk_bf16_f32 v75, v78, v79\n"
    "buffer_store_dwordx4 v[72:75], v1, s[36:39], s73 offen offset:1024 sc1\n"
    "v_mov_b32 v12, s68\n"
    "s_mov_b32 s73, 0x80000000\n"
    "s_mov_b32 s79, 0x80000000\n"
    "s_mov_b32 s80, 0x80000000\n"
    "s_mov_b32 s68, s71\n"
    "s_cmp_eq_u32 s68, 128\n"
    "s_cbranch_scc1 .LgP_done_%=\n"
    "s_waitcnt vmcnt(5)\n"
    "v_add_u32 v13, v11, v3\n"
    "v_cmp_gt_i32 vcc, s71, v13\n"
    "s_cbranch_vccnz .LgP_idle1_%=\n"
    "s_add_u32 m0, s59, 0\n"
    "s_nop 0\n"
    "buffer_load_dwordx4 v1, s[36:39], s76 offen sc1 lds\n"
    "s_add_u32 m0, s59, 4096\n"
    "s_add_u32 s76, s76, 16384\n"
    "buffer_load_dwordx4 v1, s[36:39], s76 offen sc1 lds\n"
    "s_add_u32 m0, s59, 8192\n"
    "s_add_u32 s76, s76, 16384\n"
    "buffer_load_dwordx4 v1, s[36:39], s76 offen sc1 lds\n"
    "s_add_u32 m0, s59, 12288\n"
    "s_add_u32 s76, s76, 16384\n"
    "buffer_load_dwordx4 v1, s[36:39], s76 offen sc1 lds\n"
    "s_add_u32 m0, s59, 16384\n"
    "s_add_u32 s76, s76, 16384\n"
    "buffer_load_dwordx4 v8, s[36:39], s76 offen sc1 lds\n"
    "s_waitcnt vmcnt(5)\n"
    "buffer_atomic_smax v12, v10, s[36:39], 0 offen sc1\n"
    "s_mov_b32 s69, 0x80000000\n"
    "s_mov_b32 s70, 0x80000000\n"
    "v_mov_b32 v7, v12\n"
    "s_waitcnt vmcnt(10)\n"
    "s_add_u32 s71, s68, 1\n"
    "v_add_u32 v13, v11, v3\n"
    "v_cmp_gt_i32 vcc, s71, v13\n"
    "s_nop 0\n"
    "s_or_b64 exec, vcc, s[62:63]\n"
    "buffer_atomic_smax v11, v2, s[36:39], 0 offen sc0 sc1\n"
    "s_mov_b64 exec, -1\n"
    "s_waitcnt vmcnt(2)\n"
    "s_branch .LgP_bar0_%=\n"
    ".LgP_dma1_30_%=:\n"
    "v_mfma_f32_16x16x32_bf16 v[40:43], a[120:123], v[24:27], v[40:43]\n"
    "s_add_u32 m0, s59, 0\n"
    "v_mfma_f32_16x16x32_bf16 v[44:47], a[124:127], v[24:27], v[44:47]\n"
    "buffer_load_dwordx4 v1, s[36:39], s76 offen sc1 lds\n"
    "v_mfma_f32_16x16x32_bf16 v[52:55], v[24:27], a[128:131], v[52:55]\n"
    "s_add_u32 s76, s76, 16384\n"
    "s_add_u32 m0, s59, 4096\n"
    "v_mfma_f32_16x16x32_bf16 v[60:63], v[24:27], a[132:135], v[60:63]\n"
    "buffer_load_dwordx4 v1, s[36:39], s76 offen sc1 lds\n"
    "v_mfma_f32_16x16x32_bf16 v[72:75], a[136:139], v[24:27], v[72:75]\n"
    "s_waitcnt lgkmcnt(1)\n"
    "v_mfma_f32_16x16x32_bf16 v[76:79], a[140:143], v[24:27], v[76:79]\n"
    "ds_read_b128 v[24:27], v9 offset:8192\n"
    "v_mfma_f32_16x16x32_bf16 v[40:43], a[144:147], v[16:19], v[40:43]\n"
    "s_add_u32 s76, s76, 16384\n"
    "s_add_u32 m0, s59, 8192\n"
    "v_mfma_f32_16x16x32_bf16 v[44:47], a[148:151], v[16:19], v[44:47]\n"
    "buffer_load_dwordx4 v1, s[36:39], s76 offen sc1 lds\n"
    "v_mfma_f32_16x16x32_bf16 v[52:55], v[16:19], a[152:155], v[52:55]\n"
    "v_mfma_f32_16x16x32_bf16 v[60:63], v[16:19], a[156:159], v[60:63]\n"
    "v_mfma_f32_16x16x32_bf16 v[72:75], a[160:163], v[16:19], v[72:75]\n"
    "s_waitcnt lgkmcnt(1)\n"
    "s_add_u32 s76, s76, 16384\n"
    "s_add_u32 m0, s59, 12288\n"
    "v_mfma_f32_16x16x32_bf16 v[76:79], a[164:167], v[16:19], v[76:79]\n"
    "ds_read_b128 v[16:19], v9 offset:9216\n"
    "buffer_load_dwordx4 v1, s[36:39], s76 offen sc1 lds\n"
    "v_mfma_f32_16x16x32_bf16 v[40:43], a[168:171], v[20:23], v[40:43]\n"
    "v_mfma_f32_16x16x32_bf16 v[44:47], a[172:175], v[20:23], v[44:47]\n"
    "v_mfma_f32_16x16x32_bf16 v[52:55], v[20:23], a[176:179], v[52:55]\n"
    "s_add_u32 s76, s76, 16384\n"
    "s_add_u32 m0, s59, 16384\n"
    "v_mfma_f32_16x16x32_bf16 v[60:63], v[20:23], a[180:183], v[60:63]\n"
    "buffer_load_dwordx4 v8, s[36:39], s76 offen sc1 lds\n"
    "v_mfma_f32_16x16x32_bf16 v[72:75], a[184:187], v[20:23], v[72:75]\n"
    "s_waitcnt lgkmcnt(1)\n"
    "v_mfma_f32_16x16x32_bf16 v[76:79], a[188:191], v[20:23], v[76:79]\n"
    "ds_read_b128 v[20:23], v9 offset:10240\n"
    "v_mfma_f32_16x16x32_bf16 v[40:43], a[192:195], v[24:27], v[40:43]\n"
    "v_mfma_f32_16x16x32_bf16 v[44:47], a[196:199], v[24:27], v[44:47]\n"
    "v_mfma_f32_16x16x32_bf16 v[52:55], v[24:27], a[200:203], v[52:55]\n"
    "v_mfma_f32_16x16x32_bf16 v[60:63], v[24:27], a[204:207], v[60:63]\n"
    "v_mfma_f32_16x16x32_bf16 v[72:75], a[208:211], v[24:27], v[72:75]\n"
    "s_waitcnt lgkmcnt(1)\n"
    "s_waitcnt vmcnt(5)\n"
    "buffer_atomic_smax v12, v14, s[36:39], s70 offen sc1\n"
    "v_mfma_f32_16x16x32_bf16 v[76:79], a[212:215], v[24:27], v[76:79]\n"
    "ds_read_b128 v[24:27], v9 offset:11264\n"
    "v_mfma_f32_16x16x32_bf16 v[40:43], a[216:219], v[16:19], v[40:43]\n"
    "v_mfma_f32_16x16x32_bf16 v[44:47], a[220:223], v[16:19], v[44:47]\n"
    "v_mfma_f32_16x16x32_bf16 v[52:55], v[16:19], a[224:227], v[52:55]\n"
    "v_mfma_f32_16x16x32_bf16 v[60:63], v[16:19], a[228:231], v[60:63]\n"
    "v_mfma_f32_16x16x32_bf16 v[72:75], a[232:235], v[16:19], v[72:75]\n"
    "s_waitcnt lgkmcnt(1)\n"
    "v_mfma_f32_16x16x32_bf16 v[76:79], a[236:239], v[16:19], v[76:79]\n"
    "ds_read_b128 v[16:19], v9 offset:12288\n"
    "v_mfma_f32_16x16x32_bf16 v[40:43], a[240:243], v[20:23], v[40:43]\n"
    "v_mfma_f32_16x16x32_bf16 v[44:47], a[244:247], v[20:23], v[44:47]\n"
    "v_mfma_f32_16x16x32_bf16 v[52:55], v[20:23], a[248:251], v[52:55]\n"
    "v_mfma_f32_16x16x32_bf16 v[60:63], v[20:23], a[252:255], v[60:63]\n"
    "v_mfma_f32_16x16x32_bf16 v[72:75], v[80:83], v[20:23], v[72:75]\n"
    "s_waitcnt lgkmcnt(1)\n"
    "v_mfma_f32_16x16x32_bf16 v[76:79], v[84:87], v[20:23], v[76:79]\n"
    "ds_read_b128 v[20:23], v9 offset:13312\n"
    "v_mfma_f32_16x16x32_bf16 v[40:43], v[88:91], v[24:27], v[40:43]\n"
    "v_mfma_f32_16x16x32_bf16 v[44:47], v[92:95], v[24:27], v[44:47]\n"
    "v_mfma_f32_16x16x32_bf16 v[52:55], v[24:27], v[96:99], v[52:55]\n"
    "v_mfma_f32_16x16x32_bf16 v[60:63], v[24:27], v[100:103], v[60:63]\n"
    "v_mfma_f32_16x16x32_bf16 v[72:75], v[104:107], v[24:27], v[72:75]\n"
    "s_waitcnt lgkmcnt(1)\n"
    "v_mfma_f32_16x16x32_bf16 v[76:79], v[108:111], v[24:27], v[76:79]\n"
    "ds_read_b128 v[24:27], v9 offset:14336\n"
    "v_mfma_f32_16x16x32_bf16 v[40:43], v[112:115], v[16:19], v[40:43]\n"
    "v_mfma_f32_16x16x32_bf16 v[44:47], v[116:119], v[16:19], v[44:47]\n"
    "v_mfma_f32_16x16x32_bf16 v[52:55], v[16:19], v[120:123], v[52:55]\n"
    "v_mfma_f32_16x16x32_bf16 v[60:63], v[16:19], v[124:127], v[60:63]\n"
    "v_mfma_f32_16x16x32_bf16 v[72:75], v[128:131], v[16:19], v[72:75]\n"
    "s_waitcnt lgkmcnt(1)\n"
    "v_mfma_f32_16x16x32_bf16 v[76:79], v[132:135], v[16:19], v[76:79]\n"
    "ds_read_b128 v[16:19], v9 offset:15360\n"
    "s_branch .LgP_join1_%=\n"
    ".LgP_dma1_48_%=:\n"
    "v_mfma_f32_16x16x32_bf16 v[40:43], a[192:195], v[24:27], v[40:43]\n"
    "s_add_u32 m0, s59, 0\n"
    "v_mfma_f32_16x16x32_bf16 v[44:47], a[196:199], v[24:27], v[44:47]\n"
    "buffer_load_dwordx4 v1, s[36:39], s76 offen sc1 lds\n"
    "v_mfma_f32_16x16x32_bf16 v[52:55], v[24:27], a[200:203], v[52:55]\n"
    "s_add_u32 s76, s76, 16384\n"
    "s_add_u32 m0, s59, 4096\n"
    "v_mfma_f32_16x16x32_bf16 v[60:63], v[24:27], a[204:207], v[60:63]\n"
    "buffer_load_dwordx4 v1, s[36:39], s76 offen sc1 lds\n"
    "v_mfma_f32_16x16x32_bf16 v[72:75], a[208:211], v[24:27], v[72:75]\n"
    "s_waitcnt lgkmcnt(1)\n"
    "s_waitcnt vmcnt(3)\n"
    "buffer_atomic_smax v12, v14, s[36:39], s70 offen sc1\n"
    "v_mfma_f32_16x16x32_bf16 v[76:79], a[212:215], v[24:27], v[76:79]\n"
    "ds_read_b128 v[24:27], v9 offset:11264\n"
    "v_mfma_f32_16x16x32_bf16 v[40:43], a[216:219], v[16:19], v[40:43]\n"
    "s_add_u32 s76, s76, 16384\n"
    "s_add_u32 m0, s59, 8192\n"
    "v_mfma_f32_16x16x32_bf16 v[44:47], a[220:223], v[16:19], v[44:47]\n"
    "buffer_load_dwordx4 v1, s[36:39], s76 offen sc1 lds\n"
    "v_mfma_f32_16x16x32_bf16 v[52:55], v[16:19], a[224:227], v[52:55]\n"
    "v_mfma_f32_16x16x32_bf16 v[60:63], v[16:19], a[228:231], v[60:63]\n"
    "v_mfma_f32_16x16x32_bf16 v[72:75], a[232:235], v[16:19], v[72:75]\n"
    "s_waitcnt lgkmcnt(1)\n"
    "s_add_u32 s76, s76, 16384\n"
    "s_add_u32 m0, s59, 12288\n"
    "v_mfma_f32_16x16x32_bf16 v[76:79], a[236:239], v[16:19], v[76:79]\n"
    "ds_read_b128 v[16:19], v9 offset:12288\n"
    "buffer_load_dwordx4 v1, s[36:39], s76 offen sc1 lds\n"
    "v_mfma_f32_16x16x32_bf16 v[40:43], a[240:243], v[20:23], v[40:43]\n"
    "v_mfma_f32_16x16x32_bf16 v[44:47], a[244:247], v[20:23], v[44:47]\n"
    "v_mfma_f32_16x16x32_bf16 v[52:55], v[20:23], a[248:251], v[52:55]\n"
    "s_add_u32 s76, s76, 16384\n"
    "s_add_u32 m0, s59, 16384\n"
    "v_mfma_f32_16x16x32_bf16 v[60:63], v[20:23], a[252:255], v[60:63]\n"
    "buffer_load_dwordx4 v8, s[36:39], s76 offen sc1 lds\n"
    "v_mfma_f32_16x16x32_bf16 v[72:75], v[80:83], v[20:23], v[72:75]\n"
    "s_waitcnt lgkmcnt(1)\n"
    "v_mfma_f32_16x16x32_bf16 v[76:79], v[84:87], v[20:23], v[76:79]\n"
    "ds_read_b128 v[20:23], v9 offset:13312\n"
    "v_mfma_f32_16x16x32_bf16 v[40:43], v[88:91], v[24:27], v[40:43]\n"
    "v_mfma_f32_16x16x32_bf16 v[44:47], v[92:95], v[24:27], v[44:47]\n"
    "v_mfma_f32_16x16x32_bf16 v[52:55], v[24:27], v[96:99], v[52:55]\n"
    "v_mfma_f32_16x16x32_bf16 v[60:63], v[24:27], v[100:103], v[60:63]\n"
    "v_mfma_f32_16x16x32_bf16 v[72:75], v[104:107], v[24:27], v[72:75]\n"
    "s_waitcnt lgkmcnt(1)\n"
    "v_mfma_f32_16x16x32_bf16 v[76:79], v[108:111], v[24:27], v[76:79]\n"
    "ds_read_b128 v[24:27], v9 offset:14336\n"
    "v_mfma_f32_16x16x32_bf16 v[40:43], v[112:115], v[16:19], v[40:43]\n"
    "v_mfma_f32_16x16x32_bf16 v[44:47], v[116:119], v[16:19], v[44:47]\n"
    "v_mfma_f32_16x16x32_bf16 v[52:55], v[16:19], v[120:123], v[52:55]\n"
    "v_mfma_f32_16x16x32_bf16 v[60:63], v[16:19], v[124:127], v[60:63]\n"
    "v_mfma_f32_16x16x32_bf16 v[72:75], v[128:131], v[16:19], v[72:75]\n"
    "s_waitcnt lgkmcnt(1)\n"
    "v_mfma_f32_16x16x32_bf16 v[76:79], v[132:135], v[16:19], v[76:79]\n"
    "ds_read_b128 v[16:19], v9 offset:15360\n"
    "s_branch .LgP_join1_%=\n"
    ".LgP_dma1_66_%=:\n"
    "v_mfma_f32_16x16x32_bf16 v[40:43], v[88:91], v[24:27], v[40:43]\n"
    "s_add_u32 m0, s59, 0\n"
    "v_mfma_f32_16x16x32_bf16 v[44:47], v[92:95], v[24:27], v[44:47]\n"
    "buffer_load_dwordx4 v1, s[36:39], s76 offen sc1 lds\n"
    "v_mfma_f32_16x16x32_bf16 v[52:55], v[24:27], v[96:99], v[52:55]\n"
    "s_add_u32 s76, s76, 16384\n"
    "s_add_u32 m0, s59, 4096\n"
    "v_mfma_f32_16x16x32_bf16 v[60:63], v[24:27], v[100:103], v[60:63]\n"
    "buffer_load_dwordx4 v1, s[36:39], s76 offen sc1 lds\n"
    "v_mfma_f32_16x16x32_bf16 v[72:75], v[104:107], v[24:27], v[72:75]\n"
    "s_waitcnt lgkmcnt(1)\n"
    "v_mfma_f32_16x16x32_bf16 v[76:79], v[108:111], v[24:27], v[76:79]\n"
    "ds_read_b128 v[24:27], v9 offset:14336\n"
    "v_mfma_f32_16x16x32_bf16 v[40:43], v[112:115], v[16:19], v[40:43]\n"
    "s_add_u32 s76, s76, 16384\n"
    "s_add_u32 m0, s59, 8192\n"
    "v_mfma_f32_16x16x32_bf16 v[44:47], v[116:119], v[16:19], v[44:47]\n"
    "buffer_load_dwordx4 v1, s[36:39], s76 offen sc1 lds\n"
    "v_mfma_f32_16x16x32_bf16 v[52:55], v[16:19], v[120:123], v[52:55]\n"
    "v_mfma_f32_16x16x32_bf16 v[60:63], v[16:19], v[124:127], v[60:63]\n"
    "v_mfma_f32_16x16x32_bf16 v[72:75], v[128:131], v[16:19], v[72:75]\n"
    "s_waitcnt lgkmcnt(1)\n"
    "s_add_u32 s76, s76, 16384\n"
    "s_add_u32 m0, s59, 12288\n"
    "v_mfma_f32_16x16x32_bf16 v[76:79], v[132:135], v[16:19], v[76:79]\n"
    "ds_read_b128 v[16:19], v9 offset:15360\n"
    "buffer_load_dwordx4 v1, s[36:39], s76 offen sc1 lds\n"
    "v_mfma_f32_16x16x32_bf16 v[40:43], v[136:139], v[20:23], v[40:43]\n"
    "v_mfma_f32_16x16x32_bf16 v[44:47], v[140:143], v[20:23], v[44:47]\n"
    "v_mfma_f32_16x16x32_bf16 v[52:55], v[20:23], v[144:147], v[52:55]\n"
    "s_add_u32 s76, s76, 16384\n"
    "s_add_u32 m0, s59, 16384\n"
    "v_mfma_f32_16x16x32_bf16 v[60:63], v[20:23], v[148:151], v[60:63]\n"
    "buffer_load_dwordx4 v8, s[36:39], s76 offen sc1 lds\n"
    "v_mfma_f32_16x16x32_bf16 v[72:75], v[152:155], v[20:23], v[72:75]\n"
    "s_waitcnt lgkmcnt(1)\n"
    "v_mfma_f32_16x16x32_bf16 v[76:79], v[156:159], v[20:23], v[76:79]\n"
    "ds_read_b128 v[20:23], v9 offset:16384\n"
    "v_mfma_f32_16x16x32_bf16 v[40:43], v[160:163], v[24:27], v[40:43]\n"
    "v_mfma_f32_16x16x32_bf16 v[44:47], v[164:167], v[24:27], v[44:47]\n"
    "v_mfma_f32_16x16x32_bf16 v[52:55], v[24:27], v[168:171], v[52:55]\n"
    "v_mfma_f32_16x16x32_bf16 v[60:63], v[24:27], v[172:175], v[60:63]\n"
    "v_mfma_f32_16x16x32_bf16 v[72:75], v[176:179], v[24:27], v[72:75]\n"
    "s_waitcnt lgkmcnt(1)\n"
    "v_mfma_f32_16x16x32_bf16 v[76:79], v[180:183], v[24:27], v[76:79]\n"
    "ds_read_b128 v[24:27], v9 offset:17408\n"
    "v_mfma_f32_16x16x32_bf16 v[40:43], v[184:187], v[16:19], v[40:43]\n"
    "v_mfma_f32_16x16x32_bf16 v[44:47], v[188:191], v[16:19], v[44:47]\n"
    "v_mfma_f32_16x16x32_bf16 v[52:55], v[16:19], v[192:195], v[52:55]\n"
    "v_mfma_f32_16x16x32_bf16 v[60:63], v[16:19], v[196:199], v[60:63]\n"
    "v_mfma_f32_16x16x32_bf16 v[72:75], v[200:203], v[16:19], v[72:75]\n"
    "s_waitcnt lgkmcnt(1)\n"
    "v_mfma_f32_16x16x32_bf16 v[76:79], v[204:207], v[16:19], v[76:79]\n"
    "v_mfma_f32_16x16x32_bf16 v[40:43], v[208:211], v[20:23], v[40:43]\n"
    "v_mfma_f32_16x16x32_bf16 v[44:47], v[212:215], v[20:23], v[44:47]\n"
    "v_mfma_f32_16x16x32_bf16 v[52:55], v[20:23], v[216:219], v[52:55]\n"
    "v_mfma_f32_16x16x32_bf16 v[60:63], v[20:23], v[220:223], v[60:63]\n"
    "v_mfma_f32_16x16x32_bf16 v[72:75], v[224:227], v[20:23], v[72:75]\n"
    "s_waitcnt lgkmcnt(0)\n"
    "v_mfma_f32_16x16x32_bf16 v[76:79], v[228:231], v[20:23], v[76:79]\n"
    "v_mfma_f32_16x16x32_bf16 v[40:43], v[232:235], v[24:27], v[40:43]\n"
    "v_mfma_f32_16x16x32_bf16 v[44:47], v[236:239], v[24:27], v[44:47]\n"
    "v_mfma_f32_16x16x32_bf16 v[52:55], v[24:27], v[240:243], v[52:55]\n"
    "v_mfma_f32_16x16x32_bf16 v[60:63], v[24:27], v[244:247], v[60:63]\n"
    "v_mfma_f32_16x16x32_bf16 v[72:75], v[248:251], v[24:27], v[72:75]\n"
    "v_mfma_f32_16x16x32_bf16 v[76:79], v[252:255], v[24:27], v[76:79]\n"
    "v_cvt_pk_bf16_f32 v40, v40, v41\n"
    "v_cvt_pk_bf16_f32 v41, v42, v43\n"
    "v_cvt_pk_bf16_f32 v42, v44, v45\n"
    "v_cvt_pk_bf16_f32 v43, v46, v47\n"
    "buffer_store_dwordx4 v[40:43], v1, s[36:39], s79 offen offset:1024 sc1\n"
    "v_cvt_pk_bf16_f32 v55, v54, v55\n"
    "v_cvt_pk_bf16_f32 v54, v52, v53\n"
    "v_mov_b64 v[52:53], v[28:29]\n"
    "buffer_store_dwordx4 v[52:55], v1, s[36:39], s80 offen sc1\n"
    "v_cvt_pk_bf16_f32 v63, v62, v63\n"
    "v_cvt_pk_bf16_f32 v62, v60, v61\n"
    "v_mov_b64 v[60:61], v[30:31]\n"
    "buffer_store_dwordx4 v[60:63], v1, s[36:39], s80 offen offset:1024 sc1\n"
    "v_cvt_pk_bf16_f32 v72, v72, v73\n"
    "v_cvt_pk_bf16_f32 v73, v74, v75\n"
    "v_cvt_pk_bf16_f32 v74, v76, v77\n"
    "v_cvt_pk_bf16_f32 v75, v78, v79\n"
    "buffer_store_dwordx4 v[72:75], v1, s[36:39], s73 offen offset:1024 sc1\n"
    "v_mov_b32 v12, s68\n"
    "s_mov_b32 s73, 0x80000000\n"
    "s_mov_b32 s79, 0x80000000\n"
    "s_mov_b32 s80, 0x80000000\n"
    "s_mov_b32 s68, s71\n"
    "s_mov_b32 s69, 0\n"
    "s_mov_b32 s70, 0x80000000\n"
    "v_mov_b32 v7, v12\n"
    "s_waitcnt vmcnt(10)\n"
    "s_add_u32 s71, s68, 1\n"
    "v_add_u32 v13, v11, v3\n"
    "v_cmp_gt_i32 vcc, s71, v13\n"
    "s_nop 0\n"
    "s_or_b64 exec, vcc, s[62:63]\n"
    "buffer_atomic_smax v11, v2, s[36:39], 0 offen sc0 sc1\n"
    "s_mov_b64 exec, -1\n"
    "s_waitcnt vmcnt(5)\n"
    "s_branch .LgP_bar0_%=\n"
    ".LgP_dma1_84_%=:\n"
    "v_mfma_f32_16x16x32_bf16 v[40:43], v[160:163], v[24:27], v[40:43]\n"
    "s_add_u32 m0, s59, 0\n"
    "v_mfma_f32_16x16x32_bf16 v[44:47], v[164:167], v[24:27], v[44:47]\n"
    "buffer_load_dwordx4 v1, s[36:39], s76 offen sc1 lds\n"
    "v_mfma_f32_16x16x32_bf16 v[52:55], v[24:27], v[168:171], v[52:55]\n"
    "s_add_u32 s76, s76, 16384\n"
    "s_add_u32 m0, s59, 4096\n"
    "v_mfma_f32_16x16x32_bf16 v[60:63], v[24:27], v[172:175], v[60:63]\n"
    "buffer_load_dwordx4 v1, s[36:39], s76 offen sc1 lds\n"
    "v_mfma_f32_16x16x32_bf16 v[72:75], v[176:179], v[24:27], v[72:75]\n"
    "s_waitcnt lgkmcnt(1)\n"
    "v_mfma_f32_16x16x32_bf16 v[76:79], v[180:183], v[24:27], v[76:79]\n"
    "ds_read_b128 v[24:27], v9 offset:17408\n"
    "v_mfma_f32_16x16x32_bf16 v[40:43], v[184:187], v[16:19], v[40:43]\n"
    "s_add_u32 s76, s76, 16384\n"
    "s_add_u32 m0, s59, 8192\n"
    "v_mfma_f32_16x16x32_bf16 v[44:47], v[188:191], v[16:19], v[44:47]\n"
    "buffer_load_dwordx4 v1, s[36:39], s76 offen sc1 lds\n"
    "v_mfma_f32_16x16x32_bf16 v[52:55], v[16:19], v[192:195], v[52:55]\n"
    "v_mfma_f32_16x16x32_bf16 v[60:63], v[16:19], v[196:199], v[60:63]\n"
    "v_mfma_f32_16x16x32_bf16 v[72:75], v[200:203], v[16:19], v[72:75]\n"
    "s_waitcnt lgkmcnt(1)\n"
    "s_add_u32 s76, s76, 16384\n"
    "s_add_u32 m0, s59, 12288\n"
    "v_mfma_f32_16x16x32_bf16 v[76:79], v[204:207], v[16:19], v[76:79]\n"
    "buffer_load_dwordx4 v1, s[36:39], s76 offen sc1 lds\n"
    "v_mfma_f32_16x16x32_bf16 v[40:43], v[208:211], v[20:23], v[40:43]\n"
    "v_mfma_f32_16x16x32_bf16 v[44:47], v[212:215], v[20:23], v[44:47]\n"
    "v_mfma_f32_16x16x32_bf16 v[52:55], v[20:23], v[216:219], v[52:55]\n"
    "s_add_u32 s76, s76, 16384\n"
    "s_add_u32 m0, s59, 16384\n"
    "v_mfma_f32_16x16x32_bf16 v[60:63], v[20:23], v[220:223], v[60:63]\n"
    "buffer_load_dwordx4 v8, s[36:39], s76 offen sc1 lds\n"
    "v_mfma_f32_16x16x32_bf16 v[72:75], v[224:227], v[20:23], v[72:75]\n"
    "s_waitcnt lgkmcnt(0)\n"
    "v_mfma_f32_16x16x32_bf16 v[76:79], v[228:231], v[20:23], v[76:79]\n"
    "v_mfma_f32_16x16x32_bf16 v[40:43], v[232:235], v[24:27], v[40:43]\n"
    "v_mfma_f32_16x16x32_bf16 v[44:47], v[236:239], v[24:27], v[44:47]\n"
    "v_mfma_f32_16x16x32_bf16 v[52:55], v[24:27], v[240:243], v[52:55]\n"
    "v_mfma_f32_16x16x32_bf16 v[60:63], v[24:27], v[244:247], v[60:63]\n"
    "v_mfma_f32_16x16x32_bf16 v[72:75], v[248:251], v[24:27], v[72:75]\n"
    "v_mfma_f32_16x16x32_bf16 v[76:79], v[252:255], v[24:27], v[76:79]\n"
    "v_cvt_pk_bf16_f32 v40, v40, v41\n"
    "v_cvt_pk_bf16_f32 v41, v42, v43\n"
    "v_cvt_pk_bf16_f32 v42, v44, v45\n"
    "v_cvt_pk_bf16_f32 v43, v46, v47\n"
    "buffer_store_dwordx4 v[40:43], v1, s[36:39], s79 offen offset:1024 sc1\n"
    "v_cvt_pk_bf16_f32 v55, v54, v55\n"
    "v_cvt_pk_bf16_f32 v54, v52, v53\n"
    "v_mov_b64 v[52:53], v[28:29]\n"
    "buffer_store_dwordx4 v[52:55], v1, s[36:39], s80 offen sc1\n"
    "v_cvt_pk_bf16_f32 v63, v62, v63\n"
    "v_cvt_pk_bf16_f32 v62, v60, v61\n"
    "v_mov_b64 v[60:61], v[30:31]\n"
    "buffer_store_dwordx4 v[60:63], v1, s[36:39], s80 offen offset:1024 sc1\n"
    "v_cvt_pk_bf16_f32 v72, v72, v73\n"
    "v_cvt_pk_bf16_f32 v73, v74, v75\n"
    "v_cvt_pk_bf16_f32 v74, v76, v77\n"
    "v_cvt_pk_bf16_f32 v75, v78, v79\n"
    "buffer_store_dwordx4 v[72:75], v1, s[36:39], s73 offen offset:1024 sc1\n"
    "v_mov_b32 v12, s68\n"
    "s_mov_b32 s73, 0x80000000\n"
    "s_mov_b32 s79, 0x80000000\n"
    "s_mov_b32 s80, 0x80000000\n"
    "s_mov_b32 s68, s71\n"
    "s_mov_b32 s69, 0\n"
    "s_mov_b32 s70, 0x80000000\n"
    "v_mov_b32 v7, v12\n"
    "s_waitcnt vmcnt(9)\n"
    "s_add_u32 s71, s68, 1\n"
    "v_add_u32 v13, v11, v3\n"
    "v_cmp_gt_i32 vcc, s71, v13\n"
    "s_nop 0\n"
    "s_or_b64 exec, vcc, s[62:63]\n"
    "buffer_atomic_smax v11, v2, s[36:39], 0 offen sc0 sc1\n"
    "s_mov_b64 exec, -1\n"
    "s_waitcnt vmcnt(5)\n"
    "s_branch .LgP_bar0_%=\n"
    ".LgP_dma1_102_%=:\n"
    "v_mfma_f32_16x16x32_bf16 v[40:43], v[232:235], v[24:27], v[40:43]\n"
    "s_add_u32 m0, s59, 0\n"
    "v_mfma_f32_16x16x32_bf16 v[44:47], v[236:239], v[24:27], v[44:47]\n"
    "buffer_load_dwordx4 v1, s[36:39], s76 offen sc1 lds\n"
    "v_mfma_f32_16x16x32_bf16 v[52:55], v[24:27], v[240:243], v[52:55]\n"
    "s_add_u32 s76, s76, 16384\n"
    "s_add_u32 m0, s59, 4096\n"
    "v_mfma_f32_16x16x32_bf16 v[60:63], v[24:27], v[244:247], v[60:63]\n"
    "buffer_load_dwordx4 v1, s[36:39], s76 offen sc1 lds\n"
    "v_mfma_f32_16x16x32_bf16 v[72:75], v[248:251], v[24:27], v[72:75]\n"
    "v_mfma_f32_16x16x32_bf16 v[76:79], v[252:255], v[24:27], v[76:79]\n"
    "v_cvt_pk_bf16_f32 v40, v40, v41\n"
    "v_cvt_pk_bf16_f32 v41, v42, v43\n"
    "v_cvt_pk_bf16_f32 v42, v44, v45\n"
    "v_cvt_pk_bf16_f32 v43, v46, v47\n"
    "buffer_store_dwordx4 v[40:43], v1, s[36:39], s79 offen offset:1024 sc1\n"
    "v_cvt_pk_bf16_f32 v55, v54, v55\n"
    "v_cvt_pk_bf16_f32 v54, v52, v53\n"
    "v_mov_b64 v[52:53], v[28:29]\n"
    "buffer_store_dwordx4 v[52:55], v1, s[36:39], s80 offen sc1\n"
    "v_cvt_pk_bf16_f32 v63, v62, v63\n"
    "v_cvt_pk_bf16_f32 v62, v60, v61\n"
    "v_mov_b64 v[60:61], v[30:31]\n"
    "buffer_store_dwordx4 v[60:63], v1, s[36:39], s80 offen offset:1024 sc1\n"
    "v_cvt_pk_bf16_f32 v72, v72, v73\n"
    "v_cvt_pk_bf16_f32 v73, v74, v75\n"
    "v_cvt_pk_bf16_f32 v74, v76, v77\n"
    "v_cvt_pk_bf16_f32 v75, v78, v79\n"
    "buffer_store_dwordx4 v[72:75], v1, s[36:39], s73 offen offset:1024 sc1\n"
    "v_mov_b32 v12, s68\n"
    "s_mov_b32 s73, 0x80000000\n"
    "s_mov_b32 s79, 0x80000000\n"
    "s_mov_b32 s80, 0x80000000\n"
    "s_mov_b32 s68, s71\n"
    "s_add_u32 m0, s59, 8192\n"
    "s_add_u32 s76, s76, 16384\n"
    "buffer_load_dwordx4 v1, s[36:39], s76 offen sc1 lds\n"
    "s_add_u32 m0, s59, 12288\n"
    "s_add_u32 s76, s76, 16384\n"
    "buffer_load_dwordx4 v1, s[36:39], s76 offen sc1 lds\n"
    "s_add_u32 m0, s59, 16384\n"
    "s_add_u32 s76, s76, 16384\n"
    "buffer_load_dwordx4 v8, s[36:39], s76 offen sc1 lds\n"
    "s_waitcnt vmcnt(3)\n"
    "buffer_atomic_smax v12, v10, s[36:39], 0 offen sc1\n"
    "s_mov_b32 s69, 0x80000000\n"
    "s_mov_b32 s70, 0x80000000\n"
    "v_mov_b32 v7, v12\n"
    "s_waitcnt vmcnt(10)\n"
    "s_add_u32 s71, s68, 1\n"
    "v_add_u32 v13, v11, v3\n"
    "v_cmp_gt_i32 vcc, s71, v13\n"
    "s_nop 0\n"
    "s_or_b64 exec, vcc, s[62:63]\n"
    "buffer_atomic_smax v11, v2, s[36:39], 0 offen sc0 sc1\n"
    "s_mov_b64 exec, -1\n"
    "s_waitcnt vmcnt(2)\n"
    "s_branch .LgP_bar0_%=\n"
    ".LgP_idle1_%=:\n"
    "s_or_b64 exec, vcc, s[62:63]\n"
    "buffer_atomic_smax v11, v2, s[36:39], 0 offen sc0 sc1\n"
    "s_mov_b64 exec, -1\n"
    "s_waitcnt vmcnt(1)\n"
    "buffer_atomic_smax v12, v10, s[36:39], 0 offen sc1\n"
    "s_branch .LgP_block1_0_%=\n"
    ".LgP_block1_%=:\n"
    "v_add_u32 v13, v11, v3\n"
    "v_cmp_gt_i32 vcc, s71, v13\n"
    "s_nop 0\n"
    "s_or_b64 exec, vcc, s[62:63]\n"
    "buffer_atomic_smax v11, v2, s[36:39], 0 offen sc0 sc1\n"
    "s_mov_b64 exec, -1\n"
    "s_sleep 3\n"
    "s_waitcnt vmcnt(1)\n"
    ".LgP_block1_0_%=:\n"
    "v_add_u32 v13, v5, v3\n"
    "v_cmp_gt_i32 vcc, s71, v13\n"
    "s_cbranch_vccz .LgP_go1_%=\n"
    "v_add_u32 v13, v5, v3\n"
    "v_cmp_gt_i32 vcc, s71, v13\n"
    "s_nop 0\n"
    "s_or_b64 exec, vcc, s[62:63]\n"
    "buffer_atomic_smax v5, v2, s[36:39], 0 offen sc0 sc1\n"
    "s_mov_b64 exec, -1\n"
    "s_sleep 3\n"
    "s_waitcnt vmcnt(1)\n"
    ".LgP_block1_1_%=:\n"
    "v_add_u32 v13, v11, v3\n"
    "v_cmp_gt_i32 vcc, s71, v13\n"
    "s_cbranch_vccz .LgP_go1_%=\n"
    "s_sub_u32 s72, s72, 1\n"
    "s_cbranch_scc0 .LgP_block1_%=\n"
    "s_mov_b32 s72, 0\n"
    ".LgP_go1_%=:\n"
    "s_lshr_b32 s76, s71, 1\n"
    "s_and_b32 s76, s76, s54\n"
    "s_mul_i32 s76, s76, s53\n"
    "s_add_u32 s76, s76, s52\n"
    "s_add_u32 m0, s59, 0\n"
    "s_nop 0\n"
    "buffer_load_dwordx4 v1, s[36:39], s76 offen sc1 lds\n"
    "s_add_u32 m0, s59, 4096\n"
    "s_add_u32 s76, s76, 16384\n"
    "buffer_load_dwordx4 v1, s[36:39], s76 offen sc1 lds\n"
    "s_add_u32 m0, s59, 8192\n"
    "s_add_u32 s76, s76, 16384\n"
    "buffer_load_dwordx4 v1, s[36:39], s76 offen sc1 lds\n"
    "s_add_u32 m0, s59, 12288\n"
    "s_add_u32 s76, s76, 16384\n"
    "buffer_load_dwordx4 v1, s[36:39], s76 offen sc1 lds\n"
    "s_add_u32 m0, s59, 16384\n"
    "s_add_u32 s76, s76, 16384\n"
    "buffer_load_dwordx4 v8, s[36:39], s76 offen sc1 lds\n"
    "s_mov_b32 s69, 0x80000000\n"
    "s_mov_b32 s70, 0x80000000\n"
    "v_mov_b32 v7, v12\n"
    "s_waitcnt vmcnt(5)\n"
    "s_add_u32 s71, s68, 1\n"
    "v_add_u32 v13, v11, v3\n"
    "v_cmp_gt_i32 vcc, s71, v13\n"
    "s_nop 0\n"
    "s_or_b64 exec, vcc, s[62:63]\n"
    "buffer_atomic_smax v11, v2, s[36:39], 0 offen sc0 sc1\n"
    "s_mov_b64 exec, -1\n"
    "s_waitcnt vmcnt(1)\n"
    "s_branch .LgP_bar0_%=\n"
    ".LgP_bar1_%=:\n"
    "s_barrier\n"
    "ds_read_b128 v[16:19], v9 offset:0\n"
    "ds_read_b128 v[20:23], v9 offset:1024\n"
    "ds_read_b128 v[24:27], v9 offset:2048\n"
    "v_add_u32 v13, v5, v3\n"
    "v_cmp_gt_i32 vcc, s71, v13\n"
    "s_nop 0\n"
    "s_or_b64 exec, vcc, s[62:63]\n"
    "buffer_atomic_smax v5, v2, s[36:39], 0 offen sc0 sc1\n"
    "s_mov_b64 exec, -1\n"
    "s_lshr_b32 s76, s71, 1\n"
    "s_and_b32 s76, s76, s54\n"
    "s_mul_i32 s76, s76, s53\n"
    "s_add_u32 s76, s76, s52\n"
    "s_waitcnt lgkmcnt(2)\n"
    "s_branch .LgP_head1_%=\n"
    ".LgP_FIN_%=:\n"
  );
}
DEVI void role_up(const Params& p, int L, int j, int i, u32 dbg, unsigned long long tag) {
  const int lane = threadIdx.x & 63, w = threadIdx.x >> 6, col = (6 * j + i) * 192;
  const u32 wb = 576u * 4608 * 2 - col * 2;
  GEMM_DECL;
  g_w0 = make_srd(p.w1 + col, wb); g_w1 = make_srd(p.w3 + col, wb); g_w2 = g_w1;
  g_ldw = 9216;
  g_in_base = pgb(L, B_XN2); g_in_stride = SLOT; g_in_mask = RX - 1;
  g_out_base = pgb(L, B_H + 9 * j); g_out_kb = 12 * i; g_out_mask = RH - 1;
  g_flag = lane < 3 ? mbox(L, j, SL_DOWN + lane, 4 * i + w) : OOB;
  g_n0 = 64; g_n1 = 0;
  g_paddr = mbox(L, j, SL_UP + i, lane);
  g_pbias = lane < 16 ? lane >> 3 : (lane < 19 ? 2 * RH : 1000);
  v16u sa, sb, va;
  gemm_args(GEMM_ARGS, p, dbg, tag, sa, sb, va);
  RUN_ASM(
    "s_nop 4\n"
    "s_getpc_b64 s[84:85]\n"
    ".LgUP_PC_%=:\n"
    "s_and_b32 s85, s85, 0xffff\n"
    "s_mov_b32 s86, .LgUP_FIN_%=-.LgUP_PC_%=\n"
    "s_mov_b32 s87, 0x20000\n"
    "v_mbcnt_lo_u32_b32 v40, -1, 0\n"
    "v_mbcnt_hi_u32_b32 v40, -1, v40\n"
    "v_lshlrev_b32 v40, 7, v40\n"
    "buffer_load_dword v41, v40, s[84:87], 0 offen\n"
    "v_add_u32 v40, 8192, v40\n"
    "buffer_load_dword v41, v40, s[84:87], 0 offen\n"
    "v_add_u32 v40, 8192, v40\n"
    "buffer_load_dword v41, v40, s[84:87], 0 offen\n"
    "s_mov_b32 s68, 0\n"
    "s_mov_b32 s71, 0\n"
    "v_add_u32 v9, 20480, v0\n"
    "s_mov_b32 s64, 0xbfb8aa3b\n"
    "s_mov_b32 s72, 262144\n"
    "s_add_u32 m0, s59, 0\n"
    "s_mov_b32 s74, 0\n"
    "buffer_load_dwordx4 v5, s[40:43], s74 offen lds\n"
    "s_add_u32 m0, s59, 4096\n"
    "s_mov_b32 s74, 128\n"
    "buffer_load_dwordx4 v5, s[40:43], s74 offen lds\n"
    "s_add_u32 m0, s59, 8192\n"
    "s_mov_b32 s74, 256\n"
    "buffer_load_dwordx4 v5, s[40:43], s74 offen lds\n"
    "s_add_u32 m0, s59, 12288\n"
    "s_mov_b32 s74, 0\n"
    "buffer_load_dwordx4 v5, s[44:47], s74 offen lds\n"
    "s_add_u32 m0, s59, 16384\n"
    "s_mov_b32 s74, 128\n"
    "buffer_load_dwordx4 v5, s[44:47], s74 offen lds\n"
    "s_add_u32 m0, s59, 20480\n"
    "s_mov_b32 s74, 256\n"
    "buffer_load_dwordx4 v5, s[44:47], s74 offen lds\n"
    "s_add_u32 m0, s59, 24576\n"
    "s_mov_b32 s74, 294912\n"
    "buffer_load_dwordx4 v5, s[40:43], s74 offen lds\n"
    "s_add_u32 m0, s59, 28672\n"
    "s_mov_b32 s74, 295040\n"
    "buffer_load_dwordx4 v5, s[40:43], s74 offen lds\n"
    "s_add_u32 m0, s59, 32768\n"
    "s_mov_b32 s74, 295168\n"
    "buffer_load_dwordx4 v5, s[40:43], s74 offen lds\n"
    "s_add_u32 m0, s59, 36864\n"
    "s_mov_b32 s74, 294912\n"
    "buffer_load_dwordx4 v5, s[44:47], s74 offen lds\n"
    "s_add_u32 m0, s59, 40960\n"
    "s_mov_b32 s74, 295040\n"
    "buffer_load_dwordx4 v5, s[44:47], s74 offen lds\n"
    "s_add_u32 m0, s59, 45056\n"
    "s_mov_b32 s74, 295168\n"
    "buffer_load_dwordx4 v5, s[44:47], s74 offen lds\n"
    "s_add_u32 m0, s59, 49152\n"
    "s_mov_b32 s74, 589824\n"
    "buffer_load_dwordx4 v5, s[40:43], s74 offen lds\n"
    "s_add_u32 m0, s59, 53248\n"
    "s_mov_b32 s74, 589952\n"
    "buffer_load_dwordx4 v5, s[40:43], s74 offen lds\n"
    "s_add_u32 m0, s59, 57344\n"
    "s_mov_b32 s74, 590080\n"
    "buffer_load_dwordx4 v5, s[40:43], s74 offen lds\n"
    "s_add_u32 m0, s59, 61440\n"
    "s_mov_b32 s74, 589824\n"
    "buffer_load_dwordx4 v5, s[44:47], s74 offen lds\n"
    "s_add_u32 m0, s59, 65536\n"
    "s_mov_b32 s74, 589952\n"
    "buffer_load_dwordx4 v5, s[44:47], s74 offen lds\n"
    "s_add_u32 m0, s59, 69632\n"
    "s_mov_b32 s74, 590080\n"
    "buffer_load_dwordx4 v5, s[44:47], s74 offen lds\n"
    "s_add_u32 m0, s59, 73728\n"
    "s_mov_b32 s74, 884736\n"
    "buffer_load_dwordx4 v5, s[40:43], s74 offen lds\n"
    "s_add_u32 m0, s59, 77824\n"
    "s_mov_b32 s74, 884864\n"
    "buffer_load_dwordx4 v5, s[40:43], s74 offen lds\n"
    "s_add_u32 m0, s59, 81920\n"
    "s_mov_b32 s74, 884992\n"
    "buffer_load_dwordx4 v5, s[40:43], s74 offen lds\n"
    "s_add_u32 m0, s59, 86016\n"
    "s_mov_b32 s74, 884736\n"
    "buffer_load_dwordx4 v5, s[44:47], s74 offen lds\n"
    "s_add_u32 m0, s59, 90112\n"
    "s_mov_b32 s74, 884864\n"
    "buffer_load_dwordx4 v5, s[44:47], s74 offen lds\n"
    "s_add_u32 m0, s59, 94208\n"
    "s_mov_b32 s74, 884992\n"
    "buffer_load_dwordx4 v5, s[44:47], s74 offen lds\n"
    "s_add_u32 m0, s59, 98304\n"
    "s_mov_b32 s74, 1179648\n"
    "buffer_load_dwordx4 v5, s[40:43], s74 offen lds\n"
    "s_add_u32 m0, s59, 102400\n"
    "s_mov_b32 s74, 1179776\n"
    "buffer_load_dwordx4 v5, s[40:43], s74 offen lds\n"
    "s_add_u32 m0, s59, 106496\n"
    "s_mov_b32 s74, 1179904\n"
    "buffer_load_dwordx4 v5, s[40:43], s74 offen lds\n"
    "s_add_u32 m0, s59, 110592\n"
    "s_mov_b32 s74, 1179648\n"
    "buffer_load_dwordx4 v5, s[44:47], s74 offen lds\n"
    "s_add_u32 m0, s59, 114688\n"
    "s_mov_b32 s74, 1179776\n"
    "buffer_load_dwordx4 v5, s[44:47], s74 offen lds\n"
    "s_add_u32 m0, s59, 118784\n"
    "s_mov_b32 s74, 1179904\n"
    "buffer_load_dwordx4 v5, s[44:47], s74 offen lds\n"
    "v_add_u32 v16, 49152, v6\n"
    "v_add_u32 v17, 98304, v6\n"
    "v_add_u32 v18, 49152, v7\n"
    "v_add_u32 v19, 98304, v7\n"
    "s_waitcnt vmcnt(24) lgkmcnt(0)\n"
    "s_barrier\n"
    "s_add_u32 m0, s59, 122880\n"
    "s_mov_b32 s74, 1474560\n"
    "buffer_load_dwordx4 v5, s[40:43], s74 offen lds\n"
    "s_add_u32 m0, s59, 126976\n"
    "s_mov_b32 s74, 1474688\n"
    "buffer_load_dwordx4 v5, s[40:43], s74 offen lds\n"
    "s_add_u32 m0, s59, 131072\n"
    "s_mov_b32 s74, 1474816\n"
    "buffer_load_dwordx4 v5, s[40:43], s74 offen lds\n"
    "s_add_u32 m0, s59, 135168\n"
    "s_mov_b32 s74, 1474560\n"
    "buffer_load_dwordx4 v5, s[44:47], s74 offen lds\n"
    "s_add_u32 m0, s59, 139264\n"
    "s_mov_b32 s74, 1474688\n"
    "buffer_load_dwordx4 v5, s[44:47], s74 offen lds\n"
    "s_add_u32 m0, s59, 143360\n"
    "s_mov_b32 s74, 1474816\n"
    "buffer_load_dwordx4 v5, s[44:47], s74 offen lds\n"
    "ds_read_b64_tr_b16 a[0:1], v6 offset:0\n"
    "ds_read_b64_tr_b16 a[2:3], v6 offset:512\n"
    "ds_read_b64_tr_b16 a[4:5], v6 offset:8\n"
    "ds_read_b64_tr_b16 a[6:7], v6 offset:520\n"
    "ds_read_b64_tr_b16 a[8:9], v7 offset:0\n"
    "ds_read_b64_tr_b16 a[10:11], v7 offset:512\n"
    "ds_read_b64_tr_b16 a[12:13], v6 offset:12288\n"
    "ds_read_b64_tr_b16 a[14:15], v6 offset:12800\n"
    "ds_read_b64_tr_b16 a[16:17], v6 offset:12296\n"
    "ds_read_b64_tr_b16 a[18:19], v6 offset:12808\n"
    "ds_read_b64_tr_b16 a[20:21], v7 offset:12288\n"
    "ds_read_b64_tr_b16 a[22:23], v7 offset:12800\n"
    "s_waitcnt vmcnt(24) lgkmcnt(0)\n"
    "s_barrier\n"
    "s_add_u32 m0, s59, 0\n"
    "s_mov_b32 s74, 1769472\n"
    "buffer_load_dwordx4 v5, s[40:43], s74 offen lds\n"
    "s_add_u32 m0, s59, 4096\n"
    "s_mov_b32 s74, 1769600\n"
    "buffer_load_dwordx4 v5, s[40:43], s74 offen lds\n"
    "s_add_u32 m0, s59, 8192\n"
    "s_mov_b32 s74, 1769728\n"
    "buffer_load_dwordx4 v5, s[40:43], s74 offen lds\n"
    "s_add_u32 m0, s59, 12288\n"
    "s_mov_b32 s74, 1769472\n"
    "buffer_load_dwordx4 v5, s[44:47], s74 offen lds\n"
    "s_add_u32 m0, s59, 16384\n"
    "s_mov_b32 s74, 1769600\n"
    "buffer_load_dwordx4 v5, s[44:47], s74 offen lds\n"
    "s_add_u32 m0, s59, 20480\n"
    "s_mov_b32 s74, 1769728\n"
    "buffer_load_dwordx4 v5, s[44:47], s74 offen lds\n"
    "ds_read_b64_tr_b16 a[24:25], v6 offset:24576\n"
    "ds_read_b64_tr_b16 a[26:27], v6 offset:25088\n"
    "ds_read_b64_tr_b16 a[28:29], v6 offset:24584\n"
    "ds_read_b64_tr_b16 a[30:31], v6 offset:25096\n"
    "ds_read_b64_tr_b16 a[32:33], v7 offset:24576\n"
    "ds_read_b64_tr_b16 a[34:35], v7 offset:25088\n"
    "ds_read_b64_tr_b16 a[36:37], v6 offset:36864\n"
    "ds_read_b64_tr_b16 a[38:39], v6 offset:37376\n"
    "ds_read_b64_tr_b16 a[40:41], v6 offset:36872\n"
    "ds_read_b64_tr_b16 a[42:43], v6 offset:37384\n"
    "ds_read_b64_tr_b16 a[44:45], v7 offset:36864\n"
    "ds_read_b64_tr_b16 a[46:47], v7 offset:37376\n"
    "s_waitcnt vmcnt(24) lgkmcnt(0)\n"
    "s_barrier\n"
    "s_add_u32 m0, s59, 24576\n"
    "s_mov_b32 s74, 2064384\n"
    "buffer_load_dwordx4 v5, s[40:43], s74 offen lds\n"
    "s_add_u32 m0, s59, 28672\n"
    "s_mov_b32 s74, 2064512\n"
    "buffer_load_dwordx4 v5, s[40:43], s74 offen lds\n"
    "s_add_u32 m0, s59, 32768\n"
    "s_mov_b32 s74, 2064640\n"
    "buffer_load_dwordx4 v5, s[40:43], s74 offen lds\n"
    "s_add_u32 m0, s59, 36864\n"
    "s_mov_b32 s74, 2064384\n"
    "buffer_load_dwordx4 v5, s[44:47], s74 offen lds\n"
    "s_add_u32 m0, s59, 40960\n"
    "s_mov_b32 s74, 2064512\n"
    "buffer_load_dwordx4 v5, s[44:47], s74 offen lds\n"
    "s_add_u32 m0, s59, 45056\n"
    "s_mov_b32 s74, 2064640\n"
    "buffer_load_dwordx4 v5, s[44:47], s74 offen lds\n"
    "ds_read_b64_tr_b16 a[48:49], v16 offset:0\n"
    "ds_read_b64_tr_b16 a[50:51], v16 offset:512\n"
    "ds_read_b64_tr_b16 a[52:53], v16 offset:8\n"
    "ds_read_b64_tr_b16 a[54:55], v16 offset:520\n"
    "ds_read_b64_tr_b16 a[56:57], v18 offset:0\n"
    "ds_read_b64_tr_b16 a[58:59], v18 offset:512\n"
    "ds_read_b64_tr_b16 a[60:61], v16 offset:12288\n"
    "ds_read_b64_tr_b16 a[62:63], v16 offset:12800\n"
    "ds_read_b64_tr_b16 a[64:65], v16 offset:12296\n"
    "ds_read_b64_tr_b16 a[66:67], v16 offset:12808\n"
    "ds_read_b64_tr_b16 a[68:69], v18 offset:12288\n"
    "ds_read_b64_tr_b16 a[70:71], v18 offset:12800\n"
    "s_waitcnt vmcnt(24) lgkmcnt(0)\n"
    "s_barrier\n"
    "s_add_u32 m0, s59, 49152\n"
    "s_mov_b32 s74, 2359296\n"
    "buffer_load_dwordx4 v5, s[40:43], s74 offen lds\n"
    "s_add_u32 m0, s59, 53248\n"
    "s_mov_b32 s74, 2359424\n"
    "buffer_load_dwordx4 v5, s[40:43], s74 offen lds\n"
    "s_add_u32 m0, s59, 57344\n"
    "s_mov_b32 s74, 2359552\n"
    "buffer_load_dwordx4 v5, s[40:43], s74 offen lds\n"
    "s_add_u32 m0, s59, 61440\n"
    "s_mov_b32 s74, 2359296\n"
    "buffer_load_dwordx4 v5, s[44:47], s74 offen lds\n"
    "s_add_u32 m0, s59, 65536\n"
    "s_mov_b32 s74, 2359424\n"
    "buffer_load_dwordx4 v5, s[44:47], s74 offen lds\n"
    "s_add_u32 m0, s59, 69632\n"
    "s_mov_b32 s74, 2359552\n"
    "buffer_load_dwordx4 v5, s[44:47], s74 offen lds\n"
    "ds_read_b64_tr_b16 a[72:73], v16 offset:24576\n"
    "ds_read_b64_tr_b16 a[74:75], v16 offset:25088\n"
    "ds_read_b64_tr_b16 a[76:77], v16 offset:24584\n"
    "ds_read_b64_tr_b16 a[78:79], v16 offset:25096\n"
    "ds_read_b64_tr_b16 a[80:81], v18 offset:24576\n"
    "ds_read_b64_tr_b16 a[82:83], v18 offset:25088\n"
    "ds_read_b64_tr_b16 a[84:85], v16 offset:36864\n"
    "ds_read_b64_tr_b16 a[86:87], v16 offset:37376\n"
    "ds_read_b64_tr_b16 a[88:89], v16 offset:36872\n"
    "ds_read_b64_tr_b16 a[90:91], v16 offset:37384\n"
    "ds_read_b64_tr_b16 a[92:93], v18 offset:36864\n"
    "ds_read_b64_tr_b16 a[94:95], v18 offset:37376\n"
    "s_waitcnt vmcnt(24) lgkmcnt(0)\n"
    "s_barrier\n"
    "s_add_u32 m0, s59, 73728\n"
    "s_mov_b32 s74, 2654208\n"
    "buffer_load_dwordx4 v5, s[40:43], s74 offen lds\n"
    "s_add_u32 m0, s59, 77824\n"
    "s_mov_b32 s74, 2654336\n"
    "buffer_load_dwordx4 v5, s[40:43], s74 offen lds\n"
    "s_add_u32 m0, s59, 81920\n"
    "s_mov_b32 s74, 2654464\n"
    "buffer_load_dwordx4 v5, s[40:43], s74 offen lds\n"
    "s_add_u32 m0, s59, 86016\n"
    "s_mov_b32 s74, 2654208\n"
    "buffer_load_dwordx4 v5, s[44:47], s74 offen lds\n"
    "s_add_u32 m0, s59, 90112\n"
    "s_mov_b32 s74, 2654336\n"
    "buffer_load_dwordx4 v5, s[44:47], s74 offen lds\n"
    "s_add_u32 m0, s59, 94208\n"
    "s_mov_b32 s74, 2654464\n"
    "buffer_load_dwordx4 v5, s[44:47], s74 offen lds\n"
    "ds_read_b64_tr_b16 a[96:97], v17 offset:0\n"
    "ds_read_b64_tr_b16 a[98:99], v17 offset:512\n"
    "ds_read_b64_tr_b16 a[100:101], v17 offset:8\n"
    "ds_read_b64_tr_b16 a[102:103], v17 offset:520\n"
    "ds_read_b64_tr_b16 a[104:105], v19 offset:0\n"
    "ds_read_b64_tr_b16 a[106:107], v19 offset:512\n"
    "ds_read_b64_tr_b16 a[108:109], v17 offset:12288\n"
    "ds_read_b64_tr_b16 a[110:111], v17 offset:12800\n"
    "ds_read_b64_tr_b16 a[112:113], v17 offset:12296\n"
    "ds_read_b64_tr_b16 a[114:115], v17 offset:12808\n"
    "ds_read_b64_tr_b16 a[116:117], v19 offset:12288\n"
    "ds_read_b64_tr_b16 a[118:119], v19 offset:12800\n"
    "s_waitcnt vmcnt(24) lgkmcnt(0)\n"
    "s_barrier\n"
    "s_add_u32 m0, s59, 98304\n"
    "s_mov_b32 s74, 2949120\n"
    "buffer_load_dwordx4 v5, s[40:43], s74 offen lds\n"
    "s_add_u32 m0, s59, 102400\n"
    "s_mov_b32 s74, 2949248\n"
    "buffer_load_dwordx4 v5, s[40:43], s74 offen lds\n"
    "s_add_u32 m0, s59, 106496\n"
    "s_mov_b32 s74, 2949376\n"
    "buffer_load_dwordx4 v5, s[40:43], s74 offen lds\n"
    "s_add_u32 m0, s59, 110592\n"
    "s_mov_b32 s74, 2949120\n"
    "buffer_load_dwordx4 v5, s[44:47], s74 offen lds\n"
    "s_add_u32 m0, s59, 114688\n"
    "s_mov_b32 s74, 2949248\n"
    "buffer_load_dwordx4 v5, s[44:47], s74 offen lds\n"
    "s_add_u32 m0, s59, 118784\n"
    "s_mov_b32 s74, 2949376\n"
    "buffer_load_dwordx4 v5, s[44:47], s74 offen lds\n"
    "ds_read_b64_tr_b16 a[120:121], v17 offset:24576\n"
    "ds_read_b64_tr_b16 a[122:123], v17 offset:25088\n"
    "ds_read_b64_tr_b16 a[124:125], v17 offset:24584\n"
    "ds_read_b64_tr_b16 a[126:127], v17 offset:25096\n"
    "ds_read_b64_tr_b16 a[128:129], v19 offset:24576\n"
    "ds_read_b64_tr_b16 a[130:131], v19 offset:25088\n"
    "ds_read_b64_tr_b16 a[132:133], v17 offset:36864\n"
    "ds_read_b64_tr_b16 a[134:135], v17 offset:37376\n"
    "ds_read_b64_tr_b16 a[136:137], v17 offset:36872\n"
    "ds_read_b64_tr_b16 a[138:139], v17 offset:37384\n"
    "ds_read_b64_tr_b16 a[140:141], v19 offset:36864\n"
    "ds_read_b64_tr_b16 a[142:143], v19 offset:37376\n"
    "s_waitcnt vmcnt(24) lgkmcnt(0)\n"
    "s_barrier\n"
    "s_add_u32 m0, s59, 122880\n"
    "s_mov_b32 s74, 3244032\n"
    "buffer_load_dwordx4 v5, s[40:43], s74 offen lds\n"
    "s_add_u32 m0, s59, 126976\n"
    "s_mov_b32 s74, 3244160\n"
    "buffer_load_dwordx4 v5, s[40:43], s74 offen lds\n"
    "s_add_u32 m0, s59, 131072\n"
    "s_mov_b32 s74, 3244288\n"
    "buffer_load_dwordx4 v5, s[40:43], s74 offen lds\n"
    "s_add_u32 m0, s59, 135168\n"
    "s_mov_b32 s74, 3244032\n"
    "buffer_load_dwordx4 v5, s[44:47], s74 offen lds\n"
    "s_add_u32 m0, s59, 139264\n"
    "s_mov_b32 s74, 3244160\n"
    "buffer_load_dwordx4 v5, s[44:47], s74 offen lds\n"
    "s_add_u32 m0, s59, 143360\n"
    "s_mov_b32 s74, 3244288\n"
    "buffer_load_dwordx4 v5, s[44:47], s74 offen lds\n"
    "ds_read_b64_tr_b16 a[144:145], v6 offset:0\n"
    "ds_read_b64_tr_b16 a[146:147], v6 offset:512\n"
    "ds_read_b64_tr_b16 a[148:149], v6 offset:8\n"
    "ds_read_b64_tr_b16 a[150:151], v6 offset:520\n"
    "ds_read_b64_tr_b16 a[152:153], v7 offset:0\n"
    "ds_read_b64_tr_b16 a[154:155], v7 offset:512\n"
    "ds_read_b64_tr_b16 a[156:157], v6 offset:12288\n"
    "ds_read_b64_tr_b16 a[158:159], v6 offset:12800\n"
    "ds_read_b64_tr_b16 a[160:161], v6 offset:12296\n"
    "ds_read_b64_tr_b16 a[162:163], v6 offset:12808\n"
    "ds_read_b64_tr_b16 a[164:165], v7 offset:12288\n"
    "ds_read_b64_tr_b16 a[166:167], v7 offset:12800\n"
    "s_waitcnt vmcnt(24) lgkmcnt(0)\n"
    "s_barrier\n"
    "s_add_u32 m0, s59, 0\n"
    "s_mov_b32 s74, 3538944\n"
    "buffer_load_dwordx4 v5, s[40:43], s74 offen lds\n"
    "s_add_u32 m0, s59, 4096\n"
    "s_mov_b32 s74, 3539072\n"
    "buffer_load_dwordx4 v5, s[40:43], s74 offen lds\n"
    "s_add_u32 m0, s59, 8192\n"
    "s_mov_b32 s74, 3539200\n"
    "buffer_load_dwordx4 v5, s[40:43], s74 offen lds\n"
    "s_add_u32 m0, s59, 12288\n"
    "s_mov_b32 s74, 3538944\n"
    "buffer_load_dwordx4 v5, s[44:47], s74 offen lds\n"
    "s_add_u32 m0, s59, 16384\n"
    "s_mov_b32 s74, 3539072\n"
    "buffer_load_dwordx4 v5, s[44:47], s74 offen lds\n"
    "s_add_u32 m0, s59, 20480\n"
    "s_mov_b32 s74, 3539200\n"
    "buffer_load_dwordx4 v5, s[44:47], s74 offen lds\n"
    "ds_read_b64_tr_b16 a[168:169], v6 offset:24576\n"
    "ds_read_b64_tr_b16 a[170:171], v6 offset:25088\n"
    "ds_read_b64_tr_b16 a[172:173], v6 offset:24584\n"
    "ds_read_b64_tr_b16 a[174:175], v6 offset:25096\n"
    "ds_read_b64_tr_b16 a[176:177], v7 offset:24576\n"
    "ds_read_b64_tr_b16 a[178:179], v7 offset:25088\n"
    "ds_read_b64_tr_b16 a[180:181], v6 offset:36864\n"
    "ds_read_b64_tr_b16 a[182:183], v6 offset:37376\n"
    "ds_read_b64_tr_b16 a[184:185], v6 offset:36872\n"
    "ds_read_b64_tr_b16 a[186:187], v6 offset:37384\n"
    "ds_read_b64_tr_b16 a[188:189], v7 offset:36864\n"
    "ds_read_b64_tr_b16 a[190:191], v7 offset:37376\n"
    "s_waitcnt vmcnt(24) lgkmcnt(0)\n"
    "s_barrier\n"
    "s_add_u32 m0, s59, 24576\n"
    "s_mov_b32 s74, 3833856\n"
    "buffer_load_dwordx4 v5, s[40:43], s74 offen lds\n"
    "s_add_u32 m0, s59, 28672\n"
    "s_mov_b32 s74, 3833984\n"
    "buffer_load_dwordx4 v5, s[40:43], s74 offen lds\n"
    "s_add_u32 m0, s59, 32768\n"
    "s_mov_b32 s74, 3834112\n"
    "buffer_load_dwordx4 v5, s[40:43], s74 offen lds\n"
    "s_add_u32 m0, s59, 36864\n"
    "s_mov_b32 s74, 3833856\n"
    "buffer_load_dwordx4 v5, s[44:47], s74 offen lds\n"
    "s_add_u32 m0, s59, 40960\n"
    "s_mov_b32 s74, 3833984\n"
    "buffer_load_dwordx4 v5, s[44:47], s74 offen lds\n"
    "s_add_u32 m0, s59, 45056\n"
    "s_mov_b32 s74, 3834112\n"
    "buffer_load_dwordx4 v5, s[44:47], s74 offen lds\n"
    "ds_read_b64_tr_b16 a[192:193], v16 offset:0\n"
    "ds_read_b64_tr_b16 a[194:195], v16 offset:512\n"
    "ds_read_b64_tr_b16 a[196:197], v16 offset:8\n"
    "ds_read_b64_tr_b16 a[198:199], v16 offset:520\n"
    "ds_read_b64_tr_b16 a[200:201], v18 offset:0\n"
    "ds_read_b64_tr_b16 a[202:203], v18 offset:512\n"
    "ds_read_b64_tr_b16 a[204:205], v16 offset:12288\n"
    "ds_read_b64_tr_b16 a[206:207], v16 offset:12800\n"
    "ds_read_b64_tr_b16 a[208:209], v16 offset:12296\n"
    "ds_read_b64_tr_b16 a[210:211], v16 offset:12808\n"
    "ds_read_b64_tr_b16 a[212:213], v18 offset:12288\n"
    "ds_read_b64_tr_b16 a[214:215], v18 offset:12800\n"
    "s_waitcnt vmcnt(24) lgkmcnt(0)\n"
    "s_barrier\n"
    "s_add_u32 m0, s59, 49152\n"
    "s_mov_b32 s74, 4128768\n"
    "buffer_load_dwordx4 v5, s[40:43], s74 offen lds\n"
    "s_add_u32 m0, s59, 53248\n"
    "s_mov_b32 s74, 4128896\n"
    "buffer_load_dwordx4 v5, s[40:43], s74 offen lds\n"
    "s_add_u32 m0, s59, 57344\n"
    "s_mov_b32 s74, 4129024\n"
    "buffer_load_dwordx4 v5, s[40:43], s74 offen lds\n"
    "s_add_u32 m0, s59, 61440\n"
    "s_mov_b32 s74, 4128768\n"
    "buffer_load_dwordx4 v5, s[44:47], s74 offen lds\n"
    "s_add_u32 m0, s59, 65536\n"
    "s_mov_b32 s74, 4128896\n"
    "buffer_load_dwordx4 v5, s[44:47], s74 offen lds\n"
    "s_add_u32 m0, s59, 69632\n"
    "s_mov_b32 s74, 4129024\n"
    "buffer_load_dwordx4 v5, s[44:47], s74 offen lds\n"
    "ds_read_b64_tr_b16 a[216:217], v16 offset:24576\n"
    "ds_read_b64_tr_b16 a[218:219], v16 offset:25088\n"
    "ds_read_b64_tr_b16 a[220:221], v16 offset:24584\n"
    "ds_read_b64_tr_b16 a[222:223], v16 offset:25096\n"
    "ds_read_b64_tr_b16 a[224:225], v18 offset:24576\n"
    "ds_read_b64_tr_b16 a[226:227], v18 offset:25088\n"
    "ds_read_b64_tr_b16 a[228:229], v16 offset:36864\n"
    "ds_read_b64_tr_b16 a[230:231], v16 offset:37376\n"
    "ds_read_b64_tr_b16 a[232:233], v16 offset:36872\n"
    "ds_read_b64_tr_b16 a[234:235], v16 offset:37384\n"
    "ds_read_b64_tr_b16 a[236:237], v18 offset:36864\n"
    "ds_read_b64_tr_b16 a[238:239], v18 offset:37376\n"
    "s_waitcnt vmcnt(24) lgkmcnt(0)\n"
    "s_barrier\n"
    "s_add_u32 m0, s59, 73728\n"
    "s_mov_b32 s74, 4423680\n"
    "buffer_load_dwordx4 v5, s[40:43], s74 offen lds\n"
    "s_add_u32 m0, s59, 77824\n"
    "s_mov_b32 s74, 4423808\n"
    "buffer_load_dwordx4 v5, s[40:43], s74 offen lds\n"
    "s_add_u32 m0, s59, 81920\n"
    "s_mov_b32 s74, 4423936\n"
    "buffer_load_dwordx4 v5, s[40:43], s74 offen lds\n"
    "s_add_u32 m0, s59, 86016\n"
    "s_mov_b32 s74, 4423680\n"
    "buffer_load_dwordx4 v5, s[44:47], s74 offen lds\n"
    "s_add_u32 m0, s59, 90112\n"
    "s_mov_b32 s74, 4423808\n"
    "buffer_load_dwordx4 v5, s[44:47], s74 offen lds\n"
    "s_add_u32 m0, s59, 94208\n"
    "s_mov_b32 s74, 4423936\n"
    "buffer_load_dwordx4 v5, s[44:47], s74 offen lds\n"
    "ds_read_b64_tr_b16 a[240:241], v17 offset:0\n"
    "ds_read_b64_tr_b16 a[242:243], v17 offset:512\n"
    "ds_read_b64_tr_b16 a[244:245], v17 offset:8\n"
    "ds_read_b64_tr_b16 a[246:247], v17 offset:520\n"
    "ds_read_b64_tr_b16 a[248:249], v19 offset:0\n"
    "ds_read_b64_tr_b16 a[250:251], v19 offset:512\n"
    "ds_read_b64_tr_b16 a[252:253], v17 offset:12288\n"
    "ds_read_b64_tr_b16 a[254:255], v17 offset:12800\n"
    "ds_read_b64_tr_b16 v[80:81], v17 offset:12296\n"
    "ds_read_b64_tr_b16 v[82:83], v17 offset:12808\n"
    "ds_read_b64_tr_b16 v[84:85], v19 offset:12288\n"
    "ds_read_b64_tr_b16 v[86:87], v19 offset:12800\n"
    "s_waitcnt vmcnt(24) lgkmcnt(0)\n"
    "s_barrier\n"
    "s_add_u32 m0, s59, 98304\n"
    "s_mov_b32 s74, 4718592\n"
    "buffer_load_dwordx4 v5, s[40:43], s74 offen lds\n"
    "s_add_u32 m0, s59, 102400\n"
    "s_mov_b32 s74, 4718720\n"
    "buffer_load_dwordx4 v5, s[40:43], s74 offen lds\n"
    "s_add_u32 m0, s59, 106496\n"
    "s_mov_b32 s74, 4718848\n"
    "buffer_load_dwordx4 v5, s[40:43], s74 offen lds\n"
    "s_add_u32 m0, s59, 110592\n"
    "s_mov_b32 s74, 4718592\n"
    "buffer_load_dwordx4 v5, s[44:47], s74 offen lds\n"
    "s_add_u32 m0, s59, 114688\n"
    "s_mov_b32 s74, 4718720\n"
    "buffer_load_dwordx4 v5, s[44:47], s74 offen lds\n"
    "s_add_u32 m0, s59, 118784\n"
    "s_mov_b32 s74, 4718848\n"
    "buffer_load_dwordx4 v5, s[44:47], s74 offen lds\n"
    "ds_read_b64_tr_b16 v[88:89], v17 offset:24576\n"
    "ds_read_b64_tr_b16 v[90:91], v17 offset:25088\n"
    "ds_read_b64_tr_b16 v[92:93], v17 offset:24584\n"
    "ds_read_b64_tr_b16 v[94:95], v17 offset:25096\n"
    "ds_read_b64_tr_b16 v[96:97], v19 offset:24576\n"
    "ds_read_b64_tr_b16 v[98:99], v19 offset:25088\n"
    "ds_read_b64_tr_b16 v[100:101], v17 offset:36864\n"
    "ds_read_b64_tr_b16 v[102:103], v17 offset:37376\n"
    "ds_read_b64_tr_b16 v[104:105], v17 offset:36872\n"
    "ds_read_b64_tr_b16 v[106:107], v17 offset:37384\n"
    "ds_read_b64_tr_b16 v[108:109], v19 offset:36864\n"
    "ds_read_b64_tr_b16 v[110:111], v19 offset:37376\n"
    "s_waitcnt vmcnt(24) lgkmcnt(0)\n"
    "s_barrier\n"
    "s_add_u32 m0, s59, 122880\n"
    "s_mov_b32 s74, 5013504\n"
    "buffer_load_dwordx4 v5, s[40:43], s74 offen lds\n"
    "s_add_u32 m0, s59, 126976\n"
    "s_mov_b32 s74, 5013632\n"
    "buffer_load_dwordx4 v5, s[40:43], s74 offen lds\n"
    "s_add_u32 m0, s59, 131072\n"
    "s_mov_b32 s74, 5013760\n"
    "buffer_load_dwordx4 v5, s[40:43], s74 offen lds\n"
    "s_add_u32 m0, s59, 135168\n"
    "s_mov_b32 s74, 5013504\n"
    "buffer_load_dwordx4 v5, s[44:47], s74 offen lds\n"
    "s_add_u32 m0, s59, 139264\n"
    "s_mov_b32 s74, 5013632\n"
    "buffer_load_dwordx4 v5, s[44:47], s74 offen lds\n"
    "s_add_u32 m0, s59, 143360\n"
    "s_mov_b32 s74, 5013760\n"
    "buffer_load_dwordx4 v5, s[44:47], s74 offen lds\n"
    "buffer_load_dwordx2 v[12:13], off, s[36:39], 0 sc1\n"
    "ds_read_b64_tr_b16 v[112:113], v6 offset:0\n"
    "ds_read_b64_tr_b16 v[114:115], v6 offset:512\n"
    "ds_read_b64_tr_b16 v[116:117], v6 offset:8\n"
    "ds_read_b64_tr_b16 v[118:119], v6 offset:520\n"
    "ds_read_b64_tr_b16 v[120:121], v7 offset:0\n"
    "ds_read_b64_tr_b16 v[122:123], v7 offset:512\n"
    "ds_read_b64_tr_b16 v[124:125], v6 offset:12288\n"
    "ds_read_b64_tr_b16 v[126:127], v6 offset:12800\n"
    "ds_read_b64_tr_b16 v[128:129], v6 offset:12296\n"
    "ds_read_b64_tr_b16 v[130:131], v6 offset:12808\n"
    "ds_read_b64_tr_b16 v[132:133], v7 offset:12288\n"
    "ds_read_b64_tr_b16 v[134:135], v7 offset:12800\n"
    "s_waitcnt vmcnt(25) lgkmcnt(0)\n"
    "s_barrier\n"
    "ds_read_b64_tr_b16 v[136:137], v6 offset:24576\n"
    "ds_read_b64_tr_b16 v[138:139], v6 offset:25088\n"
    "ds_read_b64_tr_b16 v[140:141], v6 offset:24584\n"
    "ds_read_b64_tr_b16 v[142:143], v6 offset:25096\n"
    "ds_read_b64_tr_b16 v[144:145], v7 offset:24576\n"
    "ds_read_b64_tr_b16 v[146:147], v7 offset:25088\n"
    "ds_read_b64_tr_b16 v[148:149], v6 offset:36864\n"
    "ds_read_b64_tr_b16 v[150:151], v6 offset:37376\n"
    "ds_read_b64_tr_b16 v[152:153], v6 offset:36872\n"
    "ds_read_b64_tr_b16 v[154:155], v6 offset:37384\n"
    "ds_read_b64_tr_b16 v[156:157], v7 offset:36864\n"
    "ds_read_b64_tr_b16 v[158:159], v7 offset:37376\n"
    "s_waitcnt vmcnt(19) lgkmcnt(0)\n"
    "s_barrier\n"
    "ds_read_b64_tr_b16 v[160:161], v16 offset:0\n"
    "ds_read_b64_tr_b16 v[162:163], v16 offset:512\n"
    "ds_read_b64_tr_b16 v[164:165], v16 offset:8\n"
    "ds_read_b64_tr_b16 v[166:167], v16 offset:520\n"
    "ds_read_b64_tr_b16 v[168:169], v18 offset:0\n"
    "ds_read_b64_tr_b16 v[170:171], v18 offset:512\n"
    "ds_read_b64_tr_b16 v[172:173], v16 offset:12288\n"
    "ds_read_b64_tr_b16 v[174:175], v16 offset:12800\n"
    "ds_read_b64_tr_b16 v[176:177], v16 offset:12296\n"
    "ds_read_b64_tr_b16 v[178:179], v16 offset:12808\n"
    "ds_read_b64_tr_b16 v[180:181], v18 offset:12288\n"
    "ds_read_b64_tr_b16 v[182:183], v18 offset:12800\n"
    "s_waitcnt vmcnt(13) lgkmcnt(0)\n"
    "s_barrier\n"
    "ds_read_b64_tr_b16 v[184:185], v16 offset:24576\n"
    "ds_read_b64_tr_b16 v[186:187], v16 offset:25088\n"
    "ds_read_b64_tr_b16 v[188:189], v16 offset:24584\n"
    "ds_read_b64_tr_b16 v[190:191], v16 offset:25096\n"
    "ds_read_b64_tr_b16 v[192:193], v18 offset:24576\n"
    "ds_read_b64_tr_b16 v[194:195], v18 offset:25088\n"
    "ds_read_b64_tr_b16 v[196:197], v16 offset:36864\n"
    "ds_read_b64_tr_b16 v[198:199], v16 offset:37376\n"
    "ds_read_b64_tr_b16 v[200:201], v16 offset:36872\n"
    "ds_read_b64_tr_b16 v[202:203], v16 offset:37384\n"
    "ds_read_b64_tr_b16 v[204:205], v18 offset:36864\n"
    "ds_read_b64_tr_b16 v[206:207], v18 offset:37376\n"
    "s_waitcnt vmcnt(7) lgkmcnt(0)\n"
    "s_barrier\n"
    "ds_read_b64_tr_b16 v[208:209], v17 offset:0\n"
    "ds_read_b64_tr_b16 v[210:211], v17 offset:512\n"
    "ds_read_b64_tr_b16 v[212:213], v17 offset:8\n"
    "ds_read_b64_tr_b16 v[214:215], v17 offset:520\n"
    "ds_read_b64_tr_b16 v[216:217], v19 offset:0\n"
    "ds_read_b64_tr_b16 v[218:219], v19 offset:512\n"
    "ds_read_b64_tr_b16 v[220:221], v17 offset:12288\n"
    "ds_read_b64_tr_b16 v[222:223], v17 offset:12800\n"
    "ds_read_b64_tr_b16 v[224:225], v17 offset:12296\n"
    "ds_read_b64_tr_b16 v[226:227], v17 offset:12808\n"
    "ds_read_b64_tr_b16 v[228:229], v19 offset:12288\n"
    "ds_read_b64_tr_b16 v[230:231], v19 offset:12800\n"
    "s_waitcnt vmcnt(0) lgkmcnt(0)\n"
    "s_barrier\n"
    ".LgUP_par_%=:\n"
    "v_readfirstlane_b32 s74, v12\n"
    "v_readfirstlane_b32 s75, v13\n"
    "s_xor_b32 s74, s74, s66\n"
    "s_xor_b32 s75, s75, s67\n"
    "s_cselect_b32 s75, 2, 0\n"
    "s_or_b32 s75, s75, s74\n"
    "s_cmp_lt_u32 s75, 2\n"
    "s_cbranch_scc1 .LgUP_par1_%=\n"
    "s_sleep 4\n"
    "buffer_load_dwordx2 v[12:13], off, s[36:39], 0 sc1\n"
    "s_waitcnt vmcnt(0)\n"
    "s_sub_u32 s72, s72, 1\n"
    "s_cbranch_scc0 .LgUP_par_%=\n"
    "s_mov_b32 s72, 0\n"
    ".LgUP_par1_%=:\n"
    "s_and_b32 s74, s74, 1\n"
    "s_lshl_b32 s74, s74, 13\n"
    "s_add_u32 s36, s36, s74\n"
    "s_addc_u32 s37, s37, 0\n"
    "v_add_u32 v13, v11, v3\n"
    "v_cmp_gt_i32 vcc, s71, v13\n"
    "s_nop 0\n"
    "s_or_b64 exec, vcc, s[62:63]\n"
    "buffer_atomic_smax v11, v2, s[36:39], 0 offen sc0 sc1\n"
    "s_mov_b64 exec, -1\n"
    "s_getpc_b64 s[84:85]\n"
    ".LgUP_PC2_%=:\n"
    "s_and_b32 s85, s85, 0xffff\n"
    "s_mov_b32 s86, .LgUP_FIN_%=-.LgUP_PC2_%=\n"
    "s_mov_b32 s87, 0x20000\n"
    "v_mbcnt_lo_u32_b32 v42, -1, 0\n"
    "v_mbcnt_hi_u32_b32 v42, -1, v42\n"
    "v_lshlrev_b32 v42, 7, v42\n"
    "buffer_load_dword v43, v42, s[84:87], 0 offen\n"
    "v_add_u32 v42, 8192, v42\n"
    "buffer_load_dword v43, v42, s[84:87], 0 offen\n"
    "v_add_u32 v42, 8192, v42\n"
    "buffer_load_dword v43, v42, s[84:87], 0 offen\n"
    "ds_read_b64_tr_b16 v[232:233], v17 offset:24576\n"
    "ds_read_b64_tr_b16 v[234:235], v17 offset:25088\n"
    "ds_read_b64_tr_b16 v[236:237], v17 offset:24584\n"
    "ds_read_b64_tr_b16 v[238:239], v17 offset:25096\n"
    "ds_read_b64_tr_b16 v[240:241], v19 offset:24576\n"
    "ds_read_b64_tr_b16 v[242:243], v19 offset:25088\n"
    "ds_read_b64_tr_b16 v[244:245], v17 offset:36864\n"
    "ds_read_b64_tr_b16 v[246:247], v17 offset:37376\n"
    "ds_read_b64_tr_b16 v[248:249], v17 offset:36872\n"
    "ds_read_b64_tr_b16 v[250:251], v17 offset:37384\n"
    "ds_read_b64_tr_b16 v[252:253], v19 offset:36864\n"
    "ds_read_b64_tr_b16 v[254:255], v19 offset:37376\n"
    "s_waitcnt lgkmcnt(0)\n"
    "s_barrier\n"
    "s_waitcnt vmcnt(0)\n"
    "v_mov_b32 v5, v11\n"
    "v_mov_b32 v12, -1\n"
    "s_mov_b32 s73, 0x80000000\n"
    "s_branch .LgUP_block1_1_%=\n"
    ".LgUP_LOOPB_%=:\n"
    ".LgUP_head0_%=:\n"
    "v_mfma_f32_16x16x32_bf16 v[32:35], a[0:3], v[16:19], 0\n"
    "v_mfma_f32_16x16x32_bf16 v[36:39], a[4:7], v[16:19], 0\n"
    "v_mul_f32 v64, v40, v64\n"
    "v_mul_f32 v65, v41, v65\n"
    "v_mfma_f32_16x16x32_bf16 v[48:51], a[8:11], v[16:19], 0\n"
    "v_mul_f32 v66, v42, v66\n"
    "v_mul_f32 v67, v43, v67\n"
    "v_mfma_f32_16x16x32_bf16 v[56:59], a[12:15], v[16:19], 0\n"
    "v_mul_f32 v68, v44, v68\n"
    "v_mul_f32 v69, v45, v69\n"
    "v_mfma_f32_16x16x32_bf16 v[60:63], a[16:19], v[16:19], 0\n"
    "s_waitcnt lgkmcnt(1)\n"
    "v_mul_f32 v70, v46, v70\n"
    "v_mul_f32 v71, v47, v71\n"
    "v_mfma_f32_16x16x32_bf16 v[72:75], a[20:23], v[16:19], 0\n"
    "ds_read_b128 v[16:19], v0 offset:3072\n"
    "v_mul_f32 v40, 0xbfb8aa3b, v40\n"
    "v_mfma_f32_16x16x32_bf16 v[32:35], a[24:27], v[20:23], v[32:35]\n"
    "v_exp_f32 v40, v40\n"
    "v_mfma_f32_16x16x32_bf16 v[36:39], a[28:31], v[20:23], v[36:39]\n"
    "s_waitcnt vmcnt(1)\n"
    "v_mul_f32 v41, 0xbfb8aa3b, v41\n"
    "v_add_u32 v13, v11, v3\n"
    "v_mfma_f32_16x16x32_bf16 v[48:51], a[32:35], v[20:23], v[48:51]\n"
    "v_mul_f32 v42, 0xbfb8aa3b, v42\n"
    "v_cmp_gt_i32 vcc, s71, v13\n"
    "v_mfma_f32_16x16x32_bf16 v[56:59], a[36:39], v[20:23], v[56:59]\n"
    "buffer_atomic_smax v7, v10, s[36:39], s69 offen sc1\n"
    "v_exp_f32 v41, v41\n"
    "v_mfma_f32_16x16x32_bf16 v[60:63], a[40:43], v[20:23], v[60:63]\n"
    "s_waitcnt lgkmcnt(1)\n"
    "v_exp_f32 v42, v42\n"
    "v_mfma_f32_16x16x32_bf16 v[72:75], a[44:47], v[20:23], v[72:75]\n"
    "ds_read_b128 v[20:23], v0 offset:4096\n"
    "s_cbranch_vccnz .LgUP_poll0_%=\n"
    "v_mfma_f32_16x16x32_bf16 v[32:35], a[48:51], v[24:27], v[32:35]\n"
    "s_add_u32 m0, s59, 20480\n"
    "v_mfma_f32_16x16x32_bf16 v[36:39], a[52:55], v[24:27], v[36:39]\n"
    "buffer_load_dwordx4 v1, s[36:39], s76 offen sc1 lds\n"
    "v_mul_f32 v43, 0xbfb8aa3b, v43\n"
    "v_mul_f32 v44, 0xbfb8aa3b, v44\n"
    "v_mfma_f32_16x16x32_bf16 v[48:51], a[56:59], v[24:27], v[48:51]\n"
    "v_exp_f32 v43, v43\n"
    "v_mfma_f32_16x16x32_bf16 v[56:59], a[60:63], v[24:27], v[56:59]\n"
    "v_exp_f32 v44, v44\n"
    "v_mfma_f32_16x16x32_bf16 v[60:63], a[64:67], v[24:27], v[60:63]\n"
    "s_waitcnt lgkmcnt(1)\n"
    "v_mul_f32 v45, 0xbfb8aa3b, v45\n"
    "v_mul_f32 v46, 0xbfb8aa3b, v46\n"
    "v_mfma_f32_16x16x32_bf16 v[72:75], a[68:71], v[24:27], v[72:75]\n"
    "ds_read_b128 v[24:27], v0 offset:5120\n"
    "v_mul_f32 v47, 0xbfb8aa3b, v47\n"
    "v_mfma_f32_16x16x32_bf16 v[32:35], a[72:75], v[16:19], v[32:35]\n"
    "v_exp_f32 v45, v45\n"
    "v_mfma_f32_16x16x32_bf16 v[36:39], a[76:79], v[16:19], v[36:39]\n"
    "v_exp_f32 v46, v46\n"
    "v_mfma_f32_16x16x32_bf16 v[48:51], a[80:83], v[16:19], v[48:51]\n"
    "s_add_u32 s76, s76, 16384\n"
    "v_exp_f32 v47, v47\n"
    "v_mfma_f32_16x16x32_bf16 v[56:59], a[84:87], v[16:19], v[56:59]\n"
    "s_add_u32 m0, s59, 24576\n"
    "v_add_f32 v40, 1.0, v40\n"
    "v_add_f32 v41, 1.0, v41\n"
    "v_mfma_f32_16x16x32_bf16 v[60:63], a[88:91], v[16:19], v[60:63]\n"
    "s_waitcnt lgkmcnt(1)\n"
    "v_rcp_f32 v40, v40\n"
    "v_mfma_f32_16x16x32_bf16 v[72:75], a[92:95], v[16:19], v[72:75]\n"
    "ds_read_b128 v[16:19], v0 offset:6144\n"
    "v_add_f32 v42, 1.0, v42\n"
    "v_mfma_f32_16x16x32_bf16 v[32:35], a[96:99], v[20:23], v[32:35]\n"
    "buffer_load_dwordx4 v1, s[36:39], s76 offen sc1 lds\n"
    "v_rcp_f32 v41, v41\n"
    "v_mfma_f32_16x16x32_bf16 v[36:39], a[100:103], v[20:23], v[36:39]\n"
    "v_add_f32 v43, 1.0, v43\n"
    "v_mfma_f32_16x16x32_bf16 v[48:51], a[104:107], v[20:23], v[48:51]\n"
    "v_add_f32 v44, 1.0, v44\n"
    "v_mfma_f32_16x16x32_bf16 v[56:59], a[108:111], v[20:23], v[56:59]\n"
    "v_rcp_f32 v42, v42\n"
    "v_mfma_f32_16x16x32_bf16 v[60:63], a[112:115], v[20:23], v[60:63]\n"
    "s_waitcnt lgkmcnt(1)\n"
    "v_rcp_f32 v43, v43\n"
    "v_mfma_f32_16x16x32_bf16 v[72:75], a[116:119], v[20:23], v[72:75]\n"
    "ds_read_b128 v[20:23], v0 offset:7168\n"
    "v_mfma_f32_16x16x32_bf16 v[32:35], a[120:123], v[24:27], v[32:35]\n"
    "s_add_u32 s76, s76, 16384\n"
    "s_add_u32 m0, s59, 28672\n"
    "v_mfma_f32_16x16x32_bf16 v[36:39], a[124:127], v[24:27], v[36:39]\n"
    "buffer_load_dwordx4 v1, s[36:39], s76 offen sc1 lds\n"
    "v_rcp_f32 v44, v44\n"
    "v_mfma_f32_16x16x32_bf16 v[48:51], a[128:131], v[24:27], v[48:51]\n"
    "v_add_f32 v45, 1.0, v45\n"
    "v_add_f32 v46, 1.0, v46\n"
    "v_mfma_f32_16x16x32_bf16 v[56:59], a[132:135], v[24:27], v[56:59]\n"
    "v_rcp_f32 v45, v45\n"
    "v_mfma_f32_16x16x32_bf16 v[60:63], a[136:139], v[24:27], v[60:63]\n"
    "s_waitcnt lgkmcnt(1)\n"
    "v_rcp_f32 v46, v46\n"
    "v_mfma_f32_16x16x32_bf16 v[72:75], a[140:143], v[24:27], v[72:75]\n"
    "ds_read_b128 v[24:27], v0 offset:8192\n"
    "v_add_f32 v47, 1.0, v47\n"
    "v_mfma_f32_16x16x32_bf16 v[32:35], a[144:147], v[16:19], v[32:35]\n"
    "v_rcp_f32 v47, v47\n"
    "v_mfma_f32_16x16x32_bf16 v[36:39], a[148:151], v[16:19], v[36:39]\n"
    "v_mul_f32 v40, v40, v64\n"
    "v_mul_f32 v41, v41, v65\n"
    "v_mfma_f32_16x16x32_bf16 v[48:51], a[152:155], v[16:19], v[48:51]\n"
    "v_mul_f32 v42, v42, v66\n"
    "v_mul_f32 v43, v43, v67\n"
    "v_mfma_f32_16x16x32_bf16 v[56:59], a[156:159], v[16:19], v[56:59]\n"
    "v_mul_f32 v44, v44, v68\n"
    "v_mul_f32 v45, v45, v69\n"
    "v_mfma_f32_16x16x32_bf16 v[60:63], a[160:163], v[16:19], v[60:63]\n"
    "s_waitcnt lgkmcnt(1)\n"
    "v_mul_f32 v46, v46, v70\n"
    "v_mul_f32 v47, v47, v71\n"
    "v_mfma_f32_16x16x32_bf16 v[72:75], a[164:167], v[16:19], v[72:75]\n"
    "ds_read_b128 v[16:19], v0 offset:9216\n"
    "v_cvt_pk_bf16_f32 v40, v40, v41\n"
    "v_mfma_f32_16x16x32_bf16 v[32:35], a[168:171], v[20:23], v[32:35]\n"
    "s_add_u32 s76, s76, 16384\n"
    "v_cvt_pk_bf16_f32 v41, v42, v43\n"
    "v_cvt_pk_bf16_f32 v42, v44, v45\n"
    "v_mfma_f32_16x16x32_bf16 v[36:39], a[172:175], v[20:23], v[36:39]\n"
    "s_add_u32 m0, s59, 32768\n"
    "v_cvt_pk_bf16_f32 v43, v46, v47\n"
    "v_mfma_f32_16x16x32_bf16 v[48:51], a[176:179], v[20:23], v[48:51]\n"
    "buffer_load_dwordx4 v1, s[36:39], s76 offen sc1 lds\n"
    "v_mul_f32 v76, v52, v76\n"
    "v_mfma_f32_16x16x32_bf16 v[56:59], a[180:183], v[20:23], v[56:59]\n"
    "buffer_store_dwordx4 v[40:43], v15, s[36:39], s73 offen offset:1024 sc1\n"
    "v_mfma_f32_16x16x32_bf16 v[60:63], a[184:187], v[20:23], v[60:63]\n"
    "s_waitcnt lgkmcnt(1)\n"
    "v_mul_f32 v77, v53, v77\n"
    "v_mul_f32 v78, v54, v78\n"
    "v_mfma_f32_16x16x32_bf16 v[72:75], a[188:191], v[20:23], v[72:75]\n"
    "ds_read_b128 v[20:23], v0 offset:10240\n"
    "v_mfma_f32_16x16x32_bf16 v[32:35], a[192:195], v[24:27], v[32:35]\n"
    "v_mfma_f32_16x16x32_bf16 v[36:39], a[196:199], v[24:27], v[36:39]\n"
    "v_mul_f32 v79, v55, v79\n"
    "v_mul_f32 v52, 0xbfb8aa3b, v52\n"
    "v_mfma_f32_16x16x32_bf16 v[48:51], a[200:203], v[24:27], v[48:51]\n"
    "s_add_u32 s76, s76, 16384\n"
    "v_exp_f32 v52, v52\n"
    "v_mfma_f32_16x16x32_bf16 v[56:59], a[204:207], v[24:27], v[56:59]\n"
    "s_add_u32 m0, s59, 36864\n"
    "v_mul_f32 v53, 0xbfb8aa3b, v53\n"
    "v_mul_f32 v54, 0xbfb8aa3b, v54\n"
    "v_mfma_f32_16x16x32_bf16 v[60:63], a[208:211], v[24:27], v[60:63]\n"
    "s_waitcnt lgkmcnt(1)\n"
    "v_exp_f32 v53, v53\n"
    "v_mfma_f32_16x16x32_bf16 v[72:75], a[212:215], v[24:27], v[72:75]\n"
    "ds_read_b128 v[24:27], v0 offset:11264\n"
    "v_mul_f32 v55, 0xbfb8aa3b, v55\n"
    "v_mfma_f32_16x16x32_bf16 v[32:35], a[216:219], v[16:19], v[32:35]\n"
    "buffer_load_dwordx4 v8, s[36:39], s76 offen sc1 lds\n"
    "v_exp_f32 v54, v54\n"
    "v_mfma_f32_16x16x32_bf16 v[36:39], a[220:223], v[16:19], v[36:39]\n"
    "v_exp_f32 v55, v55\n"
    "v_mfma_f32_16x16x32_bf16 v[48:51], a[224:227], v[16:19], v[48:51]\n"
    "v_add_f32 v52, 1.0, v52\n"
    "v_add_f32 v53, 1.0, v53\n"
    "v_mfma_f32_16x16x32_bf16 v[56:59], a[228:231], v[16:19], v[56:59]\n"
    "v_rcp_f32 v52, v52\n"
    "v_mfma_f32_16x16x32_bf16 v[60:63], a[232:235], v[16:19], v[60:63]\n"
    "s_waitcnt lgkmcnt(1)\n"
    "v_rcp_f32 v53, v53\n"
    "v_mfma_f32_16x16x32_bf16 v[72:75], a[236:239], v[16:19], v[72:75]\n"
    "ds_read_b128 v[16:19], v0 offset:12288\n"
    "v_add_f32 v54, 1.0, v54\n"
    "v_mfma_f32_16x16x32_bf16 v[32:35], a[240:243], v[20:23], v[32:35]\n"
    "v_rcp_f32 v54, v54\n"
    "v_mfma_f32_16x16x32_bf16 v[36:39], a[244:247], v[20:23], v[36:39]\n"
    "v_add_f32 v55, 1.0, v55\n"
    "v_mfma_f32_16x16x32_bf16 v[48:51], a[248:251], v[20:23], v[48:51]\n"
    "v_mul_f32 v52, v52, v76\n"
    "v_mfma_f32_16x16x32_bf16 v[56:59], a[252:255], v[20:23], v[56:59]\n"
    "v_rcp_f32 v55, v55\n"
    "v_mfma_f32_16x16x32_bf16 v[60:63], v[80:83], v[20:23], v[60:63]\n"
    "s_waitcnt lgkmcnt(1)\n"
    "v_mul_f32 v53, v53, v77\n"
    "v_mul_f32 v54, v54, v78\n"
    "v_mfma_f32_16x16x32_bf16 v[72:75], v[84:87], v[20:23], v[72:75]\n"
    "ds_read_b128 v[20:23], v0 offset:13312\n"
    "v_mfma_f32_16x16x32_bf16 v[32:35], v[88:91], v[24:27], v[32:35]\n"
    "v_mfma_f32_16x16x32_bf16 v[36:39], v[92:95], v[24:27], v[36:39]\n"
    "v_mul_f32 v55, v55, v79\n"
    "v_cvt_pk_bf16_f32 v52, v52, v53\n"
    "v_mfma_f32_16x16x32_bf16 v[48:51], v[96:99], v[24:27], v[48:51]\n"
    "v_cvt_pk_bf16_f32 v53, v54, v55\n"
    "s_nop 0\n"
    "v_mfma_f32_16x16x32_bf16 v[56:59], v[100:103], v[24:27], v[56:59]\n"
    "v_permlane16_swap_b32 v52, v54\n"
    "v_mfma_f32_16x16x32_bf16 v[60:63], v[104:107], v[24:27], v[60:63]\n"
    "s_waitcnt lgkmcnt(1)\n"
    "v_permlane16_swap_b32 v53, v55\n"
    "v_mfma_f32_16x16x32_bf16 v[72:75], v[108:111], v[24:27], v[72:75]\n"
    "ds_read_b128 v[24:27], v0 offset:14336\n"
    ".LgUP_join0_%=:\n"
    "v_mfma_f32_16x16x32_bf16 v[32:35], v[112:115], v[16:19], v[32:35]\n"
    "buffer_store_dwordx4 v[52:55], v4, s[36:39], s73 offen offset:1024 sc1\n"
    "v_mfma_f32_16x16x32_bf16 v[36:39], v[116:119], v[16:19], v[36:39]\n"
    "s_lshr_b32 s73, s68, 1\n"
    "v_mfma_f32_16x16x32_bf16 v[48:51], v[120:123], v[16:19], v[48:51]\n"
    "s_and_b32 s73, s73, s57\n"
    "v_mfma_f32_16x16x32_bf16 v[56:59], v[124:127], v[16:19], v[56:59]\n"
    "s_mul_i32 s73, s73, s56\n"
    "v_mfma_f32_16x16x32_bf16 v[60:63], v[128:131], v[16:19], v[60:63]\n"
    "s_waitcnt lgkmcnt(1)\n"
    "s_add_u32 s73, s73, s55\n"
    "v_mfma_f32_16x16x32_bf16 v[72:75], v[132:135], v[16:19], v[72:75]\n"
    "ds_read_b128 v[16:19], v0 offset:15360\n"
    "v_mfma_f32_16x16x32_bf16 v[32:35], v[136:139], v[20:23], v[32:35]\n"
    "s_add_u32 s71, s68, 2\n"
    "s_waitcnt vmcnt(6)\n"
    "v_mfma_f32_16x16x32_bf16 v[36:39], v[140:143], v[20:23], v[36:39]\n"
    "v_add_u32 v13, v11, v3\n"
    "v_mfma_f32_16x16x32_bf16 v[48:51], v[144:147], v[20:23], v[48:51]\n"
    "v_cmp_gt_i32 vcc, s71, v13\n"
    "v_mfma_f32_16x16x32_bf16 v[56:59], v[148:151], v[20:23], v[56:59]\n"
    "v_mfma_f32_16x16x32_bf16 v[60:63], v[152:155], v[20:23], v[60:63]\n"
    "s_waitcnt lgkmcnt(1)\n"
    "v_mfma_f32_16x16x32_bf16 v[72:75], v[156:159], v[20:23], v[72:75]\n"
    "ds_read_b128 v[20:23], v0 offset:16384\n"
    "v_mfma_f32_16x16x32_bf16 v[32:35], v[160:163], v[24:27], v[32:35]\n"
    "s_or_b64 exec, vcc, s[62:63]\n"
    "buffer_atomic_smax v11, v2, s[36:39], 0 offen sc0 sc1\n"
    "s_mov_b64 exec, -1\n"
    "v_mfma_f32_16x16x32_bf16 v[36:39], v[164:167], v[24:27], v[36:39]\n"
    "s_lshr_b32 s76, s71, 1\n"
    "v_mfma_f32_16x16x32_bf16 v[48:51], v[168:171], v[24:27], v[48:51]\n"
    "s_and_b32 s76, s76, s54\n"
    "v_mfma_f32_16x16x32_bf16 v[56:59], v[172:175], v[24:27], v[56:59]\n"
    "s_mul_i32 s76, s76, s53\n"
    "v_mfma_f32_16x16x32_bf16 v[60:63], v[176:179], v[24:27], v[60:63]\n"
    "s_waitcnt lgkmcnt(1)\n"
    "s_add_u32 s76, s76, s52\n"
    "v_mfma_f32_16x16x32_bf16 v[72:75], v[180:183], v[24:27], v[72:75]\n"
    "ds_read_b128 v[24:27], v0 offset:17408\n"
    "v_mfma_f32_16x16x32_bf16 v[32:35], v[184:187], v[16:19], v[32:35]\n"
    "v_mfma_f32_16x16x32_bf16 v[36:39], v[188:191], v[16:19], v[36:39]\n"
    "v_mfma_f32_16x16x32_bf16 v[48:51], v[192:195], v[16:19], v[48:51]\n"
    "v_mfma_f32_16x16x32_bf16 v[56:59], v[196:199], v[16:19], v[56:59]\n"
    "s_waitcnt vmcnt(2) lgkmcnt(0)\n"
    "s_barrier\n"
    "v_mfma_f32_16x16x32_bf16 v[60:63], v[200:203], v[16:19], v[60:63]\n"
    "v_mfma_f32_16x16x32_bf16 v[72:75], v[204:207], v[16:19], v[72:75]\n"
    "ds_read_b128 v[16:19], v9 offset:0\n"
    "v_mfma_f32_16x16x32_bf16 v[32:35], v[208:211], v[20:23], v[32:35]\n"
    "v_mfma_f32_16x16x32_bf16 v[36:39], v[212:215], v[20:23], v[36:39]\n"
    "v_add_u32 v13, v5, v3\n"
    "v_mfma_f32_16x16x32_bf16 v[48:51], v[216:219], v[20:23], v[48:51]\n"
    "v_cmp_gt_i32 vcc, s71, v13\n"
    "v_mfma_f32_16x16x32_bf16 v[56:59], v[220:223], v[20:23], v[56:59]\n"
    "v_mfma_f32_16x16x32_bf16 v[60:63], v[224:227], v[20:23], v[60:63]\n"
    "v_mfma_f32_16x16x32_bf16 v[72:75], v[228:231], v[20:23], v[72:75]\n"
    "ds_read_b128 v[20:23], v9 offset:1024\n"
    "v_mfma_f32_16x16x32_bf16 v[32:35], v[232:235], v[24:27], v[32:35]\n"
    "s_or_b64 exec, vcc, s[62:63]\n"
    "buffer_atomic_smax v5, v2, s[36:39], 0 offen sc0 sc1\n"
    "s_mov_b64 exec, -1\n"
    "v_mfma_f32_16x16x32_bf16 v[36:39], v[236:239], v[24:27], v[36:39]\n"
    "v_mfma_f32_16x16x32_bf16 v[48:51], v[240:243], v[24:27], v[48:51]\n"
    "v_mov_b32 v7, v12\n"
    "v_mfma_f32_16x16x32_bf16 v[56:59], v[244:247], v[24:27], v[56:59]\n"
    "s_mov_b32 s69, s70\n"
    "s_mov_b32 s70, 0\n"
    "v_mov_b32 v12, s68\n"
    "v_mfma_f32_16x16x32_bf16 v[60:63], v[248:251], v[24:27], v[60:63]\n"
    "s_waitcnt lgkmcnt(1)\n"
    "s_add_u32 s68, s68, 1\n"
    "v_mfma_f32_16x16x32_bf16 v[72:75], v[252:255], v[24:27], v[72:75]\n"
    "ds_read_b128 v[24:27], v9 offset:2048\n"
    ".LgUP_head1_%=:\n"
    "v_mfma_f32_16x16x32_bf16 v[40:43], a[0:3], v[16:19], 0\n"
    "v_mfma_f32_16x16x32_bf16 v[44:47], a[4:7], v[16:19], 0\n"
    "v_mul_f32 v56, v32, v56\n"
    "v_mul_f32 v57, v33, v57\n"
    "v_mfma_f32_16x16x32_bf16 v[52:55], a[8:11], v[16:19], 0\n"
    "v_mul_f32 v58, v34, v58\n"
    "v_mul_f32 v59, v35, v59\n"
    "v_mfma_f32_16x16x32_bf16 v[64:67], a[12:15], v[16:19], 0\n"
    "v_mul_f32 v60, v36, v60\n"
    "v_mul_f32 v61, v37, v61\n"
    "v_mfma_f32_16x16x32_bf16 v[68:71], a[16:19], v[16:19], 0\n"
    "s_waitcnt lgkmcnt(1)\n"
    "v_mul_f32 v62, v38, v62\n"
    "v_mul_f32 v63, v39, v63\n"
    "v_mfma_f32_16x16x32_bf16 v[76:79], a[20:23], v[16:19], 0\n"
    "ds_read_b128 v[16:19], v9 offset:3072\n"
    "v_mul_f32 v32, 0xbfb8aa3b, v32\n"
    "v_mfma_f32_16x16x32_bf16 v[40:43], a[24:27], v[20:23], v[40:43]\n"
    "v_exp_f32 v32, v32\n"
    "v_mfma_f32_16x16x32_bf16 v[44:47], a[28:31], v[20:23], v[44:47]\n"
    "s_waitcnt vmcnt(1)\n"
    "v_mul_f32 v33, 0xbfb8aa3b, v33\n"
    "v_add_u32 v13, v11, v3\n"
    "v_mfma_f32_16x16x32_bf16 v[52:55], a[32:35], v[20:23], v[52:55]\n"
    "v_mul_f32 v34, 0xbfb8aa3b, v34\n"
    "v_cmp_gt_i32 vcc, s71, v13\n"
    "v_mfma_f32_16x16x32_bf16 v[64:67], a[36:39], v[20:23], v[64:67]\n"
    "buffer_atomic_smax v7, v14, s[36:39], s69 offen sc1\n"
    "v_exp_f32 v33, v33\n"
    "v_mfma_f32_16x16x32_bf16 v[68:71], a[40:43], v[20:23], v[68:71]\n"
    "s_waitcnt lgkmcnt(1)\n"
    "v_exp_f32 v34, v34\n"
    "v_mfma_f32_16x16x32_bf16 v[76:79], a[44:47], v[20:23], v[76:79]\n"
    "ds_read_b128 v[20:23], v9 offset:4096\n"
    "s_cbranch_vccnz .LgUP_poll1_%=\n"
    "v_mfma_f32_16x16x32_bf16 v[40:43], a[48:51], v[24:27], v[40:43]\n"
    "s_add_u32 m0, s59, 0\n"
    "v_mfma_f32_16x16x32_bf16 v[44:47], a[52:55], v[24:27], v[44:47]\n"
    "buffer_load_dwordx4 v1, s[36:39], s76 offen sc1 lds\n"
    "v_mul_f32 v35, 0xbfb8aa3b, v35\n"
    "v_mul_f32 v36, 0xbfb8aa3b, v36\n"
    "v_mfma_f32_16x16x32_bf16 v[52:55], a[56:59], v[24:27], v[52:55]\n"
    "v_exp_f32 v35, v35\n"
    "v_mfma_f32_16x16x32_bf16 v[64:67], a[60:63], v[24:27], v[64:67]\n"
    "v_exp_f32 v36, v36\n"
    "v_mfma_f32_16x16x32_bf16 v[68:71], a[64:67], v[24:27], v[68:71]\n"
    "s_waitcnt lgkmcnt(1)\n"
    "v_mul_f32 v37, 0xbfb8aa3b, v37\n"
    "v_mul_f32 v38, 0xbfb8aa3b, v38\n"
    "v_mfma_f32_16x16x32_bf16 v[76:79], a[68:71], v[24:27], v[76:79]\n"
    "ds_read_b128 v[24:27], v9 offset:5120\n"
    "v_mul_f32 v39, 0xbfb8aa3b, v39\n"
    "v_mfma_f32_16x16x32_bf16 v[40:43], a[72:75], v[16:19], v[40:43]\n"
    "v_exp_f32 v37, v37\n"
    "v_mfma_f32_16x16x32_bf16 v[44:47], a[76:79], v[16:19], v[44:47]\n"
    "v_exp_f32 v38, v38\n"
    "v_mfma_f32_16x16x32_bf16 v[52:55], a[80:83], v[16:19], v[52:55]\n"
    "s_add_u32 s76, s76, 16384\n"
    "v_exp_f32 v39, v39\n"
    "v_mfma_f32_16x16x32_bf16 v[64:67], a[84:87], v[16:19], v[64:67]\n"
    "s_add_u32 m0, s59, 4096\n"
    "v_add_f32 v32, 1.0, v32\n"
    "v_add_f32 v33, 1.0, v33\n"
    "v_mfma_f32_16x16x32_bf16 v[68:71], a[88:91], v[16:19], v[68:71]\n"
    "s_waitcnt lgkmcnt(1)\n"
    "v_rcp_f32 v32, v32\n"
    "v_mfma_f32_16x16x32_bf16 v[76:79], a[92:95], v[16:19], v[76:79]\n"
    "ds_read_b128 v[16:19], v9 offset:6144\n"
    "v_add_f32 v34, 1.0, v34\n"
    "v_mfma_f32_16x16x32_bf16 v[40:43], a[96:99], v[20:23], v[40:43]\n"
    "buffer_load_dwordx4 v1, s[36:39], s76 offen sc1 lds\n"
    "v_rcp_f32 v33, v33\n"
    "v_mfma_f32_16x16x32_bf16 v[44:47], a[100:103], v[20:23], v[44:47]\n"
    "v_add_f32 v35, 1.0, v35\n"
    "v_mfma_f32_16x16x32_bf16 v[52:55], a[104:107], v[20:23], v[52:55]\n"
    "v_add_f32 v36, 1.0, v36\n"
    "v_mfma_f32_16x16x32_bf16 v[64:67], a[108:111], v[20:23], v[64:67]\n"
    "v_rcp_f32 v34, v34\n"
    "v_mfma_f32_16x16x32_bf16 v[68:71], a[112:115], v[20:23], v[68:71]\n"
    "s_waitcnt lgkmcnt(1)\n"
    "v_rcp_f32 v35, v35\n"
    "v_mfma_f32_16x16x32_bf16 v[76:79], a[116:119], v[20:23], v[76:79]\n"
    "ds_read_b128 v[20:23], v9 offset:7168\n"
    "v_mfma_f32_16x16x32_bf16 v[40:43], a[120:123], v[24:27], v[40:43]\n"
    "s_add_u32 s76, s76, 16384\n"
    "s_add_u32 m0, s59, 8192\n"
    "v_mfma_f32_16x16x32_bf16 v[44:47], a[124:127], v[24:27], v[44:47]\n"
    "buffer_load_dwordx4 v1, s[36:39], s76 offen sc1 lds\n"
    "v_rcp_f32 v36, v36\n"
    "v_mfma_f32_16x16x32_bf16 v[52:55], a[128:131], v[24:27], v[52:55]\n"
    "v_add_f32 v37, 1.0, v37\n"
    "v_add_f32 v38, 1.0, v38\n"
    "v_mfma_f32_16x16x32_bf16 v[64:67], a[132:135], v[24:27], v[64:67]\n"
    "v_rcp_f32 v37, v37\n"
    "v_mfma_f32_16x16x32_bf16 v[68:71], a[136:139], v[24:27], v[68:71]\n"
    "s_waitcnt lgkmcnt(1)\n"
    "v_rcp_f32 v38, v38\n"
    "v_mfma_f32_16x16x32_bf16 v[76:79], a[140:143], v[24:27], v[76:79]\n"
    "ds_read_b128 v[24:27], v9 offset:8192\n"
    "v_add_f32 v39, 1.0, v39\n"
    "v_mfma_f32_16x16x32_bf16 v[40:43], a[144:147], v[16:19], v[40:43]\n"
    "v_rcp_f32 v39, v39\n"
    "v_mfma_f32_16x16x32_bf16 v[44:47], a[148:151], v[16:19], v[44:47]\n"
    "v_mul_f32 v32, v32, v56\n"
    "v_mul_f32 v33, v33, v57\n"
    "v_mfma_f32_16x16x32_bf16 v[52:55], a[152:155], v[16:19], v[52:55]\n"
    "v_mul_f32 v34, v34, v58\n"
    "v_mul_f32 v35, v35, v59\n"
    "v_mfma_f32_16x16x32_bf16 v[64:67], a[156:159], v[16:19], v[64:67]\n"
    "v_mul_f32 v36, v36, v60\n"
    "v_mul_f32 v37, v37, v61\n"
    "v_mfma_f32_16x16x32_bf16 v[68:71], a[160:163], v[16:19], v[68:71]\n"
    "s_waitcnt lgkmcnt(1)\n"
    "v_mul_f32 v38, v38, v62\n"
    "v_mul_f32 v39, v39, v63\n"
    "v_mfma_f32_16x16x32_bf16 v[76:79], a[164:167], v[16:19], v[76:79]\n"
    "ds_read_b128 v[16:19], v9 offset:9216\n"
    "v_cvt_pk_bf16_f32 v32, v32, v33\n"
    "v_mfma_f32_16x16x32_bf16 v[40:43], a[168:171], v[20:23], v[40:43]\n"
    "s_add_u32 s76, s76, 16384\n"
    "v_cvt_pk_bf16_f32 v33, v34, v35\n"
    "v_cvt_pk_bf16_f32 v34, v36, v37\n"
    "v_mfma_f32_16x16x32_bf16 v[44:47], a[172:175], v[20:23], v[44:47]\n"
    "s_add_u32 m0, s59, 12288\n"
    "v_cvt_pk_bf16_f32 v35, v38, v39\n"
    "v_mfma_f32_16x16x32_bf16 v[52:55], a[176:179], v[20:23], v[52:55]\n"
    "buffer_load_dwordx4 v1, s[36:39], s76 offen sc1 lds\n"
    "v_mul_f32 v72, v48, v72\n"
    "v_mfma_f32_16x16x32_bf16 v[64:67], a[180:183], v[20:23], v[64:67]\n"
    "buffer_store_dwordx4 v[32:35], v15, s[36:39], s73 offen sc1\n"
    "v_mfma_f32_16x16x32_bf16 v[68:71], a[184:187], v[20:23], v[68:71]\n"
    "s_waitcnt lgkmcnt(1)\n"
    "v_mul_f32 v73, v49, v73\n"
    "v_mul_f32 v74, v50, v74\n"
    "v_mfma_f32_16x16x32_bf16 v[76:79], a[188:191], v[20:23], v[76:79]\n"
    "ds_read_b128 v[20:23], v9 offset:10240\n"
    "v_mfma_f32_16x16x32_bf16 v[40:43], a[192:195], v[24:27], v[40:43]\n"
    "v_mfma_f32_16x16x32_bf16 v[44:47], a[196:199], v[24:27], v[44:47]\n"
    "v_mul_f32 v75, v51, v75\n"
    "v_mul_f32 v48, 0xbfb8aa3b, v48\n"
    "v_mfma_f32_16x16x32_bf16 v[52:55], a[200:203], v[24:27], v[52:55]\n"
    "s_add_u32 s76, s76, 16384\n"
    "v_exp_f32 v48, v48\n"
    "v_mfma_f32_16x16x32_bf16 v[64:67], a[204:207], v[24:27], v[64:67]\n"
    "s_add_u32 m0, s59, 16384\n"
    "v_mul_f32 v49, 0xbfb8aa3b, v49\n"
    "v_mul_f32 v50, 0xbfb8aa3b, v50\n"
    "v_mfma_f32_16x16x32_bf16 v[68:71], a[208:211], v[24:27], v[68:71]\n"
    "s_waitcnt lgkmcnt(1)\n"
    "v_exp_f32 v49, v49\n"
    "v_mfma_f32_16x16x32_bf16 v[76:79], a[212:215], v[24:27], v[76:79]\n"
    "ds_read_b128 v[24:27], v9 offset:11264\n"
    "v_mul_f32 v51, 0xbfb8aa3b, v51\n"
    "v_mfma_f32_16x16x32_bf16 v[40:43], a[216:219], v[16:19], v[40:43]\n"
    "buffer_load_dwordx4 v8, s[36:39], s76 offen sc1 lds\n"
    "v_exp_f32 v50, v50\n"
    "v_mfma_f32_16x16x32_bf16 v[44:47], a[220:223], v[16:19], v[44:47]\n"
    "v_exp_f32 v51, v51\n"
    "v_mfma_f32_16x16x32_bf16 v[52:55], a[224:227], v[16:19], v[52:55]\n"
    "v_add_f32 v48, 1.0, v48\n"
    "v_add_f32 v49, 1.0, v49\n"
    "v_mfma_f32_16x16x32_bf16 v[64:67], a[228:231], v[16:19], v[64:67]\n"
    "v_rcp_f32 v48, v48\n"
    "v_mfma_f32_16x16x32_bf16 v[68:71], a[232:235], v[16:19], v[68:71]\n"
    "s_waitcnt lgkmcnt(1)\n"
    "v_rcp_f32 v49, v49\n"
    "v_mfma_f32_16x16x32_bf16 v[76:79], a[236:239], v[16:19], v[76:79]\n"
    "ds_read_b128 v[16:19], v9 offset:12288\n"
    "v_add_f32 v50, 1.0, v50\n"
    "v_mfma_f32_16x16x32_bf16 v[40:43], a[240:243], v[20:23], v[40:43]\n"
    "v_rcp_f32 v50, v50\n"
    "v_mfma_f32_16x16x32_bf16 v[44:47], a[244:247], v[20:23], v[44:47]\n"
    "v_add_f32 v51, 1.0, v51\n"
    "v_mfma_f32_16x16x32_bf16 v[52:55], a[248:251], v[20:23], v[52:55]\n"
    "v_mul_f32 v48, v48, v72\n"
    "v_mfma_f32_16x16x32_bf16 v[64:67], a[252:255], v[20:23], v[64:67]\n"
    "v_rcp_f32 v51, v51\n"
    "v_mfma_f32_16x16x32_bf16 v[68:71], v[80:83], v[20:23], v[68:71]\n"
    "s_waitcnt lgkmcnt(1)\n"
    "v_mul_f32 v49, v49, v73\n"
    "v_mul_f32 v50, v50, v74\n"
    "v_mfma_f32_16x16x32_bf16 v[76:79], v[84:87], v[20:23], v[76:79]\n"
    "ds_read_b128 v[20:23], v9 offset:13312\n"
    "v_mfma_f32_16x16x32_bf16 v[40:43], v[88:91], v[24:27], v[40:43]\n"
    "v_mfma_f32_16x16x32_bf16 v[44:47], v[92:95], v[24:27], v[44:47]\n"
    "v_mul_f32 v51, v51, v75\n"
    "v_cvt_pk_bf16_f32 v48, v48, v49\n"
    "v_mfma_f32_16x16x32_bf16 v[52:55], v[96:99], v[24:27], v[52:55]\n"
    "v_cvt_pk_bf16_f32 v49, v50, v51\n"
    "s_nop 0\n"
    "v_mfma_f32_16x16x32_bf16 v[64:67], v[100:103], v[24:27], v[64:67]\n"
    "v_permlane16_swap_b32 v48, v50\n"
    "v_mfma_f32_16x16x32_bf16 v[68:71], v[104:107], v[24:27], v[68:71]\n"
    "s_waitcnt lgkmcnt(1)\n"
    "v_permlane16_swap_b32 v49, v51\n"
    "v_mfma_f32_16x16x32_bf16 v[76:79], v[108:111], v[24:27], v[76:79]\n"
    "ds_read_b128 v[24:27], v9 offset:14336\n"
    ".LgUP_join1_%=:\n"
    "v_mfma_f32_16x16x32_bf16 v[40:43], v[112:115], v[16:19], v[40:43]\n"
    "buffer_store_dwordx4 v[48:51], v4, s[36:39], s73 offen sc1\n"
    "v_mfma_f32_16x16x32_bf16 v[44:47], v[116:119], v[16:19], v[44:47]\n"
    "s_lshr_b32 s73, s68, 1\n"
    "v_mfma_f32_16x16x32_bf16 v[52:55], v[120:123], v[16:19], v[52:55]\n"
    "s_and_b32 s73, s73, s57\n"
    "v_mfma_f32_16x16x32_bf16 v[64:67], v[124:127], v[16:19], v[64:67]\n"
    "s_mul_i32 s73, s73, s56\n"
    "v_mfma_f32_16x16x32_bf16 v[68:71], v[128:131], v[16:19], v[68:71]\n"
    "s_waitcnt lgkmcnt(1)\n"
    "s_add_u32 s73, s73, s55\n"
    "v_mfma_f32_16x16x32_bf16 v[76:79], v[132:135], v[16:19], v[76:79]\n"
    "ds_read_b128 v[16:19], v9 offset:15360\n"
    "v_mfma_f32_16x16x32_bf16 v[40:43], v[136:139], v[20:23], v[40:43]\n"
    "s_add_u32 s71, s68, 2\n"
    "s_waitcnt vmcnt(6)\n"
    "v_mfma_f32_16x16x32_bf16 v[44:47], v[140:143], v[20:23], v[44:47]\n"
    "v_add_u32 v13, v11, v3\n"
    "v_mfma_f32_16x16x32_bf16 v[52:55], v[144:147], v[20:23], v[52:55]\n"
    "v_cmp_gt_i32 vcc, s71, v13\n"
    "v_mfma_f32_16x16x32_bf16 v[64:67], v[148:151], v[20:23], v[64:67]\n"
    "v_mfma_f32_16x16x32_bf16 v[68:71], v[152:155], v[20:23], v[68:71]\n"
    "s_waitcnt lgkmcnt(1)\n"
    "v_mfma_f32_16x16x32_bf16 v[76:79], v[156:159], v[20:23], v[76:79]\n"
    "ds_read_b128 v[20:23], v9 offset:16384\n"
    "v_mfma_f32_16x16x32_bf16 v[40:43], v[160:163], v[24:27], v[40:43]\n"
    "s_or_b64 exec, vcc, s[62:63]\n"
    "buffer_atomic_smax v11, v2, s[36:39], 0 offen sc0 sc1\n"
    "s_mov_b64 exec, -1\n"
    "v_mfma_f32_16x16x32_bf16 v[44:47], v[164:167], v[24:27], v[44:47]\n"
    "s_lshr_b32 s76, s71, 1\n"
    "v_mfma_f32_16x16x32_bf16 v[52:55], v[168:171], v[24:27], v[52:55]\n"
    "s_and_b32 s76, s76, s54\n"
    "v_mfma_f32_16x16x32_bf16 v[64:67], v[172:175], v[24:27], v[64:67]\n"
    "s_mul_i32 s76, s76, s53\n"
    "v_mfma_f32_16x16x32_bf16 v[68:71], v[176:179], v[24:27], v[68:71]\n"
    "s_waitcnt lgkmcnt(1)\n"
    "s_add_u32 s76, s76, s52\n"
    "v_mfma_f32_16x16x32_bf16 v[76:79], v[180:183], v[24:27], v[76:79]\n"
    "ds_read_b128 v[24:27], v9 offset:17408\n"
    "s_add_u32 s76, s76, 1024\n"
    "v_mfma_f32_16x16x32_bf16 v[40:43], v[184:187], v[16:19], v[40:43]\n"
    "v_mfma_f32_16x16x32_bf16 v[44:47], v[188:191], v[16:19], v[44:47]\n"
    "v_mfma_f32_16x16x32_bf16 v[52:55], v[192:195], v[16:19], v[52:55]\n"
    "v_mfma_f32_16x16x32_bf16 v[64:67], v[196:199], v[16:19], v[64:67]\n"
    "s_waitcnt vmcnt(2) lgkmcnt(0)\n"
    "s_barrier\n"
    "v_mfma_f32_16x16x32_bf16 v[68:71], v[200:203], v[16:19], v[68:71]\n"
    "v_mfma_f32_16x16x32_bf16 v[76:79], v[204:207], v[16:19], v[76:79]\n"
    "ds_read_b128 v[16:19], v0 offset:0\n"
    "v_mfma_f32_16x16x32_bf16 v[40:43], v[208:211], v[20:23], v[40:43]\n"
    "v_mfma_f32_16x16x32_bf16 v[44:47], v[212:215], v[20:23], v[44:47]\n"
    "v_add_u32 v13, v5, v3\n"
    "v_mfma_f32_16x16x32_bf16 v[52:55], v[216:219], v[20:23], v[52:55]\n"
    "v_cmp_gt_i32 vcc, s71, v13\n"
    "v_mfma_f32_16x16x32_bf16 v[64:67], v[220:223], v[20:23], v[64:67]\n"
    "v_mfma_f32_16x16x32_bf16 v[68:71], v[224:227], v[20:23], v[68:71]\n"
    "v_mfma_f32_16x16x32_bf16 v[76:79], v[228:231], v[20:23], v[76:79]\n"
    "ds_read_b128 v[20:23], v0 offset:1024\n"
    "v_mfma_f32_16x16x32_bf16 v[40:43], v[232:235], v[24:27], v[40:43]\n"
    "s_or_b64 exec, vcc, s[62:63]\n"
    "buffer_atomic_smax v5, v2, s[36:39], 0 offen sc0 sc1\n"
    "s_mov_b64 exec, -1\n"
    "v_mfma_f32_16x16x32_bf16 v[44:47], v[236:239], v[24:27], v[44:47]\n"
    "v_mfma_f32_16x16x32_bf16 v[52:55], v[240:243], v[24:27], v[52:55]\n"
    "v_mov_b32 v7, v12\n"
    "v_mfma_f32_16x16x32_bf16 v[64:67], v[244:247], v[24:27], v[64:67]\n"
    "s_mov_b32 s69, s70\n"
    "s_mov_b32 s70, 0\n"
    "v_mov_b32 v12, s68\n"
    "v_mfma_f32_16x16x32_bf16 v[68:71], v[248:251], v[24:27], v[68:71]\n"
    "s_waitcnt lgkmcnt(1)\n"
    "s_add_u32 s68, s68, 1\n"
    "v_mfma_f32_16x16x32_bf16 v[76:79], v[252:255], v[24:27], v[76:79]\n"
    "ds_read_b128 v[24:27], v0 offset:2048\n"
    "s_branch .LgUP_head0_%=\n"
    ".LgUP_LOOPE_%=:\n"
    ".LgUP_done_%=:\n"
    "s_waitcnt vmcnt(0)\n"
    "buffer_atomic_smax v12, v10, s[36:39], 0 offen sc1\n"
    "s_waitcnt vmcnt(0)\n"
    "s_branch .LgUP_FIN_%=\n"
    ".LgUP_poll0_%=:\n"
    "v_mfma_f32_16x16x32_bf16 v[32:35], a[48:51], v[24:27], v[32:35]\n"
    "s_or_b64 exec, vcc, s[62:63]\n"
    "buffer_atomic_smax v11, v2, s[36:39], 0 offen sc0 sc1\n"
    "s_mov_b64 exec, -1\n"
    "v_mfma_f32_16x16x32_bf16 v[36:39], a[52:55], v[24:27], v[36:39]\n"
    "v_mul_f32 v43, 0xbfb8aa3b, v43\n"
    "v_mul_f32 v44, 0xbfb8aa3b, v44\n"
    "v_mfma_f32_16x16x32_bf16 v[48:51], a[56:59], v[24:27], v[48:51]\n"
    "v_exp_f32 v43, v43\n"
    "v_mfma_f32_16x16x32_bf16 v[56:59], a[60:63], v[24:27], v[56:59]\n"
    "v_exp_f32 v44, v44\n"
    "v_mfma_f32_16x16x32_bf16 v[60:63], a[64:67], v[24:27], v[60:63]\n"
    "s_waitcnt lgkmcnt(1)\n"
    "v_mul_f32 v45, 0xbfb8aa3b, v45\n"
    "v_mul_f32 v46, 0xbfb8aa3b, v46\n"
    "v_mfma_f32_16x16x32_bf16 v[72:75], a[68:71], v[24:27], v[72:75]\n"
    "ds_read_b128 v[24:27], v0 offset:5120\n"
    "v_mul_f32 v47, 0xbfb8aa3b, v47\n"
    "v_mfma_f32_16x16x32_bf16 v[32:35], a[72:75], v[16:19], v[32:35]\n"
    "v_exp_f32 v45, v45\n"
    "v_mfma_f32_16x16x32_bf16 v[36:39], a[76:79], v[16:19], v[36:39]\n"
    "v_exp_f32 v46, v46\n"
    "v_mfma_f32_16x16x32_bf16 v[48:51], a[80:83], v[16:19], v[48:51]\n"
    "v_exp_f32 v47, v47\n"
    "v_mfma_f32_16x16x32_bf16 v[56:59], a[84:87], v[16:19], v[56:59]\n"
    "v_add_f32 v40, 1.0, v40\n"
    "v_add_f32 v41, 1.0, v41\n"
    "v_mfma_f32_16x16x32_bf16 v[60:63], a[88:91], v[16:19], v[60:63]\n"
    "s_waitcnt lgkmcnt(1)\n"
    "v_rcp_f32 v40, v40\n"
    "v_mfma_f32_16x16x32_bf16 v[72:75], a[92:95], v[16:19], v[72:75]\n"
    "ds_read_b128 v[16:19], v0 offset:6144\n"
    "v_add_f32 v42, 1.0, v42\n"
    "v_mfma_f32_16x16x32_bf16 v[32:35], a[96:99], v[20:23], v[32:35]\n"
    "v_rcp_f32 v41, v41\n"
    "v_mfma_f32_16x16x32_bf16 v[36:39], a[100:103], v[20:23], v[36:39]\n"
    "s_waitcnt vmcnt(2)\n"
    "v_add_f32 v43, 1.0, v43\n"
    "v_add_u32 v13, v5, v3\n"
    "v_mfma_f32_16x16x32_bf16 v[48:51], a[104:107], v[20:23], v[48:51]\n"
    "v_add_f32 v44, 1.0, v44\n"
    "v_cmp_gt_i32 vcc, s71, v13\n"
    "v_mfma_f32_16x16x32_bf16 v[56:59], a[108:111], v[20:23], v[56:59]\n"
    "v_rcp_f32 v42, v42\n"
    "v_mfma_f32_16x16x32_bf16 v[60:63], a[112:115], v[20:23], v[60:63]\n"
    "s_waitcnt lgkmcnt(1)\n"
    "v_rcp_f32 v43, v43\n"
    "v_mfma_f32_16x16x32_bf16 v[72:75], a[116:119], v[20:23], v[72:75]\n"
    "ds_read_b128 v[20:23], v0 offset:7168\n"
    "s_cbranch_vccz .LgUP_dma0_30_%=\n"
    "v_mfma_f32_16x16x32_bf16 v[32:35], a[120:123], v[24:27], v[32:35]\n"
    "s_or_b64 exec, vcc, s[62:63]\n"
    "buffer_atomic_smax v5, v2, s[36:39], 0 offen sc0 sc1\n"
    "s_mov_b64 exec, -1\n"
    "v_mfma_f32_16x16x32_bf16 v[36:39], a[124:127], v[24:27], v[36:39]\n"
    "v_rcp_f32 v44, v44\n"
    "v_mfma_f32_16x16x32_bf16 v[48:51], a[128:131], v[24:27], v[48:51]\n"
    "v_add_f32 v45, 1.0, v45\n"
    "v_add_f32 v46, 1.0, v46\n"
    "v_mfma_f32_16x16x32_bf16 v[56:59], a[132:135], v[24:27], v[56:59]\n"
    "v_rcp_f32 v45, v45\n"
    "v_mfma_f32_16x16x32_bf16 v[60:63], a[136:139], v[24:27], v[60:63]\n"
    "s_waitcnt lgkmcnt(1)\n"
    "v_rcp_f32 v46, v46\n"
    "v_mfma_f32_16x16x32_bf16 v[72:75], a[140:143], v[24:27], v[72:75]\n"
    "ds_read_b128 v[24:27], v0 offset:8192\n"
    "v_add_f32 v47, 1.0, v47\n"
    "v_mfma_f32_16x16x32_bf16 v[32:35], a[144:147], v[16:19], v[32:35]\n"
    "v_rcp_f32 v47, v47\n"
    "v_mfma_f32_16x16x32_bf16 v[36:39], a[148:151], v[16:19], v[36:39]\n"
    "v_mul_f32 v40, v40, v64\n"
    "v_mul_f32 v41, v41, v65\n"
    "v_mfma_f32_16x16x32_bf16 v[48:51], a[152:155], v[16:19], v[48:51]\n"
    "v_mul_f32 v42, v42, v66\n"
    "v_mul_f32 v43, v43, v67\n"
    "v_mfma_f32_16x16x32_bf16 v[56:59], a[156:159], v[16:19], v[56:59]\n"
    "v_mul_f32 v44, v44, v68\n"
    "v_mul_f32 v45, v45, v69\n"
    "v_mfma_f32_16x16x32_bf16 v[60:63], a[160:163], v[16:19], v[60:63]\n"
    "s_waitcnt lgkmcnt(1)\n"
    "v_mul_f32 v46, v46, v70\n"
    "v_mul_f32 v47, v47, v71\n"
    "v_mfma_f32_16x16x32_bf16 v[72:75], a[164:167], v[16:19], v[72:75]\n"
    "ds_read_b128 v[16:19], v0 offset:9216\n"
    "v_cvt_pk_bf16_f32 v40, v40, v41\n"
    "v_mfma_f32_16x16x32_bf16 v[32:35], a[168:171], v[20:23], v[32:35]\n"
    "v_cvt_pk_bf16_f32 v41, v42, v43\n"
    "v_cvt_pk_bf16_f32 v42, v44, v45\n"
    "v_mfma_f32_16x16x32_bf16 v[36:39], a[172:175], v[20:23], v[36:39]\n"
    "s_waitcnt vmcnt(1)\n"
    "v_cvt_pk_bf16_f32 v43, v46, v47\n"
    "v_add_u32 v13, v11, v3\n"
    "v_mfma_f32_16x16x32_bf16 v[48:51], a[176:179], v[20:23], v[48:51]\n"
    "v_mul_f32 v76, v52, v76\n"
    "v_cmp_gt_i32 vcc, s71, v13\n"
    "v_mfma_f32_16x16x32_bf16 v[56:59], a[180:183], v[20:23], v[56:59]\n"
    "buffer_store_dwordx4 v[40:43], v15, s[36:39], s73 offen offset:1024 sc1\n"
    "v_mfma_f32_16x16x32_bf16 v[60:63], a[184:187], v[20:23], v[60:63]\n"
    "s_waitcnt lgkmcnt(1)\n"
    "v_mul_f32 v77, v53, v77\n"
    "v_mul_f32 v78, v54, v78\n"
    "v_mfma_f32_16x16x32_bf16 v[72:75], a[188:191], v[20:23], v[72:75]\n"
    "ds_read_b128 v[20:23], v0 offset:10240\n"
    "s_cbranch_vccz .LgUP_dma0_48_%=\n"
    "v_mfma_f32_16x16x32_bf16 v[32:35], a[192:195], v[24:27], v[32:35]\n"
    "s_or_b64 exec, vcc, s[62:63]\n"
    "buffer_atomic_smax v11, v2, s[36:39], 0 offen sc0 sc1\n"
    "s_mov_b64 exec, -1\n"
    "v_mfma_f32_16x16x32_bf16 v[36:39], a[196:199], v[24:27], v[36:39]\n"
    "v_mul_f32 v79, v55, v79\n"
    "v_mul_f32 v52, 0xbfb8aa3b, v52\n"
    "v_mfma_f32_16x16x32_bf16 v[48:51], a[200:203], v[24:27], v[48:51]\n"
    "v_exp_f32 v52, v52\n"
    "v_mfma_f32_16x16x32_bf16 v[56:59], a[204:207], v[24:27], v[56:59]\n"
    "v_mul_f32 v53, 0xbfb8aa3b, v53\n"
    "v_mul_f32 v54, 0xbfb8aa3b, v54\n"
    "v_mfma_f32_16x16x32_bf16 v[60:63], a[208:211], v[24:27], v[60:63]\n"
    "s_waitcnt lgkmcnt(1)\n"
    "v_exp_f32 v53, v53\n"
    "v_mfma_f32_16x16x32_bf16 v[72:75], a[212:215], v[24:27], v[72:75]\n"
    "ds_read_b128 v[24:27], v0 offset:11264\n"
    "v_mul_f32 v55, 0xbfb8aa3b, v55\n"
    "v_mfma_f32_16x16x32_bf16 v[32:35], a[216:219], v[16:19], v[32:35]\n"
    "v_exp_f32 v54, v54\n"
    "v_mfma_f32_16x16x32_bf16 v[36:39], a[220:223], v[16:19], v[36:39]\n"
    "v_exp_f32 v55, v55\n"
    "v_mfma_f32_16x16x32_bf16 v[48:51], a[224:227], v[16:19], v[48:51]\n"
    "v_add_f32 v52, 1.0, v52\n"
    "v_add_f32 v53, 1.0, v53\n"
    "v_mfma_f32_16x16x32_bf16 v[56:59], a[228:231], v[16:19], v[56:59]\n"
    "v_rcp_f32 v52, v52\n"
    "v_mfma_f32_16x16x32_bf16 v[60:63], a[232:235], v[16:19], v[60:63]\n"
    "s_waitcnt lgkmcnt(1)\n"
    "v_rcp_f32 v53, v53\n"
    "v_mfma_f32_16x16x32_bf16 v[72:75], a[236:239], v[16:19], v[72:75]\n"
    "ds_read_b128 v[16:19], v0 offset:12288\n"
    "v_add_f32 v54, 1.0, v54\n"
    "v_mfma_f32_16x16x32_bf16 v[32:35], a[240:243], v[20:23], v[32:35]\n"
    "v_rcp_f32 v54, v54\n"
    "v_mfma_f32_16x16x32_bf16 v[36:39], a[244:247], v[20:23], v[36:39]\n"
    "s_waitcnt vmcnt(2)\n"
    "v_add_f32 v55, 1.0, v55\n"
    "v_add_u32 v13, v5, v3\n"
    "v_mfma_f32_16x16x32_bf16 v[48:51], a[248:251], v[20:23], v[48:51]\n"
    "v_mul_f32 v52, v52, v76\n"
    "v_cmp_gt_i32 vcc, s71, v13\n"
    "v_mfma_f32_16x16x32_bf16 v[56:59], a[252:255], v[20:23], v[56:59]\n"
    "v_rcp_f32 v55, v55\n"
    "v_mfma_f32_16x16x32_bf16 v[60:63], v[80:83], v[20:23], v[60:63]\n"
    "s_waitcnt lgkmcnt(1)\n"
    "v_mul_f32 v53, v53, v77\n"
    "v_mul_f32 v54, v54, v78\n"
    "v_mfma_f32_16x16x32_bf16 v[72:75], v[84:87], v[20:23], v[72:75]\n"
    "ds_read_b128 v[20:23], v0 offset:13312\n"
    "s_cbranch_vccz .LgUP_dma0_66_%=\n"
    "v_mfma_f32_16x16x32_bf16 v[32:35], v[88:91], v[24:27], v[32:35]\n"
    "s_or_b64 exec, vcc, s[62:63]\n"
    "buffer_atomic_smax v5, v2, s[36:39], 0 offen sc0 sc1\n"
    "s_mov_b64 exec, -1\n"
    "v_mfma_f32_16x16x32_bf16 v[36:39], v[92:95], v[24:27], v[36:39]\n"
    "v_mul_f32 v55, v55, v79\n"
    "v_cvt_pk_bf16_f32 v52, v52, v53\n"
    "v_mfma_f32_16x16x32_bf16 v[48:51], v[96:99], v[24:27], v[48:51]\n"
    "v_cvt_pk_bf16_f32 v53, v54, v55\n"
    "s_nop 0\n"
    "v_mfma_f32_16x16x32_bf16 v[56:59], v[100:103], v[24:27], v[56:59]\n"
    "v_permlane16_swap_b32 v52, v54\n"
    "v_mfma_f32_16x16x32_bf16 v[60:63], v[104:107], v[24:27], v[60:63]\n"
    "s_waitcnt lgkmcnt(1)\n"
    "v_permlane16_swap_b32 v53, v55\n"
    "v_mfma_f32_16x16x32_bf16 v[72:75], v[108:111], v[24:27], v[72:75]\n"
    "ds_read_b128 v[24:27], v0 offset:14336\n"
    "v_mfma_f32_16x16x32_bf16 v[32:35], v[112:115], v[16:19], v[32:35]\n"
    "buffer_store_dwordx4 v[52:55], v4, s[36:39], s73 offen offset:1024 sc1\n"
    "v_mfma_f32_16x16x32_bf16 v[36:39], v[116:119], v[16:19], v[36:39]\n"
    "s_lshr_b32 s73, s68, 1\n"
    "v_mfma_f32_16x16x32_bf16 v[48:51], v[120:123], v[16:19], v[48:51]\n"
    "s_and_b32 s73, s73, s57\n"
    "v_mfma_f32_16x16x32_bf16 v[56:59], v[124:127], v[16:19], v[56:59]\n"
    "s_mul_i32 s73, s73, s56\n"
    "v_mfma_f32_16x16x32_bf16 v[60:63], v[128:131], v[16:19], v[60:63]\n"
    "s_waitcnt lgkmcnt(1)\n"
    "s_add_u32 s73, s73, s55\n"
    "v_mfma_f32_16x16x32_bf16 v[72:75], v[132:135], v[16:19], v[72:75]\n"
    "ds_read_b128 v[16:19], v0 offset:15360\n"
    "v_mfma_f32_16x16x32_bf16 v[32:35], v[136:139], v[20:23], v[32:35]\n"
    "v_mfma_f32_16x16x32_bf16 v[36:39], v[140:143], v[20:23], v[36:39]\n"
    "s_waitcnt vmcnt(2)\n"
    "v_add_u32 v13, v11, v3\n"
    "v_mfma_f32_16x16x32_bf16 v[48:51], v[144:147], v[20:23], v[48:51]\n"
    "v_cmp_gt_i32 vcc, s71, v13\n"
    "v_mfma_f32_16x16x32_bf16 v[56:59], v[148:151], v[20:23], v[56:59]\n"
    "v_mfma_f32_16x16x32_bf16 v[60:63], v[152:155], v[20:23], v[60:63]\n"
    "s_waitcnt lgkmcnt(1)\n"
    "v_mfma_f32_16x16x32_bf16 v[72:75], v[156:159], v[20:23], v[72:75]\n"
    "ds_read_b128 v[20:23], v0 offset:16384\n"
    "s_cbranch_vccz .LgUP_dma0_84_%=\n"
    "v_mfma_f32_16x16x32_bf16 v[32:35], v[160:163], v[24:27], v[32:35]\n"
    "s_or_b64 exec, vcc, s[62:63]\n"
    "buffer_atomic_smax v11, v2, s[36:39], 0 offen sc0 sc1\n"
    "s_mov_b64 exec, -1\n"
    "v_mfma_f32_16x16x32_bf16 v[36:39], v[164:167], v[24:27], v[36:39]\n"
    "v_mfma_f32_16x16x32_bf16 v[48:51], v[168:171], v[24:27], v[48:51]\n"
    "v_mfma_f32_16x16x32_bf16 v[56:59], v[172:175], v[24:27], v[56:59]\n"
    "v_mfma_f32_16x16x32_bf16 v[60:63], v[176:179], v[24:27], v[60:63]\n"
    "s_waitcnt lgkmcnt(1)\n"
    "v_mfma_f32_16x16x32_bf16 v[72:75], v[180:183], v[24:27], v[72:75]\n"
    "ds_read_b128 v[24:27], v0 offset:17408\n"
    "v_mfma_f32_16x16x32_bf16 v[32:35], v[184:187], v[16:19], v[32:35]\n"
    "v_mfma_f32_16x16x32_bf16 v[36:39], v[188:191], v[16:19], v[36:39]\n"
    "v_mfma_f32_16x16x32_bf16 v[48:51], v[192:195], v[16:19], v[48:51]\n"
    "v_mfma_f32_16x16x32_bf16 v[56:59], v[196:199], v[16:19], v[56:59]\n"
    "v_mfma_f32_16x16x32_bf16 v[60:63], v[200:203], v[16:19], v[60:63]\n"
    "s_waitcnt lgkmcnt(1)\n"
    "v_mfma_f32_16x16x32_bf16 v[72:75], v[204:207], v[16:19], v[72:75]\n"
    "v_mfma_f32_16x16x32_bf16 v[32:35], v[208:211], v[20:23], v[32:35]\n"
    "v_mfma_f32_16x16x32_bf16 v[36:39], v[212:215], v[20:23], v[36:39]\n"
    "s_waitcnt vmcnt(2)\n"
    "v_add_u32 v13, v5, v3\n"
    "v_mfma_f32_16x16x32_bf16 v[48:51], v[216:219], v[20:23], v[48:51]\n"
    "v_cmp_gt_i32 vcc, s71, v13\n"
    "v_mfma_f32_16x16x32_bf16 v[56:59], v[220:223], v[20:23], v[56:59]\n"
    "v_mfma_f32_16x16x32_bf16 v[60:63], v[224:227], v[20:23], v[60:63]\n"
    "s_waitcnt lgkmcnt(0)\n"
    "v_mfma_f32_16x16x32_bf16 v[72:75], v[228:231], v[20:23], v[72:75]\n"
    "s_cbranch_vccz .LgUP_dma0_102_%=\n"
    "v_mfma_f32_16x16x32_bf16 v[32:35], v[232:235], v[24:27], v[32:35]\n"
    "s_or_b64 exec, vcc, s[62:63]\n"
    "buffer_atomic_smax v5, v2, s[36:39], 0 offen sc0 sc1\n"
    "s_mov_b64 exec, -1\n"
    "v_mfma_f32_16x16x32_bf16 v[36:39], v[236:239], v[24:27], v[36:39]\n"
    "v_mfma_f32_16x16x32_bf16 v[48:51], v[240:243], v[24:27], v[48:51]\n"
    "s_waitcnt vmcnt(2)\n"
    "buffer_atomic_smax v12, v10, s[36:39], s70 offen sc1\n"
    "v_mfma_f32_16x16x32_bf16 v[56:59], v[244:247], v[24:27], v[56:59]\n"
    "v_mfma_f32_16x16x32_bf16 v[60:63], v[248:251], v[24:27], v[60:63]\n"
    "v_mfma_f32_16x16x32_bf16 v[72:75], v[252:255], v[24:27], v[72:75]\n"
    "v_pk_mul_f32 v[56:57], v[32:33], v[56:57]\n"
    "v_pk_mul_f32 v[58:59], v[34:35], v[58:59]\n"
    "s_nop 1\n"
    "v_pk_mul_f32 v[60:61], v[36:37], v[60:61]\n"
    "v_pk_mul_f32 v[62:63], v[38:39], v[62:63]\n"
    "v_pk_mul_f32 v[32:33], v[32:33], s[64:65] op_sel_hi:[1,0]\n"
    "v_pk_mul_f32 v[34:35], v[34:35], s[64:65] op_sel_hi:[1,0]\n"
    "v_pk_mul_f32 v[36:37], v[36:37], s[64:65] op_sel_hi:[1,0]\n"
    "v_pk_mul_f32 v[38:39], v[38:39], s[64:65] op_sel_hi:[1,0]\n"
    "v_exp_f32 v32, v32\n"
    "v_exp_f32 v33, v33\n"
    "v_exp_f32 v34, v34\n"
    "v_exp_f32 v35, v35\n"
    "v_exp_f32 v36, v36\n"
    "v_exp_f32 v37, v37\n"
    "v_exp_f32 v38, v38\n"
    "v_exp_f32 v39, v39\n"
    "v_pk_add_f32 v[32:33], v[32:33], 1.0 op_sel_hi:[1,0]\n"
    "v_pk_add_f32 v[34:35], v[34:35], 1.0 op_sel_hi:[1,0]\n"
    "v_pk_add_f32 v[36:37], v[36:37], 1.0 op_sel_hi:[1,0]\n"
    "v_pk_add_f32 v[38:39], v[38:39], 1.0 op_sel_hi:[1,0]\n"
    "v_rcp_f32 v32, v32\n"
    "v_rcp_f32 v33, v33\n"
    "v_rcp_f32 v34, v34\n"
    "v_rcp_f32 v35, v35\n"
    "v_rcp_f32 v36, v36\n"
    "v_rcp_f32 v37, v37\n"
    "v_rcp_f32 v38, v38\n"
    "v_rcp_f32 v39, v39\n"
    "v_pk_mul_f32 v[32:33], v[32:33], v[56:57]\n"
    "v_pk_mul_f32 v[34:35], v[34:35], v[58:59]\n"
    "v_pk_mul_f32 v[36:37], v[36:37], v[60:61]\n"
    "v_pk_mul_f32 v[38:39], v[38:39], v[62:63]\n"
    "v_cvt_pk_bf16_f32 v32, v32, v33\n"
    "v_cvt_pk_bf16_f32 v33, v34, v35\n"
    "v_cvt_pk_bf16_f32 v34, v36, v37\n"
    "v_cvt_pk_bf16_f32 v35, v38, v39\n"
    "buffer_store_dwordx4 v[32:35], v15, s[36:39], s73 offen sc1\n"
    "v_pk_mul_f32 v[72:73], v[48:49], v[72:73]\n"
    "v_pk_mul_f32 v[74:75], v[50:51], v[74:75]\n"
    "v_pk_mul_f32 v[48:49], v[48:49], s[64:65] op_sel_hi:[1,0]\n"
    "v_pk_mul_f32 v[50:51], v[50:51], s[64:65] op_sel_hi:[1,0]\n"
    "v_exp_f32 v48, v48\n"
    "v_exp_f32 v49, v49\n"
    "v_exp_f32 v50, v50\n"
    "v_exp_f32 v51, v51\n"
    "v_pk_add_f32 v[48:49], v[48:49], 1.0 op_sel_hi:[1,0]\n"
    "v_pk_add_f32 v[50:51], v[50:51], 1.0 op_sel_hi:[1,0]\n"
    "v_rcp_f32 v48, v48\n"
    "v_rcp_f32 v49, v49\n"
    "v_rcp_f32 v50, v50\n"
    "v_rcp_f32 v51, v51\n"
    "v_pk_mul_f32 v[48:49], v[48:49], v[72:73]\n"
    "v_pk_mul_f32 v[50:51], v[50:51], v[74:75]\n"
    "v_cvt_pk_bf16_f32 v48, v48, v49\n"
    "v_cvt_pk_bf16_f32 v49, v50, v51\n"
    "s_nop 0\n"
    "v_permlane16_swap_b32 v48, v50\n"
    "v_permlane16_swap_b32 v49, v51\n"
    "buffer_store_dwordx4 v[48:51], v4, s[36:39], s73 offen sc1\n"
    "v_mov_b32 v12, s68\n"
    "s_mov_b32 s73, 0x80000000\n"
    "s_mov_b32 s68, s71\n"
    "s_waitcnt vmcnt(4)\n"
    "v_add_u32 v13, v11, v3\n"
    "v_cmp_gt_i32 vcc, s71, v13\n"
    "s_cbranch_vccnz .LgUP_idle0_%=\n"
    "s_add_u32 m0, s59, 20480\n"
    "s_nop 0\n"
    "buffer_load_dwordx4 v1, s[36:39], s76 offen sc1 lds\n"
    "s_add_u32 m0, s59, 24576\n"
    "s_add_u32 s76, s76, 16384\n"
    "buffer_load_dwordx4 v1, s[36:39], s76 offen sc1 lds\n"
    "s_add_u32 m0, s59, 28672\n"
    "s_add_u32 s76, s76, 16384\n"
    "buffer_load_dwordx4 v1, s[36:39], s76 offen sc1 lds\n"
    "s_add_u32 m0, s59, 32768\n"
    "s_add_u32 s76, s76, 16384\n"
    "buffer_load_dwordx4 v1, s[36:39], s76 offen sc1 lds\n"
    "s_add_u32 m0, s59, 36864\n"
    "s_add_u32 s76, s76, 16384\n"
    "buffer_load_dwordx4 v8, s[36:39], s76 offen sc1 lds\n"
    "s_waitcnt vmcnt(5)\n"
    "buffer_atomic_smax v12, v14, s[36:39], 0 offen sc1\n"
    "s_mov_b32 s69, 0x80000000\n"
    "s_mov_b32 s70, 0x80000000\n"
    "v_mov_b32 v7, v12\n"
    "s_waitcnt vmcnt(9)\n"
    "s_add_u32 s71, s68, 1\n"
    "v_add_u32 v13, v11, v3\n"
    "v_cmp_gt_i32 vcc, s71, v13\n"
    "s_nop 0\n"
    "s_or_b64 exec, vcc, s[62:63]\n"
    "buffer_atomic_smax v11, v2, s[36:39], 0 offen sc0 sc1\n"
    "s_mov_b64 exec, -1\n"
    "s_waitcnt vmcnt(2)\n"
    "s_branch .LgUP_bar1_%=\n"
    ".LgUP_dma0_30_%=:\n"
    "v_mfma_f32_16x16x32_bf16 v[32:35], a[120:123], v[24:27], v[32:35]\n"
    "s_add_u32 m0, s59, 20480\n"
    "v_mfma_f32_16x16x32_bf16 v[36:39], a[124:127], v[24:27], v[36:39]\n"
    "buffer_load_dwordx4 v1, s[36:39], s76 offen sc1 lds\n"
    "v_rcp_f32 v44, v44\n"
    "v_mfma_f32_16x16x32_bf16 v[48:51], a[128:131], v[24:27], v[48:51]\n"
    "s_add_u32 s76, s76, 16384\n"
    "v_add_f32 v45, 1.0, v45\n"
    "v_add_f32 v46, 1.0, v46\n"
    "v_mfma_f32_16x16x32_bf16 v[56:59], a[132:135], v[24:27], v[56:59]\n"
    "s_add_u32 m0, s59, 24576\n"
    "v_rcp_f32 v45, v45\n"
    "v_mfma_f32_16x16x32_bf16 v[60:63], a[136:139], v[24:27], v[60:63]\n"
    "s_waitcnt lgkmcnt(1)\n"
    "v_rcp_f32 v46, v46\n"
    "v_mfma_f32_16x16x32_bf16 v[72:75], a[140:143], v[24:27], v[72:75]\n"
    "ds_read_b128 v[24:27], v0 offset:8192\n"
    "v_add_f32 v47, 1.0, v47\n"
    "v_mfma_f32_16x16x32_bf16 v[32:35], a[144:147], v[16:19], v[32:35]\n"
    "buffer_load_dwordx4 v1, s[36:39], s76 offen sc1 lds\n"
    "v_rcp_f32 v47, v47\n"
    "v_mfma_f32_16x16x32_bf16 v[36:39], a[148:151], v[16:19], v[36:39]\n"
    "s_add_u32 s76, s76, 16384\n"
    "v_mul_f32 v40, v40, v64\n"
    "v_mul_f32 v41, v41, v65\n"
    "v_mfma_f32_16x16x32_bf16 v[48:51], a[152:155], v[16:19], v[48:51]\n"
    "s_add_u32 m0, s59, 28672\n"
    "v_mul_f32 v42, v42, v66\n"
    "v_mul_f32 v43, v43, v67\n"
    "v_mfma_f32_16x16x32_bf16 v[56:59], a[156:159], v[16:19], v[56:59]\n"
    "buffer_load_dwordx4 v1, s[36:39], s76 offen sc1 lds\n"
    "v_mul_f32 v44, v44, v68\n"
    "v_mul_f32 v45, v45, v69\n"
    "v_mfma_f32_16x16x32_bf16 v[60:63], a[160:163], v[16:19], v[60:63]\n"
    "s_waitcnt lgkmcnt(1)\n"
    "v_mul_f32 v46, v46, v70\n"
    "v_mul_f32 v47, v47, v71\n"
    "v_mfma_f32_16x16x32_bf16 v[72:75], a[164:167], v[16:19], v[72:75]\n"
    "ds_read_b128 v[16:19], v0 offset:9216\n"
    "v_cvt_pk_bf16_f32 v40, v40, v41\n"
    "v_mfma_f32_16x16x32_bf16 v[32:35], a[168:171], v[20:23], v[32:35]\n"
    "s_add_u32 s76, s76, 16384\n"
    "v_cvt_pk_bf16_f32 v41, v42, v43\n"
    "v_cvt_pk_bf16_f32 v42, v44, v45\n"
    "v_mfma_f32_16x16x32_bf16 v[36:39], a[172:175], v[20:23], v[36:39]\n"
    "s_add_u32 m0, s59, 32768\n"
    "v_cvt_pk_bf16_f32 v43, v46, v47\n"
    "v_mfma_f32_16x16x32_bf16 v[48:51], a[176:179], v[20:23], v[48:51]\n"
    "buffer_load_dwordx4 v1, s[36:39], s76 offen sc1 lds\n"
    "s_add_u32 s76, s76, 16384\n"
    "v_mul_f32 v76, v52, v76\n"
    "v_mfma_f32_16x16x32_bf16 v[56:59], a[180:183], v[20:23], v[56:59]\n"
    "buffer_store_dwordx4 v[40:43], v15, s[36:39], s73 offen offset:1024 sc1\n"
    "v_mfma_f32_16x16x32_bf16 v[60:63], a[184:187], v[20:23], v[60:63]\n"
    "s_waitcnt lgkmcnt(1)\n"
    "s_add_u32 m0, s59, 36864\n"
    "v_mul_f32 v77, v53, v77\n"
    "v_mul_f32 v78, v54, v78\n"
    "v_mfma_f32_16x16x32_bf16 v[72:75], a[188:191], v[20:23], v[72:75]\n"
    "ds_read_b128 v[20:23], v0 offset:10240\n"
    "buffer_load_dwordx4 v8, s[36:39], s76 offen sc1 lds\n"
    "v_mfma_f32_16x16x32_bf16 v[32:35], a[192:195], v[24:27], v[32:35]\n"
    "v_mfma_f32_16x16x32_bf16 v[36:39], a[196:199], v[24:27], v[36:39]\n"
    "v_mul_f32 v79, v55, v79\n"
    "v_mul_f32 v52, 0xbfb8aa3b, v52\n"
    "v_mfma_f32_16x16x32_bf16 v[48:51], a[200:203], v[24:27], v[48:51]\n"
    "v_exp_f32 v52, v52\n"
    "v_mfma_f32_16x16x32_bf16 v[56:59], a[204:207], v[24:27], v[56:59]\n"
    "v_mul_f32 v53, 0xbfb8aa3b, v53\n"
    "v_mul_f32 v54, 0xbfb8aa3b, v54\n"
    "v_mfma_f32_16x16x32_bf16 v[60:63], a[208:211], v[24:27], v[60:63]\n"
    "s_waitcnt lgkmcnt(1)\n"
    "v_exp_f32 v53, v53\n"
    "v_mfma_f32_16x16x32_bf16 v[72:75], a[212:215], v[24:27], v[72:75]\n"
    "ds_read_b128 v[24:27], v0 offset:11264\n"
    "v_mul_f32 v55, 0xbfb8aa3b, v55\n"
    "v_mfma_f32_16x16x32_bf16 v[32:35], a[216:219], v[16:19], v[32:35]\n"
    "v_exp_f32 v54, v54\n"
    "v_mfma_f32_16x16x32_bf16 v[36:39], a[220:223], v[16:19], v[36:39]\n"
    "v_exp_f32 v55, v55\n"
    "v_mfma_f32_16x16x32_bf16 v[48:51], a[224:227], v[16:19], v[48:51]\n"
    "v_add_f32 v52, 1.0, v52\n"
    "v_add_f32 v53, 1.0, v53\n"
    "v_mfma_f32_16x16x32_bf16 v[56:59], a[228:231], v[16:19], v[56:59]\n"
    "v_rcp_f32 v52, v52\n"
    "v_mfma_f32_16x16x32_bf16 v[60:63], a[232:235], v[16:19], v[60:63]\n"
    "s_waitcnt lgkmcnt(1)\n"
    "v_rcp_f32 v53, v53\n"
    "v_mfma_f32_16x16x32_bf16 v[72:75], a[236:239], v[16:19], v[72:75]\n"
    "ds_read_b128 v[16:19], v0 offset:12288\n"
    "v_add_f32 v54, 1.0, v54\n"
    "v_mfma_f32_16x16x32_bf16 v[32:35], a[240:243], v[20:23], v[32:35]\n"
    "v_rcp_f32 v54, v54\n"
    "v_mfma_f32_16x16x32_bf16 v[36:39], a[244:247], v[20:23], v[36:39]\n"
    "v_add_f32 v55, 1.0, v55\n"
    "v_mfma_f32_16x16x32_bf16 v[48:51], a[248:251], v[20:23], v[48:51]\n"
    "v_mul_f32 v52, v52, v76\n"
    "v_mfma_f32_16x16x32_bf16 v[56:59], a[252:255], v[20:23], v[56:59]\n"
    "v_rcp_f32 v55, v55\n"
    "v_mfma_f32_16x16x32_bf16 v[60:63], v[80:83], v[20:23], v[60:63]\n"
    "s_waitcnt lgkmcnt(1)\n"
    "v_mul_f32 v53, v53, v77\n"
    "v_mul_f32 v54, v54, v78\n"
    "v_mfma_f32_16x16x32_bf16 v[72:75], v[84:87], v[20:23], v[72:75]\n"
    "ds_read_b128 v[20:23], v0 offset:13312\n"
    "v_mfma_f32_16x16x32_bf16 v[32:35], v[88:91], v[24:27], v[32:35]\n"
    "v_mfma_f32_16x16x32_bf16 v[36:39], v[92:95], v[24:27], v[36:39]\n"
    "v_mul_f32 v55, v55, v79\n"
    "v_cvt_pk_bf16_f32 v52, v52, v53\n"
    "v_mfma_f32_16x16x32_bf16 v[48:51], v[96:99], v[24:27], v[48:51]\n"
    "v_cvt_pk_bf16_f32 v53, v54, v55\n"
    "s_nop 0\n"
    "v_mfma_f32_16x16x32_bf16 v[56:59], v[100:103], v[24:27], v[56:59]\n"
    "v_permlane16_swap_b32 v52, v54\n"
    "v_mfma_f32_16x16x32_bf16 v[60:63], v[104:107], v[24:27], v[60:63]\n"
    "s_waitcnt lgkmcnt(1)\n"
    "v_permlane16_swap_b32 v53, v55\n"
    "v_mfma_f32_16x16x32_bf16 v[72:75], v[108:111], v[24:27], v[72:75]\n"
    "ds_read_b128 v[24:27], v0 offset:14336\n"
    "s_branch .LgUP_join0_%=\n"
    ".LgUP_dma0_48_%=:\n"
    "v_mfma_f32_16x16x32_bf16 v[32:35], a[192:195], v[24:27], v[32:35]\n"
    "s_add_u32 m0, s59, 20480\n"
    "v_mfma_f32_16x16x32_bf16 v[36:39], a[196:199], v[24:27], v[36:39]\n"
    "buffer_load_dwordx4 v1, s[36:39], s76 offen sc1 lds\n"
    "v_mul_f32 v79, v55, v79\n"
    "v_mul_f32 v52, 0xbfb8aa3b, v52\n"
    "v_mfma_f32_16x16x32_bf16 v[48:51], a[200:203], v[24:27], v[48:51]\n"
    "s_add_u32 s76, s76, 16384\n"
    "v_exp_f32 v52, v52\n"
    "v_mfma_f32_16x16x32_bf16 v[56:59], a[204:207], v[24:27], v[56:59]\n"
    "s_add_u32 m0, s59, 24576\n"
    "v_mul_f32 v53, 0xbfb8aa3b, v53\n"
    "v_mul_f32 v54, 0xbfb8aa3b, v54\n"
    "v_mfma_f32_16x16x32_bf16 v[60:63], a[208:211], v[24:27], v[60:63]\n"
    "s_waitcnt lgkmcnt(1)\n"
    "v_exp_f32 v53, v53\n"
    "v_mfma_f32_16x16x32_bf16 v[72:75], a[212:215], v[24:27], v[72:75]\n"
    "ds_read_b128 v[24:27], v0 offset:11264\n"
    "v_mul_f32 v55, 0xbfb8aa3b, v55\n"
    "v_mfma_f32_16x16x32_bf16 v[32:35], a[216:219], v[16:19], v[32:35]\n"
    "buffer_load_dwordx4 v1, s[36:39], s76 offen sc1 lds\n"
    "v_exp_f32 v54, v54\n"
    "v_mfma_f32_16x16x32_bf16 v[36:39], a[220:223], v[16:19], v[36:39]\n"
    "s_add_u32 s76, s76, 16384\n"
    "v_exp_f32 v55, v55\n"
    "v_mfma_f32_16x16x32_bf16 v[48:51], a[224:227], v[16:19], v[48:51]\n"
    "s_add_u32 m0, s59, 28672\n"
    "v_add_f32 v52, 1.0, v52\n"
    "v_add_f32 v53, 1.0, v53\n"
    "v_mfma_f32_16x16x32_bf16 v[56:59], a[228:231], v[16:19], v[56:59]\n"
    "buffer_load_dwordx4 v1, s[36:39], s76 offen sc1 lds\n"
    "v_rcp_f32 v52, v52\n"
    "v_mfma_f32_16x16x32_bf16 v[60:63], a[232:235], v[16:19], v[60:63]\n"
    "s_waitcnt lgkmcnt(1)\n"
    "v_rcp_f32 v53, v53\n"
    "v_mfma_f32_16x16x32_bf16 v[72:75], a[236:239], v[16:19], v[72:75]\n"
    "ds_read_b128 v[16:19], v0 offset:12288\n"
    "v_add_f32 v54, 1.0, v54\n"
    "v_mfma_f32_16x16x32_bf16 v[32:35], a[240:243], v[20:23], v[32:35]\n"
    "s_add_u32 s76, s76, 16384\n"
    "v_rcp_f32 v54, v54\n"
    "v_mfma_f32_16x16x32_bf16 v[36:39], a[244:247], v[20:23], v[36:39]\n"
    "s_add_u32 m0, s59, 32768\n"
    "v_add_f32 v55, 1.0, v55\n"
    "v_mfma_f32_16x16x32_bf16 v[48:51], a[248:251], v[20:23], v[48:51]\n"
    "buffer_load_dwordx4 v1, s[36:39], s76 offen sc1 lds\n"
    "s_add_u32 s76, s76, 16384\n"
    "v_mul_f32 v52, v52, v76\n"
    "v_mfma_f32_16x16x32_bf16 v[56:59], a[252:255], v[20:23], v[56:59]\n"
    "s_add_u32 m0, s59, 36864\n"
    "v_rcp_f32 v55, v55\n"
    "v_mfma_f32_16x16x32_bf16 v[60:63], v[80:83], v[20:23], v[60:63]\n"
    "s_waitcnt lgkmcnt(1)\n"
    "v_mul_f32 v53, v53, v77\n"
    "v_mul_f32 v54, v54, v78\n"
    "v_mfma_f32_16x16x32_bf16 v[72:75], v[84:87], v[20:23], v[72:75]\n"
    "ds_read_b128 v[20:23], v0 offset:13312\n"
    "buffer_load_dwordx4 v8, s[36:39], s76 offen sc1 lds\n"
    "v_mfma_f32_16x16x32_bf16 v[32:35], v[88:91], v[24:27], v[32:35]\n"
    "v_mfma_f32_16x16x32_bf16 v[36:39], v[92:95], v[24:27], v[36:39]\n"
    "v_mul_f32 v55, v55, v79\n"
    "v_cvt_pk_bf16_f32 v52, v52, v53\n"
    "v_mfma_f32_16x16x32_bf16 v[48:51], v[96:99], v[24:27], v[48:51]\n"
    "v_cvt_pk_bf16_f32 v53, v54, v55\n"
    "s_nop 0\n"
    "v_mfma_f32_16x16x32_bf16 v[56:59], v[100:103], v[24:27], v[56:59]\n"
    "v_permlane16_swap_b32 v52, v54\n"
    "v_mfma_f32_16x16x32_bf16 v[60:63], v[104:107], v[24:27], v[60:63]\n"
    "s_waitcnt lgkmcnt(1)\n"
    "v_permlane16_swap_b32 v53, v55\n"
    "v_mfma_f32_16x16x32_bf16 v[72:75], v[108:111], v[24:27], v[72:75]\n"
    "ds_read_b128 v[24:27], v0 offset:14336\n"
    "s_branch .LgUP_join0_%=\n"
    ".LgUP_dma0_66_%=:\n"
    "v_mfma_f32_16x16x32_bf16 v[32:35], v[88:91], v[24:27], v[32:35]\n"
    "s_add_u32 m0, s59, 20480\n"
    "v_mfma_f32_16x16x32_bf16 v[36:39], v[92:95], v[24:27], v[36:39]\n"
    "buffer_load_dwordx4 v1, s[36:39], s76 offen sc1 lds\n"
    "v_mul_f32 v55, v55, v79\n"
    "v_cvt_pk_bf16_f32 v52, v52, v53\n"
    "v_mfma_f32_16x16x32_bf16 v[48:51], v[96:99], v[24:27], v[48:51]\n"
    "s_add_u32 s76, s76, 16384\n"
    "v_cvt_pk_bf16_f32 v53, v54, v55\n"
    "s_nop 0\n"
    "v_mfma_f32_16x16x32_bf16 v[56:59], v[100:103], v[24:27], v[56:59]\n"
    "s_add_u32 m0, s59, 24576\n"
    "v_permlane16_swap_b32 v52, v54\n"
    "v_mfma_f32_16x16x32_bf16 v[60:63], v[104:107], v[24:27], v[60:63]\n"
    "s_waitcnt lgkmcnt(1)\n"
    "v_permlane16_swap_b32 v53, v55\n"
    "v_mfma_f32_16x16x32_bf16 v[72:75], v[108:111], v[24:27], v[72:75]\n"
    "ds_read_b128 v[24:27], v0 offset:14336\n"
    "buffer_load_dwordx4 v1, s[36:39], s76 offen sc1 lds\n"
    "v_mfma_f32_16x16x32_bf16 v[32:35], v[112:115], v[16:19], v[32:35]\n"
    "buffer_store_dwordx4 v[52:55], v4, s[36:39], s73 offen offset:1024 sc1\n"
    "v_mfma_f32_16x16x32_bf16 v[36:39], v[116:119], v[16:19], v[36:39]\n"
    "s_add_u32 s76, s76, 16384\n"
    "s_add_u32 m0, s59, 28672\n"
    "s_lshr_b32 s73, s68, 1\n"
    "v_mfma_f32_16x16x32_bf16 v[48:51], v[120:123], v[16:19], v[48:51]\n"
    "buffer_load_dwordx4 v1, s[36:39], s76 offen sc1 lds\n"
    "s_and_b32 s73, s73, s57\n"
    "v_mfma_f32_16x16x32_bf16 v[56:59], v[124:127], v[16:19], v[56:59]\n"
    "s_mul_i32 s73, s73, s56\n"
    "v_mfma_f32_16x16x32_bf16 v[60:63], v[128:131], v[16:19], v[60:63]\n"
    "s_waitcnt lgkmcnt(1)\n"
    "s_add_u32 s76, s76, 16384\n"
    "s_add_u32 s73, s73, s55\n"
    "v_mfma_f32_16x16x32_bf16 v[72:75], v[132:135], v[16:19], v[72:75]\n"
    "ds_read_b128 v[16:19], v0 offset:15360\n"
    "s_add_u32 m0, s59, 32768\n"
    "v_mfma_f32_16x16x32_bf16 v[32:35], v[136:139], v[20:23], v[32:35]\n"
    "buffer_load_dwordx4 v1, s[36:39], s76 offen sc1 lds\n"
    "v_mfma_f32_16x16x32_bf16 v[36:39], v[140:143], v[20:23], v[36:39]\n"
    "v_mfma_f32_16x16x32_bf16 v[48:51], v[144:147], v[20:23], v[48:51]\n"
    "s_add_u32 s76, s76, 16384\n"
    "s_add_u32 m0, s59, 36864\n"
    "v_mfma_f32_16x16x32_bf16 v[56:59], v[148:151], v[20:23], v[56:59]\n"
    "buffer_load_dwordx4 v8, s[36:39], s76 offen sc1 lds\n"
    "v_mfma_f32_16x16x32_bf16 v[60:63], v[152:155], v[20:23], v[60:63]\n"
    "s_waitcnt lgkmcnt(1)\n"
    "v_mfma_f32_16x16x32_bf16 v[72:75], v[156:159], v[20:23], v[72:75]\n"
    "ds_read_b128 v[20:23], v0 offset:16384\n"
    "v_mfma_f32_16x16x32_bf16 v[32:35], v[160:163], v[24:27], v[32:35]\n"
    "v_mfma_f32_16x16x32_bf16 v[36:39], v[164:167], v[24:27], v[36:39]\n"
    "v_mfma_f32_16x16x32_bf16 v[48:51], v[168:171], v[24:27], v[48:51]\n"
    "v_mfma_f32_16x16x32_bf16 v[56:59], v[172:175], v[24:27], v[56:59]\n"
    "v_mfma_f32_16x16x32_bf16 v[60:63], v[176:179], v[24:27], v[60:63]\n"
    "s_waitcnt lgkmcnt(1)\n"
    "v_mfma_f32_16x16x32_bf16 v[72:75], v[180:183], v[24:27], v[72:75]\n"
    "ds_read_b128 v[24:27], v0 offset:17408\n"
    "v_mfma_f32_16x16x32_bf16 v[32:35], v[184:187], v[16:19], v[32:35]\n"
    "v_mfma_f32_16x16x32_bf16 v[36:39], v[188:191], v[16:19], v[36:39]\n"
    "v_mfma_f32_16x16x32_bf16 v[48:51], v[192:195], v[16:19], v[48:51]\n"
    "v_mfma_f32_16x16x32_bf16 v[56:59], v[196:199], v[16:19], v[56:59]\n"
    "v_mfma_f32_16x16x32_bf16 v[60:63], v[200:203], v[16:19], v[60:63]\n"
    "s_waitcnt lgkmcnt(1)\n"
    "v_mfma_f32_16x16x32_bf16 v[72:75], v[204:207], v[16:19], v[72:75]\n"
    "v_mfma_f32_16x16x32_bf16 v[32:35], v[208:211], v[20:23], v[32:35]\n"
    "v_mfma_f32_16x16x32_bf16 v[36:39], v[212:215], v[20:23], v[36:39]\n"
    "v_mfma_f32_16x16x32_bf16 v[48:51], v[216:219], v[20:23], v[48:51]\n"
    "v_mfma_f32_16x16x32_bf16 v[56:59], v[220:223], v[20:23], v[56:59]\n"
    "v_mfma_f32_16x16x32_bf16 v[60:63], v[224:227], v[20:23], v[60:63]\n"
    "s_waitcnt lgkmcnt(0)\n"
    "v_mfma_f32_16x16x32_bf16 v[72:75], v[228:231], v[20:23], v[72:75]\n"
    "v_mfma_f32_16x16x32_bf16 v[32:35], v[232:235], v[24:27], v[32:35]\n"
    "v_mfma_f32_16x16x32_bf16 v[36:39], v[236:239], v[24:27], v[36:39]\n"
    "v_mfma_f32_16x16x32_bf16 v[48:51], v[240:243], v[24:27], v[48:51]\n"
    "s_waitcnt vmcnt(3)\n"
    "buffer_atomic_smax v12, v10, s[36:39], s70 offen sc1\n"
    "v_mfma_f32_16x16x32_bf16 v[56:59], v[244:247], v[24:27], v[56:59]\n"
    "v_mfma_f32_16x16x32_bf16 v[60:63], v[248:251], v[24:27], v[60:63]\n"
    "v_mfma_f32_16x16x32_bf16 v[72:75], v[252:255], v[24:27], v[72:75]\n"
    "v_pk_mul_f32 v[56:57], v[32:33], v[56:57]\n"
    "v_pk_mul_f32 v[58:59], v[34:35], v[58:59]\n"
    "s_nop 1\n"
    "v_pk_mul_f32 v[60:61], v[36:37], v[60:61]\n"
    "v_pk_mul_f32 v[62:63], v[38:39], v[62:63]\n"
    "v_pk_mul_f32 v[32:33], v[32:33], s[64:65] op_sel_hi:[1,0]\n"
    "v_pk_mul_f32 v[34:35], v[34:35], s[64:65] op_sel_hi:[1,0]\n"
    "v_pk_mul_f32 v[36:37], v[36:37], s[64:65] op_sel_hi:[1,0]\n"
    "v_pk_mul_f32 v[38:39], v[38:39], s[64:65] op_sel_hi:[1,0]\n"
    "v_exp_f32 v32, v32\n"
    "v_exp_f32 v33, v33\n"
    "v_exp_f32 v34, v34\n"
    "v_exp_f32 v35, v35\n"
    "v_exp_f32 v36, v36\n"
    "v_exp_f32 v37, v37\n"
    "v_exp_f32 v38, v38\n"
    "v_exp_f32 v39, v39\n"
    "v_pk_add_f32 v[32:33], v[32:33], 1.0 op_sel_hi:[1,0]\n"
    "v_pk_add_f32 v[34:35], v[34:35], 1.0 op_sel_hi:[1,0]\n"
    "v_pk_add_f32 v[36:37], v[36:37], 1.0 op_sel_hi:[1,0]\n"
    "v_pk_add_f32 v[38:39], v[38:39], 1.0 op_sel_hi:[1,0]\n"
    "v_rcp_f32 v32, v32\n"
    "v_rcp_f32 v33, v33\n"
    "v_rcp_f32 v34, v34\n"
    "v_rcp_f32 v35, v35\n"
    "v_rcp_f32 v36, v36\n"
    "v_rcp_f32 v37, v37\n"
    "v_rcp_f32 v38, v38\n"
    "v_rcp_f32 v39, v39\n"
    "v_pk_mul_f32 v[32:33], v[32:33], v[56:57]\n"
    "v_pk_mul_f32 v[34:35], v[34:35], v[58:59]\n"
    "v_pk_mul_f32 v[36:37], v[36:37], v[60:61]\n"
    "v_pk_mul_f32 v[38:39], v[38:39], v[62:63]\n"
    "v_cvt_pk_bf16_f32 v32, v32, v33\n"
    "v_cvt_pk_bf16_f32 v33, v34, v35\n"
    "v_cvt_pk_bf16_f32 v34, v36, v37\n"
    "v_cvt_pk_bf16_f32 v35, v38, v39\n"
    "buffer_store_dwordx4 v[32:35], v15, s[36:39], s73 offen sc1\n"
    "v_pk_mul_f32 v[72:73], v[48:49], v[72:73]\n"
    "v_pk_mul_f32 v[74:75], v[50:51], v[74:75]\n"
    "v_pk_mul_f32 v[48:49], v[48:49], s[64:65] op_sel_hi:[1,0]\n"
    "v_pk_mul_f32 v[50:51], v[50:51], s[64:65] op_sel_hi:[1,0]\n"
    "v_exp_f32 v48, v48\n"
    "v_exp_f32 v49, v49\n"
    "v_exp_f32 v50, v50\n"
    "v_exp_f32 v51, v51\n"
    "v_pk_add_f32 v[48:49], v[48:49], 1.0 op_sel_hi:[1,0]\n"
    "v_pk_add_f32 v[50:51], v[50:51], 1.0 op_sel_hi:[1,0]\n"
    "v_rcp_f32 v48, v48\n"
    "v_rcp_f32 v49, v49\n"
    "v_rcp_f32 v50, v50\n"
    "v_rcp_f32 v51, v51\n"
    "v_pk_mul_f32 v[48:49], v[48:49], v[72:73]\n"
    "v_pk_mul_f32 v[50:51], v[50:51], v[74:75]\n"
    "v_cvt_pk_bf16_f32 v48, v48, v49\n"
    "v_cvt_pk_bf16_f32 v49, v50, v51\n"
    "s_nop 0\n"
    "v_permlane16_swap_b32 v48, v50\n"
    "v_permlane16_swap_b32 v49, v51\n"
    "buffer_store_dwordx4 v[48:51], v4, s[36:39], s73 offen sc1\n"
    "v_mov_b32 v12, s68\n"
    "s_mov_b32 s73, 0x80000000\n"
    "s_mov_b32 s68, s71\n"
    "s_mov_b32 s69, 0\n"
    "s_mov_b32 s70, 0x80000000\n"
    "v_mov_b32 v7, v12\n"
    "s_waitcnt vmcnt(9)\n"
    "s_add_u32 s71, s68, 1\n"
    "v_add_u32 v13, v11, v3\n"
    "v_cmp_gt_i32 vcc, s71, v13\n"
    "s_nop 0\n"
    "s_or_b64 exec, vcc, s[62:63]\n"
    "buffer_atomic_smax v11, v2, s[36:39], 0 offen sc0 sc1\n"
    "s_mov_b64 exec, -1\n"
    "s_waitcnt vmcnt(4)\n"
    "s_branch .LgUP_bar1_%=\n"
    ".LgUP_dma0_84_%=:\n"
    "v_mfma_f32_16x16x32_bf16 v[32:35], v[160:163], v[24:27], v[32:35]\n"
    "s_add_u32 m0, s59, 20480\n"
    "v_mfma_f32_16x16x32_bf16 v[36:39], v[164:167], v[24:27], v[36:39]\n"
    "buffer_load_dwordx4 v1, s[36:39], s76 offen sc1 lds\n"
    "v_mfma_f32_16x16x32_bf16 v[48:51], v[168:171], v[24:27], v[48:51]\n"
    "s_add_u32 s76, s76, 16384\n"
    "s_add_u32 m0, s59, 24576\n"
    "v_mfma_f32_16x16x32_bf16 v[56:59], v[172:175], v[24:27], v[56:59]\n"
    "buffer_load_dwordx4 v1, s[36:39], s76 offen sc1 lds\n"
    "v_mfma_f32_16x16x32_bf16 v[60:63], v[176:179], v[24:27], v[60:63]\n"
    "s_waitcnt lgkmcnt(1)\n"
    "v_mfma_f32_16x16x32_bf16 v[72:75], v[180:183], v[24:27], v[72:75]\n"
    "ds_read_b128 v[24:27], v0 offset:17408\n"
    "v_mfma_f32_16x16x32_bf16 v[32:35], v[184:187], v[16:19], v[32:35]\n"
    "s_add_u32 s76, s76, 16384\n"
    "s_add_u32 m0, s59, 28672\n"
    "v_mfma_f32_16x16x32_bf16 v[36:39], v[188:191], v[16:19], v[36:39]\n"
    "buffer_load_dwordx4 v1, s[36:39], s76 offen sc1 lds\n"
    "v_mfma_f32_16x16x32_bf16 v[48:51], v[192:195], v[16:19], v[48:51]\n"
    "v_mfma_f32_16x16x32_bf16 v[56:59], v[196:199], v[16:19], v[56:59]\n"
    "v_mfma_f32_16x16x32_bf16 v[60:63], v[200:203], v[16:19], v[60:63]\n"
    "s_waitcnt lgkmcnt(1)\n"
    "s_add_u32 s76, s76, 16384\n"
    "s_add_u32 m0, s59, 32768\n"
    "v_mfma_f32_16x16x32_bf16 v[72:75], v[204:207], v[16:19], v[72:75]\n"
    "buffer_load_dwordx4 v1, s[36:39], s76 offen sc1 lds\n"
    "v_mfma_f32_16x16x32_bf16 v[32:35], v[208:211], v[20:23], v[32:35]\n"
    "v_mfma_f32_16x16x32_bf16 v[36:39], v[212:215], v[20:23], v[36:39]\n"
    "v_mfma_f32_16x16x32_bf16 v[48:51], v[216:219], v[20:23], v[48:51]\n"
    "s_add_u32 s76, s76, 16384\n"
    "s_add_u32 m0, s59, 36864\n"
    "v_mfma_f32_16x16x32_bf16 v[56:59], v[220:223], v[20:23], v[56:59]\n"
    "buffer_load_dwordx4 v8, s[36:39], s76 offen sc1 lds\n"
    "v_mfma_f32_16x16x32_bf16 v[60:63], v[224:227], v[20:23], v[60:63]\n"
    "s_waitcnt lgkmcnt(0)\n"
    "v_mfma_f32_16x16x32_bf16 v[72:75], v[228:231], v[20:23], v[72:75]\n"
    "v_mfma_f32_16x16x32_bf16 v[32:35], v[232:235], v[24:27], v[32:35]\n"
    "v_mfma_f32_16x16x32_bf16 v[36:39], v[236:239], v[24:27], v[36:39]\n"
    "v_mfma_f32_16x16x32_bf16 v[48:51], v[240:243], v[24:27], v[48:51]\n"
    "s_waitcnt vmcnt(5)\n"
    "buffer_atomic_smax v12, v10, s[36:39], s70 offen sc1\n"
    "v_mfma_f32_16x16x32_bf16 v[56:59], v[244:247], v[24:27], v[56:59]\n"
    "v_mfma_f32_16x16x32_bf16 v[60:63], v[248:251], v[24:27], v[60:63]\n"
    "v_mfma_f32_16x16x32_bf16 v[72:75], v[252:255], v[24:27], v[72:75]\n"
    "v_pk_mul_f32 v[56:57], v[32:33], v[56:57]\n"
    "v_pk_mul_f32 v[58:59], v[34:35], v[58:59]\n"
    "s_nop 1\n"
    "v_pk_mul_f32 v[60:61], v[36:37], v[60:61]\n"
    "v_pk_mul_f32 v[62:63], v[38:39], v[62:63]\n"
    "v_pk_mul_f32 v[32:33], v[32:33], s[64:65] op_sel_hi:[1,0]\n"
    "v_pk_mul_f32 v[34:35], v[34:35], s[64:65] op_sel_hi:[1,0]\n"
    "v_pk_mul_f32 v[36:37], v[36:37], s[64:65] op_sel_hi:[1,0]\n"
    "v_pk_mul_f32 v[38:39], v[38:39], s[64:65] op_sel_hi:[1,0]\n"
    "v_exp_f32 v32, v32\n"
    "v_exp_f32 v33, v33\n"
    "v_exp_f32 v34, v34\n"
    "v_exp_f32 v35, v35\n"
    "v_exp_f32 v36, v36\n"
    "v_exp_f32 v37, v37\n"
    "v_exp_f32 v38, v38\n"
    "v_exp_f32 v39, v39\n"
    "v_pk_add_f32 v[32:33], v[32:33], 1.0 op_sel_hi:[1,0]\n"
    "v_pk_add_f32 v[34:35], v[34:35], 1.0 op_sel_hi:[1,0]\n"
    "v_pk_add_f32 v[36:37], v[36:37], 1.0 op_sel_hi:[1,0]\n"
    "v_pk_add_f32 v[38:39], v[38:39], 1.0 op_sel_hi:[1,0]\n"
    "v_rcp_f32 v32, v32\n"
    "v_rcp_f32 v33, v33\n"
    "v_rcp_f32 v34, v34\n"
    "v_rcp_f32 v35, v35\n"
    "v_rcp_f32 v36, v36\n"
    "v_rcp_f32 v37, v37\n"
    "v_rcp_f32 v38, v38\n"
    "v_rcp_f32 v39, v39\n"
    "v_pk_mul_f32 v[32:33], v[32:33], v[56:57]\n"
    "v_pk_mul_f32 v[34:35], v[34:35], v[58:59]\n"
    "v_pk_mul_f32 v[36:37], v[36:37], v[60:61]\n"
    "v_pk_mul_f32 v[38:39], v[38:39], v[62:63]\n"
    "v_cvt_pk_bf16_f32 v32, v32, v33\n"
    "v_cvt_pk_bf16_f32 v33, v34, v35\n"
    "v_cvt_pk_bf16_f32 v34, v36, v37\n"
    "v_cvt_pk_bf16_f32 v35, v38, v39\n"
    "buffer_store_dwordx4 v[32:35], v15, s[36:39], s73 offen sc1\n"
    "v_pk_mul_f32 v[72:73], v[48:49], v[72:73]\n"
    "v_pk_mul_f32 v[74:75], v[50:51], v[74:75]\n"
    "v_pk_mul_f32 v[48:49], v[48:49], s[64:65] op_sel_hi:[1,0]\n"
    "v_pk_mul_f32 v[50:51], v[50:51], s[64:65] op_sel_hi:[1,0]\n"
    "v_exp_f32 v48, v48\n"
    "v_exp_f32 v49, v49\n"
    "v_exp_f32 v50, v50\n"
    "v_exp_f32 v51, v51\n"
    "v_pk_add_f32 v[48:49], v[48:49], 1.0 op_sel_hi:[1,0]\n"
    "v_pk_add_f32 v[50:51], v[50:51], 1.0 op_sel_hi:[1,0]\n"
    "v_rcp_f32 v48, v48\n"
    "v_rcp_f32 v49, v49\n"
    "v_rcp_f32 v50, v50\n"
    "v_rcp_f32 v51, v51\n"
    "v_pk_mul_f32 v[48:49], v[48:49], v[72:73]\n"
    "v_pk_mul_f32 v[50:51], v[50:51], v[74:75]\n"
    "v_cvt_pk_bf16_f32 v48, v48, v49\n"
    "v_cvt_pk_bf16_f32 v49, v50, v51\n"
    "s_nop 0\n"
    "v_permlane16_swap_b32 v48, v50\n"
    "v_permlane16_swap_b32 v49, v51\n"
    "buffer_store_dwordx4 v[48:51], v4, s[36:39], s73 offen sc1\n"
    "v_mov_b32 v12, s68\n"
    "s_mov_b32 s73, 0x80000000\n"
    "s_mov_b32 s68, s71\n"
    "s_mov_b32 s69, 0\n"
    "s_mov_b32 s70, 0x80000000\n"
    "v_mov_b32 v7, v12\n"
    "s_waitcnt vmcnt(9)\n"
    "s_add_u32 s71, s68, 1\n"
    "v_add_u32 v13, v11, v3\n"
    "v_cmp_gt_i32 vcc, s71, v13\n"
    "s_nop 0\n"
    "s_or_b64 exec, vcc, s[62:63]\n"
    "buffer_atomic_smax v11, v2, s[36:39], 0 offen sc0 sc1\n"
    "s_mov_b64 exec, -1\n"
    "s_waitcnt vmcnt(4)\n"
    "s_branch .LgUP_bar1_%=\n"
    ".LgUP_dma0_102_%=:\n"
    "v_mfma_f32_16x16x32_bf16 v[32:35], v[232:235], v[24:27], v[32:35]\n"
    "s_add_u32 m0, s59, 20480\n"
    "v_mfma_f32_16x16x32_bf16 v[36:39], v[236:239], v[24:27], v[36:39]\n"
    "buffer_load_dwordx4 v1, s[36:39], s76 offen sc1 lds\n"
    "v_mfma_f32_16x16x32_bf16 v[48:51], v[240:243], v[24:27], v[48:51]\n"
    "s_add_u32 s76, s76, 16384\n"
    "s_waitcnt vmcnt(2)\n"
    "buffer_atomic_smax v12, v10, s[36:39], s70 offen sc1\n"
    "v_mfma_f32_16x16x32_bf16 v[56:59], v[244:247], v[24:27], v[56:59]\n"
    "s_add_u32 m0, s59, 24576\n"
    "v_mfma_f32_16x16x32_bf16 v[60:63], v[248:251], v[24:27], v[60:63]\n"
    "buffer_load_dwordx4 v1, s[36:39], s76 offen sc1 lds\n"
    "v_mfma_f32_16x16x32_bf16 v[72:75], v[252:255], v[24:27], v[72:75]\n"
    "v_pk_mul_f32 v[56:57], v[32:33], v[56:57]\n"
    "v_pk_mul_f32 v[58:59], v[34:35], v[58:59]\n"
    "s_nop 1\n"
    "v_pk_mul_f32 v[60:61], v[36:37], v[60:61]\n"
    "v_pk_mul_f32 v[62:63], v[38:39], v[62:63]\n"
    "v_pk_mul_f32 v[32:33], v[32:33], s[64:65] op_sel_hi:[1,0]\n"
    "v_pk_mul_f32 v[34:35], v[34:35], s[64:65] op_sel_hi:[1,0]\n"
    "v_pk_mul_f32 v[36:37], v[36:37], s[64:65] op_sel_hi:[1,0]\n"
    "v_pk_mul_f32 v[38:39], v[38:39], s[64:65] op_sel_hi:[1,0]\n"
    "v_exp_f32 v32, v32\n"
    "v_exp_f32 v33, v33\n"
    "v_exp_f32 v34, v34\n"
    "v_exp_f32 v35, v35\n"
    "v_exp_f32 v36, v36\n"
    "v_exp_f32 v37, v37\n"
    "v_exp_f32 v38, v38\n"
    "v_exp_f32 v39, v39\n"
    "v_pk_add_f32 v[32:33], v[32:33], 1.0 op_sel_hi:[1,0]\n"
    "v_pk_add_f32 v[34:35], v[34:35], 1.0 op_sel_hi:[1,0]\n"
    "v_pk_add_f32 v[36:37], v[36:37], 1.0 op_sel_hi:[1,0]\n"
    "v_pk_add_f32 v[38:39], v[38:39], 1.0 op_sel_hi:[1,0]\n"
    "v_rcp_f32 v32, v32\n"
    "v_rcp_f32 v33, v33\n"
    "v_rcp_f32 v34, v34\n"
    "v_rcp_f32 v35, v35\n"
    "v_rcp_f32 v36, v36\n"
    "v_rcp_f32 v37, v37\n"
    "v_rcp_f32 v38, v38\n"
    "v_rcp_f32 v39, v39\n"
    "v_pk_mul_f32 v[32:33], v[32:33], v[56:57]\n"
    "v_pk_mul_f32 v[34:35], v[34:35], v[58:59]\n"
    "v_pk_mul_f32 v[36:37], v[36:37], v[60:61]\n"
    "v_pk_mul_f32 v[38:39], v[38:39], v[62:63]\n"
    "v_cvt_pk_bf16_f32 v32, v32, v33\n"
    "v_cvt_pk_bf16_f32 v33, v34, v35\n"
    "v_cvt_pk_bf16_f32 v34, v36, v37\n"
    "v_cvt_pk_bf16_f32 v35, v38, v39\n"
    "buffer_store_dwordx4 v[32:35], v15, s[36:39], s73 offen sc1\n"
    "v_pk_mul_f32 v[72:73], v[48:49], v[72:73]\n"
    "v_pk_mul_f32 v[74:75], v[50:51], v[74:75]\n"
    "v_pk_mul_f32 v[48:49], v[48:49], s[64:65] op_sel_hi:[1,0]\n"
    "v_pk_mul_f32 v[50:51], v[50:51], s[64:65] op_sel_hi:[1,0]\n"
    "v_exp_f32 v48, v48\n"
    "v_exp_f32 v49, v49\n"
    "v_exp_f32 v50, v50\n"
    "v_exp_f32 v51, v51\n"
    "v_pk_add_f32 v[48:49], v[48:49], 1.0 op_sel_hi:[1,0]\n"
    "v_pk_add_f32 v[50:51], v[50:51], 1.0 op_sel_hi:[1,0]\n"
    "v_rcp_f32 v48, v48\n"
    "v_rcp_f32 v49, v49\n"
    "v_rcp_f32 v50, v50\n"
    "v_rcp_f32 v51, v51\n"
    "v_pk_mul_f32 v[48:49], v[48:49], v[72:73]\n"
    "v_pk_mul_f32 v[50:51], v[50:51], v[74:75]\n"
    "v_cvt_pk_bf16_f32 v48, v48, v49\n"
    "v_cvt_pk_bf16_f32 v49, v50, v51\n"
    "s_nop 0\n"
    "v_permlane16_swap_b32 v48, v50\n"
    "v_permlane16_swap_b32 v49, v51\n"
    "buffer_store_dwordx4 v[48:51], v4, s[36:39], s73 offen sc1\n"
    "v_mov_b32 v12, s68\n"
    "s_mov_b32 s73, 0x80000000\n"
    "s_mov_b32 s68, s71\n"
    "s_add_u32 m0, s59, 28672\n"
    "s_add_u32 s76, s76, 16384\n"
    "buffer_load_dwordx4 v1, s[36:39], s76 offen sc1 lds\n"
    "s_add_u32 m0, s59, 32768\n"
    "s_add_u32 s76, s76, 16384\n"
    "buffer_load_dwordx4 v1, s[36:39], s76 offen sc1 lds\n"
    "s_add_u32 m0, s59, 36864\n"
    "s_add_u32 s76, s76, 16384\n"
    "buffer_load_dwordx4 v8, s[36:39], s76 offen sc1 lds\n"
    "s_waitcnt vmcnt(3)\n"
    "buffer_atomic_smax v12, v14, s[36:39], 0 offen sc1\n"
    "s_mov_b32 s69, 0x80000000\n"
    "s_mov_b32 s70, 0x80000000\n"
    "v_mov_b32 v7, v12\n"
    "s_waitcnt vmcnt(9)\n"
    "s_add_u32 s71, s68, 1\n"
    "v_add_u32 v13, v11, v3\n"
    "v_cmp_gt_i32 vcc, s71, v13\n"
    "s_nop 0\n"
    "s_or_b64 exec, vcc, s[62:63]\n"
    "buffer_atomic_smax v11, v2, s[36:39], 0 offen sc0 sc1\n"
    "s_mov_b64 exec, -1\n"
    "s_waitcnt vmcnt(2)\n"
    "s_branch .LgUP_bar1_%=\n"
    ".LgUP_idle0_%=:\n"
    "s_or_b64 exec, vcc, s[62:63]\n"
    "buffer_atomic_smax v11, v2, s[36:39], 0 offen sc0 sc1\n"
    "s_mov_b64 exec, -1\n"
    "s_waitcnt vmcnt(1)\n"
    "buffer_atomic_smax v12, v14, s[36:39], 0 offen sc1\n"
    "s_branch .LgUP_block0_0_%=\n"
    ".LgUP_block0_%=:\n"
    "v_add_u32 v13, v11, v3\n"
    "v_cmp_gt_i32 vcc, s71, v13\n"
    "s_nop 0\n"
    "s_or_b64 exec, vcc, s[62:63]\n"
    "buffer_atomic_smax v11, v2, s[36:39], 0 offen sc0 sc1\n"
    "s_mov_b64 exec, -1\n"
    "s_sleep 3\n"
    "s_waitcnt vmcnt(1)\n"
    ".LgUP_block0_0_%=:\n"
    "v_add_u32 v13, v5, v3\n"
    "v_cmp_gt_i32 vcc, s71, v13\n"
    "s_cbranch_vccz .LgUP_go0_%=\n"
    "v_add_u32 v13, v5, v3\n"
    "v_cmp_gt_i32 vcc, s71, v13\n"
    "s_nop 0\n"
    "s_or_b64 exec, vcc, s[62:63]\n"
    "buffer_atomic_smax v5, v2, s[36:39], 0 offen sc0 sc1\n"
    "s_mov_b64 exec, -1\n"
    "s_sleep 3\n"
    "s_waitcnt vmcnt(1)\n"
    ".LgUP_block0_1_%=:\n"
    "v_add_u32 v13, v11, v3\n"
    "v_cmp_gt_i32 vcc, s71, v13\n"
    "s_cbranch_vccz .LgUP_go0_%=\n"
    "s_sub_u32 s72, s72, 1\n"
    "s_cbranch_scc0 .LgUP_block0_%=\n"
    "s_mov_b32 s72, 0\n"
    ".LgUP_go0_%=:\n"
    "s_lshr_b32 s76, s71, 1\n"
    "s_and_b32 s76, s76, s54\n"
    "s_mul_i32 s76, s76, s53\n"
    "s_add_u32 s76, s76, s52\n"
    "s_add_u32 s76, s76, 1024\n"
    "s_add_u32 m0, s59, 20480\n"
    "s_nop 0\n"
    "buffer_load_dwordx4 v1, s[36:39], s76 offen sc1 lds\n"
    "s_add_u32 m0, s59, 24576\n"
    "s_add_u32 s76, s76, 16384\n"
    "buffer_load_dwordx4 v1, s[36:39], s76 offen sc1 lds\n"
    "s_add_u32 m0, s59, 28672\n"
    "s_add_u32 s76, s76, 16384\n"
    "buffer_load_dwordx4 v1, s[36:39], s76 offen sc1 lds\n"
    "s_add_u32 m0, s59, 32768\n"
    "s_add_u32 s76, s76, 16384\n"
    "buffer_load_dwordx4 v1, s[36:39], s76 offen sc1 lds\n"
    "s_add_u32 m0, s59, 36864\n"
    "s_add_u32 s76, s76, 16384\n"
    "buffer_load_dwordx4 v8, s[36:39], s76 offen sc1 lds\n"
    "s_mov_b32 s69, 0x80000000\n"
    "s_mov_b32 s70, 0x80000000\n"
    "v_mov_b32 v7, v12\n"
    "s_waitcnt vmcnt(5)\n"
    "s_add_u32 s71, s68, 1\n"
    "v_add_u32 v13, v11, v3\n"
    "v_cmp_gt_i32 vcc, s71, v13\n"
    "s_nop 0\n"
    "s_or_b64 exec, vcc, s[62:63]\n"
    "buffer_atomic_smax v11, v2, s[36:39], 0 offen sc0 sc1\n"
    "s_mov_b64 exec, -1\n"
    "s_waitcnt vmcnt(1)\n"
    "s_branch .LgUP_bar1_%=\n"
    ".LgUP_bar0_%=:\n"
    "s_barrier\n"
    "ds_read_b128 v[16:19], v0 offset:0\n"
    "ds_read_b128 v[20:23], v0 offset:1024\n"
    "ds_read_b128 v[24:27], v0 offset:2048\n"
    "v_add_u32 v13, v5, v3\n"
    "v_cmp_gt_i32 vcc, s71, v13\n"
    "s_nop 0\n"
    "s_or_b64 exec, vcc, s[62:63]\n"
    "buffer_atomic_smax v5, v2, s[36:39], 0 offen sc0 sc1\n"
    "s_mov_b64 exec, -1\n"
    "s_lshr_b32 s76, s71, 1\n"
    "s_and_b32 s76, s76, s54\n"
    "s_mul_i32 s76, s76, s53\n"
    "s_add_u32 s76, s76, s52\n"
    "s_add_u32 s76, s76, 1024\n"
    "s_waitcnt lgkmcnt(2)\n"
    "s_branch .LgUP_head0_%=\n"
    ".LgUP_poll1_%=:\n"
    "v_mfma_f32_16x16x32_bf16 v[40:43], a[48:51], v[24:27], v[40:43]\n"
    "s_or_b64 exec, vcc, s[62:63]\n"
    "buffer_atomic_smax v11, v2, s[36:39], 0 offen sc0 sc1\n"
    "s_mov_b64 exec, -1\n"
    "v_mfma_f32_16x16x32_bf16 v[44:47], a[52:55], v[24:27], v[44:47]\n"
    "v_mul_f32 v35, 0xbfb8aa3b, v35\n"
    "v_mul_f32 v36, 0xbfb8aa3b, v36\n"
    "v_mfma_f32_16x16x32_bf16 v[52:55], a[56:59], v[24:27], v[52:55]\n"
    "v_exp_f32 v35, v35\n"
    "v_mfma_f32_16x16x32_bf16 v[64:67], a[60:63], v[24:27], v[64:67]\n"
    "v_exp_f32 v36, v36\n"
    "v_mfma_f32_16x16x32_bf16 v[68:71], a[64:67], v[24:27], v[68:71]\n"
    "s_waitcnt lgkmcnt(1)\n"
    "v_mul_f32 v37, 0xbfb8aa3b, v37\n"
    "v_mul_f32 v38, 0xbfb8aa3b, v38\n"
    "v_mfma_f32_16x16x32_bf16 v[76:79], a[68:71], v[24:27], v[76:79]\n"
    "ds_read_b128 v[24:27], v9 offset:5120\n"
    "v_mul_f32 v39, 0xbfb8aa3b, v39\n"
    "v_mfma_f32_16x16x32_bf16 v[40:43], a[72:75], v[16:19], v[40:43]\n"
    "v_exp_f32 v37, v37\n"
    "v_mfma_f32_16x16x32_bf16 v[44:47], a[76:79], v[16:19], v[44:47]\n"
    "v_exp_f32 v38, v38\n"
    "v_mfma_f32_16x16x32_bf16 v[52:55], a[80:83], v[16:19], v[52:55]\n"
    "v_exp_f32 v39, v39\n"
    "v_mfma_f32_16x16x32_bf16 v[64:67], a[84:87], v[16:19], v[64:67]\n"
    "v_add_f32 v32, 1.0, v32\n"
    "v_add_f32 v33, 1.0, v33\n"
    "v_mfma_f32_16x16x32_bf16 v[68:71], a[88:91], v[16:19], v[68:71]\n"
    "s_waitcnt lgkmcnt(1)\n"
    "v_rcp_f32 v32, v32\n"
    "v_mfma_f32_16x16x32_bf16 v[76:79], a[92:95], v[16:19], v[76:79]\n"
    "ds_read_b128 v[16:19], v9 offset:6144\n"
    "v_add_f32 v34, 1.0, v34\n"
    "v_mfma_f32_16x16x32_bf16 v[40:43], a[96:99], v[20:23], v[40:43]\n"
    "v_rcp_f32 v33, v33\n"
    "v_mfma_f32_16x16x32_bf16 v[44:47], a[100:103], v[20:23], v[44:47]\n"
    "s_waitcnt vmcnt(2)\n"
    "v_add_f32 v35, 1.0, v35\n"
    "v_add_u32 v13, v5, v3\n"
    "v_mfma_f32_16x16x32_bf16 v[52:55], a[104:107], v[20:23], v[52:55]\n"
    "v_add_f32 v36, 1.0, v36\n"
    "v_cmp_gt_i32 vcc, s71, v13\n"
    "v_mfma_f32_16x16x32_bf16 v[64:67], a[108:111], v[20:23], v[64:67]\n"
    "v_rcp_f32 v34, v34\n"
    "v_mfma_f32_16x16x32_bf16 v[68:71], a[112:115], v[20:23], v[68:71]\n"
    "s_waitcnt lgkmcnt(1)\n"
    "v_rcp_f32 v35, v35\n"
    "v_mfma_f32_16x16x32_bf16 v[76:79], a[116:119], v[20:23], v[76:79]\n"
    "ds_read_b128 v[20:23], v9 offset:7168\n"
    "s_cbranch_vccz .LgUP_dma1_30_%=\n"
    "v_mfma_f32_16x16x32_bf16 v[40:43], a[120:123], v[24:27], v[40:43]\n"
    "s_or_b64 exec, vcc, s[62:63]\n"
    "buffer_atomic_smax v5, v2, s[36:39], 0 offen sc0 sc1\n"
    "s_mov_b64 exec, -1\n"
    "v_mfma_f32_16x16x32_bf16 v[44:47], a[124:127], v[24:27], v[44:47]\n"
    "v_rcp_f32 v36, v36\n"
    "v_mfma_f32_16x16x32_bf16 v[52:55], a[128:131], v[24:27], v[52:55]\n"
    "v_add_f32 v37, 1.0, v37\n"
    "v_add_f32 v38, 1.0, v38\n"
    "v_mfma_f32_16x16x32_bf16 v[64:67], a[132:135], v[24:27], v[64:67]\n"
    "v_rcp_f32 v37, v37\n"
    "v_mfma_f32_16x16x32_bf16 v[68:71], a[136:139], v[24:27], v[68:71]\n"
    "s_waitcnt lgkmcnt(1)\n"
    "v_rcp_f32 v38, v38\n"
    "v_mfma_f32_16x16x32_bf16 v[76:79], a[140:143], v[24:27], v[76:79]\n"
    "ds_read_b128 v[24:27], v9 offset:8192\n"
    "v_add_f32 v39, 1.0, v39\n"
    "v_mfma_f32_16x16x32_bf16 v[40:43], a[144:147], v[16:19], v[40:43]\n"
    "v_rcp_f32 v39, v39\n"
    "v_mfma_f32_16x16x32_bf16 v[44:47], a[148:151], v[16:19], v[44:47]\n"
    "v_mul_f32 v32, v32, v56\n"
    "v_mul_f32 v33, v33, v57\n"
    "v_mfma_f32_16x16x32_bf16 v[52:55], a[152:155], v[16:19], v[52:55]\n"
    "v_mul_f32 v34, v34, v58\n"
    "v_mul_f32 v35, v35, v59\n"
    "v_mfma_f32_16x16x32_bf16 v[64:67], a[156:159], v[16:19], v[64:67]\n"
    "v_mul_f32 v36, v36, v60\n"
    "v_mul_f32 v37, v37, v61\n"
    "v_mfma_f32_16x16x32_bf16 v[68:71], a[160:163], v[16:19], v[68:71]\n"
    "s_waitcnt lgkmcnt(1)\n"
    "v_mul_f32 v38, v38, v62\n"
    "v_mul_f32 v39, v39, v63\n"
    "v_mfma_f32_16x16x32_bf16 v[76:79], a[164:167], v[16:19], v[76:79]\n"
    "ds_read_b128 v[16:19], v9 offset:9216\n"
    "v_cvt_pk_bf16_f32 v32, v32, v33\n"
    "v_mfma_f32_16x16x32_bf16 v[40:43], a[168:171], v[20:23], v[40:43]\n"
    "v_cvt_pk_bf16_f32 v33, v34, v35\n"
    "v_cvt_pk_bf16_f32 v34, v36, v37\n"
    "v_mfma_f32_16x16x32_bf16 v[44:47], a[172:175], v[20:23], v[44:47]\n"
    "s_waitcnt vmcnt(1)\n"
    "v_cvt_pk_bf16_f32 v35, v38, v39\n"
    "v_add_u32 v13, v11, v3\n"
    "v_mfma_f32_16x16x32_bf16 v[52:55], a[176:179], v[20:23], v[52:55]\n"
    "v_mul_f32 v72, v48, v72\n"
    "v_cmp_gt_i32 vcc, s71, v13\n"
    "v_mfma_f32_16x16x32_bf16 v[64:67], a[180:183], v[20:23], v[64:67]\n"
    "buffer_store_dwordx4 v[32:35], v15, s[36:39], s73 offen sc1\n"
    "v_mfma_f32_16x16x32_bf16 v[68:71], a[184:187], v[20:23], v[68:71]\n"
    "s_waitcnt lgkmcnt(1)\n"
    "v_mul_f32 v73, v49, v73\n"
    "v_mul_f32 v74, v50, v74\n"
    "v_mfma_f32_16x16x32_bf16 v[76:79], a[188:191], v[20:23], v[76:79]\n"
    "ds_read_b128 v[20:23], v9 offset:10240\n"
    "s_cbranch_vccz .LgUP_dma1_48_%=\n"
    "v_mfma_f32_16x16x32_bf16 v[40:43], a[192:195], v[24:27], v[40:43]\n"
    "s_or_b64 exec, vcc, s[62:63]\n"
    "buffer_atomic_smax v11, v2, s[36:39], 0 offen sc0 sc1\n"
    "s_mov_b64 exec, -1\n"
    "v_mfma_f32_16x16x32_bf16 v[44:47], a[196:199], v[24:27], v[44:47]\n"
    "v_mul_f32 v75, v51, v75\n"
    "v_mul_f32 v48, 0xbfb8aa3b, v48\n"
    "v_mfma_f32_16x16x32_bf16 v[52:55], a[200:203], v[24:27], v[52:55]\n"
    "v_exp_f32 v48, v48\n"
    "v_mfma_f32_16x16x32_bf16 v[64:67], a[204:207], v[24:27], v[64:67]\n"
    "v_mul_f32 v49, 0xbfb8aa3b, v49\n"
    "v_mul_f32 v50, 0xbfb8aa3b, v50\n"
    "v_mfma_f32_16x16x32_bf16 v[68:71], a[208:211], v[24:27], v[68:71]\n"
    "s_waitcnt lgkmcnt(1)\n"
    "v_exp_f32 v49, v49\n"
    "v_mfma_f32_16x16x32_bf16 v[76:79], a[212:215], v[24:27], v[76:79]\n"
    "ds_read_b128 v[24:27], v9 offset:11264\n"
    "v_mul_f32 v51, 0xbfb8aa3b, v51\n"
    "v_mfma_f32_16x16x32_bf16 v[40:43], a[216:219], v[16:19], v[40:43]\n"
    "v_exp_f32 v50, v50\n"
    "v_mfma_f32_16x16x32_bf16 v[44:47], a[220:223], v[16:19], v[44:47]\n"
    "v_exp_f32 v51, v51\n"
    "v_mfma_f32_16x16x32_bf16 v[52:55], a[224:227], v[16:19], v[52:55]\n"
    "v_add_f32 v48, 1.0, v48\n"
    "v_add_f32 v49, 1.0, v49\n"
    "v_mfma_f32_16x16x32_bf16 v[64:67], a[228:231], v[16:19], v[64:67]\n"
    "v_rcp_f32 v48, v48\n"
    "v_mfma_f32_16x16x32_bf16 v[68:71], a[232:235], v[16:19], v[68:71]\n"
    "s_waitcnt lgkmcnt(1)\n"
    "v_rcp_f32 v49, v49\n"
    "v_mfma_f32_16x16x32_bf16 v[76:79], a[236:239], v[16:19], v[76:79]\n"
    "ds_read_b128 v[16:19], v9 offset:12288\n"
    "v_add_f32 v50, 1.0, v50\n"
    "v_mfma_f32_16x16x32_bf16 v[40:43], a[240:243], v[20:23], v[40:43]\n"
    "v_rcp_f32 v50, v50\n"
    "v_mfma_f32_16x16x32_bf16 v[44:47], a[244:247], v[20:23], v[44:47]\n"
    "s_waitcnt vmcnt(2)\n"
    "v_add_f32 v51, 1.0, v51\n"
    "v_add_u32 v13, v5, v3\n"
    "v_mfma_f32_16x16x32_bf16 v[52:55], a[248:251], v[20:23], v[52:55]\n"
    "v_mul_f32 v48, v48, v72\n"
    "v_cmp_gt_i32 vcc, s71, v13\n"
    "v_mfma_f32_16x16x32_bf16 v[64:67], a[252:255], v[20:23], v[64:67]\n"
    "v_rcp_f32 v51, v51\n"
    "v_mfma_f32_16x16x32_bf16 v[68:71], v[80:83], v[20:23], v[68:71]\n"
    "s_waitcnt lgkmcnt(1)\n"
    "v_mul_f32 v49, v49, v73\n"
    "v_mul_f32 v50, v50, v74\n"
    "v_mfma_f32_16x16x32_bf16 v[76:79], v[84:87], v[20:23], v[76:79]\n"
    "ds_read_b128 v[20:23], v9 offset:13312\n"
    "s_cbranch_vccz .LgUP_dma1_66_%=\n"
    "v_mfma_f32_16x16x32_bf16 v[40:43], v[88:91], v[24:27], v[40:43]\n"
    "s_or_b64 exec, vcc, s[62:63]\n"
    "buffer_atomic_smax v5, v2, s[36:39], 0 offen sc0 sc1\n"
    "s_mov_b64 exec, -1\n"
    "v_mfma_f32_16x16x32_bf16 v[44:47], v[92:95], v[24:27], v[44:47]\n"
    "v_mul_f32 v51, v51, v75\n"
    "v_cvt_pk_bf16_f32 v48, v48, v49\n"
    "v_mfma_f32_16x16x32_bf16 v[52:55], v[96:99], v[24:27], v[52:55]\n"
    "v_cvt_pk_bf16_f32 v49, v50, v51\n"
    "s_nop 0\n"
    "v_mfma_f32_16x16x32_bf16 v[64:67], v[100:103], v[24:27], v[64:67]\n"
    "v_permlane16_swap_b32 v48, v50\n"
    "v_mfma_f32_16x16x32_bf16 v[68:71], v[104:107], v[24:27], v[68:71]\n"
    "s_waitcnt lgkmcnt(1)\n"
    "v_permlane16_swap_b32 v49, v51\n"
    "v_mfma_f32_16x16x32_bf16 v[76:79], v[108:111], v[24:27], v[76:79]\n"
    "ds_read_b128 v[24:27], v9 offset:14336\n"
    "v_mfma_f32_16x16x32_bf16 v[40:43], v[112:115], v[16:19], v[40:43]\n"
    "buffer_store_dwordx4 v[48:51], v4, s[36:39], s73 offen sc1\n"
    "v_mfma_f32_16x16x32_bf16 v[44:47], v[116:119], v[16:19], v[44:47]\n"
    "s_lshr_b32 s73, s68, 1\n"
    "v_mfma_f32_16x16x32_bf16 v[52:55], v[120:123], v[16:19], v[52:55]\n"
    "s_and_b32 s73, s73, s57\n"
    "v_mfma_f32_16x16x32_bf16 v[64:67], v[124:127], v[16:19], v[64:67]\n"
    "s_mul_i32 s73, s73, s56\n"
    "v_mfma_f32_16x16x32_bf16 v[68:71], v[128:131], v[16:19], v[68:71]\n"
    "s_waitcnt lgkmcnt(1)\n"
    "s_add_u32 s73, s73, s55\n"
    "v_mfma_f32_16x16x32_bf16 v[76:79], v[132:135], v[16:19], v[76:79]\n"
    "ds_read_b128 v[16:19], v9 offset:15360\n"
    "v_mfma_f32_16x16x32_bf16 v[40:43], v[136:139], v[20:23], v[40:43]\n"
    "v_mfma_f32_16x16x32_bf16 v[44:47], v[140:143], v[20:23], v[44:47]\n"
    "s_waitcnt vmcnt(2)\n"
    "v_add_u32 v13, v11, v3\n"
    "v_mfma_f32_16x16x32_bf16 v[52:55], v[144:147], v[20:23], v[52:55]\n"
    "v_cmp_gt_i32 vcc, s71, v13\n"
    "v_mfma_f32_16x16x32_bf16 v[64:67], v[148:151], v[20:23], v[64:67]\n"
    "v_mfma_f32_16x16x32_bf16 v[68:71], v[152:155], v[20:23], v[68:71]\n"
    "s_waitcnt lgkmcnt(1)\n"
    "v_mfma_f32_16x16x32_bf16 v[76:79], v[156:159], v[20:23], v[76:79]\n"
    "ds_read_b128 v[20:23], v9 offset:16384\n"
    "s_cbranch_vccz .LgUP_dma1_84_%=\n"
    "v_mfma_f32_16x16x32_bf16 v[40:43], v[160:163], v[24:27], v[40:43]\n"
    "s_or_b64 exec, vcc, s[62:63]\n"
    "buffer_atomic_smax v11, v2, s[36:39], 0 offen sc0 sc1\n"
    "s_mov_b64 exec, -1\n"
    "v_mfma_f32_16x16x32_bf16 v[44:47], v[164:167], v[24:27], v[44:47]\n"
    "v_mfma_f32_16x16x32_bf16 v[52:55], v[168:171], v[24:27], v[52:55]\n"
    "v_mfma_f32_16x16x32_bf16 v[64:67], v[172:175], v[24:27], v[64:67]\n"
    "v_mfma_f32_16x16x32_bf16 v[68:71], v[176:179], v[24:27], v[68:71]\n"
    "s_waitcnt lgkmcnt(1)\n"
    "v_mfma_f32_16x16x32_bf16 v[76:79], v[180:183], v[24:27], v[76:79]\n"
    "ds_read_b128 v[24:27], v9 offset:17408\n"
    "v_mfma_f32_16x16x32_bf16 v[40:43], v[184:187], v[16:19], v[40:43]\n"
    "v_mfma_f32_16x16x32_bf16 v[44:47], v[188:191], v[16:19], v[44:47]\n"
    "v_mfma_f32_16x16x32_bf16 v[52:55], v[192:195], v[16:19], v[52:55]\n"
    "v_mfma_f32_16x16x32_bf16 v[64:67], v[196:199], v[16:19], v[64:67]\n"
    "v_mfma_f32_16x16x32_bf16 v[68:71], v[200:203], v[16:19], v[68:71]\n"
    "s_waitcnt lgkmcnt(1)\n"
    "v_mfma_f32_16x16x32_bf16 v[76:79], v[204:207], v[16:19], v[76:79]\n"
    "v_mfma_f32_16x16x32_bf16 v[40:43], v[208:211], v[20:23], v[40:43]\n"
    "v_mfma_f32_16x16x32_bf16 v[44:47], v[212:215], v[20:23], v[44:47]\n"
    "s_waitcnt vmcnt(2)\n"
    "v_add_u32 v13, v5, v3\n"
    "v_mfma_f32_16x16x32_bf16 v[52:55], v[216:219], v[20:23], v[52:55]\n"
    "v_cmp_gt_i32 vcc, s71, v13\n"
    "v_mfma_f32_16x16x32_bf16 v[64:67], v[220:223], v[20:23], v[64:67]\n"
    "v_mfma_f32_16x16x32_bf16 v[68:71], v[224:227], v[20:23], v[68:71]\n"
    "s_waitcnt lgkmcnt(0)\n"
    "v_mfma_f32_16x16x32_bf16 v[76:79], v[228:231], v[20:23], v[76:79]\n"
    "s_cbranch_vccz .LgUP_dma1_102_%=\n"
    "v_mfma_f32_16x16x32_bf16 v[40:43], v[232:235], v[24:27], v[40:43]\n"
    "s_or_b64 exec, vcc, s[62:63]\n"
    "buffer_atomic_smax v5, v2, s[36:39], 0 offen sc0 sc1\n"
    "s_mov_b64 exec, -1\n"
    "v_mfma_f32_16x16x32_bf16 v[44:47], v[236:239], v[24:27], v[44:47]\n"
    "v_mfma_f32_16x16x32_bf16 v[52:55], v[240:243], v[24:27], v[52:55]\n"
    "s_waitcnt vmcnt(2)\n"
    "buffer_atomic_smax v12, v14, s[36:39], s70 offen sc1\n"
    "v_mfma_f32_16x16x32_bf16 v[64:67], v[244:247], v[24:27], v[64:67]\n"
    "v_mfma_f32_16x16x32_bf16 v[68:71], v[248:251], v[24:27], v[68:71]\n"
    "v_mfma_f32_16x16x32_bf16 v[76:79], v[252:255], v[24:27], v[76:79]\n"
    "v_pk_mul_f32 v[64:65], v[40:41], v[64:65]\n"
    "v_pk_mul_f32 v[66:67], v[42:43], v[66:67]\n"
    "s_nop 1\n"
    "v_pk_mul_f32 v[68:69], v[44:45], v[68:69]\n"
    "v_pk_mul_f32 v[70:71], v[46:47], v[70:71]\n"
    "v_pk_mul_f32 v[40:41], v[40:41], s[64:65] op_sel_hi:[1,0]\n"
    "v_pk_mul_f32 v[42:43], v[42:43], s[64:65] op_sel_hi:[1,0]\n"
    "v_pk_mul_f32 v[44:45], v[44:45], s[64:65] op_sel_hi:[1,0]\n"
    "v_pk_mul_f32 v[46:47], v[46:47], s[64:65] op_sel_hi:[1,0]\n"
    "v_exp_f32 v40, v40\n"
    "v_exp_f32 v41, v41\n"
    "v_exp_f32 v42, v42\n"
    "v_exp_f32 v43, v43\n"
    "v_exp_f32 v44, v44\n"
    "v_exp_f32 v45, v45\n"
    "v_exp_f32 v46, v46\n"
    "v_exp_f32 v47, v47\n"
    "v_pk_add_f32 v[40:41], v[40:41], 1.0 op_sel_hi:[1,0]\n"
    "v_pk_add_f32 v[42:43], v[42:43], 1.0 op_sel_hi:[1,0]\n"
    "v_pk_add_f32 v[44:45], v[44:45], 1.0 op_sel_hi:[1,0]\n"
    "v_pk_add_f32 v[46:47], v[46:47], 1.0 op_sel_hi:[1,0]\n"
    "v_rcp_f32 v40, v40\n"
    "v_rcp_f32 v41, v41\n"
    "v_rcp_f32 v42, v42\n"
    "v_rcp_f32 v43, v43\n"
    "v_rcp_f32 v44, v44\n"
    "v_rcp_f32 v45, v45\n"
    "v_rcp_f32 v46, v46\n"
    "v_rcp_f32 v47, v47\n"
    "v_pk_mul_f32 v[40:41], v[40:41], v[64:65]\n"
    "v_pk_mul_f32 v[42:43], v[42:43], v[66:67]\n"
    "v_pk_mul_f32 v[44:45], v[44:45], v[68:69]\n"
    "v_pk_mul_f32 v[46:47], v[46:47], v[70:71]\n"
    "v_cvt_pk_bf16_f32 v40, v40, v41\n"
    "v_cvt_pk_bf16_f32 v41, v42, v43\n"
    "v_cvt_pk_bf16_f32 v42, v44, v45\n"
    "v_cvt_pk_bf16_f32 v43, v46, v47\n"
    "buffer_store_dwordx4 v[40:43], v15, s[36:39], s73 offen offset:1024 sc1\n"
    "v_pk_mul_f32 v[76:77], v[52:53], v[76:77]\n"
    "v_pk_mul_f32 v[78:79], v[54:55], v[78:79]\n"
    "v_pk_mul_f32 v[52:53], v[52:53], s[64:65] op_sel_hi:[1,0]\n"
    "v_pk_mul_f32 v[54:55], v[54:55], s[64:65] op_sel_hi:[1,0]\n"
    "v_exp_f32 v52, v52\n"
    "v_exp_f32 v53, v53\n"
    "v_exp_f32 v54, v54\n"
    "v_exp_f32 v55, v55\n"
    "v_pk_add_f32 v[52:53], v[52:53], 1.0 op_sel_hi:[1,0]\n"
    "v_pk_add_f32 v[54:55], v[54:55], 1.0 op_sel_hi:[1,0]\n"
    "v_rcp_f32 v52, v52\n"
    "v_rcp_f32 v53, v53\n"
    "v_rcp_f32 v54, v54\n"
    "v_rcp_f32 v55, v55\n"
    "v_pk_mul_f32 v[52:53], v[52:53], v[76:77]\n"
    "v_pk_mul_f32 v[54:55], v[54:55], v[78:79]\n"
    "v_cvt_pk_bf16_f32 v52, v52, v53\n"
    "v_cvt_pk_bf16_f32 v53, v54, v55\n"
    "s_nop 0\n"
    "v_permlane16_swap_b32 v52, v54\n"
    "v_permlane16_swap_b32 v53, v55\n"
    "buffer_store_dwordx4 v[52:55], v4, s[36:39], s73 offen offset:1024 sc1\n"
    "v_mov_b32 v12, s68\n"
    "s_mov_b32 s73, 0x80000000\n"
    "s_mov_b32 s68, s71\n"
    "s_cmp_eq_u32 s68, 128\n"
    "s_cbranch_scc1 .LgUP_done_%=\n"
    "s_waitcnt vmcnt(4)\n"
    "v_add_u32 v13, v11, v3\n"
    "v_cmp_gt_i32 vcc, s71, v13\n"
    "s_cbranch_vccnz .LgUP_idle1_%=\n"
    "s_add_u32 m0, s59, 0\n"
    "s_nop 0\n"
    "buffer_load_dwordx4 v1, s[36:39], s76 offen sc1 lds\n"
    "s_add_u32 m0, s59, 4096\n"
    "s_add_u32 s76, s76, 16384\n"
    "buffer_load_dwordx4 v1, s[36:39], s76 offen sc1 lds\n"
    "s_add_u32 m0, s59, 8192\n"
    "s_add_u32 s76, s76, 16384\n"
    "buffer_load_dwordx4 v1, s[36:39], s76 offen sc1 lds\n"
    "s_add_u32 m0, s59, 12288\n"
    "s_add_u32 s76, s76, 16384\n"
    "buffer_load_dwordx4 v1, s[36:39], s76 offen sc1 lds\n"
    "s_add_u32 m0, s59, 16384\n"
    "s_add_u32 s76, s76, 16384\n"
    "buffer_load_dwordx4 v8, s[36:39], s76 offen sc1 lds\n"
    "s_waitcnt vmcnt(5)\n"
    "buffer_atomic_smax v12, v10, s[36:39], 0 offen sc1\n"
    "s_mov_b32 s69, 0x80000000\n"
    "s_mov_b32 s70, 0x80000000\n"
    "v_mov_b32 v7, v12\n"
    "s_waitcnt vmcnt(9)\n"
    "s_add_u32 s71, s68, 1\n"
    "v_add_u32 v13, v11, v3\n"
    "v_cmp_gt_i32 vcc, s71, v13\n"
    "s_nop 0\n"
    "s_or_b64 exec, vcc, s[62:63]\n"
    "buffer_atomic_smax v11, v2, s[36:39], 0 offen sc0 sc1\n"
    "s_mov_b64 exec, -1\n"
    "s_waitcnt vmcnt(2)\n"
    "s_branch .LgUP_bar0_%=\n"
    ".LgUP_dma1_30_%=:\n"
    "v_mfma_f32_16x16x32_bf16 v[40:43], a[120:123], v[24:27], v[40:43]\n"
    "s_add_u32 m0, s59, 0\n"
    "v_mfma_f32_16x16x32_bf16 v[44:47], a[124:127], v[24:27], v[44:47]\n"
    "buffer_load_dwordx4 v1, s[36:39], s76 offen sc1 lds\n"
    "v_rcp_f32 v36, v36\n"
    "v_mfma_f32_16x16x32_bf16 v[52:55], a[128:131], v[24:27], v[52:55]\n"
    "s_add_u32 s76, s76, 16384\n"
    "v_add_f32 v37, 1.0, v37\n"
    "v_add_f32 v38, 1.0, v38\n"
    "v_mfma_f32_16x16x32_bf16 v[64:67], a[132:135], v[24:27], v[64:67]\n"
    "s_add_u32 m0, s59, 4096\n"
    "v_rcp_f32 v37, v37\n"
    "v_mfma_f32_16x16x32_bf16 v[68:71], a[136:139], v[24:27], v[68:71]\n"
    "s_waitcnt lgkmcnt(1)\n"
    "v_rcp_f32 v38, v38\n"
    "v_mfma_f32_16x16x32_bf16 v[76:79], a[140:143], v[24:27], v[76:79]\n"
    "ds_read_b128 v[24:27], v9 offset:8192\n"
    "v_add_f32 v39, 1.0, v39\n"
    "v_mfma_f32_16x16x32_bf16 v[40:43], a[144:147], v[16:19], v[40:43]\n"
    "buffer_load_dwordx4 v1, s[36:39], s76 offen sc1 lds\n"
    "v_rcp_f32 v39, v39\n"
    "v_mfma_f32_16x16x32_bf16 v[44:47], a[148:151], v[16:19], v[44:47]\n"
    "s_add_u32 s76, s76, 16384\n"
    "v_mul_f32 v32, v32, v56\n"
    "v_mul_f32 v33, v33, v57\n"
    "v_mfma_f32_16x16x32_bf16 v[52:55], a[152:155], v[16:19], v[52:55]\n"
    "s_add_u32 m0, s59, 8192\n"
    "v_mul_f32 v34, v34, v58\n"
    "v_mul_f32 v35, v35, v59\n"
    "v_mfma_f32_16x16x32_bf16 v[64:67], a[156:159], v[16:19], v[64:67]\n"
    "buffer_load_dwordx4 v1, s[36:39], s76 offen sc1 lds\n"
    "v_mul_f32 v36, v36, v60\n"
    "v_mul_f32 v37, v37, v61\n"
    "v_mfma_f32_16x16x32_bf16 v[68:71], a[160:163], v[16:19], v[68:71]\n"
    "s_waitcnt lgkmcnt(1)\n"
    "v_mul_f32 v38, v38, v62\n"
    "v_mul_f32 v39, v39, v63\n"
    "v_mfma_f32_16x16x32_bf16 v[76:79], a[164:167], v[16:19], v[76:79]\n"
    "ds_read_b128 v[16:19], v9 offset:9216\n"
    "v_cvt_pk_bf16_f32 v32, v32, v33\n"
    "v_mfma_f32_16x16x32_bf16 v[40:43], a[168:171], v[20:23], v[40:43]\n"
    "s_add_u32 s76, s76, 16384\n"
    "v_cvt_pk_bf16_f32 v33, v34, v35\n"
    "v_cvt_pk_bf16_f32 v34, v36, v37\n"
    "v_mfma_f32_16x16x32_bf16 v[44:47], a[172:175], v[20:23], v[44:47]\n"
    "s_add_u32 m0, s59, 12288\n"
    "v_cvt_pk_bf16_f32 v35, v38, v39\n"
    "v_mfma_f32_16x16x32_bf16 v[52:55], a[176:179], v[20:23], v[52:55]\n"
    "buffer_load_dwordx4 v1, s[36:39], s76 offen sc1 lds\n"
    "s_add_u32 s76, s76, 16384\n"
    "v_mul_f32 v72, v48, v72\n"
    "v_mfma_f32_16x16x32_bf16 v[64:67], a[180:183], v[20:23], v[64:67]\n"
    "buffer_store_dwordx4 v[32:35], v15, s[36:39], s73 offen sc1\n"
    "v_mfma_f32_16x16x32_bf16 v[68:71], a[184:187], v[20:23], v[68:71]\n"
    "s_waitcnt lgkmcnt(1)\n"
    "s_add_u32 m0, s59, 16384\n"
    "v_mul_f32 v73, v49, v73\n"
    "v_mul_f32 v74, v50, v74\n"
    "v_mfma_f32_16x16x32_bf16 v[76:79], a[188:191], v[20:23], v[76:79]\n"
    "ds_read_b128 v[20:23], v9 offset:10240\n"
    "buffer_load_dwordx4 v8, s[36:39], s76 offen sc1 lds\n"
    "v_mfma_f32_16x16x32_bf16 v[40:43], a[192:195], v[24:27], v[40:43]\n"
    "v_mfma_f32_16x16x32_bf16 v[44:47], a[196:199], v[24:27], v[44:47]\n"
    "v_mul_f32 v75, v51, v75\n"
    "v_mul_f32 v48, 0xbfb8aa3b, v48\n"
    "v_mfma_f32_16x16x32_bf16 v[52:55], a[200:203], v[24:27], v[52:55]\n"
    "v_exp_f32 v48, v48\n"
    "v_mfma_f32_16x16x32_bf16 v[64:67], a[204:207], v[24:27], v[64:67]\n"
    "v_mul_f32 v49, 0xbfb8aa3b, v49\n"
    "v_mul_f32 v50, 0xbfb8aa3b, v50\n"
    "v_mfma_f32_16x16x32_bf16 v[68:71], a[208:211], v[24:27], v[68:71]\n"
    "s_waitcnt lgkmcnt(1)\n"
    "v_exp_f32 v49, v49\n"
    "v_mfma_f32_16x16x32_bf16 v[76:79], a[212:215], v[24:27], v[76:79]\n"
    "ds_read_b128 v[24:27], v9 offset:11264\n"
    "v_mul_f32 v51, 0xbfb8aa3b, v51\n"
    "v_mfma_f32_16x16x32_bf16 v[40:43], a[216:219], v[16:19], v[40:43]\n"
    "v_exp_f32 v50, v50\n"
    "v_mfma_f32_16x16x32_bf16 v[44:47], a[220:223], v[16:19], v[44:47]\n"
    "v_exp_f32 v51, v51\n"
    "v_mfma_f32_16x16x32_bf16 v[52:55], a[224:227], v[16:19], v[52:55]\n"
    "v_add_f32 v48, 1.0, v48\n"
    "v_add_f32 v49, 1.0, v49\n"
    "v_mfma_f32_16x16x32_bf16 v[64:67], a[228:231], v[16:19], v[64:67]\n"
    "v_rcp_f32 v48, v48\n"
    "v_mfma_f32_16x16x32_bf16 v[68:71], a[232:235], v[16:19], v[68:71]\n"
    "s_waitcnt lgkmcnt(1)\n"
    "v_rcp_f32 v49, v49\n"
    "v_mfma_f32_16x16x32_bf16 v[76:79], a[236:239], v[16:19], v[76:79]\n"
    "ds_read_b128 v[16:19], v9 offset:12288\n"
    "v_add_f32 v50, 1.0, v50\n"
    "v_mfma_f32_16x16x32_bf16 v[40:43], a[240:243], v[20:23], v[40:43]\n"
    "v_rcp_f32 v50, v50\n"
    "v_mfma_f32_16x16x32_bf16 v[44:47], a[244:247], v[20:23], v[44:47]\n"
    "v_add_f32 v51, 1.0, v51\n"
    "v_mfma_f32_16x16x32_bf16 v[52:55], a[248:251], v[20:23], v[52:55]\n"
    "v_mul_f32 v48, v48, v72\n"
    "v_mfma_f32_16x16x32_bf16 v[64:67], a[252:255], v[20:23], v[64:67]\n"
    "v_rcp_f32 v51, v51\n"
    "v_mfma_f32_16x16x32_bf16 v[68:71], v[80:83], v[20:23], v[68:71]\n"
    "s_waitcnt lgkmcnt(1)\n"
    "v_mul_f32 v49, v49, v73\n"
    "v_mul_f32 v50, v50, v74\n"
    "v_mfma_f32_16x16x32_bf16 v[76:79], v[84:87], v[20:23], v[76:79]\n"
    "ds_read_b128 v[20:23], v9 offset:13312\n"
    "v_mfma_f32_16x16x32_bf16 v[40:43], v[88:91], v[24:27], v[40:43]\n"
    "v_mfma_f32_16x16x32_bf16 v[44:47], v[92:95], v[24:27], v[44:47]\n"
    "v_mul_f32 v51, v51, v75\n"
    "v_cvt_pk_bf16_f32 v48, v48, v49\n"
    "v_mfma_f32_16x16x32_bf16 v[52:55], v[96:99], v[24:27], v[52:55]\n"
    "v_cvt_pk_bf16_f32 v49, v50, v51\n"
    "s_nop 0\n"
    "v_mfma_f32_16x16x32_bf16 v[64:67], v[100:103], v[24:27], v[64:67]\n"
    "v_permlane16_swap_b32 v48, v50\n"
    "v_mfma_f32_16x16x32_bf16 v[68:71], v[104:107], v[24:27], v[68:71]\n"
    "s_waitcnt lgkmcnt(1)\n"
    "v_permlane16_swap_b32 v49, v51\n"
    "v_mfma_f32_16x16x32_bf16 v[76:79], v[108:111], v[24:27], v[76:79]\n"
    "ds_read_b128 v[24:27], v9 offset:14336\n"
    "s_branch .LgUP_join1_%=\n"
    ".LgUP_dma1_48_%=:\n"
    "v_mfma_f32_16x16x32_bf16 v[40:43], a[192:195], v[24:27], v[40:43]\n"
    "s_add_u32 m0, s59, 0\n"
    "v_mfma_f32_16x16x32_bf16 v[44:47], a[196:199], v[24:27], v[44:47]\n"
    "buffer_load_dwordx4 v1, s[36:39], s76 offen sc1 lds\n"
    "v_mul_f32 v75, v51, v75\n"
    "v_mul_f32 v48, 0xbfb8aa3b, v48\n"
    "v_mfma_f32_16x16x32_bf16 v[52:55], a[200:203], v[24:27], v[52:55]\n"
    "s_add_u32 s76, s76, 16384\n"
    "v_exp_f32 v48, v48\n"
    "v_mfma_f32_16x16x32_bf16 v[64:67], a[204:207], v[24:27], v[64:67]\n"
    "s_add_u32 m0, s59, 4096\n"
    "v_mul_f32 v49, 0xbfb8aa3b, v49\n"
    "v_mul_f32 v50, 0xbfb8aa3b, v50\n"
    "v_mfma_f32_16x16x32_bf16 v[68:71], a[208:211], v[24:27], v[68:71]\n"
    "s_waitcnt lgkmcnt(1)\n"
    "v_exp_f32 v49, v49\n"
    "v_mfma_f32_16x16x32_bf16 v[76:79], a[212:215], v[24:27], v[76:79]\n"
    "ds_read_b128 v[24:27], v9 offset:11264\n"
    "v_mul_f32 v51, 0xbfb8aa3b, v51\n"
    "v_mfma_f32_16x16x32_bf16 v[40:43], a[216:219], v[16:19], v[40:43]\n"
    "buffer_load_dwordx4 v1, s[36:39], s76 offen sc1 lds\n"
    "v_exp_f32 v50, v50\n"
    "v_mfma_f32_16x16x32_bf16 v[44:47], a[220:223], v[16:19], v[44:47]\n"
    "s_add_u32 s76, s76, 16384\n"
    "v_exp_f32 v51, v51\n"
    "v_mfma_f32_16x16x32_bf16 v[52:55], a[224:227], v[16:19], v[52:55]\n"
    "s_add_u32 m0, s59, 8192\n"
    "v_add_f32 v48, 1.0, v48\n"
    "v_add_f32 v49, 1.0, v49\n"
    "v_mfma_f32_16x16x32_bf16 v[64:67], a[228:231], v[16:19], v[64:67]\n"
    "buffer_load_dwordx4 v1, s[36:39], s76 offen sc1 lds\n"
    "v_rcp_f32 v48, v48\n"
    "v_mfma_f32_16x16x32_bf16 v[68:71], a[232:235], v[16:19], v[68:71]\n"
    "s_waitcnt lgkmcnt(1)\n"
    "v_rcp_f32 v49, v49\n"
    "v_mfma_f32_16x16x32_bf16 v[76:79], a[236:239], v[16:19], v[76:79]\n"
    "ds_read_b128 v[16:19], v9 offset:12288\n"
    "v_add_f32 v50, 1.0, v50\n"
    "v_mfma_f32_16x16x32_bf16 v[40:43], a[240:243], v[20:23], v[40:43]\n"
    "s_add_u32 s76, s76, 16384\n"
    "v_rcp_f32 v50, v50\n"
    "v_mfma_f32_16x16x32_bf16 v[44:47], a[244:247], v[20:23], v[44:47]\n"
    "s_add_u32 m0, s59, 12288\n"
    "v_add_f32 v51, 1.0, v51\n"
    "v_mfma_f32_16x16x32_bf16 v[52:55], a[248:251], v[20:23], v[52:55]\n"
    "buffer_load_dwordx4 v1, s[36:39], s76 offen sc1 lds\n"
    "s_add_u32 s76, s76, 16384\n"
    "v_mul_f32 v48, v48, v72\n"
    "v_mfma_f32_16x16x32_bf16 v[64:67], a[252:255], v[20:23], v[64:67]\n"
    "s_add_u32 m0, s59, 16384\n"
    "v_rcp_f32 v51, v51\n"
    "v_mfma_f32_16x16x32_bf16 v[68:71], v[80:83], v[20:23], v[68:71]\n"
    "s_waitcnt lgkmcnt(1)\n"
    "v_mul_f32 v49, v49, v73\n"
    "v_mul_f32 v50, v50, v74\n"
    "v_mfma_f32_16x16x32_bf16 v[76:79], v[84:87], v[20:23], v[76:79]\n"
    "ds_read_b128 v[20:23], v9 offset:13312\n"
    "buffer_load_dwordx4 v8, s[36:39], s76 offen sc1 lds\n"
    "v_mfma_f32_16x16x32_bf16 v[40:43], v[88:91], v[24:27], v[40:43]\n"
    "v_mfma_f32_16x16x32_bf16 v[44:47], v[92:95], v[24:27], v[44:47]\n"
    "v_mul_f32 v51, v51, v75\n"
    "v_cvt_pk_bf16_f32 v48, v48, v49\n"
    "v_mfma_f32_16x16x32_bf16 v[52:55], v[96:99], v[24:27], v[52:55]\n"
    "v_cvt_pk_bf16_f32 v49, v50, v51\n"
    "s_nop 0\n"
    "v_mfma_f32_16x16x32_bf16 v[64:67], v[100:103], v[24:27], v[64:67]\n"
    "v_permlane16_swap_b32 v48, v50\n"
    "v_mfma_f32_16x16x32_bf16 v[68:71], v[104:107], v[24:27], v[68:71]\n"
    "s_waitcnt lgkmcnt(1)\n"
    "v_permlane16_swap_b32 v49, v51\n"
    "v_mfma_f32_16x16x32_bf16 v[76:79], v[108:111], v[24:27], v[76:79]\n"
    "ds_read_b128 v[24:27], v9 offset:14336\n"
    "s_branch .LgUP_join1_%=\n"
    ".LgUP_dma1_66_%=:\n"
    "v_mfma_f32_16x16x32_bf16 v[40:43], v[88:91], v[24:27], v[40:43]\n"
    "s_add_u32 m0, s59, 0\n"
    "v_mfma_f32_16x16x32_bf16 v[44:47], v[92:95], v[24:27], v[44:47]\n"
    "buffer_load_dwordx4 v1, s[36:39], s76 offen sc1 lds\n"
    "v_mul_f32 v51, v51, v75\n"
    "v_cvt_pk_bf16_f32 v48, v48, v49\n"
    "v_mfma_f32_16x16x32_bf16 v[52:55], v[96:99], v[24:27], v[52:55]\n"
    "s_add_u32 s76, s76, 16384\n"
    "v_cvt_pk_bf16_f32 v49, v50, v51\n"
    "s_nop 0\n"
    "v_mfma_f32_16x16x32_bf16 v[64:67], v[100:103], v[24:27], v[64:67]\n"
    "s_add_u32 m0, s59, 4096\n"
    "v_permlane16_swap_b32 v48, v50\n"
    "v_mfma_f32_16x16x32_bf16 v[68:71], v[104:107], v[24:27], v[68:71]\n"
    "s_waitcnt lgkmcnt(1)\n"
    "v_permlane16_swap_b32 v49, v51\n"
    "v_mfma_f32_16x16x32_bf16 v[76:79], v[108:111], v[24:27], v[76:79]\n"
    "ds_read_b128 v[24:27], v9 offset:14336\n"
    "buffer_load_dwordx4 v1, s[36:39], s76 offen sc1 lds\n"
    "v_mfma_f32_16x16x32_bf16 v[40:43], v[112:115], v[16:19], v[40:43]\n"
    "buffer_store_dwordx4 v[48:51], v4, s[36:39], s73 offen sc1\n"
    "v_mfma_f32_16x16x32_bf16 v[44:47], v[116:119], v[16:19], v[44:47]\n"
    "s_add_u32 s76, s76, 16384\n"
    "s_add_u32 m0, s59, 8192\n"
    "s_lshr_b32 s73, s68, 1\n"
    "v_mfma_f32_16x16x32_bf16 v[52:55], v[120:123], v[16:19], v[52:55]\n"
    "buffer_load_dwordx4 v1, s[36:39], s76 offen sc1 lds\n"
    "s_and_b32 s73, s73, s57\n"
    "v_mfma_f32_16x16x32_bf16 v[64:67], v[124:127], v[16:19], v[64:67]\n"
    "s_mul_i32 s73, s73, s56\n"
    "v_mfma_f32_16x16x32_bf16 v[68:71], v[128:131], v[16:19], v[68:71]\n"
    "s_waitcnt lgkmcnt(1)\n"
    "s_add_u32 s76, s76, 16384\n"
    "s_add_u32 s73, s73, s55\n"
    "v_mfma_f32_16x16x32_bf16 v[76:79], v[132:135], v[16:19], v[76:79]\n"
    "ds_read_b128 v[16:19], v9 offset:15360\n"
    "s_add_u32 m0, s59, 12288\n"
    "v_mfma_f32_16x16x32_bf16 v[40:43], v[136:139], v[20:23], v[40:43]\n"
    "buffer_load_dwordx4 v1, s[36:39], s76 offen sc1 lds\n"
    "v_mfma_f32_16x16x32_bf16 v[44:47], v[140:143], v[20:23], v[44:47]\n"
    "v_mfma_f32_16x16x32_bf16 v[52:55], v[144:147], v[20:23], v[52:55]\n"
    "s_add_u32 s76, s76, 16384\n"
    "s_add_u32 m0, s59, 16384\n"
    "v_mfma_f32_16x16x32_bf16 v[64:67], v[148:151], v[20:23], v[64:67]\n"
    "buffer_load_dwordx4 v8, s[36:39], s76 offen sc1 lds\n"
    "v_mfma_f32_16x16x32_bf16 v[68:71], v[152:155], v[20:23], v[68:71]\n"
    "s_waitcnt lgkmcnt(1)\n"
    "v_mfma_f32_16x16x32_bf16 v[76:79], v[156:159], v[20:23], v[76:79]\n"
    "ds_read_b128 v[20:23], v9 offset:16384\n"
    "v_mfma_f32_16x16x32_bf16 v[40:43], v[160:163], v[24:27], v[40:43]\n"
    "v_mfma_f32_16x16x32_bf16 v[44:47], v[164:167], v[24:27], v[44:47]\n"
    "v_mfma_f32_16x16x32_bf16 v[52:55], v[168:171], v[24:27], v[52:55]\n"
    "v_mfma_f32_16x16x32_bf16 v[64:67], v[172:175], v[24:27], v[64:67]\n"
    "v_mfma_f32_16x16x32_bf16 v[68:71], v[176:179], v[24:27], v[68:71]\n"
    "s_waitcnt lgkmcnt(1)\n"
    "v_mfma_f32_16x16x32_bf16 v[76:79], v[180:183], v[24:27], v[76:79]\n"
    "ds_read_b128 v[24:27], v9 offset:17408\n"
    "v_mfma_f32_16x16x32_bf16 v[40:43], v[184:187], v[16:19], v[40:43]\n"
    "v_mfma_f32_16x16x32_bf16 v[44:47], v[188:191], v[16:19], v[44:47]\n"
    "v_mfma_f32_16x16x32_bf16 v[52:55], v[192:195], v[16:19], v[52:55]\n"
    "v_mfma_f32_16x16x32_bf16 v[64:67], v[196:199], v[16:19], v[64:67]\n"
    "v_mfma_f32_16x16x32_bf16 v[68:71], v[200:203], v[16:19], v[68:71]\n"
    "s_waitcnt lgkmcnt(1)\n"
    "v_mfma_f32_16x16x32_bf16 v[76:79], v[204:207], v[16:19], v[76:79]\n"
    "v_mfma_f32_16x16x32_bf16 v[40:43], v[208:211], v[20:23], v[40:43]\n"
    "v_mfma_f32_16x16x32_bf16 v[44:47], v[212:215], v[20:23], v[44:47]\n"
    "v_mfma_f32_16x16x32_bf16 v[52:55], v[216:219], v[20:23], v[52:55]\n"
    "v_mfma_f32_16x16x32_bf16 v[64:67], v[220:223], v[20:23], v[64:67]\n"
    "v_mfma_f32_16x16x32_bf16 v[68:71], v[224:227], v[20:23], v[68:71]\n"
    "s_waitcnt lgkmcnt(0)\n"
    "v_mfma_f32_16x16x32_bf16 v[76:79], v[228:231], v[20:23], v[76:79]\n"
    "v_mfma_f32_16x16x32_bf16 v[40:43], v[232:235], v[24:27], v[40:43]\n"
    "v_mfma_f32_16x16x32_bf16 v[44:47], v[236:239], v[24:27], v[44:47]\n"
    "v_mfma_f32_16x16x32_bf16 v[52:55], v[240:243], v[24:27], v[52:55]\n"
    "s_waitcnt vmcnt(3)\n"
    "buffer_atomic_smax v12, v14, s[36:39], s70 offen sc1\n"
    "v_mfma_f32_16x16x32_bf16 v[64:67], v[244:247], v[24:27], v[64:67]\n"
    "v_mfma_f32_16x16x32_bf16 v[68:71], v[248:251], v[24:27], v[68:71]\n"
    "v_mfma_f32_16x16x32_bf16 v[76:79], v[252:255], v[24:27], v[76:79]\n"
    "v_pk_mul_f32 v[64:65], v[40:41], v[64:65]\n"
    "v_pk_mul_f32 v[66:67], v[42:43], v[66:67]\n"
    "s_nop 1\n"
    "v_pk_mul_f32 v[68:69], v[44:45], v[68:69]\n"
    "v_pk_mul_f32 v[70:71], v[46:47], v[70:71]\n"
    "v_pk_mul_f32 v[40:41], v[40:41], s[64:65] op_sel_hi:[1,0]\n"
    "v_pk_mul_f32 v[42:43], v[42:43], s[64:65] op_sel_hi:[1,0]\n"
    "v_pk_mul_f32 v[44:45], v[44:45], s[64:65] op_sel_hi:[1,0]\n"
    "v_pk_mul_f32 v[46:47], v[46:47], s[64:65] op_sel_hi:[1,0]\n"
    "v_exp_f32 v40, v40\n"
    "v_exp_f32 v41, v41\n"
    "v_exp_f32 v42, v42\n"
    "v_exp_f32 v43, v43\n"
    "v_exp_f32 v44, v44\n"
    "v_exp_f32 v45, v45\n"
    "v_exp_f32 v46, v46\n"
    "v_exp_f32 v47, v47\n"
    "v_pk_add_f32 v[40:41], v[40:41], 1.0 op_sel_hi:[1,0]\n"
    "v_pk_add_f32 v[42:43], v[42:43], 1.0 op_sel_hi:[1,0]\n"
    "v_pk_add_f32 v[44:45], v[44:45], 1.0 op_sel_hi:[1,0]\n"
    "v_pk_add_f32 v[46:47], v[46:47], 1.0 op_sel_hi:[1,0]\n"
    "v_rcp_f32 v40, v40\n"
    "v_rcp_f32 v41, v41\n"
    "v_rcp_f32 v42, v42\n"
    "v_rcp_f32 v43, v43\n"
    "v_rcp_f32 v44, v44\n"
    "v_rcp_f32 v45, v45\n"
    "v_rcp_f32 v46, v46\n"
    "v_rcp_f32 v47, v47\n"
    "v_pk_mul_f32 v[40:41], v[40:41], v[64:65]\n"
    "v_pk_mul_f32 v[42:43], v[42:43], v[66:67]\n"
    "v_pk_mul_f32 v[44:45], v[44:45], v[68:69]\n"
    "v_pk_mul_f32 v[46:47], v[46:47], v[70:71]\n"
    "v_cvt_pk_bf16_f32 v40, v40, v41\n"
    "v_cvt_pk_bf16_f32 v41, v42, v43\n"
    "v_cvt_pk_bf16_f32 v42, v44, v45\n"
    "v_cvt_pk_bf16_f32 v43, v46, v47\n"
    "buffer_store_dwordx4 v[40:43], v15, s[36:39], s73 offen offset:1024 sc1\n"
    "v_pk_mul_f32 v[76:77], v[52:53], v[76:77]\n"
    "v_pk_mul_f32 v[78:79], v[54:55], v[78:79]\n"
    "v_pk_mul_f32 v[52:53], v[52:53], s[64:65] op_sel_hi:[1,0]\n"
    "v_pk_mul_f32 v[54:55], v[54:55], s[64:65] op_sel_hi:[1,0]\n"
    "v_exp_f32 v52, v52\n"
    "v_exp_f32 v53, v53\n"
    "v_exp_f32 v54, v54\n"
    "v_exp_f32 v55, v55\n"
    "v_pk_add_f32 v[52:53], v[52:53], 1.0 op_sel_hi:[1,0]\n"
    "v_pk_add_f32 v[54:55], v[54:55], 1.0 op_sel_hi:[1,0]\n"
    "v_rcp_f32 v52, v52\n"
    "v_rcp_f32 v53, v53\n"
    "v_rcp_f32 v54, v54\n"
    "v_rcp_f32 v55, v55\n"
    "v_pk_mul_f32 v[52:53], v[52:53], v[76:77]\n"
    "v_pk_mul_f32 v[54:55], v[54:55], v[78:79]\n"
    "v_cvt_pk_bf16_f32 v52, v52, v53\n"
    "v_cvt_pk_bf16_f32 v53, v54, v55\n"
    "s_nop 0\n"
    "v_permlane16_swap_b32 v52, v54\n"
    "v_permlane16_swap_b32 v53, v55\n"
    "buffer_store_dwordx4 v[52:55], v4, s[36:39], s73 offen offset:1024 sc1\n"
    "v_mov_b32 v12, s68\n"
    "s_mov_b32 s73, 0x80000000\n"
    "s_mov_b32 s68, s71\n"
    "s_mov_b32 s69, 0\n"
    "s_mov_b32 s70, 0x80000000\n"
    "v_mov_b32 v7, v12\n"
    "s_waitcnt vmcnt(9)\n"
    "s_add_u32 s71, s68, 1\n"
    "v_add_u32 v13, v11, v3\n"
    "v_cmp_gt_i32 vcc, s71, v13\n"
    "s_nop 0\n"
    "s_or_b64 exec, vcc, s[62:63]\n"
    "buffer_atomic_smax v11, v2, s[36:39], 0 offen sc0 sc1\n"
    "s_mov_b64 exec, -1\n"
    "s_waitcnt vmcnt(4)\n"
    "s_branch .LgUP_bar0_%=\n"
    ".LgUP_dma1_84_%=:\n"
    "v_mfma_f32_16x16x32_bf16 v[40:43], v[160:163], v[24:27], v[40:43]\n"
    "s_add_u32 m0, s59, 0\n"
    "v_mfma_f32_16x16x32_bf16 v[44:47], v[164:167], v[24:27], v[44:47]\n"
    "buffer_load_dwordx4 v1, s[36:39], s76 offen sc1 lds\n"
    "v_mfma_f32_16x16x32_bf16 v[52:55], v[168:171], v[24:27], v[52:55]\n"
    "s_add_u32 s76, s76, 16384\n"
    "s_add_u32 m0, s59, 4096\n"
    "v_mfma_f32_16x16x32_bf16 v[64:67], v[172:175], v[24:27], v[64:67]\n"
    "buffer_load_dwordx4 v1, s[36:39], s76 offen sc1 lds\n"
    "v_mfma_f32_16x16x32_bf16 v[68:71], v[176:179], v[24:27], v[68:71]\n"
    "s_waitcnt lgkmcnt(1)\n"
    "v_mfma_f32_16x16x32_bf16 v[76:79], v[180:183], v[24:27], v[76:79]\n"
    "ds_read_b128 v[24:27], v9 offset:17408\n"
    "v_mfma_f32_16x16x32_bf16 v[40:43], v[184:187], v[16:19], v[40:43]\n"
    "s_add_u32 s76, s76, 16384\n"
    "s_add_u32 m0, s59, 8192\n"
    "v_mfma_f32_16x16x32_bf16 v[44:47], v[188:191], v[16:19], v[44:47]\n"
    "buffer_load_dwordx4 v1, s[36:39], s76 offen sc1 lds\n"
    "v_mfma_f32_16x16x32_bf16 v[52:55], v[192:195], v[16:19], v[52:55]\n"
    "v_mfma_f32_16x16x32_bf16 v[64:67], v[196:199], v[16:19], v[64:67]\n"
    "v_mfma_f32_16x16x32_bf16 v[68:71], v[200:203], v[16:19], v[68:71]\n"
    "s_waitcnt lgkmcnt(1)\n"
    "s_add_u32 s76, s76, 16384\n"
    "s_add_u32 m0, s59, 12288\n"
    "v_mfma_f32_16x16x32_bf16 v[76:79], v[204:207], v[16:19], v[76:79]\n"
    "buffer_load_dwordx4 v1, s[36:39], s76 offen sc1 lds\n"
    "v_mfma_f32_16x16x32_bf16 v[40:43], v[208:211], v[20:23], v[40:43]\n"
    "v_mfma_f32_16x16x32_bf16 v[44:47], v[212:215], v[20:23], v[44:47]\n"
    "v_mfma_f32_16x16x32_bf16 v[52:55], v[216:219], v[20:23], v[52:55]\n"
    "s_add_u32 s76, s76, 16384\n"
    "s_add_u32 m0, s59, 16384\n"
    "v_mfma_f32_16x16x32_bf16 v[64:67], v[220:223], v[20:23], v[64:67]\n"
    "buffer_load_dwordx4 v8, s[36:39], s76 offen sc1 lds\n"
    "v_mfma_f32_16x16x32_bf16 v[68:71], v[224:227], v[20:23], v[68:71]\n"
    "s_waitcnt lgkmcnt(0)\n"
    "v_mfma_f32_16x16x32_bf16 v[76:79], v[228:231], v[20:23], v[76:79]\n"
    "v_mfma_f32_16x16x32_bf16 v[40:43], v[232:235], v[24:27], v[40:43]\n"
    "v_mfma_f32_16x16x32_bf16 v[44:47], v[236:239], v[24:27], v[44:47]\n"
    "v_mfma_f32_16x16x32_bf16 v[52:55], v[240:243], v[24:27], v[52:55]\n"
    "s_waitcnt vmcnt(5)\n"
    "buffer_atomic_smax v12, v14, s[36:39], s70 offen sc1\n"
    "v_mfma_f32_16x16x32_bf16 v[64:67], v[244:247], v[24:27], v[64:67]\n"
    "v_mfma_f32_16x16x32_bf16 v[68:71], v[248:251], v[24:27], v[68:71]\n"
    "v_mfma_f32_16x16x32_bf16 v[76:79], v[252:255], v[24:27], v[76:79]\n"
    "v_pk_mul_f32 v[64:65], v[40:41], v[64:65]\n"
    "v_pk_mul_f32 v[66:67], v[42:43], v[66:67]\n"
    "s_nop 1\n"
    "v_pk_mul_f32 v[68:69], v[44:45], v[68:69]\n"
    "v_pk_mul_f32 v[70:71], v[46:47], v[70:71]\n"
    "v_pk_mul_f32 v[40:41], v[40:41], s[64:65] op_sel_hi:[1,0]\n"
    "v_pk_mul_f32 v[42:43], v[42:43], s[64:65] op_sel_hi:[1,0]\n"
    "v_pk_mul_f32 v[44:45], v[44:45], s[64:65] op_sel_hi:[1,0]\n"
    "v_pk_mul_f32 v[46:47], v[46:47], s[64:65] op_sel_hi:[1,0]\n"
    "v_exp_f32 v40, v40\n"
    "v_exp_f32 v41, v41\n"
    "v_exp_f32 v42, v42\n"
    "v_exp_f32 v43, v43\n"
    "v_exp_f32 v44, v44\n"
    "v_exp_f32 v45, v45\n"
    "v_exp_f32 v46, v46\n"
    "v_exp_f32 v47, v47\n"
    "v_pk_add_f32 v[40:41], v[40:41], 1.0 op_sel_hi:[1,0]\n"
    "v_pk_add_f32 v[42:43], v[42:43], 1.0 op_sel_hi:[1,0]\n"
    "v_pk_add_f32 v[44:45], v[44:45], 1.0 op_sel_hi:[1,0]\n"
    "v_pk_add_f32 v[46:47], v[46:47], 1.0 op_sel_hi:[1,0]\n"
    "v_rcp_f32 v40, v40\n"
    "v_rcp_f32 v41, v41\n"
    "v_rcp_f32 v42, v42\n"
    "v_rcp_f32 v43, v43\n"
    "v_rcp_f32 v44, v44\n"
    "v_rcp_f32 v45, v45\n"
    "v_rcp_f32 v46, v46\n"
    "v_rcp_f32 v47, v47\n"
    "v_pk_mul_f32 v[40:41], v[40:41], v[64:65]\n"
    "v_pk_mul_f32 v[42:43], v[42:43], v[66:67]\n"
    "v_pk_mul_f32 v[44:45], v[44:45], v[68:69]\n"
    "v_pk_mul_f32 v[46:47], v[46:47], v[70:71]\n"
    "v_cvt_pk_bf16_f32 v40, v40, v41\n"
    "v_cvt_pk_bf16_f32 v41, v42, v43\n"
    "v_cvt_pk_bf16_f32 v42, v44, v45\n"
    "v_cvt_pk_bf16_f32 v43, v46, v47\n"
    "buffer_store_dwordx4 v[40:43], v15, s[36:39], s73 offen offset:1024 sc1\n"
    "v_pk_mul_f32 v[76:77], v[52:53], v[76:77]\n"
    "v_pk_mul_f32 v[78:79], v[54:55], v[78:79]\n"
    "v_pk_mul_f32 v[52:53], v[52:53], s[64:65] op_sel_hi:[1,0]\n"
    "v_pk_mul_f32 v[54:55], v[54:55], s[64:65] op_sel_hi:[1,0]\n"
    "v_exp_f32 v52, v52\n"
    "v_exp_f32 v53, v53\n"
    "v_exp_f32 v54, v54\n"
    "v_exp_f32 v55, v55\n"
    "v_pk_add_f32 v[52:53], v[52:53], 1.0 op_sel_hi:[1,0]\n"
    "v_pk_add_f32 v[54:55], v[54:55], 1.0 op_sel_hi:[1,0]\n"
    "v_rcp_f32 v52, v52\n"
    "v_rcp_f32 v53, v53\n"
    "v_rcp_f32 v54, v54\n"
    "v_rcp_f32 v55, v55\n"
    "v_pk_mul_f32 v[52:53], v[52:53], v[76:77]\n"
    "v_pk_mul_f32 v[54:55], v[54:55], v[78:79]\n"
    "v_cvt_pk_bf16_f32 v52, v52, v53\n"
    "v_cvt_pk_bf16_f32 v53, v54, v55\n"
    "s_nop 0\n"
    "v_permlane16_swap_b32 v52, v54\n"
    "v_permlane16_swap_b32 v53, v55\n"
    "buffer_store_dwordx4 v[52:55], v4, s[36:39], s73 offen offset:1024 sc1\n"
    "v_mov_b32 v12, s68\n"
    "s_mov_b32 s73, 0x80000000\n"
    "s_mov_b32 s68, s71\n"
    "s_mov_b32 s69, 0\n"
    "s_mov_b32 s70, 0x80000000\n"
    "v_mov_b32 v7, v12\n"
    "s_waitcnt vmcnt(9)\n"
    "s_add_u32 s71, s68, 1\n"
    "v_add_u32 v13, v11, v3\n"
    "v_cmp_gt_i32 vcc, s71, v13\n"
    "s_nop 0\n"
    "s_or_b64 exec, vcc, s[62:63]\n"
    "buffer_atomic_smax v11, v2, s[36:39], 0 offen sc0 sc1\n"
    "s_mov_b64 exec, -1\n"
    "s_waitcnt vmcnt(4)\n"
    "s_branch .LgUP_bar0_%=\n"
    ".LgUP_dma1_102_%=:\n"
    "v_mfma_f32_16x16x32_bf16 v[40:43], v[232:235], v[24:27], v[40:43]\n"
    "s_add_u32 m0, s59, 0\n"
    "v_mfma_f32_16x16x32_bf16 v[44:47], v[236:239], v[24:27], v[44:47]\n"
    "buffer_load_dwordx4 v1, s[36:39], s76 offen sc1 lds\n"
    "v_mfma_f32_16x16x32_bf16 v[52:55], v[240:243], v[24:27], v[52:55]\n"
    "s_add_u32 s76, s76, 16384\n"
    "s_waitcnt vmcnt(2)\n"
    "buffer_atomic_smax v12, v14, s[36:39], s70 offen sc1\n"
    "v_mfma_f32_16x16x32_bf16 v[64:67], v[244:247], v[24:27], v[64:67]\n"
    "s_add_u32 m0, s59, 4096\n"
    "v_mfma_f32_16x16x32_bf16 v[68:71], v[248:251], v[24:27], v[68:71]\n"
    "buffer_load_dwordx4 v1, s[36:39], s76 offen sc1 lds\n"
    "v_mfma_f32_16x16x32_bf16 v[76:79], v[252:255], v[24:27], v[76:79]\n"
    "v_pk_mul_f32 v[64:65], v[40:41], v[64:65]\n"
    "v_pk_mul_f32 v[66:67], v[42:43], v[66:67]\n"
    "s_nop 1\n"
    "v_pk_mul_f32 v[68:69], v[44:45], v[68:69]\n"
    "v_pk_mul_f32 v[70:71], v[46:47], v[70:71]\n"
    "v_pk_mul_f32 v[40:41], v[40:41], s[64:65] op_sel_hi:[1,0]\n"
    "v_pk_mul_f32 v[42:43], v[42:43], s[64:65] op_sel_hi:[1,0]\n"
    "v_pk_mul_f32 v[44:45], v[44:45], s[64:65] op_sel_hi:[1,0]\n"
    "v_pk_mul_f32 v[46:47], v[46:47], s[64:65] op_sel_hi:[1,0]\n"
    "v_exp_f32 v40, v40\n"
    "v_exp_f32 v41, v41\n"
    "v_exp_f32 v42, v42\n"
    "v_exp_f32 v43, v43\n"
    "v_exp_f32 v44, v44\n"
    "v_exp_f32 v45, v45\n"
    "v_exp_f32 v46, v46\n"
    "v_exp_f32 v47, v47\n"
    "v_pk_add_f32 v[40:41], v[40:41], 1.0 op_sel_hi:[1,0]\n"
    "v_pk_add_f32 v[42:43], v[42:43], 1.0 op_sel_hi:[1,0]\n"
    "v_pk_add_f32 v[44:45], v[44:45], 1.0 op_sel_hi:[1,0]\n"
    "v_pk_add_f32 v[46:47], v[46:47], 1.0 op_sel_hi:[1,0]\n"
    "v_rcp_f32 v40, v40\n"
    "v_rcp_f32 v41, v41\n"
    "v_rcp_f32 v42, v42\n"
    "v_rcp_f32 v43, v43\n"
    "v_rcp_f32 v44, v44\n"
    "v_rcp_f32 v45, v45\n"
    "v_rcp_f32 v46, v46\n"
    "v_rcp_f32 v47, v47\n"
    "v_pk_mul_f32 v[40:41], v[40:41], v[64:65]\n"
    "v_pk_mul_f32 v[42:43], v[42:43], v[66:67]\n"
    "v_pk_mul_f32 v[44:45], v[44:45], v[68:69]\n"
    "v_pk_mul_f32 v[46:47], v[46:47], v[70:71]\n"
    "v_cvt_pk_bf16_f32 v40, v40, v41\n"
    "v_cvt_pk_bf16_f32 v41, v42, v43\n"
    "v_cvt_pk_bf16_f32 v42, v44, v45\n"
    "v_cvt_pk_bf16_f32 v43, v46, v47\n"
    "buffer_store_dwordx4 v[40:43], v15, s[36:39], s73 offen offset:1024 sc1\n"
    "v_pk_mul_f32 v[76:77], v[52:53], v[76:77]\n"
    "v_pk_mul_f32 v[78:79], v[54:55], v[78:79]\n"
    "v_pk_mul_f32 v[52:53], v[52:53], s[64:65] op_sel_hi:[1,0]\n"
    "v_pk_mul_f32 v[54:55], v[54:55], s[64:65] op_sel_hi:[1,0]\n"
    "v_exp_f32 v52, v52\n"
    "v_exp_f32 v53, v53\n"
    "v_exp_f32 v54, v54\n"
    "v_exp_f32 v55, v55\n"
    "v_pk_add_f32 v[52:53], v[52:53], 1.0 op_sel_hi:[1,0]\n"
    "v_pk_add_f32 v[54:55], v[54:55], 1.0 op_sel_hi:[1,0]\n"
    "v_rcp_f32 v52, v52\n"
    "v_rcp_f32 v53, v53\n"
    "v_rcp_f32 v54, v54\n"
    "v_rcp_f32 v55, v55\n"
    "v_pk_mul_f32 v[52:53], v[52:53], v[76:77]\n"
    "v_pk_mul_f32 v[54:55], v[54:55], v[78:79]\n"
    "v_cvt_pk_bf16_f32 v52, v52, v53\n"
    "v_cvt_pk_bf16_f32 v53, v54, v55\n"
    "s_nop 0\n"
    "v_permlane16_swap_b32 v52, v54\n"
    "v_permlane16_swap_b32 v53, v55\n"
    "buffer_store_dwordx4 v[52:55], v4, s[36:39], s73 offen offset:1024 sc1\n"
    "v_mov_b32 v12, s68\n"
    "s_mov_b32 s73, 0x80000000\n"
    "s_mov_b32 s68, s71\n"
    "s_add_u32 m0, s59, 8192\n"
    "s_add_u32 s76, s76, 16384\n"
    "buffer_load_dwordx4 v1, s[36:39], s76 offen sc1 lds\n"
    "s_add_u32 m0, s59, 12288\n"
    "s_add_u32 s76, s76, 16384\n"
    "buffer_load_dwordx4 v1, s[36:39], s76 offen sc1 lds\n"
    "s_add_u32 m0, s59, 16384\n"
    "s_add_u32 s76, s76, 16384\n"
    "buffer_load_dwordx4 v8, s[36:39], s76 offen sc1 lds\n"
    "s_waitcnt vmcnt(3)\n"
    "buffer_atomic_smax v12, v10, s[36:39], 0 offen sc1\n"
    "s_mov_b32 s69, 0x80000000\n"
    "s_mov_b32 s70, 0x80000000\n"
    "v_mov_b32 v7, v12\n"
    "s_waitcnt vmcnt(9)\n"
    "s_add_u32 s71, s68, 1\n"
    "v_add_u32 v13, v11, v3\n"
    "v_cmp_gt_i32 vcc, s71, v13\n"
    "s_nop 0\n"
    "s_or_b64 exec, vcc, s[62:63]\n"
    "buffer_atomic_smax v11, v2, s[36:39], 0 offen sc0 sc1\n"
    "s_mov_b64 exec, -1\n"
    "s_waitcnt vmcnt(2)\n"
    "s_branch .LgUP_bar0_%=\n"
    ".LgUP_idle1_%=:\n"
    "s_or_b64 exec, vcc, s[62:63]\n"
    "buffer_atomic_smax v11, v2, s[36:39], 0 offen sc0 sc1\n"
    "s_mov_b64 exec, -1\n"
    "s_waitcnt vmcnt(1)\n"
    "buffer_atomic_smax v12, v10, s[36:39], 0 offen sc1\n"
    "s_branch .LgUP_block1_0_%=\n"
    ".LgUP_block1_%=:\n"
    "v_add_u32 v13, v11, v3\n"
    "v_cmp_gt_i32 vcc, s71, v13\n"
    "s_nop 0\n"
    "s_or_b64 exec, vcc, s[62:63]\n"
    "buffer_atomic_smax v11, v2, s[36:39], 0 offen sc0 sc1\n"
    "s_mov_b64 exec, -1\n"
    "s_sleep 3\n"
    "s_waitcnt vmcnt(1)\n"
    ".LgUP_block1_0_%=:\n"
    "v_add_u32 v13, v5, v3\n"
    "v_cmp_gt_i32 vcc, s71, v13\n"
    "s_cbranch_vccz .LgUP_go1_%=\n"
    "v_add_u32 v13, v5, v3\n"
    "v_cmp_gt_i32 vcc, s71, v13\n"
    "s_nop 0\n"
    "s_or_b64 exec, vcc, s[62:63]\n"
    "buffer_atomic_smax v5, v2, s[36:39], 0 offen sc0 sc1\n"
    "s_mov_b64 exec, -1\n"
    "s_sleep 3\n"
    "s_waitcnt vmcnt(1)\n"
    ".LgUP_block1_1_%=:\n"
    "v_add_u32 v13, v11, v3\n"
    "v_cmp_gt_i32 vcc, s71, v13\n"
    "s_cbranch_vccz .LgUP_go1_%=\n"
    "s_sub_u32 s72, s72, 1\n"
    "s_cbranch_scc0 .LgUP_block1_%=\n"
    "s_mov_b32 s72, 0\n"
    ".LgUP_go1_%=:\n"
    "s_lshr_b32 s76, s71, 1\n"
    "s_and_b32 s76, s76, s54\n"
    "s_mul_i32 s76, s76, s53\n"
    "s_add_u32 s76, s76, s52\n"
    "s_add_u32 m0, s59, 0\n"
    "s_nop 0\n"
    "buffer_load_dwordx4 v1, s[36:39], s76 offen sc1 lds\n"
    "s_add_u32 m0, s59, 4096\n"
    "s_add_u32 s76, s76, 16384\n"
    "buffer_load_dwordx4 v1, s[36:39], s76 offen sc1 lds\n"
    "s_add_u32 m0, s59, 8192\n"
    "s_add_u32 s76, s76, 16384\n"
    "buffer_load_dwordx4 v1, s[36:39], s76 offen sc1 lds\n"
    "s_add_u32 m0, s59, 12288\n"
    "s_add_u32 s76, s76, 16384\n"
    "buffer_load_dwordx4 v1, s[36:39], s76 offen sc1 lds\n"
    "s_add_u32 m0, s59, 16384\n"
    "s_add_u32 s76, s76, 16384\n"
    "buffer_load_dwordx4 v8, s[36:39], s76 offen sc1 lds\n"
    "s_mov_b32 s69, 0x80000000\n"
    "s_mov_b32 s70, 0x80000000\n"
    "v_mov_b32 v7, v12\n"
    "s_waitcnt vmcnt(5)\n"
    "s_add_u32 s71, s68, 1\n"
    "v_add_u32 v13, v11, v3\n"
    "v_cmp_gt_i32 vcc, s71, v13\n"
    "s_nop 0\n"
    "s_or_b64 exec, vcc, s[62:63]\n"
    "buffer_atomic_smax v11, v2, s[36:39], 0 offen sc0 sc1\n"
    "s_mov_b64 exec, -1\n"
    "s_waitcnt vmcnt(1)\n"
    "s_branch .LgUP_bar0_%=\n"
    ".LgUP_bar1_%=:\n"
    "s_barrier\n"
    "ds_read_b128 v[16:19], v9 offset:0\n"
    "ds_read_b128 v[20:23], v9 offset:1024\n"
    "ds_read_b128 v[24:27], v9 offset:2048\n"
    "v_add_u32 v13, v5, v3\n"
    "v_cmp_gt_i32 vcc, s71, v13\n"
    "s_nop 0\n"
    "s_or_b64 exec, vcc, s[62:63]\n"
    "buffer_atomic_smax v5, v2, s[36:39], 0 offen sc0 sc1\n"
    "s_mov_b64 exec, -1\n"
    "s_lshr_b32 s76, s71, 1\n"
    "s_and_b32 s76, s76, s54\n"
    "s_mul_i32 s76, s76, s53\n"
    "s_add_u32 s76, s76, s52\n"
    "s_waitcnt lgkmcnt(2)\n"
    "s_branch .LgUP_head1_%=\n"
    ".LgUP_FIN_%=:\n"
  );
}
DEVI void role_wo(const Params& p, int L, int j, int i, u32 dbg, unsigned long long tag) {
  const int lane = threadIdx.x & 63, w = threadIdx.x >> 6, e0 = j * 1024 * 576 + i * 192;
  GEMM_DECL;
  g_w0 = make_srd(p.Wo + e0, 4096u * 576 * 2 - e0 * 2); g_w1 = g_w0; g_w2 = g_w0;
  g_ldw = 1152;
  g_in_base = pgb(L, B_O + 8 * j); g_in_stride = SLOT; g_in_mask = RO - 1;
  g_out_base = pgb(L, B_PA + 5 * j); g_out_kb = 12 * i; g_out_mask = RP - 1;
  g_flag = lane < 4 ? mbox(L, lane, SL_REDA, 12 * j + 4 * i + w) : ((lane < 12 && w == 0) ? mbox_att(L, j, lane - 4, 4 + i) : OOB);
  g_paddr = mbox(L, j, SL_WO + i, lane);
  g_pbias = lane < 8 ? 0 : (lane < 24 ? 2 * RP : 1000);
  v16u sa, sb, va;
  gemm_args(GEMM_ARGS, p, dbg, tag, sa, sb, va);
  RUN_ASM(
    "s_nop 4\n"
    "s_getpc_b64 s[84:85]\n"
    ".LgWO_PC_%=:\n"
    "s_and_b32 s85, s85, 0xffff\n"
    "s_mov_b32 s86, .LgWO_FIN_%=-.LgWO_PC_%=\n"
    "s_mov_b32 s87, 0x20000\n"
    "v_mbcnt_lo_u32_b32 v40, -1, 0\n"
    "v_mbcnt_hi_u32_b32 v40, -1, v40\n"
    "v_lshlrev_b32 v40, 7, v40\n"
    "buffer_load_dword v41, v40, s[84:87], 0 offen\n"
    "v_add_u32 v40, 8192, v40\n"
    "buffer_load_dword v41, v40, s[84:87], 0 offen\n"
    "v_add_u32 v40, 8192, v40\n"
    "buffer_load_dword v41, v40, s[84:87], 0 offen\n"
    "s_mov_b32 s68, 0\n"
    "s_mov_b32 s71, 0\n"
    "v_add_u32 v9, 32768, v0\n"
    "s_mov_b32 s64, 0xbfb8aa3b\n"
    "s_mov_b32 s72, 262144\n"
    "s_add_u32 m0, s59, 0\n"
    "s_mov_b32 s74, 0\n"
    "buffer_load_dwordx4 v5, s[40:43], s74 offen lds\n"
    "s_add_u32 m0, s59, 4096\n"
    "s_mov_b32 s74, 128\n"
    "buffer_load_dwordx4 v5, s[40:43], s74 offen lds\n"
    "s_add_u32 m0, s59, 8192\n"
    "s_mov_b32 s74, 256\n"
    "buffer_load_dwordx4 v5, s[40:43], s74 offen lds\n"
    "s_add_u32 m0, s59, 12288\n"
    "s_mov_b32 s74, 36864\n"
    "buffer_load_dwordx4 v5, s[40:43], s74 offen lds\n"
    "s_add_u32 m0, s59, 16384\n"
    "s_mov_b32 s74, 36992\n"
    "buffer_load_dwordx4 v5, s[40:43], s74 offen lds\n"
    "s_add_u32 m0, s59, 20480\n"
    "s_mov_b32 s74, 37120\n"
    "buffer_load_dwordx4 v5, s[40:43], s74 offen lds\n"
    "s_add_u32 m0, s59, 24576\n"
    "s_mov_b32 s74, 73728\n"
    "buffer_load_dwordx4 v5, s[40:43], s74 offen lds\n"
    "s_add_u32 m0, s59, 28672\n"
    "s_mov_b32 s74, 73856\n"
    "buffer_load_dwordx4 v5, s[40:43], s74 offen lds\n"
    "s_add_u32 m0, s59, 32768\n"
    "s_mov_b32 s74, 73984\n"
    "buffer_load_dwordx4 v5, s[40:43], s74 offen lds\n"
    "s_add_u32 m0, s59, 36864\n"
    "s_mov_b32 s74, 110592\n"
    "buffer_load_dwordx4 v5, s[40:43], s74 offen lds\n"
    "s_add_u32 m0, s59, 40960\n"
    "s_mov_b32 s74, 110720\n"
    "buffer_load_dwordx4 v5, s[40:43], s74 offen lds\n"
    "s_add_u32 m0, s59, 45056\n"
    "s_mov_b32 s74, 110848\n"
    "buffer_load_dwordx4 v5, s[40:43], s74 offen lds\n"
    "s_add_u32 m0, s59, 49152\n"
    "s_mov_b32 s74, 147456\n"
    "buffer_load_dwordx4 v5, s[40:43], s74 offen lds\n"
    "s_add_u32 m0, s59, 53248\n"
    "s_mov_b32 s74, 147584\n"
    "buffer_load_dwordx4 v5, s[40:43], s74 offen lds\n"
    "s_add_u32 m0, s59, 57344\n"
    "s_mov_b32 s74, 147712\n"
    "buffer_load_dwordx4 v5, s[40:43], s74 offen lds\n"
    "s_add_u32 m0, s59, 61440\n"
    "s_mov_b32 s74, 184320\n"
    "buffer_load_dwordx4 v5, s[40:43], s74 offen lds\n"
    "s_add_u32 m0, s59, 65536\n"
    "s_mov_b32 s74, 184448\n"
    "buffer_load_dwordx4 v5, s[40:43], s74 offen lds\n"
    "s_add_u32 m0, s59, 69632\n"
    "s_mov_b32 s74, 184576\n"
    "buffer_load_dwordx4 v5, s[40:43], s74 offen lds\n"
    "s_add_u32 m0, s59, 73728\n"
    "s_mov_b32 s74, 221184\n"
    "buffer_load_dwordx4 v5, s[40:43], s74 offen lds\n"
    "s_add_u32 m0, s59, 77824\n"
    "s_mov_b32 s74, 221312\n"
    "buffer_load_dwordx4 v5, s[40:43], s74 offen lds\n"
    "s_add_u32 m0, s59, 81920\n"
    "s_mov_b32 s74, 221440\n"
    "buffer_load_dwordx4 v5, s[40:43], s74 offen lds\n"
    "s_add_u32 m0, s59, 86016\n"
    "s_mov_b32 s74, 258048\n"
    "buffer_load_dwordx4 v5, s[40:43], s74 offen lds\n"
    "s_add_u32 m0, s59, 90112\n"
    "s_mov_b32 s74, 258176\n"
    "buffer_load_dwordx4 v5, s[40:43], s74 offen lds\n"
    "s_add_u32 m0, s59, 94208\n"
    "s_mov_b32 s74, 258304\n"
    "buffer_load_dwordx4 v5, s[40:43], s74 offen lds\n"
    "s_add_u32 m0, s59, 98304\n"
    "s_mov_b32 s74, 294912\n"
    "buffer_load_dwordx4 v5, s[40:43], s74 offen lds\n"
    "s_add_u32 m0, s59, 102400\n"
    "s_mov_b32 s74, 295040\n"
    "buffer_load_dwordx4 v5, s[40:43], s74 offen lds\n"
    "s_add_u32 m0, s59, 106496\n"
    "s_mov_b32 s74, 295168\n"
    "buffer_load_dwordx4 v5, s[40:43], s74 offen lds\n"
    "s_add_u32 m0, s59, 110592\n"
    "s_mov_b32 s74, 331776\n"
    "buffer_load_dwordx4 v5, s[40:43], s74 offen lds\n"
    "s_add_u32 m0, s59, 114688\n"
    "s_mov_b32 s74, 331904\n"
    "buffer_load_dwordx4 v5, s[40:43], s74 offen lds\n"
    "s_add_u32 m0, s59, 118784\n"
    "s_mov_b32 s74, 332032\n"
    "buffer_load_dwordx4 v5, s[40:43], s74 offen lds\n"
    "v_add_u32 v16, 49152, v6\n"
    "v_add_u32 v17, 98304, v6\n"
    "v_add_u32 v18, 49152, v7\n"
    "v_add_u32 v19, 98304, v7\n"
    "s_waitcnt vmcnt(24) lgkmcnt(0)\n"
    "s_barrier\n"
    "s_add_u32 m0, s59, 122880\n"
    "s_mov_b32 s74, 368640\n"
    "buffer_load_dwordx4 v5, s[40:43], s74 offen lds\n"
    "s_add_u32 m0, s59, 126976\n"
    "s_mov_b32 s74, 368768\n"
    "buffer_load_dwordx4 v5, s[40:43], s74 offen lds\n"
    "s_add_u32 m0, s59, 131072\n"
    "s_mov_b32 s74, 368896\n"
    "buffer_load_dwordx4 v5, s[40:43], s74 offen lds\n"
    "s_add_u32 m0, s59, 135168\n"
    "s_mov_b32 s74, 405504\n"
    "buffer_load_dwordx4 v5, s[40:43], s74 offen lds\n"
    "s_add_u32 m0, s59, 139264\n"
    "s_mov_b32 s74, 405632\n"
    "buffer_load_dwordx4 v5, s[40:43], s74 offen lds\n"
    "s_add_u32 m0, s59, 143360\n"
    "s_mov_b32 s74, 405760\n"
    "buffer_load_dwordx4 v5, s[40:43], s74 offen lds\n"
    "ds_read_b64_tr_b16 a[0:1], v6 offset:0\n"
    "ds_read_b64_tr_b16 a[2:3], v6 offset:512\n"
    "ds_read_b64_tr_b16 a[4:5], v6 offset:8\n"
    "ds_read_b64_tr_b16 a[6:7], v6 offset:520\n"
    "ds_read_b64_tr_b16 a[8:9], v7 offset:0\n"
    "ds_read_b64_tr_b16 a[10:11], v7 offset:512\n"
    "ds_read_b64_tr_b16 a[12:13], v6 offset:12288\n"
    "ds_read_b64_tr_b16 a[14:15], v6 offset:12800\n"
    "ds_read_b64_tr_b16 a[16:17], v6 offset:12296\n"
    "ds_read_b64_tr_b16 a[18:19], v6 offset:12808\n"
    "ds_read_b64_tr_b16 a[20:21], v7 offset:12288\n"
    "ds_read_b64_tr_b16 a[22:23], v7 offset:12800\n"
    "s_waitcnt vmcnt(24) lgkmcnt(0)\n"
    "s_barrier\n"
    "s_add_u32 m0, s59, 0\n"
    "s_mov_b32 s74, 442368\n"
    "buffer_load_dwordx4 v5, s[40:43], s74 offen lds\n"
    "s_add_u32 m0, s59, 4096\n"
    "s_mov_b32 s74, 442496\n"
    "buffer_load_dwordx4 v5, s[40:43], s74 offen lds\n"
    "s_add_u32 m0, s59, 8192\n"
    "s_mov_b32 s74, 442624\n"
    "buffer_load_dwordx4 v5, s[40:43], s74 offen lds\n"
    "s_add_u32 m0, s59, 12288\n"
    "s_mov_b32 s74, 479232\n"
    "buffer_load_dwordx4 v5, s[40:43], s74 offen lds\n"
    "s_add_u32 m0, s59, 16384\n"
    "s_mov_b32 s74, 479360\n"
    "buffer_load_dwordx4 v5, s[40:43], s74 offen lds\n"
    "s_add_u32 m0, s59, 20480\n"
    "s_mov_b32 s74, 479488\n"
    "buffer_load_dwordx4 v5, s[40:43], s74 offen lds\n"
    "ds_read_b64_tr_b16 a[24:25], v6 offset:24576\n"
    "ds_read_b64_tr_b16 a[26:27], v6 offset:25088\n"
    "ds_read_b64_tr_b16 a[28:29], v6 offset:24584\n"
    "ds_read_b64_tr_b16 a[30:31], v6 offset:25096\n"
    "ds_read_b64_tr_b16 a[32:33], v7 offset:24576\n"
    "ds_read_b64_tr_b16 a[34:35], v7 offset:25088\n"
    "ds_read_b64_tr_b16 a[36:37], v6 offset:36864\n"
    "ds_read_b64_tr_b16 a[38:39], v6 offset:37376\n"
    "ds_read_b64_tr_b16 a[40:41], v6 offset:36872\n"
    "ds_read_b64_tr_b16 a[42:43], v6 offset:37384\n"
    "ds_read_b64_tr_b16 a[44:45], v7 offset:36864\n"
    "ds_read_b64_tr_b16 a[46:47], v7 offset:37376\n"
    "s_waitcnt vmcnt(24) lgkmcnt(0)\n"
    "s_barrier\n"
    "s_add_u32 m0, s59, 24576\n"
    "s_mov_b32 s74, 516096\n"
    "buffer_load_dwordx4 v5, s[40:43], s74 offen lds\n"
    "s_add_u32 m0, s59, 28672\n"
    "s_mov_b32 s74, 516224\n"
    "buffer_load_dwordx4 v5, s[40:43], s74 offen lds\n"
    "s_add_u32 m0, s59, 32768\n"
    "s_mov_b32 s74, 516352\n"
    "buffer_load_dwordx4 v5, s[40:43], s74 offen lds\n"
    "s_add_u32 m0, s59, 36864\n"
    "s_mov_b32 s74, 552960\n"
    "buffer_load_dwordx4 v5, s[40:43], s74 offen lds\n"
    "s_add_u32 m0, s59, 40960\n"
    "s_mov_b32 s74, 553088\n"
    "buffer_load_dwordx4 v5, s[40:43], s74 offen lds\n"
    "s_add_u32 m0, s59, 45056\n"
    "s_mov_b32 s74, 553216\n"
    "buffer_load_dwordx4 v5, s[40:43], s74 offen lds\n"
    "ds_read_b64_tr_b16 a[48:49], v16 offset:0\n"
    "ds_read_b64_tr_b16 a[50:51], v16 offset:512\n"
    "ds_read_b64_tr_b16 a[52:53], v16 offset:8\n"
    "ds_read_b64_tr_b16 a[54:55], v16 offset:520\n"
    "ds_read_b64_tr_b16 a[56:57], v18 offset:0\n"
    "ds_read_b64_tr_b16 a[58:59], v18 offset:512\n"
    "ds_read_b64_tr_b16 a[60:61], v16 offset:12288\n"
    "ds_read_b64_tr_b16 a[62:63], v16 offset:12800\n"
    "ds_read_b64_tr_b16 a[64:65], v16 offset:12296\n"
    "ds_read_b64_tr_b16 a[66:67], v16 offset:12808\n"
    "ds_read_b64_tr_b16 a[68:69], v18 offset:12288\n"
    "ds_read_b64_tr_b16 a[70:71], v18 offset:12800\n"
    "s_waitcnt vmcnt(24) lgkmcnt(0)\n"
    "s_barrier\n"
    "s_add_u32 m0, s59, 49152\n"
    "s_mov_b32 s74, 589824\n"
    "buffer_load_dwordx4 v5, s[40:43], s74 offen lds\n"
    "s_add_u32 m0, s59, 53248\n"
    "s_mov_b32 s74, 589952\n"
    "buffer_load_dwordx4 v5, s[40:43], s74 offen lds\n"
    "s_add_u32 m0, s59, 57344\n"
    "s_mov_b32 s74, 590080\n"
    "buffer_load_dwordx4 v5, s[40:43], s74 offen lds\n"
    "s_add_u32 m0, s59, 61440\n"
    "s_mov_b32 s74, 626688\n"
    "buffer_load_dwordx4 v5, s[40:43], s74 offen lds\n"
    "s_add_u32 m0, s59, 65536\n"
    "s_mov_b32 s74, 626816\n"
    "buffer_load_dwordx4 v5, s[40:43], s74 offen lds\n"
    "s_add_u32 m0, s59, 69632\n"
    "s_mov_b32 s74, 626944\n"
    "buffer_load_dwordx4 v5, s[40:43], s74 offen lds\n"
    "ds_read_b64_tr_b16 a[72:73], v16 offset:24576\n"
    "ds_read_b64_tr_b16 a[74:75], v16 offset:25088\n"
    "ds_read_b64_tr_b16 a[76:77], v16 offset:24584\n"
    "ds_read_b64_tr_b16 a[78:79], v16 offset:25096\n"
    "ds_read_b64_tr_b16 a[80:81], v18 offset:24576\n"
    "ds_read_b64_tr_b16 a[82:83], v18 offset:25088\n"
    "ds_read_b64_tr_b16 a[84:85], v16 offset:36864\n"
    "ds_read_b64_tr_b16 a[86:87], v16 offset:37376\n"
    "ds_read_b64_tr_b16 a[88:89], v16 offset:36872\n"
    "ds_read_b64_tr_b16 a[90:91], v16 offset:37384\n"
    "ds_read_b64_tr_b16 a[92:93], v18 offset:36864\n"
    "ds_read_b64_tr_b16 a[94:95], v18 offset:37376\n"
    "s_waitcnt vmcnt(24) lgkmcnt(0)\n"
    "s_barrier\n"
    "s_add_u32 m0, s59, 73728\n"
    "s_mov_b32 s74, 663552\n"
    "buffer_load_dwordx4 v5, s[40:43], s74 offen lds\n"
    "s_add_u32 m0, s59, 77824\n"
    "s_mov_b32 s74, 663680\n"
    "buffer_load_dwordx4 v5, s[40:43], s74 offen lds\n"
    "s_add_u32 m0, s59, 81920\n"
    "s_mov_b32 s74, 663808\n"
    "buffer_load_dwordx4 v5, s[40:43], s74 offen lds\n"
    "s_add_u32 m0, s59, 86016\n"
    "s_mov_b32 s74, 700416\n"
    "buffer_load_dwordx4 v5, s[40:43], s74 offen lds\n"
    "s_add_u32 m0, s59, 90112\n"
    "s_mov_b32 s74, 700544\n"
    "buffer_load_dwordx4 v5, s[40:43], s74 offen lds\n"
    "s_add_u32 m0, s59, 94208\n"
    "s_mov_b32 s74, 700672\n"
    "buffer_load_dwordx4 v5, s[40:43], s74 offen lds\n"
    "ds_read_b64_tr_b16 a[96:97], v17 offset:0\n"
    "ds_read_b64_tr_b16 a[98:99], v17 offset:512\n"
    "ds_read_b64_tr_b16 a[100:101], v17 offset:8\n"
    "ds_read_b64_tr_b16 a[102:103], v17 offset:520\n"
    "ds_read_b64_tr_b16 a[104:105], v19 offset:0\n"
    "ds_read_b64_tr_b16 a[106:107], v19 offset:512\n"
    "ds_read_b64_tr_b16 a[108:109], v17 offset:12288\n"
    "ds_read_b64_tr_b16 a[110:111], v17 offset:12800\n"
    "ds_read_b64_tr_b16 a[112:113], v17 offset:12296\n"
    "ds_read_b64_tr_b16 a[114:115], v17 offset:12808\n"
    "ds_read_b64_tr_b16 a[116:117], v19 offset:12288\n"
    "ds_read_b64_tr_b16 a[118:119], v19 offset:12800\n"
    "s_waitcnt vmcnt(24) lgkmcnt(0)\n"
    "s_barrier\n"
    "s_add_u32 m0, s59, 98304\n"
    "s_mov_b32 s74, 737280\n"
    "buffer_load_dwordx4 v5, s[40:43], s74 offen lds\n"
    "s_add_u32 m0, s59, 102400\n"
    "s_mov_b32 s74, 737408\n"
    "buffer_load_dwordx4 v5, s[40:43], s74 offen lds\n"
    "s_add_u32 m0, s59, 106496\n"
    "s_mov_b32 s74, 737536\n"
    "buffer_load_dwordx4 v5, s[40:43], s74 offen lds\n"
    "s_add_u32 m0, s59, 110592\n"
    "s_mov_b32 s74, 774144\n"
    "buffer_load_dwordx4 v5, s[40:43], s74 offen lds\n"
    "s_add_u32 m0, s59, 114688\n"
    "s_mov_b32 s74, 774272\n"
    "buffer_load_dwordx4 v5, s[40:43], s74 offen lds\n"
    "s_add_u32 m0, s59, 118784\n"
    "s_mov_b32 s74, 774400\n"
    "buffer_load_dwordx4 v5, s[40:43], s74 offen lds\n"
    "ds_read_b64_tr_b16 a[120:121], v17 offset:24576\n"
    "ds_read_b64_tr_b16 a[122:123], v17 offset:25088\n"
    "ds_read_b64_tr_b16 a[124:125], v17 offset:24584\n"
    "ds_read_b64_tr_b16 a[126:127], v17 offset:25096\n"
    "ds_read_b64_tr_b16 a[128:129], v19 offset:24576\n"
    "ds_read_b64_tr_b16 a[130:131], v19 offset:25088\n"
    "ds_read_b64_tr_b16 a[132:133], v17 offset:36864\n"
    "ds_read_b64_tr_b16 a[134:135], v17 offset:37376\n"
    "ds_read_b64_tr_b16 a[136:137], v17 offset:36872\n"
    "ds_read_b64_tr_b16 a[138:139], v17 offset:37384\n"
    "ds_read_b64_tr_b16 a[140:141], v19 offset:36864\n"
    "ds_read_b64_tr_b16 a[142:143], v19 offset:37376\n"
    "s_waitcnt vmcnt(24) lgkmcnt(0)\n"
    "s_barrier\n"
    "s_add_u32 m0, s59, 122880\n"
    "s_mov_b32 s74, 811008\n"
    "buffer_load_dwordx4 v5, s[40:43], s74 offen lds\n"
    "s_add_u32 m0, s59, 126976\n"
    "s_mov_b32 s74, 811136\n"
    "buffer_load_dwordx4 v5, s[40:43], s74 offen lds\n"
    "s_add_u32 m0, s59, 131072\n"
    "s_mov_b32 s74, 811264\n"
    "buffer_load_dwordx4 v5, s[40:43], s74 offen lds\n"
    "s_add_u32 m0, s59, 135168\n"
    "s_mov_b32 s74, 847872\n"
    "buffer_load_dwordx4 v5, s[40:43], s74 offen lds\n"
    "s_add_u32 m0, s59, 139264\n"
    "s_mov_b32 s74, 848000\n"
    "buffer_load_dwordx4 v5, s[40:43], s74 offen lds\n"
    "s_add_u32 m0, s59, 143360\n"
    "s_mov_b32 s74, 848128\n"
    "buffer_load_dwordx4 v5, s[40:43], s74 offen lds\n"
    "ds_read_b64_tr_b16 a[144:145], v6 offset:0\n"
    "ds_read_b64_tr_b16 a[146:147], v6 offset:512\n"
    "ds_read_b64_tr_b16 a[148:149], v6 offset:8\n"
    "ds_read_b64_tr_b16 a[150:151], v6 offset:520\n"
    "ds_read_b64_tr_b16 a[152:153], v7 offset:0\n"
    "ds_read_b64_tr_b16 a[154:155], v7 offset:512\n"
    "ds_read_b64_tr_b16 a[156:157], v6 offset:12288\n"
    "ds_read_b64_tr_b16 a[158:159], v6 offset:12800\n"
    "ds_read_b64_tr_b16 a[160:161], v6 offset:12296\n"
    "ds_read_b64_tr_b16 a[162:163], v6 offset:12808\n"
    "ds_read_b64_tr_b16 a[164:165], v7 offset:12288\n"
    "ds_read_b64_tr_b16 a[166:167], v7 offset:12800\n"
    "s_waitcnt vmcnt(24) lgkmcnt(0)\n"
    "s_barrier\n"
    "s_add_u32 m0, s59, 0\n"
    "s_mov_b32 s74, 884736\n"
    "buffer_load_dwordx4 v5, s[40:43], s74 offen lds\n"
    "s_add_u32 m0, s59, 4096\n"
    "s_mov_b32 s74, 884864\n"
    "buffer_load_dwordx4 v5, s[40:43], s74 offen lds\n"
    "s_add_u32 m0, s59, 8192\n"
    "s_mov_b32 s74, 884992\n"
    "buffer_load_dwordx4 v5, s[40:43], s74 offen lds\n"
    "s_add_u32 m0, s59, 12288\n"
    "s_mov_b32 s74, 921600\n"
    "buffer_load_dwordx4 v5, s[40:43], s74 offen lds\n"
    "s_add_u32 m0, s59, 16384\n"
    "s_mov_b32 s74, 921728\n"
    "buffer_load_dwordx4 v5, s[40:43], s74 offen lds\n"
    "s_add_u32 m0, s59, 20480\n"
    "s_mov_b32 s74, 921856\n"
    "buffer_load_dwordx4 v5, s[40:43], s74 offen lds\n"
    "ds_read_b64_tr_b16 a[168:169], v6 offset:24576\n"
    "ds_read_b64_tr_b16 a[170:171], v6 offset:25088\n"
    "ds_read_b64_tr_b16 a[172:173], v6 offset:24584\n"
    "ds_read_b64_tr_b16 a[174:175], v6 offset:25096\n"
    "ds_read_b64_tr_b16 a[176:177], v7 offset:24576\n"
    "ds_read_b64_tr_b16 a[178:179], v7 offset:25088\n"
    "ds_read_b64_tr_b16 a[180:181], v6 offset:36864\n"
    "ds_read_b64_tr_b16 a[182:183], v6 offset:37376\n"
    "ds_read_b64_tr_b16 a[184:185], v6 offset:36872\n"
    "ds_read_b64_tr_b16 a[186:187], v6 offset:37384\n"
    "ds_read_b64_tr_b16 a[188:189], v7 offset:36864\n"
    "ds_read_b64_tr_b16 a[190:191], v7 offset:37376\n"
    "s_waitcnt vmcnt(24) lgkmcnt(0)\n"
    "s_barrier\n"
    "s_add_u32 m0, s59, 24576\n"
    "s_mov_b32 s74, 958464\n"
    "buffer_load_dwordx4 v5, s[40:43], s74 offen lds\n"
    "s_add_u32 m0, s59, 28672\n"
    "s_mov_b32 s74, 958592\n"
    "buffer_load_dwordx4 v5, s[40:43], s74 offen lds\n"
    "s_add_u32 m0, s59, 32768\n"
    "s_mov_b32 s74, 958720\n"
    "buffer_load_dwordx4 v5, s[40:43], s74 offen lds\n"
    "s_add_u32 m0, s59, 36864\n"
    "s_mov_b32 s74, 995328\n"
    "buffer_load_dwordx4 v5, s[40:43], s74 offen lds\n"
    "s_add_u32 m0, s59, 40960\n"
    "s_mov_b32 s74, 995456\n"
    "buffer_load_dwordx4 v5, s[40:43], s74 offen lds\n"
    "s_add_u32 m0, s59, 45056\n"
    "s_mov_b32 s74, 995584\n"
    "buffer_load_dwordx4 v5, s[40:43], s74 offen lds\n"
    "ds_read_b64_tr_b16 a[192:193], v16 offset:0\n"
    "ds_read_b64_tr_b16 a[194:195], v16 offset:512\n"
    "ds_read_b64_tr_b16 a[196:197], v16 offset:8\n"
    "ds_read_b64_tr_b16 a[198:199], v16 offset:520\n"
    "ds_read_b64_tr_b16 a[200:201], v18 offset:0\n"
    "ds_read_b64_tr_b16 a[202:203], v18 offset:512\n"
    "ds_read_b64_tr_b16 a[204:205], v16 offset:12288\n"
    "ds_read_b64_tr_b16 a[206:207], v16 offset:12800\n"
    "ds_read_b64_tr_b16 a[208:209], v16 offset:12296\n"
    "ds_read_b64_tr_b16 a[210:211], v16 offset:12808\n"
    "ds_read_b64_tr_b16 a[212:213], v18 offset:12288\n"
    "ds_read_b64_tr_b16 a[214:215], v18 offset:12800\n"
    "s_waitcnt vmcnt(24) lgkmcnt(0)\n"
    "s_barrier\n"
    "s_add_u32 m0, s59, 49152\n"
    "s_mov_b32 s74, 1032192\n"
    "buffer_load_dwordx4 v5, s[40:43], s74 offen lds\n"
    "s_add_u32 m0, s59, 53248\n"
    "s_mov_b32 s74, 1032320\n"
    "buffer_load_dwordx4 v5, s[40:43], s74 offen lds\n"
    "s_add_u32 m0, s59, 57344\n"
    "s_mov_b32 s74, 1032448\n"
    "buffer_load_dwordx4 v5, s[40:43], s74 offen lds\n"
    "s_add_u32 m0, s59, 61440\n"
    "s_mov_b32 s74, 1069056\n"
    "buffer_load_dwordx4 v5, s[40:43], s74 offen lds\n"
    "s_add_u32 m0, s59, 65536\n"
    "s_mov_b32 s74, 1069184\n"
    "buffer_load_dwordx4 v5, s[40:43], s74 offen lds\n"
    "s_add_u32 m0, s59, 69632\n"
    "s_mov_b32 s74, 1069312\n"
    "buffer_load_dwordx4 v5, s[40:43], s74 offen lds\n"
    "ds_read_b64_tr_b16 a[216:217], v16 offset:24576\n"
    "ds_read_b64_tr_b16 a[218:219], v16 offset:25088\n"
    "ds_read_b64_tr_b16 a[220:221], v16 offset:24584\n"
    "ds_read_b64_tr_b16 a[222:223], v16 offset:25096\n"
    "ds_read_b64_tr_b16 a[224:225], v18 offset:24576\n"
    "ds_read_b64_tr_b16 a[226:227], v18 offset:25088\n"
    "ds_read_b64_tr_b16 a[228:229], v16 offset:36864\n"
    "ds_read_b64_tr_b16 a[230:231], v16 offset:37376\n"
    "ds_read_b64_tr_b16 a[232:233], v16 offset:36872\n"
    "ds_read_b64_tr_b16 a[234:235], v16 offset:37384\n"
    "ds_read_b64_tr_b16 a[236:237], v18 offset:36864\n"
    "ds_read_b64_tr_b16 a[238:239], v18 offset:37376\n"
    "s_waitcnt vmcnt(24) lgkmcnt(0)\n"
    "s_barrier\n"
    "s_add_u32 m0, s59, 73728\n"
    "s_mov_b32 s74, 1105920\n"
    "buffer_load_dwordx4 v5, s[40:43], s74 offen lds\n"
    "s_add_u32 m0, s59, 77824\n"
    "s_mov_b32 s74, 1106048\n"
    "buffer_load_dwordx4 v5, s[40:43], s74 offen lds\n"
    "s_add_u32 m0, s59, 81920\n"
    "s_mov_b32 s74, 1106176\n"
    "buffer_load_dwordx4 v5, s[40:43], s74 offen lds\n"
    "s_add_u32 m0, s59, 86016\n"
    "s_mov_b32 s74, 1142784\n"
    "buffer_load_dwordx4 v5, s[40:43], s74 offen lds\n"
    "s_add_u32 m0, s59, 90112\n"
    "s_mov_b32 s74, 1142912\n"
    "buffer_load_dwordx4 v5, s[40:43], s74 offen lds\n"
    "s_add_u32 m0, s59, 94208\n"
    "s_mov_b32 s74, 1143040\n"
    "buffer_load_dwordx4 v5, s[40:43], s74 offen lds\n"
    "buffer_load_dwordx2 v[12:13], off, s[36:39], 0 sc1\n"
    "ds_read_b64_tr_b16 a[240:241], v17 offset:0\n"
    "ds_read_b64_tr_b16 a[242:243], v17 offset:512\n"
    "ds_read_b64_tr_b16 a[244:245], v17 offset:8\n"
    "ds_read_b64_tr_b16 a[246:247], v17 offset:520\n"
    "ds_read_b64_tr_b16 a[248:249], v19 offset:0\n"
    "ds_read_b64_tr_b16 a[250:251], v19 offset:512\n"
    "ds_read_b64_tr_b16 a[252:253], v17 offset:12288\n"
    "ds_read_b64_tr_b16 a[254:255], v17 offset:12800\n"
    "ds_read_b64_tr_b16 v[128:129], v17 offset:12296\n"
    "ds_read_b64_tr_b16 v[130:131], v17 offset:12808\n"
    "ds_read_b64_tr_b16 v[132:133], v19 offset:12288\n"
    "ds_read_b64_tr_b16 v[134:135], v19 offset:12800\n"
    "s_waitcnt vmcnt(25) lgkmcnt(0)\n"
    "s_barrier\n"
    "ds_read_b64_tr_b16 v[136:137], v17 offset:24576\n"
    "ds_read_b64_tr_b16 v[138:139], v17 offset:25088\n"
    "ds_read_b64_tr_b16 v[140:141], v17 offset:24584\n"
    "ds_read_b64_tr_b16 v[142:143], v17 offset:25096\n"
    "ds_read_b64_tr_b16 v[144:145], v19 offset:24576\n"
    "ds_read_b64_tr_b16 v[146:147], v19 offset:25088\n"
    "ds_read_b64_tr_b16 v[148:149], v17 offset:36864\n"
    "ds_read_b64_tr_b16 v[150:151], v17 offset:37376\n"
    "ds_read_b64_tr_b16 v[152:153], v17 offset:36872\n"
    "ds_read_b64_tr_b16 v[154:155], v17 offset:37384\n"
    "ds_read_b64_tr_b16 v[156:157], v19 offset:36864\n"
    "ds_read_b64_tr_b16 v[158:159], v19 offset:37376\n"
    "s_waitcnt vmcnt(19) lgkmcnt(0)\n"
    "s_barrier\n"
    "ds_read_b64_tr_b16 v[160:161], v6 offset:0\n"
    "ds_read_b64_tr_b16 v[162:163], v6 offset:512\n"
    "ds_read_b64_tr_b16 v[164:165], v6 offset:8\n"
    "ds_read_b64_tr_b16 v[166:167], v6 offset:520\n"
    "ds_read_b64_tr_b16 v[168:169], v7 offset:0\n"
    "ds_read_b64_tr_b16 v[170:171], v7 offset:512\n"
    "ds_read_b64_tr_b16 v[172:173], v6 offset:12288\n"
    "ds_read_b64_tr_b16 v[174:175], v6 offset:12800\n"
    "ds_read_b64_tr_b16 v[176:177], v6 offset:12296\n"
    "ds_read_b64_tr_b16 v[178:179], v6 offset:12808\n"
    "ds_read_b64_tr_b16 v[180:181], v7 offset:12288\n"
    "ds_read_b64_tr_b16 v[182:183], v7 offset:12800\n"
    "s_waitcnt vmcnt(13) lgkmcnt(0)\n"
    "s_barrier\n"
    "ds_read_b64_tr_b16 v[184:185], v6 offset:24576\n"
    "ds_read_b64_tr_b16 v[186:187], v6 offset:25088\n"
    "ds_read_b64_tr_b16 v[188:189], v6 offset:24584\n"
    "ds_read_b64_tr_b16 v[190:191], v6 offset:25096\n"
    "ds_read_b64_tr_b16 v[192:193], v7 offset:24576\n"
    "ds_read_b64_tr_b16 v[194:195], v7 offset:25088\n"
    "ds_read_b64_tr_b16 v[196:197], v6 offset:36864\n"
    "ds_read_b64_tr_b16 v[198:199], v6 offset:37376\n"
    "ds_read_b64_tr_b16 v[200:201], v6 offset:36872\n"
    "ds_read_b64_tr_b16 v[202:203], v6 offset:37384\n"
    "ds_read_b64_tr_b16 v[204:205], v7 offset:36864\n"
    "ds_read_b64_tr_b16 v[206:207], v7 offset:37376\n"
    "s_waitcnt vmcnt(7) lgkmcnt(0)\n"
    "s_barrier\n"
    "ds_read_b64_tr_b16 v[208:209], v16 offset:0\n"
    "ds_read_b64_tr_b16 v[210:211], v16 offset:512\n"
    "ds_read_b64_tr_b16 v[212:213], v16 offset:8\n"
    "ds_read_b64_tr_b16 v[214:215], v16 offset:520\n"
    "ds_read_b64_tr_b16 v[216:217], v18 offset:0\n"
    "ds_read_b64_tr_b16 v[218:219], v18 offset:512\n"
    "ds_read_b64_tr_b16 v[220:221], v16 offset:12288\n"
    "ds_read_b64_tr_b16 v[222:223], v16 offset:12800\n"
    "ds_read_b64_tr_b16 v[224:225], v16 offset:12296\n"
    "ds_read_b64_tr_b16 v[226:227], v16 offset:12808\n"
    "ds_read_b64_tr_b16 v[228:229], v18 offset:12288\n"
    "ds_read_b64_tr_b16 v[230:231], v18 offset:12800\n"
    "s_waitcnt vmcnt(0) lgkmcnt(0)\n"
    "s_barrier\n"
    ".LgWO_par_%=:\n"
    "v_readfirstlane_b32 s74, v12\n"
    "v_readfirstlane_b32 s75, v13\n"
    "s_xor_b32 s74, s74, s66\n"
    "s_xor_b32 s75, s75, s67\n"
    "s_cselect_b32 s75, 2, 0\n"
    "s_or_b32 s75, s75, s74\n"
    "s_cmp_lt_u32 s75, 2\n"
    "s_cbranch_scc1 .LgWO_par1_%=\n"
    "s_sleep 4\n"
    "buffer_load_dwordx2 v[12:13], off, s[36:39], 0 sc1\n"
    "s_waitcnt vmcnt(0)\n"
    "s_sub_u32 s72, s72, 1\n"
    "s_cbranch_scc0 .LgWO_par_%=\n"
    "s_mov_b32 s72, 0\n"
    ".LgWO_par1_%=:\n"
    "s_and_b32 s74, s74, 1\n"
    "s_lshl_b32 s74, s74, 13\n"
    "s_add_u32 s36, s36, s74\n"
    "s_addc_u32 s37, s37, 0\n"
    "v_add_u32 v13, v11, v3\n"
    "v_cmp_gt_i32 vcc, s71, v13\n"
    "s_nop 0\n"
    "s_or_b64 exec, vcc, s[62:63]\n"
    "buffer_atomic_smax v11, v2, s[36:39], 0 offen sc0 sc1\n"
    "s_mov_b64 exec, -1\n"
    "s_getpc_b64 s[84:85]\n"
    ".LgWO_PC2_%=:\n"
    "s_and_b32 s85, s85, 0xffff\n"
    "s_mov_b32 s86, .LgWO_FIN_%=-.LgWO_PC2_%=\n"
    "s_mov_b32 s87, 0x20000\n"
    "v_mbcnt_lo_u32_b32 v42, -1, 0\n"
    "v_mbcnt_hi_u32_b32 v42, -1, v42\n"
    "v_lshlrev_b32 v42, 7, v42\n"
    "buffer_load_dword v43, v42, s[84:87], 0 offen\n"
    "v_add_u32 v42, 8192, v42\n"
    "buffer_load_dword v43, v42, s[84:87], 0 offen\n"
    "v_add_u32 v42, 8192, v42\n"
    "buffer_load_dword v43, v42, s[84:87], 0 offen\n"
    "ds_read_b64_tr_b16 v[232:233], v16 offset:24576\n"
    "ds_read_b64_tr_b16 v[234:235], v16 offset:25088\n"
    "ds_read_b64_tr_b16 v[236:237], v16 offset:24584\n"
    "ds_read_b64_tr_b16 v[238:239], v16 offset:25096\n"
    "ds_read_b64_tr_b16 v[240:241], v18 offset:24576\n"
    "ds_read_b64_tr_b16 v[242:243], v18 offset:25088\n"
    "ds_read_b64_tr_b16 v[244:245], v16 offset:36864\n"
    "ds_read_b64_tr_b16 v[246:247], v16 offset:37376\n"
    "ds_read_b64_tr_b16 v[248:249], v16 offset:36872\n"
    "ds_read_b64_tr_b16 v[250:251], v16 offset:37384\n"
    "ds_read_b64_tr_b16 v[252:253], v18 offset:36864\n"
    "ds_read_b64_tr_b16 v[254:255], v18 offset:37376\n"
    "s_waitcnt lgkmcnt(0)\n"
    "s_barrier\n"
    "s_waitcnt vmcnt(0)\n"
    "v_mov_b32 v5, v11\n"
    "v_mov_b32 v12, -1\n"
    "s_mov_b32 s73, 0x80000000\n"
    "s_branch .LgWO_block1_1_%=\n"
    ".LgWO_LOOPB_%=:\n"
    ".LgWO_head0_%=:\n"
    "v_mfma_f32_16x16x32_bf16 v[48:51], a[0:3], v[16:19], 0\n"
    "v_mfma_f32_16x16x32_bf16 v[52:55], a[4:7], v[16:19], 0\n"
    "s_waitcnt lgkmcnt(2)\n"
    "v_cvt_pk_bf16_f32 v56, v56, v57\n"
    "v_cvt_pk_bf16_f32 v57, v58, v59\n"
    "v_mfma_f32_16x16x32_bf16 v[64:67], a[8:11], v[16:19], 0\n"
    "ds_read_b128 v[16:19], v0 offset:4096\n"
    "v_cvt_pk_bf16_f32 v58, v60, v61\n"
    "v_mfma_f32_16x16x32_bf16 v[48:51], a[12:15], v[20:23], v[48:51]\n"
    "v_cvt_pk_bf16_f32 v59, v62, v63\n"
    "v_mfma_f32_16x16x32_bf16 v[52:55], a[16:19], v[20:23], v[52:55]\n"
    "s_waitcnt lgkmcnt(2)\n"
    "v_cvt_pk_bf16_f32 v68, v68, v69\n"
    "v_cvt_pk_bf16_f32 v69, v70, v71\n"
    "v_mfma_f32_16x16x32_bf16 v[64:67], a[20:23], v[20:23], v[64:67]\n"
    "ds_read_b128 v[20:23], v0 offset:5120\n"
    "s_nop 0\n"
    "v_mfma_f32_16x16x32_bf16 v[48:51], a[24:27], v[24:27], v[48:51]\n"
    "buffer_store_dwordx4 v[56:59], v15, s[36:39], s73 offen offset:1024 sc1\n"
    "v_mfma_f32_16x16x32_bf16 v[52:55], a[28:31], v[24:27], v[52:55]\n"
    "s_waitcnt lgkmcnt(2)\n"
    "s_waitcnt vmcnt(2)\n"
    "v_add_u32 v13, v11, v3\n"
    "v_mfma_f32_16x16x32_bf16 v[64:67], a[32:35], v[24:27], v[64:67]\n"
    "ds_read_b128 v[24:27], v0 offset:6144\n"
    "v_cmp_gt_i32 vcc, s71, v13\n"
    "v_mfma_f32_16x16x32_bf16 v[48:51], a[36:39], v[28:31], v[48:51]\n"
    "buffer_atomic_smax v7, v10, s[36:39], s69 offen sc1\n"
    "v_permlane16_swap_b32 v68, v70\n"
    "v_mfma_f32_16x16x32_bf16 v[52:55], a[40:43], v[28:31], v[52:55]\n"
    "s_waitcnt lgkmcnt(2)\n"
    "v_permlane16_swap_b32 v69, v71\n"
    "v_mfma_f32_16x16x32_bf16 v[64:67], a[44:47], v[28:31], v[64:67]\n"
    "ds_read_b128 v[28:31], v0 offset:7168\n"
    "s_cbranch_vccnz .LgWO_poll0_%=\n"
    "v_mfma_f32_16x16x32_bf16 v[48:51], a[48:51], v[16:19], v[48:51]\n"
    "s_add_u32 m0, s59, 32768\n"
    "v_mfma_f32_16x16x32_bf16 v[52:55], a[52:55], v[16:19], v[52:55]\n"
    "s_waitcnt lgkmcnt(2)\n"
    "buffer_load_dwordx4 v1, s[36:39], s76 offen sc1 lds\n"
    "v_mfma_f32_16x16x32_bf16 v[64:67], a[56:59], v[16:19], v[64:67]\n"
    "ds_read_b128 v[16:19], v0 offset:8192\n"
    "v_mfma_f32_16x16x32_bf16 v[48:51], a[60:63], v[20:23], v[48:51]\n"
    "buffer_store_dwordx4 v[68:71], v4, s[36:39], s73 offen offset:1024 sc1\n"
    "v_mfma_f32_16x16x32_bf16 v[52:55], a[64:67], v[20:23], v[52:55]\n"
    "s_waitcnt lgkmcnt(2)\n"
    "s_add_u32 s76, s76, 16384\n"
    "s_lshr_b32 s73, s68, 1\n"
    "v_mfma_f32_16x16x32_bf16 v[64:67], a[68:71], v[20:23], v[64:67]\n"
    "ds_read_b128 v[20:23], v0 offset:9216\n"
    "s_and_b32 s73, s73, s57\n"
    "v_mfma_f32_16x16x32_bf16 v[48:51], a[72:75], v[24:27], v[48:51]\n"
    "s_add_u32 m0, s59, 36864\n"
    "s_mul_i32 s73, s73, s56\n"
    "v_mfma_f32_16x16x32_bf16 v[52:55], a[76:79], v[24:27], v[52:55]\n"
    "s_waitcnt lgkmcnt(2)\n"
    "buffer_load_dwordx4 v1, s[36:39], s76 offen sc1 lds\n"
    "s_add_u32 s73, s73, s55\n"
    "v_mfma_f32_16x16x32_bf16 v[64:67], a[80:83], v[24:27], v[64:67]\n"
    "ds_read_b128 v[24:27], v0 offset:10240\n"
    "v_mfma_f32_16x16x32_bf16 v[48:51], a[84:87], v[28:31], v[48:51]\n"
    "s_add_u32 s76, s76, 16384\n"
    "s_add_u32 m0, s59, 40960\n"
    "v_mfma_f32_16x16x32_bf16 v[52:55], a[88:91], v[28:31], v[52:55]\n"
    "s_waitcnt lgkmcnt(2)\n"
    "buffer_load_dwordx4 v1, s[36:39], s76 offen sc1 lds\n"
    "v_mfma_f32_16x16x32_bf16 v[64:67], a[92:95], v[28:31], v[64:67]\n"
    "ds_read_b128 v[28:31], v0 offset:11264\n"
    "v_mfma_f32_16x16x32_bf16 v[48:51], a[96:99], v[16:19], v[48:51]\n"
    "v_mfma_f32_16x16x32_bf16 v[52:55], a[100:103], v[16:19], v[52:55]\n"
    "s_waitcnt lgkmcnt(2)\n"
    "v_mfma_f32_16x16x32_bf16 v[64:67], a[104:107], v[16:19], v[64:67]\n"
    "ds_read_b128 v[16:19], v0 offset:12288\n"
    "s_add_u32 s76, s76, 16384\n"
    "v_mfma_f32_16x16x32_bf16 v[48:51], a[108:111], v[20:23], v[48:51]\n"
    "s_add_u32 m0, s59, 45056\n"
    "v_mfma_f32_16x16x32_bf16 v[52:55], a[112:115], v[20:23], v[52:55]\n"
    "s_waitcnt lgkmcnt(2)\n"
    "buffer_load_dwordx4 v1, s[36:39], s76 offen sc1 lds\n"
    "v_mfma_f32_16x16x32_bf16 v[64:67], a[116:119], v[20:23], v[64:67]\n"
    "ds_read_b128 v[20:23], v0 offset:13312\n"
    "v_mfma_f32_16x16x32_bf16 v[48:51], a[120:123], v[24:27], v[48:51]\n"
    "v_mfma_f32_16x16x32_bf16 v[52:55], a[124:127], v[24:27], v[52:55]\n"
    "s_waitcnt lgkmcnt(2)\n"
    "v_mfma_f32_16x16x32_bf16 v[64:67], a[128:131], v[24:27], v[64:67]\n"
    "ds_read_b128 v[24:27], v0 offset:14336\n"
    "s_add_u32 s76, s76, 16384\n"
    "v_mfma_f32_16x16x32_bf16 v[48:51], a[132:135], v[28:31], v[48:51]\n"
    "s_add_u32 m0, s59, 49152\n"
    "v_mfma_f32_16x16x32_bf16 v[52:55], a[136:139], v[28:31], v[52:55]\n"
    "s_waitcnt lgkmcnt(2)\n"
    "buffer_load_dwordx4 v1, s[36:39], s76 offen sc1 lds\n"
    "v_mfma_f32_16x16x32_bf16 v[64:67], a[140:143], v[28:31], v[64:67]\n"
    "ds_read_b128 v[28:31], v0 offset:15360\n"
    "v_mfma_f32_16x16x32_bf16 v[48:51], a[144:147], v[16:19], v[48:51]\n"
    "v_mfma_f32_16x16x32_bf16 v[52:55], a[148:151], v[16:19], v[52:55]\n"
    "s_waitcnt lgkmcnt(2)\n"
    "v_mfma_f32_16x16x32_bf16 v[64:67], a[152:155], v[16:19], v[64:67]\n"
    "ds_read_b128 v[16:19], v0 offset:16384\n"
    "s_add_u32 s76, s76, 16384\n"
    "v_mfma_f32_16x16x32_bf16 v[48:51], a[156:159], v[20:23], v[48:51]\n"
    "s_add_u32 m0, s59, 53248\n"
    "v_mfma_f32_16x16x32_bf16 v[52:55], a[160:163], v[20:23], v[52:55]\n"
    "s_waitcnt lgkmcnt(2)\n"
    "buffer_load_dwordx4 v1, s[36:39], s76 offen sc1 lds\n"
    "v_mfma_f32_16x16x32_bf16 v[64:67], a[164:167], v[20:23], v[64:67]\n"
    "ds_read_b128 v[20:23], v0 offset:17408\n"
    "v_mfma_f32_16x16x32_bf16 v[48:51], a[168:171], v[24:27], v[48:51]\n"
    "v_mfma_f32_16x16x32_bf16 v[52:55], a[172:175], v[24:27], v[52:55]\n"
    "s_waitcnt lgkmcnt(2)\n"
    "s_add_u32 s76, s76, 16384\n"
    "s_add_u32 m0, s59, 57344\n"
    "v_mfma_f32_16x16x32_bf16 v[64:67], a[176:179], v[24:27], v[64:67]\n"
    "ds_read_b128 v[24:27], v0 offset:18432\n"
    "buffer_load_dwordx4 v1, s[36:39], s76 offen sc1 lds\n"
    "v_mfma_f32_16x16x32_bf16 v[48:51], a[180:183], v[28:31], v[48:51]\n"
    "v_mfma_f32_16x16x32_bf16 v[52:55], a[184:187], v[28:31], v[52:55]\n"
    "s_waitcnt lgkmcnt(2)\n"
    "v_mfma_f32_16x16x32_bf16 v[64:67], a[188:191], v[28:31], v[64:67]\n"
    "ds_read_b128 v[28:31], v0 offset:19456\n"
    "v_mfma_f32_16x16x32_bf16 v[48:51], a[192:195], v[16:19], v[48:51]\n"
    "v_mfma_f32_16x16x32_bf16 v[52:55], a[196:199], v[16:19], v[52:55]\n"
    "s_waitcnt lgkmcnt(2)\n"
    "s_add_u32 s76, s76, 16384\n"
    "s_add_u32 m0, s59, 61440\n"
    "v_mfma_f32_16x16x32_bf16 v[64:67], a[200:203], v[16:19], v[64:67]\n"
    "ds_read_b128 v[16:19], v0 offset:20480\n"
    "buffer_load_dwordx4 v1, s[36:39], s76 offen sc1 lds\n"
    "v_mfma_f32_16x16x32_bf16 v[48:51], a[204:207], v[20:23], v[48:51]\n"
    "v_mfma_f32_16x16x32_bf16 v[52:55], a[208:211], v[20:23], v[52:55]\n"
    "s_waitcnt lgkmcnt(2)\n"
    "v_mfma_f32_16x16x32_bf16 v[64:67], a[212:215], v[20:23], v[64:67]\n"
    "ds_read_b128 v[20:23], v0 offset:21504\n"
    "v_mfma_f32_16x16x32_bf16 v[48:51], a[216:219], v[24:27], v[48:51]\n"
    "v_mfma_f32_16x16x32_bf16 v[52:55], a[220:223], v[24:27], v[52:55]\n"
    "s_waitcnt lgkmcnt(2)\n"
    "s_waitcnt vmcnt(7)\n"
    "buffer_atomic_smax v12, v10, s[36:39], s70 offen sc1\n"
    "v_mfma_f32_16x16x32_bf16 v[64:67], a[224:227], v[24:27], v[64:67]\n"
    "ds_read_b128 v[24:27], v0 offset:22528\n"
    "v_mfma_f32_16x16x32_bf16 v[48:51], a[228:231], v[28:31], v[48:51]\n"
    "v_mfma_f32_16x16x32_bf16 v[52:55], a[232:235], v[28:31], v[52:55]\n"
    "s_waitcnt lgkmcnt(2)\n"
    "v_mfma_f32_16x16x32_bf16 v[64:67], a[236:239], v[28:31], v[64:67]\n"
    "ds_read_b128 v[28:31], v0 offset:23552\n"
    ".LgWO_join0_%=:\n"
    "v_mfma_f32_16x16x32_bf16 v[48:51], a[240:243], v[16:19], v[48:51]\n"
    "s_add_u32 s71, s68, 2\n"
    "s_waitcnt vmcnt(8)\n"
    "v_mfma_f32_16x16x32_bf16 v[52:55], a[244:247], v[16:19], v[52:55]\n"
    "s_waitcnt lgkmcnt(2)\n"
    "v_add_u32 v13, v11, v3\n"
    "v_mfma_f32_16x16x32_bf16 v[64:67], a[248:251], v[16:19], v[64:67]\n"
    "ds_read_b128 v[16:19], v0 offset:24576\n"
    "v_cmp_gt_i32 vcc, s71, v13\n"
    "v_mfma_f32_16x16x32_bf16 v[48:51], a[252:255], v[20:23], v[48:51]\n"
    "v_mfma_f32_16x16x32_bf16 v[52:55], v[128:131], v[20:23], v[52:55]\n"
    "s_waitcnt lgkmcnt(2)\n"
    "v_mfma_f32_16x16x32_bf16 v[64:67], v[132:135], v[20:23], v[64:67]\n"
    "ds_read_b128 v[20:23], v0 offset:25600\n"
    "v_mfma_f32_16x16x32_bf16 v[48:51], v[136:139], v[24:27], v[48:51]\n"
    "s_or_b64 exec, vcc, s[62:63]\n"
    "buffer_atomic_smax v11, v2, s[36:39], 0 offen sc0 sc1\n"
    "s_mov_b64 exec, -1\n"
    "v_mfma_f32_16x16x32_bf16 v[52:55], v[140:143], v[24:27], v[52:55]\n"
    "s_waitcnt lgkmcnt(2)\n"
    "s_lshr_b32 s76, s71, 1\n"
    "v_mfma_f32_16x16x32_bf16 v[64:67], v[144:147], v[24:27], v[64:67]\n"
    "ds_read_b128 v[24:27], v0 offset:26624\n"
    "s_and_b32 s76, s76, s54\n"
    "v_mfma_f32_16x16x32_bf16 v[48:51], v[148:151], v[28:31], v[48:51]\n"
    "s_mul_i32 s76, s76, s53\n"
    "v_mfma_f32_16x16x32_bf16 v[52:55], v[152:155], v[28:31], v[52:55]\n"
    "s_waitcnt lgkmcnt(2)\n"
    "s_add_u32 s76, s76, s52\n"
    "v_mfma_f32_16x16x32_bf16 v[64:67], v[156:159], v[28:31], v[64:67]\n"
    "ds_read_b128 v[28:31], v0 offset:27648\n"
    "v_mfma_f32_16x16x32_bf16 v[48:51], v[160:163], v[16:19], v[48:51]\n"
    "v_mfma_f32_16x16x32_bf16 v[52:55], v[164:167], v[16:19], v[52:55]\n"
    "s_waitcnt lgkmcnt(2)\n"
    "v_mfma_f32_16x16x32_bf16 v[64:67], v[168:171], v[16:19], v[64:67]\n"
    "ds_read_b128 v[16:19], v0 offset:28672\n"
    "v_mfma_f32_16x16x32_bf16 v[48:51], v[172:175], v[20:23], v[48:51]\n"
    "v_mfma_f32_16x16x32_bf16 v[52:55], v[176:179], v[20:23], v[52:55]\n"
    "s_waitcnt lgkmcnt(2)\n"
    "v_mfma_f32_16x16x32_bf16 v[64:67], v[180:183], v[20:23], v[64:67]\n"
    "ds_read_b128 v[20:23], v0 offset:29696\n"
    "v_mfma_f32_16x16x32_bf16 v[48:51], v[184:187], v[24:27], v[48:51]\n"
    "v_mfma_f32_16x16x32_bf16 v[52:55], v[188:191], v[24:27], v[52:55]\n"
    "s_waitcnt lgkmcnt(2)\n"
    "v_mfma_f32_16x16x32_bf16 v[64:67], v[192:195], v[24:27], v[64:67]\n"
    "ds_read_b128 v[24:27], v0 offset:30720\n"
    "v_mfma_f32_16x16x32_bf16 v[48:51], v[196:199], v[28:31], v[48:51]\n"
    "v_mfma_f32_16x16x32_bf16 v[52:55], v[200:203], v[28:31], v[52:55]\n"
    "s_waitcnt lgkmcnt(2)\n"
    "v_add_u32 v13, v5, v3\n"
    "v_mfma_f32_16x16x32_bf16 v[64:67], v[204:207], v[28:31], v[64:67]\n"
    "ds_read_b128 v[28:31], v0 offset:31744\n"
    "v_cmp_gt_i32 vcc, s71, v13\n"
    "v_mfma_f32_16x16x32_bf16 v[48:51], v[208:211], v[16:19], v[48:51]\n"
    "v_mfma_f32_16x16x32_bf16 v[52:55], v[212:215], v[16:19], v[52:55]\n"
    "s_waitcnt lgkmcnt(2)\n"
    "v_mfma_f32_16x16x32_bf16 v[64:67], v[216:219], v[16:19], v[64:67]\n"
    "v_mfma_f32_16x16x32_bf16 v[48:51], v[220:223], v[20:23], v[48:51]\n"
    "s_waitcnt vmcnt(1) lgkmcnt(0)\n"
    "s_barrier\n"
    "ds_read_b128 v[16:19], v9 offset:0\n"
    "s_or_b64 exec, vcc, s[62:63]\n"
    "buffer_atomic_smax v5, v2, s[36:39], 0 offen sc0 sc1\n"
    "s_mov_b64 exec, -1\n"
    "v_mfma_f32_16x16x32_bf16 v[52:55], v[224:227], v[20:23], v[52:55]\n"
    "v_mfma_f32_16x16x32_bf16 v[64:67], v[228:231], v[20:23], v[64:67]\n"
    "ds_read_b128 v[20:23], v9 offset:1024\n"
    "v_mfma_f32_16x16x32_bf16 v[48:51], v[232:235], v[24:27], v[48:51]\n"
    "v_mfma_f32_16x16x32_bf16 v[52:55], v[236:239], v[24:27], v[52:55]\n"
    "v_mfma_f32_16x16x32_bf16 v[64:67], v[240:243], v[24:27], v[64:67]\n"
    "ds_read_b128 v[24:27], v9 offset:2048\n"
    "v_mfma_f32_16x16x32_bf16 v[48:51], v[244:247], v[28:31], v[48:51]\n"
    "s_mov_b32 s69, 0x80000000\n"
    "s_mov_b32 s70, 0\n"
    "v_mov_b32 v12, s68\n"
    "v_mfma_f32_16x16x32_bf16 v[52:55], v[248:251], v[28:31], v[52:55]\n"
    "s_waitcnt lgkmcnt(2)\n"
    "s_add_u32 s68, s68, 1\n"
    "v_mfma_f32_16x16x32_bf16 v[64:67], v[252:255], v[28:31], v[64:67]\n"
    "ds_read_b128 v[28:31], v9 offset:3072\n"
    ".LgWO_head1_%=:\n"
    "v_mfma_f32_16x16x32_bf16 v[56:59], a[0:3], v[16:19], 0\n"
    "v_mfma_f32_16x16x32_bf16 v[60:63], a[4:7], v[16:19], 0\n"
    "s_waitcnt lgkmcnt(2)\n"
    "v_cvt_pk_bf16_f32 v48, v48, v49\n"
    "v_cvt_pk_bf16_f32 v49, v50, v51\n"
    "v_mfma_f32_16x16x32_bf16 v[68:71], a[8:11], v[16:19], 0\n"
    "ds_read_b128 v[16:19], v9 offset:4096\n"
    "v_cvt_pk_bf16_f32 v50, v52, v53\n"
    "v_mfma_f32_16x16x32_bf16 v[56:59], a[12:15], v[20:23], v[56:59]\n"
    "v_cvt_pk_bf16_f32 v51, v54, v55\n"
    "v_mfma_f32_16x16x32_bf16 v[60:63], a[16:19], v[20:23], v[60:63]\n"
    "s_waitcnt lgkmcnt(2)\n"
    "v_cvt_pk_bf16_f32 v64, v64, v65\n"
    "v_cvt_pk_bf16_f32 v65, v66, v67\n"
    "v_mfma_f32_16x16x32_bf16 v[68:71], a[20:23], v[20:23], v[68:71]\n"
    "ds_read_b128 v[20:23], v9 offset:5120\n"
    "s_nop 0\n"
    "v_mfma_f32_16x16x32_bf16 v[56:59], a[24:27], v[24:27], v[56:59]\n"
    "buffer_store_dwordx4 v[48:51], v15, s[36:39], s73 offen sc1\n"
    "v_mfma_f32_16x16x32_bf16 v[60:63], a[28:31], v[24:27], v[60:63]\n"
    "s_waitcnt lgkmcnt(2)\n"
    "s_waitcnt vmcnt(2)\n"
    "v_add_u32 v13, v11, v3\n"
    "v_mfma_f32_16x16x32_bf16 v[68:71], a[32:35], v[24:27], v[68:71]\n"
    "ds_read_b128 v[24:27], v9 offset:6144\n"
    "v_cmp_gt_i32 vcc, s71, v13\n"
    "v_mfma_f32_16x16x32_bf16 v[56:59], a[36:39], v[28:31], v[56:59]\n"
    "buffer_atomic_smax v7, v14, s[36:39], s69 offen sc1\n"
    "v_permlane16_swap_b32 v64, v66\n"
    "v_mfma_f32_16x16x32_bf16 v[60:63], a[40:43], v[28:31], v[60:63]\n"
    "s_waitcnt lgkmcnt(2)\n"
    "v_permlane16_swap_b32 v65, v67\n"
    "v_mfma_f32_16x16x32_bf16 v[68:71], a[44:47], v[28:31], v[68:71]\n"
    "ds_read_b128 v[28:31], v9 offset:7168\n"
    "s_cbranch_vccnz .LgWO_poll1_%=\n"
    "v_mfma_f32_16x16x32_bf16 v[56:59], a[48:51], v[16:19], v[56:59]\n"
    "s_add_u32 m0, s59, 0\n"
    "v_mfma_f32_16x16x32_bf16 v[60:63], a[52:55], v[16:19], v[60:63]\n"
    "s_waitcnt lgkmcnt(2)\n"
    "buffer_load_dwordx4 v1, s[36:39], s76 offen sc1 lds\n"
    "v_mfma_f32_16x16x32_bf16 v[68:71], a[56:59], v[16:19], v[68:71]\n"
    "ds_read_b128 v[16:19], v9 offset:8192\n"
    "v_mfma_f32_16x16x32_bf16 v[56:59], a[60:63], v[20:23], v[56:59]\n"
    "buffer_store_dwordx4 v[64:67], v4, s[36:39], s73 offen sc1\n"
    "v_mfma_f32_16x16x32_bf16 v[60:63], a[64:67], v[20:23], v[60:63]\n"
    "s_waitcnt lgkmcnt(2)\n"
    "s_add_u32 s76, s76, 16384\n"
    "s_lshr_b32 s73, s68, 1\n"
    "v_mfma_f32_16x16x32_bf16 v[68:71], a[68:71], v[20:23], v[68:71]\n"
    "ds_read_b128 v[20:23], v9 offset:9216\n"
    "s_and_b32 s73, s73, s57\n"
    "v_mfma_f32_16x16x32_bf16 v[56:59], a[72:75], v[24:27], v[56:59]\n"
    "s_add_u32 m0, s59, 4096\n"
    "s_mul_i32 s73, s73, s56\n"
    "v_mfma_f32_16x16x32_bf16 v[60:63], a[76:79], v[24:27], v[60:63]\n"
    "s_waitcnt lgkmcnt(2)\n"
    "buffer_load_dwordx4 v1, s[36:39], s76 offen sc1 lds\n"
    "s_add_u32 s73, s73, s55\n"
    "v_mfma_f32_16x16x32_bf16 v[68:71], a[80:83], v[24:27], v[68:71]\n"
    "ds_read_b128 v[24:27], v9 offset:10240\n"
    "v_mfma_f32_16x16x32_bf16 v[56:59], a[84:87], v[28:31], v[56:59]\n"
    "s_add_u32 s76, s76, 16384\n"
    "s_add_u32 m0, s59, 8192\n"
    "v_mfma_f32_16x16x32_bf16 v[60:63], a[88:91], v[28:31], v[60:63]\n"
    "s_waitcnt lgkmcnt(2)\n"
    "buffer_load_dwordx4 v1, s[36:39], s76 offen sc1 lds\n"
    "v_mfma_f32_16x16x32_bf16 v[68:71], a[92:95], v[28:31], v[68:71]\n"
    "ds_read_b128 v[28:31], v9 offset:11264\n"
    "v_mfma_f32_16x16x32_bf16 v[56:59], a[96:99], v[16:19], v[56:59]\n"
    "v_mfma_f32_16x16x32_bf16 v[60:63], a[100:103], v[16:19], v[60:63]\n"
    "s_waitcnt lgkmcnt(2)\n"
    "v_mfma_f32_16x16x32_bf16 v[68:71], a[104:107], v[16:19], v[68:71]\n"
    "ds_read_b128 v[16:19], v9 offset:12288\n"
    "s_add_u32 s76, s76, 16384\n"
    "v_mfma_f32_16x16x32_bf16 v[56:59], a[108:111], v[20:23], v[56:59]\n"
    "s_add_u32 m0, s59, 12288\n"
    "v_mfma_f32_16x16x32_bf16 v[60:63], a[112:115], v[20:23], v[60:63]\n"
    "s_waitcnt lgkmcnt(2)\n"
    "buffer_load_dwordx4 v1, s[36:39], s76 offen sc1 lds\n"
    "v_mfma_f32_16x16x32_bf16 v[68:71], a[116:119], v[20:23], v[68:71]\n"
    "ds_read_b128 v[20:23], v9 offset:13312\n"
    "v_mfma_f32_16x16x32_bf16 v[56:59], a[120:123], v[24:27], v[56:59]\n"
    "v_mfma_f32_16x16x32_bf16 v[60:63], a[124:127], v[24:27], v[60:63]\n"
    "s_waitcnt lgkmcnt(2)\n"
    "v_mfma_f32_16x16x32_bf16 v[68:71], a[128:131], v[24:27], v[68:71]\n"
    "ds_read_b128 v[24:27], v9 offset:14336\n"
    "s_add_u32 s76, s76, 16384\n"
    "v_mfma_f32_16x16x32_bf16 v[56:59], a[132:135], v[28:31], v[56:59]\n"
    "s_add_u32 m0, s59, 16384\n"
    "v_mfma_f32_16x16x32_bf16 v[60:63], a[136:139], v[28:31], v[60:63]\n"
    "s_waitcnt lgkmcnt(2)\n"
    "buffer_load_dwordx4 v1, s[36:39], s76 offen sc1 lds\n"
    "v_mfma_f32_16x16x32_bf16 v[68:71], a[140:143], v[28:31], v[68:71]\n"
    "ds_read_b128 v[28:31], v9 offset:15360\n"
    "v_mfma_f32_16x16x32_bf16 v[56:59], a[144:147], v[16:19], v[56:59]\n"
    "v_mfma_f32_16x16x32_bf16 v[60:63], a[148:151], v[16:19], v[60:63]\n"
    "s_waitcnt lgkmcnt(2)\n"
    "v_mfma_f32_16x16x32_bf16 v[68:71], a[152:155], v[16:19], v[68:71]\n"
    "ds_read_b128 v[16:19], v9 offset:16384\n"
    "s_add_u32 s76, s76, 16384\n"
    "v_mfma_f32_16x16x32_bf16 v[56:59], a[156:159], v[20:23], v[56:59]\n"
    "s_add_u32 m0, s59, 20480\n"
    "v_mfma_f32_16x16x32_bf16 v[60:63], a[160:163], v[20:23], v[60:63]\n"
    "s_waitcnt lgkmcnt(2)\n"
    "buffer_load_dwordx4 v1, s[36:39], s76 offen sc1 lds\n"
    "v_mfma_f32_16x16x32_bf16 v[68:71], a[164:167], v[20:23], v[68:71]\n"
    "ds_read_b128 v[20:23], v9 offset:17408\n"
    "v_mfma_f32_16x16x32_bf16 v[56:59], a[168:171], v[24:27], v[56:59]\n"
    "v_mfma_f32_16x16x32_bf16 v[60:63], a[172:175], v[24:27], v[60:63]\n"
    "s_waitcnt lgkmcnt(2)\n"
    "s_add_u32 s76, s76, 16384\n"
    "s_add_u32 m0, s59, 24576\n"
    "v_mfma_f32_16x16x32_bf16 v[68:71], a[176:179], v[24:27], v[68:71]\n"
    "ds_read_b128 v[24:27], v9 offset:18432\n"
    "buffer_load_dwordx4 v1, s[36:39], s76 offen sc1 lds\n"
    "v_mfma_f32_16x16x32_bf16 v[56:59], a[180:183], v[28:31], v[56:59]\n"
    "v_mfma_f32_16x16x32_bf16 v[60:63], a[184:187], v[28:31], v[60:63]\n"
    "s_waitcnt lgkmcnt(2)\n"
    "v_mfma_f32_16x16x32_bf16 v[68:71], a[188:191], v[28:31], v[68:71]\n"
    "ds_read_b128 v[28:31], v9 offset:19456\n"
    "v_mfma_f32_16x16x32_bf16 v[56:59], a[192:195], v[16:19], v[56:59]\n"
    "v_mfma_f32_16x16x32_bf16 v[60:63], a[196:199], v[16:19], v[60:63]\n"
    "s_waitcnt lgkmcnt(2)\n"
    "s_add_u32 s76, s76, 16384\n"
    "s_add_u32 m0, s59, 28672\n"
    "v_mfma_f32_16x16x32_bf16 v[68:71], a[200:203], v[16:19], v[68:71]\n"
    "ds_read_b128 v[16:19], v9 offset:20480\n"
    "buffer_load_dwordx4 v1, s[36:39], s76 offen sc1 lds\n"
    "v_mfma_f32_16x16x32_bf16 v[56:59], a[204:207], v[20:23], v[56:59]\n"
    "v_mfma_f32_16x16x32_bf16 v[60:63], a[208:211], v[20:23], v[60:63]\n"
    "s_waitcnt lgkmcnt(2)\n"
    "v_mfma_f32_16x16x32_bf16 v[68:71], a[212:215], v[20:23], v[68:71]\n"
    "ds_read_b128 v[20:23], v9 offset:21504\n"
    "v_mfma_f32_16x16x32_bf16 v[56:59], a[216:219], v[24:27], v[56:59]\n"
    "v_mfma_f32_16x16x32_bf16 v[60:63], a[220:223], v[24:27], v[60:63]\n"
    "s_waitcnt lgkmcnt(2)\n"
    "s_waitcnt vmcnt(7)\n"
    "buffer_atomic_smax v12, v14, s[36:39], s70 offen sc1\n"
    "v_mfma_f32_16x16x32_bf16 v[68:71], a[224:227], v[24:27], v[68:71]\n"
    "ds_read_b128 v[24:27], v9 offset:22528\n"
    "v_mfma_f32_16x16x32_bf16 v[56:59], a[228:231], v[28:31], v[56:59]\n"
    "v_mfma_f32_16x16x32_bf16 v[60:63], a[232:235], v[28:31], v[60:63]\n"
    "s_waitcnt lgkmcnt(2)\n"
    "v_mfma_f32_16x16x32_bf16 v[68:71], a[236:239], v[28:31], v[68:71]\n"
    "ds_read_b128 v[28:31], v9 offset:23552\n"
    ".LgWO_join1_%=:\n"
    "v_mfma_f32_16x16x32_bf16 v[56:59], a[240:243], v[16:19], v[56:59]\n"
    "s_add_u32 s71, s68, 2\n"
    "s_waitcnt vmcnt(8)\n"
    "v_mfma_f32_16x16x32_bf16 v[60:63], a[244:247], v[16:19], v[60:63]\n"
    "s_waitcnt lgkmcnt(2)\n"
    "v_add_u32 v13, v11, v3\n"
    "v_mfma_f32_16x16x32_bf16 v[68:71], a[248:251], v[16:19], v[68:71]\n"
    "ds_read_b128 v[16:19], v9 offset:24576\n"
    "v_cmp_gt_i32 vcc, s71, v13\n"
    "v_mfma_f32_16x16x32_bf16 v[56:59], a[252:255], v[20:23], v[56:59]\n"
    "v_mfma_f32_16x16x32_bf16 v[60:63], v[128:131], v[20:23], v[60:63]\n"
    "s_waitcnt lgkmcnt(2)\n"
    "v_mfma_f32_16x16x32_bf16 v[68:71], v[132:135], v[20:23], v[68:71]\n"
    "ds_read_b128 v[20:23], v9 offset:25600\n"
    "v_mfma_f32_16x16x32_bf16 v[56:59], v[136:139], v[24:27], v[56:59]\n"
    "s_or_b64 exec, vcc, s[62:63]\n"
    "buffer_atomic_smax v11, v2, s[36:39], 0 offen sc0 sc1\n"
    "s_mov_b64 exec, -1\n"
    "v_mfma_f32_16x16x32_bf16 v[60:63], v[140:143], v[24:27], v[60:63]\n"
    "s_waitcnt lgkmcnt(2)\n"
    "s_lshr_b32 s76, s71, 1\n"
    "v_mfma_f32_16x16x32_bf16 v[68:71], v[144:147], v[24:27], v[68:71]\n"
    "ds_read_b128 v[24:27], v9 offset:26624\n"
    "s_and_b32 s76, s76, s54\n"
    "v_mfma_f32_16x16x32_bf16 v[56:59], v[148:151], v[28:31], v[56:59]\n"
    "s_mul_i32 s76, s76, s53\n"
    "v_mfma_f32_16x16x32_bf16 v[60:63], v[152:155], v[28:31], v[60:63]\n"
    "s_waitcnt lgkmcnt(2)\n"
    "s_add_u32 s76, s76, s52\n"
    "v_mfma_f32_16x16x32_bf16 v[68:71], v[156:159], v[28:31], v[68:71]\n"
    "ds_read_b128 v[28:31], v9 offset:27648\n"
    "s_add_u32 s76, s76, 1024\n"
    "v_mfma_f32_16x16x32_bf16 v[56:59], v[160:163], v[16:19], v[56:59]\n"
    "v_mfma_f32_16x16x32_bf16 v[60:63], v[164:167], v[16:19], v[60:63]\n"
    "s_waitcnt lgkmcnt(2)\n"
    "v_mfma_f32_16x16x32_bf16 v[68:71], v[168:171], v[16:19], v[68:71]\n"
    "ds_read_b128 v[16:19], v9 offset:28672\n"
    "v_mfma_f32_16x16x32_bf16 v[56:59], v[172:175], v[20:23], v[56:59]\n"
    "v_mfma_f32_16x16x32_bf16 v[60:63], v[176:179], v[20:23], v[60:63]\n"
    "s_waitcnt lgkmcnt(2)\n"
    "v_mfma_f32_16x16x32_bf16 v[68:71], v[180:183], v[20:23], v[68:71]\n"
    "ds_read_b128 v[20:23], v9 offset:29696\n"
    "v_mfma_f32_16x16x32_bf16 v[56:59], v[184:187], v[24:27], v[56:59]\n"
    "v_mfma_f32_16x16x32_bf16 v[60:63], v[188:191], v[24:27], v[60:63]\n"
    "s_waitcnt lgkmcnt(2)\n"
    "v_mfma_f32_16x16x32_bf16 v[68:71], v[192:195], v[24:27], v[68:71]\n"
    "ds_read_b128 v[24:27], v9 offset:30720\n"
    "v_mfma_f32_16x16x32_bf16 v[56:59], v[196:199], v[28:31], v[56:59]\n"
    "v_mfma_f32_16x16x32_bf16 v[60:63], v[200:203], v[28:31], v[60:63]\n"
    "s_waitcnt lgkmcnt(2)\n"
    "v_add_u32 v13, v5, v3\n"
    "v_mfma_f32_16x16x32_bf16 v[68:71], v[204:207], v[28:31], v[68:71]\n"
    "ds_read_b128 v[28:31], v9 offset:31744\n"
    "v_cmp_gt_i32 vcc, s71, v13\n"
    "v_mfma_f32_16x16x32_bf16 v[56:59], v[208:211], v[16:19], v[56:59]\n"
    "v_mfma_f32_16x16x32_bf16 v[60:63], v[212:215], v[16:19], v[60:63]\n"
    "s_waitcnt lgkmcnt(2)\n"
    "v_mfma_f32_16x16x32_bf16 v[68:71], v[216:219], v[16:19], v[68:71]\n"
    "v_mfma_f32_16x16x32_bf16 v[56:59], v[220:223], v[20:23], v[56:59]\n"
    "s_waitcnt vmcnt(1) lgkmcnt(0)\n"
    "s_barrier\n"
    "ds_read_b128 v[16:19], v0 offset:0\n"
    "s_or_b64 exec, vcc, s[62:63]\n"
    "buffer_atomic_smax v5, v2, s[36:39], 0 offen sc0 sc1\n"
    "s_mov_b64 exec, -1\n"
    "v_mfma_f32_16x16x32_bf16 v[60:63], v[224:227], v[20:23], v[60:63]\n"
    "v_mfma_f32_16x16x32_bf16 v[68:71], v[228:231], v[20:23], v[68:71]\n"
    "ds_read_b128 v[20:23], v0 offset:1024\n"
    "v_mfma_f32_16x16x32_bf16 v[56:59], v[232:235], v[24:27], v[56:59]\n"
    "v_mfma_f32_16x16x32_bf16 v[60:63], v[236:239], v[24:27], v[60:63]\n"
    "v_mfma_f32_16x16x32_bf16 v[68:71], v[240:243], v[24:27], v[68:71]\n"
    "ds_read_b128 v[24:27], v0 offset:2048\n"
    "v_mfma_f32_16x16x32_bf16 v[56:59], v[244:247], v[28:31], v[56:59]\n"
    "s_mov_b32 s69, 0x80000000\n"
    "s_mov_b32 s70, 0\n"
    "v_mov_b32 v12, s68\n"
    "v_mfma_f32_16x16x32_bf16 v[60:63], v[248:251], v[28:31], v[60:63]\n"
    "s_waitcnt lgkmcnt(2)\n"
    "s_add_u32 s68, s68, 1\n"
    "v_mfma_f32_16x16x32_bf16 v[68:71], v[252:255], v[28:31], v[68:71]\n"
    "ds_read_b128 v[28:31], v0 offset:3072\n"
    "s_branch .LgWO_head0_%=\n"
    ".LgWO_LOOPE_%=:\n"
    ".LgWO_done_%=:\n"
    "s_waitcnt vmcnt(0)\n"
    "buffer_atomic_smax v12, v10, s[36:39], 0 offen sc1\n"
    "s_waitcnt vmcnt(0)\n"
    "s_branch .LgWO_FIN_%=\n"
    ".LgWO_poll0_%=:\n"
    "v_mfma_f32_16x16x32_bf16 v[48:51], a[48:51], v[16:19], v[48:51]\n"
    "s_or_b64 exec, vcc, s[62:63]\n"
    "buffer_atomic_smax v11, v2, s[36:39], 0 offen sc0 sc1\n"
    "s_mov_b64 exec, -1\n"
    "v_mfma_f32_16x16x32_bf16 v[52:55], a[52:55], v[16:19], v[52:55]\n"
    "s_waitcnt lgkmcnt(2)\n"
    "v_mfma_f32_16x16x32_bf16 v[64:67], a[56:59], v[16:19], v[64:67]\n"
    "ds_read_b128 v[16:19], v0 offset:8192\n"
    "v_mfma_f32_16x16x32_bf16 v[48:51], a[60:63], v[20:23], v[48:51]\n"
    "buffer_store_dwordx4 v[68:71], v4, s[36:39], s73 offen offset:1024 sc1\n"
    "v_mfma_f32_16x16x32_bf16 v[52:55], a[64:67], v[20:23], v[52:55]\n"
    "s_waitcnt lgkmcnt(2)\n"
    "s_lshr_b32 s73, s68, 1\n"
    "v_mfma_f32_16x16x32_bf16 v[64:67], a[68:71], v[20:23], v[64:67]\n"
    "ds_read_b128 v[20:23], v0 offset:9216\n"
    "s_and_b32 s73, s73, s57\n"
    "v_mfma_f32_16x16x32_bf16 v[48:51], a[72:75], v[24:27], v[48:51]\n"
    "s_mul_i32 s73, s73, s56\n"
    "v_mfma_f32_16x16x32_bf16 v[52:55], a[76:79], v[24:27], v[52:55]\n"
    "s_waitcnt lgkmcnt(2)\n"
    "s_add_u32 s73, s73, s55\n"
    "v_mfma_f32_16x16x32_bf16 v[64:67], a[80:83], v[24:27], v[64:67]\n"
    "ds_read_b128 v[24:27], v0 offset:10240\n"
    "v_mfma_f32_16x16x32_bf16 v[48:51], a[84:87], v[28:31], v[48:51]\n"
    "v_mfma_f32_16x16x32_bf16 v[52:55], a[88:91], v[28:31], v[52:55]\n"
    "s_waitcnt lgkmcnt(2)\n"
    "v_mfma_f32_16x16x32_bf16 v[64:67], a[92:95], v[28:31], v[64:67]\n"
    "ds_read_b128 v[28:31], v0 offset:11264\n"
    "v_mfma_f32_16x16x32_bf16 v[48:51], a[96:99], v[16:19], v[48:51]\n"
    "v_mfma_f32_16x16x32_bf16 v[52:55], a[100:103], v[16:19], v[52:55]\n"
    "s_waitcnt lgkmcnt(2)\n"
    "v_mfma_f32_16x16x32_bf16 v[64:67], a[104:107], v[16:19], v[64:67]\n"
    "ds_read_b128 v[16:19], v0 offset:12288\n"
    "v_mfma_f32_16x16x32_bf16 v[48:51], a[108:111], v[20:23], v[48:51]\n"
    "v_mfma_f32_16x16x32_bf16 v[52:55], a[112:115], v[20:23], v[52:55]\n"
    "s_waitcnt lgkmcnt(2)\n"
    "s_waitcnt vmcnt(4)\n"
    "v_add_u32 v13, v5, v3\n"
    "v_mfma_f32_16x16x32_bf16 v[64:67], a[116:119], v[20:23], v[64:67]\n"
    "ds_read_b128 v[20:23], v0 offset:13312\n"
    "v_cmp_gt_i32 vcc, s71, v13\n"
    "v_mfma_f32_16x16x32_bf16 v[48:51], a[120:123], v[24:27], v[48:51]\n"
    "v_mfma_f32_16x16x32_bf16 v[52:55], a[124:127], v[24:27], v[52:55]\n"
    "s_waitcnt lgkmcnt(2)\n"
    "v_mfma_f32_16x16x32_bf16 v[64:67], a[128:131], v[24:27], v[64:67]\n"
    "ds_read_b128 v[24:27], v0 offset:14336\n"
    "s_cbranch_vccz .LgWO_dma0_33_%=\n"
    "v_mfma_f32_16x16x32_bf16 v[48:51], a[132:135], v[28:31], v[48:51]\n"
    "s_or_b64 exec, vcc, s[62:63]\n"
    "buffer_atomic_smax v5, v2, s[36:39], 0 offen sc0 sc1\n"
    "s_mov_b64 exec, -1\n"
    "v_mfma_f32_16x16x32_bf16 v[52:55], a[136:139], v[28:31], v[52:55]\n"
    "s_waitcnt lgkmcnt(2)\n"
    "v_mfma_f32_16x16x32_bf16 v[64:67], a[140:143], v[28:31], v[64:67]\n"
    "ds_read_b128 v[28:31], v0 offset:15360\n"
    "v_mfma_f32_16x16x32_bf16 v[48:51], a[144:147], v[16:19], v[48:51]\n"
    "v_mfma_f32_16x16x32_bf16 v[52:55], a[148:151], v[16:19], v[52:55]\n"
    "s_waitcnt lgkmcnt(2)\n"
    "v_mfma_f32_16x16x32_bf16 v[64:67], a[152:155], v[16:19], v[64:67]\n"
    "ds_read_b128 v[16:19], v0 offset:16384\n"
    "v_mfma_f32_16x16x32_bf16 v[48:51], a[156:159], v[20:23], v[48:51]\n"
    "v_mfma_f32_16x16x32_bf16 v[52:55], a[160:163], v[20:23], v[52:55]\n"
    "s_waitcnt lgkmcnt(2)\n"
    "v_mfma_f32_16x16x32_bf16 v[64:67], a[164:167], v[20:23], v[64:67]\n"
    "ds_read_b128 v[20:23], v0 offset:17408\n"
    "v_mfma_f32_16x16x32_bf16 v[48:51], a[168:171], v[24:27], v[48:51]\n"
    "v_mfma_f32_16x16x32_bf16 v[52:55], a[172:175], v[24:27], v[52:55]\n"
    "s_waitcnt lgkmcnt(2)\n"
    "v_mfma_f32_16x16x32_bf16 v[64:67], a[176:179], v[24:27], v[64:67]\n"
    "ds_read_b128 v[24:27], v0 offset:18432\n"
    "v_mfma_f32_16x16x32_bf16 v[48:51], a[180:183], v[28:31], v[48:51]\n"
    "v_mfma_f32_16x16x32_bf16 v[52:55], a[184:187], v[28:31], v[52:55]\n"
    "s_waitcnt lgkmcnt(2)\n"
    "v_mfma_f32_16x16x32_bf16 v[64:67], a[188:191], v[28:31], v[64:67]\n"
    "ds_read_b128 v[28:31], v0 offset:19456\n"
    "v_mfma_f32_16x16x32_bf16 v[48:51], a[192:195], v[16:19], v[48:51]\n"
    "v_mfma_f32_16x16x32_bf16 v[52:55], a[196:199], v[16:19], v[52:55]\n"
    "s_waitcnt lgkmcnt(2)\n"
    "s_waitcnt vmcnt(2)\n"
    "v_add_u32 v13, v11, v3\n"
    "v_mfma_f32_16x16x32_bf16 v[64:67], a[200:203], v[16:19], v[64:67]\n"
    "ds_read_b128 v[16:19], v0 offset:20480\n"
    "v_cmp_gt_i32 vcc, s71, v13\n"
    "v_mfma_f32_16x16x32_bf16 v[48:51], a[204:207], v[20:23], v[48:51]\n"
    "v_mfma_f32_16x16x32_bf16 v[52:55], a[208:211], v[20:23], v[52:55]\n"
    "s_waitcnt lgkmcnt(2)\n"
    "v_mfma_f32_16x16x32_bf16 v[64:67], a[212:215], v[20:23], v[64:67]\n"
    "ds_read_b128 v[20:23], v0 offset:21504\n"
    "s_cbranch_vccz .LgWO_dma0_54_%=\n"
    "v_mfma_f32_16x16x32_bf16 v[48:51], a[216:219], v[24:27], v[48:51]\n"
    "s_or_b64 exec, vcc, s[62:63]\n"
    "buffer_atomic_smax v11, v2, s[36:39], 0 offen sc0 sc1\n"
    "s_mov_b64 exec, -1\n"
    "v_mfma_f32_16x16x32_bf16 v[52:55], a[220:223], v[24:27], v[52:55]\n"
    "s_waitcnt lgkmcnt(2)\n"
    "s_waitcnt vmcnt(2)\n"
    "buffer_atomic_smax v12, v10, s[36:39], s70 offen sc1\n"
    "v_mfma_f32_16x16x32_bf16 v[64:67], a[224:227], v[24:27], v[64:67]\n"
    "ds_read_b128 v[24:27], v0 offset:22528\n"
    "v_mfma_f32_16x16x32_bf16 v[48:51], a[228:231], v[28:31], v[48:51]\n"
    "v_mfma_f32_16x16x32_bf16 v[52:55], a[232:235], v[28:31], v[52:55]\n"
    "s_waitcnt lgkmcnt(2)\n"
    "v_mfma_f32_16x16x32_bf16 v[64:67], a[236:239], v[28:31], v[64:67]\n"
    "ds_read_b128 v[28:31], v0 offset:23552\n"
    "v_mfma_f32_16x16x32_bf16 v[48:51], a[240:243], v[16:19], v[48:51]\n"
    "v_mfma_f32_16x16x32_bf16 v[52:55], a[244:247], v[16:19], v[52:55]\n"
    "s_waitcnt lgkmcnt(2)\n"
    "v_mfma_f32_16x16x32_bf16 v[64:67], a[248:251], v[16:19], v[64:67]\n"
    "ds_read_b128 v[16:19], v0 offset:24576\n"
    "v_mfma_f32_16x16x32_bf16 v[48:51], a[252:255], v[20:23], v[48:51]\n"
    "v_mfma_f32_16x16x32_bf16 v[52:55], v[128:131], v[20:23], v[52:55]\n"
    "s_waitcnt lgkmcnt(2)\n"
    "v_mfma_f32_16x16x32_bf16 v[64:67], v[132:135], v[20:23], v[64:67]\n"
    "ds_read_b128 v[20:23], v0 offset:25600\n"
    "v_mfma_f32_16x16x32_bf16 v[48:51], v[136:139], v[24:27], v[48:51]\n"
    "v_mfma_f32_16x16x32_bf16 v[52:55], v[140:143], v[24:27], v[52:55]\n"
    "s_waitcnt lgkmcnt(2)\n"
    "v_mfma_f32_16x16x32_bf16 v[64:67], v[144:147], v[24:27], v[64:67]\n"
    "ds_read_b128 v[24:27], v0 offset:26624\n"
    "v_mfma_f32_16x16x32_bf16 v[48:51], v[148:151], v[28:31], v[48:51]\n"
    "v_mfma_f32_16x16x32_bf16 v[52:55], v[152:155], v[28:31], v[52:55]\n"
    "s_waitcnt lgkmcnt(2)\n"
    "s_waitcnt vmcnt(2)\n"
    "v_add_u32 v13, v5, v3\n"
    "v_mfma_f32_16x16x32_bf16 v[64:67], v[156:159], v[28:31], v[64:67]\n"
    "ds_read_b128 v[28:31], v0 offset:27648\n"
    "v_cmp_gt_i32 vcc, s71, v13\n"
    "v_mfma_f32_16x16x32_bf16 v[48:51], v[160:163], v[16:19], v[48:51]\n"
    "v_mfma_f32_16x16x32_bf16 v[52:55], v[164:167], v[16:19], v[52:55]\n"
    "s_waitcnt lgkmcnt(2)\n"
    "v_mfma_f32_16x16x32_bf16 v[64:67], v[168:171], v[16:19], v[64:67]\n"
    "ds_read_b128 v[16:19], v0 offset:28672\n"
    "s_cbranch_vccz .LgWO_dma0_75_%=\n"
    "v_mfma_f32_16x16x32_bf16 v[48:51], v[172:175], v[20:23], v[48:51]\n"
    "s_or_b64 exec, vcc, s[62:63]\n"
    "buffer_atomic_smax v5, v2, s[36:39], 0 offen sc0 sc1\n"
    "s_mov_b64 exec, -1\n"
    "v_mfma_f32_16x16x32_bf16 v[52:55], v[176:179], v[20:23], v[52:55]\n"
    "s_waitcnt lgkmcnt(2)\n"
    "v_mfma_f32_16x16x32_bf16 v[64:67], v[180:183], v[20:23], v[64:67]\n"
    "ds_read_b128 v[20:23], v0 offset:29696\n"
    "v_mfma_f32_16x16x32_bf16 v[48:51], v[184:187], v[24:27], v[48:51]\n"
    "v_mfma_f32_16x16x32_bf16 v[52:55], v[188:191], v[24:27], v[52:55]\n"
    "s_waitcnt lgkmcnt(2)\n"
    "v_mfma_f32_16x16x32_bf16 v[64:67], v[192:195], v[24:27], v[64:67]\n"
    "ds_read_b128 v[24:27], v0 offset:30720\n"
    "v_mfma_f32_16x16x32_bf16 v[48:51], v[196:199], v[28:31], v[48:51]\n"
    "v_mfma_f32_16x16x32_bf16 v[52:55], v[200:203], v[28:31], v[52:55]\n"
    "s_waitcnt lgkmcnt(2)\n"
    "v_mfma_f32_16x16x32_bf16 v[64:67], v[204:207], v[28:31], v[64:67]\n"
    "ds_read_b128 v[28:31], v0 offset:31744\n"
    "v_mfma_f32_16x16x32_bf16 v[48:51], v[208:211], v[16:19], v[48:51]\n"
    "v_mfma_f32_16x16x32_bf16 v[52:55], v[212:215], v[16:19], v[52:55]\n"
    "s_waitcnt lgkmcnt(2)\n"
    "v_mfma_f32_16x16x32_bf16 v[64:67], v[216:219], v[16:19], v[64:67]\n"
    "v_mfma_f32_16x16x32_bf16 v[48:51], v[220:223], v[20:23], v[48:51]\n"
    "v_mfma_f32_16x16x32_bf16 v[52:55], v[224:227], v[20:23], v[52:55]\n"
    "s_waitcnt lgkmcnt(1)\n"
    "v_mfma_f32_16x16x32_bf16 v[64:67], v[228:231], v[20:23], v[64:67]\n"
    "v_mfma_f32_16x16x32_bf16 v[48:51], v[232:235], v[24:27], v[48:51]\n"
    "v_mfma_f32_16x16x32_bf16 v[52:55], v[236:239], v[24:27], v[52:55]\n"
    "s_waitcnt lgkmcnt(0)\n"
    "v_mfma_f32_16x16x32_bf16 v[64:67], v[240:243], v[24:27], v[64:67]\n"
    "v_mfma_f32_16x16x32_bf16 v[48:51], v[244:247], v[28:31], v[48:51]\n"
    "v_mfma_f32_16x16x32_bf16 v[52:55], v[248:251], v[28:31], v[52:55]\n"
    "v_mfma_f32_16x16x32_bf16 v[64:67], v[252:255], v[28:31], v[64:67]\n"
    "v_cvt_pk_bf16_f32 v48, v48, v49\n"
    "v_cvt_pk_bf16_f32 v49, v50, v51\n"
    "s_nop 1\n"
    "v_cvt_pk_bf16_f32 v50, v52, v53\n"
    "v_cvt_pk_bf16_f32 v51, v54, v55\n"
    "buffer_store_dwordx4 v[48:51], v15, s[36:39], s73 offen sc1\n"
    "s_nop 0\n"
    "v_cvt_pk_bf16_f32 v64, v64, v65\n"
    "v_cvt_pk_bf16_f32 v65, v66, v67\n"
    "s_nop 0\n"
    "v_permlane16_swap_b32 v64, v66\n"
    "v_permlane16_swap_b32 v65, v67\n"
    "buffer_store_dwordx4 v[64:67], v4, s[36:39], s73 offen sc1\n"
    "v_mov_b32 v12, s68\n"
    "s_mov_b32 s73, 0x80000000\n"
    "s_mov_b32 s68, s71\n"
    "s_waitcnt vmcnt(4)\n"
    "v_add_u32 v13, v11, v3\n"
    "v_cmp_gt_i32 vcc, s71, v13\n"
    "s_cbranch_vccnz .LgWO_idle0_%=\n"
    "s_add_u32 m0, s59, 32768\n"
    "s_nop 0\n"
    "buffer_load_dwordx4 v1, s[36:39], s76 offen sc1 lds\n"
    "s_add_u32 m0, s59, 36864\n"
    "s_add_u32 s76, s76, 16384\n"
    "buffer_load_dwordx4 v1, s[36:39], s76 offen sc1 lds\n"
    "s_add_u32 m0, s59, 40960\n"
    "s_add_u32 s76, s76, 16384\n"
    "buffer_load_dwordx4 v1, s[36:39], s76 offen sc1 lds\n"
    "s_add_u32 m0, s59, 45056\n"
    "s_add_u32 s76, s76, 16384\n"
    "buffer_load_dwordx4 v1, s[36:39], s76 offen sc1 lds\n"
    "s_add_u32 m0, s59, 49152\n"
    "s_add_u32 s76, s76, 16384\n"
    "buffer_load_dwordx4 v1, s[36:39], s76 offen sc1 lds\n"
    "s_add_u32 m0, s59, 53248\n"
    "s_add_u32 s76, s76, 16384\n"
    "buffer_load_dwordx4 v1, s[36:39], s76 offen sc1 lds\n"
    "s_add_u32 m0, s59, 57344\n"
    "s_add_u32 s76, s76, 16384\n"
    "buffer_load_dwordx4 v1, s[36:39], s76 offen sc1 lds\n"
    "s_add_u32 m0, s59, 61440\n"
    "s_add_u32 s76, s76, 16384\n"
    "buffer_load_dwordx4 v1, s[36:39], s76 offen sc1 lds\n"
    "s_waitcnt vmcnt(8)\n"
    "buffer_atomic_smax v12, v14, s[36:39], 0 offen sc1\n"
    "s_mov_b32 s69, 0x80000000\n"
    "s_mov_b32 s70, 0x80000000\n"
    "v_mov_b32 v7, v12\n"
    "s_waitcnt vmcnt(11)\n"
    "s_add_u32 s71, s68, 1\n"
    "v_add_u32 v13, v11, v3\n"
    "v_cmp_gt_i32 vcc, s71, v13\n"
    "s_nop 0\n"
    "s_or_b64 exec, vcc, s[62:63]\n"
    "buffer_atomic_smax v11, v2, s[36:39], 0 offen sc0 sc1\n"
    "s_mov_b64 exec, -1\n"
    "s_waitcnt vmcnt(2)\n"
    "s_branch .LgWO_bar1_%=\n"
    ".LgWO_dma0_33_%=:\n"
    "v_mfma_f32_16x16x32_bf16 v[48:51], a[132:135], v[28:31], v[48:51]\n"
    "s_add_u32 m0, s59, 32768\n"
    "v_mfma_f32_16x16x32_bf16 v[52:55], a[136:139], v[28:31], v[52:55]\n"
    "s_waitcnt lgkmcnt(2)\n"
    "buffer_load_dwordx4 v1, s[36:39], s76 offen sc1 lds\n"
    "v_mfma_f32_16x16x32_bf16 v[64:67], a[140:143], v[28:31], v[64:67]\n"
    "ds_read_b128 v[28:31], v0 offset:15360\n"
    "s_add_u32 s76, s76, 16384\n"
    "v_mfma_f32_16x16x32_bf16 v[48:51], a[144:147], v[16:19], v[48:51]\n"
    "s_add_u32 m0, s59, 36864\n"
    "v_mfma_f32_16x16x32_bf16 v[52:55], a[148:151], v[16:19], v[52:55]\n"
    "s_waitcnt lgkmcnt(2)\n"
    "buffer_load_dwordx4 v1, s[36:39], s76 offen sc1 lds\n"
    "v_mfma_f32_16x16x32_bf16 v[64:67], a[152:155], v[16:19], v[64:67]\n"
    "ds_read_b128 v[16:19], v0 offset:16384\n"
    "v_mfma_f32_16x16x32_bf16 v[48:51], a[156:159], v[20:23], v[48:51]\n"
    "s_add_u32 s76, s76, 16384\n"
    "s_add_u32 m0, s59, 40960\n"
    "v_mfma_f32_16x16x32_bf16 v[52:55], a[160:163], v[20:23], v[52:55]\n"
    "s_waitcnt lgkmcnt(2)\n"
    "buffer_load_dwordx4 v1, s[36:39], s76 offen sc1 lds\n"
    "v_mfma_f32_16x16x32_bf16 v[64:67], a[164:167], v[20:23], v[64:67]\n"
    "ds_read_b128 v[20:23], v0 offset:17408\n"
    "v_mfma_f32_16x16x32_bf16 v[48:51], a[168:171], v[24:27], v[48:51]\n"
    "v_mfma_f32_16x16x32_bf16 v[52:55], a[172:175], v[24:27], v[52:55]\n"
    "s_waitcnt lgkmcnt(2)\n"
    "s_add_u32 s76, s76, 16384\n"
    "s_add_u32 m0, s59, 45056\n"
    "v_mfma_f32_16x16x32_bf16 v[64:67], a[176:179], v[24:27], v[64:67]\n"
    "ds_read_b128 v[24:27], v0 offset:18432\n"
    "buffer_load_dwordx4 v1, s[36:39], s76 offen sc1 lds\n"
    "v_mfma_f32_16x16x32_bf16 v[48:51], a[180:183], v[28:31], v[48:51]\n"
    "v_mfma_f32_16x16x32_bf16 v[52:55], a[184:187], v[28:31], v[52:55]\n"
    "s_waitcnt lgkmcnt(2)\n"
    "v_mfma_f32_16x16x32_bf16 v[64:67], a[188:191], v[28:31], v[64:67]\n"
    "ds_read_b128 v[28:31], v0 offset:19456\n"
    "s_add_u32 s76, s76, 16384\n"
    "v_mfma_f32_16x16x32_bf16 v[48:51], a[192:195], v[16:19], v[48:51]\n"
    "s_add_u32 m0, s59, 49152\n"
    "v_mfma_f32_16x16x32_bf16 v[52:55], a[196:199], v[16:19], v[52:55]\n"
    "s_waitcnt lgkmcnt(2)\n"
    "buffer_load_dwordx4 v1, s[36:39], s76 offen sc1 lds\n"
    "v_mfma_f32_16x16x32_bf16 v[64:67], a[200:203], v[16:19], v[64:67]\n"
    "ds_read_b128 v[16:19], v0 offset:20480\n"
    "v_mfma_f32_16x16x32_bf16 v[48:51], a[204:207], v[20:23], v[48:51]\n"
    "s_add_u32 s76, s76, 16384\n"
    "s_add_u32 m0, s59, 53248\n"
    "v_mfma_f32_16x16x32_bf16 v[52:55], a[208:211], v[20:23], v[52:55]\n"
    "s_waitcnt lgkmcnt(2)\n"
    "buffer_load_dwordx4 v1, s[36:39], s76 offen sc1 lds\n"
    "v_mfma_f32_16x16x32_bf16 v[64:67], a[212:215], v[20:23], v[64:67]\n"
    "ds_read_b128 v[20:23], v0 offset:21504\n"
    "v_mfma_f32_16x16x32_bf16 v[48:51], a[216:219], v[24:27], v[48:51]\n"
    "v_mfma_f32_16x16x32_bf16 v[52:55], a[220:223], v[24:27], v[52:55]\n"
    "s_waitcnt lgkmcnt(2)\n"
    "s_waitcnt vmcnt(6)\n"
    "buffer_atomic_smax v12, v10, s[36:39], s70 offen sc1\n"
    "v_mfma_f32_16x16x32_bf16 v[64:67], a[224:227], v[24:27], v[64:67]\n"
    "ds_read_b128 v[24:27], v0 offset:22528\n"
    "s_add_u32 s76, s76, 16384\n"
    "v_mfma_f32_16x16x32_bf16 v[48:51], a[228:231], v[28:31], v[48:51]\n"
    "s_add_u32 m0, s59, 57344\n"
    "v_mfma_f32_16x16x32_bf16 v[52:55], a[232:235], v[28:31], v[52:55]\n"
    "s_waitcnt lgkmcnt(2)\n"
    "buffer_load_dwordx4 v1, s[36:39], s76 offen sc1 lds\n"
    "v_mfma_f32_16x16x32_bf16 v[64:67], a[236:239], v[28:31], v[64:67]\n"
    "ds_read_b128 v[28:31], v0 offset:23552\n"
    "s_add_u32 s76, s76, 16384\n"
    "v_mfma_f32_16x16x32_bf16 v[48:51], a[240:243], v[16:19], v[48:51]\n"
    "s_add_u32 m0, s59, 61440\n"
    "v_mfma_f32_16x16x32_bf16 v[52:55], a[244:247], v[16:19], v[52:55]\n"
    "s_waitcnt lgkmcnt(2)\n"
    "buffer_load_dwordx4 v1, s[36:39], s76 offen sc1 lds\n"
    "v_mfma_f32_16x16x32_bf16 v[64:67], a[248:251], v[16:19], v[64:67]\n"
    "ds_read_b128 v[16:19], v0 offset:24576\n"
    "v_mfma_f32_16x16x32_bf16 v[48:51], a[252:255], v[20:23], v[48:51]\n"
    "v_mfma_f32_16x16x32_bf16 v[52:55], v[128:131], v[20:23], v[52:55]\n"
    "s_waitcnt lgkmcnt(2)\n"
    "v_mfma_f32_16x16x32_bf16 v[64:67], v[132:135], v[20:23], v[64:67]\n"
    "ds_read_b128 v[20:23], v0 offset:25600\n"
    "v_mfma_f32_16x16x32_bf16 v[48:51], v[136:139], v[24:27], v[48:51]\n"
    "v_mfma_f32_16x16x32_bf16 v[52:55], v[140:143], v[24:27], v[52:55]\n"
    "s_waitcnt lgkmcnt(2)\n"
    "v_mfma_f32_16x16x32_bf16 v[64:67], v[144:147], v[24:27], v[64:67]\n"
    "ds_read_b128 v[24:27], v0 offset:26624\n"
    "v_mfma_f32_16x16x32_bf16 v[48:51], v[148:151], v[28:31], v[48:51]\n"
    "v_mfma_f32_16x16x32_bf16 v[52:55], v[152:155], v[28:31], v[52:55]\n"
    "s_waitcnt lgkmcnt(2)\n"
    "v_mfma_f32_16x16x32_bf16 v[64:67], v[156:159], v[28:31], v[64:67]\n"
    "ds_read_b128 v[28:31], v0 offset:27648\n"
    "v_mfma_f32_16x16x32_bf16 v[48:51], v[160:163], v[16:19], v[48:51]\n"
    "v_mfma_f32_16x16x32_bf16 v[52:55], v[164:167], v[16:19], v[52:55]\n"
    "s_waitcnt lgkmcnt(2)\n"
    "v_mfma_f32_16x16x32_bf16 v[64:67], v[168:171], v[16:19], v[64:67]\n"
    "ds_read_b128 v[16:19], v0 offset:28672\n"
    "v_mfma_f32_16x16x32_bf16 v[48:51], v[172:175], v[20:23], v[48:51]\n"
    "v_mfma_f32_16x16x32_bf16 v[52:55], v[176:179], v[20:23], v[52:55]\n"
    "s_waitcnt lgkmcnt(2)\n"
    "v_mfma_f32_16x16x32_bf16 v[64:67], v[180:183], v[20:23], v[64:67]\n"
    "ds_read_b128 v[20:23], v0 offset:29696\n"
    "v_mfma_f32_16x16x32_bf16 v[48:51], v[184:187], v[24:27], v[48:51]\n"
    "v_mfma_f32_16x16x32_bf16 v[52:55], v[188:191], v[24:27], v[52:55]\n"
    "s_waitcnt lgkmcnt(2)\n"
    "v_mfma_f32_16x16x32_bf16 v[64:67], v[192:195], v[24:27], v[64:67]\n"
    "ds_read_b128 v[24:27], v0 offset:30720\n"
    "v_mfma_f32_16x16x32_bf16 v[48:51], v[196:199], v[28:31], v[48:51]\n"
    "v_mfma_f32_16x16x32_bf16 v[52:55], v[200:203], v[28:31], v[52:55]\n"
    "s_waitcnt lgkmcnt(2)\n"
    "v_mfma_f32_16x16x32_bf16 v[64:67], v[204:207], v[28:31], v[64:67]\n"
    "ds_read_b128 v[28:31], v0 offset:31744\n"
    "v_mfma_f32_16x16x32_bf16 v[48:51], v[208:211], v[16:19], v[48:51]\n"
    "v_mfma_f32_16x16x32_bf16 v[52:55], v[212:215], v[16:19], v[52:55]\n"
    "s_waitcnt lgkmcnt(2)\n"
    "v_mfma_f32_16x16x32_bf16 v[64:67], v[216:219], v[16:19], v[64:67]\n"
    "v_mfma_f32_16x16x32_bf16 v[48:51], v[220:223], v[20:23], v[48:51]\n"
    "v_mfma_f32_16x16x32_bf16 v[52:55], v[224:227], v[20:23], v[52:55]\n"
    "s_waitcnt lgkmcnt(1)\n"
    "v_mfma_f32_16x16x32_bf16 v[64:67], v[228:231], v[20:23], v[64:67]\n"
    "v_mfma_f32_16x16x32_bf16 v[48:51], v[232:235], v[24:27], v[48:51]\n"
    "v_mfma_f32_16x16x32_bf16 v[52:55], v[236:239], v[24:27], v[52:55]\n"
    "s_waitcnt lgkmcnt(0)\n"
    "v_mfma_f32_16x16x32_bf16 v[64:67], v[240:243], v[24:27], v[64:67]\n"
    "v_mfma_f32_16x16x32_bf16 v[48:51], v[244:247], v[28:31], v[48:51]\n"
    "v_mfma_f32_16x16x32_bf16 v[52:55], v[248:251], v[28:31], v[52:55]\n"
    "v_mfma_f32_16x16x32_bf16 v[64:67], v[252:255], v[28:31], v[64:67]\n"
    "v_cvt_pk_bf16_f32 v48, v48, v49\n"
    "v_cvt_pk_bf16_f32 v49, v50, v51\n"
    "s_nop 1\n"
    "v_cvt_pk_bf16_f32 v50, v52, v53\n"
    "v_cvt_pk_bf16_f32 v51, v54, v55\n"
    "buffer_store_dwordx4 v[48:51], v15, s[36:39], s73 offen sc1\n"
    "s_nop 0\n"
    "v_cvt_pk_bf16_f32 v64, v64, v65\n"
    "v_cvt_pk_bf16_f32 v65, v66, v67\n"
    "s_nop 0\n"
    "v_permlane16_swap_b32 v64, v66\n"
    "v_permlane16_swap_b32 v65, v67\n"
    "buffer_store_dwordx4 v[64:67], v4, s[36:39], s73 offen sc1\n"
    "v_mov_b32 v12, s68\n"
    "s_mov_b32 s73, 0x80000000\n"
    "s_mov_b32 s68, s71\n"
    "s_mov_b32 s69, 0\n"
    "s_mov_b32 s70, 0x80000000\n"
    "v_mov_b32 v7, v12\n"
    "s_waitcnt vmcnt(12)\n"
    "s_add_u32 s71, s68, 1\n"
    "v_add_u32 v13, v11, v3\n"
    "v_cmp_gt_i32 vcc, s71, v13\n"
    "s_nop 0\n"
    "s_or_b64 exec, vcc, s[62:63]\n"
    "buffer_atomic_smax v11, v2, s[36:39], 0 offen sc0 sc1\n"
    "s_mov_b64 exec, -1\n"
    "s_waitcnt vmcnt(3)\n"
    "s_branch .LgWO_bar1_%=\n"
    ".LgWO_dma0_54_%=:\n"
    "v_mfma_f32_16x16x32_bf16 v[48:51], a[216:219], v[24:27], v[48:51]\n"
    "s_add_u32 m0, s59, 32768\n"
    "v_mfma_f32_16x16x32_bf16 v[52:55], a[220:223], v[24:27], v[52:55]\n"
    "s_waitcnt lgkmcnt(2)\n"
    "s_waitcnt vmcnt(1)\n"
    "buffer_atomic_smax v12, v10, s[36:39], s70 offen sc1\n"
    "v_mfma_f32_16x16x32_bf16 v[64:67], a[224:227], v[24:27], v[64:67]\n"
    "ds_read_b128 v[24:27], v0 offset:22528\n"
    "buffer_load_dwordx4 v1, s[36:39], s76 offen sc1 lds\n"
    "v_mfma_f32_16x16x32_bf16 v[48:51], a[228:231], v[28:31], v[48:51]\n"
    "s_add_u32 s76, s76, 16384\n"
    "s_add_u32 m0, s59, 36864\n"
    "v_mfma_f32_16x16x32_bf16 v[52:55], a[232:235], v[28:31], v[52:55]\n"
    "s_waitcnt lgkmcnt(2)\n"
    "buffer_load_dwordx4 v1, s[36:39], s76 offen sc1 lds\n"
    "v_mfma_f32_16x16x32_bf16 v[64:67], a[236:239], v[28:31], v[64:67]\n"
    "ds_read_b128 v[28:31], v0 offset:23552\n"
    "v_mfma_f32_16x16x32_bf16 v[48:51], a[240:243], v[16:19], v[48:51]\n"
    "s_add_u32 s76, s76, 16384\n"
    "s_add_u32 m0, s59, 40960\n"
    "v_mfma_f32_16x16x32_bf16 v[52:55], a[244:247], v[16:19], v[52:55]\n"
    "s_waitcnt lgkmcnt(2)\n"
    "buffer_load_dwordx4 v1, s[36:39], s76 offen sc1 lds\n"
    "v_mfma_f32_16x16x32_bf16 v[64:67], a[248:251], v[16:19], v[64:67]\n"
    "ds_read_b128 v[16:19], v0 offset:24576\n"
    "v_mfma_f32_16x16x32_bf16 v[48:51], a[252:255], v[20:23], v[48:51]\n"
    "v_mfma_f32_16x16x32_bf16 v[52:55], v[128:131], v[20:23], v[52:55]\n"
    "s_waitcnt lgkmcnt(2)\n"
    "s_add_u32 s76, s76, 16384\n"
    "s_add_u32 m0, s59, 45056\n"
    "v_mfma_f32_16x16x32_bf16 v[64:67], v[132:135], v[20:23], v[64:67]\n"
    "ds_read_b128 v[20:23], v0 offset:25600\n"
    "buffer_load_dwordx4 v1, s[36:39], s76 offen sc1 lds\n"
    "v_mfma_f32_16x16x32_bf16 v[48:51], v[136:139], v[24:27], v[48:51]\n"
    "v_mfma_f32_16x16x32_bf16 v[52:55], v[140:143], v[24:27], v[52:55]\n"
    "s_waitcnt lgkmcnt(2)\n"
    "v_mfma_f32_16x16x32_bf16 v[64:67], v[144:147], v[24:27], v[64:67]\n"
    "ds_read_b128 v[24:27], v0 offset:26624\n"
    "s_add_u32 s76, s76, 16384\n"
    "v_mfma_f32_16x16x32_bf16 v[48:51], v[148:151], v[28:31], v[48:51]\n"
    "s_add_u32 m0, s59, 49152\n"
    "v_mfma_f32_16x16x32_bf16 v[52:55], v[152:155], v[28:31], v[52:55]\n"
    "s_waitcnt lgkmcnt(2)\n"
    "buffer_load_dwordx4 v1, s[36:39], s76 offen sc1 lds\n"
    "v_mfma_f32_16x16x32_bf16 v[64:67], v[156:159], v[28:31], v[64:67]\n"
    "ds_read_b128 v[28:31], v0 offset:27648\n"
    "v_mfma_f32_16x16x32_bf16 v[48:51], v[160:163], v[16:19], v[48:51]\n"
    "s_add_u32 s76, s76, 16384\n"
    "s_add_u32 m0, s59, 53248\n"
    "v_mfma_f32_16x16x32_bf16 v[52:55], v[164:167], v[16:19], v[52:55]\n"
    "s_waitcnt lgkmcnt(2)\n"
    "buffer_load_dwordx4 v1, s[36:39], s76 offen sc1 lds\n"
    "v_mfma_f32_16x16x32_bf16 v[64:67], v[168:171], v[16:19], v[64:67]\n"
    "ds_read_b128 v[16:19], v0 offset:28672\n"
    "v_mfma_f32_16x16x32_bf16 v[48:51], v[172:175], v[20:23], v[48:51]\n"
    "v_mfma_f32_16x16x32_bf16 v[52:55], v[176:179], v[20:23], v[52:55]\n"
    "s_waitcnt lgkmcnt(2)\n"
    "s_add_u32 s76, s76, 16384\n"
    "s_add_u32 m0, s59, 57344\n"
    "v_mfma_f32_16x16x32_bf16 v[64:67], v[180:183], v[20:23], v[64:67]\n"
    "ds_read_b128 v[20:23], v0 offset:29696\n"
    "buffer_load_dwordx4 v1, s[36:39], s76 offen sc1 lds\n"
    "v_mfma_f32_16x16x32_bf16 v[48:51], v[184:187], v[24:27], v[48:51]\n"
    "v_mfma_f32_16x16x32_bf16 v[52:55], v[188:191], v[24:27], v[52:55]\n"
    "s_waitcnt lgkmcnt(2)\n"
    "v_mfma_f32_16x16x32_bf16 v[64:67], v[192:195], v[24:27], v[64:67]\n"
    "ds_read_b128 v[24:27], v0 offset:30720\n"
    "s_add_u32 s76, s76, 16384\n"
    "v_mfma_f32_16x16x32_bf16 v[48:51], v[196:199], v[28:31], v[48:51]\n"
    "s_add_u32 m0, s59, 61440\n"
    "v_mfma_f32_16x16x32_bf16 v[52:55], v[200:203], v[28:31], v[52:55]\n"
    "s_waitcnt lgkmcnt(2)\n"
    "buffer_load_dwordx4 v1, s[36:39], s76 offen sc1 lds\n"
    "v_mfma_f32_16x16x32_bf16 v[64:67], v[204:207], v[28:31], v[64:67]\n"
    "ds_read_b128 v[28:31], v0 offset:31744\n"
    "v_mfma_f32_16x16x32_bf16 v[48:51], v[208:211], v[16:19], v[48:51]\n"
    "v_mfma_f32_16x16x32_bf16 v[52:55], v[212:215], v[16:19], v[52:55]\n"
    "s_waitcnt lgkmcnt(2)\n"
    "v_mfma_f32_16x16x32_bf16 v[64:67], v[216:219], v[16:19], v[64:67]\n"
    "v_mfma_f32_16x16x32_bf16 v[48:51], v[220:223], v[20:23], v[48:51]\n"
    "v_mfma_f32_16x16x32_bf16 v[52:55], v[224:227], v[20:23], v[52:55]\n"
    "s_waitcnt lgkmcnt(1)\n"
    "v_mfma_f32_16x16x32_bf16 v[64:67], v[228:231], v[20:23], v[64:67]\n"
    "v_mfma_f32_16x16x32_bf16 v[48:51], v[232:235], v[24:27], v[48:51]\n"
    "v_mfma_f32_16x16x32_bf16 v[52:55], v[236:239], v[24:27], v[52:55]\n"
    "s_waitcnt lgkmcnt(0)\n"
    "v_mfma_f32_16x16x32_bf16 v[64:67], v[240:243], v[24:27], v[64:67]\n"
    "v_mfma_f32_16x16x32_bf16 v[48:51], v[244:247], v[28:31], v[48:51]\n"
    "v_mfma_f32_16x16x32_bf16 v[52:55], v[248:251], v[28:31], v[52:55]\n"
    "v_mfma_f32_16x16x32_bf16 v[64:67], v[252:255], v[28:31], v[64:67]\n"
    "v_cvt_pk_bf16_f32 v48, v48, v49\n"
    "v_cvt_pk_bf16_f32 v49, v50, v51\n"
    "s_nop 1\n"
    "v_cvt_pk_bf16_f32 v50, v52, v53\n"
    "v_cvt_pk_bf16_f32 v51, v54, v55\n"
    "buffer_store_dwordx4 v[48:51], v15, s[36:39], s73 offen sc1\n"
    "s_nop 0\n"
    "v_cvt_pk_bf16_f32 v64, v64, v65\n"
    "v_cvt_pk_bf16_f32 v65, v66, v67\n"
    "s_nop 0\n"
    "v_permlane16_swap_b32 v64, v66\n"
    "v_permlane16_swap_b32 v65, v67\n"
    "buffer_store_dwordx4 v[64:67], v4, s[36:39], s73 offen sc1\n"
    "v_mov_b32 v12, s68\n"
    "s_mov_b32 s73, 0x80000000\n"
    "s_mov_b32 s68, s71\n"
    "s_mov_b32 s69, 0\n"
    "s_mov_b32 s70, 0x80000000\n"
    "v_mov_b32 v7, v12\n"
    "s_waitcnt vmcnt(11)\n"
    "s_add_u32 s71, s68, 1\n"
    "v_add_u32 v13, v11, v3\n"
    "v_cmp_gt_i32 vcc, s71, v13\n"
    "s_nop 0\n"
    "s_or_b64 exec, vcc, s[62:63]\n"
    "buffer_atomic_smax v11, v2, s[36:39], 0 offen sc0 sc1\n"
    "s_mov_b64 exec, -1\n"
    "s_waitcnt vmcnt(3)\n"
    "s_branch .LgWO_bar1_%=\n"
    ".LgWO_dma0_75_%=:\n"
    "v_mfma_f32_16x16x32_bf16 v[48:51], v[172:175], v[20:23], v[48:51]\n"
    "s_add_u32 m0, s59, 32768\n"
    "v_mfma_f32_16x16x32_bf16 v[52:55], v[176:179], v[20:23], v[52:55]\n"
    "s_waitcnt lgkmcnt(2)\n"
    "buffer_load_dwordx4 v1, s[36:39], s76 offen sc1 lds\n"
    "v_mfma_f32_16x16x32_bf16 v[64:67], v[180:183], v[20:23], v[64:67]\n"
    "ds_read_b128 v[20:23], v0 offset:29696\n"
    "s_add_u32 s76, s76, 16384\n"
    "v_mfma_f32_16x16x32_bf16 v[48:51], v[184:187], v[24:27], v[48:51]\n"
    "s_add_u32 m0, s59, 36864\n"
    "v_mfma_f32_16x16x32_bf16 v[52:55], v[188:191], v[24:27], v[52:55]\n"
    "s_waitcnt lgkmcnt(2)\n"
    "buffer_load_dwordx4 v1, s[36:39], s76 offen sc1 lds\n"
    "v_mfma_f32_16x16x32_bf16 v[64:67], v[192:195], v[24:27], v[64:67]\n"
    "ds_read_b128 v[24:27], v0 offset:30720\n"
    "v_mfma_f32_16x16x32_bf16 v[48:51], v[196:199], v[28:31], v[48:51]\n"
    "s_add_u32 s76, s76, 16384\n"
    "s_add_u32 m0, s59, 40960\n"
    "v_mfma_f32_16x16x32_bf16 v[52:55], v[200:203], v[28:31], v[52:55]\n"
    "s_waitcnt lgkmcnt(2)\n"
    "buffer_load_dwordx4 v1, s[36:39], s76 offen sc1 lds\n"
    "v_mfma_f32_16x16x32_bf16 v[64:67], v[204:207], v[28:31], v[64:67]\n"
    "ds_read_b128 v[28:31], v0 offset:31744\n"
    "v_mfma_f32_16x16x32_bf16 v[48:51], v[208:211], v[16:19], v[48:51]\n"
    "v_mfma_f32_16x16x32_bf16 v[52:55], v[212:215], v[16:19], v[52:55]\n"
    "s_waitcnt lgkmcnt(2)\n"
    "s_add_u32 s76, s76, 16384\n"
    "s_add_u32 m0, s59, 45056\n"
    "v_mfma_f32_16x16x32_bf16 v[64:67], v[216:219], v[16:19], v[64:67]\n"
    "buffer_load_dwordx4 v1, s[36:39], s76 offen sc1 lds\n"
    "v_mfma_f32_16x16x32_bf16 v[48:51], v[220:223], v[20:23], v[48:51]\n"
    "v_mfma_f32_16x16x32_bf16 v[52:55], v[224:227], v[20:23], v[52:55]\n"
    "s_waitcnt lgkmcnt(1)\n"
    "v_mfma_f32_16x16x32_bf16 v[64:67], v[228:231], v[20:23], v[64:67]\n"
    "s_add_u32 s76, s76, 16384\n"
    "v_mfma_f32_16x16x32_bf16 v[48:51], v[232:235], v[24:27], v[48:51]\n"
    "s_add_u32 m0, s59, 49152\n"
    "v_mfma_f32_16x16x32_bf16 v[52:55], v[236:239], v[24:27], v[52:55]\n"
    "s_waitcnt lgkmcnt(0)\n"
    "buffer_load_dwordx4 v1, s[36:39], s76 offen sc1 lds\n"
    "v_mfma_f32_16x16x32_bf16 v[64:67], v[240:243], v[24:27], v[64:67]\n"
    "v_mfma_f32_16x16x32_bf16 v[48:51], v[244:247], v[28:31], v[48:51]\n"
    "s_add_u32 s76, s76, 16384\n"
    "s_add_u32 m0, s59, 53248\n"
    "v_mfma_f32_16x16x32_bf16 v[52:55], v[248:251], v[28:31], v[52:55]\n"
    "buffer_load_dwordx4 v1, s[36:39], s76 offen sc1 lds\n"
    "v_mfma_f32_16x16x32_bf16 v[64:67], v[252:255], v[28:31], v[64:67]\n"
    "v_cvt_pk_bf16_f32 v48, v48, v49\n"
    "v_cvt_pk_bf16_f32 v49, v50, v51\n"
    "s_nop 1\n"
    "v_cvt_pk_bf16_f32 v50, v52, v53\n"
    "v_cvt_pk_bf16_f32 v51, v54, v55\n"
    "buffer_store_dwordx4 v[48:51], v15, s[36:39], s73 offen sc1\n"
    "s_nop 0\n"
    "v_cvt_pk_bf16_f32 v64, v64, v65\n"
    "v_cvt_pk_bf16_f32 v65, v66, v67\n"
    "s_nop 0\n"
    "v_permlane16_swap_b32 v64, v66\n"
    "v_permlane16_swap_b32 v65, v67\n"
    "buffer_store_dwordx4 v[64:67], v4, s[36:39], s73 offen sc1\n"
    "v_mov_b32 v12, s68\n"
    "s_mov_b32 s73, 0x80000000\n"
    "s_mov_b32 s68, s71\n"
    "s_add_u32 m0, s59, 57344\n"
    "s_add_u32 s76, s76, 16384\n"
    "buffer_load_dwordx4 v1, s[36:39], s76 offen sc1 lds\n"
    "s_add_u32 m0, s59, 61440\n"
    "s_add_u32 s76, s76, 16384\n"
    "buffer_load_dwordx4 v1, s[36:39], s76 offen sc1 lds\n"
    "s_waitcnt vmcnt(2)\n"
    "buffer_atomic_smax v12, v14, s[36:39], 0 offen sc1\n"
    "s_mov_b32 s69, 0x80000000\n"
    "s_mov_b32 s70, 0x80000000\n"
    "v_mov_b32 v7, v12\n"
    "s_waitcnt vmcnt(12)\n"
    "s_add_u32 s71, s68, 1\n"
    "v_add_u32 v13, v11, v3\n"
    "v_cmp_gt_i32 vcc, s71, v13\n"
    "s_nop 0\n"
    "s_or_b64 exec, vcc, s[62:63]\n"
    "buffer_atomic_smax v11, v2, s[36:39], 0 offen sc0 sc1\n"
    "s_mov_b64 exec, -1\n"
    "s_waitcnt vmcnt(2)\n"
    "s_branch .LgWO_bar1_%=\n"
    ".LgWO_idle0_%=:\n"
    "s_or_b64 exec, vcc, s[62:63]\n"
    "buffer_atomic_smax v11, v2, s[36:39], 0 offen sc0 sc1\n"
    "s_mov_b64 exec, -1\n"
    "s_waitcnt vmcnt(1)\n"
    "buffer_atomic_smax v12, v14, s[36:39], 0 offen sc1\n"
    "s_branch .LgWO_block0_0_%=\n"
    ".LgWO_block0_%=:\n"
    "v_add_u32 v13, v11, v3\n"
    "v_cmp_gt_i32 vcc, s71, v13\n"
    "s_nop 0\n"
    "s_or_b64 exec, vcc, s[62:63]\n"
    "buffer_atomic_smax v11, v2, s[36:39], 0 offen sc0 sc1\n"
    "s_mov_b64 exec, -1\n"
    "s_sleep 3\n"
    "s_waitcnt vmcnt(1)\n"
    ".LgWO_block0_0_%=:\n"
    "v_add_u32 v13, v5, v3\n"
    "v_cmp_gt_i32 vcc, s71, v13\n"
    "s_cbranch_vccz .LgWO_go0_%=\n"
    "v_add_u32 v13, v5, v3\n"
    "v_cmp_gt_i32 vcc, s71, v13\n"
    "s_nop 0\n"
    "s_or_b64 exec, vcc, s[62:63]\n"
    "buffer_atomic_smax v5, v2, s[36:39], 0 offen sc0 sc1\n"
    "s_mov_b64 exec, -1\n"
    "s_sleep 3\n"
    "s_waitcnt vmcnt(1)\n"
    ".LgWO_block0_1_%=:\n"
    "v_add_u32 v13, v11, v3\n"
    "v_cmp_gt_i32 vcc, s71, v13\n"
    "s_cbranch_vccz .LgWO_go0_%=\n"
    "s_sub_u32 s72, s72, 1\n"
    "s_cbranch_scc0 .LgWO_block0_%=\n"
    "s_mov_b32 s72, 0\n"
    ".LgWO_go0_%=:\n"
    "s_lshr_b32 s76, s71, 1\n"
    "s_and_b32 s76, s76, s54\n"
    "s_mul_i32 s76, s76, s53\n"
    "s_add_u32 s76, s76, s52\n"
    "s_add_u32 s76, s76, 1024\n"
    "s_add_u32 m0, s59, 32768\n"
    "s_nop 0\n"
    "buffer_load_dwordx4 v1, s[36:39], s76 offen sc1 lds\n"
    "s_add_u32 m0, s59, 36864\n"
    "s_add_u32 s76, s76, 16384\n"
    "buffer_load_dwordx4 v1, s[36:39], s76 offen sc1 lds\n"
    "s_add_u32 m0, s59, 40960\n"
    "s_add_u32 s76, s76, 16384\n"
    "buffer_load_dwordx4 v1, s[36:39], s76 offen sc1 lds\n"
    "s_add_u32 m0, s59, 45056\n"
    "s_add_u32 s76, s76, 16384\n"
    "buffer_load_dwordx4 v1, s[36:39], s76 offen sc1 lds\n"
    "s_add_u32 m0, s59, 49152\n"
    "s_add_u32 s76, s76, 16384\n"
    "buffer_load_dwordx4 v1, s[36:39], s76 offen sc1 lds\n"
    "s_add_u32 m0, s59, 53248\n"
    "s_add_u32 s76, s76, 16384\n"
    "buffer_load_dwordx4 v1, s[36:39], s76 offen sc1 lds\n"
    "s_add_u32 m0, s59, 57344\n"
    "s_add_u32 s76, s76, 16384\n"
    "buffer_load_dwordx4 v1, s[36:39], s76 offen sc1 lds\n"
    "s_add_u32 m0, s59, 61440\n"
    "s_add_u32 s76, s76, 16384\n"
    "buffer_load_dwordx4 v1, s[36:39], s76 offen sc1 lds\n"
    "s_mov_b32 s69, 0x80000000\n"
    "s_mov_b32 s70, 0x80000000\n"
    "v_mov_b32 v7, v12\n"
    "s_waitcnt vmcnt(8)\n"
    "s_add_u32 s71, s68, 1\n"
    "v_add_u32 v13, v11, v3\n"
    "v_cmp_gt_i32 vcc, s71, v13\n"
    "s_nop 0\n"
    "s_or_b64 exec, vcc, s[62:63]\n"
    "buffer_atomic_smax v11, v2, s[36:39], 0 offen sc0 sc1\n"
    "s_mov_b64 exec, -1\n"
    "s_waitcnt vmcnt(1)\n"
    "s_branch .LgWO_bar1_%=\n"
    ".LgWO_bar0_%=:\n"
    "s_barrier\n"
    "ds_read_b128 v[16:19], v0 offset:0\n"
    "ds_read_b128 v[20:23], v0 offset:1024\n"
    "ds_read_b128 v[24:27], v0 offset:2048\n"
    "ds_read_b128 v[28:31], v0 offset:3072\n"
    "v_add_u32 v13, v5, v3\n"
    "v_cmp_gt_i32 vcc, s71, v13\n"
    "s_nop 0\n"
    "s_or_b64 exec, vcc, s[62:63]\n"
    "buffer_atomic_smax v5, v2, s[36:39], 0 offen sc0 sc1\n"
    "s_mov_b64 exec, -1\n"
    "s_lshr_b32 s76, s71, 1\n"
    "s_and_b32 s76, s76, s54\n"
    "s_mul_i32 s76, s76, s53\n"
    "s_add_u32 s76, s76, s52\n"
    "s_add_u32 s76, s76, 1024\n"
    "s_waitcnt lgkmcnt(3)\n"
    "s_branch .LgWO_head0_%=\n"
    ".LgWO_poll1_%=:\n"
    "v_mfma_f32_16x16x32_bf16 v[56:59], a[48:51], v[16:19], v[56:59]\n"
    "s_or_b64 exec, vcc, s[62:63]\n"
    "buffer_atomic_smax v11, v2, s[36:39], 0 offen sc0 sc1\n"
    "s_mov_b64 exec, -1\n"
    "v_mfma_f32_16x16x32_bf16 v[60:63], a[52:55], v[16:19], v[60:63]\n"
    "s_waitcnt lgkmcnt(2)\n"
    "v_mfma_f32_16x16x32_bf16 v[68:71], a[56:59], v[16:19], v[68:71]\n"
    "ds_read_b128 v[16:19], v9 offset:8192\n"
    "v_mfma_f32_16x16x32_bf16 v[56:59], a[60:63], v[20:23], v[56:59]\n"
    "buffer_store_dwordx4 v[64:67], v4, s[36:39], s73 offen sc1\n"
    "v_mfma_f32_16x16x32_bf16 v[60:63], a[64:67], v[20:23], v[60:63]\n"
    "s_waitcnt lgkmcnt(2)\n"
    "s_lshr_b32 s73, s68, 1\n"
    "v_mfma_f32_16x16x32_bf16 v[68:71], a[68:71], v[20:23], v[68:71]\n"
    "ds_read_b128 v[20:23], v9 offset:9216\n"
    "s_and_b32 s73, s73, s57\n"
    "v_mfma_f32_16x16x32_bf16 v[56:59], a[72:75], v[24:27], v[56:59]\n"
    "s_mul_i32 s73, s73, s56\n"
    "v_mfma_f32_16x16x32_bf16 v[60:63], a[76:79], v[24:27], v[60:63]\n"
    "s_waitcnt lgkmcnt(2)\n"
    "s_add_u32 s73, s73, s55\n"
    "v_mfma_f32_16x16x32_bf16 v[68:71], a[80:83], v[24:27], v[68:71]\n"
    "ds_read_b128 v[24:27], v9 offset:10240\n"
    "v_mfma_f32_16x16x32_bf16 v[56:59], a[84:87], v[28:31], v[56:59]\n"
    "v_mfma_f32_16x16x32_bf16 v[60:63], a[88:91], v[28:31], v[60:63]\n"
    "s_waitcnt lgkmcnt(2)\n"
    "v_mfma_f32_16x16x32_bf16 v[68:71], a[92:95], v[28:31], v[68:71]\n"
    "ds_read_b128 v[28:31], v9 offset:11264\n"
    "v_mfma_f32_16x16x32_bf16 v[56:59], a[96:99], v[16:19], v[56:59]\n"
    "v_mfma_f32_16x16x32_bf16 v[60:63], a[100:103], v[16:19], v[60:63]\n"
    "s_waitcnt lgkmcnt(2)\n"
    "v_mfma_f32_16x16x32_bf16 v[68:71], a[104:107], v[16:19], v[68:71]\n"
    "ds_read_b128 v[16:19], v9 offset:12288\n"
    "v_mfma_f32_16x16x32_bf16 v[56:59], a[108:111], v[20:23], v[56:59]\n"
    "v_mfma_f32_16x16x32_bf16 v[60:63], a[112:115], v[20:23], v[60:63]\n"
    "s_waitcnt lgkmcnt(2)\n"
    "s_waitcnt vmcnt(4)\n"
    "v_add_u32 v13, v5, v3\n"
    "v_mfma_f32_16x16x32_bf16 v[68:71], a[116:119], v[20:23], v[68:71]\n"
    "ds_read_b128 v[20:23], v9 offset:13312\n"
    "v_cmp_gt_i32 vcc, s71, v13\n"
    "v_mfma_f32_16x16x32_bf16 v[56:59], a[120:123], v[24:27], v[56:59]\n"
    "v_mfma_f32_16x16x32_bf16 v[60:63], a[124:127], v[24:27], v[60:63]\n"
    "s_waitcnt lgkmcnt(2)\n"
    "v_mfma_f32_16x16x32_bf16 v[68:71], a[128:131], v[24:27], v[68:71]\n"
    "ds_read_b128 v[24:27], v9 offset:14336\n"
    "s_cbranch_vccz .LgWO_dma1_33_%=\n"
    "v_mfma_f32_16x16x32_bf16 v[56:59], a[132:135], v[28:31], v[56:59]\n"
    "s_or_b64 exec, vcc, s[62:63]\n"
    "buffer_atomic_smax v5, v2, s[36:39], 0 offen sc0 sc1\n"
    "s_mov_b64 exec, -1\n"
    "v_mfma_f32_16x16x32_bf16 v[60:63], a[136:139], v[28:31], v[60:63]\n"
    "s_waitcnt lgkmcnt(2)\n"
    "v_mfma_f32_16x16x32_bf16 v[68:71], a[140:143], v[28:31], v[68:71]\n"
    "ds_read_b128 v[28:31], v9 offset:15360\n"
    "v_mfma_f32_16x16x32_bf16 v[56:59], a[144:147], v[16:19], v[56:59]\n"
    "v_mfma_f32_16x16x32_bf16 v[60:63], a[148:151], v[16:19], v[60:63]\n"
    "s_waitcnt lgkmcnt(2)\n"
    "v_mfma_f32_16x16x32_bf16 v[68:71], a[152:155], v[16:19], v[68:71]\n"
    "ds_read_b128 v[16:19], v9 offset:16384\n"
    "v_mfma_f32_16x16x32_bf16 v[56:59], a[156:159], v[20:23], v[56:59]\n"
    "v_mfma_f32_16x16x32_bf16 v[60:63], a[160:163], v[20:23], v[60:63]\n"
    "s_waitcnt lgkmcnt(2)\n"
    "v_mfma_f32_16x16x32_bf16 v[68:71], a[164:167], v[20:23], v[68:71]\n"
    "ds_read_b128 v[20:23], v9 offset:17408\n"
    "v_mfma_f32_16x16x32_bf16 v[56:59], a[168:171], v[24:27], v[56:59]\n"
    "v_mfma_f32_16x16x32_bf16 v[60:63], a[172:175], v[24:27], v[60:63]\n"
    "s_waitcnt lgkmcnt(2)\n"
    "v_mfma_f32_16x16x32_bf16 v[68:71], a[176:179], v[24:27], v[68:71]\n"
    "ds_read_b128 v[24:27], v9 offset:18432\n"
    "v_mfma_f32_16x16x32_bf16 v[56:59], a[180:183], v[28:31], v[56:59]\n"
    "v_mfma_f32_16x16x32_bf16 v[60:63], a[184:187], v[28:31], v[60:63]\n"
    "s_waitcnt lgkmcnt(2)\n"
    "v_mfma_f32_16x16x32_bf16 v[68:71], a[188:191], v[28:31], v[68:71]\n"
    "ds_read_b128 v[28:31], v9 offset:19456\n"
    "v_mfma_f32_16x16x32_bf16 v[56:59], a[192:195], v[16:19], v[56:59]\n"
    "v_mfma_f32_16x16x32_bf16 v[60:63], a[196:199], v[16:19], v[60:63]\n"
    "s_waitcnt lgkmcnt(2)\n"
    "s_waitcnt vmcnt(2)\n"
    "v_add_u32 v13, v11, v3\n"
    "v_mfma_f32_16x16x32_bf16 v[68:71], a[200:203], v[16:19], v[68:71]\n"
    "ds_read_b128 v[16:19], v9 offset:20480\n"
    "v_cmp_gt_i32 vcc, s71, v13\n"
    "v_mfma_f32_16x16x32_bf16 v[56:59], a[204:207], v[20:23], v[56:59]\n"
    "v_mfma_f32_16x16x32_bf16 v[60:63], a[208:211], v[20:23], v[60:63]\n"
    "s_waitcnt lgkmcnt(2)\n"
    "v_mfma_f32_16x16x32_bf16 v[68:71], a[212:215], v[20:23], v[68:71]\n"
    "ds_read_b128 v[20:23], v9 offset:21504\n"
    "s_cbranch_vccz .LgWO_dma1_54_%=\n"
    "v_mfma_f32_16x16x32_bf16 v[56:59], a[216:219], v[24:27], v[56:59]\n"
    "s_or_b64 exec, vcc, s[62:63]\n"
    "buffer_atomic_smax v11, v2, s[36:39], 0 offen sc0 sc1\n"
    "s_mov_b64 exec, -1\n"
    "v_mfma_f32_16x16x32_bf16 v[60:63], a[220:223], v[24:27], v[60:63]\n"
    "s_waitcnt lgkmcnt(2)\n"
    "s_waitcnt vmcnt(2)\n"
    "buffer_atomic_smax v12, v14, s[36:39], s70 offen sc1\n"
    "v_mfma_f32_16x16x32_bf16 v[68:71], a[224:227], v[24:27], v[68:71]\n"
    "ds_read_b128 v[24:27], v9 offset:22528\n"
    "v_mfma_f32_16x16x32_bf16 v[56:59], a[228:231], v[28:31], v[56:59]\n"
    "v_mfma_f32_16x16x32_bf16 v[60:63], a[232:235], v[28:31], v[60:63]\n"
    "s_waitcnt lgkmcnt(2)\n"
    "v_mfma_f32_16x16x32_bf16 v[68:71], a[236:239], v[28:31], v[68:71]\n"
    "ds_read_b128 v[28:31], v9 offset:23552\n"
    "v_mfma_f32_16x16x32_bf16 v[56:59], a[240:243], v[16:19], v[56:59]\n"
    "v_mfma_f32_16x16x32_bf16 v[60:63], a[244:247], v[16:19], v[60:63]\n"
    "s_waitcnt lgkmcnt(2)\n"
    "v_mfma_f32_16x16x32_bf16 v[68:71], a[248:251], v[16:19], v[68:71]\n"
    "ds_read_b128 v[16:19], v9 offset:24576\n"
    "v_mfma_f32_16x16x32_bf16 v[56:59], a[252:255], v[20:23], v[56:59]\n"
    "v_mfma_f32_16x16x32_bf16 v[60:63], v[128:131], v[20:23], v[60:63]\n"
    "s_waitcnt lgkmcnt(2)\n"
    "v_mfma_f32_16x16x32_bf16 v[68:71], v[132:135], v[20:23], v[68:71]\n"
    "ds_read_b128 v[20:23], v9 offset:25600\n"
    "v_mfma_f32_16x16x32_bf16 v[56:59], v[136:139], v[24:27], v[56:59]\n"
    "v_mfma_f32_16x16x32_bf16 v[60:63], v[140:143], v[24:27], v[60:63]\n"
    "s_waitcnt lgkmcnt(2)\n"
    "v_mfma_f32_16x16x32_bf16 v[68:71], v[144:147], v[24:27], v[68:71]\n"
    "ds_read_b128 v[24:27], v9 offset:26624\n"
    "v_mfma_f32_16x16x32_bf16 v[56:59], v[148:151], v[28:31], v[56:59]\n"
    "v_mfma_f32_16x16x32_bf16 v[60:63], v[152:155], v[28:31], v[60:63]\n"
    "s_waitcnt lgkmcnt(2)\n"
    "s_waitcnt vmcnt(2)\n"
    "v_add_u32 v13, v5, v3\n"
    "v_mfma_f32_16x16x32_bf16 v[68:71], v[156:159], v[28:31], v[68:71]\n"
    "ds_read_b128 v[28:31], v9 offset:27648\n"
    "v_cmp_gt_i32 vcc, s71, v13\n"
    "v_mfma_f32_16x16x32_bf16 v[56:59], v[160:163], v[16:19], v[56:59]\n"
    "v_mfma_f32_16x16x32_bf16 v[60:63], v[164:167], v[16:19], v[60:63]\n"
    "s_waitcnt lgkmcnt(2)\n"
    "v_mfma_f32_16x16x32_bf16 v[68:71], v[168:171], v[16:19], v[68:71]\n"
    "ds_read_b128 v[16:19], v9 offset:28672\n"
    "s_cbranch_vccz .LgWO_dma1_75_%=\n"
    "v_mfma_f32_16x16x32_bf16 v[56:59], v[172:175], v[20:23], v[56:59]\n"
    "s_or_b64 exec, vcc, s[62:63]\n"
    "buffer_atomic_smax v5, v2, s[36:39], 0 offen sc0 sc1\n"
    "s_mov_b64 exec, -1\n"
    "v_mfma_f32_16x16x32_bf16 v[60:63], v[176:179], v[20:23], v[60:63]\n"
    "s_waitcnt lgkmcnt(2)\n"
    "v_mfma_f32_16x16x32_bf16 v[68:71], v[180:183], v[20:23], v[68:71]\n"
    "ds_read_b128 v[20:23], v9 offset:29696\n"
    "v_mfma_f32_16x16x32_bf16 v[56:59], v[184:187], v[24:27], v[56:59]\n"
    "v_mfma_f32_16x16x32_bf16 v[60:63], v[188:191], v[24:27], v[60:63]\n"
    "s_waitcnt lgkmcnt(2)\n"
    "v_mfma_f32_16x16x32_bf16 v[68:71], v[192:195], v[24:27], v[68:71]\n"
    "ds_read_b128 v[24:27], v9 offset:30720\n"
    "v_mfma_f32_16x16x32_bf16 v[56:59], v[196:199], v[28:31], v[56:59]\n"
    "v_mfma_f32_16x16x32_bf16 v[60:63], v[200:203], v[28:31], v[60:63]\n"
    "s_waitcnt lgkmcnt(2)\n"
    "v_mfma_f32_16x16x32_bf16 v[68:71], v[204:207], v[28:31], v[68:71]\n"
    "ds_read_b128 v[28:31], v9 offset:31744\n"
    "v_mfma_f32_16x16x32_bf16 v[56:59], v[208:211], v[16:19], v[56:59]\n"
    "v_mfma_f32_16x16x32_bf16 v[60:63], v[212:215], v[16:19], v[60:63]\n"
    "s_waitcnt lgkmcnt(2)\n"
    "v_mfma_f32_16x16x32_bf16 v[68:71], v[216:219], v[16:19], v[68:71]\n"
    "v_mfma_f32_16x16x32_bf16 v[56:59], v[220:223], v[20:23], v[56:59]\n"
    "v_mfma_f32_16x16x32_bf16 v[60:63], v[224:227], v[20:23], v[60:63]\n"
    "s_waitcnt lgkmcnt(1)\n"
    "v_mfma_f32_16x16x32_bf16 v[68:71], v[228:231], v[20:23], v[68:71]\n"
    "v_mfma_f32_16x16x32_bf16 v[56:59], v[232:235], v[24:27], v[56:59]\n"
    "v_mfma_f32_16x16x32_bf16 v[60:63], v[236:239], v[24:27], v[60:63]\n"
    "s_waitcnt lgkmcnt(0)\n"
    "v_mfma_f32_16x16x32_bf16 v[68:71], v[240:243], v[24:27], v[68:71]\n"
    "v_mfma_f32_16x16x32_bf16 v[56:59], v[244:247], v[28:31], v[56:59]\n"
    "v_mfma_f32_16x16x32_bf16 v[60:63], v[248:251], v[28:31], v[60:63]\n"
    "v_mfma_f32_16x16x32_bf16 v[68:71], v[252:255], v[28:31], v[68:71]\n"
    "v_cvt_pk_bf16_f32 v56, v56, v57\n"
    "v_cvt_pk_bf16_f32 v57, v58, v59\n"
    "s_nop 1\n"
    "v_cvt_pk_bf16_f32 v58, v60, v61\n"
    "v_cvt_pk_bf16_f32 v59, v62, v63\n"
    "buffer_store_dwordx4 v[56:59], v15, s[36:39], s73 offen offset:1024 sc1\n"
    "s_nop 0\n"
    "v_cvt_pk_bf16_f32 v68, v68, v69\n"
    "v_cvt_pk_bf16_f32 v69, v70, v71\n"
    "s_nop 0\n"
    "v_permlane16_swap_b32 v68, v70\n"
    "v_permlane16_swap_b32 v69, v71\n"
    "buffer_store_dwordx4 v[68:71], v4, s[36:39], s73 offen offset:1024 sc1\n"
    "v_mov_b32 v12, s68\n"
    "s_mov_b32 s73, 0x80000000\n"
    "s_mov_b32 s68, s71\n"
    "s_cmp_eq_u32 s68, 128\n"
    "s_cbranch_scc1 .LgWO_done_%=\n"
    "s_waitcnt vmcnt(4)\n"
    "v_add_u32 v13, v11, v3\n"
    "v_cmp_gt_i32 vcc, s71, v13\n"
    "s_cbranch_vccnz .LgWO_idle1_%=\n"
    "s_add_u32 m0, s59, 0\n"
    "s_nop 0\n"
    "buffer_load_dwordx4 v1, s[36:39], s76 offen sc1 lds\n"
    "s_add_u32 m0, s59, 4096\n"
    "s_add_u32 s76, s76, 16384\n"
    "buffer_load_dwordx4 v1, s[36:39], s76 offen sc1 lds\n"
    "s_add_u32 m0, s59, 8192\n"
    "s_add_u32 s76, s76, 16384\n"
    "buffer_load_dwordx4 v1, s[36:39], s76 offen sc1 lds\n"
    "s_add_u32 m0, s59, 12288\n"
    "s_add_u32 s76, s76, 16384\n"
    "buffer_load_dwordx4 v1, s[36:39], s76 offen sc1 lds\n"
    "s_add_u32 m0, s59, 16384\n"
    "s_add_u32 s76, s76, 16384\n"
    "buffer_load_dwordx4 v1, s[36:39], s76 offen sc1 lds\n"
    "s_add_u32 m0, s59, 20480\n"
    "s_add_u32 s76, s76, 16384\n"
    "buffer_load_dwordx4 v1, s[36:39], s76 offen sc1 lds\n"
    "s_add_u32 m0, s59, 24576\n"
    "s_add_u32 s76, s76, 16384\n"
    "buffer_load_dwordx4 v1, s[36:39], s76 offen sc1 lds\n"
    "s_add_u32 m0, s59, 28672\n"
    "s_add_u32 s76, s76, 16384\n"
    "buffer_load_dwordx4 v1, s[36:39], s76 offen sc1 lds\n"
    "s_waitcnt vmcnt(8)\n"
    "buffer_atomic_smax v12, v10, s[36:39], 0 offen sc1\n"
    "s_mov_b32 s69, 0x80000000\n"
    "s_mov_b32 s70, 0x80000000\n"
    "v_mov_b32 v7, v12\n"
    "s_waitcnt vmcnt(11)\n"
    "s_add_u32 s71, s68, 1\n"
    "v_add_u32 v13, v11, v3\n"
    "v_cmp_gt_i32 vcc, s71, v13\n"
    "s_nop 0\n"
    "s_or_b64 exec, vcc, s[62:63]\n"
    "buffer_atomic_smax v11, v2, s[36:39], 0 offen sc0 sc1\n"
    "s_mov_b64 exec, -1\n"
    "s_waitcnt vmcnt(2)\n"
    "s_branch .LgWO_bar0_%=\n"
    ".LgWO_dma1_33_%=:\n"
    "v_mfma_f32_16x16x32_bf16 v[56:59], a[132:135], v[28:31], v[56:59]\n"
    "s_add_u32 m0, s59, 0\n"
    "v_mfma_f32_16x16x32_bf16 v[60:63], a[136:139], v[28:31], v[60:63]\n"
    "s_waitcnt lgkmcnt(2)\n"
    "buffer_load_dwordx4 v1, s[36:39], s76 offen sc1 lds\n"
    "v_mfma_f32_16x16x32_bf16 v[68:71], a[140:143], v[28:31], v[68:71]\n"
    "ds_read_b128 v[28:31], v9 offset:15360\n"
    "s_add_u32 s76, s76, 16384\n"
    "v_mfma_f32_16x16x32_bf16 v[56:59], a[144:147], v[16:19], v[56:59]\n"
    "s_add_u32 m0, s59, 4096\n"
    "v_mfma_f32_16x16x32_bf16 v[60:63], a[148:151], v[16:19], v[60:63]\n"
    "s_waitcnt lgkmcnt(2)\n"
    "buffer_load_dwordx4 v1, s[36:39], s76 offen sc1 lds\n"
    "v_mfma_f32_16x16x32_bf16 v[68:71], a[152:155], v[16:19], v[68:71]\n"
    "ds_read_b128 v[16:19], v9 offset:16384\n"
    "v_mfma_f32_16x16x32_bf16 v[56:59], a[156:159], v[20:23], v[56:59]\n"
    "s_add_u32 s76, s76, 16384\n"
    "s_add_u32 m0, s59, 8192\n"
    "v_mfma_f32_16x16x32_bf16 v[60:63], a[160:163], v[20:23], v[60:63]\n"
    "s_waitcnt lgkmcnt(2)\n"
    "buffer_load_dwordx4 v1, s[36:39], s76 offen sc1 lds\n"
    "v_mfma_f32_16x16x32_bf16 v[68:71], a[164:167], v[20:23], v[68:71]\n"
    "ds_read_b128 v[20:23], v9 offset:17408\n"
    "v_mfma_f32_16x16x32_bf16 v[56:59], a[168:171], v[24:27], v[56:59]\n"
    "v_mfma_f32_16x16x32_bf16 v[60:63], a[172:175], v[24:27], v[60:63]\n"
    "s_waitcnt lgkmcnt(2)\n"
    "s_add_u32 s76, s76, 16384\n"
    "s_add_u32 m0, s59, 12288\n"
    "v_mfma_f32_16x16x32_bf16 v[68:71], a[176:179], v[24:27], v[68:71]\n"
    "ds_read_b128 v[24:27], v9 offset:18432\n"
    "buffer_load_dwordx4 v1, s[36:39], s76 offen sc1 lds\n"
    "v_mfma_f32_16x16x32_bf16 v[56:59], a[180:183], v[28:31], v[56:59]\n"
    "v_mfma_f32_16x16x32_bf16 v[60:63], a[184:187], v[28:31], v[60:63]\n"
    "s_waitcnt lgkmcnt(2)\n"
    "v_mfma_f32_16x16x32_bf16 v[68:71], a[188:191], v[28:31], v[68:71]\n"
    "ds_read_b128 v[28:31], v9 offset:19456\n"
    "s_add_u32 s76, s76, 16384\n"
    "v_mfma_f32_16x16x32_bf16 v[56:59], a[192:195], v[16:19], v[56:59]\n"
    "s_add_u32 m0, s59, 16384\n"
    "v_mfma_f32_16x16x32_bf16 v[60:63], a[196:199], v[16:19], v[60:63]\n"
    "s_waitcnt lgkmcnt(2)\n"
    "buffer_load_dwordx4 v1, s[36:39], s76 offen sc1 lds\n"
    "v_mfma_f32_16x16x32_bf16 v[68:71], a[200:203], v[16:19], v[68:71]\n"
    "ds_read_b128 v[16:19], v9 offset:20480\n"
    "v_mfma_f32_16x16x32_bf16 v[56:59], a[204:207], v[20:23], v[56:59]\n"
    "s_add_u32 s76, s76, 16384\n"
    "s_add_u32 m0, s59, 20480\n"
    "v_mfma_f32_16x16x32_bf16 v[60:63], a[208:211], v[20:23], v[60:63]\n"
    "s_waitcnt lgkmcnt(2)\n"
    "buffer_load_dwordx4 v1, s[36:39], s76 offen sc1 lds\n"
    "v_mfma_f32_16x16x32_bf16 v[68:71], a[212:215], v[20:23], v[68:71]\n"
    "ds_read_b128 v[20:23], v9 offset:21504\n"
    "v_mfma_f32_16x16x32_bf16 v[56:59], a[216:219], v[24:27], v[56:59]\n"
    "v_mfma_f32_16x16x32_bf16 v[60:63], a[220:223], v[24:27], v[60:63]\n"
    "s_waitcnt lgkmcnt(2)\n"
    "s_waitcnt vmcnt(6)\n"
    "buffer_atomic_smax v12, v14, s[36:39], s70 offen sc1\n"
    "v_mfma_f32_16x16x32_bf16 v[68:71], a[224:227], v[24:27], v[68:71]\n"
    "ds_read_b128 v[24:27], v9 offset:22528\n"
    "s_add_u32 s76, s76, 16384\n"
    "v_mfma_f32_16x16x32_bf16 v[56:59], a[228:231], v[28:31], v[56:59]\n"
    "s_add_u32 m0, s59, 24576\n"
    "v_mfma_f32_16x16x32_bf16 v[60:63], a[232:235], v[28:31], v[60:63]\n"
    "s_waitcnt lgkmcnt(2)\n"
    "buffer_load_dwordx4 v1, s[36:39], s76 offen sc1 lds\n"
    "v_mfma_f32_16x16x32_bf16 v[68:71], a[236:239], v[28:31], v[68:71]\n"
    "ds_read_b128 v[28:31], v9 offset:23552\n"
    "s_add_u32 s76, s76, 16384\n"
    "v_mfma_f32_16x16x32_bf16 v[56:59], a[240:243], v[16:19], v[56:59]\n"
    "s_add_u32 m0, s59, 28672\n"
    "v_mfma_f32_16x16x32_bf16 v[60:63], a[244:247], v[16:19], v[60:63]\n"
    "s_waitcnt lgkmcnt(2)\n"
    "buffer_load_dwordx4 v1, s[36:39], s76 offen sc1 lds\n"
    "v_mfma_f32_16x16x32_bf16 v[68:71], a[248:251], v[16:19], v[68:71]\n"
    "ds_read_b128 v[16:19], v9 offset:24576\n"
    "v_mfma_f32_16x16x32_bf16 v[56:59], a[252:255], v[20:23], v[56:59]\n"
    "v_mfma_f32_16x16x32_bf16 v[60:63], v[128:131], v[20:23], v[60:63]\n"
    "s_waitcnt lgkmcnt(2)\n"
    "v_mfma_f32_16x16x32_bf16 v[68:71], v[132:135], v[20:23], v[68:71]\n"
    "ds_read_b128 v[20:23], v9 offset:25600\n"
    "v_mfma_f32_16x16x32_bf16 v[56:59], v[136:139], v[24:27], v[56:59]\n"
    "v_mfma_f32_16x16x32_bf16 v[60:63], v[140:143], v[24:27], v[60:63]\n"
    "s_waitcnt lgkmcnt(2)\n"
    "v_mfma_f32_16x16x32_bf16 v[68:71], v[144:147], v[24:27], v[68:71]\n"
    "ds_read_b128 v[24:27], v9 offset:26624\n"
    "v_mfma_f32_16x16x32_bf16 v[56:59], v[148:151], v[28:31], v[56:59]\n"
    "v_mfma_f32_16x16x32_bf16 v[60:63], v[152:155], v[28:31], v[60:63]\n"
    "s_waitcnt lgkmcnt(2)\n"
    "v_mfma_f32_16x16x32_bf16 v[68:71], v[156:159], v[28:31], v[68:71]\n"
    "ds_read_b128 v[28:31], v9 offset:27648\n"
    "v_mfma_f32_16x16x32_bf16 v[56:59], v[160:163], v[16:19], v[56:59]\n"
    "v_mfma_f32_16x16x32_bf16 v[60:63], v[164:167], v[16:19], v[60:63]\n"
    "s_waitcnt lgkmcnt(2)\n"
    "v_mfma_f32_16x16x32_bf16 v[68:71], v[168:171], v[16:19], v[68:71]\n"
    "ds_read_b128 v[16:19], v9 offset:28672\n"
    "v_mfma_f32_16x16x32_bf16 v[56:59], v[172:175], v[20:23], v[56:59]\n"
    "v_mfma_f32_16x16x32_bf16 v[60:63], v[176:179], v[20:23], v[60:63]\n"
    "s_waitcnt lgkmcnt(2)\n"
    "v_mfma_f32_16x16x32_bf16 v[68:71], v[180:183], v[20:23], v[68:71]\n"
    "ds_read_b128 v[20:23], v9 offset:29696\n"
    "v_mfma_f32_16x16x32_bf16 v[56:59], v[184:187], v[24:27], v[56:59]\n"
    "v_mfma_f32_16x16x32_bf16 v[60:63], v[188:191], v[24:27], v[60:63]\n"
    "s_waitcnt lgkmcnt(2)\n"
    "v_mfma_f32_16x16x32_bf16 v[68:71], v[192:195], v[24:27], v[68:71]\n"
    "ds_read_b128 v[24:27], v9 offset:30720\n"
    "v_mfma_f32_16x16x32_bf16 v[56:59], v[196:199], v[28:31], v[56:59]\n"
    "v_mfma_f32_16x16x32_bf16 v[60:63], v[200:203], v[28:31], v[60:63]\n"
    "s_waitcnt lgkmcnt(2)\n"
    "v_mfma_f32_16x16x32_bf16 v[68:71], v[204:207], v[28:31], v[68:71]\n"
    "ds_read_b128 v[28:31], v9 offset:31744\n"
    "v_mfma_f32_16x16x32_bf16 v[56:59], v[208:211], v[16:19], v[56:59]\n"
    "v_mfma_f32_16x16x32_bf16 v[60:63], v[212:215], v[16:19], v[60:63]\n"
    "s_waitcnt lgkmcnt(2)\n"
    "v_mfma_f32_16x16x32_bf16 v[68:71], v[216:219], v[16:19], v[68:71]\n"
    "v_mfma_f32_16x16x32_bf16 v[56:59], v[220:223], v[20:23], v[56:59]\n"
    "v_mfma_f32_16x16x32_bf16 v[60:63], v[224:227], v[20:23], v[60:63]\n"
    "s_waitcnt lgkmcnt(1)\n"
    "v_mfma_f32_16x16x32_bf16 v[68:71], v[228:231], v[20:23], v[68:71]\n"
    "v_mfma_f32_16x16x32_bf16 v[56:59], v[232:235], v[24:27], v[56:59]\n"
    "v_mfma_f32_16x16x32_bf16 v[60:63], v[236:239], v[24:27], v[60:63]\n"
    "s_waitcnt lgkmcnt(0)\n"
    "v_mfma_f32_16x16x32_bf16 v[68:71], v[240:243], v[24:27], v[68:71]\n"
    "v_mfma_f32_16x16x32_bf16 v[56:59], v[244:247], v[28:31], v[56:59]\n"
    "v_mfma_f32_16x16x32_bf16 v[60:63], v[248:251], v[28:31], v[60:63]\n"
    "v_mfma_f32_16x16x32_bf16 v[68:71], v[252:255], v[28:31], v[68:71]\n"
    "v_cvt_pk_bf16_f32 v56, v56, v57\n"
    "v_cvt_pk_bf16_f32 v57, v58, v59\n"
    "s_nop 1\n"
    "v_cvt_pk_bf16_f32 v58, v60, v61\n"
    "v_cvt_pk_bf16_f32 v59, v62, v63\n"
    "buffer_store_dwordx4 v[56:59], v15, s[36:39], s73 offen offset:1024 sc1\n"
    "s_nop 0\n"
    "v_cvt_pk_bf16_f32 v68, v68, v69\n"
    "v_cvt_pk_bf16_f32 v69, v70, v71\n"
    "s_nop 0\n"
    "v_permlane16_swap_b32 v68, v70\n"
    "v_permlane16_swap_b32 v69, v71\n"
    "buffer_store_dwordx4 v[68:71], v4, s[36:39], s73 offen offset:1024 sc1\n"
    "v_mov_b32 v12, s68\n"
    "s_mov_b32 s73, 0x80000000\n"
    "s_mov_b32 s68, s71\n"
    "s_mov_b32 s69, 0\n"
    "s_mov_b32 s70, 0x80000000\n"
    "v_mov_b32 v7, v12\n"
    "s_waitcnt vmcnt(12)\n"
    "s_add_u32 s71, s68, 1\n"
    "v_add_u32 v13, v11, v3\n"
    "v_cmp_gt_i32 vcc, s71, v13\n"
    "s_nop 0\n"
    "s_or_b64 exec, vcc, s[62:63]\n"
    "buffer_atomic_smax v11, v2, s[36:39], 0 offen sc0 sc1\n"
    "s_mov_b64 exec, -1\n"
    "s_waitcnt vmcnt(3)\n"
    "s_branch .LgWO_bar0_%=\n"
    ".LgWO_dma1_54_%=:\n"
    "v_mfma_f32_16x16x32_bf16 v[56:59], a[216:219], v[24:27], v[56:59]\n"
    "s_add_u32 m0, s59, 0\n"
    "v_mfma_f32_16x16x32_bf16 v[60:63], a[220:223], v[24:27], v[60:63]\n"
    "s_waitcnt lgkmcnt(2)\n"
    "s_waitcnt vmcnt(1)\n"
    "buffer_atomic_smax v12, v14, s[36:39], s70 offen sc1\n"
    "v_mfma_f32_16x16x32_bf16 v[68:71], a[224:227], v[24:27], v[68:71]\n"
    "ds_read_b128 v[24:27], v9 offset:22528\n"
    "buffer_load_dwordx4 v1, s[36:39], s76 offen sc1 lds\n"
    "v_mfma_f32_16x16x32_bf16 v[56:59], a[228:231], v[28:31], v[56:59]\n"
    "s_add_u32 s76, s76, 16384\n"
    "s_add_u32 m0, s59, 4096\n"
    "v_mfma_f32_16x16x32_bf16 v[60:63], a[232:235], v[28:31], v[60:63]\n"
    "s_waitcnt lgkmcnt(2)\n"
    "buffer_load_dwordx4 v1, s[36:39], s76 offen sc1 lds\n"
    "v_mfma_f32_16x16x32_bf16 v[68:71], a[236:239], v[28:31], v[68:71]\n"
    "ds_read_b128 v[28:31], v9 offset:23552\n"
    "v_mfma_f32_16x16x32_bf16 v[56:59], a[240:243], v[16:19], v[56:59]\n"
    "s_add_u32 s76, s76, 16384\n"
    "s_add_u32 m0, s59, 8192\n"
    "v_mfma_f32_16x16x32_bf16 v[60:63], a[244:247], v[16:19], v[60:63]\n"
    "s_waitcnt lgkmcnt(2)\n"
    "buffer_load_dwordx4 v1, s[36:39], s76 offen sc1 lds\n"
    "v_mfma_f32_16x16x32_bf16 v[68:71], a[248:251], v[16:19], v[68:71]\n"
    "ds_read_b128 v[16:19], v9 offset:24576\n"
    "v_mfma_f32_16x16x32_bf16 v[56:59], a[252:255], v[20:23], v[56:59]\n"
    "v_mfma_f32_16x16x32_bf16 v[60:63], v[128:131], v[20:23], v[60:63]\n"
    "s_waitcnt lgkmcnt(2)\n"
    "s_add_u32 s76, s76, 16384\n"
    "s_add_u32 m0, s59, 12288\n"
    "v_mfma_f32_16x16x32_bf16 v[68:71], v[132:135], v[20:23], v[68:71]\n"
    "ds_read_b128 v[20:23], v9 offset:25600\n"
    "buffer_load_dwordx4 v1, s[36:39], s76 offen sc1 lds\n"
    "v_mfma_f32_16x16x32_bf16 v[56:59], v[136:139], v[24:27], v[56:59]\n"
    "v_mfma_f32_16x16x32_bf16 v[60:63], v[140:143], v[24:27], v[60:63]\n"
    "s_waitcnt lgkmcnt(2)\n"
    "v_mfma_f32_16x16x32_bf16 v[68:71], v[144:147], v[24:27], v[68:71]\n"
    "ds_read_b128 v[24:27], v9 offset:26624\n"
    "s_add_u32 s76, s76, 16384\n"
    "v_mfma_f32_16x16x32_bf16 v[56:59], v[148:151], v[28:31], v[56:59]\n"
    "s_add_u32 m0, s59, 16384\n"
    "v_mfma_f32_16x16x32_bf16 v[60:63], v[152:155], v[28:31], v[60:63]\n"
    "s_waitcnt lgkmcnt(2)\n"
    "buffer_load_dwordx4 v1, s[36:39], s76 offen sc1 lds\n"
    "v_mfma_f32_16x16x32_bf16 v[68:71], v[156:159], v[28:31], v[68:71]\n"
    "ds_read_b128 v[28:31], v9 offset:27648\n"
    "v_mfma_f32_16x16x32_bf16 v[56:59], v[160:163], v[16:19], v[56:59]\n"
    "s_add_u32 s76, s76, 16384\n"
    "s_add_u32 m0, s59, 20480\n"
    "v_mfma_f32_16x16x32_bf16 v[60:63], v[164:167], v[16:19], v[60:63]\n"
    "s_waitcnt lgkmcnt(2)\n"
    "buffer_load_dwordx4 v1, s[36:39], s76 offen sc1 lds\n"
    "v_mfma_f32_16x16x32_bf16 v[68:71], v[168:171], v[16:19], v[68:71]\n"
    "ds_read_b128 v[16:19], v9 offset:28672\n"
    "v_mfma_f32_16x16x32_bf16 v[56:59], v[172:175], v[20:23], v[56:59]\n"
    "v_mfma_f32_16x16x32_bf16 v[60:63], v[176:179], v[20:23], v[60:63]\n"
    "s_waitcnt lgkmcnt(2)\n"
    "s_add_u32 s76, s76, 16384\n"
    "s_add_u32 m0, s59, 24576\n"
    "v_mfma_f32_16x16x32_bf16 v[68:71], v[180:183], v[20:23], v[68:71]\n"
    "ds_read_b128 v[20:23], v9 offset:29696\n"
    "buffer_load_dwordx4 v1, s[36:39], s76 offen sc1 lds\n"
    "v_mfma_f32_16x16x32_bf16 v[56:59], v[184:187], v[24:27], v[56:59]\n"
    "v_mfma_f32_16x16x32_bf16 v[60:63], v[188:191], v[24:27], v[60:63]\n"
    "s_waitcnt lgkmcnt(2)\n"
    "v_mfma_f32_16x16x32_bf16 v[68:71], v[192:195], v[24:27], v[68:71]\n"
    "ds_read_b128 v[24:27], v9 offset:30720\n"
    "s_add_u32 s76, s76, 16384\n"
    "v_mfma_f32_16x16x32_bf16 v[56:59], v[196:199], v[28:31], v[56:59]\n"
    "s_add_u32 m0, s59, 28672\n"
    "v_mfma_f32_16x16x32_bf16 v[60:63], v[200:203], v[28:31], v[60:63]\n"
    "s_waitcnt lgkmcnt(2)\n"
    "buffer_load_dwordx4 v1, s[36:39], s76 offen sc1 lds\n"
    "v_mfma_f32_16x16x32_bf16 v[68:71], v[204:207], v[28:31], v[68:71]\n"
    "ds_read_b128 v[28:31], v9 offset:31744\n"
    "v_mfma_f32_16x16x32_bf16 v[56:59], v[208:211], v[16:19], v[56:59]\n"
    "v_mfma_f32_16x16x32_bf16 v[60:63], v[212:215], v[16:19], v[60:63]\n"
    "s_waitcnt lgkmcnt(2)\n"
    "v_mfma_f32_16x16x32_bf16 v[68:71], v[216:219], v[16:19], v[68:71]\n"
    "v_mfma_f32_16x16x32_bf16 v[56:59], v[220:223], v[20:23], v[56:59]\n"
    "v_mfma_f32_16x16x32_bf16 v[60:63], v[224:227], v[20:23], v[60:63]\n"
    "s_waitcnt lgkmcnt(1)\n"
    "v_mfma_f32_16x16x32_bf16 v[68:71], v[228:231], v[20:23], v[68:71]\n"
    "v_mfma_f32_16x16x32_bf16 v[56:59], v[232:235], v[24:27], v[56:59]\n"
    "v_mfma_f32_16x16x32_bf16 v[60:63], v[236:239], v[24:27], v[60:63]\n"
    "s_waitcnt lgkmcnt(0)\n"
    "v_mfma_f32_16x16x32_bf16 v[68:71], v[240:243], v[24:27], v[68:71]\n"
    "v_mfma_f32_16x16x32_bf16 v[56:59], v[244:247], v[28:31], v[56:59]\n"
    "v_mfma_f32_16x16x32_bf16 v[60:63], v[248:251], v[28:31], v[60:63]\n"
    "v_mfma_f32_16x16x32_bf16 v[68:71], v[252:255], v[28:31], v[68:71]\n"
    "v_cvt_pk_bf16_f32 v56, v56, v57\n"
    "v_cvt_pk_bf16_f32 v57, v58, v59\n"
    "s_nop 1\n"
    "v_cvt_pk_bf16_f32 v58, v60, v61\n"
    "v_cvt_pk_bf16_f32 v59, v62, v63\n"
    "buffer_store_dwordx4 v[56:59], v15, s[36:39], s73 offen offset:1024 sc1\n"
    "s_nop 0\n"
    "v_cvt_pk_bf16_f32 v68, v68, v69\n"
    "v_cvt_pk_bf16_f32 v69, v70, v71\n"
    "s_nop 0\n"
    "v_permlane16_swap_b32 v68, v70\n"
    "v_permlane16_swap_b32 v69, v71\n"
    "buffer_store_dwordx4 v[68:71], v4, s[36:39], s73 offen offset:1024 sc1\n"
    "v_mov_b32 v12, s68\n"
    "s_mov_b32 s73, 0x80000000\n"
    "s_mov_b32 s68, s71\n"
    "s_mov_b32 s69, 0\n"
    "s_mov_b32 s70, 0x80000000\n"
    "v_mov_b32 v7, v12\n"
    "s_waitcnt vmcnt(11)\n"
    "s_add_u32 s71, s68, 1\n"
    "v_add_u32 v13, v11, v3\n"
    "v_cmp_gt_i32 vcc, s71, v13\n"
    "s_nop 0\n"
    "s_or_b64 exec, vcc, s[62:63]\n"
    "buffer_atomic_smax v11, v2, s[36:39], 0 offen sc0 sc1\n"
    "s_mov_b64 exec, -1\n"
    "s_waitcnt vmcnt(3)\n"
    "s_branch .LgWO_bar0_%=\n"
    ".LgWO_dma1_75_%=:\n"
    "v_mfma_f32_16x16x32_bf16 v[56:59], v[172:175], v[20:23], v[56:59]\n"
    "s_add_u32 m0, s59, 0\n"
    "v_mfma_f32_16x16x32_bf16 v[60:63], v[176:179], v[20:23], v[60:63]\n"
    "s_waitcnt lgkmcnt(2)\n"
    "buffer_load_dwordx4 v1, s[36:39], s76 offen sc1 lds\n"
    "v_mfma_f32_16x16x32_bf16 v[68:71], v[180:183], v[20:23], v[68:71]\n"
    "ds_read_b128 v[20:23], v9 offset:29696\n"
    "s_add_u32 s76, s76, 16384\n"
    "v_mfma_f32_16x16x32_bf16 v[56:59], v[184:187], v[24:27], v[56:59]\n"
    "s_add_u32 m0, s59, 4096\n"
    "v_mfma_f32_16x16x32_bf16 v[60:63], v[188:191], v[24:27], v[60:63]\n"
    "s_waitcnt lgkmcnt(2)\n"
    "buffer_load_dwordx4 v1, s[36:39], s76 offen sc1 lds\n"
    "v_mfma_f32_16x16x32_bf16 v[68:71], v[192:195], v[24:27], v[68:71]\n"
    "ds_read_b128 v[24:27], v9 offset:30720\n"
    "v_mfma_f32_16x16x32_bf16 v[56:59], v[196:199], v[28:31], v[56:59]\n"
    "s_add_u32 s76, s76, 16384\n"
    "s_add_u32 m0, s59, 8192\n"
    "v_mfma_f32_16x16x32_bf16 v[60:63], v[200:203], v[28:31], v[60:63]\n"
    "s_waitcnt lgkmcnt(2)\n"
    "buffer_load_dwordx4 v1, s[36:39], s76 offen sc1 lds\n"
    "v_mfma_f32_16x16x32_bf16 v[68:71], v[204:207], v[28:31], v[68:71]\n"
    "ds_read_b128 v[28:31], v9 offset:31744\n"
    "v_mfma_f32_16x16x32_bf16 v[56:59], v[208:211], v[16:19], v[56:59]\n"
    "v_mfma_f32_16x16x32_bf16 v[60:63], v[212:215], v[16:19], v[60:63]\n"
    "s_waitcnt lgkmcnt(2)\n"
    "s_add_u32 s76, s76, 16384\n"
    "s_add_u32 m0, s59, 12288\n"
    "v_mfma_f32_16x16x32_bf16 v[68:71], v[216:219], v[16:19], v[68:71]\n"
    "buffer_load_dwordx4 v1, s[36:39], s76 offen sc1 lds\n"
    "v_mfma_f32_16x16x32_bf16 v[56:59], v[220:223], v[20:23], v[56:59]\n"
    "v_mfma_f32_16x16x32_bf16 v[60:63], v[224:227], v[20:23], v[60:63]\n"
    "s_waitcnt lgkmcnt(1)\n"
    "v_mfma_f32_16x16x32_bf16 v[68:71], v[228:231], v[20:23], v[68:71]\n"
    "s_add_u32 s76, s76, 16384\n"
    "v_mfma_f32_16x16x32_bf16 v[56:59], v[232:235], v[24:27], v[56:59]\n"
    "s_add_u32 m0, s59, 16384\n"
    "v_mfma_f32_16x16x32_bf16 v[60:63], v[236:239], v[24:27], v[60:63]\n"
    "s_waitcnt lgkmcnt(0)\n"
    "buffer_load_dwordx4 v1, s[36:39], s76 offen sc1 lds\n"
    "v_mfma_f32_16x16x32_bf16 v[68:71], v[240:243], v[24:27], v[68:71]\n"
    "v_mfma_f32_16x16x32_bf16 v[56:59], v[244:247], v[28:31], v[56:59]\n"
    "s_add_u32 s76, s76, 16384\n"
    "s_add_u32 m0, s59, 20480\n"
    "v_mfma_f32_16x16x32_bf16 v[60:63], v[248:251], v[28:31], v[60:63]\n"
    "buffer_load_dwordx4 v1, s[36:39], s76 offen sc1 lds\n"
    "v_mfma_f32_16x16x32_bf16 v[68:71], v[252:255], v[28:31], v[68:71]\n"
    "v_cvt_pk_bf16_f32 v56, v56, v57\n"
    "v_cvt_pk_bf16_f32 v57, v58, v59\n"
    "s_nop 1\n"
    "v_cvt_pk_bf16_f32 v58, v60, v61\n"
    "v_cvt_pk_bf16_f32 v59, v62, v63\n"
    "buffer_store_dwordx4 v[56:59], v15, s[36:39], s73 offen offset:1024 sc1\n"
    "s_nop 0\n"
    "v_cvt_pk_bf16_f32 v68, v68, v69\n"
    "v_cvt_pk_bf16_f32 v69, v70, v71\n"
    "s_nop 0\n"
    "v_permlane16_swap_b32 v68, v70\n"
    "v_permlane16_swap_b32 v69, v71\n"
    "buffer_store_dwordx4 v[68:71], v4, s[36:39], s73 offen offset:1024 sc1\n"
    "v_mov_b32 v12, s68\n"
    "s_mov_b32 s73, 0x80000000\n"
    "s_mov_b32 s68, s71\n"
    "s_add_u32 m0, s59, 24576\n"
    "s_add_u32 s76, s76, 16384\n"
    "buffer_load_dwordx4 v1, s[36:39], s76 offen sc1 lds\n"
    "s_add_u32 m0, s59, 28672\n"
    "s_add_u32 s76, s76, 16384\n"
    "buffer_load_dwordx4 v1, s[36:39], s76 offen sc1 lds\n"
    "s_waitcnt vmcnt(2)\n"
    "buffer_atomic_smax v12, v10, s[36:39], 0 offen sc1\n"
    "s_mov_b32 s69, 0x80000000\n"
    "s_mov_b32 s70, 0x80000000\n"
    "v_mov_b32 v7, v12\n"
    "s_waitcnt vmcnt(12)\n"
    "s_add_u32 s71, s68, 1\n"
    "v_add_u32 v13, v11, v3\n"
    "v_cmp_gt_i32 vcc, s71, v13\n"
    "s_nop 0\n"
    "s_or_b64 exec, vcc, s[62:63]\n"
    "buffer_atomic_smax v11, v2, s[36:39], 0 offen sc0 sc1\n"
    "s_mov_b64 exec, -1\n"
    "s_waitcnt vmcnt(2)\n"
    "s_branch .LgWO_bar0_%=\n"
    ".LgWO_idle1_%=:\n"
    "s_or_b64 exec, vcc, s[62:63]\n"
    "buffer_atomic_smax v11, v2, s[36:39], 0 offen sc0 sc1\n"
    "s_mov_b64 exec, -1\n"
    "s_waitcnt vmcnt(1)\n"
    "buffer_atomic_smax v12, v10, s[36:39], 0 offen sc1\n"
    "s_branch .LgWO_block1_0_%=\n"
    ".LgWO_block1_%=:\n"
    "v_add_u32 v13, v11, v3\n"
    "v_cmp_gt_i32 vcc, s71, v13\n"
    "s_nop 0\n"
    "s_or_b64 exec, vcc, s[62:63]\n"
    "buffer_atomic_smax v11, v2, s[36:39], 0 offen sc0 sc1\n"
    "s_mov_b64 exec, -1\n"
    "s_sleep 3\n"
    "s_waitcnt vmcnt(1)\n"
    ".LgWO_block1_0_%=:\n"
    "v_add_u32 v13, v5, v3\n"
    "v_cmp_gt_i32 vcc, s71, v13\n"
    "s_cbranch_vccz .LgWO_go1_%=\n"
    "v_add_u32 v13, v5, v3\n"
    "v_cmp_gt_i32 vcc, s71, v13\n"
    "s_nop 0\n"
    "s_or_b64 exec, vcc, s[62:63]\n"
    "buffer_atomic_smax v5, v2, s[36:39], 0 offen sc0 sc1\n"
    "s_mov_b64 exec, -1\n"
    "s_sleep 3\n"
    "s_waitcnt vmcnt(1)\n"
    ".LgWO_block1_1_%=:\n"
    "v_add_u32 v13, v11, v3\n"
    "v_cmp_gt_i32 vcc, s71, v13\n"
    "s_cbranch_vccz .LgWO_go1_%=\n"
    "s_sub_u32 s72, s72, 1\n"
    "s_cbranch_scc0 .LgWO_block1_%=\n"
    "s_mov_b32 s72, 0\n"
    ".LgWO_go1_%=:\n"
    "s_lshr_b32 s76, s71, 1\n"
    "s_and_b32 s76, s76, s54\n"
    "s_mul_i32 s76, s76, s53\n"
    "s_add_u32 s76, s76, s52\n"
    "s_add_u32 m0, s59, 0\n"
    "s_nop 0\n"
    "buffer_load_dwordx4 v1, s[36:39], s76 offen sc1 lds\n"
    "s_add_u32 m0, s59, 4096\n"
    "s_add_u32 s76, s76, 16384\n"
    "buffer_load_dwordx4 v1, s[36:39], s76 offen sc1 lds\n"
    "s_add_u32 m0, s59, 8192\n"
    "s_add_u32 s76, s76, 16384\n"
    "buffer_load_dwordx4 v1, s[36:39], s76 offen sc1 lds\n"
    "s_add_u32 m0, s59, 12288\n"
    "s_add_u32 s76, s76, 16384\n"
    "buffer_load_dwordx4 v1, s[36:39], s76 offen sc1 lds\n"
    "s_add_u32 m0, s59, 16384\n"
    "s_add_u32 s76, s76, 16384\n"
    "buffer_load_dwordx4 v1, s[36:39], s76 offen sc1 lds\n"
    "s_add_u32 m0, s59, 20480\n"
    "s_add_u32 s76, s76, 16384\n"
    "buffer_load_dwordx4 v1, s[36:39], s76 offen sc1 lds\n"
    "s_add_u32 m0, s59, 24576\n"
    "s_add_u32 s76, s76, 16384\n"
    "buffer_load_dwordx4 v1, s[36:39], s76 offen sc1 lds\n"
    "s_add_u32 m0, s59, 28672\n"
    "s_add_u32 s76, s76, 16384\n"
    "buffer_load_dwordx4 v1, s[36:39], s76 offen sc1 lds\n"
    "s_mov_b32 s69, 0x80000000\n"
    "s_mov_b32 s70, 0x80000000\n"
    "v_mov_b32 v7, v12\n"
    "s_waitcnt vmcnt(8)\n"
    "s_add_u32 s71, s68, 1\n"
    "v_add_u32 v13, v11, v3\n"
    "v_cmp_gt_i32 vcc, s71, v13\n"
    "s_nop 0\n"
    "s_or_b64 exec, vcc, s[62:63]\n"
    "buffer_atomic_smax v11, v2, s[36:39], 0 offen sc0 sc1\n"
    "s_mov_b64 exec, -1\n"
    "s_waitcnt vmcnt(1)\n"
    "s_branch .LgWO_bar0_%=\n"
    ".LgWO_bar1_%=:\n"
    "s_barrier\n"
    "ds_read_b128 v[16:19], v9 offset:0\n"
    "ds_read_b128 v[20:23], v9 offset:1024\n"
    "ds_read_b128 v[24:27], v9 offset:2048\n"
    "ds_read_b128 v[28:31], v9 offset:3072\n"
    "v_add_u32 v13, v5, v3\n"
    "v_cmp_gt_i32 vcc, s71, v13\n"
    "s_nop 0\n"
    "s_or_b64 exec, vcc, s[62:63]\n"
    "buffer_atomic_smax v5, v2, s[36:39], 0 offen sc0 sc1\n"
    "s_mov_b64 exec, -1\n"
    "s_lshr_b32 s76, s71, 1\n"
    "s_and_b32 s76, s76, s54\n"
    "s_mul_i32 s76, s76, s53\n"
    "s_add_u32 s76, s76, s52\n"
    "s_waitcnt lgkmcnt(3)\n"
    "s_branch .LgWO_head1_%=\n"
    ".LgWO_FIN_%=:\n"
  );
}
DEVI void role_down(const Params& p, int L, int j, int i, u32 dbg, unsigned long long tag) {
  const int lane = threadIdx.x & 63, w = threadIdx.x >> 6, e0 = j * 1152 * 576 + i * 192;
  GEMM_DECL;
  g_w0 = make_srd(p.w2 + e0, 4608u * 576 * 2 - e0 * 2); g_w1 = g_w0; g_w2 = g_w0;
  g_ldw = 1152;
  g_in_base = pgb(L, B_H + 9 * j); g_in_stride = SLOT; g_in_mask = RH - 1;
  g_out_base = pgb(L, B_PF + 5 * j); g_out_kb = 12 * i; g_out_mask = RP - 1;
  g_flag = lane < 4 ? mbox(L, lane, SL_REDF, 12 * j + 4 * i + w) : ((lane < 10 && w == 0) ? mbox(L, j, SL_UP + lane - 4, 16 + i) : OOB);
  g_paddr = mbox(L, j, SL_DOWN + i, lane);
  g_pbias = lane < 24 ? 0 : (lane < 40 ? 2 * RP : 1000);
  v16u sa, sb, va;
  gemm_args(GEMM_ARGS, p, dbg, tag, sa, sb, va);
  RUN_ASM(
    "s_nop 4\n"
    "s_getpc_b64 s[84:85]\n"
    ".LgDOWN_PC_%=:\n"
    "s_and_b32 s85, s85, 0xffff\n"
    "s_mov_b32 s86, .LgDOWN_FIN_%=-.LgDOWN_PC_%=\n"
    "s_mov_b32 s87, 0x20000\n"
    "v_mbcnt_lo_u32_b32 v40, -1, 0\n"
    "v_mbcnt_hi_u32_b32 v40, -1, v40\n"
    "v_lshlrev_b32 v40, 7, v40\n"
    "buffer_load_dword v41, v40, s[84:87], 0 offen\n"
    "v_add_u32 v40, 8192, v40\n"
    "buffer_load_dword v41, v40, s[84:87], 0 offen\n"
    "v_add_u32 v40, 8192, v40\n"
    "buffer_load_dword v41, v40, s[84:87], 0 offen\n"
    "s_mov_b32 s68, 0\n"
    "s_mov_b32 s71, 0\n"
    "v_add_u32 v9, 36864, v0\n"
    "s_mov_b32 s64, 0xbfb8aa3b\n"
    "s_mov_b32 s72, 262144\n"
    "s_add_u32 m0, s59, 0\n"
    "s_mov_b32 s74, 0\n"
    "buffer_load_dwordx4 v5, s[40:43], s74 offen lds\n"
    "s_add_u32 m0, s59, 4096\n"
    "s_mov_b32 s74, 128\n"
    "buffer_load_dwordx4 v5, s[40:43], s74 offen lds\n"
    "s_add_u32 m0, s59, 8192\n"
    "s_mov_b32 s74, 256\n"
    "buffer_load_dwordx4 v5, s[40:43], s74 offen lds\n"
    "s_add_u32 m0, s59, 12288\n"
    "s_mov_b32 s74, 36864\n"
    "buffer_load_dwordx4 v5, s[40:43], s74 offen lds\n"
    "s_add_u32 m0, s59, 16384\n"
    "s_mov_b32 s74, 36992\n"
    "buffer_load_dwordx4 v5, s[40:43], s74 offen lds\n"
    "s_add_u32 m0, s59, 20480\n"
    "s_mov_b32 s74, 37120\n"
    "buffer_load_dwordx4 v5, s[40:43], s74 offen lds\n"
    "s_add_u32 m0, s59, 24576\n"
    "s_mov_b32 s74, 73728\n"
    "buffer_load_dwordx4 v5, s[40:43], s74 offen lds\n"
    "s_add_u32 m0, s59, 28672\n"
    "s_mov_b32 s74, 73856\n"
    "buffer_load_dwordx4 v5, s[40:43], s74 offen lds\n"
    "s_add_u32 m0, s59, 32768\n"
    "s_mov_b32 s74, 73984\n"
    "buffer_load_dwordx4 v5, s[40:43], s74 offen lds\n"
    "s_add_u32 m0, s59, 36864\n"
    "s_mov_b32 s74, 110592\n"
    "buffer_load_dwordx4 v5, s[40:43], s74 offen lds\n"
    "s_add_u32 m0, s59, 40960\n"
    "s_mov_b32 s74, 110720\n"
    "buffer_load_dwordx4 v5, s[40:43], s74 offen lds\n"
    "s_add_u32 m0, s59, 45056\n"
    "s_mov_b32 s74, 110848\n"
    "buffer_load_dwordx4 v5, s[40:43], s74 offen lds\n"
    "s_add_u32 m0, s59, 49152\n"
    "s_mov_b32 s74, 147456\n"
    "buffer_load_dwordx4 v5, s[40:43], s74 offen lds\n"
    "s_add_u32 m0, s59, 53248\n"
    "s_mov_b32 s74, 147584\n"
    "buffer_load_dwordx4 v5, s[40:43], s74 offen lds\n"
    "s_add_u32 m0, s59, 57344\n"
    "s_mov_b32 s74, 147712\n"
    "buffer_load_dwordx4 v5, s[40:43], s74 offen lds\n"
    "s_add_u32 m0, s59, 61440\n"
    "s_mov_b32 s74, 184320\n"
    "buffer_load_dwordx4 v5, s[40:43], s74 offen lds\n"
    "s_add_u32 m0, s59, 65536\n"
    "s_mov_b32 s74, 184448\n"
    "buffer_load_dwordx4 v5, s[40:43], s74 offen lds\n"
    "s_add_u32 m0, s59, 69632\n"
    "s_mov_b32 s74, 184576\n"
    "buffer_load_dwordx4 v5, s[40:43], s74 offen lds\n"
    "s_add_u32 m0, s59, 73728\n"
    "s_mov_b32 s74, 221184\n"
    "buffer_load_dwordx4 v5, s[40:43], s74 offen lds\n"
    "s_add_u32 m0, s59, 77824\n"
    "s_mov_b32 s74, 221312\n"
    "buffer_load_dwordx4 v5, s[40:43], s74 offen lds\n"
    "s_add_u32 m0, s59, 81920\n"
    "s_mov_b32 s74, 221440\n"
    "buffer_load_dwordx4 v5, s[40:43], s74 offen lds\n"
    "s_add_u32 m0, s59, 86016\n"
    "s_mov_b32 s74, 258048\n"
    "buffer_load_dwordx4 v5, s[40:43], s74 offen lds\n"
    "s_add_u32 m0, s59, 90112\n"
    "s_mov_b32 s74, 258176\n"
    "buffer_load_dwordx4 v5, s[40:43], s74 offen lds\n"
    "s_add_u32 m0, s59, 94208\n"
    "s_mov_b32 s74, 258304\n"
    "buffer_load_dwordx4 v5, s[40:43], s74 offen lds\n"
    "s_add_u32 m0, s59, 98304\n"
    "s_mov_b32 s74, 294912\n"
    "buffer_load_dwordx4 v5, s[40:43], s74 offen lds\n"
    "s_add_u32 m0, s59, 102400\n"
    "s_mov_b32 s74, 295040\n"
    "buffer_load_dwordx4 v5, s[40:43], s74 offen lds\n"
    "s_add_u32 m0, s59, 106496\n"
    "s_mov_b32 s74, 295168\n"
    "buffer_load_dwordx4 v5, s[40:43], s74 offen lds\n"
    "s_add_u32 m0, s59, 110592\n"
    "s_mov_b32 s74, 331776\n"
    "buffer_load_dwordx4 v5, s[40:43], s74 offen lds\n"
    "s_add_u32 m0, s59, 114688\n"
    "s_mov_b32 s74, 331904\n"
    "buffer_load_dwordx4 v5, s[40:43], s74 offen lds\n"
    "s_add_u32 m0, s59, 118784\n"
    "s_mov_b32 s74, 332032\n"
    "buffer_load_dwordx4 v5, s[40:43], s74 offen lds\n"
    "v_add_u32 v16, 49152, v6\n"
    "v_add_u32 v17, 98304, v6\n"
    "v_add_u32 v18, 49152, v7\n"
    "v_add_u32 v19, 98304, v7\n"
    "s_waitcnt vmcnt(24) lgkmcnt(0)\n"
    "s_barrier\n"
    "s_add_u32 m0, s59, 122880\n"
    "s_mov_b32 s74, 368640\n"
    "buffer_load_dwordx4 v5, s[40:43], s74 offen lds\n"
    "s_add_u32 m0, s59, 126976\n"
    "s_mov_b32 s74, 368768\n"
    "buffer_load_dwordx4 v5, s[40:43], s74 offen lds\n"
    "s_add_u32 m0, s59, 131072\n"
    "s_mov_b32 s74, 368896\n"
    "buffer_load_dwordx4 v5, s[40:43], s74 offen lds\n"
    "s_add_u32 m0, s59, 135168\n"
    "s_mov_b32 s74, 405504\n"
    "buffer_load_dwordx4 v5, s[40:43], s74 offen lds\n"
    "s_add_u32 m0, s59, 139264\n"
    "s_mov_b32 s74, 405632\n"
    "buffer_load_dwordx4 v5, s[40:43], s74 offen lds\n"
    "s_add_u32 m0, s59, 143360\n"
    "s_mov_b32 s74, 405760\n"
    "buffer_load_dwordx4 v5, s[40:43], s74 offen lds\n"
    "ds_read_b64_tr_b16 a[0:1], v6 offset:0\n"
    "ds_read_b64_tr_b16 a[2:3], v6 offset:512\n"
    "ds_read_b64_tr_b16 a[4:5], v6 offset:8\n"
    "ds_read_b64_tr_b16 a[6:7], v6 offset:520\n"
    "ds_read_b64_tr_b16 a[8:9], v7 offset:0\n"
    "ds_read_b64_tr_b16 a[10:11], v7 offset:512\n"
    "ds_read_b64_tr_b16 a[12:13], v6 offset:12288\n"
    "ds_read_b64_tr_b16 a[14:15], v6 offset:12800\n"
    "ds_read_b64_tr_b16 a[16:17], v6 offset:12296\n"
    "ds_read_b64_tr_b16 a[18:19], v6 offset:12808\n"
    "ds_read_b64_tr_b16 a[20:21], v7 offset:12288\n"
    "ds_read_b64_tr_b16 a[22:23], v7 offset:12800\n"
    "s_waitcnt vmcnt(24) lgkmcnt(0)\n"
    "s_barrier\n"
    "s_add_u32 m0, s59, 0\n"
    "s_mov_b32 s74, 442368\n"
    "buffer_load_dwordx4 v5, s[40:43], s74 offen lds\n"
    "s_add_u32 m0, s59, 4096\n"
    "s_mov_b32 s74, 442496\n"
    "buffer_load_dwordx4 v5, s[40:43], s74 offen lds\n"
    "s_add_u32 m0, s59, 8192\n"
    "s_mov_b32 s74, 442624\n"
    "buffer_load_dwordx4 v5, s[40:43], s74 offen lds\n"
    "s_add_u32 m0, s59, 12288\n"
    "s_mov_b32 s74, 479232\n"
    "buffer_load_dwordx4 v5, s[40:43], s74 offen lds\n"
    "s_add_u32 m0, s59, 16384\n"
    "s_mov_b32 s74, 479360\n"
    "buffer_load_dwordx4 v5, s[40:43], s74 offen lds\n"
    "s_add_u32 m0, s59, 20480\n"
    "s_mov_b32 s74, 479488\n"
    "buffer_load_dwordx4 v5, s[40:43], s74 offen lds\n"
    "ds_read_b64_tr_b16 a[24:25], v6 offset:24576\n"
    "ds_read_b64_tr_b16 a[26:27], v6 offset:25088\n"
    "ds_read_b64_tr_b16 a[28:29], v6 offset:24584\n"
    "ds_read_b64_tr_b16 a[30:31], v6 offset:25096\n"
    "ds_read_b64_tr_b16 a[32:33], v7 offset:24576\n"
    "ds_read_b64_tr_b16 a[34:35], v7 offset:25088\n"
    "ds_read_b64_tr_b16 a[36:37], v6 offset:36864\n"
    "ds_read_b64_tr_b16 a[38:39], v6 offset:37376\n"
    "ds_read_b64_tr_b16 a[40:41], v6 offset:36872\n"
    "ds_read_b64_tr_b16 a[42:43], v6 offset:37384\n"
    "ds_read_b64_tr_b16 a[44:45], v7 offset:36864\n"
    "ds_read_b64_tr_b16 a[46:47], v7 offset:37376\n"
    "s_waitcnt vmcnt(24) lgkmcnt(0)\n"
    "s_barrier\n"
    "s_add_u32 m0, s59, 24576\n"
    "s_mov_b32 s74, 516096\n"
    "buffer_load_dwordx4 v5, s[40:43], s74 offen lds\n"
    "s_add_u32 m0, s59, 28672\n"
    "s_mov_b32 s74, 516224\n"
    "buffer_load_dwordx4 v5, s[40:43], s74 offen lds\n"
    "s_add_u32 m0, s59, 32768\n"
    "s_mov_b32 s74, 516352\n"
    "buffer_load_dwordx4 v5, s[40:43], s74 offen lds\n"
    "s_add_u32 m0, s59, 36864\n"
    "s_mov_b32 s74, 552960\n"
    "buffer_load_dwordx4 v5, s[40:43], s74 offen lds\n"
    "s_add_u32 m0, s59, 40960\n"
    "s_mov_b32 s74, 553088\n"
    "buffer_load_dwordx4 v5, s[40:43], s74 offen lds\n"
    "s_add_u32 m0, s59, 45056\n"
    "s_mov_b32 s74, 553216\n"
    "buffer_load_dwordx4 v5, s[40:43], s74 offen lds\n"
    "ds_read_b64_tr_b16 a[48:49], v16 offset:0\n"
    "ds_read_b64_tr_b16 a[50:51], v16 offset:512\n"
    "ds_read_b64_tr_b16 a[52:53], v16 offset:8\n"
    "ds_read_b64_tr_b16 a[54:55], v16 offset:520\n"
    "ds_read_b64_tr_b16 a[56:57], v18 offset:0\n"
    "ds_read_b64_tr_b16 a[58:59], v18 offset:512\n"
    "ds_read_b64_tr_b16 a[60:61], v16 offset:12288\n"
    "ds_read_b64_tr_b16 a[62:63], v16 offset:12800\n"
    "ds_read_b64_tr_b16 a[64:65], v16 offset:12296\n"
    "ds_read_b64_tr_b16 a[66:67], v16 offset:12808\n"
    "ds_read_b64_tr_b16 a[68:69], v18 offset:12288\n"
    "ds_read_b64_tr_b16 a[70:71], v18 offset:12800\n"
    "s_waitcnt vmcnt(24) lgkmcnt(0)\n"
    "s_barrier\n"
    "s_add_u32 m0, s59, 49152\n"
    "s_mov_b32 s74, 589824\n"
    "buffer_load_dwordx4 v5, s[40:43], s74 offen lds\n"
    "s_add_u32 m0, s59, 53248\n"
    "s_mov_b32 s74, 589952\n"
    "buffer_load_dwordx4 v5, s[40:43], s74 offen lds\n"
    "s_add_u32 m0, s59, 57344\n"
    "s_mov_b32 s74, 590080\n"
    "buffer_load_dwordx4 v5, s[40:43], s74 offen lds\n"
    "s_add_u32 m0, s59, 61440\n"
    "s_mov_b32 s74, 626688\n"
    "buffer_load_dwordx4 v5, s[40:43], s74 offen lds\n"
    "s_add_u32 m0, s59, 65536\n"
    "s_mov_b32 s74, 626816\n"
    "buffer_load_dwordx4 v5, s[40:43], s74 offen lds\n"
    "s_add_u32 m0, s59, 69632\n"
    "s_mov_b32 s74, 626944\n"
    "buffer_load_dwordx4 v5, s[40:43], s74 offen lds\n"
    "ds_read_b64_tr_b16 a[72:73], v16 offset:24576\n"
    "ds_read_b64_tr_b16 a[74:75], v16 offset:25088\n"
    "ds_read_b64_tr_b16 a[76:77], v16 offset:24584\n"
    "ds_read_b64_tr_b16 a[78:79], v16 offset:25096\n"
    "ds_read_b64_tr_b16 a[80:81], v18 offset:24576\n"
    "ds_read_b64_tr_b16 a[82:83], v18 offset:25088\n"
    "ds_read_b64_tr_b16 a[84:85], v16 offset:36864\n"
    "ds_read_b64_tr_b16 a[86:87], v16 offset:37376\n"
    "ds_read_b64_tr_b16 a[88:89], v16 offset:36872\n"
    "ds_read_b64_tr_b16 a[90:91], v16 offset:37384\n"
    "ds_read_b64_tr_b16 a[92:93], v18 offset:36864\n"
    "ds_read_b64_tr_b16 a[94:95], v18 offset:37376\n"
    "s_waitcnt vmcnt(24) lgkmcnt(0)\n"
    "s_barrier\n"
    "s_add_u32 m0, s59, 73728\n"
    "s_mov_b32 s74, 663552\n"
    "buffer_load_dwordx4 v5, s[40:43], s74 offen lds\n"
    "s_add_u32 m0, s59, 77824\n"
    "s_mov_b32 s74, 663680\n"
    "buffer_load_dwordx4 v5, s[40:43], s74 offen lds\n"
    "s_add_u32 m0, s59, 81920\n"
    "s_mov_b32 s74, 663808\n"
    "buffer_load_dwordx4 v5, s[40:43], s74 offen lds\n"
    "s_add_u32 m0, s59, 86016\n"
    "s_mov_b32 s74, 700416\n"
    "buffer_load_dwordx4 v5, s[40:43], s74 offen lds\n"
    "s_add_u32 m0, s59, 90112\n"
    "s_mov_b32 s74, 700544\n"
    "buffer_load_dwordx4 v5, s[40:43], s74 offen lds\n"
    "s_add_u32 m0, s59, 94208\n"
    "s_mov_b32 s74, 700672\n"
    "buffer_load_dwordx4 v5, s[40:43], s74 offen lds\n"
    "ds_read_b64_tr_b16 a[96:97], v17 offset:0\n"
    "ds_read_b64_tr_b16 a[98:99], v17 offset:512\n"
    "ds_read_b64_tr_b16 a[100:101], v17 offset:8\n"
    "ds_read_b64_tr_b16 a[102:103], v17 offset:520\n"
    "ds_read_b64_tr_b16 a[104:105], v19 offset:0\n"
    "ds_read_b64_tr_b16 a[106:107], v19 offset:512\n"
    "ds_read_b64_tr_b16 a[108:109], v17 offset:12288\n"
    "ds_read_b64_tr_b16 a[110:111], v17 offset:12800\n"
    "ds_read_b64_tr_b16 a[112:113], v17 offset:12296\n"
    "ds_read_b64_tr_b16 a[114:115], v17 offset:12808\n"
    "ds_read_b64_tr_b16 a[116:117], v19 offset:12288\n"
    "ds_read_b64_tr_b16 a[118:119], v19 offset:12800\n"
    "s_waitcnt vmcnt(24) lgkmcnt(0)\n"
    "s_barrier\n"
    "s_add_u32 m0, s59, 98304\n"
    "s_mov_b32 s74, 737280\n"
    "buffer_load_dwordx4 v5, s[40:43], s74 offen lds\n"
    "s_add_u32 m0, s59, 102400\n"
    "s_mov_b32 s74, 737408\n"
    "buffer_load_dwordx4 v5, s[40:43], s74 offen lds\n"
    "s_add_u32 m0, s59, 106496\n"
    "s_mov_b32 s74, 737536\n"
    "buffer_load_dwordx4 v5, s[40:43], s74 offen lds\n"
    "s_add_u32 m0, s59, 110592\n"
    "s_mov_b32 s74, 774144\n"
    "buffer_load_dwordx4 v5, s[40:43], s74 offen lds\n"
    "s_add_u32 m0, s59, 114688\n"
    "s_mov_b32 s74, 774272\n"
    "buffer_load_dwordx4 v5, s[40:43], s74 offen lds\n"
    "s_add_u32 m0, s59, 118784\n"
    "s_mov_b32 s74, 774400\n"
    "buffer_load_dwordx4 v5, s[40:43], s74 offen lds\n"
    "ds_read_b64_tr_b16 a[120:121], v17 offset:24576\n"
    "ds_read_b64_tr_b16 a[122:123], v17 offset:25088\n"
    "ds_read_b64_tr_b16 a[124:125], v17 offset:24584\n"
    "ds_read_b64_tr_b16 a[126:127], v17 offset:25096\n"
    "ds_read_b64_tr_b16 a[128:129], v19 offset:24576\n"
    "ds_read_b64_tr_b16 a[130:131], v19 offset:25088\n"
    "ds_read_b64_tr_b16 a[132:133], v17 offset:36864\n"
    "ds_read_b64_tr_b16 a[134:135], v17 offset:37376\n"
    "ds_read_b64_tr_b16 a[136:137], v17 offset:36872\n"
    "ds_read_b64_tr_b16 a[138:139], v17 offset:37384\n"
    "ds_read_b64_tr_b16 a[140:141], v19 offset:36864\n"
    "ds_read_b64_tr_b16 a[142:143], v19 offset:37376\n"
    "s_waitcnt vmcnt(24) lgkmcnt(0)\n"
    "s_barrier\n"
    "s_add_u32 m0, s59, 122880\n"
    "s_mov_b32 s74, 811008\n"
    "buffer_load_dwordx4 v5, s[40:43], s74 offen lds\n"
    "s_add_u32 m0, s59, 126976\n"
    "s_mov_b32 s74, 811136\n"
    "buffer_load_dwordx4 v5, s[40:43], s74 offen lds\n"
    "s_add_u32 m0, s59, 131072\n"
    "s_mov_b32 s74, 811264\n"
    "buffer_load_dwordx4 v5, s[40:43], s74 offen lds\n"
    "s_add_u32 m0, s59, 135168\n"
    "s_mov_b32 s74, 847872\n"
    "buffer_load_dwordx4 v5, s[40:43], s74 offen lds\n"
    "s_add_u32 m0, s59, 139264\n"
    "s_mov_b32 s74, 848000\n"
    "buffer_load_dwordx4 v5, s[40:43], s74 offen lds\n"
    "s_add_u32 m0, s59, 143360\n"
    "s_mov_b32 s74, 848128\n"
    "buffer_load_dwordx4 v5, s[40:43], s74 offen lds\n"
    "ds_read_b64_tr_b16 a[144:145], v6 offset:0\n"
    "ds_read_b64_tr_b16 a[146:147], v6 offset:512\n"
    "ds_read_b64_tr_b16 a[148:149], v6 offset:8\n"
    "ds_read_b64_tr_b16 a[150:151], v6 offset:520\n"
    "ds_read_b64_tr_b16 a[152:153], v7 offset:0\n"
    "ds_read_b64_tr_b16 a[154:155], v7 offset:512\n"
    "ds_read_b64_tr_b16 a[156:157], v6 offset:12288\n"
    "ds_read_b64_tr_b16 a[158:159], v6 offset:12800\n"
    "ds_read_b64_tr_b16 a[160:161], v6 offset:12296\n"
    "ds_read_b64_tr_b16 a[162:163], v6 offset:12808\n"
    "ds_read_b64_tr_b16 a[164:165], v7 offset:12288\n"
    "ds_read_b64_tr_b16 a[166:167], v7 offset:12800\n"
    "s_waitcnt vmcnt(24) lgkmcnt(0)\n"
    "s_barrier\n"
    "s_add_u32 m0, s59, 0\n"
    "s_mov_b32 s74, 884736\n"
    "buffer_load_dwordx4 v5, s[40:43], s74 offen lds\n"
    "s_add_u32 m0, s59, 4096\n"
    "s_mov_b32 s74, 884864\n"
    "buffer_load_dwordx4 v5, s[40:43], s74 offen lds\n"
    "s_add_u32 m0, s59, 8192\n"
    "s_mov_b32 s74, 884992\n"
    "buffer_load_dwordx4 v5, s[40:43], s74 offen lds\n"
    "s_add_u32 m0, s59, 12288\n"
    "s_mov_b32 s74, 921600\n"
    "buffer_load_dwordx4 v5, s[40:43], s74 offen lds\n"
    "s_add_u32 m0, s59, 16384\n"
    "s_mov_b32 s74, 921728\n"
    "buffer_load_dwordx4 v5, s[40:43], s74 offen lds\n"
    "s_add_u32 m0, s59, 20480\n"
    "s_mov_b32 s74, 921856\n"
    "buffer_load_dwordx4 v5, s[40:43], s74 offen lds\n"
    "ds_read_b64_tr_b16 a[168:169], v6 offset:24576\n"
    "ds_read_b64_tr_b16 a[170:171], v6 offset:25088\n"
    "ds_read_b64_tr_b16 a[172:173], v6 offset:24584\n"
    "ds_read_b64_tr_b16 a[174:175], v6 offset:25096\n"
    "ds_read_b64_tr_b16 a[176:177], v7 offset:24576\n"
    "ds_read_b64_tr_b16 a[178:179], v7 offset:25088\n"
    "ds_read_b64_tr_b16 a[180:181], v6 offset:36864\n"
    "ds_read_b64_tr_b16 a[182:183], v6 offset:37376\n"
    "ds_read_b64_tr_b16 a[184:185], v6 offset:36872\n"
    "ds_read_b64_tr_b16 a[186:187], v6 offset:37384\n"
    "ds_read_b64_tr_b16 a[188:189], v7 offset:36864\n"
    "ds_read_b64_tr_b16 a[190:191], v7 offset:37376\n"
    "s_waitcnt vmcnt(24) lgkmcnt(0)\n"
    "s_barrier\n"
    "s_add_u32 m0, s59, 24576\n"
    "s_mov_b32 s74, 958464\n"
    "buffer_load_dwordx4 v5, s[40:43], s74 offen lds\n"
    "s_add_u32 m0, s59, 28672\n"
    "s_mov_b32 s74, 958592\n"
    "buffer_load_dwordx4 v5, s[40:43], s74 offen lds\n"
    "s_add_u32 m0, s59, 32768\n"
    "s_mov_b32 s74, 958720\n"
    "buffer_load_dwordx4 v5, s[40:43], s74 offen lds\n"
    "s_add_u32 m0, s59, 36864\n"
    "s_mov_b32 s74, 995328\n"
    "buffer_load_dwordx4 v5, s[40:43], s74 offen lds\n"
    "s_add_u32 m0, s59, 40960\n"
    "s_mov_b32 s74, 995456\n"
    "buffer_load_dwordx4 v5, s[40:43], s74 offen lds\n"
    "s_add_u32 m0, s59, 45056\n"
    "s_mov_b32 s74, 995584\n"
    "buffer_load_dwordx4 v5, s[40:43], s74 offen lds\n"
    "ds_read_b64_tr_b16 a[192:193], v16 offset:0\n"
    "ds_read_b64_tr_b16 a[194:195], v16 offset:512\n"
    "ds_read_b64_tr_b16 a[196:197], v16 offset:8\n"
    "ds_read_b64_tr_b16 a[198:199], v16 offset:520\n"
    "ds_read_b64_tr_b16 a[200:201], v18 offset:0\n"
    "ds_read_b64_tr_b16 a[202:203], v18 offset:512\n"
    "ds_read_b64_tr_b16 a[204:205], v16 offset:12288\n"
    "ds_read_b64_tr_b16 a[206:207], v16 offset:12800\n"
    "ds_read_b64_tr_b16 a[208:209], v16 offset:12296\n"
    "ds_read_b64_tr_b16 a[210:211], v16 offset:12808\n"
    "ds_read_b64_tr_b16 a[212:213], v18 offset:12288\n"
    "ds_read_b64_tr_b16 a[214:215], v18 offset:12800\n"
    "s_waitcnt vmcnt(24) lgkmcnt(0)\n"
    "s_barrier\n"
    "s_add_u32 m0, s59, 49152\n"
    "s_mov_b32 s74, 1032192\n"
    "buffer_load_dwordx4 v5, s[40:43], s74 offen lds\n"
    "s_add_u32 m0, s59, 53248\n"
    "s_mov_b32 s74, 1032320\n"
    "buffer_load_dwordx4 v5, s[40:43], s74 offen lds\n"
    "s_add_u32 m0, s59, 57344\n"
    "s_mov_b32 s74, 1032448\n"
    "buffer_load_dwordx4 v5, s[40:43], s74 offen lds\n"
    "s_add_u32 m0, s59, 61440\n"
    "s_mov_b32 s74, 1069056\n"
    "buffer_load_dwordx4 v5, s[40:43], s74 offen lds\n"
    "s_add_u32 m0, s59, 65536\n"
    "s_mov_b32 s74, 1069184\n"
    "buffer_load_dwordx4 v5, s[40:43], s74 offen lds\n"
    "s_add_u32 m0, s59, 69632\n"
    "s_mov_b32 s74, 1069312\n"
    "buffer_load_dwordx4 v5, s[40:43], s74 offen lds\n"
    "ds_read_b64_tr_b16 a[216:217], v16 offset:24576\n"
    "ds_read_b64_tr_b16 a[218:219], v16 offset:25088\n"
    "ds_read_b64_tr_b16 a[220:221], v16 offset:24584\n"
    "ds_read_b64_tr_b16 a[222:223], v16 offset:25096\n"
    "ds_read_b64_tr_b16 a[224:225], v18 offset:24576\n"
    "ds_read_b64_tr_b16 a[226:227], v18 offset:25088\n"
    "ds_read_b64_tr_b16 a[228:229], v16 offset:36864\n"
    "ds_read_b64_tr_b16 a[230:231], v16 offset:37376\n"
    "ds_read_b64_tr_b16 a[232:233], v16 offset:36872\n"
    "ds_read_b64_tr_b16 a[234:235], v16 offset:37384\n"
    "ds_read_b64_tr_b16 a[236:237], v18 offset:36864\n"
    "ds_read_b64_tr_b16 a[238:239], v18 offset:37376\n"
    "s_waitcnt vmcnt(24) lgkmcnt(0)\n"
    "s_barrier\n"
    "s_add_u32 m0, s59, 73728\n"
    "s_mov_b32 s74, 1105920\n"
    "buffer_load_dwordx4 v5, s[40:43], s74 offen lds\n"
    "s_add_u32 m0, s59, 77824\n"
    "s_mov_b32 s74, 1106048\n"
    "buffer_load_dwordx4 v5, s[40:43], s74 offen lds\n"
    "s_add_u32 m0, s59, 81920\n"
    "s_mov_b32 s74, 1106176\n"
    "buffer_load_dwordx4 v5, s[40:43], s74 offen lds\n"
    "s_add_u32 m0, s59, 86016\n"
    "s_mov_b32 s74, 1142784\n"
    "buffer_load_dwordx4 v5, s[40:43], s74 offen lds\n"
    "s_add_u32 m0, s59, 90112\n"
    "s_mov_b32 s74, 1142912\n"
    "buffer_load_dwordx4 v5, s[40:43], s74 offen lds\n"
    "s_add_u32 m0, s59, 94208\n"
    "s_mov_b32 s74, 1143040\n"
    "buffer_load_dwordx4 v5, s[40:43], s74 offen lds\n"
    "ds_read_b64_tr_b16 a[240:241], v17 offset:0\n"
    "ds_read_b64_tr_b16 a[242:243], v17 offset:512\n"
    "ds_read_b64_tr_b16 a[244:245], v17 offset:8\n"
    "ds_read_b64_tr_b16 a[246:247], v17 offset:520\n"
    "ds_read_b64_tr_b16 a[248:249], v19 offset:0\n"
    "ds_read_b64_tr_b16 a[250:251], v19 offset:512\n"
    "ds_read_b64_tr_b16 a[252:253], v17 offset:12288\n"
    "ds_read_b64_tr_b16 a[254:255], v17 offset:12800\n"
    "ds_read_b64_tr_b16 v[80:81], v17 offset:12296\n"
    "ds_read_b64_tr_b16 v[82:83], v17 offset:12808\n"
    "ds_read_b64_tr_b16 v[84:85], v19 offset:12288\n"
    "ds_read_b64_tr_b16 v[86:87], v19 offset:12800\n"
    "s_waitcnt vmcnt(24) lgkmcnt(0)\n"
    "s_barrier\n"
    "s_add_u32 m0, s59, 98304\n"
    "s_mov_b32 s74, 1179648\n"
    "buffer_load_dwordx4 v5, s[40:43], s74 offen lds\n"
    "s_add_u32 m0, s59, 102400\n"
    "s_mov_b32 s74, 1179776\n"
    "buffer_load_dwordx4 v5, s[40:43], s74 offen lds\n"
    "s_add_u32 m0, s59, 106496\n"
    "s_mov_b32 s74, 1179904\n"
    "buffer_load_dwordx4 v5, s[40:43], s74 offen lds\n"
    "s_add_u32 m0, s59, 110592\n"
    "s_mov_b32 s74, 1216512\n"
    "buffer_load_dwordx4 v5, s[40:43], s74 offen lds\n"
    "s_add_u32 m0, s59, 114688\n"
    "s_mov_b32 s74, 1216640\n"
    "buffer_load_dwordx4 v5, s[40:43], s74 offen lds\n"
    "s_add_u32 m0, s59, 118784\n"
    "s_mov_b32 s74, 1216768\n"
    "buffer_load_dwordx4 v5, s[40:43], s74 offen lds\n"
    "ds_read_b64_tr_b16 v[88:89], v17 offset:24576\n"
    "ds_read_b64_tr_b16 v[90:91], v17 offset:25088\n"
    "ds_read_b64_tr_b16 v[92:93], v17 offset:24584\n"
    "ds_read_b64_tr_b16 v[94:95], v17 offset:25096\n"
    "ds_read_b64_tr_b16 v[96:97], v19 offset:24576\n"
    "ds_read_b64_tr_b16 v[98:99], v19 offset:25088\n"
    "ds_read_b64_tr_b16 v[100:101], v17 offset:36864\n"
    "ds_read_b64_tr_b16 v[102:103], v17 offset:37376\n"
    "ds_read_b64_tr_b16 v[104:105], v17 offset:36872\n"
    "ds_read_b64_tr_b16 v[106:107], v17 offset:37384\n"
    "ds_read_b64_tr_b16 v[108:109], v19 offset:36864\n"
    "ds_read_b64_tr_b16 v[110:111], v19 offset:37376\n"
    "s_waitcnt vmcnt(24) lgkmcnt(0)\n"
    "s_barrier\n"
    "s_add_u32 m0, s59, 122880\n"
    "s_mov_b32 s74, 1253376\n"
    "buffer_load_dwordx4 v5, s[40:43], s74 offen lds\n"
    "s_add_u32 m0, s59, 126976\n"
    "s_mov_b32 s74, 1253504\n"
    "buffer_load_dwordx4 v5, s[40:43], s74 offen lds\n"
    "s_add_u32 m0, s59, 131072\n"
    "s_mov_b32 s74, 1253632\n"
    "buffer_load_dwordx4 v5, s[40:43], s74 offen lds\n"
    "s_add_u32 m0, s59, 135168\n"
    "s_mov_b32 s74, 1290240\n"
    "buffer_load_dwordx4 v5, s[40:43], s74 offen lds\n"
    "s_add_u32 m0, s59, 139264\n"
    "s_mov_b32 s74, 1290368\n"
    "buffer_load_dwordx4 v5, s[40:43], s74 offen lds\n"
    "s_add_u32 m0, s59, 143360\n"
    "s_mov_b32 s74, 1290496\n"
    "buffer_load_dwordx4 v5, s[40:43], s74 offen lds\n"
    "buffer_load_dwordx2 v[12:13], off, s[36:39], 0 sc1\n"
    "ds_read_b64_tr_b16 v[112:113], v6 offset:0\n"
    "ds_read_b64_tr_b16 v[114:115], v6 offset:512\n"
    "ds_read_b64_tr_b16 v[116:117], v6 offset:8\n"
    "ds_read_b64_tr_b16 v[118:119], v6 offset:520\n"
    "ds_read_b64_tr_b16 v[120:121], v7 offset:0\n"
    "ds_read_b64_tr_b16 v[122:123], v7 offset:512\n"
    "ds_read_b64_tr_b16 v[124:125], v6 offset:12288\n"
    "ds_read_b64_tr_b16 v[126:127], v6 offset:12800\n"
    "ds_read_b64_tr_b16 v[128:129], v6 offset:12296\n"
    "ds_read_b64_tr_b16 v[130:131], v6 offset:12808\n"
    "ds_read_b64_tr_b16 v[132:133], v7 offset:12288\n"
    "ds_read_b64_tr_b16 v[134:135], v7 offset:12800\n"
    "s_waitcnt vmcnt(25) lgkmcnt(0)\n"
    "s_barrier\n"
    "ds_read_b64_tr_b16 v[136:137], v6 offset:24576\n"
    "ds_read_b64_tr_b16 v[138:139], v6 offset:25088\n"
    "ds_read_b64_tr_b16 v[140:141], v6 offset:24584\n"
    "ds_read_b64_tr_b16 v[142:143], v6 offset:25096\n"
    "ds_read_b64_tr_b16 v[144:145], v7 offset:24576\n"
    "ds_read_b64_tr_b16 v[146:147], v7 offset:25088\n"
    "ds_read_b64_tr_b16 v[148:149], v6 offset:36864\n"
    "ds_read_b64_tr_b16 v[150:151], v6 offset:37376\n"
    "ds_read_b64_tr_b16 v[152:153], v6 offset:36872\n"
    "ds_read_b64_tr_b16 v[154:155], v6 offset:37384\n"
    "ds_read_b64_tr_b16 v[156:157], v7 offset:36864\n"
    "ds_read_b64_tr_b16 v[158:159], v7 offset:37376\n"
    "s_waitcnt vmcnt(19) lgkmcnt(0)\n"
    "s_barrier\n"
    "ds_read_b64_tr_b16 v[160:161], v16 offset:0\n"
    "ds_read_b64_tr_b16 v[162:163], v16 offset:512\n"
    "ds_read_b64_tr_b16 v[164:165], v16 offset:8\n"
    "ds_read_b64_tr_b16 v[166:167], v16 offset:520\n"
    "ds_read_b64_tr_b16 v[168:169], v18 offset:0\n"
    "ds_read_b64_tr_b16 v[170:171], v18 offset:512\n"
    "ds_read_b64_tr_b16 v[172:173], v16 offset:12288\n"
    "ds_read_b64_tr_b16 v[174:175], v16 offset:12800\n"
    "ds_read_b64_tr_b16 v[176:177], v16 offset:12296\n"
    "ds_read_b64_tr_b16 v[178:179], v16 offset:12808\n"
    "ds_read_b64_tr_b16 v[180:181], v18 offset:12288\n"
    "ds_read_b64_tr_b16 v[182:183], v18 offset:12800\n"
    "s_waitcnt vmcnt(13) lgkmcnt(0)\n"
    "s_barrier\n"
    "ds_read_b64_tr_b16 v[184:185], v16 offset:24576\n"
    "ds_read_b64_tr_b16 v[186:187], v16 offset:25088\n"
    "ds_read_b64_tr_b16 v[188:189], v16 offset:24584\n"
    "ds_read_b64_tr_b16 v[190:191], v16 offset:25096\n"
    "ds_read_b64_tr_b16 v[192:193], v18 offset:24576\n"
    "ds_read_b64_tr_b16 v[194:195], v18 offset:25088\n"
    "ds_read_b64_tr_b16 v[196:197], v16 offset:36864\n"
    "ds_read_b64_tr_b16 v[198:199], v16 offset:37376\n"
    "ds_read_b64_tr_b16 v[200:201], v16 offset:36872\n"
    "ds_read_b64_tr_b16 v[202:203], v16 offset:37384\n"
    "ds_read_b64_tr_b16 v[204:205], v18 offset:36864\n"
    "ds_read_b64_tr_b16 v[206:207], v18 offset:37376\n"
    "s_waitcnt vmcnt(7) lgkmcnt(0)\n"
    "s_barrier\n"
    "ds_read_b64_tr_b16 v[208:209], v17 offset:0\n"
    "ds_read_b64_tr_b16 v[210:211], v17 offset:512\n"
    "ds_read_b64_tr_b16 v[212:213], v17 offset:8\n"
    "ds_read_b64_tr_b16 v[214:215], v17 offset:520\n"
    "ds_read_b64_tr_b16 v[216:217], v19 offset:0\n"
    "ds_read_b64_tr_b16 v[218:219], v19 offset:512\n"
    "ds_read_b64_tr_b16 v[220:221], v17 offset:12288\n"
    "ds_read_b64_tr_b16 v[222:223], v17 offset:12800\n"
    "ds_read_b64_tr_b16 v[224:225], v17 offset:12296\n"
    "ds_read_b64_tr_b16 v[226:227], v17 offset:12808\n"
    "ds_read_b64_tr_b16 v[228:229], v19 offset:12288\n"
    "ds_read_b64_tr_b16 v[230:231], v19 offset:12800\n"
    "s_waitcnt vmcnt(0) lgkmcnt(0)\n"
    "s_barrier\n"
    ".LgDOWN_par_%=:\n"
    "v_readfirstlane_b32 s74, v12\n"
    "v_readfirstlane_b32 s75, v13\n"
    "s_xor_b32 s74, s74, s66\n"
    "s_xor_b32 s75, s75, s67\n"
    "s_cselect_b32 s75, 2, 0\n"
    "s_or_b32 s75, s75, s74\n"
    "s_cmp_lt_u32 s75, 2\n"
    "s_cbranch_scc1 .LgDOWN_par1_%=\n"
    "s_sleep 4\n"
    "buffer_load_dwordx2 v[12:13], off, s[36:39], 0 sc1\n"
    "s_waitcnt vmcnt(0)\n"
    "s_sub_u32 s72, s72, 1\n"
    "s_cbranch_scc0 .LgDOWN_par_%=\n"
    "s_mov_b32 s72, 0\n"
    ".LgDOWN_par1_%=:\n"
    "s_and_b32 s74, s74, 1\n"
    "s_lshl_b32 s74, s74, 13\n"
    "s_add_u32 s36, s36, s74\n"
    "s_addc_u32 s37, s37, 0\n"
    "v_add_u32 v13, v11, v3\n"
    "v_cmp_gt_i32 vcc, s71, v13\n"
    "s_nop 0\n"
    "s_or_b64 exec, vcc, s[62:63]\n"
    "buffer_atomic_smax v11, v2, s[36:39], 0 offen sc0 sc1\n"
    "s_mov_b64 exec, -1\n"
    "s_getpc_b64 s[84:85]\n"
    ".LgDOWN_PC2_%=:\n"
    "s_and_b32 s85, s85, 0xffff\n"
    "s_mov_b32 s86, .LgDOWN_FIN_%=-.LgDOWN_PC2_%=\n"
    "s_mov_b32 s87, 0x20000\n"
    "v_mbcnt_lo_u32_b32 v42, -1, 0\n"
    "v_mbcnt_hi_u32_b32 v42, -1, v42\n"
    "v_lshlrev_b32 v42, 7, v42\n"
    "buffer_load_dword v43, v42, s[84:87], 0 offen\n"
    "v_add_u32 v42, 8192, v42\n"
    "buffer_load_dword v43, v42, s[84:87], 0 offen\n"
    "v_add_u32 v42, 8192, v42\n"
    "buffer_load_dword v43, v42, s[84:87], 0 offen\n"
    "ds_read_b64_tr_b16 v[232:233], v17 offset:24576\n"
    "ds_read_b64_tr_b16 v[234:235], v17 offset:25088\n"
    "ds_read_b64_tr_b16 v[236:237], v17 offset:24584\n"
    "ds_read_b64_tr_b16 v[238:239], v17 offset:25096\n"
    "ds_read_b64_tr_b16 v[240:241], v19 offset:24576\n"
    "ds_read_b64_tr_b16 v[242:243], v19 offset:25088\n"
    "ds_read_b64_tr_b16 v[244:245], v17 offset:36864\n"
    "ds_read_b64_tr_b16 v[246:247], v17 offset:37376\n"
    "ds_read_b64_tr_b16 v[248:249], v17 offset:36872\n"
    "ds_read_b64_tr_b16 v[250:251], v17 offset:37384\n"
    "ds_read_b64_tr_b16 v[252:253], v19 offset:36864\n"
    "ds_read_b64_tr_b16 v[254:255], v19 offset:37376\n"
    "s_waitcnt lgkmcnt(0)\n"
    "s_barrier\n"
    "s_waitcnt vmcnt(0)\n"
    "v_mov_b32 v5, v11\n"
    "v_mov_b32 v12, -1\n"
    "s_mov_b32 s73, 0x80000000\n"
    "s_branch .LgDOWN_block1_1_%=\n"
    ".LgDOWN_LOOPB_%=:\n"
    ".LgDOWN_head0_%=:\n"
    "v_mfma_f32_16x16x32_bf16 v[48:51], a[0:3], v[16:19], 0\n"
    "v_mfma_f32_16x16x32_bf16 v[52:55], a[4:7], v[16:19], 0\n"
    "s_waitcnt lgkmcnt(2)\n"
    "v_cvt_pk_bf16_f32 v56, v56, v57\n"
    "v_cvt_pk_bf16_f32 v57, v58, v59\n"
    "v_mfma_f32_16x16x32_bf16 v[64:67], a[8:11], v[16:19], 0\n"
    "ds_read_b128 v[16:19], v0 offset:4096\n"
    "v_cvt_pk_bf16_f32 v58, v60, v61\n"
    "v_mfma_f32_16x16x32_bf16 v[48:51], a[12:15], v[20:23], v[48:51]\n"
    "v_cvt_pk_bf16_f32 v59, v62, v63\n"
    "v_mfma_f32_16x16x32_bf16 v[52:55], a[16:19], v[20:23], v[52:55]\n"
    "s_waitcnt lgkmcnt(2)\n"
    "v_cvt_pk_bf16_f32 v68, v68, v69\n"
    "v_cvt_pk_bf16_f32 v69, v70, v71\n"
    "v_mfma_f32_16x16x32_bf16 v[64:67], a[20:23], v[20:23], v[64:67]\n"
    "ds_read_b128 v[20:23], v0 offset:5120\n"
    "s_nop 0\n"
    "v_mfma_f32_16x16x32_bf16 v[48:51], a[24:27], v[24:27], v[48:51]\n"
    "buffer_store_dwordx4 v[56:59], v15, s[36:39], s73 offen offset:1024 sc1\n"
    "v_mfma_f32_16x16x32_bf16 v[52:55], a[28:31], v[24:27], v[52:55]\n"
    "s_waitcnt lgkmcnt(2)\n"
    "s_waitcnt vmcnt(2)\n"
    "v_add_u32 v13, v11, v3\n"
    "v_mfma_f32_16x16x32_bf16 v[64:67], a[32:35], v[24:27], v[64:67]\n"
    "ds_read_b128 v[24:27], v0 offset:6144\n"
    "v_cmp_gt_i32 vcc, s71, v13\n"
    "v_mfma_f32_16x16x32_bf16 v[48:51], a[36:39], v[28:31], v[48:51]\n"
    "buffer_atomic_smax v7, v10, s[36:39], s69 offen sc1\n"
    "v_permlane16_swap_b32 v68, v70\n"
    "v_mfma_f32_16x16x32_bf16 v[52:55], a[40:43], v[28:31], v[52:55]\n"
    "s_waitcnt lgkmcnt(2)\n"
    "v_permlane16_swap_b32 v69, v71\n"
    "v_mfma_f32_16x16x32_bf16 v[64:67], a[44:47], v[28:31], v[64:67]\n"
    "ds_read_b128 v[28:31], v0 offset:7168\n"
    "s_cbranch_vccnz .LgDOWN_poll0_%=\n"
    "v_mfma_f32_16x16x32_bf16 v[48:51], a[48:51], v[16:19], v[48:51]\n"
    "s_add_u32 m0, s59, 36864\n"
    "v_mfma_f32_16x16x32_bf16 v[52:55], a[52:55], v[16:19], v[52:55]\n"
    "s_waitcnt lgkmcnt(2)\n"
    "buffer_load_dwordx4 v1, s[36:39], s76 offen sc1 lds\n"
    "v_mfma_f32_16x16x32_bf16 v[64:67], a[56:59], v[16:19], v[64:67]\n"
    "ds_read_b128 v[16:19], v0 offset:8192\n"
    "v_mfma_f32_16x16x32_bf16 v[48:51], a[60:63], v[20:23], v[48:51]\n"
    "buffer_store_dwordx4 v[68:71], v4, s[36:39], s73 offen offset:1024 sc1\n"
    "v_mfma_f32_16x16x32_bf16 v[52:55], a[64:67], v[20:23], v[52:55]\n"
    "s_waitcnt lgkmcnt(2)\n"
    "s_add_u32 s76, s76, 16384\n"
    "s_lshr_b32 s73, s68, 1\n"
    "v_mfma_f32_16x16x32_bf16 v[64:67], a[68:71], v[20:23], v[64:67]\n"
    "ds_read_b128 v[20:23], v0 offset:9216\n"
    "s_and_b32 s73, s73, s57\n"
    "v_mfma_f32_16x16x32_bf16 v[48:51], a[72:75], v[24:27], v[48:51]\n"
    "s_add_u32 m0, s59, 40960\n"
    "s_mul_i32 s73, s73, s56\n"
    "v_mfma_f32_16x16x32_bf16 v[52:55], a[76:79], v[24:27], v[52:55]\n"
    "s_waitcnt lgkmcnt(2)\n"
    "buffer_load_dwordx4 v1, s[36:39], s76 offen sc1 lds\n"
    "s_add_u32 s73, s73, s55\n"
    "v_mfma_f32_16x16x32_bf16 v[64:67], a[80:83], v[24:27], v[64:67]\n"
    "ds_read_b128 v[24:27], v0 offset:10240\n"
    "v_mfma_f32_16x16x32_bf16 v[48:51], a[84:87], v[28:31], v[48:51]\n"
    "v_mfma_f32_16x16x32_bf16 v[52:55], a[88:91], v[28:31], v[52:55]\n"
    "s_waitcnt lgkmcnt(2)\n"
    "s_add_u32 s76, s76, 16384\n"
    "s_add_u32 m0, s59, 45056\n"
    "v_mfma_f32_16x16x32_bf16 v[64:67], a[92:95], v[28:31], v[64:67]\n"
    "ds_read_b128 v[28:31], v0 offset:11264\n"
    "buffer_load_dwordx4 v1, s[36:39], s76 offen sc1 lds\n"
    "v_mfma_f32_16x16x32_bf16 v[48:51], a[96:99], v[16:19], v[48:51]\n"
    "v_mfma_f32_16x16x32_bf16 v[52:55], a[100:103], v[16:19], v[52:55]\n"
    "s_waitcnt lgkmcnt(2)\n"
    "v_mfma_f32_16x16x32_bf16 v[64:67], a[104:107], v[16:19], v[64:67]\n"
    "ds_read_b128 v[16:19], v0 offset:12288\n"
    "v_mfma_f32_16x16x32_bf16 v[48:51], a[108:111], v[20:23], v[48:51]\n"
    "v_mfma_f32_16x16x32_bf16 v[52:55], a[112:115], v[20:23], v[52:55]\n"
    "s_waitcnt lgkmcnt(2)\n"
    "v_mfma_f32_16x16x32_bf16 v[64:67], a[116:119], v[20:23], v[64:67]\n"
    "ds_read_b128 v[20:23], v0 offset:13312\n"
    "s_add_u32 s76, s76, 16384\n"
    "v_mfma_f32_16x16x32_bf16 v[48:51], a[120:123], v[24:27], v[48:51]\n"
    "s_add_u32 m0, s59, 49152\n"
    "v_mfma_f32_16x16x32_bf16 v[52:55], a[124:127], v[24:27], v[52:55]\n"
    "s_waitcnt lgkmcnt(2)\n"
    "buffer_load_dwordx4 v1, s[36:39], s76 offen sc1 lds\n"
    "v_mfma_f32_16x16x32_bf16 v[64:67], a[128:131], v[24:27], v[64:67]\n"
    "ds_read_b128 v[24:27], v0 offset:14336\n"
    "v_mfma_f32_16x16x32_bf16 v[48:51], a[132:135], v[28:31], v[48:51]\n"
    "v_mfma_f32_16x16x32_bf16 v[52:55], a[136:139], v[28:31], v[52:55]\n"
    "s_waitcnt lgkmcnt(2)\n"
    "v_mfma_f32_16x16x32_bf16 v[64:67], a[140:143], v[28:31], v[64:67]\n"
    "ds_read_b128 v[28:31], v0 offset:15360\n"
    "s_add_u32 s76, s76, 16384\n"
    "v_mfma_f32_16x16x32_bf16 v[48:51], a[144:147], v[16:19], v[48:51]\n"
    "s_add_u32 m0, s59, 53248\n"
    "v_mfma_f32_16x16x32_bf16 v[52:55], a[148:151], v[16:19], v[52:55]\n"
    "s_waitcnt lgkmcnt(2)\n"
    "buffer_load_dwordx4 v1, s[36:39], s76 offen sc1 lds\n"
    "v_mfma_f32_16x16x32_bf16 v[64:67], a[152:155], v[16:19], v[64:67]\n"
    "ds_read_b128 v[16:19], v0 offset:16384\n"
    "v_mfma_f32_16x16x32_bf16 v[48:51], a[156:159], v[20:23], v[48:51]\n"
    "v_mfma_f32_16x16x32_bf16 v[52:55], a[160:163], v[20:23], v[52:55]\n"
    "s_waitcnt lgkmcnt(2)\n"
    "v_mfma_f32_16x16x32_bf16 v[64:67], a[164:167], v[20:23], v[64:67]\n"
    "ds_read_b128 v[20:23], v0 offset:17408\n"
    "s_add_u32 s76, s76, 16384\n"
    "v_mfma_f32_16x16x32_bf16 v[48:51], a[168:171], v[24:27], v[48:51]\n"
    "s_add_u32 m0, s59, 57344\n"
    "v_mfma_f32_16x16x32_bf16 v[52:55], a[172:175], v[24:27], v[52:55]\n"
    "s_waitcnt lgkmcnt(2)\n"
    "buffer_load_dwordx4 v1, s[36:39], s76 offen sc1 lds\n"
    "v_mfma_f32_16x16x32_bf16 v[64:67], a[176:179], v[24:27], v[64:67]\n"
    "ds_read_b128 v[24:27], v0 offset:18432\n"
    "v_mfma_f32_16x16x32_bf16 v[48:51], a[180:183], v[28:31], v[48:51]\n"
    "v_mfma_f32_16x16x32_bf16 v[52:55], a[184:187], v[28:31], v[52:55]\n"
    "s_waitcnt lgkmcnt(2)\n"
    "v_mfma_f32_16x16x32_bf16 v[64:67], a[188:191], v[28:31], v[64:67]\n"
    "ds_read_b128 v[28:31], v0 offset:19456\n"
    "v_mfma_f32_16x16x32_bf16 v[48:51], a[192:195], v[16:19], v[48:51]\n"
    "s_add_u32 s76, s76, 16384\n"
    "s_add_u32 m0, s59, 61440\n"
    "v_mfma_f32_16x16x32_bf16 v[52:55], a[196:199], v[16:19], v[52:55]\n"
    "s_waitcnt lgkmcnt(2)\n"
    "buffer_load_dwordx4 v1, s[36:39], s76 offen sc1 lds\n"
    "v_mfma_f32_16x16x32_bf16 v[64:67], a[200:203], v[16:19], v[64:67]\n"
    "ds_read_b128 v[16:19], v0 offset:20480\n"
    "v_mfma_f32_16x16x32_bf16 v[48:51], a[204:207], v[20:23], v[48:51]\n"
    "v_mfma_f32_16x16x32_bf16 v[52:55], a[208:211], v[20:23], v[52:55]\n"
    "s_waitcnt lgkmcnt(2)\n"
    "v_mfma_f32_16x16x32_bf16 v[64:67], a[212:215], v[20:23], v[64:67]\n"
    "ds_read_b128 v[20:23], v0 offset:21504\n"
    "v_mfma_f32_16x16x32_bf16 v[48:51], a[216:219], v[24:27], v[48:51]\n"
    "s_add_u32 s76, s76, 16384\n"
    "s_add_u32 m0, s59, 65536\n"
    "v_mfma_f32_16x16x32_bf16 v[52:55], a[220:223], v[24:27], v[52:55]\n"
    "s_waitcnt lgkmcnt(2)\n"
    "s_waitcnt vmcnt(6)\n"
    "buffer_atomic_smax v12, v10, s[36:39], s70 offen sc1\n"
    "v_mfma_f32_16x16x32_bf16 v[64:67], a[224:227], v[24:27], v[64:67]\n"
    "ds_read_b128 v[24:27], v0 offset:22528\n"
    "buffer_load_dwordx4 v1, s[36:39], s76 offen sc1 lds\n"
    "v_mfma_f32_16x16x32_bf16 v[48:51], a[228:231], v[28:31], v[48:51]\n"
    "v_mfma_f32_16x16x32_bf16 v[52:55], a[232:235], v[28:31], v[52:55]\n"
    "s_waitcnt lgkmcnt(2)\n"
    "v_mfma_f32_16x16x32_bf16 v[64:67], a[236:239], v[28:31], v[64:67]\n"
    "ds_read_b128 v[28:31], v0 offset:23552\n"
    "v_mfma_f32_16x16x32_bf16 v[48:51], a[240:243], v[16:19], v[48:51]\n"
    "s_add_u32 s76, s76, 16384\n"
    "s_add_u32 m0, s59, 69632\n"
    "v_mfma_f32_16x16x32_bf16 v[52:55], a[244:247], v[16:19], v[52:55]\n"
    "s_waitcnt lgkmcnt(2)\n"
    "buffer_load_dwordx4 v1, s[36:39], s76 offen sc1 lds\n"
    "v_mfma_f32_16x16x32_bf16 v[64:67], a[248:251], v[16:19], v[64:67]\n"
    "ds_read_b128 v[16:19], v0 offset:24576\n"
    "v_mfma_f32_16x16x32_bf16 v[48:51], a[252:255], v[20:23], v[48:51]\n"
    "v_mfma_f32_16x16x32_bf16 v[52:55], v[80:83], v[20:23], v[52:55]\n"
    "s_waitcnt lgkmcnt(2)\n"
    "v_mfma_f32_16x16x32_bf16 v[64:67], v[84:87], v[20:23], v[64:67]\n"
    "ds_read_b128 v[20:23], v0 offset:25600\n"
    "v_mfma_f32_16x16x32_bf16 v[48:51], v[88:91], v[24:27], v[48:51]\n"
    "v_mfma_f32_16x16x32_bf16 v[52:55], v[92:95], v[24:27], v[52:55]\n"
    "s_waitcnt lgkmcnt(2)\n"
    "v_mfma_f32_16x16x32_bf16 v[64:67], v[96:99], v[24:27], v[64:67]\n"
    "ds_read_b128 v[24:27], v0 offset:26624\n"
    "v_mfma_f32_16x16x32_bf16 v[48:51], v[100:103], v[28:31], v[48:51]\n"
    "v_mfma_f32_16x16x32_bf16 v[52:55], v[104:107], v[28:31], v[52:55]\n"
    "s_waitcnt lgkmcnt(2)\n"
    "v_mfma_f32_16x16x32_bf16 v[64:67], v[108:111], v[28:31], v[64:67]\n"
    "ds_read_b128 v[28:31], v0 offset:27648\n"
    ".LgDOWN_join0_%=:\n"
    "v_mfma_f32_16x16x32_bf16 v[48:51], v[112:115], v[16:19], v[48:51]\n"
    "s_add_u32 s71, s68, 2\n"
    "s_waitcnt vmcnt(9)\n"
    "v_mfma_f32_16x16x32_bf16 v[52:55], v[116:119], v[16:19], v[52:55]\n"
    "s_waitcnt lgkmcnt(2)\n"
    "v_add_u32 v13, v11, v3\n"
    "v_mfma_f32_16x16x32_bf16 v[64:67], v[120:123], v[16:19], v[64:67]\n"
    "ds_read_b128 v[16:19], v0 offset:28672\n"
    "v_cmp_gt_i32 vcc, s71, v13\n"
    "v_mfma_f32_16x16x32_bf16 v[48:51], v[124:127], v[20:23], v[48:51]\n"
    "v_mfma_f32_16x16x32_bf16 v[52:55], v[128:131], v[20:23], v[52:55]\n"
    "s_waitcnt lgkmcnt(2)\n"
    "v_mfma_f32_16x16x32_bf16 v[64:67], v[132:135], v[20:23], v[64:67]\n"
    "ds_read_b128 v[20:23], v0 offset:29696\n"
    "v_mfma_f32_16x16x32_bf16 v[48:51], v[136:139], v[24:27], v[48:51]\n"
    "s_or_b64 exec, vcc, s[62:63]\n"
    "buffer_atomic_smax v11, v2, s[36:39], 0 offen sc0 sc1\n"
    "s_mov_b64 exec, -1\n"
    "v_mfma_f32_16x16x32_bf16 v[52:55], v[140:143], v[24:27], v[52:55]\n"
    "s_waitcnt lgkmcnt(2)\n"
    "s_lshr_b32 s76, s71, 1\n"
    "v_mfma_f32_16x16x32_bf16 v[64:67], v[144:147], v[24:27], v[64:67]\n"
    "ds_read_b128 v[24:27], v0 offset:30720\n"
    "s_and_b32 s76, s76, s54\n"
    "v_mfma_f32_16x16x32_bf16 v[48:51], v[148:151], v[28:31], v[48:51]\n"
    "s_mul_i32 s76, s76, s53\n"
    "v_mfma_f32_16x16x32_bf16 v[52:55], v[152:155], v[28:31], v[52:55]\n"
    "s_waitcnt lgkmcnt(2)\n"
    "s_add_u32 s76, s76, s52\n"
    "v_mfma_f32_16x16x32_bf16 v[64:67], v[156:159], v[28:31], v[64:67]\n"
    "ds_read_b128 v[28:31], v0 offset:31744\n"
    "v_mfma_f32_16x16x32_bf16 v[48:51], v[160:163], v[16:19], v[48:51]\n"
    "v_mfma_f32_16x16x32_bf16 v[52:55], v[164:167], v[16:19], v[52:55]\n"
    "s_waitcnt lgkmcnt(2)\n"
    "v_mfma_f32_16x16x32_bf16 v[64:67], v[168:171], v[16:19], v[64:67]\n"
    "ds_read_b128 v[16:19], v0 offset:32768\n"
    "v_mfma_f32_16x16x32_bf16 v[48:51], v[172:175], v[20:23], v[48:51]\n"
    "v_mfma_f32_16x16x32_bf16 v[52:55], v[176:179], v[20:23], v[52:55]\n"
    "s_waitcnt lgkmcnt(2)\n"
    "v_mfma_f32_16x16x32_bf16 v[64:67], v[180:183], v[20:23], v[64:67]\n"
    "ds_read_b128 v[20:23], v0 offset:33792\n"
    "v_mfma_f32_16x16x32_bf16 v[48:51], v[184:187], v[24:27], v[48:51]\n"
    "v_mfma_f32_16x16x32_bf16 v[52:55], v[188:191], v[24:27], v[52:55]\n"
    "s_waitcnt lgkmcnt(2)\n"
    "v_mfma_f32_16x16x32_bf16 v[64:67], v[192:195], v[24:27], v[64:67]\n"
    "ds_read_b128 v[24:27], v0 offset:34816\n"
    "v_mfma_f32_16x16x32_bf16 v[48:51], v[196:199], v[28:31], v[48:51]\n"
    "v_mfma_f32_16x16x32_bf16 v[52:55], v[200:203], v[28:31], v[52:55]\n"
    "s_waitcnt lgkmcnt(2)\n"
    "v_add_u32 v13, v5, v3\n"
    "v_mfma_f32_16x16x32_bf16 v[64:67], v[204:207], v[28:31], v[64:67]\n"
    "ds_read_b128 v[28:31], v0 offset:35840\n"
    "v_cmp_gt_i32 vcc, s71, v13\n"
    "v_mfma_f32_16x16x32_bf16 v[48:51], v[208:211], v[16:19], v[48:51]\n"
    "v_mfma_f32_16x16x32_bf16 v[52:55], v[212:215], v[16:19], v[52:55]\n"
    "s_waitcnt lgkmcnt(2)\n"
    "v_mfma_f32_16x16x32_bf16 v[64:67], v[216:219], v[16:19], v[64:67]\n"
    "v_mfma_f32_16x16x32_bf16 v[48:51], v[220:223], v[20:23], v[48:51]\n"
    "s_waitcnt vmcnt(1) lgkmcnt(0)\n"
    "s_barrier\n"
    "ds_read_b128 v[16:19], v9 offset:0\n"
    "s_or_b64 exec, vcc, s[62:63]\n"
    "buffer_atomic_smax v5, v2, s[36:39], 0 offen sc0 sc1\n"
    "s_mov_b64 exec, -1\n"
    "v_mfma_f32_16x16x32_bf16 v[52:55], v[224:227], v[20:23], v[52:55]\n"
    "v_mfma_f32_16x16x32_bf16 v[64:67], v[228:231], v[20:23], v[64:67]\n"
    "ds_read_b128 v[20:23], v9 offset:1024\n"
    "v_mfma_f32_16x16x32_bf16 v[48:51], v[232:235], v[24:27], v[48:51]\n"
    "v_mfma_f32_16x16x32_bf16 v[52:55], v[236:239], v[24:27], v[52:55]\n"
    "v_mfma_f32_16x16x32_bf16 v[64:67], v[240:243], v[24:27], v[64:67]\n"
    "ds_read_b128 v[24:27], v9 offset:2048\n"
    "v_mfma_f32_16x16x32_bf16 v[48:51], v[244:247], v[28:31], v[48:51]\n"
    "s_mov_b32 s69, 0x80000000\n"
    "s_mov_b32 s70, 0\n"
    "v_mov_b32 v12, s68\n"
    "v_mfma_f32_16x16x32_bf16 v[52:55], v[248:251], v[28:31], v[52:55]\n"
    "s_waitcnt lgkmcnt(2)\n"
    "s_add_u32 s68, s68, 1\n"
    "v_mfma_f32_16x16x32_bf16 v[64:67], v[252:255], v[28:31], v[64:67]\n"
    "ds_read_b128 v[28:31], v9 offset:3072\n"
    ".LgDOWN_head1_%=:\n"
    "v_mfma_f32_16x16x32_bf16 v[56:59], a[0:3], v[16:19], 0\n"
    "v_mfma_f32_16x16x32_bf16 v[60:63], a[4:7], v[16:19], 0\n"
    "s_waitcnt lgkmcnt(2)\n"
    "v_cvt_pk_bf16_f32 v48, v48, v49\n"
    "v_cvt_pk_bf16_f32 v49, v50, v51\n"
    "v_mfma_f32_16x16x32_bf16 v[68:71], a[8:11], v[16:19], 0\n"
    "ds_read_b128 v[16:19], v9 offset:4096\n"
    "v_cvt_pk_bf16_f32 v50, v52, v53\n"
    "v_mfma_f32_16x16x32_bf16 v[56:59], a[12:15], v[20:23], v[56:59]\n"
    "v_cvt_pk_bf16_f32 v51, v54, v55\n"
    "v_mfma_f32_16x16x32_bf16 v[60:63], a[16:19], v[20:23], v[60:63]\n"
    "s_waitcnt lgkmcnt(2)\n"
    "v_cvt_pk_bf16_f32 v64, v64, v65\n"
    "v_cvt_pk_bf16_f32 v65, v66, v67\n"
    "v_mfma_f32_16x16x32_bf16 v[68:71], a[20:23], v[20:23], v[68:71]\n"
    "ds_read_b128 v[20:23], v9 offset:5120\n"
    "s_nop 0\n"
    "v_mfma_f32_16x16x32_bf16 v[56:59], a[24:27], v[24:27], v[56:59]\n"
    "buffer_store_dwordx4 v[48:51], v15, s[36:39], s73 offen sc1\n"
    "v_mfma_f32_16x16x32_bf16 v[60:63], a[28:31], v[24:27], v[60:63]\n"
    "s_waitcnt lgkmcnt(2)\n"
    "s_waitcnt vmcnt(2)\n"
    "v_add_u32 v13, v11, v3\n"
    "v_mfma_f32_16x16x32_bf16 v[68:71], a[32:35], v[24:27], v[68:71]\n"
    "ds_read_b128 v[24:27], v9 offset:6144\n"
    "v_cmp_gt_i32 vcc, s71, v13\n"
    "v_mfma_f32_16x16x32_bf16 v[56:59], a[36:39], v[28:31], v[56:59]\n"
    "buffer_atomic_smax v7, v14, s[36:39], s69 offen sc1\n"
    "v_permlane16_swap_b32 v64, v66\n"
    "v_mfma_f32_16x16x32_bf16 v[60:63], a[40:43], v[28:31], v[60:63]\n"
    "s_waitcnt lgkmcnt(2)\n"
    "v_permlane16_swap_b32 v65, v67\n"
    "v_mfma_f32_16x16x32_bf16 v[68:71], a[44:47], v[28:31], v[68:71]\n"
    "ds_read_b128 v[28:31], v9 offset:7168\n"
    "s_cbranch_vccnz .LgDOWN_poll1_%=\n"
    "v_mfma_f32_16x16x32_bf16 v[56:59], a[48:51], v[16:19], v[56:59]\n"
    "s_add_u32 m0, s59, 0\n"
    "v_mfma_f32_16x16x32_bf16 v[60:63], a[52:55], v[16:19], v[60:63]\n"
    "s_waitcnt lgkmcnt(2)\n"
    "buffer_load_dwordx4 v1, s[36:39], s76 offen sc1 lds\n"
    "v_mfma_f32_16x16x32_bf16 v[68:71], a[56:59], v[16:19], v[68:71]\n"
    "ds_read_b128 v[16:19], v9 offset:8192\n"
    "v_mfma_f32_16x16x32_bf16 v[56:59], a[60:63], v[20:23], v[56:59]\n"
    "buffer_store_dwordx4 v[64:67], v4, s[36:39], s73 offen sc1\n"
    "v_mfma_f32_16x16x32_bf16 v[60:63], a[64:67], v[20:23], v[60:63]\n"
    "s_waitcnt lgkmcnt(2)\n"
    "s_add_u32 s76, s76, 16384\n"
    "s_lshr_b32 s73, s68, 1\n"
    "v_mfma_f32_16x16x32_bf16 v[68:71], a[68:71], v[20:23], v[68:71]\n"
    "ds_read_b128 v[20:23], v9 offset:9216\n"
    "s_and_b32 s73, s73, s57\n"
    "v_mfma_f32_16x16x32_bf16 v[56:59], a[72:75], v[24:27], v[56:59]\n"
    "s_add_u32 m0, s59, 4096\n"
    "s_mul_i32 s73, s73, s56\n"
    "v_mfma_f32_16x16x32_bf16 v[60:63], a[76:79], v[24:27], v[60:63]\n"
    "s_waitcnt lgkmcnt(2)\n"
    "buffer_load_dwordx4 v1, s[36:39], s76 offen sc1 lds\n"
    "s_add_u32 s73, s73, s55\n"
    "v_mfma_f32_16x16x32_bf16 v[68:71], a[80:83], v[24:27], v[68:71]\n"
    "ds_read_b128 v[24:27], v9 offset:10240\n"
    "v_mfma_f32_16x16x32_bf16 v[56:59], a[84:87], v[28:31], v[56:59]\n"
    "v_mfma_f32_16x16x32_bf16 v[60:63], a[88:91], v[28:31], v[60:63]\n"
    "s_waitcnt lgkmcnt(2)\n"
    "s_add_u32 s76, s76, 16384\n"
    "s_add_u32 m0, s59, 8192\n"
    "v_mfma_f32_16x16x32_bf16 v[68:71], a[92:95], v[28:31], v[68:71]\n"
    "ds_read_b128 v[28:31], v9 offset:11264\n"
    "buffer_load_dwordx4 v1, s[36:39], s76 offen sc1 lds\n"
    "v_mfma_f32_16x16x32_bf16 v[56:59], a[96:99], v[16:19], v[56:59]\n"
    "v_mfma_f32_16x16x32_bf16 v[60:63], a[100:103], v[16:19], v[60:63]\n"
    "s_waitcnt lgkmcnt(2)\n"
    "v_mfma_f32_16x16x32_bf16 v[68:71], a[104:107], v[16:19], v[68:71]\n"
    "ds_read_b128 v[16:19], v9 offset:12288\n"
    "v_mfma_f32_16x16x32_bf16 v[56:59], a[108:111], v[20:23], v[56:59]\n"
    "v_mfma_f32_16x16x32_bf16 v[60:63], a[112:115], v[20:23], v[60:63]\n"
    "s_waitcnt lgkmcnt(2)\n"
    "v_mfma_f32_16x16x32_bf16 v[68:71], a[116:119], v[20:23], v[68:71]\n"
    "ds_read_b128 v[20:23], v9 offset:13312\n"
    "s_add_u32 s76, s76, 16384\n"
    "v_mfma_f32_16x16x32_bf16 v[56:59], a[120:123], v[24:27], v[56:59]\n"
    "s_add_u32 m0, s59, 12288\n"
    "v_mfma_f32_16x16x32_bf16 v[60:63], a[124:127], v[24:27], v[60:63]\n"
    "s_waitcnt lgkmcnt(2)\n"
    "buffer_load_dwordx4 v1, s[36:39], s76 offen sc1 lds\n"
    "v_mfma_f32_16x16x32_bf16 v[68:71], a[128:131], v[24:27], v[68:71]\n"
    "ds_read_b128 v[24:27], v9 offset:14336\n"
    "v_mfma_f32_16x16x32_bf16 v[56:59], a[132:135], v[28:31], v[56:59]\n"
    "v_mfma_f32_16x16x32_bf16 v[60:63], a[136:139], v[28:31], v[60:63]\n"
    "s_waitcnt lgkmcnt(2)\n"
    "v_mfma_f32_16x16x32_bf16 v[68:71], a[140:143], v[28:31], v[68:71]\n"
    "ds_read_b128 v[28:31], v9 offset:15360\n"
    "s_add_u32 s76, s76, 16384\n"
    "v_mfma_f32_16x16x32_bf16 v[56:59], a[144:147], v[16:19], v[56:59]\n"
    "s_add_u32 m0, s59, 16384\n"
    "v_mfma_f32_16x16x32_bf16 v[60:63], a[148:151], v[16:19], v[60:63]\n"
    "s_waitcnt lgkmcnt(2)\n"
    "buffer_load_dwordx4 v1, s[36:39], s76 offen sc1 lds\n"
    "v_mfma_f32_16x16x32_bf16 v[68:71], a[152:155], v[16:19], v[68:71]\n"
    "ds_read_b128 v[16:19], v9 offset:16384\n"
    "v_mfma_f32_16x16x32_bf16 v[56:59], a[156:159], v[20:23], v[56:59]\n"
    "v_mfma_f32_16x16x32_bf16 v[60:63], a[160:163], v[20:23], v[60:63]\n"
    "s_waitcnt lgkmcnt(2)\n"
    "v_mfma_f32_16x16x32_bf16 v[68:71], a[164:167], v[20:23], v[68:71]\n"
    "ds_read_b128 v[20:23], v9 offset:17408\n"
    "s_add_u32 s76, s76, 16384\n"
    "v_mfma_f32_16x16x32_bf16 v[56:59], a[168:171], v[24:27], v[56:59]\n"
    "s_add_u32 m0, s59, 20480\n"
    "v_mfma_f32_16x16x32_bf16 v[60:63], a[172:175], v[24:27], v[60:63]\n"
    "s_waitcnt lgkmcnt(2)\n"
    "buffer_load_dwordx4 v1, s[36:39], s76 offen sc1 lds\n"
    "v_mfma_f32_16x16x32_bf16 v[68:71], a[176:179], v[24:27], v[68:71]\n"
    "ds_read_b128 v[24:27], v9 offset:18432\n"
    "v_mfma_f32_16x16x32_bf16 v[56:59], a[180:183], v[28:31], v[56:59]\n"
    "v_mfma_f32_16x16x32_bf16 v[60:63], a[184:187], v[28:31], v[60:63]\n"
    "s_waitcnt lgkmcnt(2)\n"
    "v_mfma_f32_16x16x32_bf16 v[68:71], a[188:191], v[28:31], v[68:71]\n"
    "ds_read_b128 v[28:31], v9 offset:19456\n"
    "v_mfma_f32_16x16x32_bf16 v[56:59], a[192:195], v[16:19], v[56:59]\n"
    "s_add_u32 s76, s76, 16384\n"
    "s_add_u32 m0, s59, 24576\n"
    "v_mfma_f32_16x16x32_bf16 v[60:63], a[196:199], v[16:19], v[60:63]\n"
    "s_waitcnt lgkmcnt(2)\n"
    "buffer_load_dwordx4 v1, s[36:39], s76 offen sc1 lds\n"
    "v_mfma_f32_16x16x32_bf16 v[68:71], a[200:203], v[16:19], v[68:71]\n"
    "ds_read_b128 v[16:19], v9 offset:20480\n"
    "v_mfma_f32_16x16x32_bf16 v[56:59], a[204:207], v[20:23], v[56:59]\n"
    "v_mfma_f32_16x16x32_bf16 v[60:63], a[208:211], v[20:23], v[60:63]\n"
    "s_waitcnt lgkmcnt(2)\n"
    "v_mfma_f32_16x16x32_bf16 v[68:71], a[212:215], v[20:23], v[68:71]\n"
    "ds_read_b128 v[20:23], v9 offset:21504\n"
    "v_mfma_f32_16x16x32_bf16 v[56:59], a[216:219], v[24:27], v[56:59]\n"
    "s_add_u32 s76, s76, 16384\n"
    "s_add_u32 m0, s59, 28672\n"
    "v_mfma_f32_16x16x32_bf16 v[60:63], a[220:223], v[24:27], v[60:63]\n"
    "s_waitcnt lgkmcnt(2)\n"
    "s_waitcnt vmcnt(6)\n"
    "buffer_atomic_smax v12, v14, s[36:39], s70 offen sc1\n"
    "v_mfma_f32_16x16x32_bf16 v[68:71], a[224:227], v[24:27], v[68:71]\n"
    "ds_read_b128 v[24:27], v9 offset:22528\n"
    "buffer_load_dwordx4 v1, s[36:39], s76 offen sc1 lds\n"
    "v_mfma_f32_16x16x32_bf16 v[56:59], a[228:231], v[28:31], v[56:59]\n"
    "v_mfma_f32_16x16x32_bf16 v[60:63], a[232:235], v[28:31], v[60:63]\n"
    "s_waitcnt lgkmcnt(2)\n"
    "v_mfma_f32_16x16x32_bf16 v[68:71], a[236:239], v[28:31], v[68:71]\n"
    "ds_read_b128 v[28:31], v9 offset:23552\n"
    "v_mfma_f32_16x16x32_bf16 v[56:59], a[240:243], v[16:19], v[56:59]\n"
    "s_add_u32 s76, s76, 16384\n"
    "s_add_u32 m0, s59, 32768\n"
    "v_mfma_f32_16x16x32_bf16 v[60:63], a[244:247], v[16:19], v[60:63]\n"
    "s_waitcnt lgkmcnt(2)\n"
    "buffer_load_dwordx4 v1, s[36:39], s76 offen sc1 lds\n"
    "v_mfma_f32_16x16x32_bf16 v[68:71], a[248:251], v[16:19], v[68:71]\n"
    "ds_read_b128 v[16:19], v9 offset:24576\n"
    "v_mfma_f32_16x16x32_bf16 v[56:59], a[252:255], v[20:23], v[56:59]\n"
    "v_mfma_f32_16x16x32_bf16 v[60:63], v[80:83], v[20:23], v[60:63]\n"
    "s_waitcnt lgkmcnt(2)\n"
    "v_mfma_f32_16x16x32_bf16 v[68:71], v[84:87], v[20:23], v[68:71]\n"
    "ds_read_b128 v[20:23], v9 offset:25600\n"
    "v_mfma_f32_16x16x32_bf16 v[56:59], v[88:91], v[24:27], v[56:59]\n"
    "v_mfma_f32_16x16x32_bf16 v[60:63], v[92:95], v[24:27], v[60:63]\n"
    "s_waitcnt lgkmcnt(2)\n"
    "v_mfma_f32_16x16x32_bf16 v[68:71], v[96:99], v[24:27], v[68:71]\n"
    "ds_read_b128 v[24:27], v9 offset:26624\n"
    "v_mfma_f32_16x16x32_bf16 v[56:59], v[100:103], v[28:31], v[56:59]\n"
    "v_mfma_f32_16x16x32_bf16 v[60:63], v[104:107], v[28:31], v[60:63]\n"
    "s_waitcnt lgkmcnt(2)\n"
    "v_mfma_f32_16x16x32_bf16 v[68:71], v[108:111], v[28:31], v[68:71]\n"
    "ds_read_b128 v[28:31], v9 offset:27648\n"
    ".LgDOWN_join1_%=:\n"
    "v_mfma_f32_16x16x32_bf16 v[56:59], v[112:115], v[16:19], v[56:59]\n"
    "s_add_u32 s71, s68, 2\n"
    "s_waitcnt vmcnt(9)\n"
    "v_mfma_f32_16x16x32_bf16 v[60:63], v[116:119], v[16:19], v[60:63]\n"
    "s_waitcnt lgkmcnt(2)\n"
    "v_add_u32 v13, v11, v3\n"
    "v_mfma_f32_16x16x32_bf16 v[68:71], v[120:123], v[16:19], v[68:71]\n"
    "ds_read_b128 v[16:19], v9 offset:28672\n"
    "v_cmp_gt_i32 vcc, s71, v13\n"
    "v_mfma_f32_16x16x32_bf16 v[56:59], v[124:127], v[20:23], v[56:59]\n"
    "v_mfma_f32_16x16x32_bf16 v[60:63], v[128:131], v[20:23], v[60:63]\n"
    "s_waitcnt lgkmcnt(2)\n"
    "v_mfma_f32_16x16x32_bf16 v[68:71], v[132:135], v[20:23], v[68:71]\n"
    "ds_read_b128 v[20:23], v9 offset:29696\n"
    "v_mfma_f32_16x16x32_bf16 v[56:59], v[136:139], v[24:27], v[56:59]\n"
    "s_or_b64 exec, vcc, s[62:63]\n"
    "buffer_atomic_smax v11, v2, s[36:39], 0 offen sc0 sc1\n"
    "s_mov_b64 exec, -1\n"
    "v_mfma_f32_16x16x32_bf16 v[60:63], v[140:143], v[24:27], v[60:63]\n"
    "s_waitcnt lgkmcnt(2)\n"
    "s_lshr_b32 s76, s71, 1\n"
    "v_mfma_f32_16x16x32_bf16 v[68:71], v[144:147], v[24:27], v[68:71]\n"
    "ds_read_b128 v[24:27], v9 offset:30720\n"
    "s_and_b32 s76, s76, s54\n"
    "v_mfma_f32_16x16x32_bf16 v[56:59], v[148:151], v[28:31], v[56:59]\n"
    "s_mul_i32 s76, s76, s53\n"
    "v_mfma_f32_16x16x32_bf16 v[60:63], v[152:155], v[28:31], v[60:63]\n"
    "s_waitcnt lgkmcnt(2)\n"
    "s_add_u32 s76, s76, s52\n"
    "v_mfma_f32_16x16x32_bf16 v[68:71], v[156:159], v[28:31], v[68:71]\n"
    "ds_read_b128 v[28:31], v9 offset:31744\n"
    "s_add_u32 s76, s76, 1024\n"
    "v_mfma_f32_16x16x32_bf16 v[56:59], v[160:163], v[16:19], v[56:59]\n"
    "v_mfma_f32_16x16x32_bf16 v[60:63], v[164:167], v[16:19], v[60:63]\n"
    "s_waitcnt lgkmcnt(2)\n"
    "v_mfma_f32_16x16x32_bf16 v[68:71], v[168:171], v[16:19], v[68:71]\n"
    "ds_read_b128 v[16:19], v9 offset:32768\n"
    "v_mfma_f32_16x16x32_bf16 v[56:59], v[172:175], v[20:23], v[56:59]\n"
    "v_mfma_f32_16x16x32_bf16 v[60:63], v[176:179], v[20:23], v[60:63]\n"
    "s_waitcnt lgkmcnt(2)\n"
    "v_mfma_f32_16x16x32_bf16 v[68:71], v[180:183], v[20:23], v[68:71]\n"
    "ds_read_b128 v[20:23], v9 offset:33792\n"
    "v_mfma_f32_16x16x32_bf16 v[56:59], v[184:187], v[24:27], v[56:59]\n"
    "v_mfma_f32_16x16x32_bf16 v[60:63], v[188:191], v[24:27], v[60:63]\n"
    "s_waitcnt lgkmcnt(2)\n"
    "v_mfma_f32_16x16x32_bf16 v[68:71], v[192:195], v[24:27], v[68:71]\n"
    "ds_read_b128 v[24:27], v9 offset:34816\n"
    "v_mfma_f32_16x16x32_bf16 v[56:59], v[196:199], v[28:31], v[56:59]\n"
    "v_mfma_f32_16x16x32_bf16 v[60:63], v[200:203], v[28:31], v[60:63]\n"
    "s_waitcnt lgkmcnt(2)\n"
    "v_add_u32 v13, v5, v3\n"
    "v_mfma_f32_16x16x32_bf16 v[68:71], v[204:207], v[28:31], v[68:71]\n"
    "ds_read_b128 v[28:31], v9 offset:35840\n"
    "v_cmp_gt_i32 vcc, s71, v13\n"
    "v_mfma_f32_16x16x32_bf16 v[56:59], v[208:211], v[16:19], v[56:59]\n"
    "v_mfma_f32_16x16x32_bf16 v[60:63], v[212:215], v[16:19], v[60:63]\n"
    "s_waitcnt lgkmcnt(2)\n"
    "v_mfma_f32_16x16x32_bf16 v[68:71], v[216:219], v[16:19], v[68:71]\n"
    "v_mfma_f32_16x16x32_bf16 v[56:59], v[220:223], v[20:23], v[56:59]\n"
    "s_waitcnt vmcnt(1) lgkmcnt(0)\n"
    "s_barrier\n"
    "ds_read_b128 v[16:19], v0 offset:0\n"
    "s_or_b64 exec, vcc, s[62:63]\n"
    "buffer_atomic_smax v5, v2, s[36:39], 0 offen sc0 sc1\n"
    "s_mov_b64 exec, -1\n"
    "v_mfma_f32_16x16x32_bf16 v[60:63], v[224:227], v[20:23], v[60:63]\n"
    "v_mfma_f32_16x16x32_bf16 v[68:71], v[228:231], v[20:23], v[68:71]\n"
    "ds_read_b128 v[20:23], v0 offset:1024\n"
    "v_mfma_f32_16x16x32_bf16 v[56:59], v[232:235], v[24:27], v[56:59]\n"
    "v_mfma_f32_16x16x32_bf16 v[60:63], v[236:239], v[24:27], v[60:63]\n"
    "v_mfma_f32_16x16x32_bf16 v[68:71], v[240:243], v[24:27], v[68:71]\n"
    "ds_read_b128 v[24:27], v0 offset:2048\n"
    "v_mfma_f32_16x16x32_bf16 v[56:59], v[244:247], v[28:31], v[56:59]\n"
    "s_mov_b32 s69, 0x80000000\n"
    "s_mov_b32 s70, 0\n"
    "v_mov_b32 v12, s68\n"
    "v_mfma_f32_16x16x32_bf16 v[60:63], v[248:251], v[28:31], v[60:63]\n"
    "s_waitcnt lgkmcnt(2)\n"
    "s_add_u32 s68, s68, 1\n"
    "v_mfma_f32_16x16x32_bf16 v[68:71], v[252:255], v[28:31], v[68:71]\n"
    "ds_read_b128 v[28:31], v0 offset:3072\n"
    "s_branch .LgDOWN_head0_%=\n"
    ".LgDOWN_LOOPE_%=:\n"
    ".LgDOWN_done_%=:\n"
    "s_waitcnt vmcnt(0)\n"
    "buffer_atomic_smax v12, v10, s[36:39], 0 offen sc1\n"
    "s_waitcnt vmcnt(0)\n"
    "s_branch .LgDOWN_FIN_%=\n"
    ".LgDOWN_poll0_%=:\n"
    "v_mfma_f32_16x16x32_bf16 v[48:51], a[48:51], v[16:19], v[48:51]\n"
    "s_or_b64 exec, vcc, s[62:63]\n"
    "buffer_atomic_smax v11, v2, s[36:39], 0 offen sc0 sc1\n"
    "s_mov_b64 exec, -1\n"
    "v_mfma_f32_16x16x32_bf16 v[52:55], a[52:55], v[16:19], v[52:55]\n"
    "s_waitcnt lgkmcnt(2)\n"
    "v_mfma_f32_16x16x32_bf16 v[64:67], a[56:59], v[16:19], v[64:67]\n"
    "ds_read_b128 v[16:19], v0 offset:8192\n"
    "v_mfma_f32_16x16x32_bf16 v[48:51], a[60:63], v[20:23], v[48:51]\n"
    "buffer_store_dwordx4 v[68:71], v4, s[36:39], s73 offen offset:1024 sc1\n"
    "v_mfma_f32_16x16x32_bf16 v[52:55], a[64:67], v[20:23], v[52:55]\n"
    "s_waitcnt lgkmcnt(2)\n"
    "s_lshr_b32 s73, s68, 1\n"
    "v_mfma_f32_16x16x32_bf16 v[64:67], a[68:71], v[20:23], v[64:67]\n"
    "ds_read_b128 v[20:23], v0 offset:9216\n"
    "s_and_b32 s73, s73, s57\n"
    "v_mfma_f32_16x16x32_bf16 v[48:51], a[72:75], v[24:27], v[48:51]\n"
    "s_mul_i32 s73, s73, s56\n"
    "v_mfma_f32_16x16x32_bf16 v[52:55], a[76:79], v[24:27], v[52:55]\n"
    "s_waitcnt lgkmcnt(2)\n"
    "s_add_u32 s73, s73, s55\n"
    "v_mfma_f32_16x16x32_bf16 v[64:67], a[80:83], v[24:27], v[64:67]\n"
    "ds_read_b128 v[24:27], v0 offset:10240\n"
    "v_mfma_f32_16x16x32_bf16 v[48:51], a[84:87], v[28:31], v[48:51]\n"
    "v_mfma_f32_16x16x32_bf16 v[52:55], a[88:91], v[28:31], v[52:55]\n"
    "s_waitcnt lgkmcnt(2)\n"
    "v_mfma_f32_16x16x32_bf16 v[64:67], a[92:95], v[28:31], v[64:67]\n"
    "ds_read_b128 v[28:31], v0 offset:11264\n"
    "v_mfma_f32_16x16x32_bf16 v[48:51], a[96:99], v[16:19], v[48:51]\n"
    "v_mfma_f32_16x16x32_bf16 v[52:55], a[100:103], v[16:19], v[52:55]\n"
    "s_waitcnt lgkmcnt(2)\n"
    "v_mfma_f32_16x16x32_bf16 v[64:67], a[104:107], v[16:19], v[64:67]\n"
    "ds_read_b128 v[16:19], v0 offset:12288\n"
    "v_mfma_f32_16x16x32_bf16 v[48:51], a[108:111], v[20:23], v[48:51]\n"
    "v_mfma_f32_16x16x32_bf16 v[52:55], a[112:115], v[20:23], v[52:55]\n"
    "s_waitcnt lgkmcnt(2)\n"
    "s_waitcnt vmcnt(4)\n"
    "v_add_u32 v13, v5, v3\n"
    "v_mfma_f32_16x16x32_bf16 v[64:67], a[116:119], v[20:23], v[64:67]\n"
    "ds_read_b128 v[20:23], v0 offset:13312\n"
    "v_cmp_gt_i32 vcc, s71, v13\n"
    "v_mfma_f32_16x16x32_bf16 v[48:51], a[120:123], v[24:27], v[48:51]\n"
    "v_mfma_f32_16x16x32_bf16 v[52:55], a[124:127], v[24:27], v[52:55]\n"
    "s_waitcnt lgkmcnt(2)\n"
    "v_mfma_f32_16x16x32_bf16 v[64:67], a[128:131], v[24:27], v[64:67]\n"
    "ds_read_b128 v[24:27], v0 offset:14336\n"
    "s_cbranch_vccz .LgDOWN_dma0_33_%=\n"
    "v_mfma_f32_16x16x32_bf16 v[48:51], a[132:135], v[28:31], v[48:51]\n"
    "s_or_b64 exec, vcc, s[62:63]\n"
    "buffer_atomic_smax v5, v2, s[36:39], 0 offen sc0 sc1\n"
    "s_mov_b64 exec, -1\n"
    "v_mfma_f32_16x16x32_bf16 v[52:55], a[136:139], v[28:31], v[52:55]\n"
    "s_waitcnt lgkmcnt(2)\n"
    "v_mfma_f32_16x16x32_bf16 v[64:67], a[140:143], v[28:31], v[64:67]\n"
    "ds_read_b128 v[28:31], v0 offset:15360\n"
    "v_mfma_f32_16x16x32_bf16 v[48:51], a[144:147], v[16:19], v[48:51]\n"
    "v_mfma_f32_16x16x32_bf16 v[52:55], a[148:151], v[16:19], v[52:55]\n"
    "s_waitcnt lgkmcnt(2)\n"
    "v_mfma_f32_16x16x32_bf16 v[64:67], a[152:155], v[16:19], v[64:67]\n"
    "ds_read_b128 v[16:19], v0 offset:16384\n"
    "v_mfma_f32_16x16x32_bf16 v[48:51], a[156:159], v[20:23], v[48:51]\n"
    "v_mfma_f32_16x16x32_bf16 v[52:55], a[160:163], v[20:23], v[52:55]\n"
    "s_waitcnt lgkmcnt(2)\n"
    "v_mfma_f32_16x16x32_bf16 v[64:67], a[164:167], v[20:23], v[64:67]\n"
    "ds_read_b128 v[20:23], v0 offset:17408\n"
    "v_mfma_f32_16x16x32_bf16 v[48:51], a[168:171], v[24:27], v[48:51]\n"
    "v_mfma_f32_16x16x32_bf16 v[52:55], a[172:175], v[24:27], v[52:55]\n"
    "s_waitcnt lgkmcnt(2)\n"
    "v_mfma_f32_16x16x32_bf16 v[64:67], a[176:179], v[24:27], v[64:67]\n"
    "ds_read_b128 v[24:27], v0 offset:18432\n"
    "v_mfma_f32_16x16x32_bf16 v[48:51], a[180:183], v[28:31], v[48:51]\n"
    "v_mfma_f32_16x16x32_bf16 v[52:55], a[184:187], v[28:31], v[52:55]\n"
    "s_waitcnt lgkmcnt(2)\n"
    "v_mfma_f32_16x16x32_bf16 v[64:67], a[188:191], v[28:31], v[64:67]\n"
    "ds_read_b128 v[28:31], v0 offset:19456\n"
    "v_mfma_f32_16x16x32_bf16 v[48:51], a[192:195], v[16:19], v[48:51]\n"
    "v_mfma_f32_16x16x32_bf16 v[52:55], a[196:199], v[16:19], v[52:55]\n"
    "s_waitcnt lgkmcnt(2)\n"
    "s_waitcnt vmcnt(2)\n"
    "v_add_u32 v13, v11, v3\n"
    "v_mfma_f32_16x16x32_bf16 v[64:67], a[200:203], v[16:19], v[64:67]\n"
    "ds_read_b128 v[16:19], v0 offset:20480\n"
    "v_cmp_gt_i32 vcc, s71, v13\n"
    "v_mfma_f32_16x16x32_bf16 v[48:51], a[204:207], v[20:23], v[48:51]\n"
    "v_mfma_f32_16x16x32_bf16 v[52:55], a[208:211], v[20:23], v[52:55]\n"
    "s_waitcnt lgkmcnt(2)\n"
    "v_mfma_f32_16x16x32_bf16 v[64:67], a[212:215], v[20:23], v[64:67]\n"
    "ds_read_b128 v[20:23], v0 offset:21504\n"
    "s_cbranch_vccz .LgDOWN_dma0_54_%=\n"
    "v_mfma_f32_16x16x32_bf16 v[48:51], a[216:219], v[24:27], v[48:51]\n"
    "s_or_b64 exec, vcc, s[62:63]\n"
    "buffer_atomic_smax v11, v2, s[36:39], 0 offen sc0 sc1\n"
    "s_mov_b64 exec, -1\n"
    "v_mfma_f32_16x16x32_bf16 v[52:55], a[220:223], v[24:27], v[52:55]\n"
    "s_waitcnt lgkmcnt(2)\n"
    "s_waitcnt vmcnt(2)\n"
    "buffer_atomic_smax v12, v10, s[36:39], s70 offen sc1\n"
    "v_mfma_f32_16x16x32_bf16 v[64:67], a[224:227], v[24:27], v[64:67]\n"
    "ds_read_b128 v[24:27], v0 offset:22528\n"
    "v_mfma_f32_16x16x32_bf16 v[48:51], a[228:231], v[28:31], v[48:51]\n"
    "v_mfma_f32_16x16x32_bf16 v[52:55], a[232:235], v[28:31], v[52:55]\n"
    "s_waitcnt lgkmcnt(2)\n"
    "v_mfma_f32_16x16x32_bf16 v[64:67], a[236:239], v[28:31], v[64:67]\n"
    "ds_read_b128 v[28:31], v0 offset:23552\n"
    "v_mfma_f32_16x16x32_bf16 v[48:51], a[240:243], v[16:19], v[48:51]\n"
    "v_mfma_f32_16x16x32_bf16 v[52:55], a[244:247], v[16:19], v[52:55]\n"
    "s_waitcnt lgkmcnt(2)\n"
    "v_mfma_f32_16x16x32_bf16 v[64:67], a[248:251], v[16:19], v[64:67]\n"
    "ds_read_b128 v[16:19], v0 offset:24576\n"
    "v_mfma_f32_16x16x32_bf16 v[48:51], a[252:255], v[20:23], v[48:51]\n"
    "v_mfma_f32_16x16x32_bf16 v[52:55], v[80:83], v[20:23], v[52:55]\n"
    "s_waitcnt lgkmcnt(2)\n"
    "v_mfma_f32_16x16x32_bf16 v[64:67], v[84:87], v[20:23], v[64:67]\n"
    "ds_read_b128 v[20:23], v0 offset:25600\n"
    "v_mfma_f32_16x16x32_bf16 v[48:51], v[88:91], v[24:27], v[48:51]\n"
    "v_mfma_f32_16x16x32_bf16 v[52:55], v[92:95], v[24:27], v[52:55]\n"
    "s_waitcnt lgkmcnt(2)\n"
    "v_mfma_f32_16x16x32_bf16 v[64:67], v[96:99], v[24:27], v[64:67]\n"
    "ds_read_b128 v[24:27], v0 offset:26624\n"
    "v_mfma_f32_16x16x32_bf16 v[48:51], v[100:103], v[28:31], v[48:51]\n"
    "v_mfma_f32_16x16x32_bf16 v[52:55], v[104:107], v[28:31], v[52:55]\n"
    "s_waitcnt lgkmcnt(2)\n"
    "s_waitcnt vmcnt(2)\n"
    "v_add_u32 v13, v5, v3\n"
    "v_mfma_f32_16x16x32_bf16 v[64:67], v[108:111], v[28:31], v[64:67]\n"
    "ds_read_b128 v[28:31], v0 offset:27648\n"
    "v_cmp_gt_i32 vcc, s71, v13\n"
    "v_mfma_f32_16x16x32_bf16 v[48:51], v[112:115], v[16:19], v[48:51]\n"
    "v_mfma_f32_16x16x32_bf16 v[52:55], v[116:119], v[16:19], v[52:55]\n"
    "s_waitcnt lgkmcnt(2)\n"
    "v_mfma_f32_16x16x32_bf16 v[64:67], v[120:123], v[16:19], v[64:67]\n"
    "ds_read_b128 v[16:19], v0 offset:28672\n"
    "s_cbranch_vccz .LgDOWN_dma0_75_%=\n"
    "v_mfma_f32_16x16x32_bf16 v[48:51], v[124:127], v[20:23], v[48:51]\n"
    "s_or_b64 exec, vcc, s[62:63]\n"
    "buffer_atomic_smax v5, v2, s[36:39], 0 offen sc0 sc1\n"
    "s_mov_b64 exec, -1\n"
    "v_mfma_f32_16x16x32_bf16 v[52:55], v[128:131], v[20:23], v[52:55]\n"
    "s_waitcnt lgkmcnt(2)\n"
    "v_mfma_f32_16x16x32_bf16 v[64:67], v[132:135], v[20:23], v[64:67]\n"
    "ds_read_b128 v[20:23], v0 offset:29696\n"
    "v_mfma_f32_16x16x32_bf16 v[48:51], v[136:139], v[24:27], v[48:51]\n"
    "v_mfma_f32_16x16x32_bf16 v[52:55], v[140:143], v[24:27], v[52:55]\n"
    "s_waitcnt lgkmcnt(2)\n"
    "v_mfma_f32_16x16x32_bf16 v[64:67], v[144:147], v[24:27], v[64:67]\n"
    "ds_read_b128 v[24:27], v0 offset:30720\n"
    "v_mfma_f32_16x16x32_bf16 v[48:51], v[148:151], v[28:31], v[48:51]\n"
    "v_mfma_f32_16x16x32_bf16 v[52:55], v[152:155], v[28:31], v[52:55]\n"
    "s_waitcnt lgkmcnt(2)\n"
    "v_mfma_f32_16x16x32_bf16 v[64:67], v[156:159], v[28:31], v[64:67]\n"
    "ds_read_b128 v[28:31], v0 offset:31744\n"
    "v_mfma_f32_16x16x32_bf16 v[48:51], v[160:163], v[16:19], v[48:51]\n"
    "v_mfma_f32_16x16x32_bf16 v[52:55], v[164:167], v[16:19], v[52:55]\n"
    "s_waitcnt lgkmcnt(2)\n"
    "v_mfma_f32_16x16x32_bf16 v[64:67], v[168:171], v[16:19], v[64:67]\n"
    "ds_read_b128 v[16:19], v0 offset:32768\n"
    "v_mfma_f32_16x16x32_bf16 v[48:51], v[172:175], v[20:23], v[48:51]\n"
    "v_mfma_f32_16x16x32_bf16 v[52:55], v[176:179], v[20:23], v[52:55]\n"
    "s_waitcnt lgkmcnt(2)\n"
    "v_mfma_f32_16x16x32_bf16 v[64:67], v[180:183], v[20:23], v[64:67]\n"
    "ds_read_b128 v[20:23], v0 offset:33792\n"
    "v_mfma_f32_16x16x32_bf16 v[48:51], v[184:187], v[24:27], v[48:51]\n"
    "v_mfma_f32_16x16x32_bf16 v[52:55], v[188:191], v[24:27], v[52:55]\n"
    "s_waitcnt lgkmcnt(2)\n"
    "s_waitcnt vmcnt(2)\n"
    "v_add_u32 v13, v11, v3\n"
    "v_mfma_f32_16x16x32_bf16 v[64:67], v[192:195], v[24:27], v[64:67]\n"
    "ds_read_b128 v[24:27], v0 offset:34816\n"
    "v_cmp_gt_i32 vcc, s71, v13\n"
    "v_mfma_f32_16x16x32_bf16 v[48:51], v[196:199], v[28:31], v[48:51]\n"
    "v_mfma_f32_16x16x32_bf16 v[52:55], v[200:203], v[28:31], v[52:55]\n"
    "s_waitcnt lgkmcnt(2)\n"
    "v_mfma_f32_16x16x32_bf16 v[64:67], v[204:207], v[28:31], v[64:67]\n"
    "ds_read_b128 v[28:31], v0 offset:35840\n"
    "s_cbranch_vccz .LgDOWN_dma0_96_%=\n"
    "v_mfma_f32_16x16x32_bf16 v[48:51], v[208:211], v[16:19], v[48:51]\n"
    "s_or_b64 exec, vcc, s[62:63]\n"
    "buffer_atomic_smax v11, v2, s[36:39], 0 offen sc0 sc1\n"
    "s_mov_b64 exec, -1\n"
    "v_mfma_f32_16x16x32_bf16 v[52:55], v[212:215], v[16:19], v[52:55]\n"
    "s_waitcnt lgkmcnt(2)\n"
    "v_mfma_f32_16x16x32_bf16 v[64:67], v[216:219], v[16:19], v[64:67]\n"
    "v_mfma_f32_16x16x32_bf16 v[48:51], v[220:223], v[20:23], v[48:51]\n"
    "v_mfma_f32_16x16x32_bf16 v[52:55], v[224:227], v[20:23], v[52:55]\n"
    "s_waitcnt lgkmcnt(1)\n"
    "v_mfma_f32_16x16x32_bf16 v[64:67], v[228:231], v[20:23], v[64:67]\n"
    "v_mfma_f32_16x16x32_bf16 v[48:51], v[232:235], v[24:27], v[48:51]\n"
    "v_mfma_f32_16x16x32_bf16 v[52:55], v[236:239], v[24:27], v[52:55]\n"
    "s_waitcnt lgkmcnt(0)\n"
    "v_mfma_f32_16x16x32_bf16 v[64:67], v[240:243], v[24:27], v[64:67]\n"
    "v_mfma_f32_16x16x32_bf16 v[48:51], v[244:247], v[28:31], v[48:51]\n"
    "v_mfma_f32_16x16x32_bf16 v[52:55], v[248:251], v[28:31], v[52:55]\n"
    "v_mfma_f32_16x16x32_bf16 v[64:67], v[252:255], v[28:31], v[64:67]\n"
    "v_cvt_pk_bf16_f32 v48, v48, v49\n"
    "v_cvt_pk_bf16_f32 v49, v50, v51\n"
    "s_nop 1\n"
    "v_cvt_pk_bf16_f32 v50, v52, v53\n"
    "v_cvt_pk_bf16_f32 v51, v54, v55\n"
    "buffer_store_dwordx4 v[48:51], v15, s[36:39], s73 offen sc1\n"
    "s_nop 0\n"
    "v_cvt_pk_bf16_f32 v64, v64, v65\n"
    "v_cvt_pk_bf16_f32 v65, v66, v67\n"
    "s_nop 0\n"
    "v_permlane16_swap_b32 v64, v66\n"
    "v_permlane16_swap_b32 v65, v67\n"
    "buffer_store_dwordx4 v[64:67], v4, s[36:39], s73 offen sc1\n"
    "v_mov_b32 v12, s68\n"
    "s_mov_b32 s73, 0x80000000\n"
    "s_mov_b32 s68, s71\n"
    "s_waitcnt vmcnt(3)\n"
    "v_add_u32 v13, v5, v3\n"
    "v_cmp_gt_i32 vcc, s71, v13\n"
    "s_cbranch_vccnz .LgDOWN_idle0_%=\n"
    "s_add_u32 m0, s59, 36864\n"
    "s_nop 0\n"
    "buffer_load_dwordx4 v1, s[36:39], s76 offen sc1 lds\n"
    "s_add_u32 m0, s59, 40960\n"
    "s_add_u32 s76, s76, 16384\n"
    "buffer_load_dwordx4 v1, s[36:39], s76 offen sc1 lds\n"
    "s_add_u32 m0, s59, 45056\n"
    "s_add_u32 s76, s76, 16384\n"
    "buffer_load_dwordx4 v1, s[36:39], s76 offen sc1 lds\n"
    "s_add_u32 m0, s59, 49152\n"
    "s_add_u32 s76, s76, 16384\n"
    "buffer_load_dwordx4 v1, s[36:39], s76 offen sc1 lds\n"
    "s_add_u32 m0, s59, 53248\n"
    "s_add_u32 s76, s76, 16384\n"
    "buffer_load_dwordx4 v1, s[36:39], s76 offen sc1 lds\n"
    "s_add_u32 m0, s59, 57344\n"
    "s_add_u32 s76, s76, 16384\n"
    "buffer_load_dwordx4 v1, s[36:39], s76 offen sc1 lds\n"
    "s_add_u32 m0, s59, 61440\n"
    "s_add_u32 s76, s76, 16384\n"
    "buffer_load_dwordx4 v1, s[36:39], s76 offen sc1 lds\n"
    "s_add_u32 m0, s59, 65536\n"
    "s_add_u32 s76, s76, 16384\n"
    "buffer_load_dwordx4 v1, s[36:39], s76 offen sc1 lds\n"
    "s_add_u32 m0, s59, 69632\n"
    "s_add_u32 s76, s76, 16384\n"
    "buffer_load_dwordx4 v1, s[36:39], s76 offen sc1 lds\n"
    "s_waitcnt vmcnt(9)\n"
    "buffer_atomic_smax v12, v14, s[36:39], 0 offen sc1\n"
    "s_mov_b32 s69, 0x80000000\n"
    "s_mov_b32 s70, 0x80000000\n"
    "v_mov_b32 v7, v12\n"
    "s_waitcnt vmcnt(12)\n"
    "s_add_u32 s71, s68, 1\n"
    "v_add_u32 v13, v11, v3\n"
    "v_cmp_gt_i32 vcc, s71, v13\n"
    "s_nop 0\n"
    "s_or_b64 exec, vcc, s[62:63]\n"
    "buffer_atomic_smax v11, v2, s[36:39], 0 offen sc0 sc1\n"
    "s_mov_b64 exec, -1\n"
    "s_waitcnt vmcnt(2)\n"
    "s_branch .LgDOWN_bar1_%=\n"
    ".LgDOWN_dma0_33_%=:\n"
    "v_mfma_f32_16x16x32_bf16 v[48:51], a[132:135], v[28:31], v[48:51]\n"
    "s_add_u32 m0, s59, 36864\n"
    "v_mfma_f32_16x16x32_bf16 v[52:55], a[136:139], v[28:31], v[52:55]\n"
    "s_waitcnt lgkmcnt(2)\n"
    "buffer_load_dwordx4 v1, s[36:39], s76 offen sc1 lds\n"
    "v_mfma_f32_16x16x32_bf16 v[64:67], a[140:143], v[28:31], v[64:67]\n"
    "ds_read_b128 v[28:31], v0 offset:15360\n"
    "s_add_u32 s76, s76, 16384\n"
    "v_mfma_f32_16x16x32_bf16 v[48:51], a[144:147], v[16:19], v[48:51]\n"
    "s_add_u32 m0, s59, 40960\n"
    "v_mfma_f32_16x16x32_bf16 v[52:55], a[148:151], v[16:19], v[52:55]\n"
    "s_waitcnt lgkmcnt(2)\n"
    "buffer_load_dwordx4 v1, s[36:39], s76 offen sc1 lds\n"
    "v_mfma_f32_16x16x32_bf16 v[64:67], a[152:155], v[16:19], v[64:67]\n"
    "ds_read_b128 v[16:19], v0 offset:16384\n"
    "v_mfma_f32_16x16x32_bf16 v[48:51], a[156:159], v[20:23], v[48:51]\n"
    "s_add_u32 s76, s76, 16384\n"
    "s_add_u32 m0, s59, 45056\n"
    "v_mfma_f32_16x16x32_bf16 v[52:55], a[160:163], v[20:23], v[52:55]\n"
    "s_waitcnt lgkmcnt(2)\n"
    "buffer_load_dwordx4 v1, s[36:39], s76 offen sc1 lds\n"
    "v_mfma_f32_16x16x32_bf16 v[64:67], a[164:167], v[20:23], v[64:67]\n"
    "ds_read_b128 v[20:23], v0 offset:17408\n"
    "v_mfma_f32_16x16x32_bf16 v[48:51], a[168:171], v[24:27], v[48:51]\n"
    "v_mfma_f32_16x16x32_bf16 v[52:55], a[172:175], v[24:27], v[52:55]\n"
    "s_waitcnt lgkmcnt(2)\n"
    "s_add_u32 s76, s76, 16384\n"
    "s_add_u32 m0, s59, 49152\n"
    "v_mfma_f32_16x16x32_bf16 v[64:67], a[176:179], v[24:27], v[64:67]\n"
    "ds_read_b128 v[24:27], v0 offset:18432\n"
    "buffer_load_dwordx4 v1, s[36:39], s76 offen sc1 lds\n"
    "v_mfma_f32_16x16x32_bf16 v[48:51], a[180:183], v[28:31], v[48:51]\n"
    "v_mfma_f32_16x16x32_bf16 v[52:55], a[184:187], v[28:31], v[52:55]\n"
    "s_waitcnt lgkmcnt(2)\n"
    "v_mfma_f32_16x16x32_bf16 v[64:67], a[188:191], v[28:31], v[64:67]\n"
    "ds_read_b128 v[28:31], v0 offset:19456\n"
    "s_add_u32 s76, s76, 16384\n"
    "v_mfma_f32_16x16x32_bf16 v[48:51], a[192:195], v[16:19], v[48:51]\n"
    "s_add_u32 m0, s59, 53248\n"
    "v_mfma_f32_16x16x32_bf16 v[52:55], a[196:199], v[16:19], v[52:55]\n"
    "s_waitcnt lgkmcnt(2)\n"
    "buffer_load_dwordx4 v1, s[36:39], s76 offen sc1 lds\n"
    "v_mfma_f32_16x16x32_bf16 v[64:67], a[200:203], v[16:19], v[64:67]\n"
    "ds_read_b128 v[16:19], v0 offset:20480\n"
    "v_mfma_f32_16x16x32_bf16 v[48:51], a[204:207], v[20:23], v[48:51]\n"
    "s_add_u32 s76, s76, 16384\n"
    "s_add_u32 m0, s59, 57344\n"
    "v_mfma_f32_16x16x32_bf16 v[52:55], a[208:211], v[20:23], v[52:55]\n"
    "s_waitcnt lgkmcnt(2)\n"
    "buffer_load_dwordx4 v1, s[36:39], s76 offen sc1 lds\n"
    "v_mfma_f32_16x16x32_bf16 v[64:67], a[212:215], v[20:23], v[64:67]\n"
    "ds_read_b128 v[20:23], v0 offset:21504\n"
    "v_mfma_f32_16x16x32_bf16 v[48:51], a[216:219], v[24:27], v[48:51]\n"
    "v_mfma_f32_16x16x32_bf16 v[52:55], a[220:223], v[24:27], v[52:55]\n"
    "s_waitcnt lgkmcnt(2)\n"
    "s_waitcnt vmcnt(6)\n"
    "buffer_atomic_smax v12, v10, s[36:39], s70 offen sc1\n"
    "v_mfma_f32_16x16x32_bf16 v[64:67], a[224:227], v[24:27], v[64:67]\n"
    "ds_read_b128 v[24:27], v0 offset:22528\n"
    "s_add_u32 s76, s76, 16384\n"
    "v_mfma_f32_16x16x32_bf16 v[48:51], a[228:231], v[28:31], v[48:51]\n"
    "s_add_u32 m0, s59, 61440\n"
    "v_mfma_f32_16x16x32_bf16 v[52:55], a[232:235], v[28:31], v[52:55]\n"
    "s_waitcnt lgkmcnt(2)\n"
    "buffer_load_dwordx4 v1, s[36:39], s76 offen sc1 lds\n"
    "v_mfma_f32_16x16x32_bf16 v[64:67], a[236:239], v[28:31], v[64:67]\n"
    "ds_read_b128 v[28:31], v0 offset:23552\n"
    "s_add_u32 s76, s76, 16384\n"
    "v_mfma_f32_16x16x32_bf16 v[48:51], a[240:243], v[16:19], v[48:51]\n"
    "s_add_u32 m0, s59, 65536\n"
    "v_mfma_f32_16x16x32_bf16 v[52:55], a[244:247], v[16:19], v[52:55]\n"
    "s_waitcnt lgkmcnt(2)\n"
    "buffer_load_dwordx4 v1, s[36:39], s76 offen sc1 lds\n"
    "v_mfma_f32_16x16x32_bf16 v[64:67], a[248:251], v[16:19], v[64:67]\n"
    "ds_read_b128 v[16:19], v0 offset:24576\n"
    "v_mfma_f32_16x16x32_bf16 v[48:51], a[252:255], v[20:23], v[48:51]\n"
    "s_add_u32 s76, s76, 16384\n"
    "s_add_u32 m0, s59, 69632\n"
    "v_mfma_f32_16x16x32_bf16 v[52:55], v[80:83], v[20:23], v[52:55]\n"
    "s_waitcnt lgkmcnt(2)\n"
    "buffer_load_dwordx4 v1, s[36:39], s76 offen sc1 lds\n"
    "v_mfma_f32_16x16x32_bf16 v[64:67], v[84:87], v[20:23], v[64:67]\n"
    "ds_read_b128 v[20:23], v0 offset:25600\n"
    "v_mfma_f32_16x16x32_bf16 v[48:51], v[88:91], v[24:27], v[48:51]\n"
    "v_mfma_f32_16x16x32_bf16 v[52:55], v[92:95], v[24:27], v[52:55]\n"
    "s_waitcnt lgkmcnt(2)\n"
    "v_mfma_f32_16x16x32_bf16 v[64:67], v[96:99], v[24:27], v[64:67]\n"
    "ds_read_b128 v[24:27], v0 offset:26624\n"
    "v_mfma_f32_16x16x32_bf16 v[48:51], v[100:103], v[28:31], v[48:51]\n"
    "v_mfma_f32_16x16x32_bf16 v[52:55], v[104:107], v[28:31], v[52:55]\n"
    "s_waitcnt lgkmcnt(2)\n"
    "v_mfma_f32_16x16x32_bf16 v[64:67], v[108:111], v[28:31], v[64:67]\n"
    "ds_read_b128 v[28:31], v0 offset:27648\n"
    "s_branch .LgDOWN_join0_%=\n"
    ".LgDOWN_dma0_54_%=:\n"
    "v_mfma_f32_16x16x32_bf16 v[48:51], a[216:219], v[24:27], v[48:51]\n"
    "s_add_u32 m0, s59, 36864\n"
    "v_mfma_f32_16x16x32_bf16 v[52:55], a[220:223], v[24:27], v[52:55]\n"
    "s_waitcnt lgkmcnt(2)\n"
    "s_waitcnt vmcnt(1)\n"
    "buffer_atomic_smax v12, v10, s[36:39], s70 offen sc1\n"
    "v_mfma_f32_16x16x32_bf16 v[64:67], a[224:227], v[24:27], v[64:67]\n"
    "ds_read_b128 v[24:27], v0 offset:22528\n"
    "buffer_load_dwordx4 v1, s[36:39], s76 offen sc1 lds\n"
    "v_mfma_f32_16x16x32_bf16 v[48:51], a[228:231], v[28:31], v[48:51]\n"
    "s_add_u32 s76, s76, 16384\n"
    "s_add_u32 m0, s59, 40960\n"
    "v_mfma_f32_16x16x32_bf16 v[52:55], a[232:235], v[28:31], v[52:55]\n"
    "s_waitcnt lgkmcnt(2)\n"
    "buffer_load_dwordx4 v1, s[36:39], s76 offen sc1 lds\n"
    "v_mfma_f32_16x16x32_bf16 v[64:67], a[236:239], v[28:31], v[64:67]\n"
    "ds_read_b128 v[28:31], v0 offset:23552\n"
    "v_mfma_f32_16x16x32_bf16 v[48:51], a[240:243], v[16:19], v[48:51]\n"
    "s_add_u32 s76, s76, 16384\n"
    "s_add_u32 m0, s59, 45056\n"
    "v_mfma_f32_16x16x32_bf16 v[52:55], a[244:247], v[16:19], v[52:55]\n"
    "s_waitcnt lgkmcnt(2)\n"
    "buffer_load_dwordx4 v1, s[36:39], s76 offen sc1 lds\n"
    "v_mfma_f32_16x16x32_bf16 v[64:67], a[248:251], v[16:19], v[64:67]\n"
    "ds_read_b128 v[16:19], v0 offset:24576\n"
    "v_mfma_f32_16x16x32_bf16 v[48:51], a[252:255], v[20:23], v[48:51]\n"
    "v_mfma_f32_16x16x32_bf16 v[52:55], v[80:83], v[20:23], v[52:55]\n"
    "s_waitcnt lgkmcnt(2)\n"
    "s_add_u32 s76, s76, 16384\n"
    "s_add_u32 m0, s59, 49152\n"
    "v_mfma_f32_16x16x32_bf16 v[64:67], v[84:87], v[20:23], v[64:67]\n"
    "ds_read_b128 v[20:23], v0 offset:25600\n"
    "buffer_load_dwordx4 v1, s[36:39], s76 offen sc1 lds\n"
    "v_mfma_f32_16x16x32_bf16 v[48:51], v[88:91], v[24:27], v[48:51]\n"
    "v_mfma_f32_16x16x32_bf16 v[52:55], v[92:95], v[24:27], v[52:55]\n"
    "s_waitcnt lgkmcnt(2)\n"
    "v_mfma_f32_16x16x32_bf16 v[64:67], v[96:99], v[24:27], v[64:67]\n"
    "ds_read_b128 v[24:27], v0 offset:26624\n"
    "s_add_u32 s76, s76, 16384\n"
    "v_mfma_f32_16x16x32_bf16 v[48:51], v[100:103], v[28:31], v[48:51]\n"
    "s_add_u32 m0, s59, 53248\n"
    "v_mfma_f32_16x16x32_bf16 v[52:55], v[104:107], v[28:31], v[52:55]\n"
    "s_waitcnt lgkmcnt(2)\n"
    "buffer_load_dwordx4 v1, s[36:39], s76 offen sc1 lds\n"
    "v_mfma_f32_16x16x32_bf16 v[64:67], v[108:111], v[28:31], v[64:67]\n"
    "ds_read_b128 v[28:31], v0 offset:27648\n"
    "v_mfma_f32_16x16x32_bf16 v[48:51], v[112:115], v[16:19], v[48:51]\n"
    "s_add_u32 s76, s76, 16384\n"
    "s_add_u32 m0, s59, 57344\n"
    "v_mfma_f32_16x16x32_bf16 v[52:55], v[116:119], v[16:19], v[52:55]\n"
    "s_waitcnt lgkmcnt(2)\n"
    "buffer_load_dwordx4 v1, s[36:39], s76 offen sc1 lds\n"
    "v_mfma_f32_16x16x32_bf16 v[64:67], v[120:123], v[16:19], v[64:67]\n"
    "ds_read_b128 v[16:19], v0 offset:28672\n"
    "v_mfma_f32_16x16x32_bf16 v[48:51], v[124:127], v[20:23], v[48:51]\n"
    "v_mfma_f32_16x16x32_bf16 v[52:55], v[128:131], v[20:23], v[52:55]\n"
    "s_waitcnt lgkmcnt(2)\n"
    "s_add_u32 s76, s76, 16384\n"
    "s_add_u32 m0, s59, 61440\n"
    "v_mfma_f32_16x16x32_bf16 v[64:67], v[132:135], v[20:23], v[64:67]\n"
    "ds_read_b128 v[20:23], v0 offset:29696\n"
    "buffer_load_dwordx4 v1, s[36:39], s76 offen sc1 lds\n"
    "v_mfma_f32_16x16x32_bf16 v[48:51], v[136:139], v[24:27], v[48:51]\n"
    "v_mfma_f32_16x16x32_bf16 v[52:55], v[140:143], v[24:27], v[52:55]\n"
    "s_waitcnt lgkmcnt(2)\n"
    "v_mfma_f32_16x16x32_bf16 v[64:67], v[144:147], v[24:27], v[64:67]\n"
    "ds_read_b128 v[24:27], v0 offset:30720\n"
    "s_add_u32 s76, s76, 16384\n"
    "v_mfma_f32_16x16x32_bf16 v[48:51], v[148:151], v[28:31], v[48:51]\n"
    "s_add_u32 m0, s59, 65536\n"
    "v_mfma_f32_16x16x32_bf16 v[52:55], v[152:155], v[28:31], v[52:55]\n"
    "s_waitcnt lgkmcnt(2)\n"
    "buffer_load_dwordx4 v1, s[36:39], s76 offen sc1 lds\n"
    "v_mfma_f32_16x16x32_bf16 v[64:67], v[156:159], v[28:31], v[64:67]\n"
    "ds_read_b128 v[28:31], v0 offset:31744\n"
    "v_mfma_f32_16x16x32_bf16 v[48:51], v[160:163], v[16:19], v[48:51]\n"
    "s_add_u32 s76, s76, 16384\n"
    "s_add_u32 m0, s59, 69632\n"
    "v_mfma_f32_16x16x32_bf16 v[52:55], v[164:167], v[16:19], v[52:55]\n"
    "s_waitcnt lgkmcnt(2)\n"
    "buffer_load_dwordx4 v1, s[36:39], s76 offen sc1 lds\n"
    "v_mfma_f32_16x16x32_bf16 v[64:67], v[168:171], v[16:19], v[64:67]\n"
    "ds_read_b128 v[16:19], v0 offset:32768\n"
    "v_mfma_f32_16x16x32_bf16 v[48:51], v[172:175], v[20:23], v[48:51]\n"
    "v_mfma_f32_16x16x32_bf16 v[52:55], v[176:179], v[20:23], v[52:55]\n"
    "s_waitcnt lgkmcnt(2)\n"
    "v_mfma_f32_16x16x32_bf16 v[64:67], v[180:183], v[20:23], v[64:67]\n"
    "ds_read_b128 v[20:23], v0 offset:33792\n"
    "v_mfma_f32_16x16x32_bf16 v[48:51], v[184:187], v[24:27], v[48:51]\n"
    "v_mfma_f32_16x16x32_bf16 v[52:55], v[188:191], v[24:27], v[52:55]\n"
    "s_waitcnt lgkmcnt(2)\n"
    "v_mfma_f32_16x16x32_bf16 v[64:67], v[192:195], v[24:27], v[64:67]\n"
    "ds_read_b128 v[24:27], v0 offset:34816\n"
    "v_mfma_f32_16x16x32_bf16 v[48:51], v[196:199], v[28:31], v[48:51]\n"
    "v_mfma_f32_16x16x32_bf16 v[52:55], v[200:203], v[28:31], v[52:55]\n"
    "s_waitcnt lgkmcnt(2)\n"
    "v_mfma_f32_16x16x32_bf16 v[64:67], v[204:207], v[28:31], v[64:67]\n"
    "ds_read_b128 v[28:31], v0 offset:35840\n"
    "v_mfma_f32_16x16x32_bf16 v[48:51], v[208:211], v[16:19], v[48:51]\n"
    "v_mfma_f32_16x16x32_bf16 v[52:55], v[212:215], v[16:19], v[52:55]\n"
    "s_waitcnt lgkmcnt(2)\n"
    "v_mfma_f32_16x16x32_bf16 v[64:67], v[216:219], v[16:19], v[64:67]\n"
    "v_mfma_f32_16x16x32_bf16 v[48:51], v[220:223], v[20:23], v[48:51]\n"
    "v_mfma_f32_16x16x32_bf16 v[52:55], v[224:227], v[20:23], v[52:55]\n"
    "s_waitcnt lgkmcnt(1)\n"
    "v_mfma_f32_16x16x32_bf16 v[64:67], v[228:231], v[20:23], v[64:67]\n"
    "v_mfma_f32_16x16x32_bf16 v[48:51], v[232:235], v[24:27], v[48:51]\n"
    "v_mfma_f32_16x16x32_bf16 v[52:55], v[236:239], v[24:27], v[52:55]\n"
    "s_waitcnt lgkmcnt(0)\n"
    "v_mfma_f32_16x16x32_bf16 v[64:67], v[240:243], v[24:27], v[64:67]\n"
    "v_mfma_f32_16x16x32_bf16 v[48:51], v[244:247], v[28:31], v[48:51]\n"
    "v_mfma_f32_16x16x32_bf16 v[52:55], v[248:251], v[28:31], v[52:55]\n"
    "v_mfma_f32_16x16x32_bf16 v[64:67], v[252:255], v[28:31], v[64:67]\n"
    "v_cvt_pk_bf16_f32 v48, v48, v49\n"
    "v_cvt_pk_bf16_f32 v49, v50, v51\n"
    "s_nop 1\n"
    "v_cvt_pk_bf16_f32 v50, v52, v53\n"
    "v_cvt_pk_bf16_f32 v51, v54, v55\n"
    "buffer_store_dwordx4 v[48:51], v15, s[36:39], s73 offen sc1\n"
    "s_nop 0\n"
    "v_cvt_pk_bf16_f32 v64, v64, v65\n"
    "v_cvt_pk_bf16_f32 v65, v66, v67\n"
    "s_nop 0\n"
    "v_permlane16_swap_b32 v64, v66\n"
    "v_permlane16_swap_b32 v65, v67\n"
    "buffer_store_dwordx4 v[64:67], v4, s[36:39], s73 offen sc1\n"
    "v_mov_b32 v12, s68\n"
    "s_mov_b32 s73, 0x80000000\n"
    "s_mov_b32 s68, s71\n"
    "s_mov_b32 s69, 0\n"
    "s_mov_b32 s70, 0x80000000\n"
    "v_mov_b32 v7, v12\n"
    "s_waitcnt vmcnt(12)\n"
    "s_add_u32 s71, s68, 1\n"
    "v_add_u32 v13, v11, v3\n"
    "v_cmp_gt_i32 vcc, s71, v13\n"
    "s_nop 0\n"
    "s_or_b64 exec, vcc, s[62:63]\n"
    "buffer_atomic_smax v11, v2, s[36:39], 0 offen sc0 sc1\n"
    "s_mov_b64 exec, -1\n"
    "s_waitcnt vmcnt(3)\n"
    "s_branch .LgDOWN_bar1_%=\n"
    ".LgDOWN_dma0_75_%=:\n"
    "v_mfma_f32_16x16x32_bf16 v[48:51], v[124:127], v[20:23], v[48:51]\n"
    "s_add_u32 m0, s59, 36864\n"
    "v_mfma_f32_16x16x32_bf16 v[52:55], v[128:131], v[20:23], v[52:55]\n"
    "s_waitcnt lgkmcnt(2)\n"
    "buffer_load_dwordx4 v1, s[36:39], s76 offen sc1 lds\n"
    "v_mfma_f32_16x16x32_bf16 v[64:67], v[132:135], v[20:23], v[64:67]\n"
    "ds_read_b128 v[20:23], v0 offset:29696\n"
    "s_add_u32 s76, s76, 16384\n"
    "v_mfma_f32_16x16x32_bf16 v[48:51], v[136:139], v[24:27], v[48:51]\n"
    "s_add_u32 m0, s59, 40960\n"
    "v_mfma_f32_16x16x32_bf16 v[52:55], v[140:143], v[24:27], v[52:55]\n"
    "s_waitcnt lgkmcnt(2)\n"
    "buffer_load_dwordx4 v1, s[36:39], s76 offen sc1 lds\n"
    "v_mfma_f32_16x16x32_bf16 v[64:67], v[144:147], v[24:27], v[64:67]\n"
    "ds_read_b128 v[24:27], v0 offset:30720\n"
    "v_mfma_f32_16x16x32_bf16 v[48:51], v[148:151], v[28:31], v[48:51]\n"
    "s_add_u32 s76, s76, 16384\n"
    "s_add_u32 m0, s59, 45056\n"
    "v_mfma_f32_16x16x32_bf16 v[52:55], v[152:155], v[28:31], v[52:55]\n"
    "s_waitcnt lgkmcnt(2)\n"
    "buffer_load_dwordx4 v1, s[36:39], s76 offen sc1 lds\n"
    "v_mfma_f32_16x16x32_bf16 v[64:67], v[156:159], v[28:31], v[64:67]\n"
    "ds_read_b128 v[28:31], v0 offset:31744\n"
    "v_mfma_f32_16x16x32_bf16 v[48:51], v[160:163], v[16:19], v[48:51]\n"
    "v_mfma_f32_16x16x32_bf16 v[52:55], v[164:167], v[16:19], v[52:55]\n"
    "s_waitcnt lgkmcnt(2)\n"
    "s_add_u32 s76, s76, 16384\n"
    "s_add_u32 m0, s59, 49152\n"
    "v_mfma_f32_16x16x32_bf16 v[64:67], v[168:171], v[16:19], v[64:67]\n"
    "ds_read_b128 v[16:19], v0 offset:32768\n"
    "buffer_load_dwordx4 v1, s[36:39], s76 offen sc1 lds\n"
    "v_mfma_f32_16x16x32_bf16 v[48:51], v[172:175], v[20:23], v[48:51]\n"
    "v_mfma_f32_16x16x32_bf16 v[52:55], v[176:179], v[20:23], v[52:55]\n"
    "s_waitcnt lgkmcnt(2)\n"
    "v_mfma_f32_16x16x32_bf16 v[64:67], v[180:183], v[20:23], v[64:67]\n"
    "ds_read_b128 v[20:23], v0 offset:33792\n"
    "s_add_u32 s76, s76, 16384\n"
    "v_mfma_f32_16x16x32_bf16 v[48:51], v[184:187], v[24:27], v[48:51]\n"
    "s_add_u32 m0, s59, 53248\n"
    "v_mfma_f32_16x16x32_bf16 v[52:55], v[188:191], v[24:27], v[52:55]\n"
    "s_waitcnt lgkmcnt(2)\n"
    "buffer_load_dwordx4 v1, s[36:39], s76 offen sc1 lds\n"
    "v_mfma_f32_16x16x32_bf16 v[64:67], v[192:195], v[24:27], v[64:67]\n"
    "ds_read_b128 v[24:27], v0 offset:34816\n"
    "v_mfma_f32_16x16x32_bf16 v[48:51], v[196:199], v[28:31], v[48:51]\n"
    "s_add_u32 s76, s76, 16384\n"
    "s_add_u32 m0, s59, 57344\n"
    "v_mfma_f32_16x16x32_bf16 v[52:55], v[200:203], v[28:31], v[52:55]\n"
    "s_waitcnt lgkmcnt(2)\n"
    "buffer_load_dwordx4 v1, s[36:39], s76 offen sc1 lds\n"
    "v_mfma_f32_16x16x32_bf16 v[64:67], v[204:207], v[28:31], v[64:67]\n"
    "ds_read_b128 v[28:31], v0 offset:35840\n"
    "v_mfma_f32_16x16x32_bf16 v[48:51], v[208:211], v[16:19], v[48:51]\n"
    "v_mfma_f32_16x16x32_bf16 v[52:55], v[212:215], v[16:19], v[52:55]\n"
    "s_waitcnt lgkmcnt(2)\n"
    "s_add_u32 s76, s76, 16384\n"
    "s_add_u32 m0, s59, 61440\n"
    "v_mfma_f32_16x16x32_bf16 v[64:67], v[216:219], v[16:19], v[64:67]\n"
    "buffer_load_dwordx4 v1, s[36:39], s76 offen sc1 lds\n"
    "v_mfma_f32_16x16x32_bf16 v[48:51], v[220:223], v[20:23], v[48:51]\n"
    "v_mfma_f32_16x16x32_bf16 v[52:55], v[224:227], v[20:23], v[52:55]\n"
    "s_waitcnt lgkmcnt(1)\n"
    "v_mfma_f32_16x16x32_bf16 v[64:67], v[228:231], v[20:23], v[64:67]\n"
    "s_add_u32 s76, s76, 16384\n"
    "v_mfma_f32_16x16x32_bf16 v[48:51], v[232:235], v[24:27], v[48:51]\n"
    "s_add_u32 m0, s59, 65536\n"
    "v_mfma_f32_16x16x32_bf16 v[52:55], v[236:239], v[24:27], v[52:55]\n"
    "s_waitcnt lgkmcnt(0)\n"
    "buffer_load_dwordx4 v1, s[36:39], s76 offen sc1 lds\n"
    "v_mfma_f32_16x16x32_bf16 v[64:67], v[240:243], v[24:27], v[64:67]\n"
    "v_mfma_f32_16x16x32_bf16 v[48:51], v[244:247], v[28:31], v[48:51]\n"
    "s_add_u32 s76, s76, 16384\n"
    "s_add_u32 m0, s59, 69632\n"
    "v_mfma_f32_16x16x32_bf16 v[52:55], v[248:251], v[28:31], v[52:55]\n"
    "buffer_load_dwordx4 v1, s[36:39], s76 offen sc1 lds\n"
    "v_mfma_f32_16x16x32_bf16 v[64:67], v[252:255], v[28:31], v[64:67]\n"
    "v_cvt_pk_bf16_f32 v48, v48, v49\n"
    "v_cvt_pk_bf16_f32 v49, v50, v51\n"
    "s_nop 1\n"
    "v_cvt_pk_bf16_f32 v50, v52, v53\n"
    "v_cvt_pk_bf16_f32 v51, v54, v55\n"
    "buffer_store_dwordx4 v[48:51], v15, s[36:39], s73 offen sc1\n"
    "s_nop 0\n"
    "v_cvt_pk_bf16_f32 v64, v64, v65\n"
    "v_cvt_pk_bf16_f32 v65, v66, v67\n"
    "s_nop 0\n"
    "v_permlane16_swap_b32 v64, v66\n"
    "v_permlane16_swap_b32 v65, v67\n"
    "buffer_store_dwordx4 v[64:67], v4, s[36:39], s73 offen sc1\n"
    "v_mov_b32 v12, s68\n"
    "s_mov_b32 s73, 0x80000000\n"
    "s_mov_b32 s68, s71\n"
    "s_mov_b32 s69, 0\n"
    "s_mov_b32 s70, 0x80000000\n"
    "v_mov_b32 v7, v12\n"
    "s_waitcnt vmcnt(12)\n"
    "s_add_u32 s71, s68, 1\n"
    "v_add_u32 v13, v11, v3\n"
    "v_cmp_gt_i32 vcc, s71, v13\n"
    "s_nop 0\n"
    "s_or_b64 exec, vcc, s[62:63]\n"
    "buffer_atomic_smax v11, v2, s[36:39], 0 offen sc0 sc1\n"
    "s_mov_b64 exec, -1\n"
    "s_waitcnt vmcnt(3)\n"
    "s_branch .LgDOWN_bar1_%=\n"
    ".LgDOWN_dma0_96_%=:\n"
    "v_mfma_f32_16x16x32_bf16 v[48:51], v[208:211], v[16:19], v[48:51]\n"
    "s_add_u32 m0, s59, 36864\n"
    "v_mfma_f32_16x16x32_bf16 v[52:55], v[212:215], v[16:19], v[52:55]\n"
    "s_waitcnt lgkmcnt(2)\n"
    "buffer_load_dwordx4 v1, s[36:39], s76 offen sc1 lds\n"
    "v_mfma_f32_16x16x32_bf16 v[64:67], v[216:219], v[16:19], v[64:67]\n"
    "s_add_u32 s76, s76, 16384\n"
    "s_add_u32 m0, s59, 40960\n"
    "v_mfma_f32_16x16x32_bf16 v[48:51], v[220:223], v[20:23], v[48:51]\n"
    "buffer_load_dwordx4 v1, s[36:39], s76 offen sc1 lds\n"
    "v_mfma_f32_16x16x32_bf16 v[52:55], v[224:227], v[20:23], v[52:55]\n"
    "s_waitcnt lgkmcnt(1)\n"
    "v_mfma_f32_16x16x32_bf16 v[64:67], v[228:231], v[20:23], v[64:67]\n"
    "v_mfma_f32_16x16x32_bf16 v[48:51], v[232:235], v[24:27], v[48:51]\n"
    "s_add_u32 s76, s76, 16384\n"
    "s_add_u32 m0, s59, 45056\n"
    "v_mfma_f32_16x16x32_bf16 v[52:55], v[236:239], v[24:27], v[52:55]\n"
    "s_waitcnt lgkmcnt(0)\n"
    "buffer_load_dwordx4 v1, s[36:39], s76 offen sc1 lds\n"
    "v_mfma_f32_16x16x32_bf16 v[64:67], v[240:243], v[24:27], v[64:67]\n"
    "v_mfma_f32_16x16x32_bf16 v[48:51], v[244:247], v[28:31], v[48:51]\n"
    "v_mfma_f32_16x16x32_bf16 v[52:55], v[248:251], v[28:31], v[52:55]\n"
    "s_add_u32 s76, s76, 16384\n"
    "s_add_u32 m0, s59, 49152\n"
    "v_mfma_f32_16x16x32_bf16 v[64:67], v[252:255], v[28:31], v[64:67]\n"
    "buffer_load_dwordx4 v1, s[36:39], s76 offen sc1 lds\n"
    "v_cvt_pk_bf16_f32 v48, v48, v49\n"
    "v_cvt_pk_bf16_f32 v49, v50, v51\n"
    "s_nop 0\n"
    "v_cvt_pk_bf16_f32 v50, v52, v53\n"
    "v_cvt_pk_bf16_f32 v51, v54, v55\n"
    "buffer_store_dwordx4 v[48:51], v15, s[36:39], s73 offen sc1\n"
    "s_nop 0\n"
    "v_cvt_pk_bf16_f32 v64, v64, v65\n"
    "v_cvt_pk_bf16_f32 v65, v66, v67\n"
    "s_nop 0\n"
    "v_permlane16_swap_b32 v64, v66\n"
    "v_permlane16_swap_b32 v65, v67\n"
    "buffer_store_dwordx4 v[64:67], v4, s[36:39], s73 offen sc1\n"
    "v_mov_b32 v12, s68\n"
    "s_mov_b32 s73, 0x80000000\n"
    "s_mov_b32 s68, s71\n"
    "s_add_u32 m0, s59, 53248\n"
    "s_add_u32 s76, s76, 16384\n"
    "buffer_load_dwordx4 v1, s[36:39], s76 offen sc1 lds\n"
    "s_add_u32 m0, s59, 57344\n"
    "s_add_u32 s76, s76, 16384\n"
    "buffer_load_dwordx4 v1, s[36:39], s76 offen sc1 lds\n"
    "s_add_u32 m0, s59, 61440\n"
    "s_add_u32 s76, s76, 16384\n"
    "buffer_load_dwordx4 v1, s[36:39], s76 offen sc1 lds\n"
    "s_add_u32 m0, s59, 65536\n"
    "s_add_u32 s76, s76, 16384\n"
    "buffer_load_dwordx4 v1, s[36:39], s76 offen sc1 lds\n"
    "s_add_u32 m0, s59, 69632\n"
    "s_add_u32 s76, s76, 16384\n"
    "buffer_load_dwordx4 v1, s[36:39], s76 offen sc1 lds\n"
    "s_waitcnt vmcnt(5)\n"
    "buffer_atomic_smax v12, v14, s[36:39], 0 offen sc1\n"
    "s_mov_b32 s69, 0x80000000\n"
    "s_mov_b32 s70, 0x80000000\n"
    "v_mov_b32 v7, v12\n"
    "s_waitcnt vmcnt(12)\n"
    "s_add_u32 s71, s68, 1\n"
    "v_add_u32 v13, v11, v3\n"
    "v_cmp_gt_i32 vcc, s71, v13\n"
    "s_nop 0\n"
    "s_or_b64 exec, vcc, s[62:63]\n"
    "buffer_atomic_smax v11, v2, s[36:39], 0 offen sc0 sc1\n"
    "s_mov_b64 exec, -1\n"
    "s_waitcnt vmcnt(2)\n"
    "s_branch .LgDOWN_bar1_%=\n"
    ".LgDOWN_idle0_%=:\n"
    "s_or_b64 exec, vcc, s[62:63]\n"
    "buffer_atomic_smax v5, v2, s[36:39], 0 offen sc0 sc1\n"
    "s_mov_b64 exec, -1\n"
    "s_waitcnt vmcnt(1)\n"
    "buffer_atomic_smax v12, v14, s[36:39], 0 offen sc1\n"
    "s_branch .LgDOWN_block0_1_%=\n"
    ".LgDOWN_block0_%=:\n"
    "v_add_u32 v13, v11, v3\n"
    "v_cmp_gt_i32 vcc, s71, v13\n"
    "s_nop 0\n"
    "s_or_b64 exec, vcc, s[62:63]\n"
    "buffer_atomic_smax v11, v2, s[36:39], 0 offen sc0 sc1\n"
    "s_mov_b64 exec, -1\n"
    "s_sleep 3\n"
    "s_waitcnt vmcnt(1)\n"
    ".LgDOWN_block0_0_%=:\n"
    "v_add_u32 v13, v5, v3\n"
    "v_cmp_gt_i32 vcc, s71, v13\n"
    "s_cbranch_vccz .LgDOWN_go0_%=\n"
    "v_add_u32 v13, v5, v3\n"
    "v_cmp_gt_i32 vcc, s71, v13\n"
    "s_nop 0\n"
    "s_or_b64 exec, vcc, s[62:63]\n"
    "buffer_atomic_smax v5, v2, s[36:39], 0 offen sc0 sc1\n"
    "s_mov_b64 exec, -1\n"
    "s_sleep 3\n"
    "s_waitcnt vmcnt(1)\n"
    ".LgDOWN_block0_1_%=:\n"
    "v_add_u32 v13, v11, v3\n"
    "v_cmp_gt_i32 vcc, s71, v13\n"
    "s_cbranch_vccz .LgDOWN_go0_%=\n"
    "s_sub_u32 s72, s72, 1\n"
    "s_cbranch_scc0 .LgDOWN_block0_%=\n"
    "s_mov_b32 s72, 0\n"
    ".LgDOWN_go0_%=:\n"
    "s_lshr_b32 s76, s71, 1\n"
    "s_and_b32 s76, s76, s54\n"
    "s_mul_i32 s76, s76, s53\n"
    "s_add_u32 s76, s76, s52\n"
    "s_add_u32 s76, s76, 1024\n"
    "s_add_u32 m0, s59, 36864\n"
    "s_nop 0\n"
    "buffer_load_dwordx4 v1, s[36:39], s76 offen sc1 lds\n"
    "s_add_u32 m0, s59, 40960\n"
    "s_add_u32 s76, s76, 16384\n"
    "buffer_load_dwordx4 v1, s[36:39], s76 offen sc1 lds\n"
    "s_add_u32 m0, s59, 45056\n"
    "s_add_u32 s76, s76, 16384\n"
    "buffer_load_dwordx4 v1, s[36:39], s76 offen sc1 lds\n"
    "s_add_u32 m0, s59, 49152\n"
    "s_add_u32 s76, s76, 16384\n"
    "buffer_load_dwordx4 v1, s[36:39], s76 offen sc1 lds\n"
    "s_add_u32 m0, s59, 53248\n"
    "s_add_u32 s76, s76, 16384\n"
    "buffer_load_dwordx4 v1, s[36:39], s76 offen sc1 lds\n"
    "s_add_u32 m0, s59, 57344\n"
    "s_add_u32 s76, s76, 16384\n"
    "buffer_load_dwordx4 v1, s[36:39], s76 offen sc1 lds\n"
    "s_add_u32 m0, s59, 61440\n"
    "s_add_u32 s76, s76, 16384\n"
    "buffer_load_dwordx4 v1, s[36:39], s76 offen sc1 lds\n"
    "s_add_u32 m0, s59, 65536\n"
    "s_add_u32 s76, s76, 16384\n"
    "buffer_load_dwordx4 v1, s[36:39], s76 offen sc1 lds\n"
    "s_add_u32 m0, s59, 69632\n"
    "s_add_u32 s76, s76, 16384\n"
    "buffer_load_dwordx4 v1, s[36:39], s76 offen sc1 lds\n"
    "s_mov_b32 s69, 0x80000000\n"
    "s_mov_b32 s70, 0x80000000\n"
    "v_mov_b32 v7, v12\n"
    "s_waitcnt vmcnt(9)\n"
    "s_add_u32 s71, s68, 1\n"
    "v_add_u32 v13, v11, v3\n"
    "v_cmp_gt_i32 vcc, s71, v13\n"
    "s_nop 0\n"
    "s_or_b64 exec, vcc, s[62:63]\n"
    "buffer_atomic_smax v11, v2, s[36:39], 0 offen sc0 sc1\n"
    "s_mov_b64 exec, -1\n"
    "s_waitcnt vmcnt(1)\n"
    "s_branch .LgDOWN_bar1_%=\n"
    ".LgDOWN_bar0_%=:\n"
    "s_barrier\n"
    "ds_read_b128 v[16:19], v0 offset:0\n"
    "ds_read_b128 v[20:23], v0 offset:1024\n"
    "ds_read_b128 v[24:27], v0 offset:2048\n"
    "ds_read_b128 v[28:31], v0 offset:3072\n"
    "v_add_u32 v13, v5, v3\n"
    "v_cmp_gt_i32 vcc, s71, v13\n"
    "s_nop 0\n"
    "s_or_b64 exec, vcc, s[62:63]\n"
    "buffer_atomic_smax v5, v2, s[36:39], 0 offen sc0 sc1\n"
    "s_mov_b64 exec, -1\n"
    "s_lshr_b32 s76, s71, 1\n"
    "s_and_b32 s76, s76, s54\n"
    "s_mul_i32 s76, s76, s53\n"
    "s_add_u32 s76, s76, s52\n"
    "s_add_u32 s76, s76, 1024\n"
    "s_waitcnt lgkmcnt(3)\n"
    "s_branch .LgDOWN_head0_%=\n"
    ".LgDOWN_poll1_%=:\n"
    "v_mfma_f32_16x16x32_bf16 v[56:59], a[48:51], v[16:19], v[56:59]\n"
    "s_or_b64 exec, vcc, s[62:63]\n"
    "buffer_atomic_smax v11, v2, s[36:39], 0 offen sc0 sc1\n"
    "s_mov_b64 exec, -1\n"
    "v_mfma_f32_16x16x32_bf16 v[60:63], a[52:55], v[16:19], v[60:63]\n"
    "s_waitcnt lgkmcnt(2)\n"
    "v_mfma_f32_16x16x32_bf16 v[68:71], a[56:59], v[16:19], v[68:71]\n"
    "ds_read_b128 v[16:19], v9 offset:8192\n"
    "v_mfma_f32_16x16x32_bf16 v[56:59], a[60:63], v[20:23], v[56:59]\n"
    "buffer_store_dwordx4 v[64:67], v4, s[36:39], s73 offen sc1\n"
    "v_mfma_f32_16x16x32_bf16 v[60:63], a[64:67], v[20:23], v[60:63]\n"
    "s_waitcnt lgkmcnt(2)\n"
    "s_lshr_b32 s73, s68, 1\n"
    "v_mfma_f32_16x16x32_bf16 v[68:71], a[68:71], v[20:23], v[68:71]\n"
    "ds_read_b128 v[20:23], v9 offset:9216\n"
    "s_and_b32 s73, s73, s57\n"
    "v_mfma_f32_16x16x32_bf16 v[56:59], a[72:75], v[24:27], v[56:59]\n"
    "s_mul_i32 s73, s73, s56\n"
    "v_mfma_f32_16x16x32_bf16 v[60:63], a[76:79], v[24:27], v[60:63]\n"
    "s_waitcnt lgkmcnt(2)\n"
    "s_add_u32 s73, s73, s55\n"
    "v_mfma_f32_16x16x32_bf16 v[68:71], a[80:83], v[24:27], v[68:71]\n"
    "ds_read_b128 v[24:27], v9 offset:10240\n"
    "v_mfma_f32_16x16x32_bf16 v[56:59], a[84:87], v[28:31], v[56:59]\n"
    "v_mfma_f32_16x16x32_bf16 v[60:63], a[88:91], v[28:31], v[60:63]\n"
    "s_waitcnt lgkmcnt(2)\n"
    "v_mfma_f32_16x16x32_bf16 v[68:71], a[92:95], v[28:31], v[68:71]\n"
    "ds_read_b128 v[28:31], v9 offset:11264\n"
    "v_mfma_f32_16x16x32_bf16 v[56:59], a[96:99], v[16:19], v[56:59]\n"
    "v_mfma_f32_16x16x32_bf16 v[60:63], a[100:103], v[16:19], v[60:63]\n"
    "s_waitcnt lgkmcnt(2)\n"
    "v_mfma_f32_16x16x32_bf16 v[68:71], a[104:107], v[16:19], v[68:71]\n"
    "ds_read_b128 v[16:19], v9 offset:12288\n"
    "v_mfma_f32_16x16x32_bf16 v[56:59], a[108:111], v[20:23], v[56:59]\n"
    "v_mfma_f32_16x16x32_bf16 v[60:63], a[112:115], v[20:23], v[60:63]\n"
    "s_waitcnt lgkmcnt(2)\n"
    "s_waitcnt vmcnt(4)\n"
    "v_add_u32 v13, v5, v3\n"
    "v_mfma_f32_16x16x32_bf16 v[68:71], a[116:119], v[20:23], v[68:71]\n"
    "ds_read_b128 v[20:23], v9 offset:13312\n"
    "v_cmp_gt_i32 vcc, s71, v13\n"
    "v_mfma_f32_16x16x32_bf16 v[56:59], a[120:123], v[24:27], v[56:59]\n"
    "v_mfma_f32_16x16x32_bf16 v[60:63], a[124:127], v[24:27], v[60:63]\n"
    "s_waitcnt lgkmcnt(2)\n"
    "v_mfma_f32_16x16x32_bf16 v[68:71], a[128:131], v[24:27], v[68:71]\n"
    "ds_read_b128 v[24:27], v9 offset:14336\n"
    "s_cbranch_vccz .LgDOWN_dma1_33_%=\n"
    "v_mfma_f32_16x16x32_bf16 v[56:59], a[132:135], v[28:31], v[56:59]\n"
    "s_or_b64 exec, vcc, s[62:63]\n"
    "buffer_atomic_smax v5, v2, s[36:39], 0 offen sc0 sc1\n"
    "s_mov_b64 exec, -1\n"
    "v_mfma_f32_16x16x32_bf16 v[60:63], a[136:139], v[28:31], v[60:63]\n"
    "s_waitcnt lgkmcnt(2)\n"
    "v_mfma_f32_16x16x32_bf16 v[68:71], a[140:143], v[28:31], v[68:71]\n"
    "ds_read_b128 v[28:31], v9 offset:15360\n"
    "v_mfma_f32_16x16x32_bf16 v[56:59], a[144:147], v[16:19], v[56:59]\n"
    "v_mfma_f32_16x16x32_bf16 v[60:63], a[148:151], v[16:19], v[60:63]\n"
    "s_waitcnt lgkmcnt(2)\n"
    "v_mfma_f32_16x16x32_bf16 v[68:71], a[152:155], v[16:19], v[68:71]\n"
    "ds_read_b128 v[16:19], v9 offset:16384\n"
    "v_mfma_f32_16x16x32_bf16 v[56:59], a[156:159], v[20:23], v[56:59]\n"
    "v_mfma_f32_16x16x32_bf16 v[60:63], a[160:163], v[20:23], v[60:63]\n"
    "s_waitcnt lgkmcnt(2)\n"
    "v_mfma_f32_16x16x32_bf16 v[68:71], a[164:167], v[20:23], v[68:71]\n"
    "ds_read_b128 v[20:23], v9 offset:17408\n"
    "v_mfma_f32_16x16x32_bf16 v[56:59], a[168:171], v[24:27], v[56:59]\n"
    "v_mfma_f32_16x16x32_bf16 v[60:63], a[172:175], v[24:27], v[60:63]\n"
    "s_waitcnt lgkmcnt(2)\n"
    "v_mfma_f32_16x16x32_bf16 v[68:71], a[176:179], v[24:27], v[68:71]\n"
    "ds_read_b128 v[24:27], v9 offset:18432\n"
    "v_mfma_f32_16x16x32_bf16 v[56:59], a[180:183], v[28:31], v[56:59]\n"
    "v_mfma_f32_16x16x32_bf16 v[60:63], a[184:187], v[28:31], v[60:63]\n"
    "s_waitcnt lgkmcnt(2)\n"
    "v_mfma_f32_16x16x32_bf16 v[68:71], a[188:191], v[28:31], v[68:71]\n"
    "ds_read_b128 v[28:31], v9 offset:19456\n"
    "v_mfma_f32_16x16x32_bf16 v[56:59], a[192:195], v[16:19], v[56:59]\n"
    "v_mfma_f32_16x16x32_bf16 v[60:63], a[196:199], v[16:19], v[60:63]\n"
    "s_waitcnt lgkmcnt(2)\n"
    "s_waitcnt vmcnt(2)\n"
    "v_add_u32 v13, v11, v3\n"
    "v_mfma_f32_16x16x32_bf16 v[68:71], a[200:203], v[16:19], v[68:71]\n"
    "ds_read_b128 v[16:19], v9 offset:20480\n"
    "v_cmp_gt_i32 vcc, s71, v13\n"
    "v_mfma_f32_16x16x32_bf16 v[56:59], a[204:207], v[20:23], v[56:59]\n"
    "v_mfma_f32_16x16x32_bf16 v[60:63], a[208:211], v[20:23], v[60:63]\n"
    "s_waitcnt lgkmcnt(2)\n"
    "v_mfma_f32_16x16x32_bf16 v[68:71], a[212:215], v[20:23], v[68:71]\n"
    "ds_read_b128 v[20:23], v9 offset:21504\n"
    "s_cbranch_vccz .LgDOWN_dma1_54_%=\n"
    "v_mfma_f32_16x16x32_bf16 v[56:59], a[216:219], v[24:27], v[56:59]\n"
    "s_or_b64 exec, vcc, s[62:63]\n"
    "buffer_atomic_smax v11, v2, s[36:39], 0 offen sc0 sc1\n"
    "s_mov_b64 exec, -1\n"
    "v_mfma_f32_16x16x32_bf16 v[60:63], a[220:223], v[24:27], v[60:63]\n"
    "s_waitcnt lgkmcnt(2)\n"
    "s_waitcnt vmcnt(2)\n"
    "buffer_atomic_smax v12, v14, s[36:39], s70 offen sc1\n"
    "v_mfma_f32_16x16x32_bf16 v[68:71], a[224:227], v[24:27], v[68:71]\n"
    "ds_read_b128 v[24:27], v9 offset:22528\n"
    "v_mfma_f32_16x16x32_bf16 v[56:59], a[228:231], v[28:31], v[56:59]\n"
    "v_mfma_f32_16x16x32_bf16 v[60:63], a[232:235], v[28:31], v[60:63]\n"
    "s_waitcnt lgkmcnt(2)\n"
    "v_mfma_f32_16x16x32_bf16 v[68:71], a[236:239], v[28:31], v[68:71]\n"
    "ds_read_b128 v[28:31], v9 offset:23552\n"
    "v_mfma_f32_16x16x32_bf16 v[56:59], a[240:243], v[16:19], v[56:59]\n"
    "v_mfma_f32_16x16x32_bf16 v[60:63], a[244:247], v[16:19], v[60:63]\n"
    "s_waitcnt lgkmcnt(2)\n"
    "v_mfma_f32_16x16x32_bf16 v[68:71], a[248:251], v[16:19], v[68:71]\n"
    "ds_read_b128 v[16:19], v9 offset:24576\n"
    "v_mfma_f32_16x16x32_bf16 v[56:59], a[252:255], v[20:23], v[56:59]\n"
    "v_mfma_f32_16x16x32_bf16 v[60:63], v[80:83], v[20:23], v[60:63]\n"
    "s_waitcnt lgkmcnt(2)\n"
    "v_mfma_f32_16x16x32_bf16 v[68:71], v[84:87], v[20:23], v[68:71]\n"
    "ds_read_b128 v[20:23], v9 offset:25600\n"
    "v_mfma_f32_16x16x32_bf16 v[56:59], v[88:91], v[24:27], v[56:59]\n"
    "v_mfma_f32_16x16x32_bf16 v[60:63], v[92:95], v[24:27], v[60:63]\n"
    "s_waitcnt lgkmcnt(2)\n"
    "v_mfma_f32_16x16x32_bf16 v[68:71], v[96:99], v[24:27], v[68:71]\n"
    "ds_read_b128 v[24:27], v9 offset:26624\n"
    "v_mfma_f32_16x16x32_bf16 v[56:59], v[100:103], v[28:31], v[56:59]\n"
    "v_mfma_f32_16x16x32_bf16 v[60:63], v[104:107], v[28:31], v[60:63]\n"
    "s_waitcnt lgkmcnt(2)\n"
    "s_waitcnt vmcnt(2)\n"
    "v_add_u32 v13, v5, v3\n"
    "v_mfma_f32_16x16x32_bf16 v[68:71], v[108:111], v[28:31], v[68:71]\n"
    "ds_read_b128 v[28:31], v9 offset:27648\n"
    "v_cmp_gt_i32 vcc, s71, v13\n"
    "v_mfma_f32_16x16x32_bf16 v[56:59], v[112:115], v[16:19], v[56:59]\n"
    "v_mfma_f32_16x16x32_bf16 v[60:63], v[116:119], v[16:19], v[60:63]\n"
    "s_waitcnt lgkmcnt(2)\n"
    "v_mfma_f32_16x16x32_bf16 v[68:71], v[120:123], v[16:19], v[68:71]\n"
    "ds_read_b128 v[16:19], v9 offset:28672\n"
    "s_cbranch_vccz .LgDOWN_dma1_75_%=\n"
    "v_mfma_f32_16x16x32_bf16 v[56:59], v[124:127], v[20:23], v[56:59]\n"
    "s_or_b64 exec, vcc, s[62:63]\n"
    "buffer_atomic_smax v5, v2, s[36:39], 0 offen sc0 sc1\n"
    "s_mov_b64 exec, -1\n"
    "v_mfma_f32_16x16x32_bf16 v[60:63], v[128:131], v[20:23], v[60:63]\n"
    "s_waitcnt lgkmcnt(2)\n"
    "v_mfma_f32_16x16x32_bf16 v[68:71], v[132:135], v[20:23], v[68:71]\n"
    "ds_read_b128 v[20:23], v9 offset:29696\n"
    "v_mfma_f32_16x16x32_bf16 v[56:59], v[136:139], v[24:27], v[56:59]\n"
    "v_mfma_f32_16x16x32_bf16 v[60:63], v[140:143], v[24:27], v[60:63]\n"
    "s_waitcnt lgkmcnt(2)\n"
    "v_mfma_f32_16x16x32_bf16 v[68:71], v[144:147], v[24:27], v[68:71]\n"
    "ds_read_b128 v[24:27], v9 offset:30720\n"
    "v_mfma_f32_16x16x32_bf16 v[56:59], v[148:151], v[28:31], v[56:59]\n"
    "v_mfma_f32_16x16x32_bf16 v[60:63], v[152:155], v[28:31], v[60:63]\n"
    "s_waitcnt lgkmcnt(2)\n"
    "v_mfma_f32_16x16x32_bf16 v[68:71], v[156:159], v[28:31], v[68:71]\n"
    "ds_read_b128 v[28:31], v9 offset:31744\n"
    "v_mfma_f32_16x16x32_bf16 v[56:59], v[160:163], v[16:19], v[56:59]\n"
    "v_mfma_f32_16x16x32_bf16 v[60:63], v[164:167], v[16:19], v[60:63]\n"
    "s_waitcnt lgkmcnt(2)\n"
    "v_mfma_f32_16x16x32_bf16 v[68:71], v[168:171], v[16:19], v[68:71]\n"
    "ds_read_b128 v[16:19], v9 offset:32768\n"
    "v_mfma_f32_16x16x32_bf16 v[56:59], v[172:175], v[20:23], v[56:59]\n"
    "v_mfma_f32_16x16x32_bf16 v[60:63], v[176:179], v[20:23], v[60:63]\n"
    "s_waitcnt lgkmcnt(2)\n"
    "v_mfma_f32_16x16x32_bf16 v[68:71], v[180:183], v[20:23], v[68:71]\n"
    "ds_read_b128 v[20:23], v9 offset:33792\n"
    "v_mfma_f32_16x16x32_bf16 v[56:59], v[184:187], v[24:27], v[56:59]\n"
    "v_mfma_f32_16x16x32_bf16 v[60:63], v[188:191], v[24:27], v[60:63]\n"
    "s_waitcnt lgkmcnt(2)\n"
    "s_waitcnt vmcnt(2)\n"
    "v_add_u32 v13, v11, v3\n"
    "v_mfma_f32_16x16x32_bf16 v[68:71], v[192:195], v[24:27], v[68:71]\n"
    "ds_read_b128 v[24:27], v9 offset:34816\n"
    "v_cmp_gt_i32 vcc, s71, v13\n"
    "v_mfma_f32_16x16x32_bf16 v[56:59], v[196:199], v[28:31], v[56:59]\n"
    "v_mfma_f32_16x16x32_bf16 v[60:63], v[200:203], v[28:31], v[60:63]\n"
    "s_waitcnt lgkmcnt(2)\n"
    "v_mfma_f32_16x16x32_bf16 v[68:71], v[204:207], v[28:31], v[68:71]\n"
    "ds_read_b128 v[28:31], v9 offset:35840\n"
    "s_cbranch_vccz .LgDOWN_dma1_96_%=\n"
    "v_mfma_f32_16x16x32_bf16 v[56:59], v[208:211], v[16:19], v[56:59]\n"
    "s_or_b64 exec, vcc, s[62:63]\n"
    "buffer_atomic_smax v11, v2, s[36:39], 0 offen sc0 sc1\n"
    "s_mov_b64 exec, -1\n"
    "v_mfma_f32_16x16x32_bf16 v[60:63], v[212:215], v[16:19], v[60:63]\n"
    "s_waitcnt lgkmcnt(2)\n"
    "v_mfma_f32_16x16x32_bf16 v[68:71], v[216:219], v[16:19], v[68:71]\n"
    "v_mfma_f32_16x16x32_bf16 v[56:59], v[220:223], v[20:23], v[56:59]\n"
    "v_mfma_f32_16x16x32_bf16 v[60:63], v[224:227], v[20:23], v[60:63]\n"
    "s_waitcnt lgkmcnt(1)\n"
    "v_mfma_f32_16x16x32_bf16 v[68:71], v[228:231], v[20:23], v[68:71]\n"
    "v_mfma_f32_16x16x32_bf16 v[56:59], v[232:235], v[24:27], v[56:59]\n"
    "v_mfma_f32_16x16x32_bf16 v[60:63], v[236:239], v[24:27], v[60:63]\n"
    "s_waitcnt lgkmcnt(0)\n"
    "v_mfma_f32_16x16x32_bf16 v[68:71], v[240:243], v[24:27], v[68:71]\n"
    "v_mfma_f32_16x16x32_bf16 v[56:59], v[244:247], v[28:31], v[56:59]\n"
    "v_mfma_f32_16x16x32_bf16 v[60:63], v[248:251], v[28:31], v[60:63]\n"
    "v_mfma_f32_16x16x32_bf16 v[68:71], v[252:255], v[28:31], v[68:71]\n"
    "v_cvt_pk_bf16_f32 v56, v56, v57\n"
    "v_cvt_pk_bf16_f32 v57, v58, v59\n"
    "s_nop 1\n"
    "v_cvt_pk_bf16_f32 v58, v60, v61\n"
    "v_cvt_pk_bf16_f32 v59, v62, v63\n"
    "buffer_store_dwordx4 v[56:59], v15, s[36:39], s73 offen offset:1024 sc1\n"
    "s_nop 0\n"
    "v_cvt_pk_bf16_f32 v68, v68, v69\n"
    "v_cvt_pk_bf16_f32 v69, v70, v71\n"
    "s_nop 0\n"
    "v_permlane16_swap_b32 v68, v70\n"
    "v_permlane16_swap_b32 v69, v71\n"
    "buffer_store_dwordx4 v[68:71], v4, s[36:39], s73 offen offset:1024 sc1\n"
    "v_mov_b32 v12, s68\n"
    "s_mov_b32 s73, 0x80000000\n"
    "s_mov_b32 s68, s71\n"
    "s_cmp_eq_u32 s68, 128\n"
    "s_cbranch_scc1 .LgDOWN_done_%=\n"
    "s_waitcnt vmcnt(3)\n"
    "v_add_u32 v13, v5, v3\n"
    "v_cmp_gt_i32 vcc, s71, v13\n"
    "s_cbranch_vccnz .LgDOWN_idle1_%=\n"
    "s_add_u32 m0, s59, 0\n"
    "s_nop 0\n"
    "buffer_load_dwordx4 v1, s[36:39], s76 offen sc1 lds\n"
    "s_add_u32 m0, s59, 4096\n"
    "s_add_u32 s76, s76, 16384\n"
    "buffer_load_dwordx4 v1, s[36:39], s76 offen sc1 lds\n"
    "s_add_u32 m0, s59, 8192\n"
    "s_add_u32 s76, s76, 16384\n"
    "buffer_load_dwordx4 v1, s[36:39], s76 offen sc1 lds\n"
    "s_add_u32 m0, s59, 12288\n"
    "s_add_u32 s76, s76, 16384\n"
    "buffer_load_dwordx4 v1, s[36:39], s76 offen sc1 lds\n"
    "s_add_u32 m0, s59, 16384\n"
    "s_add_u32 s76, s76, 16384\n"
    "buffer_load_dwordx4 v1, s[36:39], s76 offen sc1 lds\n"
    "s_add_u32 m0, s59, 20480\n"
    "s_add_u32 s76, s76, 16384\n"
    "buffer_load_dwordx4 v1, s[36:39], s76 offen sc1 lds\n"
    "s_add_u32 m0, s59, 24576\n"
    "s_add_u32 s76, s76, 16384\n"
    "buffer_load_dwordx4 v1, s[36:39], s76 offen sc1 lds\n"
    "s_add_u32 m0, s59, 28672\n"
    "s_add_u32 s76, s76, 16384\n"
    "buffer_load_dwordx4 v1, s[36:39], s76 offen sc1 lds\n"
    "s_add_u32 m0, s59, 32768\n"
    "s_add_u32 s76, s76, 16384\n"
    "buffer_load_dwordx4 v1, s[36:39], s76 offen sc1 lds\n"
    "s_waitcnt vmcnt(9)\n"
    "buffer_atomic_smax v12, v10, s[36:39], 0 offen sc1\n"
    "s_mov_b32 s69, 0x80000000\n"
    "s_mov_b32 s70, 0x80000000\n"
    "v_mov_b32 v7, v12\n"
    "s_waitcnt vmcnt(12)\n"
    "s_add_u32 s71, s68, 1\n"
    "v_add_u32 v13, v11, v3\n"
    "v_cmp_gt_i32 vcc, s71, v13\n"
    "s_nop 0\n"
    "s_or_b64 exec, vcc, s[62:63]\n"
    "buffer_atomic_smax v11, v2, s[36:39], 0 offen sc0 sc1\n"
    "s_mov_b64 exec, -1\n"
    "s_waitcnt vmcnt(2)\n"
    "s_branch .LgDOWN_bar0_%=\n"
    ".LgDOWN_dma1_33_%=:\n"
    "v_mfma_f32_16x16x32_bf16 v[56:59], a[132:135], v[28:31], v[56:59]\n"
    "s_add_u32 m0, s59, 0\n"
    "v_mfma_f32_16x16x32_bf16 v[60:63], a[136:139], v[28:31], v[60:63]\n"
    "s_waitcnt lgkmcnt(2)\n"
    "buffer_load_dwordx4 v1, s[36:39], s76 offen sc1 lds\n"
    "v_mfma_f32_16x16x32_bf16 v[68:71], a[140:143], v[28:31], v[68:71]\n"
    "ds_read_b128 v[28:31], v9 offset:15360\n"
    "s_add_u32 s76, s76, 16384\n"
    "v_mfma_f32_16x16x32_bf16 v[56:59], a[144:147], v[16:19], v[56:59]\n"
    "s_add_u32 m0, s59, 4096\n"
    "v_mfma_f32_16x16x32_bf16 v[60:63], a[148:151], v[16:19], v[60:63]\n"
    "s_waitcnt lgkmcnt(2)\n"
    "buffer_load_dwordx4 v1, s[36:39], s76 offen sc1 lds\n"
    "v_mfma_f32_16x16x32_bf16 v[68:71], a[152:155], v[16:19], v[68:71]\n"
    "ds_read_b128 v[16:19], v9 offset:16384\n"
    "v_mfma_f32_16x16x32_bf16 v[56:59], a[156:159], v[20:23], v[56:59]\n"
    "s_add_u32 s76, s76, 16384\n"
    "s_add_u32 m0, s59, 8192\n"
    "v_mfma_f32_16x16x32_bf16 v[60:63], a[160:163], v[20:23], v[60:63]\n"
    "s_waitcnt lgkmcnt(2)\n"
    "buffer_load_dwordx4 v1, s[36:39], s76 offen sc1 lds\n"
    "v_mfma_f32_16x16x32_bf16 v[68:71], a[164:167], v[20:23], v[68:71]\n"
    "ds_read_b128 v[20:23], v9 offset:17408\n"
    "v_mfma_f32_16x16x32_bf16 v[56:59], a[168:171], v[24:27], v[56:59]\n"
    "v_mfma_f32_16x16x32_bf16 v[60:63], a[172:175], v[24:27], v[60:63]\n"
    "s_waitcnt lgkmcnt(2)\n"
    "s_add_u32 s76, s76, 16384\n"
    "s_add_u32 m0, s59, 12288\n"
    "v_mfma_f32_16x16x32_bf16 v[68:71], a[176:179], v[24:27], v[68:71]\n"
    "ds_read_b128 v[24:27], v9 offset:18432\n"
    "buffer_load_dwordx4 v1, s[36:39], s76 offen sc1 lds\n"
    "v_mfma_f32_16x16x32_bf16 v[56:59], a[180:183], v[28:31], v[56:59]\n"
    "v_mfma_f32_16x16x32_bf16 v[60:63], a[184:187], v[28:31], v[60:63]\n"
    "s_waitcnt lgkmcnt(2)\n"
    "v_mfma_f32_16x16x32_bf16 v[68:71], a[188:191], v[28:31], v[68:71]\n"
    "ds_read_b128 v[28:31], v9 offset:19456\n"
    "s_add_u32 s76, s76, 16384\n"
    "v_mfma_f32_16x16x32_bf16 v[56:59], a[192:195], v[16:19], v[56:59]\n"
    "s_add_u32 m0, s59, 16384\n"
    "v_mfma_f32_16x16x32_bf16 v[60:63], a[196:199], v[16:19], v[60:63]\n"
    "s_waitcnt lgkmcnt(2)\n"
    "buffer_load_dwordx4 v1, s[36:39], s76 offen sc1 lds\n"
    "v_mfma_f32_16x16x32_bf16 v[68:71], a[200:203], v[16:19], v[68:71]\n"
    "ds_read_b128 v[16:19], v9 offset:20480\n"
    "v_mfma_f32_16x16x32_bf16 v[56:59], a[204:207], v[20:23], v[56:59]\n"
    "s_add_u32 s76, s76, 16384\n"
    "s_add_u32 m0, s59, 20480\n"
    "v_mfma_f32_16x16x32_bf16 v[60:63], a[208:211], v[20:23], v[60:63]\n"
    "s_waitcnt lgkmcnt(2)\n"
    "buffer_load_dwordx4 v1, s[36:39], s76 offen sc1 lds\n"
    "v_mfma_f32_16x16x32_bf16 v[68:71], a[212:215], v[20:23], v[68:71]\n"
    "ds_read_b128 v[20:23], v9 offset:21504\n"
    "v_mfma_f32_16x16x32_bf16 v[56:59], a[216:219], v[24:27], v[56:59]\n"
    "v_mfma_f32_16x16x32_bf16 v[60:63], a[220:223], v[24:27], v[60:63]\n"
    "s_waitcnt lgkmcnt(2)\n"
    "s_waitcnt vmcnt(6)\n"
    "buffer_atomic_smax v12, v14, s[36:39], s70 offen sc1\n"
    "v_mfma_f32_16x16x32_bf16 v[68:71], a[224:227], v[24:27], v[68:71]\n"
    "ds_read_b128 v[24:27], v9 offset:22528\n"
    "s_add_u32 s76, s76, 16384\n"
    "v_mfma_f32_16x16x32_bf16 v[56:59], a[228:231], v[28:31], v[56:59]\n"
    "s_add_u32 m0, s59, 24576\n"
    "v_mfma_f32_16x16x32_bf16 v[60:63], a[232:235], v[28:31], v[60:63]\n"
    "s_waitcnt lgkmcnt(2)\n"
    "buffer_load_dwordx4 v1, s[36:39], s76 offen sc1 lds\n"
    "v_mfma_f32_16x16x32_bf16 v[68:71], a[236:239], v[28:31], v[68:71]\n"
    "ds_read_b128 v[28:31], v9 offset:23552\n"
    "s_add_u32 s76, s76, 16384\n"
    "v_mfma_f32_16x16x32_bf16 v[56:59], a[240:243], v[16:19], v[56:59]\n"
    "s_add_u32 m0, s59, 28672\n"
    "v_mfma_f32_16x16x32_bf16 v[60:63], a[244:247], v[16:19], v[60:63]\n"
    "s_waitcnt lgkmcnt(2)\n"
    "buffer_load_dwordx4 v1, s[36:39], s76 offen sc1 lds\n"
    "v_mfma_f32_16x16x32_bf16 v[68:71], a[248:251], v[16:19], v[68:71]\n"
    "ds_read_b128 v[16:19], v9 offset:24576\n"
    "v_mfma_f32_16x16x32_bf16 v[56:59], a[252:255], v[20:23], v[56:59]\n"
    "s_add_u32 s76, s76, 16384\n"
    "s_add_u32 m0, s59, 32768\n"
    "v_mfma_f32_16x16x32_bf16 v[60:63], v[80:83], v[20:23], v[60:63]\n"
    "s_waitcnt lgkmcnt(2)\n"
    "buffer_load_dwordx4 v1, s[36:39], s76 offen sc1 lds\n"
    "v_mfma_f32_16x16x32_bf16 v[68:71], v[84:87], v[20:23], v[68:71]\n"
    "ds_read_b128 v[20:23], v9 offset:25600\n"
    "v_mfma_f32_16x16x32_bf16 v[56:59], v[88:91], v[24:27], v[56:59]\n"
    "v_mfma_f32_16x16x32_bf16 v[60:63], v[92:95], v[24:27], v[60:63]\n"
    "s_waitcnt lgkmcnt(2)\n"
    "v_mfma_f32_16x16x32_bf16 v[68:71], v[96:99], v[24:27], v[68:71]\n"
    "ds_read_b128 v[24:27], v9 offset:26624\n"
    "v_mfma_f32_16x16x32_bf16 v[56:59], v[100:103], v[28:31], v[56:59]\n"
    "v_mfma_f32_16x16x32_bf16 v[60:63], v[104:107], v[28:31], v[60:63]\n"
    "s_waitcnt lgkmcnt(2)\n"
    "v_mfma_f32_16x16x32_bf16 v[68:71], v[108:111], v[28:31], v[68:71]\n"
    "ds_read_b128 v[28:31], v9 offset:27648\n"
    "s_branch .LgDOWN_join1_%=\n"
    ".LgDOWN_dma1_54_%=:\n"
    "v_mfma_f32_16x16x32_bf16 v[56:59], a[216:219], v[24:27], v[56:59]\n"
    "s_add_u32 m0, s59, 0\n"
    "v_mfma_f32_16x16x32_bf16 v[60:63], a[220:223], v[24:27], v[60:63]\n"
    "s_waitcnt lgkmcnt(2)\n"
    "s_waitcnt vmcnt(1)\n"
    "buffer_atomic_smax v12, v14, s[36:39], s70 offen sc1\n"
    "v_mfma_f32_16x16x32_bf16 v[68:71], a[224:227], v[24:27], v[68:71]\n"
    "ds_read_b128 v[24:27], v9 offset:22528\n"
    "buffer_load_dwordx4 v1, s[36:39], s76 offen sc1 lds\n"
    "v_mfma_f32_16x16x32_bf16 v[56:59], a[228:231], v[28:31], v[56:59]\n"
    "s_add_u32 s76, s76, 16384\n"
    "s_add_u32 m0, s59, 4096\n"
    "v_mfma_f32_16x16x32_bf16 v[60:63], a[232:235], v[28:31], v[60:63]\n"
    "s_waitcnt lgkmcnt(2)\n"
    "buffer_load_dwordx4 v1, s[36:39], s76 offen sc1 lds\n"
    "v_mfma_f32_16x16x32_bf16 v[68:71], a[236:239], v[28:31], v[68:71]\n"
    "ds_read_b128 v[28:31], v9 offset:23552\n"
    "v_mfma_f32_16x16x32_bf16 v[56:59], a[240:243], v[16:19], v[56:59]\n"
    "s_add_u32 s76, s76, 16384\n"
    "s_add_u32 m0, s59, 8192\n"
    "v_mfma_f32_16x16x32_bf16 v[60:63], a[244:247], v[16:19], v[60:63]\n"
    "s_waitcnt lgkmcnt(2)\n"
    "buffer_load_dwordx4 v1, s[36:39], s76 offen sc1 lds\n"
    "v_mfma_f32_16x16x32_bf16 v[68:71], a[248:251], v[16:19], v[68:71]\n"
    "ds_read_b128 v[16:19], v9 offset:24576\n"
    "v_mfma_f32_16x16x32_bf16 v[56:59], a[252:255], v[20:23], v[56:59]\n"
    "v_mfma_f32_16x16x32_bf16 v[60:63], v[80:83], v[20:23], v[60:63]\n"
    "s_waitcnt lgkmcnt(2)\n"
    "s_add_u32 s76, s76, 16384\n"
    "s_add_u32 m0, s59, 12288\n"
    "v_mfma_f32_16x16x32_bf16 v[68:71], v[84:87], v[20:23], v[68:71]\n"
    "ds_read_b128 v[20:23], v9 offset:25600\n"
    "buffer_load_dwordx4 v1, s[36:39], s76 offen sc1 lds\n"
    "v_mfma_f32_16x16x32_bf16 v[56:59], v[88:91], v[24:27], v[56:59]\n"
    "v_mfma_f32_16x16x32_bf16 v[60:63], v[92:95], v[24:27], v[60:63]\n"
    "s_waitcnt lgkmcnt(2)\n"
    "v_mfma_f32_16x16x32_bf16 v[68:71], v[96:99], v[24:27], v[68:71]\n"
    "ds_read_b128 v[24:27], v9 offset:26624\n"
    "s_add_u32 s76, s76, 16384\n"
    "v_mfma_f32_16x16x32_bf16 v[56:59], v[100:103], v[28:31], v[56:59]\n"
    "s_add_u32 m0, s59, 16384\n"
    "v_mfma_f32_16x16x32_bf16 v[60:63], v[104:107], v[28:31], v[60:63]\n"
    "s_waitcnt lgkmcnt(2)\n"
    "buffer_load_dwordx4 v1, s[36:39], s76 offen sc1 lds\n"
    "v_mfma_f32_16x16x32_bf16 v[68:71], v[108:111], v[28:31], v[68:71]\n"
    "ds_read_b128 v[28:31], v9 offset:27648\n"
    "v_mfma_f32_16x16x32_bf16 v[56:59], v[112:115], v[16:19], v[56:59]\n"
    "s_add_u32 s76, s76, 16384\n"
    "s_add_u32 m0, s59, 20480\n"
    "v_mfma_f32_16x16x32_bf16 v[60:63], v[116:119], v[16:19], v[60:63]\n"
    "s_waitcnt lgkmcnt(2)\n"
    "buffer_load_dwordx4 v1, s[36:39], s76 offen sc1 lds\n"
    "v_mfma_f32_16x16x32_bf16 v[68:71], v[120:123], v[16:19], v[68:71]\n"
    "ds_read_b128 v[16:19], v9 offset:28672\n"
    "v_mfma_f32_16x16x32_bf16 v[56:59], v[124:127], v[20:23], v[56:59]\n"
    "v_mfma_f32_16x16x32_bf16 v[60:63], v[128:131], v[20:23], v[60:63]\n"
    "s_waitcnt lgkmcnt(2)\n"
    "s_add_u32 s76, s76, 16384\n"
    "s_add_u32 m0, s59, 24576\n"
    "v_mfma_f32_16x16x32_bf16 v[68:71], v[132:135], v[20:23], v[68:71]\n"
    "ds_read_b128 v[20:23], v9 offset:29696\n"
    "buffer_load_dwordx4 v1, s[36:39], s76 offen sc1 lds\n"
    "v_mfma_f32_16x16x32_bf16 v[56:59], v[136:139], v[24:27], v[56:59]\n"
    "v_mfma_f32_16x16x32_bf16 v[60:63], v[140:143], v[24:27], v[60:63]\n"
    "s_waitcnt lgkmcnt(2)\n"
    "v_mfma_f32_16x16x32_bf16 v[68:71], v[144:147], v[24:27], v[68:71]\n"
    "ds_read_b128 v[24:27], v9 offset:30720\n"
    "s_add_u32 s76, s76, 16384\n"
    "v_mfma_f32_16x16x32_bf16 v[56:59], v[148:151], v[28:31], v[56:59]\n"
    "s_add_u32 m0, s59, 28672\n"
    "v_mfma_f32_16x16x32_bf16 v[60:63], v[152:155], v[28:31], v[60:63]\n"
    "s_waitcnt lgkmcnt(2)\n"
    "buffer_load_dwordx4 v1, s[36:39], s76 offen sc1 lds\n"
    "v_mfma_f32_16x16x32_bf16 v[68:71], v[156:159], v[28:31], v[68:71]\n"
    "ds_read_b128 v[28:31], v9 offset:31744\n"
    "v_mfma_f32_16x16x32_bf16 v[56:59], v[160:163], v[16:19], v[56:59]\n"
    "s_add_u32 s76, s76, 16384\n"
    "s_add_u32 m0, s59, 32768\n"
    "v_mfma_f32_16x16x32_bf16 v[60:63], v[164:167], v[16:19], v[60:63]\n"
    "s_waitcnt lgkmcnt(2)\n"
    "buffer_load_dwordx4 v1, s[36:39], s76 offen sc1 lds\n"
    "v_mfma_f32_16x16x32_bf16 v[68:71], v[168:171], v[16:19], v[68:71]\n"
    "ds_read_b128 v[16:19], v9 offset:32768\n"
    "v_mfma_f32_16x16x32_bf16 v[56:59], v[172:175], v[20:23], v[56:59]\n"
    "v_mfma_f32_16x16x32_bf16 v[60:63], v[176:179], v[20:23], v[60:63]\n"
    "s_waitcnt lgkmcnt(2)\n"
    "v_mfma_f32_16x16x32_bf16 v[68:71], v[180:183], v[20:23], v[68:71]\n"
    "ds_read_b128 v[20:23], v9 offset:33792\n"
    "v_mfma_f32_16x16x32_bf16 v[56:59], v[184:187], v[24:27], v[56:59]\n"
    "v_mfma_f32_16x16x32_bf16 v[60:63], v[188:191], v[24:27], v[60:63]\n"
    "s_waitcnt lgkmcnt(2)\n"
    "v_mfma_f32_16x16x32_bf16 v[68:71], v[192:195], v[24:27], v[68:71]\n"
    "ds_read_b128 v[24:27], v9 offset:34816\n"
    "v_mfma_f32_16x16x32_bf16 v[56:59], v[196:199], v[28:31], v[56:59]\n"
    "v_mfma_f32_16x16x32_bf16 v[60:63], v[200:203], v[28:31], v[60:63]\n"
    "s_waitcnt lgkmcnt(2)\n"
    "v_mfma_f32_16x16x32_bf16 v[68:71], v[204:207], v[28:31], v[68:71]\n"
    "ds_read_b128 v[28:31], v9 offset:35840\n"
    "v_mfma_f32_16x16x32_bf16 v[56:59], v[208:211], v[16:19], v[56:59]\n"
    "v_mfma_f32_16x16x32_bf16 v[60:63], v[212:215], v[16:19], v[60:63]\n"
    "s_waitcnt lgkmcnt(2)\n"
    "v_mfma_f32_16x16x32_bf16 v[68:71], v[216:219], v[16:19], v[68:71]\n"
    "v_mfma_f32_16x16x32_bf16 v[56:59], v[220:223], v[20:23], v[56:59]\n"
    "v_mfma_f32_16x16x32_bf16 v[60:63], v[224:227], v[20:23], v[60:63]\n"
    "s_waitcnt lgkmcnt(1)\n"
    "v_mfma_f32_16x16x32_bf16 v[68:71], v[228:231], v[20:23], v[68:71]\n"
    "v_mfma_f32_16x16x32_bf16 v[56:59], v[232:235], v[24:27], v[56:59]\n"
    "v_mfma_f32_16x16x32_bf16 v[60:63], v[236:239], v[24:27], v[60:63]\n"
    "s_waitcnt lgkmcnt(0)\n"
    "v_mfma_f32_16x16x32_bf16 v[68:71], v[240:243], v[24:27], v[68:71]\n"
    "v_mfma_f32_16x16x32_bf16 v[56:59], v[244:247], v[28:31], v[56:59]\n"
    "v_mfma_f32_16x16x32_bf16 v[60:63], v[248:251], v[28:31], v[60:63]\n"
    "v_mfma_f32_16x16x32_bf16 v[68:71], v[252:255], v[28:31], v[68:71]\n"
    "v_cvt_pk_bf16_f32 v56, v56, v57\n"
    "v_cvt_pk_bf16_f32 v57, v58, v59\n"
    "s_nop 1\n"
    "v_cvt_pk_bf16_f32 v58, v60, v61\n"
    "v_cvt_pk_bf16_f32 v59, v62, v63\n"
    "buffer_store_dwordx4 v[56:59], v15, s[36:39], s73 offen offset:1024 sc1\n"
    "s_nop 0\n"
    "v_cvt_pk_bf16_f32 v68, v68, v69\n"
    "v_cvt_pk_bf16_f32 v69, v70, v71\n"
    "s_nop 0\n"
    "v_permlane16_swap_b32 v68, v70\n"
    "v_permlane16_swap_b32 v69, v71\n"
    "buffer_store_dwordx4 v[68:71], v4, s[36:39], s73 offen offset:1024 sc1\n"
    "v_mov_b32 v12, s68\n"
    "s_mov_b32 s73, 0x80000000\n"
    "s_mov_b32 s68, s71\n"
    "s_mov_b32 s69, 0\n"
    "s_mov_b32 s70, 0x80000000\n"
    "v_mov_b32 v7, v12\n"
    "s_waitcnt vmcnt(12)\n"
    "s_add_u32 s71, s68, 1\n"
    "v_add_u32 v13, v11, v3\n"
    "v_cmp_gt_i32 vcc, s71, v13\n"
    "s_nop 0\n"
    "s_or_b64 exec, vcc, s[62:63]\n"
    "buffer_atomic_smax v11, v2, s[36:39], 0 offen sc0 sc1\n"
    "s_mov_b64 exec, -1\n"
    "s_waitcnt vmcnt(3)\n"
    "s_branch .LgDOWN_bar0_%=\n"
    ".LgDOWN_dma1_75_%=:\n"
    "v_mfma_f32_16x16x32_bf16 v[56:59], v[124:127], v[20:23], v[56:59]\n"
    "s_add_u32 m0, s59, 0\n"
    "v_mfma_f32_16x16x32_bf16 v[60:63], v[128:131], v[20:23], v[60:63]\n"
    "s_waitcnt lgkmcnt(2)\n"
    "buffer_load_dwordx4 v1, s[36:39], s76 offen sc1 lds\n"
    "v_mfma_f32_16x16x32_bf16 v[68:71], v[132:135], v[20:23], v[68:71]\n"
    "ds_read_b128 v[20:23], v9 offset:29696\n"
    "s_add_u32 s76, s76, 16384\n"
    "v_mfma_f32_16x16x32_bf16 v[56:59], v[136:139], v[24:27], v[56:59]\n"
    "s_add_u32 m0, s59, 4096\n"
    "v_mfma_f32_16x16x32_bf16 v[60:63], v[140:143], v[24:27], v[60:63]\n"
    "s_waitcnt lgkmcnt(2)\n"
    "buffer_load_dwordx4 v1, s[36:39], s76 offen sc1 lds\n"
    "v_mfma_f32_16x16x32_bf16 v[68:71], v[144:147], v[24:27], v[68:71]\n"
    "ds_read_b128 v[24:27], v9 offset:30720\n"
    "v_mfma_f32_16x16x32_bf16 v[56:59], v[148:151], v[28:31], v[56:59]\n"
    "s_add_u32 s76, s76, 16384\n"
    "s_add_u32 m0, s59, 8192\n"
    "v_mfma_f32_16x16x32_bf16 v[60:63], v[152:155], v[28:31], v[60:63]\n"
    "s_waitcnt lgkmcnt(2)\n"
    "buffer_load_dwordx4 v1, s[36:39], s76 offen sc1 lds\n"
    "v_mfma_f32_16x16x32_bf16 v[68:71], v[156:159], v[28:31], v[68:71]\n"
    "ds_read_b128 v[28:31], v9 offset:31744\n"
    "v_mfma_f32_16x16x32_bf16 v[56:59], v[160:163], v[16:19], v[56:59]\n"
    "v_mfma_f32_16x16x32_bf16 v[60:63], v[164:167], v[16:19], v[60:63]\n"
    "s_waitcnt lgkmcnt(2)\n"
    "s_add_u32 s76, s76, 16384\n"
    "s_add_u32 m0, s59, 12288\n"
    "v_mfma_f32_16x16x32_bf16 v[68:71], v[168:171], v[16:19], v[68:71]\n"
    "ds_read_b128 v[16:19], v9 offset:32768\n"
    "buffer_load_dwordx4 v1, s[36:39], s76 offen sc1 lds\n"
    "v_mfma_f32_16x16x32_bf16 v[56:59], v[172:175], v[20:23], v[56:59]\n"
    "v_mfma_f32_16x16x32_bf16 v[60:63], v[176:179], v[20:23], v[60:63]\n"
    "s_waitcnt lgkmcnt(2)\n"
    "v_mfma_f32_16x16x32_bf16 v[68:71], v[180:183], v[20:23], v[68:71]\n"
    "ds_read_b128 v[20:23], v9 offset:33792\n"
    "s_add_u32 s76, s76, 16384\n"
    "v_mfma_f32_16x16x32_bf16 v[56:59], v[184:187], v[24:27], v[56:59]\n"
    "s_add_u32 m0, s59, 16384\n"
    "v_mfma_f32_16x16x32_bf16 v[60:63], v[188:191], v[24:27], v[60:63]\n"
    "s_waitcnt lgkmcnt(2)\n"
    "buffer_load_dwordx4 v1, s[36:39], s76 offen sc1 lds\n"
    "v_mfma_f32_16x16x32_bf16 v[68:71], v[192:195], v[24:27], v[68:71]\n"
    "ds_read_b128 v[24:27], v9 offset:34816\n"
    "v_mfma_f32_16x16x32_bf16 v[56:59], v[196:199], v[28:31], v[56:59]\n"
    "s_add_u32 s76, s76, 16384\n"
    "s_add_u32 m0, s59, 20480\n"
    "v_mfma_f32_16x16x32_bf16 v[60:63], v[200:203], v[28:31], v[60:63]\n"
    "s_waitcnt lgkmcnt(2)\n"
    "buffer_load_dwordx4 v1, s[36:39], s76 offen sc1 lds\n"
    "v_mfma_f32_16x16x32_bf16 v[68:71], v[204:207], v[28:31], v[68:71]\n"
    "ds_read_b128 v[28:31], v9 offset:35840\n"
    "v_mfma_f32_16x16x32_bf16 v[56:59], v[208:211], v[16:19], v[56:59]\n"
    "v_mfma_f32_16x16x32_bf16 v[60:63], v[212:215], v[16:19], v[60:63]\n"
    "s_waitcnt lgkmcnt(2)\n"
    "s_add_u32 s76, s76, 16384\n"
    "s_add_u32 m0, s59, 24576\n"
    "v_mfma_f32_16x16x32_bf16 v[68:71], v[216:219], v[16:19], v[68:71]\n"
    "buffer_load_dwordx4 v1, s[36:39], s76 offen sc1 lds\n"
    "v_mfma_f32_16x16x32_bf16 v[56:59], v[220:223], v[20:23], v[56:59]\n"
    "v_mfma_f32_16x16x32_bf16 v[60:63], v[224:227], v[20:23], v[60:63]\n"
    "s_waitcnt lgkmcnt(1)\n"
    "v_mfma_f32_16x16x32_bf16 v[68:71], v[228:231], v[20:23], v[68:71]\n"
    "s_add_u32 s76, s76, 16384\n"
    "v_mfma_f32_16x16x32_bf16 v[56:59], v[232:235], v[24:27], v[56:59]\n"
    "s_add_u32 m0, s59, 28672\n"
    "v_mfma_f32_16x16x32_bf16 v[60:63], v[236:239], v[24:27], v[60:63]\n"
    "s_waitcnt lgkmcnt(0)\n"
    "buffer_load_dwordx4 v1, s[36:39], s76 offen sc1 lds\n"
    "v_mfma_f32_16x16x32_bf16 v[68:71], v[240:243], v[24:27], v[68:71]\n"
    "v_mfma_f32_16x16x32_bf16 v[56:59], v[244:247], v[28:31], v[56:59]\n"
    "s_add_u32 s76, s76, 16384\n"
    "s_add_u32 m0, s59, 32768\n"
    "v_mfma_f32_16x16x32_bf16 v[60:63], v[248:251], v[28:31], v[60:63]\n"
    "buffer_load_dwordx4 v1, s[36:39], s76 offen sc1 lds\n"
    "v_mfma_f32_16x16x32_bf16 v[68:71], v[252:255], v[28:31], v[68:71]\n"
    "v_cvt_pk_bf16_f32 v56, v56, v57\n"
    "v_cvt_pk_bf16_f32 v57, v58, v59\n"
    "s_nop 1\n"
    "v_cvt_pk_bf16_f32 v58, v60, v61\n"
    "v_cvt_pk_bf16_f32 v59, v62, v63\n"
    "buffer_store_dwordx4 v[56:59], v15, s[36:39], s73 offen offset:1024 sc1\n"
    "s_nop 0\n"
    "v_cvt_pk_bf16_f32 v68, v68, v69\n"
    "v_cvt_pk_bf16_f32 v69, v70, v71\n"
    "s_nop 0\n"
    "v_permlane16_swap_b32 v68, v70\n"
    "v_permlane16_swap_b32 v69, v71\n"
    "buffer_store_dwordx4 v[68:71], v4, s[36:39], s73 offen offset:1024 sc1\n"
    "v_mov_b32 v12, s68\n"
    "s_mov_b32 s73, 0x80000000\n"
    "s_mov_b32 s68, s71\n"
    "s_mov_b32 s69, 0\n"
    "s_mov_b32 s70, 0x80000000\n"
    "v_mov_b32 v7, v12\n"
    "s_waitcnt vmcnt(12)\n"
    "s_add_u32 s71, s68, 1\n"
    "v_add_u32 v13, v11, v3\n"
    "v_cmp_gt_i32 vcc, s71, v13\n"
    "s_nop 0\n"
    "s_or_b64 exec, vcc, s[62:63]\n"
    "buffer_atomic_smax v11, v2, s[36:39], 0 offen sc0 sc1\n"
    "s_mov_b64 exec, -1\n"
    "s_waitcnt vmcnt(3)\n"
    "s_branch .LgDOWN_bar0_%=\n"
    ".LgDOWN_dma1_96_%=:\n"
    "v_mfma_f32_16x16x32_bf16 v[56:59], v[208:211], v[16:19], v[56:59]\n"
    "s_add_u32 m0, s59, 0\n"
    "v_mfma_f32_16x16x32_bf16 v[60:63], v[212:215], v[16:19], v[60:63]\n"
    "s_waitcnt lgkmcnt(2)\n"
    "buffer_load_dwordx4 v1, s[36:39], s76 offen sc1 lds\n"
    "v_mfma_f32_16x16x32_bf16 v[68:71], v[216:219], v[16:19], v[68:71]\n"
    "s_add_u32 s76, s76, 16384\n"
    "s_add_u32 m0, s59, 4096\n"
    "v_mfma_f32_16x16x32_bf16 v[56:59], v[220:223], v[20:23], v[56:59]\n"
    "buffer_load_dwordx4 v1, s[36:39], s76 offen sc1 lds\n"
    "v_mfma_f32_16x16x32_bf16 v[60:63], v[224:227], v[20:23], v[60:63]\n"
    "s_waitcnt lgkmcnt(1)\n"
    "v_mfma_f32_16x16x32_bf16 v[68:71], v[228:231], v[20:23], v[68:71]\n"
    "v_mfma_f32_16x16x32_bf16 v[56:59], v[232:235], v[24:27], v[56:59]\n"
    "s_add_u32 s76, s76, 16384\n"
    "s_add_u32 m0, s59, 8192\n"
    "v_mfma_f32_16x16x32_bf16 v[60:63], v[236:239], v[24:27], v[60:63]\n"
    "s_waitcnt lgkmcnt(0)\n"
    "buffer_load_dwordx4 v1, s[36:39], s76 offen sc1 lds\n"
    "v_mfma_f32_16x16x32_bf16 v[68:71], v[240:243], v[24:27], v[68:71]\n"
    "v_mfma_f32_16x16x32_bf16 v[56:59], v[244:247], v[28:31], v[56:59]\n"
    "v_mfma_f32_16x16x32_bf16 v[60:63], v[248:251], v[28:31], v[60:63]\n"
    "s_add_u32 s76, s76, 16384\n"
    "s_add_u32 m0, s59, 12288\n"
    "v_mfma_f32_16x16x32_bf16 v[68:71], v[252:255], v[28:31], v[68:71]\n"
    "buffer_load_dwordx4 v1, s[36:39], s76 offen sc1 lds\n"
    "v_cvt_pk_bf16_f32 v56, v56, v57\n"
    "v_cvt_pk_bf16_f32 v57, v58, v59\n"
    "s_nop 0\n"
    "v_cvt_pk_bf16_f32 v58, v60, v61\n"
    "v_cvt_pk_bf16_f32 v59, v62, v63\n"
    "buffer_store_dwordx4 v[56:59], v15, s[36:39], s73 offen offset:1024 sc1\n"
    "s_nop 0\n"
    "v_cvt_pk_bf16_f32 v68, v68, v69\n"
    "v_cvt_pk_bf16_f32 v69, v70, v71\n"
    "s_nop 0\n"
    "v_permlane16_swap_b32 v68, v70\n"
    "v_permlane16_swap_b32 v69, v71\n"
    "buffer_store_dwordx4 v[68:71], v4, s[36:39], s73 offen offset:1024 sc1\n"
    "v_mov_b32 v12, s68\n"
    "s_mov_b32 s73, 0x80000000\n"
    "s_mov_b32 s68, s71\n"
    "s_add_u32 m0, s59, 16384\n"
    "s_add_u32 s76, s76, 16384\n"
    "buffer_load_dwordx4 v1, s[36:39], s76 offen sc1 lds\n"
    "s_add_u32 m0, s59, 20480\n"
    "s_add_u32 s76, s76, 16384\n"
    "buffer_load_dwordx4 v1, s[36:39], s76 offen sc1 lds\n"
    "s_add_u32 m0, s59, 24576\n"
    "s_add_u32 s76, s76, 16384\n"
    "buffer_load_dwordx4 v1, s[36:39], s76 offen sc1 lds\n"
    "s_add_u32 m0, s59, 28672\n"
    "s_add_u32 s76, s76, 16384\n"
    "buffer_load_dwordx4 v1, s[36:39], s76 offen sc1 lds\n"
    "s_add_u32 m0, s59, 32768\n"
    "s_add_u32 s76, s76, 16384\n"
    "buffer_load_dwordx4 v1, s[36:39], s76 offen sc1 lds\n"
    "s_waitcnt vmcnt(5)\n"
    "buffer_atomic_smax v12, v10, s[36:39], 0 offen sc1\n"
    "s_mov_b32 s69, 0x80000000\n"
    "s_mov_b32 s70, 0x80000000\n"
    "v_mov_b32 v7, v12\n"
    "s_waitcnt vmcnt(12)\n"
    "s_add_u32 s71, s68, 1\n"
    "v_add_u32 v13, v11, v3\n"
    "v_cmp_gt_i32 vcc, s71, v13\n"
    "s_nop 0\n"
    "s_or_b64 exec, vcc, s[62:63]\n"
    "buffer_atomic_smax v11, v2, s[36:39], 0 offen sc0 sc1\n"
    "s_mov_b64 exec, -1\n"
    "s_waitcnt vmcnt(2)\n"
    "s_branch .LgDOWN_bar0_%=\n"
    ".LgDOWN_idle1_%=:\n"
    "s_or_b64 exec, vcc, s[62:63]\n"
    "buffer_atomic_smax v5, v2, s[36:39], 0 offen sc0 sc1\n"
    "s_mov_b64 exec, -1\n"
    "s_waitcnt vmcnt(1)\n"
    "buffer_atomic_smax v12, v10, s[36:39], 0 offen sc1\n"
    "s_branch .LgDOWN_block1_1_%=\n"
    ".LgDOWN_block1_%=:\n"
    "v_add_u32 v13, v11, v3\n"
    "v_cmp_gt_i32 vcc, s71, v13\n"
    "s_nop 0\n"
    "s_or_b64 exec, vcc, s[62:63]\n"
    "buffer_atomic_smax v11, v2, s[36:39], 0 offen sc0 sc1\n"
    "s_mov_b64 exec, -1\n"
    "s_sleep 3\n"
    "s_waitcnt vmcnt(1)\n"
    ".LgDOWN_block1_0_%=:\n"
    "v_add_u32 v13, v5, v3\n"
    "v_cmp_gt_i32 vcc, s71, v13\n"
    "s_cbranch_vccz .LgDOWN_go1_%=\n"
    "v_add_u32 v13, v5, v3\n"
    "v_cmp_gt_i32 vcc, s71, v13\n"
    "s_nop 0\n"
    "s_or_b64 exec, vcc, s[62:63]\n"
    "buffer_atomic_smax v5, v2, s[36:39], 0 offen sc0 sc1\n"
    "s_mov_b64 exec, -1\n"
    "s_sleep 3\n"
    "s_waitcnt vmcnt(1)\n"
    ".LgDOWN_block1_1_%=:\n"
    "v_add_u32 v13, v11, v3\n"
    "v_cmp_gt_i32 vcc, s71, v13\n"
    "s_cbranch_vccz .LgDOWN_go1_%=\n"
    "s_sub_u32 s72, s72, 1\n"
    "s_cbranch_scc0 .LgDOWN_block1_%=\n"
    "s_mov_b32 s72, 0\n"
    ".LgDOWN_go1_%=:\n"
    "s_lshr_b32 s76, s71, 1\n"
    "s_and_b32 s76, s76, s54\n"
    "s_mul_i32 s76, s76, s53\n"
    "s_add_u32 s76, s76, s52\n"
    "s_add_u32 m0, s59, 0\n"
    "s_nop 0\n"
    "buffer_load_dwordx4 v1, s[36:39], s76 offen sc1 lds\n"
    "s_add_u32 m0, s59, 4096\n"
    "s_add_u32 s76, s76, 16384\n"
    "buffer_load_dwordx4 v1, s[36:39], s76 offen sc1 lds\n"
    "s_add_u32 m0, s59, 8192\n"
    "s_add_u32 s76, s76, 16384\n"
    "buffer_load_dwordx4 v1, s[36:39], s76 offen sc1 lds\n"
    "s_add_u32 m0, s59, 12288\n"
    "s_add_u32 s76, s76, 16384\n"
    "buffer_load_dwordx4 v1, s[36:39], s76 offen sc1 lds\n"
    "s_add_u32 m0, s59, 16384\n"
    "s_add_u32 s76, s76, 16384\n"
    "buffer_load_dwordx4 v1, s[36:39], s76 offen sc1 lds\n"
    "s_add_u32 m0, s59, 20480\n"
    "s_add_u32 s76, s76, 16384\n"
    "buffer_load_dwordx4 v1, s[36:39], s76 offen sc1 lds\n"
    "s_add_u32 m0, s59, 24576\n"
    "s_add_u32 s76, s76, 16384\n"
    "buffer_load_dwordx4 v1, s[36:39], s76 offen sc1 lds\n"
    "s_add_u32 m0, s59, 28672\n"
    "s_add_u32 s76, s76, 16384\n"
    "buffer_load_dwordx4 v1, s[36:39], s76 offen sc1 lds\n"
    "s_add_u32 m0, s59, 32768\n"
    "s_add_u32 s76, s76, 16384\n"
    "buffer_load_dwordx4 v1, s[36:39], s76 offen sc1 lds\n"
    "s_mov_b32 s69, 0x80000000\n"
    "s_mov_b32 s70, 0x80000000\n"
    "v_mov_b32 v7, v12\n"
    "s_waitcnt vmcnt(9)\n"
    "s_add_u32 s71, s68, 1\n"
    "v_add_u32 v13, v11, v3\n"
    "v_cmp_gt_i32 vcc, s71, v13\n"
    "s_nop 0\n"
    "s_or_b64 exec, vcc, s[62:63]\n"
    "buffer_atomic_smax v11, v2, s[36:39], 0 offen sc0 sc1\n"
    "s_mov_b64 exec, -1\n"
    "s_waitcnt vmcnt(1)\n"
    "s_branch .LgDOWN_bar0_%=\n"
    ".LgDOWN_bar1_%=:\n"
    "s_barrier\n"
    "ds_read_b128 v[16:19], v9 offset:0\n"
    "ds_read_b128 v[20:23], v9 offset:1024\n"
    "ds_read_b128 v[24:27], v9 offset:2048\n"
    "ds_read_b128 v[28:31], v9 offset:3072\n"
    "v_add_u32 v13, v5, v3\n"
    "v_cmp_gt_i32 vcc, s71, v13\n"
    "s_nop 0\n"
    "s_or_b64 exec, vcc, s[62:63]\n"
    "buffer_atomic_smax v5, v2, s[36:39], 0 offen sc0 sc1\n"
    "s_mov_b64 exec, -1\n"
    "s_lshr_b32 s76, s71, 1\n"
    "s_and_b32 s76, s76, s54\n"
    "s_mul_i32 s76, s76, s53\n"
    "s_add_u32 s76, s76, s52\n"
    "s_waitcnt lgkmcnt(3)\n"
    "s_branch .LgDOWN_head1_%=\n"
    ".LgDOWN_FIN_%=:\n"
  );
}

DEVI void role_rec(const Params& p, int L, int j, int i, u32 dbg, int& rid, unsigned long long& tag, bool& sure, unsigned char* smem) {
  const int tid = threadIdx.x, lane = tid & 63, w = (int)rfl(tid >> 6), h = 8 * j + i;
  const int c = lane & 15, g = lane >> 4, pp = lane & 3;
  {
    v4u t;
    const int q0 = w == 0 ? 4 * (c >> 3) + (c & 3) : (c & 7);
    const u32 one = (w == 0 && ((c >> 2) & 1) != (g & 1)) ? 0u : 0x3F80u << (16 * (q0 & 1));
    const v4u t1 = {(u32)(((g & 1) * 16 + c) * 8), (u32)(g * 128 + (c & 7) * 16 + (c >> 3) * 8), c < 8 ? (u32)(g * 128 + c * 16) : OOB, (u32)((lane & 31) * 64 + (lane >> 5) * 8)};
    const v4u t2 = {(u32)(g * 16384 + c * 16), g == 0 ? (u32)(c * 16) : OOB,
                    (u32)((g >> 1) * 128 + (c & 7) * 16 + (2 * (g & 1) + (c >> 3)) * 32768), t1.w};
#pragma unroll
    for (int d = 0; d < 4; ++d) t[d] = w == 1 ? t1[d] : (w == 2 ? t2[d] : ((q0 >> 1) == d ? one : 0u));
    *(v4u*)(smem + 12288 + tid * 16) = t;
    asm volatile("s_waitcnt lgkmcnt(0)" ::: "memory");
  }
  const u32 wb = 576u * 4096 * 2 - h * 256;
  v16u sa = cat4(make_srd(p.ws, WS_END), make_srd(p.Wq + h * 128, wb), make_srd(p.Wk + h * 128, wb), make_srd(p.Wv + h * 128, wb));
  const v4u so = make_srd(p.Wo + h * 128 * 576, 128u * 1152);
  const int mine = 4 * i + j, unit = w == 0 ? mine : ((w == 1 && i == 3) ? 32 + j : -1);
  const u32 hot = HOT + L * 8192, hot2 = HOT2 + (1 - L) * 8192, img = hot2 + j * 2 * CHUNK;
  v16u sb = {so.x, so.y, so.z, so.w,
             pga(L, B_QKV + 3 * h + 1) + w * 2048, pga(L, B_KVM + h) + w * 1536, mbox_att(L, j, i, w), mbox(L, j, SL_REC + i, w),
             (u32)w * 1024, dbg + w * 64, INITF + L * 8192, hot + 4 * w * CHUNK + h * 256,
             unit < 0 ? OOB : hot + unit * CHUNK, unit < 0 ? OOB : hot2 + (unit >> 5) * CHUNK + (unit & 31) * 256, img, hot + (32 + w) * CHUNK + h * 256};
  const int jr = lane >> 3;
  const u32 row = (u32)(16 * (w >> 1) + 8 * ((jr >> 1) & 1) + 4 * (w & 1) + 2 * (jr >> 2) + (jr & 1));
  const u32 ch = (((u32)lane & 7) ^ wswz(jr, w)) << 4;
  v16u va = {0, 0, 0, 0, 0, 0, 0, 0, 0, 0, 0, 0, 0, 0, 0, 0};
  va[0] = lane * 16;
  va[1] = 16384 + w * 2048 + lane * 16;
  va[2] = (u32)((c >> 3) * 0xAAAA) | 0x4444u;
  const int qq = (lane >> 2) & 3;
  va[3] = (u32)((g >> 1) * 4096 + (g & 1) * 2048 + (4 * (qq >> 1) + (qq & 1)) * 128 + ((2 * w + (pp >> 1)) << 4) + 8 * (pp & 1));
  va[4] = row * 8192 + ch;
  va[5] = row * 1152 + ch;
  va[6] = (u32)(w >> 1) * 4096 + tr_base(lane, 4 * (w & 1) + pp);
  va[7] = (u32)w * 4096 + tr_base(lane, 4 * (qq >> 1) + (pp >> 1)) + 8 * (pp & 1);
  va[8] = hot2 + 32 * CHUNK + ((w == 0 && i < 4) ? 0 : 1);
  va[9] = (u32)(h * 64 + 4 * w);
  va[10] = hot + mine * CHUNK + w * 2048;
  va[11] = w == 3 ? hot + (32 + j) * CHUNK + i * 1024 : (w == 2 ? img + i * 1024 : ((w == 1 && i == 1) ? img + CHUNK : OOB));
  va[12] = w == 3 ? ~0u : 0u;
  va[13] = w == 3 ? 0u : ~0u;
  asm volatile("" : "+v"(va));
  if (!confirm(rid, tag, sure, smem)) return;
  va[14] = (u32)(2 * tag); va[15] = (u32)((2 * tag) >> 32);
  RUN_ASM(
    "s_nop 4\n"
    "s_getpc_b64 s[88:89]\n"
    ".Lrec_PC_%=:\n"
    "s_and_b32 s89, s89, 0xffff\n"
    "s_mov_b32 s90, .Lrec_FIN_%=-.Lrec_PC_%=\n"
    "s_mov_b32 s91, 0x20000\n"
    "v_mbcnt_lo_u32_b32 v78, -1, 0\n"
    "v_mbcnt_hi_u32_b32 v78, -1, v78\n"
    "v_lshlrev_b32 v78, 7, v78\n"
    "buffer_load_dword v79, v78, s[88:91], 0 offen\n"
    "v_add_u32 v78, 8192, v78\n"
    "buffer_load_dword v79, v78, s[88:91], 0 offen\n"
    "v_readfirstlane_b32 s76, v8\n"
    "v_readfirstlane_b32 s77, v9\n"
    "v_readfirstlane_b32 s78, v10\n"
    "v_readfirstlane_b32 s79, v11\n"
    "v_readfirstlane_b32 s80, v12\n"
    "v_readfirstlane_b32 s81, v13\n"
    "v_readfirstlane_b32 s30, v14\n"
    "v_readfirstlane_b32 s31, v15\n"
    "s_memtime s[34:35]\n"
    "s_memrealtime s[92:93]\n"
    "s_mov_b32 s69, 262144\n"
    "s_add_u32 m0, s60, 16384\n"
    "s_mov_b32 s73, 0\n"
    "buffer_load_dwordx4 v4, s[40:43], s73 offen lds\n"
    "s_add_u32 m0, s60, 20480\n"
    "s_mov_b32 s73, 128\n"
    "buffer_load_dwordx4 v4, s[40:43], s73 offen lds\n"
    "s_add_u32 m0, s60, 24576\n"
    "s_mov_b32 s73, 0\n"
    "buffer_load_dwordx4 v4, s[44:47], s73 offen lds\n"
    "s_add_u32 m0, s60, 28672\n"
    "s_mov_b32 s73, 128\n"
    "buffer_load_dwordx4 v4, s[44:47], s73 offen lds\n"
    "s_add_u32 m0, s60, 32768\n"
    "s_mov_b32 s73, 0\n"
    "buffer_load_dwordx4 v4, s[48:51], s73 offen lds\n"
    "s_add_u32 m0, s60, 36864\n"
    "s_mov_b32 s73, 128\n"
    "buffer_load_dwordx4 v4, s[48:51], s73 offen lds\n"
    "s_add_u32 m0, s60, 40960\n"
    "s_mov_b32 s73, 262144\n"
    "buffer_load_dwordx4 v4, s[40:43], s73 offen lds\n"
    "s_add_u32 m0, s60, 45056\n"
    "s_mov_b32 s73, 262272\n"
    "buffer_load_dwordx4 v4, s[40:43], s73 offen lds\n"
    "s_add_u32 m0, s60, 49152\n"
    "s_mov_b32 s73, 262144\n"
    "buffer_load_dwordx4 v4, s[44:47], s73 offen lds\n"
    "s_add_u32 m0, s60, 53248\n"
    "s_mov_b32 s73, 262272\n"
    "buffer_load_dwordx4 v4, s[44:47], s73 offen lds\n"
    "s_add_u32 m0, s60, 57344\n"
    "s_mov_b32 s73, 262144\n"
    "buffer_load_dwordx4 v4, s[48:51], s73 offen lds\n"
    "s_add_u32 m0, s60, 61440\n"
    "s_mov_b32 s73, 262272\n"
    "buffer_load_dwordx4 v4, s[48:51], s73 offen lds\n"
    "s_add_u32 m0, s60, 65536\n"
    "s_mov_b32 s73, 524288\n"
    "buffer_load_dwordx4 v4, s[40:43], s73 offen lds\n"
    "s_add_u32 m0, s60, 69632\n"
    "s_mov_b32 s73, 524416\n"
    "buffer_load_dwordx4 v4, s[40:43], s73 offen lds\n"
    "s_add_u32 m0, s60, 73728\n"
    "s_mov_b32 s73, 524288\n"
    "buffer_load_dwordx4 v4, s[44:47], s73 offen lds\n"
    "s_add_u32 m0, s60, 77824\n"
    "s_mov_b32 s73, 524416\n"
    "buffer_load_dwordx4 v4, s[44:47], s73 offen lds\n"
    "s_add_u32 m0, s60, 81920\n"
    "s_mov_b32 s73, 524288\n"
    "buffer_load_dwordx4 v4, s[48:51], s73 offen lds\n"
    "s_add_u32 m0, s60, 86016\n"
    "s_mov_b32 s73, 524416\n"
    "buffer_load_dwordx4 v4, s[48:51], s73 offen lds\n"
    "s_add_u32 m0, s60, 90112\n"
    "s_mov_b32 s73, 786432\n"
    "buffer_load_dwordx4 v4, s[40:43], s73 offen lds\n"
    "s_add_u32 m0, s60, 94208\n"
    "s_mov_b32 s73, 786560\n"
    "buffer_load_dwordx4 v4, s[40:43], s73 offen lds\n"
    "s_add_u32 m0, s60, 98304\n"
    "s_mov_b32 s73, 786432\n"
    "buffer_load_dwordx4 v4, s[44:47], s73 offen lds\n"
    "s_add_u32 m0, s60, 102400\n"
    "s_mov_b32 s73, 786560\n"
    "buffer_load_dwordx4 v4, s[44:47], s73 offen lds\n"
    "s_add_u32 m0, s60, 106496\n"
    "s_mov_b32 s73, 786432\n"
    "buffer_load_dwordx4 v4, s[48:51], s73 offen lds\n"
    "s_add_u32 m0, s60, 110592\n"
    "s_mov_b32 s73, 786560\n"
    "buffer_load_dwordx4 v4, s[48:51], s73 offen lds\n"
    "s_add_u32 m0, s60, 114688\n"
    "s_mov_b32 s73, 1048576\n"
    "buffer_load_dwordx4 v4, s[40:43], s73 offen lds\n"
    "s_add_u32 m0, s60, 118784\n"
    "s_mov_b32 s73, 1048704\n"
    "buffer_load_dwordx4 v4, s[40:43], s73 offen lds\n"
    "s_add_u32 m0, s60, 122880\n"
    "s_mov_b32 s73, 1048576\n"
    "buffer_load_dwordx4 v4, s[44:47], s73 offen lds\n"
    "s_add_u32 m0, s60, 126976\n"
    "s_mov_b32 s73, 1048704\n"
    "buffer_load_dwordx4 v4, s[44:47], s73 offen lds\n"
    "s_add_u32 m0, s60, 131072\n"
    "s_mov_b32 s73, 1048576\n"
    "buffer_load_dwordx4 v4, s[48:51], s73 offen lds\n"
    "s_add_u32 m0, s60, 135168\n"
    "s_mov_b32 s73, 1048704\n"
    "buffer_load_dwordx4 v4, s[48:51], s73 offen lds\n"
    "s_add_u32 m0, s60, 139264\n"
    "s_mov_b32 s73, 1310720\n"
    "buffer_load_dwordx4 v4, s[40:43], s73 offen lds\n"
    "s_add_u32 m0, s60, 143360\n"
    "s_mov_b32 s73, 1310848\n"
    "buffer_load_dwordx4 v4, s[40:43], s73 offen lds\n"
    "s_add_u32 m0, s60, 147456\n"
    "s_mov_b32 s73, 1310720\n"
    "buffer_load_dwordx4 v4, s[44:47], s73 offen lds\n"
    "s_add_u32 m0, s60, 151552\n"
    "s_mov_b32 s73, 1310848\n"
    "buffer_load_dwordx4 v4, s[44:47], s73 offen lds\n"
    "s_add_u32 m0, s60, 155648\n"
    "s_mov_b32 s73, 1310720\n"
    "buffer_load_dwordx4 v4, s[48:51], s73 offen lds\n"
    "s_add_u32 m0, s60, 159744\n"
    "s_mov_b32 s73, 1310848\n"
    "buffer_load_dwordx4 v4, s[48:51], s73 offen lds\n"
    "s_waitcnt vmcnt(30) lgkmcnt(0)\n"
    "s_barrier\n"
    "v_add_u32 v8, 16384, v6\n"
    "ds_read_b64_tr_b16 a[0:1], v8 offset:0\n"
    "ds_read_b64_tr_b16 a[2:3], v8 offset:512\n"
    "ds_read_b64_tr_b16 a[8:9], v8 offset:8\n"
    "ds_read_b64_tr_b16 a[10:11], v8 offset:520\n"
    "ds_read_b64_tr_b16 a[144:145], v8 offset:8192\n"
    "ds_read_b64_tr_b16 a[146:147], v8 offset:8704\n"
    "ds_read_b64_tr_b16 a[152:153], v8 offset:8200\n"
    "ds_read_b64_tr_b16 a[154:155], v8 offset:8712\n"
    "ds_read_b64_tr_b16 v[112:113], v8 offset:16384\n"
    "ds_read_b64_tr_b16 v[114:115], v8 offset:16896\n"
    "ds_read_b64_tr_b16 v[224:225], v8 offset:16392\n"
    "ds_read_b64_tr_b16 v[226:227], v8 offset:16904\n"
    "s_waitcnt vmcnt(24) lgkmcnt(0)\n"
    "s_barrier\n"
    "s_add_u32 m0, s60, 16384\n"
    "s_mov_b32 s73, 1572864\n"
    "buffer_load_dwordx4 v4, s[40:43], s73 offen lds\n"
    "s_add_u32 m0, s60, 20480\n"
    "s_mov_b32 s73, 1572992\n"
    "buffer_load_dwordx4 v4, s[40:43], s73 offen lds\n"
    "s_add_u32 m0, s60, 24576\n"
    "s_mov_b32 s73, 1572864\n"
    "buffer_load_dwordx4 v4, s[44:47], s73 offen lds\n"
    "s_add_u32 m0, s60, 28672\n"
    "s_mov_b32 s73, 1572992\n"
    "buffer_load_dwordx4 v4, s[44:47], s73 offen lds\n"
    "s_add_u32 m0, s60, 32768\n"
    "s_mov_b32 s73, 1572864\n"
    "buffer_load_dwordx4 v4, s[48:51], s73 offen lds\n"
    "s_add_u32 m0, s60, 36864\n"
    "s_mov_b32 s73, 1572992\n"
    "buffer_load_dwordx4 v4, s[48:51], s73 offen lds\n"
    "v_add_u32 v8, 40960, v6\n"
    "ds_read_b64_tr_b16 a[4:5], v8 offset:0\n"
    "ds_read_b64_tr_b16 a[6:7], v8 offset:512\n"
    "ds_read_b64_tr_b16 a[12:13], v8 offset:8\n"
    "ds_read_b64_tr_b16 a[14:15], v8 offset:520\n"
    "ds_read_b64_tr_b16 a[148:149], v8 offset:8192\n"
    "ds_read_b64_tr_b16 a[150:151], v8 offset:8704\n"
    "ds_read_b64_tr_b16 a[156:157], v8 offset:8200\n"
    "ds_read_b64_tr_b16 a[158:159], v8 offset:8712\n"
    "ds_read_b64_tr_b16 v[116:117], v8 offset:16384\n"
    "ds_read_b64_tr_b16 v[118:119], v8 offset:16896\n"
    "ds_read_b64_tr_b16 v[228:229], v8 offset:16392\n"
    "ds_read_b64_tr_b16 v[230:231], v8 offset:16904\n"
    "s_waitcnt vmcnt(24) lgkmcnt(0)\n"
    "s_barrier\n"
    "s_add_u32 m0, s60, 40960\n"
    "s_mov_b32 s73, 1835008\n"
    "buffer_load_dwordx4 v4, s[40:43], s73 offen lds\n"
    "s_add_u32 m0, s60, 45056\n"
    "s_mov_b32 s73, 1835136\n"
    "buffer_load_dwordx4 v4, s[40:43], s73 offen lds\n"
    "s_add_u32 m0, s60, 49152\n"
    "s_mov_b32 s73, 1835008\n"
    "buffer_load_dwordx4 v4, s[44:47], s73 offen lds\n"
    "s_add_u32 m0, s60, 53248\n"
    "s_mov_b32 s73, 1835136\n"
    "buffer_load_dwordx4 v4, s[44:47], s73 offen lds\n"
    "s_add_u32 m0, s60, 57344\n"
    "s_mov_b32 s73, 1835008\n"
    "buffer_load_dwordx4 v4, s[48:51], s73 offen lds\n"
    "s_add_u32 m0, s60, 61440\n"
    "s_mov_b32 s73, 1835136\n"
    "buffer_load_dwordx4 v4, s[48:51], s73 offen lds\n"
    "v_add_u32 v8, 65536, v6\n"
    "ds_read_b64_tr_b16 a[16:17], v8 offset:0\n"
    "ds_read_b64_tr_b16 a[18:19], v8 offset:512\n"
    "ds_read_b64_tr_b16 a[24:25], v8 offset:8\n"
    "ds_read_b64_tr_b16 a[26:27], v8 offset:520\n"
    "ds_read_b64_tr_b16 a[160:161], v8 offset:8192\n"
    "ds_read_b64_tr_b16 a[162:163], v8 offset:8704\n"
    "ds_read_b64_tr_b16 a[168:169], v8 offset:8200\n"
    "ds_read_b64_tr_b16 a[170:171], v8 offset:8712\n"
    "ds_read_b64_tr_b16 v[120:121], v8 offset:16384\n"
    "ds_read_b64_tr_b16 v[122:123], v8 offset:16896\n"
    "ds_read_b64_tr_b16 v[232:233], v8 offset:16392\n"
    "ds_read_b64_tr_b16 v[234:235], v8 offset:16904\n"
    "s_waitcnt vmcnt(24) lgkmcnt(0)\n"
    "s_barrier\n"
    "s_add_u32 m0, s60, 65536\n"
    "s_mov_b32 s73, 2097152\n"
    "buffer_load_dwordx4 v4, s[40:43], s73 offen lds\n"
    "s_add_u32 m0, s60, 69632\n"
    "s_mov_b32 s73, 2097280\n"
    "buffer_load_dwordx4 v4, s[40:43], s73 offen lds\n"
    "s_add_u32 m0, s60, 73728\n"
    "s_mov_b32 s73, 2097152\n"
    "buffer_load_dwordx4 v4, s[44:47], s73 offen lds\n"
    "s_add_u32 m0, s60, 77824\n"
    "s_mov_b32 s73, 2097280\n"
    "buffer_load_dwordx4 v4, s[44:47], s73 offen lds\n"
    "s_add_u32 m0, s60, 81920\n"
    "s_mov_b32 s73, 2097152\n"
    "buffer_load_dwordx4 v4, s[48:51], s73 offen lds\n"
    "s_add_u32 m0, s60, 86016\n"
    "s_mov_b32 s73, 2097280\n"
    "buffer_load_dwordx4 v4, s[48:51], s73 offen lds\n"
    "v_add_u32 v8, 90112, v6\n"
    "ds_read_b64_tr_b16 a[20:21], v8 offset:0\n"
    "ds_read_b64_tr_b16 a[22:23], v8 offset:512\n"
    "ds_read_b64_tr_b16 a[28:29], v8 offset:8\n"
    "ds_read_b64_tr_b16 a[30:31], v8 offset:520\n"
    "ds_read_b64_tr_b16 a[164:165], v8 offset:8192\n"
    "ds_read_b64_tr_b16 a[166:167], v8 offset:8704\n"
    "ds_read_b64_tr_b16 a[172:173], v8 offset:8200\n"
    "ds_read_b64_tr_b16 a[174:175], v8 offset:8712\n"
    "ds_read_b64_tr_b16 v[124:125], v8 offset:16384\n"
    "ds_read_b64_tr_b16 v[126:127], v8 offset:16896\n"
    "ds_read_b64_tr_b16 v[236:237], v8 offset:16392\n"
    "ds_read_b64_tr_b16 v[238:239], v8 offset:16904\n"
    "s_waitcnt vmcnt(24) lgkmcnt(0)\n"
    "s_barrier\n"
    "s_add_u32 m0, s60, 90112\n"
    "s_mov_b32 s73, 2359296\n"
    "buffer_load_dwordx4 v4, s[40:43], s73 offen lds\n"
    "s_add_u32 m0, s60, 94208\n"
    "s_mov_b32 s73, 2359424\n"
    "buffer_load_dwordx4 v4, s[40:43], s73 offen lds\n"
    "s_add_u32 m0, s60, 98304\n"
    "s_mov_b32 s73, 2359296\n"
    "buffer_load_dwordx4 v4, s[44:47], s73 offen lds\n"
    "s_add_u32 m0, s60, 102400\n"
    "s_mov_b32 s73, 2359424\n"
    "buffer_load_dwordx4 v4, s[44:47], s73 offen lds\n"
    "s_add_u32 m0, s60, 106496\n"
    "s_mov_b32 s73, 2359296\n"
    "buffer_load_dwordx4 v4, s[48:51], s73 offen lds\n"
    "s_add_u32 m0, s60, 110592\n"
    "s_mov_b32 s73, 2359424\n"
    "buffer_load_dwordx4 v4, s[48:51], s73 offen lds\n"
    "v_add_u32 v8, 114688, v6\n"
    "ds_read_b64_tr_b16 a[32:33], v8 offset:0\n"
    "ds_read_b64_tr_b16 a[34:35], v8 offset:512\n"
    "ds_read_b64_tr_b16 a[40:41], v8 offset:8\n"
    "ds_read_b64_tr_b16 a[42:43], v8 offset:520\n"
    "ds_read_b64_tr_b16 a[176:177], v8 offset:8192\n"
    "ds_read_b64_tr_b16 a[178:179], v8 offset:8704\n"
    "ds_read_b64_tr_b16 a[184:185], v8 offset:8200\n"
    "ds_read_b64_tr_b16 a[186:187], v8 offset:8712\n"
    "ds_read_b64_tr_b16 v[128:129], v8 offset:16384\n"
    "ds_read_b64_tr_b16 v[130:131], v8 offset:16896\n"
    "ds_read_b64_tr_b16 v[240:241], v8 offset:16392\n"
    "ds_read_b64_tr_b16 v[242:243], v8 offset:16904\n"
    "s_waitcnt vmcnt(24) lgkmcnt(0)\n"
    "s_barrier\n"
    "s_add_u32 m0, s60, 114688\n"
    "s_mov_b32 s73, 2621440\n"
    "buffer_load_dwordx4 v4, s[40:43], s73 offen lds\n"
    "s_add_u32 m0, s60, 118784\n"
    "s_mov_b32 s73, 2621568\n"
    "buffer_load_dwordx4 v4, s[40:43], s73 offen lds\n"
    "s_add_u32 m0, s60, 122880\n"
    "s_mov_b32 s73, 2621440\n"
    "buffer_load_dwordx4 v4, s[44:47], s73 offen lds\n"
    "s_add_u32 m0, s60, 126976\n"
    "s_mov_b32 s73, 2621568\n"
    "buffer_load_dwordx4 v4, s[44:47], s73 offen lds\n"
    "s_add_u32 m0, s60, 131072\n"
    "s_mov_b32 s73, 2621440\n"
    "buffer_load_dwordx4 v4, s[48:51], s73 offen lds\n"
    "s_add_u32 m0, s60, 135168\n"
    "s_mov_b32 s73, 2621568\n"
    "buffer_load_dwordx4 v4, s[48:51], s73 offen lds\n"
    "v_add_u32 v8, 139264, v6\n"
    "ds_read_b64_tr_b16 a[36:37], v8 offset:0\n"
    "ds_read_b64_tr_b16 a[38:39], v8 offset:512\n"
    "ds_read_b64_tr_b16 a[44:45], v8 offset:8\n"
    "ds_read_b64_tr_b16 a[46:47], v8 offset:520\n"
    "ds_read_b64_tr_b16 a[180:181], v8 offset:8192\n"
    "ds_read_b64_tr_b16 a[182:183], v8 offset:8704\n"
    "ds_read_b64_tr_b16 a[188:189], v8 offset:8200\n"
    "ds_read_b64_tr_b16 a[190:191], v8 offset:8712\n"
    "ds_read_b64_tr_b16 v[132:133], v8 offset:16384\n"
    "ds_read_b64_tr_b16 v[134:135], v8 offset:16896\n"
    "ds_read_b64_tr_b16 v[244:245], v8 offset:16392\n"
    "ds_read_b64_tr_b16 v[246:247], v8 offset:16904\n"
    "s_waitcnt vmcnt(24) lgkmcnt(0)\n"
    "s_barrier\n"
    "s_add_u32 m0, s60, 139264\n"
    "s_mov_b32 s73, 2883584\n"
    "buffer_load_dwordx4 v4, s[40:43], s73 offen lds\n"
    "s_add_u32 m0, s60, 143360\n"
    "s_mov_b32 s73, 2883712\n"
    "buffer_load_dwordx4 v4, s[40:43], s73 offen lds\n"
    "s_add_u32 m0, s60, 147456\n"
    "s_mov_b32 s73, 2883584\n"
    "buffer_load_dwordx4 v4, s[44:47], s73 offen lds\n"
    "s_add_u32 m0, s60, 151552\n"
    "s_mov_b32 s73, 2883712\n"
    "buffer_load_dwordx4 v4, s[44:47], s73 offen lds\n"
    "s_add_u32 m0, s60, 155648\n"
    "s_mov_b32 s73, 2883584\n"
    "buffer_load_dwordx4 v4, s[48:51], s73 offen lds\n"
    "s_add_u32 m0, s60, 159744\n"
    "s_mov_b32 s73, 2883712\n"
    "buffer_load_dwordx4 v4, s[48:51], s73 offen lds\n"
    "v_add_u32 v8, 16384, v6\n"
    "ds_read_b64_tr_b16 a[48:49], v8 offset:0\n"
    "ds_read_b64_tr_b16 a[50:51], v8 offset:512\n"
    "ds_read_b64_tr_b16 a[56:57], v8 offset:8\n"
    "ds_read_b64_tr_b16 a[58:59], v8 offset:520\n"
    "ds_read_b64_tr_b16 a[192:193], v8 offset:8192\n"
    "ds_read_b64_tr_b16 a[194:195], v8 offset:8704\n"
    "ds_read_b64_tr_b16 a[200:201], v8 offset:8200\n"
    "ds_read_b64_tr_b16 a[202:203], v8 offset:8712\n"
    "ds_read_b64_tr_b16 v[136:137], v8 offset:16384\n"
    "ds_read_b64_tr_b16 v[138:139], v8 offset:16896\n"
    "ds_read_b64_tr_b16 v[248:249], v8 offset:16392\n"
    "ds_read_b64_tr_b16 v[250:251], v8 offset:16904\n"
    "s_waitcnt vmcnt(24) lgkmcnt(0)\n"
    "s_barrier\n"
    "s_add_u32 m0, s60, 16384\n"
    "s_mov_b32 s73, 3145728\n"
    "buffer_load_dwordx4 v4, s[40:43], s73 offen lds\n"
    "s_add_u32 m0, s60, 20480\n"
    "s_mov_b32 s73, 3145856\n"
    "buffer_load_dwordx4 v4, s[40:43], s73 offen lds\n"
    "s_add_u32 m0, s60, 24576\n"
    "s_mov_b32 s73, 3145728\n"
    "buffer_load_dwordx4 v4, s[44:47], s73 offen lds\n"
    "s_add_u32 m0, s60, 28672\n"
    "s_mov_b32 s73, 3145856\n"
    "buffer_load_dwordx4 v4, s[44:47], s73 offen lds\n"
    "s_add_u32 m0, s60, 32768\n"
    "s_mov_b32 s73, 3145728\n"
    "buffer_load_dwordx4 v4, s[48:51], s73 offen lds\n"
    "s_add_u32 m0, s60, 36864\n"
    "s_mov_b32 s73, 3145856\n"
    "buffer_load_dwordx4 v4, s[48:51], s73 offen lds\n"
    "v_add_u32 v8, 40960, v6\n"
    "ds_read_b64_tr_b16 a[52:53], v8 offset:0\n"
    "ds_read_b64_tr_b16 a[54:55], v8 offset:512\n"
    "ds_read_b64_tr_b16 a[60:61], v8 offset:8\n"
    "ds_read_b64_tr_b16 a[62:63], v8 offset:520\n"
    "ds_read_b64_tr_b16 a[196:197], v8 offset:8192\n"
    "ds_read_b64_tr_b16 a[198:199], v8 offset:8704\n"
    "ds_read_b64_tr_b16 a[204:205], v8 offset:8200\n"
    "ds_read_b64_tr_b16 a[206:207], v8 offset:8712\n"
    "ds_read_b64_tr_b16 v[140:141], v8 offset:16384\n"
    "ds_read_b64_tr_b16 v[142:143], v8 offset:16896\n"
    "ds_read_b64_tr_b16 v[252:253], v8 offset:16392\n"
    "ds_read_b64_tr_b16 v[254:255], v8 offset:16904\n"
    "s_waitcnt vmcnt(24) lgkmcnt(0)\n"
    "s_barrier\n"
    "s_add_u32 m0, s60, 40960\n"
    "s_mov_b32 s73, 3407872\n"
    "buffer_load_dwordx4 v4, s[40:43], s73 offen lds\n"
    "s_add_u32 m0, s60, 45056\n"
    "s_mov_b32 s73, 3408000\n"
    "buffer_load_dwordx4 v4, s[40:43], s73 offen lds\n"
    "s_add_u32 m0, s60, 49152\n"
    "s_mov_b32 s73, 3407872\n"
    "buffer_load_dwordx4 v4, s[44:47], s73 offen lds\n"
    "s_add_u32 m0, s60, 53248\n"
    "s_mov_b32 s73, 3408000\n"
    "buffer_load_dwordx4 v4, s[44:47], s73 offen lds\n"
    "s_add_u32 m0, s60, 57344\n"
    "s_mov_b32 s73, 3407872\n"
    "buffer_load_dwordx4 v4, s[48:51], s73 offen lds\n"
    "s_add_u32 m0, s60, 61440\n"
    "s_mov_b32 s73, 3408000\n"
    "buffer_load_dwordx4 v4, s[48:51], s73 offen lds\n"
    "v_add_u32 v8, 65536, v6\n"
    "ds_read_b64_tr_b16 a[64:65], v8 offset:0\n"
    "ds_read_b64_tr_b16 a[66:67], v8 offset:512\n"
    "ds_read_b64_tr_b16 a[72:73], v8 offset:8\n"
    "ds_read_b64_tr_b16 a[74:75], v8 offset:520\n"
    "ds_read_b64_tr_b16 a[208:209], v8 offset:8192\n"
    "ds_read_b64_tr_b16 a[210:211], v8 offset:8704\n"
    "ds_read_b64_tr_b16 a[216:217], v8 offset:8200\n"
    "ds_read_b64_tr_b16 a[218:219], v8 offset:8712\n"
    "ds_read_b64_tr_b16 v[144:145], v8 offset:16384\n"
    "ds_read_b64_tr_b16 v[146:147], v8 offset:16896\n"
    "ds_read_b64_tr_b16 v[208:209], v8 offset:16392\n"
    "ds_read_b64_tr_b16 v[210:211], v8 offset:16904\n"
    "s_waitcnt vmcnt(24) lgkmcnt(0)\n"
    "s_barrier\n"
    "s_add_u32 m0, s60, 65536\n"
    "s_mov_b32 s73, 3670016\n"
    "buffer_load_dwordx4 v4, s[40:43], s73 offen lds\n"
    "s_add_u32 m0, s60, 69632\n"
    "s_mov_b32 s73, 3670144\n"
    "buffer_load_dwordx4 v4, s[40:43], s73 offen lds\n"
    "s_add_u32 m0, s60, 73728\n"
    "s_mov_b32 s73, 3670016\n"
    "buffer_load_dwordx4 v4, s[44:47], s73 offen lds\n"
    "s_add_u32 m0, s60, 77824\n"
    "s_mov_b32 s73, 3670144\n"
    "buffer_load_dwordx4 v4, s[44:47], s73 offen lds\n"
    "s_add_u32 m0, s60, 81920\n"
    "s_mov_b32 s73, 3670016\n"
    "buffer_load_dwordx4 v4, s[48:51], s73 offen lds\n"
    "s_add_u32 m0, s60, 86016\n"
    "s_mov_b32 s73, 3670144\n"
    "buffer_load_dwordx4 v4, s[48:51], s73 offen lds\n"
    "v_add_u32 v8, 90112, v6\n"
    "ds_read_b64_tr_b16 a[68:69], v8 offset:0\n"
    "ds_read_b64_tr_b16 a[70:71], v8 offset:512\n"
    "ds_read_b64_tr_b16 a[76:77], v8 offset:8\n"
    "ds_read_b64_tr_b16 a[78:79], v8 offset:520\n"
    "ds_read_b64_tr_b16 a[212:213], v8 offset:8192\n"
    "ds_read_b64_tr_b16 a[214:215], v8 offset:8704\n"
    "ds_read_b64_tr_b16 a[220:221], v8 offset:8200\n"
    "ds_read_b64_tr_b16 a[222:223], v8 offset:8712\n"
    "ds_read_b64_tr_b16 v[148:149], v8 offset:16384\n"
    "ds_read_b64_tr_b16 v[150:151], v8 offset:16896\n"
    "ds_read_b64_tr_b16 v[212:213], v8 offset:16392\n"
    "ds_read_b64_tr_b16 v[214:215], v8 offset:16904\n"
    "s_waitcnt vmcnt(24) lgkmcnt(0)\n"
    "s_barrier\n"
    "s_add_u32 m0, s60, 90112\n"
    "s_mov_b32 s73, 3932160\n"
    "buffer_load_dwordx4 v4, s[40:43], s73 offen lds\n"
    "s_add_u32 m0, s60, 94208\n"
    "s_mov_b32 s73, 3932288\n"
    "buffer_load_dwordx4 v4, s[40:43], s73 offen lds\n"
    "s_add_u32 m0, s60, 98304\n"
    "s_mov_b32 s73, 3932160\n"
    "buffer_load_dwordx4 v4, s[44:47], s73 offen lds\n"
    "s_add_u32 m0, s60, 102400\n"
    "s_mov_b32 s73, 3932288\n"
    "buffer_load_dwordx4 v4, s[44:47], s73 offen lds\n"
    "s_add_u32 m0, s60, 106496\n"
    "s_mov_b32 s73, 3932160\n"
    "buffer_load_dwordx4 v4, s[48:51], s73 offen lds\n"
    "s_add_u32 m0, s60, 110592\n"
    "s_mov_b32 s73, 3932288\n"
    "buffer_load_dwordx4 v4, s[48:51], s73 offen lds\n"
    "v_add_u32 v8, 114688, v6\n"
    "ds_read_b64_tr_b16 a[80:81], v8 offset:0\n"
    "ds_read_b64_tr_b16 a[82:83], v8 offset:512\n"
    "ds_read_b64_tr_b16 a[88:89], v8 offset:8\n"
    "ds_read_b64_tr_b16 a[90:91], v8 offset:520\n"
    "ds_read_b64_tr_b16 a[224:225], v8 offset:8192\n"
    "ds_read_b64_tr_b16 a[226:227], v8 offset:8704\n"
    "ds_read_b64_tr_b16 a[232:233], v8 offset:8200\n"
    "ds_read_b64_tr_b16 a[234:235], v8 offset:8712\n"
    "ds_read_b64_tr_b16 v[152:153], v8 offset:16384\n"
    "ds_read_b64_tr_b16 v[154:155], v8 offset:16896\n"
    "ds_read_b64_tr_b16 v[216:217], v8 offset:16392\n"
    "ds_read_b64_tr_b16 v[218:219], v8 offset:16904\n"
    "s_waitcnt vmcnt(24) lgkmcnt(0)\n"
    "s_barrier\n"
    "s_add_u32 m0, s60, 114688\n"
    "s_mov_b32 s73, 4194304\n"
    "buffer_load_dwordx4 v4, s[40:43], s73 offen lds\n"
    "s_add_u32 m0, s60, 118784\n"
    "s_mov_b32 s73, 4194432\n"
    "buffer_load_dwordx4 v4, s[40:43], s73 offen lds\n"
    "s_add_u32 m0, s60, 122880\n"
    "s_mov_b32 s73, 4194304\n"
    "buffer_load_dwordx4 v4, s[44:47], s73 offen lds\n"
    "s_add_u32 m0, s60, 126976\n"
    "s_mov_b32 s73, 4194432\n"
    "buffer_load_dwordx4 v4, s[44:47], s73 offen lds\n"
    "s_add_u32 m0, s60, 131072\n"
    "s_mov_b32 s73, 4194304\n"
    "buffer_load_dwordx4 v4, s[48:51], s73 offen lds\n"
    "s_add_u32 m0, s60, 135168\n"
    "s_mov_b32 s73, 4194432\n"
    "buffer_load_dwordx4 v4, s[48:51], s73 offen lds\n"
    "v_add_u32 v8, 139264, v6\n"
    "ds_read_b64_tr_b16 a[84:85], v8 offset:0\n"
    "ds_read_b64_tr_b16 a[86:87], v8 offset:512\n"
    "ds_read_b64_tr_b16 a[92:93], v8 offset:8\n"
    "ds_read_b64_tr_b16 a[94:95], v8 offset:520\n"
    "ds_read_b64_tr_b16 a[228:229], v8 offset:8192\n"
    "ds_read_b64_tr_b16 a[230:231], v8 offset:8704\n"
    "ds_read_b64_tr_b16 a[236:237], v8 offset:8200\n"
    "ds_read_b64_tr_b16 a[238:239], v8 offset:8712\n"
    "ds_read_b64_tr_b16 v[156:157], v8 offset:16384\n"
    "ds_read_b64_tr_b16 v[158:159], v8 offset:16896\n"
    "ds_read_b64_tr_b16 v[220:221], v8 offset:16392\n"
    "ds_read_b64_tr_b16 v[222:223], v8 offset:16904\n"
    "s_waitcnt vmcnt(24) lgkmcnt(0)\n"
    "s_barrier\n"
    "s_add_u32 m0, s60, 139264\n"
    "s_mov_b32 s73, 4456448\n"
    "buffer_load_dwordx4 v4, s[40:43], s73 offen lds\n"
    "s_add_u32 m0, s60, 143360\n"
    "s_mov_b32 s73, 4456576\n"
    "buffer_load_dwordx4 v4, s[40:43], s73 offen lds\n"
    "s_add_u32 m0, s60, 147456\n"
    "s_mov_b32 s73, 4456448\n"
    "buffer_load_dwordx4 v4, s[44:47], s73 offen lds\n"
    "s_add_u32 m0, s60, 151552\n"
    "s_mov_b32 s73, 4456576\n"
    "buffer_load_dwordx4 v4, s[44:47], s73 offen lds\n"
    "s_add_u32 m0, s60, 155648\n"
    "s_mov_b32 s73, 4456448\n"
    "buffer_load_dwordx4 v4, s[48:51], s73 offen lds\n"
    "s_add_u32 m0, s60, 159744\n"
    "s_mov_b32 s73, 4456576\n"
    "buffer_load_dwordx4 v4, s[48:51], s73 offen lds\n"
    "v_add_u32 v8, 16384, v6\n"
    "ds_read_b64_tr_b16 a[96:97], v8 offset:0\n"
    "ds_read_b64_tr_b16 a[98:99], v8 offset:512\n"
    "ds_read_b64_tr_b16 a[104:105], v8 offset:8\n"
    "ds_read_b64_tr_b16 a[106:107], v8 offset:520\n"
    "ds_read_b64_tr_b16 a[240:241], v8 offset:8192\n"
    "ds_read_b64_tr_b16 a[242:243], v8 offset:8704\n"
    "ds_read_b64_tr_b16 a[248:249], v8 offset:8200\n"
    "ds_read_b64_tr_b16 a[250:251], v8 offset:8712\n"
    "ds_read_b64_tr_b16 v[160:161], v8 offset:16384\n"
    "ds_read_b64_tr_b16 v[162:163], v8 offset:16896\n"
    "ds_read_b64_tr_b16 v[168:169], v8 offset:16392\n"
    "ds_read_b64_tr_b16 v[170:171], v8 offset:16904\n"
    "s_waitcnt vmcnt(24) lgkmcnt(0)\n"
    "s_barrier\n"
    "v_add_u32 v8, 40960, v6\n"
    "ds_read_b64_tr_b16 a[100:101], v8 offset:0\n"
    "ds_read_b64_tr_b16 a[102:103], v8 offset:512\n"
    "ds_read_b64_tr_b16 a[108:109], v8 offset:8\n"
    "ds_read_b64_tr_b16 a[110:111], v8 offset:520\n"
    "ds_read_b64_tr_b16 a[244:245], v8 offset:8192\n"
    "ds_read_b64_tr_b16 a[246:247], v8 offset:8704\n"
    "ds_read_b64_tr_b16 a[252:253], v8 offset:8200\n"
    "ds_read_b64_tr_b16 a[254:255], v8 offset:8712\n"
    "ds_read_b64_tr_b16 v[164:165], v8 offset:16384\n"
    "ds_read_b64_tr_b16 v[166:167], v8 offset:16896\n"
    "ds_read_b64_tr_b16 v[172:173], v8 offset:16392\n"
    "ds_read_b64_tr_b16 v[174:175], v8 offset:16904\n"
    "s_waitcnt vmcnt(18) lgkmcnt(0)\n"
    "s_barrier\n"
    "s_add_u32 m0, s60, 16384\n"
    "s_mov_b32 s73, 74240\n"
    "buffer_load_dwordx4 v5, s[52:55], s73 offen lds\n"
    "s_add_u32 m0, s60, 20480\n"
    "s_mov_b32 s73, 74368\n"
    "buffer_load_dwordx4 v5, s[52:55], s73 offen lds\n"
    "s_add_u32 m0, s60, 24576\n"
    "s_mov_b32 s73, 74496\n"
    "buffer_load_dwordx4 v5, s[52:55], s73 offen lds\n"
    "s_add_u32 m0, s60, 28672\n"
    "s_mov_b32 s73, 74624\n"
    "buffer_load_dwordx4 v5, s[52:55], s73 offen lds\n"
    "s_add_u32 m0, s60, 32768\n"
    "s_mov_b32 s73, 111104\n"
    "buffer_load_dwordx4 v5, s[52:55], s73 offen lds\n"
    "s_add_u32 m0, s60, 36864\n"
    "s_mov_b32 s73, 111232\n"
    "buffer_load_dwordx4 v5, s[52:55], s73 offen lds\n"
    "s_add_u32 m0, s60, 40960\n"
    "s_mov_b32 s73, 111360\n"
    "buffer_load_dwordx4 v5, s[52:55], s73 offen lds\n"
    "s_add_u32 m0, s60, 45056\n"
    "s_mov_b32 s73, 111488\n"
    "buffer_load_dwordx4 v5, s[52:55], s73 offen lds\n"
    "v_add_u32 v8, 65536, v6\n"
    "ds_read_b64_tr_b16 a[112:113], v8 offset:0\n"
    "ds_read_b64_tr_b16 a[114:115], v8 offset:512\n"
    "ds_read_b64_tr_b16 a[120:121], v8 offset:8\n"
    "ds_read_b64_tr_b16 a[122:123], v8 offset:520\n"
    "ds_read_b64_tr_b16 v[80:81], v8 offset:8192\n"
    "ds_read_b64_tr_b16 v[82:83], v8 offset:8704\n"
    "ds_read_b64_tr_b16 v[88:89], v8 offset:8200\n"
    "ds_read_b64_tr_b16 v[90:91], v8 offset:8712\n"
    "ds_read_b64_tr_b16 v[176:177], v8 offset:16384\n"
    "ds_read_b64_tr_b16 v[178:179], v8 offset:16896\n"
    "ds_read_b64_tr_b16 v[184:185], v8 offset:16392\n"
    "ds_read_b64_tr_b16 v[186:187], v8 offset:16904\n"
    "s_waitcnt vmcnt(20) lgkmcnt(0)\n"
    "s_barrier\n"
    "s_add_u32 m0, s60, 49152\n"
    "s_mov_b32 s73, 73728\n"
    "buffer_load_dwordx4 v5, s[52:55], s73 offen lds\n"
    "s_add_u32 m0, s60, 53248\n"
    "s_mov_b32 s73, 73856\n"
    "buffer_load_dwordx4 v5, s[52:55], s73 offen lds\n"
    "s_add_u32 m0, s60, 57344\n"
    "s_mov_b32 s73, 73984\n"
    "buffer_load_dwordx4 v5, s[52:55], s73 offen lds\n"
    "s_add_u32 m0, s60, 61440\n"
    "s_mov_b32 s73, 74112\n"
    "buffer_load_dwordx4 v5, s[52:55], s73 offen lds\n"
    "s_add_u32 m0, s60, 65536\n"
    "s_mov_b32 s73, 110592\n"
    "buffer_load_dwordx4 v5, s[52:55], s73 offen lds\n"
    "s_add_u32 m0, s60, 69632\n"
    "s_mov_b32 s73, 110720\n"
    "buffer_load_dwordx4 v5, s[52:55], s73 offen lds\n"
    "s_add_u32 m0, s60, 73728\n"
    "s_mov_b32 s73, 110848\n"
    "buffer_load_dwordx4 v5, s[52:55], s73 offen lds\n"
    "s_add_u32 m0, s60, 77824\n"
    "s_mov_b32 s73, 110976\n"
    "buffer_load_dwordx4 v5, s[52:55], s73 offen lds\n"
    "v_add_u32 v8, 90112, v6\n"
    "ds_read_b64_tr_b16 a[116:117], v8 offset:0\n"
    "ds_read_b64_tr_b16 a[118:119], v8 offset:512\n"
    "ds_read_b64_tr_b16 a[124:125], v8 offset:8\n"
    "ds_read_b64_tr_b16 a[126:127], v8 offset:520\n"
    "ds_read_b64_tr_b16 v[84:85], v8 offset:8192\n"
    "ds_read_b64_tr_b16 v[86:87], v8 offset:8704\n"
    "ds_read_b64_tr_b16 v[92:93], v8 offset:8200\n"
    "ds_read_b64_tr_b16 v[94:95], v8 offset:8712\n"
    "ds_read_b64_tr_b16 v[180:181], v8 offset:16384\n"
    "ds_read_b64_tr_b16 v[182:183], v8 offset:16896\n"
    "ds_read_b64_tr_b16 v[188:189], v8 offset:16392\n"
    "ds_read_b64_tr_b16 v[190:191], v8 offset:16904\n"
    "s_waitcnt vmcnt(22) lgkmcnt(0)\n"
    "s_barrier\n"
    "s_add_u32 m0, s60, 81920\n"
    "s_mov_b32 s73, 0\n"
    "buffer_load_dwordx4 v5, s[52:55], s73 offen lds\n"
    "s_add_u32 m0, s60, 86016\n"
    "s_mov_b32 s73, 128\n"
    "buffer_load_dwordx4 v5, s[52:55], s73 offen lds\n"
    "s_add_u32 m0, s60, 90112\n"
    "s_mov_b32 s73, 256\n"
    "buffer_load_dwordx4 v5, s[52:55], s73 offen lds\n"
    "s_add_u32 m0, s60, 94208\n"
    "s_mov_b32 s73, 384\n"
    "buffer_load_dwordx4 v5, s[52:55], s73 offen lds\n"
    "s_add_u32 m0, s60, 98304\n"
    "s_mov_b32 s73, 36864\n"
    "buffer_load_dwordx4 v5, s[52:55], s73 offen lds\n"
    "s_add_u32 m0, s60, 102400\n"
    "s_mov_b32 s73, 36992\n"
    "buffer_load_dwordx4 v5, s[52:55], s73 offen lds\n"
    "s_add_u32 m0, s60, 106496\n"
    "s_mov_b32 s73, 37120\n"
    "buffer_load_dwordx4 v5, s[52:55], s73 offen lds\n"
    "s_add_u32 m0, s60, 110592\n"
    "s_mov_b32 s73, 37248\n"
    "buffer_load_dwordx4 v5, s[52:55], s73 offen lds\n"
    "v_add_u32 v8, 114688, v6\n"
    "ds_read_b64_tr_b16 a[128:129], v8 offset:0\n"
    "ds_read_b64_tr_b16 a[130:131], v8 offset:512\n"
    "ds_read_b64_tr_b16 a[136:137], v8 offset:8\n"
    "ds_read_b64_tr_b16 a[138:139], v8 offset:520\n"
    "ds_read_b64_tr_b16 v[96:97], v8 offset:8192\n"
    "ds_read_b64_tr_b16 v[98:99], v8 offset:8704\n"
    "ds_read_b64_tr_b16 v[104:105], v8 offset:8200\n"
    "ds_read_b64_tr_b16 v[106:107], v8 offset:8712\n"
    "ds_read_b64_tr_b16 v[192:193], v8 offset:16384\n"
    "ds_read_b64_tr_b16 v[194:195], v8 offset:16896\n"
    "ds_read_b64_tr_b16 v[200:201], v8 offset:16392\n"
    "ds_read_b64_tr_b16 v[202:203], v8 offset:16904\n"
    "s_waitcnt vmcnt(24) lgkmcnt(0)\n"
    "s_barrier\n"
    "v_add_u32 v8, 139264, v6\n"
    "ds_read_b64_tr_b16 a[132:133], v8 offset:0\n"
    "ds_read_b64_tr_b16 a[134:135], v8 offset:512\n"
    "ds_read_b64_tr_b16 a[140:141], v8 offset:8\n"
    "ds_read_b64_tr_b16 a[142:143], v8 offset:520\n"
    "ds_read_b64_tr_b16 v[100:101], v8 offset:8192\n"
    "ds_read_b64_tr_b16 v[102:103], v8 offset:8704\n"
    "ds_read_b64_tr_b16 v[108:109], v8 offset:8200\n"
    "ds_read_b64_tr_b16 v[110:111], v8 offset:8712\n"
    "ds_read_b64_tr_b16 v[196:197], v8 offset:16384\n"
    "ds_read_b64_tr_b16 v[198:199], v8 offset:16896\n"
    "ds_read_b64_tr_b16 v[204:205], v8 offset:16392\n"
    "ds_read_b64_tr_b16 v[206:207], v8 offset:16904\n"
    "s_waitcnt vmcnt(16) lgkmcnt(0)\n"
    "s_barrier\n"
    "s_add_u32 m0, s60, 114688\n"
    "s_mov_b32 s73, 512\n"
    "buffer_load_dwordx4 v5, s[52:55], s73 offen lds\n"
    "s_add_u32 m0, s60, 118784\n"
    "s_mov_b32 s73, 640\n"
    "buffer_load_dwordx4 v5, s[52:55], s73 offen lds\n"
    "s_add_u32 m0, s60, 122880\n"
    "s_mov_b32 s73, 768\n"
    "buffer_load_dwordx4 v5, s[52:55], s73 offen lds\n"
    "s_add_u32 m0, s60, 126976\n"
    "s_mov_b32 s73, 896\n"
    "buffer_load_dwordx4 v5, s[52:55], s73 offen lds\n"
    "s_add_u32 m0, s60, 131072\n"
    "s_mov_b32 s73, 37376\n"
    "buffer_load_dwordx4 v5, s[52:55], s73 offen lds\n"
    "s_add_u32 m0, s60, 135168\n"
    "s_mov_b32 s73, 37504\n"
    "buffer_load_dwordx4 v5, s[52:55], s73 offen lds\n"
    "s_add_u32 m0, s60, 139264\n"
    "s_mov_b32 s73, 37632\n"
    "buffer_load_dwordx4 v5, s[52:55], s73 offen lds\n"
    "s_add_u32 m0, s60, 143360\n"
    "s_mov_b32 s73, 37760\n"
    "buffer_load_dwordx4 v5, s[52:55], s73 offen lds\n"
    "s_add_u32 m0, s60, 147456\n"
    "s_mov_b32 s73, 1024\n"
    "buffer_load_dwordx4 v5, s[52:55], s73 offen lds\n"
    "s_add_u32 m0, s60, 151552\n"
    "s_mov_b32 s73, 37888\n"
    "buffer_load_dwordx4 v5, s[52:55], s73 offen lds\n"
    "s_add_u32 m0, s60, 155648\n"
    "s_mov_b32 s73, 74752\n"
    "buffer_load_dwordx4 v5, s[52:55], s73 offen lds\n"
    "s_add_u32 m0, s60, 159744\n"
    "s_mov_b32 s73, 111616\n"
    "buffer_load_dwordx4 v5, s[52:55], s73 offen lds\n"
    "buffer_load_dwordx2 v[12:13], off, s[36:39], 0 sc1\n"
    "v_add_u32 v8, 16384, v7\n"
    "v_xor_b32 v9, 32, v8\n"
    "v_xor_b32 v10, 320, v8\n"
    "v_xor_b32 v11, 32, v10\n"
    "ds_read_b64_tr_b16 v[16:17], v8 offset:0\n"
    "ds_read_b64_tr_b16 v[18:19], v8 offset:512\n"
    "ds_read_b64_tr_b16 v[20:21], v8 offset:16384\n"
    "ds_read_b64_tr_b16 v[22:23], v8 offset:16896\n"
    "ds_read_b64_tr_b16 v[24:25], v9 offset:0\n"
    "ds_read_b64_tr_b16 v[26:27], v9 offset:512\n"
    "ds_read_b64_tr_b16 v[28:29], v9 offset:16384\n"
    "ds_read_b64_tr_b16 v[30:31], v9 offset:16896\n"
    "s_waitcnt lgkmcnt(4)\n"
    "ds_read_b64_tr_b16 v[32:33], v10 offset:0\n"
    "ds_read_b64_tr_b16 v[34:35], v10 offset:512\n"
    "ds_read_b64_tr_b16 v[36:37], v10 offset:16384\n"
    "ds_read_b64_tr_b16 v[38:39], v10 offset:16896\n"
    "ds_read_b64_tr_b16 v[40:41], v11 offset:0\n"
    "ds_read_b64_tr_b16 v[42:43], v11 offset:512\n"
    "ds_read_b64_tr_b16 v[44:45], v11 offset:16384\n"
    "ds_read_b64_tr_b16 v[46:47], v11 offset:16896\n"
    "s_waitcnt vmcnt(21) lgkmcnt(0)\n"
    "s_barrier\n"
    "v_add_u32 v14, 0, v1\n"
    "ds_write_b128 v14, v[224:227] offset:0\n"
    "ds_write_b128 v14, v[228:231] offset:1024\n"
    "ds_write_b128 v14, v[232:235] offset:8192\n"
    "ds_write_b128 v14, v[236:239] offset:9216\n"
    "ds_write_b128 v14, v[240:243] offset:16384\n"
    "ds_write_b128 v14, v[244:247] offset:17408\n"
    "ds_write_b128 v14, v[248:251] offset:24576\n"
    "ds_write_b128 v14, v[252:255] offset:25600\n"
    "s_waitcnt lgkmcnt(4)\n"
    "v_add_u32 v8, 49152, v7\n"
    "v_xor_b32 v9, 32, v8\n"
    "v_xor_b32 v10, 320, v8\n"
    "v_xor_b32 v11, 32, v10\n"
    "ds_read_b64_tr_b16 v[48:49], v10 offset:0\n"
    "ds_read_b64_tr_b16 v[50:51], v10 offset:512\n"
    "ds_read_b64_tr_b16 v[52:53], v10 offset:16384\n"
    "ds_read_b64_tr_b16 v[54:55], v10 offset:16896\n"
    "ds_read_b64_tr_b16 v[56:57], v11 offset:0\n"
    "ds_read_b64_tr_b16 v[58:59], v11 offset:512\n"
    "ds_read_b64_tr_b16 v[60:61], v11 offset:16384\n"
    "ds_read_b64_tr_b16 v[62:63], v11 offset:16896\n"
    "s_waitcnt lgkmcnt(4)\n"
    "ds_read_b64_tr_b16 v[64:65], v8 offset:0\n"
    "ds_read_b64_tr_b16 v[66:67], v8 offset:512\n"
    "ds_read_b64_tr_b16 v[68:69], v8 offset:16384\n"
    "ds_read_b64_tr_b16 v[70:71], v8 offset:16896\n"
    "ds_read_b64_tr_b16 v[72:73], v9 offset:0\n"
    "ds_read_b64_tr_b16 v[74:75], v9 offset:512\n"
    "ds_read_b64_tr_b16 v[76:77], v9 offset:16384\n"
    "ds_read_b64_tr_b16 v[78:79], v9 offset:16896\n"
    "s_waitcnt vmcnt(13) lgkmcnt(0)\n"
    "s_barrier\n"
    "v_mov_b64 v[224:225], v[16:17]\n"
    "v_mov_b64 v[226:227], v[18:19]\n"
    "v_mov_b64 v[228:229], v[20:21]\n"
    "v_mov_b64 v[230:231], v[22:23]\n"
    "v_mov_b64 v[232:233], v[24:25]\n"
    "v_mov_b64 v[234:235], v[26:27]\n"
    "v_mov_b64 v[236:237], v[28:29]\n"
    "v_mov_b64 v[238:239], v[30:31]\n"
    "v_mov_b64 v[240:241], v[32:33]\n"
    "v_mov_b64 v[242:243], v[34:35]\n"
    "v_mov_b64 v[244:245], v[36:37]\n"
    "v_mov_b64 v[246:247], v[38:39]\n"
    "v_mov_b64 v[248:249], v[40:41]\n"
    "v_mov_b64 v[250:251], v[42:43]\n"
    "v_mov_b64 v[252:253], v[44:45]\n"
    "v_mov_b64 v[254:255], v[46:47]\n"
    "v_add_u32 v14, 32768, v1\n"
    "ds_write_b128 v14, v[208:211] offset:0\n"
    "ds_write_b128 v14, v[212:215] offset:1024\n"
    "ds_write_b128 v14, v[216:219] offset:8192\n"
    "ds_write_b128 v14, v[220:223] offset:9216\n"
    "ds_write_b128 v14, v[64:67] offset:16384\n"
    "ds_write_b128 v14, v[68:71] offset:17408\n"
    "ds_write_b128 v14, v[72:75] offset:24576\n"
    "ds_write_b128 v14, v[76:79] offset:25600\n"
    "s_waitcnt lgkmcnt(4)\n"
    "v_add_u32 v8, 81920, v7\n"
    "v_xor_b32 v9, 32, v8\n"
    "v_xor_b32 v10, 320, v8\n"
    "v_xor_b32 v11, 32, v10\n"
    "ds_read_b64_tr_b16 v[16:17], v8 offset:0\n"
    "ds_read_b64_tr_b16 v[18:19], v8 offset:512\n"
    "ds_read_b64_tr_b16 v[20:21], v8 offset:16384\n"
    "ds_read_b64_tr_b16 v[22:23], v8 offset:16896\n"
    "ds_read_b64_tr_b16 v[24:25], v9 offset:0\n"
    "ds_read_b64_tr_b16 v[26:27], v9 offset:512\n"
    "ds_read_b64_tr_b16 v[28:29], v9 offset:16384\n"
    "ds_read_b64_tr_b16 v[30:31], v9 offset:16896\n"
    "s_waitcnt lgkmcnt(4)\n"
    "ds_read_b64_tr_b16 v[32:33], v10 offset:0\n"
    "ds_read_b64_tr_b16 v[34:35], v10 offset:512\n"
    "ds_read_b64_tr_b16 v[36:37], v10 offset:16384\n"
    "ds_read_b64_tr_b16 v[38:39], v10 offset:16896\n"
    "ds_read_b64_tr_b16 v[40:41], v11 offset:0\n"
    "ds_read_b64_tr_b16 v[42:43], v11 offset:512\n"
    "ds_read_b64_tr_b16 v[44:45], v11 offset:16384\n"
    "ds_read_b64_tr_b16 v[46:47], v11 offset:16896\n"
    "s_waitcnt vmcnt(5) lgkmcnt(0)\n"
    "s_barrier\n"
    "v_mov_b64 v[208:209], v[48:49]\n"
    "v_mov_b64 v[210:211], v[50:51]\n"
    "v_mov_b64 v[212:213], v[52:53]\n"
    "v_mov_b64 v[214:215], v[54:55]\n"
    "v_mov_b64 v[216:217], v[56:57]\n"
    "v_mov_b64 v[218:219], v[58:59]\n"
    "v_mov_b64 v[220:221], v[60:61]\n"
    "v_mov_b64 v[222:223], v[62:63]\n"
    "v_add_u32 v14, 65536, v1\n"
    "ds_write_b128 v14, v[16:19] offset:0\n"
    "ds_write_b128 v14, v[20:23] offset:1024\n"
    "ds_write_b128 v14, v[24:27] offset:8192\n"
    "ds_write_b128 v14, v[28:31] offset:9216\n"
    "ds_write_b128 v14, v[32:35] offset:16384\n"
    "ds_write_b128 v14, v[36:39] offset:17408\n"
    "ds_write_b128 v14, v[40:43] offset:24576\n"
    "ds_write_b128 v14, v[44:47] offset:25600\n"
    "s_waitcnt lgkmcnt(4)\n"
    "v_add_u32 v8, 114688, v7\n"
    "v_xor_b32 v9, 32, v8\n"
    "v_xor_b32 v10, 320, v8\n"
    "v_xor_b32 v11, 32, v10\n"
    "ds_read_b64_tr_b16 v[48:49], v8 offset:0\n"
    "ds_read_b64_tr_b16 v[50:51], v8 offset:512\n"
    "ds_read_b64_tr_b16 v[52:53], v8 offset:16384\n"
    "ds_read_b64_tr_b16 v[54:55], v8 offset:16896\n"
    "ds_read_b64_tr_b16 v[56:57], v9 offset:0\n"
    "ds_read_b64_tr_b16 v[58:59], v9 offset:512\n"
    "ds_read_b64_tr_b16 v[60:61], v9 offset:16384\n"
    "ds_read_b64_tr_b16 v[62:63], v9 offset:16896\n"
    "s_waitcnt lgkmcnt(4)\n"
    "ds_read_b64_tr_b16 v[64:65], v10 offset:0\n"
    "ds_read_b64_tr_b16 v[66:67], v10 offset:512\n"
    "ds_read_b64_tr_b16 v[68:69], v10 offset:16384\n"
    "ds_read_b64_tr_b16 v[70:71], v10 offset:16896\n"
    "ds_read_b64_tr_b16 v[72:73], v11 offset:0\n"
    "ds_read_b64_tr_b16 v[74:75], v11 offset:512\n"
    "ds_read_b64_tr_b16 v[76:77], v11 offset:16384\n"
    "ds_read_b64_tr_b16 v[78:79], v11 offset:16896\n"
    "s_waitcnt vmcnt(0) lgkmcnt(0)\n"
    "s_barrier\n"
    ".Lrec_par_%=:\n"
    "v_readfirstlane_b32 s73, v12\n"
    "v_readfirstlane_b32 s74, v13\n"
    "s_xor_b32 s73, s73, s30\n"
    "s_xor_b32 s74, s74, s31\n"
    "s_cselect_b32 s74, 2, 0\n"
    "s_or_b32 s74, s74, s73\n"
    "s_cmp_lt_u32 s74, 2\n"
    "s_cbranch_scc1 .Lrec_par1_%=\n"
    "s_sleep 4\n"
    "buffer_load_dwordx2 v[12:13], off, s[36:39], 0 sc1\n"
    "s_waitcnt vmcnt(0)\n"
    "s_sub_u32 s69, s69, 1\n"
    "s_cbranch_scc0 .Lrec_par_%=\n"
    "s_mov_b32 s69, 0\n"
    ".Lrec_par1_%=:\n"
    "s_and_b32 s73, s73, 1\n"
    "s_lshl_b32 s73, s73, 13\n"
    "s_add_u32 s36, s36, s73\n"
    "s_addc_u32 s37, s37, 0\n"
    "v_lshlrev_b32 v44, 3, v0\n"
    "v_mov_b32 v45, 0x80000000\n"
    "v_cmp_gt_u32 vcc, 192, v0\n"
    "s_nop 1\n"
    "v_cndmask_b32 v44, v45, v44, vcc\n"
    "v_mov_b32 v46, -1\n"
    "buffer_load_dword v45, v44, s[36:39], s62 offen sc1\n"
    "s_mov_b64 exec, 1\n"
    "buffer_atomic_smax v46, off, s[36:39], s59 sc0 sc1\n"
    "s_mov_b64 exec, -1\n"
    "v_add_u32 v14, 98304, v1\n"
    "ds_write_b128 v14, v[48:51] offset:0\n"
    "ds_write_b128 v14, v[52:55] offset:1024\n"
    "ds_write_b128 v14, v[56:59] offset:8192\n"
    "ds_write_b128 v14, v[60:63] offset:9216\n"
    "ds_write_b128 v14, v[64:67] offset:16384\n"
    "ds_write_b128 v14, v[68:71] offset:17408\n"
    "ds_write_b128 v14, v[72:75] offset:24576\n"
    "ds_write_b128 v14, v[76:79] offset:25600\n"
    "s_waitcnt lgkmcnt(4)\n"
    "v_add_u32 v8, 147456, v3\n"
    "v_xor_b32 v9, 32, v8\n"
    "v_xor_b32 v10, 320, v8\n"
    "v_xor_b32 v11, 32, v10\n"
    "ds_read_b64_tr_b16 v[16:17], v8 offset:0\n"
    "ds_read_b64_tr_b16 v[18:19], v9 offset:1024\n"
    "ds_read_b64_tr_b16 v[20:21], v10 offset:0\n"
    "ds_read_b64_tr_b16 v[22:23], v11 offset:1024\n"
    "ds_read_b64_tr_b16 v[24:25], v8 offset:8192\n"
    "ds_read_b64_tr_b16 v[26:27], v9 offset:9216\n"
    "ds_read_b64_tr_b16 v[28:29], v10 offset:8192\n"
    "ds_read_b64_tr_b16 v[30:31], v11 offset:9216\n"
    "s_waitcnt lgkmcnt(4)\n"
    "s_waitcnt lgkmcnt(0)\n"
    "s_barrier\n"
    "v_add_u32 v14, 131072, v1\n"
    "ds_write_b128 v14, v[16:19] offset:0\n"
    "ds_write_b128 v14, v[20:23] offset:1024\n"
    "ds_write_b128 v14, v[24:27] offset:8192\n"
    "ds_write_b128 v14, v[28:31] offset:9216\n"
    "s_memtime s[32:33]\n"
    "s_memrealtime s[28:29]\n"
    "s_mov_b32 s84, 0x3e0293ee\n"
    "s_waitcnt vmcnt(0)\n"
    ".Lrec_init_%=:\n"
    "v_cmp_gt_i32 vcc, 0, v45\n"
    "s_cbranch_vccz .Lrec_init1_%=\n"
    "s_sleep 2\n"
    "buffer_load_dword v45, v44, s[36:39], s62 offen sc1\n"
    "s_waitcnt vmcnt(0)\n"
    "s_sub_u32 s69, s69, 1\n"
    "s_cbranch_scc0 .Lrec_init_%=\n"
    "s_mov_b32 s69, 0\n"
    ".Lrec_init1_%=:\n"
    "v_mov_b32 v3, v46\n"
    "s_mov_b32 s40, s76\n"
    "s_mov_b32 s41, s77\n"
    "s_mov_b32 s42, s78\n"
    "s_mov_b32 s43, s79\n"
    "s_mov_b32 s44, s80\n"
    "s_mov_b32 s45, s81\n"
    "s_mov_b32 s50, 0\n"
    "s_waitcnt lgkmcnt(0)\n"
    "s_mov_b32 s46, s34\n"
    "s_lshl_b32 s47, s92, 2\n"
    "s_mov_b32 s33, s32\n"
    "s_lshl_b32 s28, s28, 2\n"
    "s_mov_b32 s52, 1200\n"
    "s_and_b32 s35, s40, 1\n"
    "s_and_b32 s40, s40, -2\n"
    "s_mul_i32 s35, s35, 20\n"
    "s_add_u32 s34, s35, 38\n"
    "s_lshl_b32 s35, s35, 2\n"
    "s_mov_b32 s94, 40\n"
    "s_mov_b32 s68, -1\n"
    "s_add_u32 s68, s68, 1\n"
    "s_and_b32 s70, s68, 3\n"
    "s_lshl_b32 s70, s70, 17\n"
    "s_add_u32 s70, s70, s66\n"
    "s_add_u32 s71, s70, 4096\n"
    "s_add_u32 s72, s70, 16384\n"
    "s_sub_u32 s46, s33, s46\n"
    "s_sub_u32 s47, s28, s47\n"
    "v_cvt_f32_u32 v40, s46\n"
    "v_cvt_f32_u32 v41, s47\n"
    "v_rcp_f32 v41, v41\n"
    "s_mov_b32 s46, s33\n"
    "s_mov_b32 s47, s28\n"
    "v_mul_f32 v40, v40, v41\n"
    "v_mov_b32 v42, 0x40c80000\n"
    "v_med3_f32 v40, v40, 4.0, v42\n"
    "v_rcp_f32 v41, v40\n"
    "v_mul_f32 v40, 0x43800000, v40\n"
    "v_mul_f32 v41, 0x44b54000, v41\n"
    "v_cvt_u32_f32 v40, v40\n"
    "v_cvt_u32_f32 v41, v41\n"
    "v_readfirstlane_b32 s48, v40\n"
    "v_readfirstlane_b32 s49, v41\n"
    "v_mov_b64 v[40:41], 0\n"
    "v_mov_b64 v[42:43], 0\n"
    "v_mov_b64 v[44:45], 0\n"
    "v_mov_b64 v[46:47], 0\n"
    "v_mov_b64 v[48:49], 0\n"
    "v_mov_b64 v[50:51], 0\n"
    "v_mov_b64 v[52:53], 0\n"
    "v_mov_b64 v[54:55], 0\n"
    "v_mov_b64 v[56:57], 0\n"
    "v_mov_b64 v[58:59], 0\n"
    "v_mov_b64 v[60:61], 0\n"
    "v_mov_b64 v[62:63], 0\n"
    "s_waitcnt lgkmcnt(0)\n"
    "s_barrier\n"
    "s_branch .Lrec_entry_%=\n"
    ".Lrec_LOOPB_%=:\n"
    ".Lrec_top_%=:\n"
    "buffer_load_dwordx4 v[4:7], v0, s[36:39], s70 offen sc1\n"
    "s_mov_b32 s82, s50\n"
    "s_add_u32 s50, s50, 589824\n"
    "s_cmp_eq_u32 s50, 1769472\n"
    "s_cselect_b32 s50, 0, s50\n"
    "s_add_u32 s80, s68, 2\n"
    "s_and_b32 s80, s80, 3\n"
    "s_lshl_b32 s80, s80, 17\n"
    "buffer_load_dwordx4 v[8:11], v0, s[36:39], s70 offen offset:1024 sc1\n"
    "s_and_b32 s80, s80, s45\n"
    "s_and_b32 s91, s50, s44\n"
    "s_add_u32 s80, s80, s91\n"
    "s_add_u32 s80, s80, s43\n"
    "s_add_u32 s89, s42, s50\n"
    "s_lshr_b32 s90, s68, 2\n"
    "s_lshl_b32 s90, s90, 14\n"
    "buffer_load_dwordx4 v[12:15], v0, s[36:39], s70 offen offset:2048 sc1\n"
    "s_and_b32 s91, s68, 3\n"
    "s_lshl_b32 s91, s91, 11\n"
    "s_add_u32 s90, s90, s91\n"
    "s_add_u32 s90, s90, s40\n"
    "s_add_u32 s51, s90, s41\n"
    "s_and_b32 s76, s68, 7\n"
    "s_mul_i32 s76, s76, 4194304\n"
    "buffer_load_dwordx4 v[16:19], v0, s[36:39], s70 offen offset:3072 sc1\n"
    "s_add_u32 s75, s76, s56\n"
    "s_add_u32 s83, s75, 16384\n"
    "s_add_u32 s76, s76, s57\n"
    "s_add_u32 s77, s63, s82\n"
    "s_add_u32 s86, s77, 262144\n"
    "s_add_u32 s87, s67, s82\n"
    "s_add_u32 s91, s68, 1\n"
    "buffer_load_dwordx4 v[20:23], v0, s[36:39], s71 offen sc1\n"
    "s_and_b32 s79, s91, 3\n"
    "s_lshl_b32 s79, s79, 17\n"
    "s_add_u32 s79, s79, s65\n"
    "s_add_u32 s78, s64, s82\n"
    "s_add_u32 s88, s78, 4096\n"
    "s_nop 1\n"
    "buffer_load_dwordx4 v[24:27], v0, s[36:39], s71 offen offset:1024 sc1\n"
    "s_nop 6\n"
    "buffer_load_dwordx4 v[28:31], v0, s[36:39], s71 offen offset:2048 sc1\n"
    "s_nop 6\n"
    "buffer_load_dwordx4 v[32:35], v0, s[36:39], s71 offen offset:3072 sc1\n"
    "s_waitcnt vmcnt(7)\n"
    ".Lrec_start_%=:\n"
    "v_smfmac_f32_16x16x64_bf16 v[40:43], v[4:7], a[0:7], v2\n"
    "v_max3_u32 v3, v4, v5, v6\n"
    "v_smfmac_f32_16x16x64_bf16 v[44:47], v[4:7], a[8:15], v2\n"
    "v_max_u32 v3, v3, v7\n"
    "v_smfmac_f32_16x16x64_bf16 v[48:51], v[4:7], a[144:151], v2\n"
    "v_cmp_eq_u32 vcc, -1, v3\n"
    "v_smfmac_f32_16x16x64_bf16 v[52:55], v[4:7], a[152:159], v2\n"
    "buffer_load_dwordx4 v[36:39], v0, s[36:39], s72 offen sc1\n"
    "v_smfmac_f32_16x16x64_bf16 v[56:59], v[4:7], v[112:119], v2\n"
    "s_cbranch_vccnz .Lrec_retry0_%=\n"
    ".Lrec_back0_%=:\n"
    "s_waitcnt lgkmcnt(0)\n"
    "v_smfmac_f32_16x16x64_bf16 v[60:63], v[4:7], v[72:79], v2\n"
    "ds_read_b128 v[72:75], v1 offset:8192\n"
    "s_waitcnt vmcnt(7)\n"
    "v_smfmac_f32_16x16x64_bf16 v[40:43], v[8:11], a[16:23], v2\n"
    "ds_read_b128 v[76:79], v1 offset:9216\n"
    "v_max3_u32 v3, v3, v8, v9\n"
    "v_smfmac_f32_16x16x64_bf16 v[44:47], v[8:11], a[24:31], v2\n"
    "buffer_load_dwordx4 v[68:71], v0, s[36:39], s75 offen offset:1024 sc1\n"
    "v_smfmac_f32_16x16x64_bf16 v[48:51], v[8:11], a[160:167], v2\n"
    "v_max3_u32 v3, v3, v10, v11\n"
    "v_smfmac_f32_16x16x64_bf16 v[52:55], v[8:11], a[168:175], v2\n"
    "buffer_load_dwordx4 v[64:67], v0, s[36:39], s75 offen sc1\n"
    "v_smfmac_f32_16x16x64_bf16 v[56:59], v[8:11], v[120:127], v2\n"
    "s_waitcnt vmcnt(8) lgkmcnt(0)\n"
    "v_smfmac_f32_16x16x64_bf16 v[60:63], v[8:11], v[72:79], v2\n"
    "ds_read_b128 v[72:75], v1 offset:16384\n"
    "v_smfmac_f32_16x16x64_bf16 v[40:43], v[12:15], a[32:39], v2\n"
    "ds_read_b128 v[76:79], v1 offset:17408\n"
    "v_max3_u32 v3, v3, v12, v13\n"
    "v_smfmac_f32_16x16x64_bf16 v[44:47], v[12:15], a[40:47], v2\n"
    "v_max3_u32 v3, v3, v14, v15\n"
    "v_smfmac_f32_16x16x64_bf16 v[48:51], v[12:15], a[176:183], v2\n"
    "buffer_load_dwordx4 v[4:7], v0, s[36:39], s83 offen sc1\n"
    "v_smfmac_f32_16x16x64_bf16 v[52:55], v[12:15], a[184:191], v2\n"
    "buffer_load_dwordx4 v[8:11], v0, s[36:39], s83 offen offset:1024 sc1\n"
    "v_smfmac_f32_16x16x64_bf16 v[56:59], v[12:15], v[128:135], v2\n"
    "s_waitcnt vmcnt(9) lgkmcnt(0)\n"
    "v_max3_u32 v3, v3, v16, v17\n"
    "v_smfmac_f32_16x16x64_bf16 v[60:63], v[12:15], v[72:79], v2\n"
    "ds_read_b128 v[72:75], v1 offset:24576\n"
    "v_max3_u32 v3, v3, v18, v19\n"
    "v_smfmac_f32_16x16x64_bf16 v[40:43], v[16:19], a[48:55], v2\n"
    "ds_read_b128 v[76:79], v1 offset:25600\n"
    "v_cmp_eq_u32 vcc, -1, v3\n"
    "v_smfmac_f32_16x16x64_bf16 v[44:47], v[16:19], a[56:63], v2\n"
    "v_mov_b64 v[12:13], -1\n"
    "v_smfmac_f32_16x16x64_bf16 v[48:51], v[16:19], a[192:199], v2\n"
    "s_cbranch_vccnz .Lrec_retry1_%=\n"
    ".Lrec_back1_%=:\n"
    "v_mov_b64 v[14:15], -1\n"
    "v_smfmac_f32_16x16x64_bf16 v[52:55], v[16:19], a[200:207], v2\n"
    "s_waitcnt vmcnt(8) lgkmcnt(0)\n"
    "v_max3_u32 v3, v3, v20, v21\n"
    "v_smfmac_f32_16x16x64_bf16 v[60:63], v[16:19], v[72:79], v2\n"
    "ds_read_b128 v[72:75], v1 offset:32768\n"
    "v_max3_u32 v3, v3, v22, v23\n"
    "v_smfmac_f32_16x16x64_bf16 v[40:43], v[20:23], a[64:71], v2\n"
    "ds_read_b128 v[76:79], v1 offset:33792\n"
    "v_smfmac_f32_16x16x64_bf16 v[44:47], v[20:23], a[72:79], v2\n"
    "buffer_store_dwordx4 v[12:15], v0, s[36:39], s89 offen sc1\n"
    "v_smfmac_f32_16x16x64_bf16 v[48:51], v[20:23], a[208:215], v2\n"
    "v_smfmac_f32_16x16x64_bf16 v[52:55], v[20:23], a[216:223], v2\n"
    "s_waitcnt vmcnt(7) lgkmcnt(0)\n"
    "v_max3_u32 v3, v3, v24, v25\n"
    "v_smfmac_f32_16x16x64_bf16 v[60:63], v[20:23], v[72:79], v2\n"
    "ds_read_b128 v[72:75], v1 offset:40960\n"
    "v_max3_u32 v3, v3, v26, v27\n"
    "v_smfmac_f32_16x16x64_bf16 v[40:43], v[24:27], a[80:87], v2\n"
    "ds_read_b128 v[76:79], v1 offset:41984\n"
    "v_max3_u32 v3, v3, v28, v29\n"
    "v_smfmac_f32_16x16x64_bf16 v[44:47], v[24:27], a[88:95], v2\n"
    "v_max3_u32 v3, v3, v30, v31\n"
    "v_smfmac_f32_16x16x64_bf16 v[48:51], v[24:27], a[224:231], v2\n"
    "buffer_store_dwordx4 v[12:15], v0, s[36:39], s89 offen offset:1024 sc1\n"
    "v_smfmac_f32_16x16x64_bf16 v[52:55], v[24:27], a[232:239], v2\n"
    "s_waitcnt vmcnt(7) lgkmcnt(0)\n"
    "v_max3_u32 v3, v3, v32, v33\n"
    "v_smfmac_f32_16x16x64_bf16 v[60:63], v[24:27], v[72:79], v2\n"
    "ds_read_b128 v[72:75], v0 offset:13312\n"
    "v_max3_u32 v3, v3, v34, v35\n"
    "v_smfmac_f32_16x16x64_bf16 v[40:43], v[28:31], a[96:103], v2\n"
    "s_waitcnt vmcnt(6)\n"
    "buffer_store_dwordx4 v[12:15], v0, s[36:39], s80 offen sc1\n"
    "v_max3_u32 v3, v3, v36, v37\n"
    "v_smfmac_f32_16x16x64_bf16 v[44:47], v[28:31], a[104:111], v2\n"
    "ds_read_b128 v[12:15], v0 offset:12288\n"
    "v_max3_u32 v3, v3, v38, v39\n"
    "v_smfmac_f32_16x16x64_bf16 v[40:43], v[32:35], a[112:119], v2\n"
    "s_lshr_b32 s73, s60, 1\n"
    "v_cmp_eq_u32 vcc, -1, v3\n"
    "v_smfmac_f32_16x16x64_bf16 v[44:47], v[32:35], a[120:127], v2\n"
    "v_add_u32 v79, s60, v0\n"
    "v_smfmac_f32_16x16x64_bf16 v[48:51], v[28:31], a[240:247], v2\n"
    "s_cbranch_vccnz .Lrec_retry2_%=\n"
    ".Lrec_back2_%=:\n"
    "v_xor_b32 v76, 1024, v79\n"
    "v_smfmac_f32_16x16x64_bf16 v[40:43], v[36:39], a[128:135], v2\n"
    "s_waitcnt lgkmcnt(0)\n"
    "s_memrealtime s[28:29]\n"
    "v_xor_b32 v77, 2048, v79\n"
    "v_smfmac_f32_16x16x64_bf16 v[44:47], v[36:39], a[136:143], v2\n"
    "s_memrealtime s[30:31]\n"
    "v_xor_b32 v78, 3072, v79\n"
    "v_smfmac_f32_16x16x64_bf16 v[52:55], v[28:31], a[248:255], v2\n"
    "s_memtime s[32:33]\n"
    "v_cvt_pk_bf16_f32 v40, v40, v41\n"
    "v_smfmac_f32_16x16x64_bf16 v[48:51], v[32:35], v[80:87], v2\n"
    "s_memrealtime s[96:97]\n"
    "v_cvt_pk_bf16_f32 v41, v42, v43\n"
    "v_smfmac_f32_16x16x64_bf16 v[52:55], v[32:35], v[88:95], v2\n"
    "s_memrealtime s[98:99]\n"
    "v_cvt_pk_bf16_f32 v42, v44, v45\n"
    "v_smfmac_f32_16x16x64_bf16 v[48:51], v[36:39], v[96:103], v2\n"
    "v_cvt_pk_bf16_f32 v43, v46, v47\n"
    "v_smfmac_f32_16x16x64_bf16 v[52:55], v[36:39], v[104:111], v2\n"
    "v_add_u32 v1, 49152, v1\n"
    "v_mfma_f32_16x16x32_bf16 v[44:47], v[40:43], v[12:15], 0\n"
    "s_waitcnt lgkmcnt(0)\n"
    "v_cvt_pk_bf16_f32 v48, v48, v49\n"
    "v_cvt_pk_bf16_f32 v49, v50, v51\n"
    "v_lshrrev_b32 v3, 1, v79\n"
    "v_cvt_pk_bf16_f32 v50, v52, v53\n"
    "v_cvt_pk_bf16_f32 v51, v54, v55\n"
    "s_add_u32 s28, s28, s30\n"
    "s_add_u32 s96, s96, s98\n"
    "v_mfma_f32_16x16x32_bf16 v[52:55], v[48:51], v[12:15], 0\n"
    "s_add_u32 s28, s28, s96\n"
    "v_cvt_pk_bf16_f32 v40, v44, v45\n"
    "v_cvt_pk_bf16_f32 v41, v46, v47\n"
    "v_mov_b32 v44, s28\n"
    "v_mov_b32_dpp v42, v40 row_ror:8 row_mask:0xf bank_mask:0xf\n"
    "v_mov_b32_dpp v43, v41 row_ror:8 row_mask:0xf bank_mask:0xf\n"
    "v_add_u32 v44, s49, v44\n"
    "buffer_store_dword v44, off, s[36:39], s51 sc1\n"
    "s_waitcnt vmcnt(6)\n"
    "v_mfma_f32_16x16x32_bf16 v[44:47], v[68:71], v[40:43], 0\n"
    "v_cvt_pk_bf16_f32 v48, v52, v53\n"
    "v_cvt_pk_bf16_f32 v49, v54, v55\n"
    "v_mfma_f32_16x16x32_bf16 v[68:71], v[64:67], v[40:43], 0\n"
    "v_mov_b32_dpp v50, v48 row_ror:8 row_mask:0xf bank_mask:0xf\n"
    "v_mov_b32_dpp v51, v49 row_ror:8 row_mask:0xf bank_mask:0xf\n"
    "s_nop 1\n"
    "v_mfma_f32_16x16x32_bf16 v[52:55], v[48:51], v[40:43], 0\n"
    "s_nop 2\n"
    "v_mov_b32_dpp v68, v44 row_ror:8 row_mask:0xf bank_mask:0xc\n"
    "v_mov_b32_dpp v69, v45 row_ror:8 row_mask:0xf bank_mask:0xc\n"
    "v_mov_b32_dpp v70, v46 row_ror:8 row_mask:0xf bank_mask:0xc\n"
    "v_mov_b32_dpp v71, v47 row_ror:8 row_mask:0xf bank_mask:0xc\n"
    "ds_write_b128 v79, v[68:71] offset:0\n"
    "v_mov_b32_dpp v52, v54 row_ror:8 row_mask:0xf bank_mask:0xc\n"
    "v_mov_b32_dpp v53, v55 row_ror:8 row_mask:0xf bank_mask:0xc\n"
    "ds_write_b64 v3, v[52:53] offset:4096\n"
    "v_smfmac_f32_16x16x64_bf16 v[56:59], v[16:19], v[136:143], v2\n"
    "v_smfmac_f32_16x16x64_bf16 v[56:59], v[20:23], v[144:151], v2\n"
    "ds_read_b128 v[16:19], v1 offset:16384\n"
    "v_smfmac_f32_16x16x64_bf16 v[56:59], v[24:27], v[152:159], v2\n"
    "ds_read_b128 v[20:23], v1 offset:17408\n"
    "v_smfmac_f32_16x16x64_bf16 v[56:59], v[28:31], v[160:167], v2\n"
    "s_waitcnt lgkmcnt(2)\n"
    "s_barrier\n"
    "ds_read_b128 v[64:67], v76 offset:0\n"
    "v_smfmac_f32_16x16x64_bf16 v[60:63], v[28:31], v[168:175], v2\n"
    "ds_read_b128 v[40:43], v77 offset:0\n"
    "v_smfmac_f32_16x16x64_bf16 v[56:59], v[32:35], v[176:183], v2\n"
    "ds_read_b128 v[44:47], v78 offset:0\n"
    "v_smfmac_f32_16x16x64_bf16 v[60:63], v[32:35], v[184:191], v2\n"
    "ds_read_b64 v[12:13], v72 offset:4096\n"
    "ds_read_b64 v[14:15], v72 offset:4608\n"
    "v_smfmac_f32_16x16x64_bf16 v[56:59], v[36:39], v[192:199], v2\n"
    "ds_read_b64 v[52:53], v72 offset:5120\n"
    "ds_read_b64 v[54:55], v72 offset:5632\n"
    "v_smfmac_f32_16x16x64_bf16 v[60:63], v[36:39], v[200:207], v2\n"
    "buffer_store_dwordx4 v[48:51], v74, s[36:39], s76 offen sc1\n"
    "s_cmp_eq_u32 s68, 63\n"
    "s_waitcnt lgkmcnt(4)\n"
    "v_add_f32 v68, v68, v64\n"
    "v_add_f32 v69, v69, v65\n"
    "v_add_f32 v70, v70, v66\n"
    "v_add_f32 v71, v71, v67\n"
    "v_pk_add_f32 v[40:41], v[40:41], v[44:45]\n"
    "v_pk_add_f32 v[42:43], v[42:43], v[46:47]\n"
    "s_waitcnt lgkmcnt(0)\n"
    "v_pk_add_f32 v[12:13], v[12:13], v[14:15]\n"
    "v_pk_add_f32 v[52:53], v[52:53], v[54:55]\n"
    "v_pk_add_f32 v[68:69], v[68:69], v[40:41]\n"
    "v_pk_add_f32 v[70:71], v[70:71], v[42:43]\n"
    "v_pk_add_f32 v[12:13], v[12:13], v[52:53]\n"
    "v_max3_f32 v46, v68, v69, v70\n"
    "v_max3_f32 v47, v71, v12, v13\n"
    "v_max_f32 v3, v46, v47\n"
    "v_cvt_pk_bf16_f32 v56, v56, v57\n"
    "v_cvt_pk_bf16_f32 v57, v58, v59\n"
    "v_max_f32_dpp v46, v3, v3 row_ror:8 row_mask:0xf bank_mask:0xf\n"
    "v_max_f32_dpp v47, v3, v3 row_ror:8 row_mask:0xf bank_mask:0xf\n"
    "v_cvt_pk_bf16_f32 v58, v60, v61\n"
    "v_cvt_pk_bf16_f32 v59, v62, v63\n"
    "v_permlane16_swap_b32 v46, v47\n"
    "v_max_f32 v3, v46, v47\n"
    "v_max_f32 v46, v46, v47\n"
    "v_add_u32 v14, s73, v73\n"
    "v_xor_b32 v15, 128, v0\n"
    "v_permlane32_swap_b32 v3, v46\n"
    "v_max_f32 v3, v3, v46\n"
    "v_mul_f32 v46, 0xbe0293ee, v3\n"
    "v_pk_fma_f32 v[68:69], v[68:69], s[84:85], v[46:47] op_sel_hi:[1,0,0]\n"
    "v_pk_fma_f32 v[70:71], v[70:71], s[84:85], v[46:47] op_sel_hi:[1,0,0]\n"
    "v_pk_fma_f32 v[12:13], v[12:13], s[84:85], v[46:47] op_sel_hi:[1,0,0]\n"
    "v_exp_f32 v68, v68\n"
    "v_exp_f32 v69, v69\n"
    "v_exp_f32 v70, v70\n"
    "v_exp_f32 v71, v71\n"
    "v_exp_f32 v12, v12\n"
    "v_cvt_pk_bf16_f32 v40, v68, v69\n"
    "v_cvt_pk_bf16_f32 v41, v70, v71\n"
    "v_exp_f32 v13, v13\n"
    "v_mov_b32_dpp v42, v40 row_ror:8 row_mask:0xf bank_mask:0xf\n"
    "v_mov_b32_dpp v43, v41 row_ror:8 row_mask:0xf bank_mask:0xf\n"
    "v_cvt_pk_bf16_f32 v44, v12, v13\n"
    "v_pk_add_f32 v[68:69], v[68:69], v[70:71]\n"
    "s_waitcnt vmcnt(5)\n"
    "v_mfma_f32_16x16x32_bf16 v[48:51], v[4:7], v[40:43], 0\n"
    "ds_read_b128 v[28:31], v1 offset:1024\n"
    "v_mov_b32_dpp v45, v44 row_ror:8 row_mask:0xf bank_mask:0xf\n"
    "v_add_f32 v3, v12, v13\n"
    "v_mfma_f32_16x16x32_bf16 v[52:55], v[8:11], v[40:43], 0\n"
    "ds_read_b128 v[24:27], v1 offset:0\n"
    "v_add_f32 v46, v68, v69\n"
    "v_fma_f32 v3, v3, 0.5, v46\n"
    "v_mfma_f32_16x16x16_bf16 v[48:51], v[56:57], v[44:45], v[48:51]\n"
    "ds_read_b128 v[32:35], v1 offset:24576\n"
    "v_add_f32_dpp v46, v3, v3 row_ror:8 row_mask:0xf bank_mask:0xf\n"
    "v_add_f32_dpp v47, v3, v3 row_ror:8 row_mask:0xf bank_mask:0xf\n"
    "v_mfma_f32_16x16x16_bf16 v[52:55], v[58:59], v[44:45], v[52:55]\n"
    "buffer_load_dwordx2 v[12:13], v75, s[36:39], s90 offen sc1\n"
    "v_permlane16_swap_b32 v46, v47\n"
    "v_add_f32 v3, v46, v47\n"
    "v_add_f32 v46, v46, v47\n"
    "v_mov_b64 v[64:65], 0\n"
    "v_mov_b64 v[66:67], 0\n"
    "v_permlane32_swap_b32 v3, v46\n"
    "v_add_f32 v3, v3, v46\n"
    "v_rcp_f32 v46, v3\n"
    "v_mov_b32_dpp v48, v52 row_ror:8 row_mask:0xf bank_mask:0xc\n"
    "v_mov_b32_dpp v49, v53 row_ror:8 row_mask:0xf bank_mask:0xc\n"
    "v_mov_b32_dpp v50, v54 row_ror:8 row_mask:0xf bank_mask:0xc\n"
    "v_mov_b32_dpp v51, v55 row_ror:8 row_mask:0xf bank_mask:0xc\n"
    "v_pk_mul_f32 v[48:49], v[48:49], v[46:47] op_sel_hi:[1,0]\n"
    "v_pk_mul_f32 v[50:51], v[50:51], v[46:47] op_sel_hi:[1,0]\n"
    "v_cvt_pk_bf16_f32 v48, v48, v49\n"
    "v_cvt_pk_bf16_f32 v49, v50, v51\n"
    "ds_write_b64 v14, v[48:49] offset:8192\n"
    "s_cbranch_scc1 .Lrec_fin_%=\n"
    "ds_read_b128 v[36:39], v1 offset:25600\n"
    "ds_read_b128 v[72:75], v1 offset:8192\n"
    "ds_read_b128 v[76:79], v1 offset:9216\n"
    "ds_read_b128 v[4:7], v1 offset:32768\n"
    "ds_read_b128 v[8:11], v1 offset:33792\n"
    "v_mov_b64 v[68:69], 0\n"
    "v_mov_b64 v[70:71], 0\n"
    "s_waitcnt vmcnt(3) lgkmcnt(5)\n"
    "s_barrier\n"
    "ds_read_b128 v[40:43], v0 offset:8192\n"
    "ds_read_b128 v[44:47], v0 offset:9216\n"
    "ds_read_b128 v[48:51], v15 offset:8192\n"
    "ds_read_b128 v[52:55], v15 offset:9216\n"
    "buffer_store_dwordx4 v[56:59], v0, s[36:39], s76 offen offset:512 sc1\n"
    "s_waitcnt lgkmcnt(3)\n"
    "v_smfmac_f32_16x16x64_bf16 v[64:67], v[40:43], v[16:23], v2\n"
    "ds_read_b128 v[16:19], v1 offset:40960\n"
    "s_waitcnt lgkmcnt(3)\n"
    "v_smfmac_f32_16x16x64_bf16 v[64:67], v[44:47], v[24:31], v2\n"
    "ds_read_b128 v[20:23], v1 offset:41984\n"
    "v_smfmac_f32_16x16x64_bf16 v[68:71], v[40:43], v[32:39], v2\n"
    "ds_read_b128 v[24:27], v1 offset:49152\n"
    "v_mov_b64 v[56:57], 0\n"
    "v_smfmac_f32_16x16x64_bf16 v[68:71], v[44:47], v[72:79], v2\n"
    "ds_read_b128 v[28:31], v1 offset:50176\n"
    "v_mov_b64 v[58:59], 0\n"
    "s_waitcnt lgkmcnt(5)\n"
    "v_smfmac_f32_16x16x64_bf16 v[64:67], v[48:51], v[4:11], v2\n"
    "ds_read_b128 v[32:35], v1 offset:57344\n"
    "v_mov_b64 v[60:61], 0\n"
    "s_waitcnt lgkmcnt(5)\n"
    "v_smfmac_f32_16x16x64_bf16 v[64:67], v[52:55], v[208:215], v2\n"
    "ds_read_b128 v[36:39], v1 offset:58368\n"
    "ds_read_b128 v[8:11], v0 offset:14336\n"
    "s_waitcnt lgkmcnt(5)\n"
    "v_smfmac_f32_16x16x64_bf16 v[68:71], v[48:51], v[16:23], v2\n"
    "v_add_u32 v1, 32768, v1\n"
    "ds_read_b128 v[72:75], v1 offset:32768\n"
    "v_mov_b64 v[62:63], 0\n"
    "v_smfmac_f32_16x16x64_bf16 v[68:71], v[52:55], v[216:223], v2\n"
    "ds_read_b128 v[76:79], v1 offset:33792\n"
    "v_cvt_pk_bf16_f32 v64, v64, v65\n"
    "s_waitcnt lgkmcnt(5)\n"
    "v_smfmac_f32_16x16x64_bf16 v[56:59], v[40:43], v[24:31], v2\n"
    "ds_read_b128 v[16:19], v1 offset:40960\n"
    "v_cvt_pk_bf16_f32 v65, v66, v67\n"
    "v_smfmac_f32_16x16x64_bf16 v[56:59], v[44:47], v[224:231], v2\n"
    "ds_read_b128 v[20:23], v1 offset:41984\n"
    "v_cvt_pk_bf16_f32 v66, v68, v69\n"
    "v_cvt_pk_bf16_f32 v67, v70, v71\n"
    "s_waitcnt lgkmcnt(5)\n"
    "v_smfmac_f32_16x16x64_bf16 v[60:63], v[40:43], v[32:39], v2\n"
    "ds_read_b128 v[24:27], v1 offset:49152\n"
    "v_permlane16_swap_b32 v64, v66\n"
    "v_smfmac_f32_16x16x64_bf16 v[60:63], v[44:47], v[232:239], v2\n"
    "ds_read_b128 v[28:31], v1 offset:50176\n"
    "v_permlane16_swap_b32 v65, v67\n"
    "s_waitcnt lgkmcnt(4)\n"
    "v_smfmac_f32_16x16x64_bf16 v[56:59], v[48:51], v[72:79], v2\n"
    "ds_read_b128 v[32:35], v1 offset:57344\n"
    "buffer_store_dwordx4 v[64:67], v8, s[36:39], s77 offen sc1\n"
    "v_smfmac_f32_16x16x64_bf16 v[56:59], v[52:55], v[240:247], v2\n"
    "ds_read_b128 v[36:39], v1 offset:58368\n"
    "s_waitcnt lgkmcnt(4)\n"
    "v_smfmac_f32_16x16x64_bf16 v[60:63], v[48:51], v[16:23], v2\n"
    "v_smfmac_f32_16x16x64_bf16 v[60:63], v[52:55], v[248:255], v2\n"
    "v_cvt_pk_bf16_f32 v56, v56, v57\n"
    "s_waitcnt lgkmcnt(2)\n"
    "v_mfma_f32_16x16x32_bf16 v[4:7], v[40:43], v[24:27], 0\n"
    "v_cvt_pk_bf16_f32 v57, v58, v59\n"
    "v_mfma_f32_16x16x32_bf16 v[4:7], v[48:51], v[28:31], v[4:7]\n"
    "v_cvt_pk_bf16_f32 v58, v60, v61\n"
    "v_cvt_pk_bf16_f32 v59, v62, v63\n"
    "s_waitcnt lgkmcnt(0)\n"
    "v_mfma_f32_16x16x32_bf16 v[4:7], v[44:47], v[32:35], v[4:7]\n"
    "v_permlane16_swap_b32 v56, v58\n"
    "v_mfma_f32_16x16x32_bf16 v[4:7], v[52:55], v[36:39], v[4:7]\n"
    "v_permlane16_swap_b32 v57, v59\n"
    "buffer_store_dwordx4 v[56:59], v8, s[36:39], s86 offen sc1\n"
    "s_waitcnt vmcnt(3)\n"
    "v_max_u32 v12, v12, v13\n"
    "s_mov_b32 s33, s32\n"
    "s_nop 0\n"
    "v_max_u32_dpp v12, v12, v12 row_ror:8 row_mask:0xf bank_mask:0xf\n"
    "s_nop 1\n"
    "v_max_u32_dpp v12, v12, v12 row_ror:4 row_mask:0xf bank_mask:0xf\n"
    "v_cvt_pk_bf16_f32 v4, v4, v5\n"
    "v_cvt_pk_bf16_f32 v5, v6, v7\n"
    "v_max_u32_dpp v12, v12, v12 row_ror:2 row_mask:0xf bank_mask:0xf\n"
    "s_nop 0\n"
    "v_permlane16_swap_b32 v4, v6\n"
    "v_max_u32_dpp v12, v12, v12 row_ror:1 row_mask:0xf bank_mask:0xf\n"
    "v_permlane16_swap_b32 v5, v7\n"
    "buffer_store_dwordx4 v[4:7], v9, s[36:39], s87 offen sc1\n"
    "v_readlane_b32 s95, v12, 0\n"
    "v_readlane_b32 s99, v12, 16\n"
    "v_readlane_b32 s30, v12, 32\n"
    "v_readlane_b32 s31, v12, 48\n"
    "s_max_u32 s95, s95, s99\n"
    "s_max_u32 s30, s30, s31\n"
    "s_max_u32 s95, s95, s30\n"
    "s_cmp_eq_u32 s95, -1\n"
    "s_cbranch_scc1 .Lrec_tsretry_%=\n"
    ".Lrec_tsgo_%=:\n"
    "s_memtime s[96:97]\n"
    "v_mov_b32 v76, v10\n"
    "s_sub_u32 s95, s95, s28\n"
    "s_min_u32 s95, s95, 4000\n"
    "s_add_u32 s95, s95, s34\n"
    "s_add_u32 s95, s95, s94\n"
    "s_mul_i32 s95, s95, s48\n"
    "s_lshr_b32 s95, s95, 8\n"
    "s_add_u32 s32, s32, s95\n"
    "s_waitcnt lgkmcnt(0)\n"
    "s_sub_u32 s95, s32, s96\n"
    "s_mul_i32 s95, s95, 43\n"
    "s_ashr_i32 s95, s95, 10\n"
    "s_max_i32 s95, s95, 0\n"
    "s_min_i32 s95, s95, 100\n"
    ".Lrec_tsloop_%=:\n"
    "s_sub_u32 s95, s95, 1\n"
    "s_cbranch_scc0 .Lrec_tsloop_%=\n"
    "s_cmp_lg_u32 s60, 0\n"
    "s_cbranch_scc1 .Lrec_nored_%=\n"
    ".Lrec_red_%=:\n"
    "buffer_load_dwordx4 v[4:7], v0, s[36:39], s78 offen sc1\n"
    "buffer_load_dwordx4 v[8:11], v0, s[36:39], s78 offen offset:1024 sc1\n"
    "buffer_load_dwordx4 v[12:15], v0, s[36:39], s78 offen offset:2048 sc1\n"
    "buffer_load_dwordx4 v[16:19], v0, s[36:39], s78 offen offset:3072 sc1\n"
    "buffer_load_dwordx4 v[20:23], v0, s[36:39], s88 offen sc1\n"
    "buffer_load_dwordx4 v[24:27], v0, s[36:39], s88 offen offset:1024 sc1\n"
    "buffer_load_dwordx4 v[28:31], v0, s[36:39], s88 offen offset:2048 sc1\n"
    "buffer_load_dwordx4 v[32:35], v0, s[36:39], s88 offen offset:3072 sc1\n"
    "ds_read_b128 v[72:75], v0 offset:15360\n"
    "v_lshl_or_b32 v77, s68, 1, 1\n"
    "s_waitcnt vmcnt(11)\n"
    "s_mov_b64 exec, 1\n"
    "buffer_atomic_smax v77, off, s[36:39], s58 sc1\n"
    "s_mov_b64 exec, -1\n"
    "v_mov_b32 v3, -1\n"
    "v_subrev_u32 v1, 81920, v1\n"
    "s_sub_u32 s52, s52, 24\n"
    "s_max_u32 s52, s52, 900\n"
    "s_sub_u32 s94, s52, 900\n"
    "s_lshr_b32 s94, s94, 3\n"
    "s_sub_u32 s95, s52, s35\n"
    "s_mul_i32 s95, s48, s95\n"
    "s_lshr_b32 s95, s95, 10\n"
    "s_add_u32 s32, s32, s95\n"
    "s_add_u32 s68, s68, 1\n"
    "s_and_b32 s70, s68, 3\n"
    "s_lshl_b32 s70, s70, 17\n"
    "s_add_u32 s70, s70, s66\n"
    "s_add_u32 s71, s70, 4096\n"
    "s_add_u32 s72, s70, 16384\n"
    "s_sub_u32 s46, s33, s46\n"
    "s_sub_u32 s47, s28, s47\n"
    "v_cvt_f32_u32 v40, s46\n"
    "v_cvt_f32_u32 v41, s47\n"
    "v_rcp_f32 v41, v41\n"
    "s_mov_b32 s46, s33\n"
    "s_mov_b32 s47, s28\n"
    "v_mul_f32 v40, v40, v41\n"
    "v_mov_b32 v42, 0x40c80000\n"
    "v_med3_f32 v40, v40, 4.0, v42\n"
    "v_rcp_f32 v41, v40\n"
    "v_mul_f32 v40, 0x43800000, v40\n"
    "v_mul_f32 v41, 0x44b54000, v41\n"
    "v_cvt_u32_f32 v40, v40\n"
    "v_cvt_u32_f32 v41, v41\n"
    "v_readfirstlane_b32 s48, v40\n"
    "v_readfirstlane_b32 s49, v41\n"
    "v_mov_b64 v[40:41], 0\n"
    "v_mov_b64 v[42:43], 0\n"
    "v_mov_b64 v[44:45], 0\n"
    "v_mov_b64 v[46:47], 0\n"
    "v_mov_b64 v[48:49], 0\n"
    "v_mov_b64 v[50:51], 0\n"
    "v_mov_b64 v[52:53], 0\n"
    "v_mov_b64 v[54:55], 0\n"
    "v_mov_b64 v[56:57], 0\n"
    "v_mov_b64 v[58:59], 0\n"
    "v_mov_b64 v[60:61], 0\n"
    "v_mov_b64 v[62:63], 0\n"
    "s_mov_b64 exec, 1\n"
    "buffer_atomic_smax v3, off, s[36:39], s59 sc0 sc1\n"
    "s_mov_b64 exec, -1\n"
    "s_waitcnt lgkmcnt(0)\n"
    ".Lrec_rsum_%=:\n"
    "s_waitcnt vmcnt(9)\n"
    "v_mfma_f32_16x16x32_bf16 v[36:39], v[4:7], v[72:75], 0\n"
    "v_max3_u32 v78, v4, v5, v6\n"
    "v_max_u32 v78, v78, v7\n"
    "s_waitcnt vmcnt(8)\n"
    "v_mfma_f32_16x16x32_bf16 v[36:39], v[8:11], v[72:75], v[36:39]\n"
    "v_max3_u32 v78, v78, v8, v9\n"
    "v_max3_u32 v78, v78, v10, v11\n"
    "s_waitcnt vmcnt(7)\n"
    "v_mfma_f32_16x16x32_bf16 v[36:39], v[12:15], v[72:75], v[36:39]\n"
    "v_max3_u32 v78, v78, v12, v13\n"
    "v_max3_u32 v78, v78, v14, v15\n"
    "s_waitcnt vmcnt(6)\n"
    "v_mfma_f32_16x16x32_bf16 v[36:39], v[16:19], v[72:75], v[36:39]\n"
    "v_max3_u32 v78, v78, v16, v17\n"
    "v_max3_u32 v78, v78, v18, v19\n"
    "s_waitcnt vmcnt(5)\n"
    "v_mfma_f32_16x16x32_bf16 v[36:39], v[20:23], v[72:75], v[36:39]\n"
    "v_max3_u32 v78, v78, v20, v21\n"
    "v_max3_u32 v78, v78, v22, v23\n"
    "s_waitcnt vmcnt(4)\n"
    "v_mfma_f32_16x16x32_bf16 v[36:39], v[24:27], v[72:75], v[36:39]\n"
    "v_max3_u32 v78, v78, v24, v25\n"
    "v_max3_u32 v78, v78, v26, v27\n"
    "s_waitcnt vmcnt(3)\n"
    "v_mfma_f32_16x16x32_bf16 v[36:39], v[28:31], v[72:75], v[36:39]\n"
    "v_max3_u32 v78, v78, v28, v29\n"
    "v_max3_u32 v78, v78, v30, v31\n"
    "s_waitcnt vmcnt(2)\n"
    "v_mfma_f32_16x16x32_bf16 v[36:39], v[32:35], v[72:75], v[36:39]\n"
    "v_max3_u32 v78, v78, v32, v33\n"
    "v_max3_u32 v78, v78, v34, v35\n"
    "v_cmp_eq_u32 vcc, -1, v78\n"
    "s_nop 4\n"
    "v_cvt_pk_bf16_f32 v4, v36, v37\n"
    "v_cvt_pk_bf16_f32 v6, v36, v37\n"
    "v_cvt_pk_bf16_f32 v5, v38, v39\n"
    "v_cvt_pk_bf16_f32 v7, v38, v39\n"
    "s_cbranch_vccnz .Lrec_rretry_%=\n"
    ".Lrec_rgo_%=:\n"
    "v_permlane16_swap_b32 v4, v6\n"
    "v_permlane16_swap_b32 v5, v7\n"
    "buffer_store_dwordx4 v[4:7], v76, s[36:39], s79 offen sc1\n"
    "s_waitcnt vmcnt(1)\n"
    ".Lrec_b3_%=:\n"
    ".Lrec_entry_%=:\n"
    "s_memtime s[96:97]\n"
    "ds_read_b128 v[72:75], v1 offset:0\n"
    "ds_read_b128 v[76:79], v1 offset:1024\n"
    "v_readfirstlane_b32 s81, v3\n"
    "s_lshl_b32 s73, s68, 1\n"
    "s_cmp_le_i32 s81, s73\n"
    "s_cbranch_scc1 .Lrec_waitp_%=\n"
    "s_waitcnt lgkmcnt(0)\n"
    "s_sub_u32 s95, s32, s96\n"
    "s_mul_i32 s95, s95, 43\n"
    "s_ashr_i32 s95, s95, 10\n"
    "s_max_i32 s95, s95, 0\n"
    "s_min_i32 s95, s95, 100\n"
    ".Lrec_gloop_%=:\n"
    "s_sub_u32 s95, s95, 1\n"
    "s_cbranch_scc0 .Lrec_gloop_%=\n"
    "s_branch .Lrec_top_%=\n"
    ".Lrec_LOOPE_%=:\n"
    ".Lrec_fin_%=:\n"
    "buffer_store_dwordx4 v[56:59], v0, s[36:39], s76 offen offset:512 sc1\n"
    "s_waitcnt vmcnt(0) lgkmcnt(0)\n"
    "v_lshl_or_b32 v46, s68, 1, 1\n"
    "s_mov_b64 exec, 1\n"
    "buffer_atomic_smax v46, off, s[36:39], s58 sc1\n"
    "s_mov_b64 exec, -1\n"
    "s_waitcnt vmcnt(0)\n"
    "s_branch .Lrec_FIN_%=\n"
    ".Lrec_retry0_%=:\n"
    "s_cmp_eq_u32 s69, 0\n"
    "s_cbranch_scc1 .Lrec_back0_%=\n"
    "s_waitcnt lgkmcnt(0)\n"
    "ds_read_b128 v[72:75], v1 offset:0\n"
    "ds_read_b128 v[76:79], v1 offset:1024\n"
    ".Lrec_again0_%=:\n"
    "buffer_load_dwordx4 v[4:7], v0, s[36:39], s70 offen sc1\n"
    "s_nop 7\n"
    "v_mov_b64 v[40:41], 0\n"
    "v_mov_b64 v[42:43], 0\n"
    "v_mov_b64 v[44:45], 0\n"
    "v_mov_b64 v[46:47], 0\n"
    "v_mov_b64 v[48:49], 0\n"
    "v_mov_b64 v[50:51], 0\n"
    "v_mov_b64 v[52:53], 0\n"
    "v_mov_b64 v[54:55], 0\n"
    "v_mov_b64 v[56:57], 0\n"
    "v_mov_b64 v[58:59], 0\n"
    "v_mov_b64 v[60:61], 0\n"
    "v_mov_b64 v[62:63], 0\n"
    "s_waitcnt vmcnt(0)\n"
    "v_max3_u32 v3, v4, v5, v6\n"
    "v_max_u32 v3, v3, v7\n"
    "v_cmp_eq_u32 vcc, -1, v3\n"
    "s_cbranch_vccz .Lrec_start_%=\n"
    "s_sub_u32 s69, s69, 1\n"
    "s_cbranch_scc0 .Lrec_again0_%=\n"
    "s_mov_b32 s69, 0\n"
    "s_branch .Lrec_start_%=\n"
    ".Lrec_retry1_%=:\n"
    "s_cmp_eq_u32 s69, 0\n"
    "s_cbranch_scc1 .Lrec_back1_%=\n"
    "s_waitcnt lgkmcnt(0)\n"
    "ds_read_b128 v[72:75], v1 offset:0\n"
    "ds_read_b128 v[76:79], v1 offset:1024\n"
    ".Lrec_again1_%=:\n"
    "buffer_load_dwordx4 v[4:7], v0, s[36:39], s70 offen sc1\n"
    "buffer_load_dwordx4 v[8:11], v0, s[36:39], s70 offen offset:1024 sc1\n"
    "buffer_load_dwordx4 v[12:15], v0, s[36:39], s70 offen offset:2048 sc1\n"
    "buffer_load_dwordx4 v[16:19], v0, s[36:39], s70 offen offset:3072 sc1\n"
    "s_nop 7\n"
    "v_mov_b64 v[40:41], 0\n"
    "v_mov_b64 v[42:43], 0\n"
    "v_mov_b64 v[44:45], 0\n"
    "v_mov_b64 v[46:47], 0\n"
    "v_mov_b64 v[48:49], 0\n"
    "v_mov_b64 v[50:51], 0\n"
    "v_mov_b64 v[52:53], 0\n"
    "v_mov_b64 v[54:55], 0\n"
    "v_mov_b64 v[56:57], 0\n"
    "v_mov_b64 v[58:59], 0\n"
    "v_mov_b64 v[60:61], 0\n"
    "v_mov_b64 v[62:63], 0\n"
    "s_waitcnt vmcnt(0)\n"
    "v_max3_u32 v3, v4, v5, v6\n"
    "v_max3_u32 v3, v3, v7, v8\n"
    "v_max3_u32 v3, v3, v9, v10\n"
    "v_max3_u32 v3, v3, v11, v12\n"
    "v_max3_u32 v3, v3, v13, v14\n"
    "v_max3_u32 v3, v3, v15, v16\n"
    "v_max3_u32 v3, v3, v17, v18\n"
    "v_max_u32 v3, v3, v19\n"
    "v_cmp_eq_u32 vcc, -1, v3\n"
    "s_cbranch_vccz .Lrec_start_%=\n"
    "s_sub_u32 s69, s69, 1\n"
    "s_cbranch_scc0 .Lrec_again1_%=\n"
    "s_mov_b32 s69, 0\n"
    "s_branch .Lrec_start_%=\n"
    ".Lrec_retry2_%=:\n"
    "s_cmp_eq_u32 s69, 0\n"
    "s_cbranch_scc1 .Lrec_back2_%=\n"
    "s_waitcnt lgkmcnt(0)\n"
    "ds_read_b128 v[72:75], v1 offset:0\n"
    "ds_read_b128 v[76:79], v1 offset:1024\n"
    ".Lrec_again2_%=:\n"
    "buffer_load_dwordx4 v[4:7], v0, s[36:39], s70 offen sc1\n"
    "buffer_load_dwordx4 v[8:11], v0, s[36:39], s70 offen offset:1024 sc1\n"
    "buffer_load_dwordx4 v[12:15], v0, s[36:39], s70 offen offset:2048 sc1\n"
    "buffer_load_dwordx4 v[16:19], v0, s[36:39], s70 offen offset:3072 sc1\n"
    "buffer_load_dwordx4 v[20:23], v0, s[36:39], s71 offen sc1\n"
    "buffer_load_dwordx4 v[24:27], v0, s[36:39], s71 offen offset:1024 sc1\n"
    "buffer_load_dwordx4 v[28:31], v0, s[36:39], s71 offen offset:2048 sc1\n"
    "buffer_load_dwordx4 v[32:35], v0, s[36:39], s71 offen offset:3072 sc1\n"
    "buffer_load_dwordx4 v[36:39], v0, s[36:39], s72 offen sc1\n"
    "s_nop 7\n"
    "v_mov_b64 v[40:41], 0\n"
    "v_mov_b64 v[42:43], 0\n"
    "v_mov_b64 v[44:45], 0\n"
    "v_mov_b64 v[46:47], 0\n"
    "v_mov_b64 v[48:49], 0\n"
    "v_mov_b64 v[50:51], 0\n"
    "v_mov_b64 v[52:53], 0\n"
    "v_mov_b64 v[54:55], 0\n"
    "v_mov_b64 v[56:57], 0\n"
    "v_mov_b64 v[58:59], 0\n"
    "v_mov_b64 v[60:61], 0\n"
    "v_mov_b64 v[62:63], 0\n"
    "s_waitcnt vmcnt(0)\n"
    "v_max3_u32 v3, v4, v5, v6\n"
    "v_max3_u32 v3, v3, v7, v8\n"
    "v_max3_u32 v3, v3, v9, v10\n"
    "v_max3_u32 v3, v3, v11, v12\n"
    "v_max3_u32 v3, v3, v13, v14\n"
    "v_max3_u32 v3, v3, v15, v16\n"
    "v_max3_u32 v3, v3, v17, v18\n"
    "v_max3_u32 v3, v3, v19, v20\n"
    "v_max3_u32 v3, v3, v21, v22\n"
    "v_max3_u32 v3, v3, v23, v24\n"
    "v_max3_u32 v3, v3, v25, v26\n"
    "v_max3_u32 v3, v3, v27, v28\n"
    "v_max3_u32 v3, v3, v29, v30\n"
    "v_max3_u32 v3, v3, v31, v32\n"
    "v_max3_u32 v3, v3, v33, v34\n"
    "v_max3_u32 v3, v3, v35, v36\n"
    "v_max3_u32 v3, v3, v37, v38\n"
    "v_max_u32 v3, v3, v39\n"
    "v_cmp_eq_u32 vcc, -1, v3\n"
    "s_cbranch_vccz .Lrec_start_%=\n"
    "s_sub_u32 s69, s69, 1\n"
    "s_cbranch_scc0 .Lrec_again2_%=\n"
    "s_mov_b32 s69, 0\n"
    "s_branch .Lrec_start_%=\n"
    ".Lrec_tsretry_%=:\n"
    "s_mov_b32 s98, 24\n"
    ".Lrec_tsretry1_%=:\n"
    "s_mov_b32 s95, s28\n"
    "s_sub_u32 s98, s98, 1\n"
    "s_cbranch_scc1 .Lrec_tsgo_%=\n"
    "s_sleep 2\n"
    "buffer_load_dwordx2 v[12:13], v11, s[36:39], s90 offen sc1\n"
    "s_waitcnt vmcnt(0)\n"
    "v_max_u32 v12, v12, v13\n"
    "s_nop 1\n"
    "v_max_u32_dpp v12, v12, v12 row_ror:8 row_mask:0xf bank_mask:0xf\n"
    "s_nop 1\n"
    "v_max_u32_dpp v12, v12, v12 row_ror:4 row_mask:0xf bank_mask:0xf\n"
    "s_nop 1\n"
    "v_max_u32_dpp v12, v12, v12 row_ror:2 row_mask:0xf bank_mask:0xf\n"
    "s_nop 1\n"
    "v_max_u32_dpp v12, v12, v12 row_ror:1 row_mask:0xf bank_mask:0xf\n"
    "s_nop 0\n"
    "v_readlane_b32 s95, v12, 0\n"
    "v_readlane_b32 s99, v12, 16\n"
    "v_readlane_b32 s30, v12, 32\n"
    "v_readlane_b32 s31, v12, 48\n"
    "s_max_u32 s95, s95, s99\n"
    "s_max_u32 s30, s30, s31\n"
    "s_max_u32 s95, s95, s30\n"
    "s_cmp_eq_u32 s95, -1\n"
    "s_cbranch_scc1 .Lrec_tsretry1_%=\n"
    "s_branch .Lrec_tsgo_%=\n"
    ".Lrec_rretry_%=:\n"
    "s_sub_u32 s69, s69, 1\n"
    "s_cbranch_scc1 .Lrec_rdead_%=\n"
    "s_nop 7\n"
    "buffer_load_dwordx4 v[4:7], v0, s[36:39], s78 offen sc1\n"
    "buffer_load_dwordx4 v[8:11], v0, s[36:39], s78 offen offset:1024 sc1\n"
    "buffer_load_dwordx4 v[12:15], v0, s[36:39], s78 offen offset:2048 sc1\n"
    "buffer_load_dwordx4 v[16:19], v0, s[36:39], s78 offen offset:3072 sc1\n"
    "buffer_load_dwordx4 v[20:23], v0, s[36:39], s88 offen sc1\n"
    "buffer_load_dwordx4 v[24:27], v0, s[36:39], s88 offen offset:1024 sc1\n"
    "buffer_load_dwordx4 v[28:31], v0, s[36:39], s88 offen offset:2048 sc1\n"
    "buffer_load_dwordx4 v[32:35], v0, s[36:39], s88 offen offset:3072 sc1\n"
    "s_waitcnt vmcnt(0)\n"
    "s_branch .Lrec_rsum_%=\n"
    ".Lrec_rdead_%=:\n"
    "s_mov_b32 s69, 0\n"
    "s_nop 7\n"
    "s_branch .Lrec_rgo_%=\n"
    ".Lrec_nored_%=:\n"
    "s_nop 15\n"
    "s_cmp_lt_i32 s64, 0\n"
    "s_cbranch_scc0 .Lrec_red_%=\n"
    "v_lshl_or_b32 v77, s68, 1, 1\n"
    "s_waitcnt vmcnt(3)\n"
    "s_mov_b64 exec, 1\n"
    "buffer_atomic_smax v77, off, s[36:39], s58 sc1\n"
    "s_mov_b64 exec, -1\n"
    "v_mov_b32 v3, -1\n"
    "v_subrev_u32 v1, 81920, v1\n"
    "s_sub_u32 s52, s52, 24\n"
    "s_max_u32 s52, s52, 900\n"
    "s_sub_u32 s94, s52, 900\n"
    "s_lshr_b32 s94, s94, 3\n"
    "s_sub_u32 s95, s52, s35\n"
    "s_mul_i32 s95, s48, s95\n"
    "s_lshr_b32 s95, s95, 10\n"
    "s_add_u32 s32, s32, s95\n"
    "s_add_u32 s68, s68, 1\n"
    "s_and_b32 s70, s68, 3\n"
    "s_lshl_b32 s70, s70, 17\n"
    "s_add_u32 s70, s70, s66\n"
    "s_add_u32 s71, s70, 4096\n"
    "s_add_u32 s72, s70, 16384\n"
    "s_sub_u32 s46, s33, s46\n"
    "s_sub_u32 s47, s28, s47\n"
    "v_cvt_f32_u32 v40, s46\n"
    "v_cvt_f32_u32 v41, s47\n"
    "v_rcp_f32 v41, v41\n"
    "s_mov_b32 s46, s33\n"
    "s_mov_b32 s47, s28\n"
    "v_mul_f32 v40, v40, v41\n"
    "v_mov_b32 v42, 0x40c80000\n"
    "v_med3_f32 v40, v40, 4.0, v42\n"
    "v_rcp_f32 v41, v40\n"
    "v_mul_f32 v40, 0x43800000, v40\n"
    "v_mul_f32 v41, 0x44b54000, v41\n"
    "v_cvt_u32_f32 v40, v40\n"
    "v_cvt_u32_f32 v41, v41\n"
    "v_readfirstlane_b32 s48, v40\n"
    "v_readfirstlane_b32 s49, v41\n"
    "v_mov_b64 v[40:41], 0\n"
    "v_mov_b64 v[42:43], 0\n"
    "v_mov_b64 v[44:45], 0\n"
    "v_mov_b64 v[46:47], 0\n"
    "v_mov_b64 v[48:49], 0\n"
    "v_mov_b64 v[50:51], 0\n"
    "v_mov_b64 v[52:53], 0\n"
    "v_mov_b64 v[54:55], 0\n"
    "v_mov_b64 v[56:57], 0\n"
    "v_mov_b64 v[58:59], 0\n"
    "v_mov_b64 v[60:61], 0\n"
    "v_mov_b64 v[62:63], 0\n"
    "s_mov_b64 exec, 1\n"
    "buffer_atomic_smax v3, off, s[36:39], s59 sc0 sc1\n"
    "s_mov_b64 exec, -1\n"
    "s_waitcnt vmcnt(0)\n"
    "s_branch .Lrec_b3_%=\n"
    ".Lrec_waitp_%=:\n"
    "v_mov_b32 v3, -1\n"
    "s_mov_b64 exec, 1\n"
    "buffer_atomic_smax v3, off, s[36:39], s59 sc0 sc1\n"
    "s_mov_b64 exec, -1\n"
    "s_waitcnt vmcnt(0)\n"
    "v_readfirstlane_b32 s81, v3\n"
    "s_nop 0\n"
    "s_cmp_gt_i32 s81, s73\n"
    "s_cbranch_scc1 .Lrec_waitp1_%=\n"
    "s_sleep 1\n"
    "s_sub_u32 s69, s69, 1\n"
    "s_cbranch_scc0 .Lrec_waitp_%=\n"
    "s_mov_b32 s69, 0\n"
    ".Lrec_waitp1_%=:\n"
    "s_memtime s[96:97]\n"
    "s_waitcnt lgkmcnt(0)\n"
    "s_sub_u32 s95, s32, s96\n"
    "s_mul_i32 s95, s95, 43\n"
    "s_ashr_i32 s95, s95, 10\n"
    "s_max_i32 s95, s95, 0\n"
    "s_min_i32 s95, s95, 100\n"
    ".Lrec_gloop2_%=:\n"
    "s_sub_u32 s95, s95, 1\n"
    "s_cbranch_scc0 .Lrec_gloop2_%=\n"
    "s_branch .Lrec_top_%=\n"
    ".Lrec_FIN_%=:\n"
  );
}

typedef unsigned long long u64;
DEVI u64 ld8u(rsrc_t r, u32 voff) { v2u v = ld8(r, voff, 0); asm volatile("" : "+v"(v)); return (u64)v.x | ((u64)v.y << 32); }
DEVI u32 find_shift(rsrc_t rw, bool decide, u64 tag) {
  const int lane = threadIdx.x & 63;
  unsigned budget = SPIN_LIMIT;
  if (decide) {
    u32 best[2] = {~0u, ~0u};
    const u32 probe = 126 * CHUNK + 8192;
#pragma unroll 1
    for (int r = 0; r < 5; ++r) {
      const u32 a = lane == 0 ? probe + (u32)(r & 1) * 8192 : OOB;
      u32 v = 0;
      const unsigned long long t0 = __builtin_amdgcn_s_memtime();
      asm volatile("s_waitcnt lgkmcnt(0)\n\tbuffer_atomic_or %0, %1, %2, 0 offen sc0 sc1\n\ts_waitcnt vmcnt(0)" : "+v"(v) : "v"(a), "s"(rw) : "memory");
      const u32 dt = (u32)(__builtin_amdgcn_s_memtime() - t0);
      if (r & 1) best[1] = min(best[1], dt); else best[0] = min(best[0], dt);
    }
    for (;;) {
      v2u v[4];
      const u32 a = READY + (u32)lane * 8;
#pragma unroll
      for (int k = 0; k < 4; ++k) v[k] = (v2u){0u, 0u};
      asm volatile("buffer_atomic_or_x2 %0, %4, %5, 0 offen sc0 sc1\n\tbuffer_atomic_or_x2 %1, %4, %5, 0 offen offset:512 sc0 sc1\n\t"
                   "buffer_atomic_or_x2 %2, %4, %5, 0 offen offset:1024 sc0 sc1\n\tbuffer_atomic_or_x2 %3, %4, %5, 0 offen offset:1536 sc0 sc1\n\ts_waitcnt vmcnt(0)"
                   : "+v"(v[0]), "+v"(v[1]), "+v"(v[2]), "+v"(v[3]) : "v"(a), "s"(rw) : "memory");
      bool ok = true;
#pragma unroll
      for (int k = 0; k < 4; ++k) ok = ok & (v[k].x == (u32)tag) & (v[k].y == (u32)(tag >> 32));
      if (__all(ok) || budget == 0) break;
      --budget;
    }
    const u64 v = lane == 0 ? 2 * tag + (best[0] < best[1] ? 0u : 1u) : tag;
    st8(rw, (v2u){(u32)v, (u32)(v >> 32)}, lane == 0 ? PARW : (lane == 1 ? EPOCH : OOB), 0);
  }
  u64 v;
  for (;;) {
    cbar();
    v = ld8u(rw, PARW) ^ (2 * tag);
    if (__all(v < 2) || budget == 0) break;
    --budget;
    __builtin_amdgcn_s_sleep(4);
  }
  cbar();
  return (rfl((u32)v) & 1u) * 8192;
}
DEVI void hot_init(const Params& p, rsrc_t rw, int L, int n, u32 shift) {
  const int tid = threadIdx.x, w = (int)rfl(tid >> 6);
  const u32 hot = HOT + L * 8192 + shift, hot2 = HOT2 + (1 - L) * 8192 + shift;
  const v4u ones = {~0u, ~0u, ~0u, ~0u};
#pragma unroll
  for (int k = 0; k < 9; ++k) {
    st16(rw, ones, tid * 16, hot + (u32)(9 * n + k) * CHUNK);
    st16(rw, ones, tid * 16 + 4096, hot + (u32)(9 * n + k) * CHUNK);
  }
  {
    const u32 b = hot2 + (u32)(4 + n) * 2 * CHUNK;
    st16(rw, ones, tid * 16, b); st16(rw, ones, tid * 16 + 4096, b);
    st16(rw, ones, w == 0 ? (u32)tid * 16 : OOB, b + CHUNK);
  }
  if (n < 8) {
#pragma unroll
    for (int k = 0; k < 2; ++k) {
      st16(rw, ones, tid * 16, hot2 + (u32)(32 + 2 * n + k) * CHUNK);
      st16(rw, ones, tid * 16 + 4096, hot2 + (u32)(32 + 2 * n + k) * CHUNK);
    }
  }
  if (n >= 8) {
    const u32 b = hot2 + (u32)(n - 8) * 2 * CHUNK;
#pragma unroll
    for (int k = 0; k < 3; ++k) {
      const int pc = tid + 256 * k;
      if (pc < 576) {
        v4u v = *(const v4u*)(p.mem0 + (pc & 7) * 576 + (pc >> 3) * 8);
#pragma unroll
        for (int d = 0; d < 4; ++d) v[d] = min(v[d], 0xFFFFFFFEu);
        st16(rw, v, pc < 512 ? (u32)pc * 16 : CHUNK + (u32)(pc - 512) * 16, b);
      }
    }
  }
  drain();
  __syncthreads();
  if (tid == 0) st4(rw, 0u, INITF + L * 8192 + n * 128 + shift, 0);
}

DEVI void role_att(const Params& p, int L, int j, int a, u32 dbg, u32 shift) {
  const int tid = threadIdx.x, lane = tid & 63, w = (int)rfl(tid >> 6);
  const int c = lane & 15, g = lane >> 4;
  const int hl = 4 * a + w, h = 8 * j + hl;
  const v4u ws = make_srd(p.ws + shift, WS_END);
  v16u sa = cat4(ws, ws, ws, ws);
  v16u sb = {pga(L, B_QKV + 3 * h), pga(L, B_KVM + h), pgb(L, B_O + h), 0, dbg + w * 64,
             0, 0, 0, 0, 0, 0, 0, 0, 0, 0, 0};
  v16u va = {0, 0, 0, 0, 0, 0, 0, 0, 0, 0, 0, 0, 0, 0, 0, 0};
  va[0] = lane * 16;
  va[1] = (u32)(g * 128 + (c & 7) * 16);
  va[2] = lane < 3 ? mbox(L, j, SL_WO + lane, hl) : mbox(L, j, SL_P + hl, 48);
  va[4] = lane < 16 ? mbox_att(L, j, hl, lane) : OOB;
  va[5] = (u32)(lane < 4 ? 0 : (lane < 7 ? 2 * RO : ((lane >> 2) == 2 ? -2 : 1000)));
  va[6] = (u32)(c - 4 * g);
  va[7] = 0;
  RUN_ASM(
    "s_nop 4\n"
    "s_mov_b32 s20, 0\n"
    "s_mov_b32 s21, 0x80000000\n"
    "s_getpc_b64 s[84:85]\n"
    ".Latt_PC_%=:\n"
    "s_and_b32 s85, s85, 0xffff\n"
    "s_mov_b32 s86, .Latt_FIN_%=-.Latt_PC_%=\n"
    "s_mov_b32 s87, 0x20000\n"
    "v_mbcnt_lo_u32_b32 v240, -1, 0\n"
    "v_mbcnt_hi_u32_b32 v240, -1, v240\n"
    "v_lshlrev_b32 v240, 7, v240\n"
    "buffer_load_dword v241, v240, s[84:87], 0 offen\n"
    "v_add_u32 v240, 8192, v240\n"
    "buffer_load_dword v241, v240, s[84:87], 0 offen\n"
    "v_cmp_gt_i32 s[10:11], 0, v6\n"
    "v_cmp_gt_i32 s[12:13], 1, v6\n"
    "v_cmp_gt_i32 s[14:15], 2, v6\n"
    "v_cmp_gt_i32 s[16:17], 3, v6\n"
    "v_mov_b32 v10, 0xff61b1e6\n"
    "s_mov_b32 s18, 0x3e0293ee\n"
    "s_mov_b32 s68, 0\n"
    "s_mov_b32 s77, 1\n"
    "v_mov_b32 v8, -1\n"
    "v_mov_b32 v12, -1\n"
    "s_mov_b32 s69, 262144\n"
    "s_branch .Latt_waitp_%=\n"
    ".Latt_LOOPB_%=:\n"
    ".Latt_top_%=:\n"
    "s_waitcnt vmcnt(15)\n"
    "v_mfma_f32_16x16x32_bf16 v[160:163], v[16:19], v[48:51], 0\n"
    "v_mfma_f32_16x16x32_bf16 v[164:167], v[16:19], v[52:55], 0\n"
    "v_mfma_f32_16x16x32_bf16 v[168:171], v[20:23], v[52:55], 0\n"
    "s_waitcnt vmcnt(12)\n"
    "v_mfma_f32_16x16x32_bf16 v[160:163], v[24:27], v[56:59], v[160:163]\n"
    "v_mfma_f32_16x16x32_bf16 v[164:167], v[24:27], v[60:63], v[164:167]\n"
    "v_mfma_f32_16x16x32_bf16 v[168:171], v[28:31], v[60:63], v[168:171]\n"
    "s_waitcnt vmcnt(10)\n"
    "v_mfma_f32_16x16x32_bf16 v[160:163], v[32:35], v[64:67], v[160:163]\n"
    "v_mfma_f32_16x16x32_bf16 v[164:167], v[32:35], v[68:71], v[164:167]\n"
    "v_mfma_f32_16x16x32_bf16 v[168:171], v[36:39], v[68:71], v[168:171]\n"
    "s_waitcnt vmcnt(8)\n"
    "v_mfma_f32_16x16x32_bf16 v[160:163], v[40:43], v[72:75], v[160:163]\n"
    "v_mfma_f32_16x16x32_bf16 v[164:167], v[40:43], v[76:79], v[164:167]\n"
    "v_mfma_f32_16x16x32_bf16 v[168:171], v[44:47], v[76:79], v[168:171]\n"
    "v_add_u32 v11, v8, v5\n"
    "v_cmp_gt_i32 vcc, s77, v11\n"
    "s_nop 0\n"
    "s_or_b64 exec, vcc, s[20:21]\n"
    "buffer_atomic_smax v8, v4, s[36:39], 0 offen sc0 sc1\n"
    "s_mov_b64 exec, -1\n"
    "s_add_u32 s72, s68, 1\n"
    "s_and_b32 s4, s72, 7\n"
    "s_mul_i32 s4, s4, 4194304\n"
    "s_add_u32 s4, s4, s52\n"
    "s_add_u32 s5, s4, 4096\n"
    "s_add_u32 s6, s4, 16384\n"
    "s_add_u32 s7, s4, 20480\n"
    "s_add_u32 s8, s4, 32768\n"
    "s_add_u32 s9, s4, 36864\n"
    "s_and_b32 s73, s68, 7\n"
    "s_mul_i32 s73, s73, 4194304\n"
    "s_add_u32 s73, s73, s53\n"
    "s_add_u32 s74, s73, 3072\n"
    "s_and_b32 s75, s68, 7\n"
    "s_lshl_b32 s75, s75, 22\n"
    "s_add_u32 s75, s75, s54\n"
    "s_add_u32 s76, s75, 4096\n"
    "v_cndmask_b32 v160, v160, v10, s[10:11]\n"
    "v_cndmask_b32 v161, v161, v10, s[12:13]\n"
    "v_cndmask_b32 v162, v162, v10, s[14:15]\n"
    "v_cndmask_b32 v163, v163, v10, s[16:17]\n"
    "v_cndmask_b32 v168, v168, v10, s[10:11]\n"
    "v_cndmask_b32 v169, v169, v10, s[12:13]\n"
    "v_cndmask_b32 v170, v170, v10, s[14:15]\n"
    "v_cndmask_b32 v171, v171, v10, s[16:17]\n"
    "v_max3_f32 v224, v160, v161, v162\n"
    "v_max3_f32 v226, v164, v165, v166\n"
    "v_max_f32 v224, v224, v163\n"
    "v_max3_f32 v226, v226, v167, v168\n"
    "v_max3_f32 v226, v226, v169, v170\n"
    "v_max_f32 v226, v226, v171\n"
    "v_mov_b32 v238, v224\n"
    "v_mov_b32 v239, v226\n"
    "s_nop 0\n"
    "v_permlane16_swap_b32 v224, v238\n"
    "v_permlane16_swap_b32 v226, v239\n"
    "v_max_f32 v224, v224, v238\n"
    "v_max_f32 v226, v226, v239\n"
    "v_mov_b32 v238, v224\n"
    "v_mov_b32 v239, v226\n"
    "s_nop 0\n"
    "v_permlane32_swap_b32 v224, v238\n"
    "v_permlane32_swap_b32 v226, v239\n"
    "v_max_f32 v224, v224, v238\n"
    "v_max_f32 v226, v226, v239\n"
    "v_mul_f32 v230, 0xbe0293ee, v224\n"
    "v_mul_f32 v232, 0xbe0293ee, v226\n"
    "v_pk_fma_f32 v[160:161], v[160:161], s[18:19], v[230:231] op_sel_hi:[1,0,0]\n"
    "v_pk_fma_f32 v[162:163], v[162:163], s[18:19], v[230:231] op_sel_hi:[1,0,0]\n"
    "v_pk_fma_f32 v[164:165], v[164:165], s[18:19], v[232:233] op_sel_hi:[1,0,0]\n"
    "v_pk_fma_f32 v[166:167], v[166:167], s[18:19], v[232:233] op_sel_hi:[1,0,0]\n"
    "v_pk_fma_f32 v[168:169], v[168:169], s[18:19], v[232:233] op_sel_hi:[1,0,0]\n"
    "v_pk_fma_f32 v[170:171], v[170:171], s[18:19], v[232:233] op_sel_hi:[1,0,0]\n"
    "v_exp_f32 v160, v160\n"
    "v_exp_f32 v161, v161\n"
    "v_exp_f32 v162, v162\n"
    "v_exp_f32 v163, v163\n"
    "v_exp_f32 v164, v164\n"
    "v_exp_f32 v165, v165\n"
    "v_exp_f32 v166, v166\n"
    "v_exp_f32 v167, v167\n"
    "v_exp_f32 v168, v168\n"
    "v_exp_f32 v169, v169\n"
    "v_exp_f32 v170, v170\n"
    "v_exp_f32 v171, v171\n"
    "v_pk_add_f32 v[230:231], v[160:161], v[162:163]\n"
    "v_pk_add_f32 v[232:233], v[164:165], v[166:167]\n"
    "v_pk_add_f32 v[234:235], v[168:169], v[170:171]\n"
    "v_pk_add_f32 v[232:233], v[232:233], v[234:235]\n"
    "v_add_f32 v228, v230, v231\n"
    "v_add_f32 v229, v232, v233\n"
    "v_mov_b32 v238, v228\n"
    "v_mov_b32 v239, v229\n"
    "s_nop 0\n"
    "v_permlane16_swap_b32 v228, v238\n"
    "v_permlane16_swap_b32 v229, v239\n"
    "v_add_f32 v228, v228, v238\n"
    "v_add_f32 v229, v229, v239\n"
    "v_mov_b32 v238, v228\n"
    "v_mov_b32 v239, v229\n"
    "s_nop 0\n"
    "v_permlane32_swap_b32 v228, v238\n"
    "v_permlane32_swap_b32 v229, v239\n"
    "v_add_f32 v228, v228, v238\n"
    "v_add_f32 v229, v229, v239\n"
    "v_add_u32 v11, v12, v5\n"
    "v_cmp_gt_i32 vcc, s77, v11\n"
    "s_nop 0\n"
    "s_or_b64 exec, vcc, s[20:21]\n"
    "buffer_atomic_smax v12, v4, s[36:39], 0 offen sc0 sc1\n"
    "s_mov_b64 exec, -1\n"
    "s_waitcnt vmcnt(1)\n"
    "v_add_u32 v11, v8, v5\n"
    "v_cmp_gt_i32 vcc, s77, v11\n"
    "s_nop 0\n"
    "s_and_b32 s71, vcc_lo, 0xff\n"
    "s_cbranch_scc1 .Latt_waitr_%=\n"
    ".Latt_gor_%=:\n"
    "buffer_load_dwordx4 v[128:131], v1, s[36:39], s73 offen sc1\n"
    "buffer_load_dwordx4 v[132:135], v1, s[36:39], s73 offen offset:1536 sc1\n"
    "buffer_load_dwordx4 v[136:139], v1, s[36:39], s74 offen sc1\n"
    "buffer_load_dwordx4 v[140:143], v1, s[36:39], s74 offen offset:1536 sc1\n"
    "buffer_load_dwordx4 v[144:147], v0, s[36:39], s73 offen offset:512 sc1\n"
    "buffer_load_dwordx4 v[148:151], v0, s[36:39], s73 offen offset:2048 sc1\n"
    "buffer_load_dwordx4 v[152:155], v0, s[36:39], s74 offen offset:512 sc1\n"
    "buffer_load_dwordx4 v[156:159], v0, s[36:39], s74 offen offset:2048 sc1\n"
    "s_and_b32 s71, vcc_lo, 0xf00\n"
    "s_cselect_b32 s70, 0, 1\n"
    "s_cselect_b32 s71, 0x80000000, 0\n"
    "v_or_b32 v7, s71, v0\n"
    "s_nop 15\n"
    "buffer_load_dwordx4 v[16:19], v7, s[36:39], s6 offen sc1\n"
    "s_nop 15\n"
    "buffer_load_dwordx4 v[20:23], v7, s[36:39], s6 offen offset:1024 sc1\n"
    "s_waitcnt vmcnt(9)\n"
    "v_mfma_f32_16x16x32_bf16 v[172:175], v[128:131], v[48:51], 0\n"
    "v_mfma_f32_16x16x32_bf16 v[176:179], v[128:131], v[52:55], 0\n"
    "s_waitcnt vmcnt(8)\n"
    "v_mfma_f32_16x16x32_bf16 v[172:175], v[132:135], v[56:59], v[172:175]\n"
    "v_mfma_f32_16x16x32_bf16 v[176:179], v[132:135], v[60:63], v[176:179]\n"
    "s_waitcnt vmcnt(7)\n"
    "v_mfma_f32_16x16x32_bf16 v[172:175], v[136:139], v[64:67], v[172:175]\n"
    "v_mfma_f32_16x16x32_bf16 v[176:179], v[136:139], v[68:71], v[176:179]\n"
    "s_waitcnt vmcnt(6)\n"
    "v_mfma_f32_16x16x32_bf16 v[172:175], v[140:143], v[72:75], v[172:175]\n"
    "v_mfma_f32_16x16x32_bf16 v[176:179], v[140:143], v[76:79], v[176:179]\n"
    "s_waitcnt vmcnt(2)\n"
    "v_mov_b64 v[80:81], v[144:145]\n"
    "v_mov_b64 v[86:87], v[146:147]\n"
    "v_mov_b64 v[92:93], v[148:149]\n"
    "v_mov_b64 v[98:99], v[150:151]\n"
    "v_mov_b64 v[104:105], v[152:153]\n"
    "v_mov_b64 v[110:111], v[154:155]\n"
    "v_mov_b64 v[116:117], v[156:157]\n"
    "v_mov_b64 v[122:123], v[158:159]\n"
    "v_max3_f32 v230, v172, v173, v174\n"
    "v_max3_f32 v232, v176, v177, v178\n"
    "v_max3_f32 v230, v230, v175, v224\n"
    "v_max3_f32 v232, v232, v179, v226\n"
    "v_mov_b32 v238, v230\n"
    "v_mov_b32 v239, v232\n"
    "s_nop 0\n"
    "v_permlane16_swap_b32 v230, v238\n"
    "v_permlane16_swap_b32 v232, v239\n"
    "v_max_f32 v230, v230, v238\n"
    "v_max_f32 v232, v232, v239\n"
    "v_mul_f32 v230, 0xbe0293ee, v230\n"
    "v_mul_f32 v232, 0xbe0293ee, v232\n"
    "v_pk_fma_f32 v[172:173], v[172:173], s[18:19], v[230:231] op_sel_hi:[1,0,0]\n"
    "v_pk_fma_f32 v[174:175], v[174:175], s[18:19], v[230:231] op_sel_hi:[1,0,0]\n"
    "v_pk_fma_f32 v[176:177], v[176:177], s[18:19], v[232:233] op_sel_hi:[1,0,0]\n"
    "v_pk_fma_f32 v[178:179], v[178:179], s[18:19], v[232:233] op_sel_hi:[1,0,0]\n"
    "v_fma_f32 v224, v224, s18, v230\n"
    "v_fma_f32 v226, v226, s18, v232\n"
    "v_exp_f32 v172, v172\n"
    "v_exp_f32 v173, v173\n"
    "v_exp_f32 v174, v174\n"
    "v_exp_f32 v175, v175\n"
    "buffer_load_dwordx4 v[24:27], v7, s[36:39], s6 offen offset:2048 sc1\n"
    "v_exp_f32 v176, v176\n"
    "v_exp_f32 v177, v177\n"
    "v_exp_f32 v178, v178\n"
    "v_exp_f32 v179, v179\n"
    "buffer_load_dwordx4 v[28:31], v7, s[36:39], s6 offen offset:3072 sc1\n"
    "v_exp_f32 v224, v224\n"
    "v_exp_f32 v226, v226\n"
    "v_pk_add_f32 v[230:231], v[172:173], v[174:175]\n"
    "v_pk_add_f32 v[232:233], v[176:177], v[178:179]\n"
    "v_add_f32 v230, v230, v231\n"
    "v_add_f32 v232, v232, v233\n"
    "v_mov_b32 v238, v230\n"
    "v_mov_b32 v239, v232\n"
    "s_nop 0\n"
    "v_permlane16_swap_b32 v230, v238\n"
    "v_permlane16_swap_b32 v232, v239\n"
    "v_add_f32 v230, v230, v238\n"
    "v_add_f32 v232, v232, v239\n"
    "v_fmac_f32 v230, v228, v224\n"
    "v_fmac_f32 v232, v229, v226\n"
    "v_rcp_f32 v230, v230\n"
    "v_rcp_f32 v232, v232\n"
    "buffer_load_dwordx4 v[32:35], v7, s[36:39], s7 offen sc1\n"
    "v_mul_f32 v224, v224, v230\n"
    "v_mul_f32 v226, v226, v232\n"
    "v_pk_mul_f32 v[164:165], v[164:165], v[226:227] op_sel_hi:[1,0]\n"
    "v_pk_mul_f32 v[166:167], v[166:167], v[226:227] op_sel_hi:[1,0]\n"
    "v_pk_mul_f32 v[168:169], v[168:169], v[226:227] op_sel_hi:[1,0]\n"
    "v_pk_mul_f32 v[170:171], v[170:171], v[226:227] op_sel_hi:[1,0]\n"
    "v_pk_mul_f32 v[176:177], v[176:177], v[232:233] op_sel_hi:[1,0]\n"
    "v_pk_mul_f32 v[178:179], v[178:179], v[232:233] op_sel_hi:[1,0]\n"
    "v_pk_mul_f32 v[172:173], v[172:173], v[230:231] op_sel_hi:[1,0]\n"
    "v_pk_mul_f32 v[174:175], v[174:175], v[230:231] op_sel_hi:[1,0]\n"
    "v_pk_mul_f32 v[160:161], v[160:161], v[224:225] op_sel_hi:[1,0]\n"
    "v_pk_mul_f32 v[162:163], v[162:163], v[224:225] op_sel_hi:[1,0]\n"
    "v_cvt_pk_bf16_f32 v216, v164, v165\n"
    "v_cvt_pk_bf16_f32 v217, v166, v167\n"
    "v_cvt_pk_bf16_f32 v218, v168, v169\n"
    "v_cvt_pk_bf16_f32 v219, v170, v171\n"
    "v_cvt_pk_bf16_f32 v220, v176, v177\n"
    "v_cvt_pk_bf16_f32 v221, v178, v179\n"
    "v_cvt_pk_bf16_f32 v212, v172, v173\n"
    "v_cvt_pk_bf16_f32 v213, v174, v175\n"
    "v_cvt_pk_bf16_f32 v214, v160, v161\n"
    "v_cvt_pk_bf16_f32 v215, v162, v163\n"
    "v_mfma_f32_16x16x32_bf16 v[188:191], v[82:85], v[216:219], 0\n"
    "v_mfma_f32_16x16x32_bf16 v[192:195], v[88:91], v[216:219], 0\n"
    "v_mfma_f32_16x16x32_bf16 v[180:183], v[80:83], v[212:215], 0\n"
    "v_mfma_f32_16x16x32_bf16 v[184:187], v[86:89], v[212:215], 0\n"
    "v_mfma_f32_16x16x16_bf16 v[188:191], v[80:81], v[220:221], v[188:191]\n"
    "v_mfma_f32_16x16x16_bf16 v[192:195], v[86:87], v[220:221], v[192:195]\n"
    "v_mfma_f32_16x16x32_bf16 v[204:207], v[94:97], v[216:219], 0\n"
    "v_mfma_f32_16x16x32_bf16 v[208:211], v[100:103], v[216:219], 0\n"
    "v_cvt_pk_bf16_f32 v180, v180, v181\n"
    "v_cvt_pk_bf16_f32 v181, v182, v183\n"
    "v_mfma_f32_16x16x32_bf16 v[196:199], v[92:95], v[212:215], 0\n"
    "v_cvt_pk_bf16_f32 v182, v184, v185\n"
    "v_cvt_pk_bf16_f32 v183, v186, v187\n"
    "v_mfma_f32_16x16x32_bf16 v[200:203], v[98:101], v[212:215], 0\n"
    "buffer_store_dwordx4 v[180:183], v0, s[36:39], s75 offen sc1\n"
    "v_cvt_pk_bf16_f32 v188, v188, v189\n"
    "v_mfma_f32_16x16x16_bf16 v[204:207], v[92:93], v[220:221], v[204:207]\n"
    "v_cvt_pk_bf16_f32 v189, v190, v191\n"
    "v_cvt_pk_bf16_f32 v190, v192, v193\n"
    "v_mfma_f32_16x16x16_bf16 v[208:211], v[98:99], v[220:221], v[208:211]\n"
    "v_cvt_pk_bf16_f32 v191, v194, v195\n"
    "buffer_store_dwordx4 v[188:191], v0, s[36:39], s75 offen offset:1024 sc1\n"
    "s_nop 1\n"
    "v_mfma_f32_16x16x32_bf16 v[188:191], v[106:109], v[216:219], 0\n"
    "v_mfma_f32_16x16x32_bf16 v[192:195], v[112:115], v[216:219], 0\n"
    "v_cvt_pk_bf16_f32 v196, v196, v197\n"
    "v_cvt_pk_bf16_f32 v197, v198, v199\n"
    "v_mfma_f32_16x16x32_bf16 v[180:183], v[104:107], v[212:215], 0\n"
    "v_cvt_pk_bf16_f32 v198, v200, v201\n"
    "v_cvt_pk_bf16_f32 v199, v202, v203\n"
    "v_mfma_f32_16x16x32_bf16 v[184:187], v[110:113], v[212:215], 0\n"
    "buffer_store_dwordx4 v[196:199], v0, s[36:39], s75 offen offset:2048 sc1\n"
    "v_cvt_pk_bf16_f32 v204, v204, v205\n"
    "v_mfma_f32_16x16x16_bf16 v[188:191], v[104:105], v[220:221], v[188:191]\n"
    "v_cvt_pk_bf16_f32 v205, v206, v207\n"
    "v_cvt_pk_bf16_f32 v206, v208, v209\n"
    "v_mfma_f32_16x16x16_bf16 v[192:195], v[110:111], v[220:221], v[192:195]\n"
    "v_cvt_pk_bf16_f32 v207, v210, v211\n"
    "buffer_store_dwordx4 v[204:207], v0, s[36:39], s75 offen offset:3072 sc1\n"
    "s_nop 1\n"
    "v_mfma_f32_16x16x32_bf16 v[204:207], v[118:121], v[216:219], 0\n"
    "v_mfma_f32_16x16x32_bf16 v[208:211], v[124:127], v[216:219], 0\n"
    "v_cvt_pk_bf16_f32 v180, v180, v181\n"
    "v_cvt_pk_bf16_f32 v181, v182, v183\n"
    "v_mfma_f32_16x16x32_bf16 v[196:199], v[116:119], v[212:215], 0\n"
    "v_cvt_pk_bf16_f32 v182, v184, v185\n"
    "v_cvt_pk_bf16_f32 v183, v186, v187\n"
    "v_mfma_f32_16x16x32_bf16 v[200:203], v[122:125], v[212:215], 0\n"
    "buffer_store_dwordx4 v[180:183], v0, s[36:39], s76 offen sc1\n"
    "v_cvt_pk_bf16_f32 v188, v188, v189\n"
    "v_mfma_f32_16x16x16_bf16 v[204:207], v[116:117], v[220:221], v[204:207]\n"
    "v_cvt_pk_bf16_f32 v189, v190, v191\n"
    "v_cvt_pk_bf16_f32 v190, v192, v193\n"
    "v_mfma_f32_16x16x16_bf16 v[208:211], v[122:123], v[220:221], v[208:211]\n"
    "v_cvt_pk_bf16_f32 v191, v194, v195\n"
    "buffer_store_dwordx4 v[188:191], v0, s[36:39], s76 offen offset:1024 sc1\n"
    "v_cvt_pk_bf16_f32 v196, v196, v197\n"
    "v_cvt_pk_bf16_f32 v197, v198, v199\n"
    "v_cvt_pk_bf16_f32 v198, v200, v201\n"
    "v_cvt_pk_bf16_f32 v199, v202, v203\n"
    "buffer_store_dwordx4 v[196:199], v0, s[36:39], s76 offen offset:2048 sc1\n"
    "v_cvt_pk_bf16_f32 v204, v204, v205\n"
    "v_cvt_pk_bf16_f32 v205, v206, v207\n"
    "v_cvt_pk_bf16_f32 v206, v208, v209\n"
    "v_cvt_pk_bf16_f32 v207, v210, v211\n"
    "buffer_store_dwordx4 v[204:207], v0, s[36:39], s76 offen offset:3072 sc1\n"
    "s_nop 13\n"
    "buffer_load_dwordx4 v[36:39], v7, s[36:39], s7 offen offset:1024 sc1\n"
    "s_nop 13\n"
    "buffer_load_dwordx4 v[40:43], v7, s[36:39], s7 offen offset:2048 sc1\n"
    "s_nop 13\n"
    "buffer_load_dwordx4 v[44:47], v7, s[36:39], s7 offen offset:3072 sc1\n"
    "s_nop 13\n"
    "buffer_load_dwordx4 v[48:51], v7, s[36:39], s4 offen sc1\n"
    "s_nop 13\n"
    "buffer_load_dwordx4 v[52:55], v7, s[36:39], s4 offen offset:1024 sc1\n"
    "v_mov_b32 v9, s77\n"
    "s_waitcnt vmcnt(5)\n"
    "s_mov_b64 exec, 15\n"
    "buffer_atomic_smax v9, v2, s[36:39], 0 offen sc1\n"
    "s_mov_b64 exec, -1\n"
    "buffer_load_dwordx4 v[56:59], v7, s[36:39], s4 offen offset:2048 sc1\n"
    "buffer_load_dwordx4 v[60:63], v7, s[36:39], s4 offen offset:3072 sc1\n"
    "buffer_load_dwordx4 v[64:67], v7, s[36:39], s5 offen sc1\n"
    "buffer_load_dwordx4 v[68:71], v7, s[36:39], s5 offen offset:1024 sc1\n"
    "buffer_load_dwordx4 v[72:75], v7, s[36:39], s5 offen offset:2048 sc1\n"
    "buffer_load_dwordx4 v[76:79], v7, s[36:39], s5 offen offset:3072 sc1\n"
    "buffer_load_dwordx4 v[82:85], v7, s[36:39], s8 offen sc1\n"
    "buffer_load_dwordx4 v[88:91], v7, s[36:39], s8 offen offset:1024 sc1\n"
    "buffer_load_dwordx4 v[94:97], v7, s[36:39], s8 offen offset:2048 sc1\n"
    "buffer_load_dwordx4 v[100:103], v7, s[36:39], s8 offen offset:3072 sc1\n"
    "buffer_load_dwordx4 v[106:109], v7, s[36:39], s9 offen sc1\n"
    "buffer_load_dwordx4 v[112:115], v7, s[36:39], s9 offen offset:1024 sc1\n"
    "buffer_load_dwordx4 v[118:121], v7, s[36:39], s9 offen offset:2048 sc1\n"
    "buffer_load_dwordx4 v[124:127], v7, s[36:39], s9 offen offset:3072 sc1\n"
    "s_add_u32 s68, s68, 1\n"
    "s_add_u32 s77, s77, 2\n"
    "s_cmp_lg_u32 s70, 0\n"
    "s_cbranch_scc1 .Latt_top_%=\n"
    ".Latt_LOOPE_%=:\n"
    "s_cmp_lg_u32 s68, 64\n"
    "s_cbranch_scc1 .Latt_waitp_%=\n"
    "s_waitcnt vmcnt(0)\n"
    "s_branch .Latt_FIN_%=\n"
    ".Latt_waitp_%=:\n"
    "s_sub_u32 s72, s77, 2\n"
    "v_add_u32 v11, v8, v5\n"
    "v_cmp_gt_i32 vcc, s72, v11\n"
    "s_nop 0\n"
    "s_or_b64 exec, vcc, s[20:21]\n"
    "buffer_atomic_smax v8, v4, s[36:39], 0 offen sc0 sc1\n"
    "s_mov_b64 exec, -1\n"
    "s_waitcnt vmcnt(0)\n"
    "v_add_u32 v11, v8, v5\n"
    "v_cmp_gt_i32 vcc, s72, v11\n"
    "s_cbranch_vccz .Latt_gop_%=\n"
    "s_sleep 3\n"
    "s_sub_u32 s69, s69, 1\n"
    "s_cbranch_scc0 .Latt_waitp_%=\n"
    "s_mov_b32 s69, 0\n"
    ".Latt_gop_%=:\n"
    "s_and_b32 s4, s68, 7\n"
    "s_mul_i32 s4, s4, 4194304\n"
    "s_add_u32 s4, s4, s52\n"
    "s_add_u32 s5, s4, 4096\n"
    "s_add_u32 s6, s4, 16384\n"
    "s_add_u32 s7, s4, 20480\n"
    "s_add_u32 s8, s4, 32768\n"
    "s_add_u32 s9, s4, 36864\n"
    "buffer_load_dwordx4 v[16:19], v0, s[36:39], s6 offen sc1\n"
    "buffer_load_dwordx4 v[20:23], v0, s[36:39], s6 offen offset:1024 sc1\n"
    "buffer_load_dwordx4 v[24:27], v0, s[36:39], s6 offen offset:2048 sc1\n"
    "buffer_load_dwordx4 v[28:31], v0, s[36:39], s6 offen offset:3072 sc1\n"
    "buffer_load_dwordx4 v[32:35], v0, s[36:39], s7 offen sc1\n"
    "buffer_load_dwordx4 v[36:39], v0, s[36:39], s7 offen offset:1024 sc1\n"
    "buffer_load_dwordx4 v[40:43], v0, s[36:39], s7 offen offset:2048 sc1\n"
    "buffer_load_dwordx4 v[44:47], v0, s[36:39], s7 offen offset:3072 sc1\n"
    "buffer_load_dwordx4 v[48:51], v0, s[36:39], s4 offen sc1\n"
    "buffer_load_dwordx4 v[52:55], v0, s[36:39], s4 offen offset:1024 sc1\n"
    "buffer_load_dwordx4 v[56:59], v0, s[36:39], s4 offen offset:2048 sc1\n"
    "buffer_load_dwordx4 v[60:63], v0, s[36:39], s4 offen offset:3072 sc1\n"
    "buffer_load_dwordx4 v[64:67], v0, s[36:39], s5 offen sc1\n"
    "buffer_load_dwordx4 v[68:71], v0, s[36:39], s5 offen offset:1024 sc1\n"
    "buffer_load_dwordx4 v[72:75], v0, s[36:39], s5 offen offset:2048 sc1\n"
    "buffer_load_dwordx4 v[76:79], v0, s[36:39], s5 offen offset:3072 sc1\n"
    "buffer_load_dwordx4 v[82:85], v0, s[36:39], s8 offen sc1\n"
    "buffer_load_dwordx4 v[88:91], v0, s[36:39], s8 offen offset:1024 sc1\n"
    "buffer_load_dwordx4 v[94:97], v0, s[36:39], s8 offen offset:2048 sc1\n"
    "buffer_load_dwordx4 v[100:103], v0, s[36:39], s8 offen offset:3072 sc1\n"
    "buffer_load_dwordx4 v[106:109], v0, s[36:39], s9 offen sc1\n"
    "buffer_load_dwordx4 v[112:115], v0, s[36:39], s9 offen offset:1024 sc1\n"
    "buffer_load_dwordx4 v[118:121], v0, s[36:39], s9 offen offset:2048 sc1\n"
    "buffer_load_dwordx4 v[124:127], v0, s[36:39], s9 offen offset:3072 sc1\n"
    "s_waitcnt vmcnt(8)\n"
    "s_branch .Latt_top_%=\n"
    ".Latt_waitr_%=:\n"
    "s_or_b64 exec, vcc, s[20:21]\n"
    "buffer_atomic_smax v8, v4, s[36:39], 0 offen sc0 sc1\n"
    "s_mov_b64 exec, -1\n"
    "s_sleep 3\n"
    "s_waitcnt vmcnt(1)\n"
    "v_add_u32 v11, v12, v5\n"
    "v_cmp_gt_i32 vcc, s77, v11\n"
    "s_nop 0\n"
    "s_and_b32 s71, vcc_lo, 0xff\n"
    "s_cbranch_scc0 .Latt_gor_%=\n"
    "s_or_b64 exec, vcc, s[20:21]\n"
    "buffer_atomic_smax v12, v4, s[36:39], 0 offen sc0 sc1\n"
    "s_mov_b64 exec, -1\n"
    "s_sleep 3\n"
    "s_waitcnt vmcnt(1)\n"
    "v_add_u32 v11, v8, v5\n"
    "v_cmp_gt_i32 vcc, s77, v11\n"
    "s_nop 0\n"
    "s_and_b32 s71, vcc_lo, 0xff\n"
    "s_cbranch_scc0 .Latt_gor_%=\n"
    "s_sub_u32 s69, s69, 1\n"
    "s_cbranch_scc0 .Latt_waitr_%=\n"
    ".Latt_deadr_%=:\n"
    "s_mov_b32 s69, 0\n"
    "s_branch .Latt_gor_%=\n"
    ".Latt_FIN_%=:\n"
  );
}

template <int MODE>
DEVI void red_args(const Params& p, int L, int q, u32 dbg, u64 tag, v16u& sa, v16u& sb, v16u& va, v16u& vb) {
  const int tid = threadIdx.x, lane = tid & 63, w = (int)rfl(tid >> 6);
  const int c = lane & 15, g = lane >> 4;
  const bool rowres = MODE == 0 || (MODE == 1 && L == 0);
  const bool fin = MODE == 2 && L == 1;
  const v4u ws = make_srd(p.ws, WS_END), rx = make_srd(p.x, 2048 * 1152), ro = make_srd(p.out, 2048 * 1152);
  u32 ft = OOB;
  if (lane < 32) {
    if (MODE == 1) { if (lane < 24) ft = mbox(L, lane / 6, SL_UP + lane % 6, 4 * q + w); }
    else if (!fin) ft = mbox(MODE == 0 ? 0 : 1, lane >> 3, SL_P + (lane & 7), (MODE == 0 ? 16 * L : 0) + 4 * q + w);
  } else if (MODE != 0) {
    if (lane < 44) ft = mbox(L, (lane - 32) / 3, (MODE == 1 ? SL_WO : SL_DOWN) + (lane - 32) % 3, (MODE == 1 ? 8 : 24) + 4 * q + w);
    else if (lane == 44 && !rowres) ft = MODE == 1 ? mbox(0, q, SL_REDF, 48 + w) : mbox(L, q, SL_REDA, 48 + w);
  }
  const unsigned long long fm = __ballot(ft != OOB);
  sa = cat4(ws, rowres ? rx : ws, fin ? ro : ws, (v4u){(u32)(2 * tag), (u32)((2 * tag) >> 32), 0u, 0u});
  sb = (v16u){pgb(L, MODE == 1 ? B_PA : B_PF),
              rowres ? 0u : (MODE == 1 ? pgb(1, B_X) : pgb(L, B_X1)), rowres ? 36864u : SLOT, (u32)(rowres ? 63 : (MODE == 1 ? RX2 - 1 : RX - 1)), rowres ? 128u : 1024u,
              MODE == 1 ? pgb(L, B_X1) : pgb(1, B_X), (u32)((MODE == 0 || fin) ? 63 : (MODE == 1 ? RX - 1 : RX2 - 1)),
              fin ? 0u : (MODE == 0 ? OFF_XN0 : (MODE == 1 ? pgb(L, B_XN2) : pgb(1, B_XN))), 4096u,
              rowres ? 0u : ~0u, dbg + w * 64, fin ? 0u : ~0u, (u32)fm, (u32)(fm >> 32), (u32)(L == 0 ? 0 : (L == 1 ? LN_B : LN_C)), (u32)(L == 0 ? LN_B - 1 : (L == 1 ? LN_C - 1 : 63))};
  const int need = w == 0 ? 0x10F : (w == 1 ? 0x23F : (w == 2 ? 0x4F0 : 0xF00));
  const bool in = MODE != 0 && lane < 48 && ((need >> (lane % 12)) & 1);
  const bool back = MODE != 0 && lane >= 48 && lane < 52 && !fin;
  auto frag = [&](int I) { return kb_off(2 * (I >> 1)) + (u32)((I & 1) * 512 + (q >> 1) * 1024 + (c >> 3) * 256 + (8 * (q & 1) + (c & 7)) * 16); };
  auto rowm = [&](int I) { return (u32)((8 * q + (c & 7)) * 1152 + (2 * I + (c >> 3)) * 16); };
  const int I0 = 8 * w + 2 * (g & 1) + (g >> 1);
  const u32 pv = (u32)(q * CHUNK + w * 2048 + lane * 16), pv8 = (u32)(4 * CHUNK + (4 * q + w) * 256 + lane * 16);
  va[0] = lane == 63 ? OOB : mbox(L, q, MODE == 1 ? SL_REDA : SL_REDF, lane);
  va[1] = (u32)(in ? -(q >> 1) : (back ? 2 * (MODE == 1 ? RX : RX2) : 1000));
  va[2] = (u32)g * 5 * CHUNK + frag(8 * w);
  va[3] = (u32)g * 5 * CHUNK + frag(32 + w);
  va[4] = fin ? (u32)(q * 9216 + tid * 16) : frag(I0);
  va[5] = fin ? (w == 0 ? (u32)(q * 9216 + 8192 + tid * 16) : OOB) : (g == 0 ? frag(32 + w) : OOB);
  va[6] = rowres ? rowm(I0) : pv;
  va[7] = g == 0 ? (rowres ? rowm(32 + w) : pv8) : OOB;
  va[8] = pv;
  va[9] = g == 0 ? pv8 : OOB;
  va[10] = fin ? rowm(I0) - q * 9216 : (u32)(((c & 7) * 4 + w) * 8);
  va[11] = fin ? (u32)tid * 16 : (u32)((c & 7) * 32);
  va[12] = fin ? (g == 0 ? rowm(32 + w) - q * 9216 : (u32)(12288 + tid * 16)) : (g < 2 ? 0x3F800000u : 0u);
  va[13] = ft; va[14] = 0; va[15] = 0;
  const u32 one = 0x3F80u << (16 * (c & 1));
#pragma unroll
  for (int d = 0; d < 4; ++d) {
    const bool on = ((c & 7) >> 1) == d;
    vb[d] = (on && c < 8) ? one : 0u;
    vb[4 + d] = (on && c >= 8) ? one : 0u;
    vb[8 + d] = (on && g == 2 * (c >> 3)) ? one : 0u;
    vb[12 + d] = (on && g == 2 * (c >> 3) + 1) ? one : 0u;
  }
#pragma unroll
  for (int i = 0; i < 16; ++i) { sa[i] = rfl(sa[i]); sb[i] = rfl(sb[i]); }
}
#define RUN_RED(TEXT) asm volatile(TEXT : "+{s[36:51]}"(sa), "+{s[52:67]}"(sb), "+{v[0:15]}"(va), "+{v[16:31]}"(vb) :: CLOB_RED)
#define CLOB_RED \
  "v32", "v33", "v34", "v35", "v36", "v37", "v38", "v39", "v40", "v41", "v42", "v43", "v44", "v45", "v46", "v47", "v48", "v49", "v50", "v51", "v52", "v53", "v54", "v55", \
  "v56", "v57", "v58", "v59", "v60", "v61", "v62", "v63", "v64", "v65", "v66", "v67", "v68", "v69", "v70", "v71", "v72", "v73", "v74", "v75", "v76", "v77", "v78", "v79", \
  "v80", "v81", "v82", "v83", "v84", "v85", "v86", "v87", "v88", "v89", "v90", "v91", "v92", "v93", "v94", "v95", "v96", "v97", "v98", "v99", "v100", "v101", "v102", "v103", \
  "v104", "v105", "v106", "v107", "v108", "v109", "v110", "v111", "v112", "v113", "v114", "v115", "v116", "v117", "v118", "v119", "v120", "v121", "v122", "v123", "v124", "v125", "v126", "v127", \
  "v128", "v129", "v130", "v131", "v132", "v133", "v134", "v135", "s68", "s69", "s70", "s71", "s72", "s73", "s74", "s75", "s76", "s77", "s78", "s79", "s80", "s81", "s82", "s83", \
  "s84", "s85", "s86", "s87", "s88", "s89", "s90", "s91", "s92", "s93", "s94", "s95", "s96", "s97", "s98", "s99", "vcc", "scc", "memory"
DEVI void role_ln0(const Params& p, int grp, int q, u32 dbg, u64 tag) {
  v16u sa, sb, va, vb;
  red_args<0>(p, grp, q, dbg, tag, sa, sb, va, vb);
  RUN_RED(
    "s_nop 4\n"
    "s_mov_b32 s50, 0\n"
    "s_mov_b32 s51, 0x80000000\n"
    "s_getpc_b64 s[84:85]\n"
    ".LrLN0_PC_%=:\n"
    "s_and_b32 s85, s85, 0xffff\n"
    "s_mov_b32 s86, .LrLN0_FIN_%=-.LrLN0_PC_%=\n"
    "s_mov_b32 s87, 0x20000\n"
    "v_mbcnt_lo_u32_b32 v134, -1, 0\n"
    "v_mbcnt_hi_u32_b32 v134, -1, v134\n"
    "v_lshlrev_b32 v134, 7, v134\n"
    "buffer_load_dword v135, v134, s[84:87], 0 offen\n"
    "v_add_u32 v134, 8192, v134\n"
    "buffer_load_dword v135, v134, s[84:87], 0 offen\n"
    "s_mov_b32 s69, 0\n"
    "v_mov_b32 v122, -1\n"
    "v_mov_b32 v123, -1\n"
    "s_mov_b32 s70, 262144\n"
    "s_add_u32 s69, s66, 0\n"
    "s_and_b32 s73, s69, s55\n"
    "s_mul_i32 s73, s73, s54\n"
    "s_add_u32 s73, s73, s53\n"
    "s_add_u32 s74, s73, s56\n"
    "buffer_load_dwordx4 v[40:43], v6, s[40:43], s73 offen sc1\n"
    "buffer_load_dwordx4 v[44:47], v6, s[40:43], s74 offen sc1\n"
    "buffer_load_dwordx4 v[48:51], v7, s[40:43], s73 offen sc1\n"
    "s_add_u32 s69, s66, 1\n"
    "s_and_b32 s73, s69, s55\n"
    "s_mul_i32 s73, s73, s54\n"
    "s_add_u32 s73, s73, s53\n"
    "s_add_u32 s74, s73, s56\n"
    "buffer_load_dwordx4 v[52:55], v6, s[40:43], s73 offen sc1\n"
    "buffer_load_dwordx4 v[56:59], v6, s[40:43], s74 offen sc1\n"
    "buffer_load_dwordx4 v[60:63], v7, s[40:43], s73 offen sc1\n"
    "s_add_u32 s69, s66, 2\n"
    "s_and_b32 s73, s69, s55\n"
    "s_mul_i32 s73, s73, s54\n"
    "s_add_u32 s73, s73, s53\n"
    "s_add_u32 s74, s73, s56\n"
    "buffer_load_dwordx4 v[68:71], v6, s[40:43], s73 offen sc1\n"
    "buffer_load_dwordx4 v[72:75], v6, s[40:43], s74 offen sc1\n"
    "buffer_load_dwordx4 v[76:79], v7, s[40:43], s73 offen sc1\n"
    "s_add_u32 s69, s66, 3\n"
    "s_and_b32 s73, s69, s55\n"
    "s_mul_i32 s73, s73, s54\n"
    "s_add_u32 s73, s73, s53\n"
    "s_add_u32 s74, s73, s56\n"
    "buffer_load_dwordx4 v[124:127], v6, s[40:43], s73 offen sc1\n"
    "buffer_load_dwordx4 v[128:131], v6, s[40:43], s74 offen sc1\n"
    "buffer_load_dwordx4 v[132:135], v7, s[40:43], s73 offen sc1\n"
    "s_sub_u32 s68, s66, 1\n"
    ".LrLN0_par_%=:\n"
    "buffer_load_dwordx2 v[14:15], off, s[36:39], 0 sc1\n"
    "s_waitcnt vmcnt(0)\n"
    "v_readfirstlane_b32 s75, v14\n"
    "v_readfirstlane_b32 s76, v15\n"
    "s_xor_b32 s75, s75, s48\n"
    "s_xor_b32 s76, s76, s49\n"
    "s_cselect_b32 s76, 2, 0\n"
    "s_or_b32 s76, s76, s75\n"
    "s_cmp_lt_u32 s76, 2\n"
    "s_cbranch_scc1 .LrLN0_par1_%=\n"
    "s_sleep 4\n"
    "s_sub_u32 s70, s70, 1\n"
    "s_cbranch_scc0 .LrLN0_par_%=\n"
    "s_mov_b32 s70, 0\n"
    ".LrLN0_par1_%=:\n"
    "s_and_b32 s75, s75, 1\n"
    "s_lshl_b32 s75, s75, 13\n"
    "s_add_u32 s36, s36, s75\n"
    "s_addc_u32 s37, s37, 0\n"
    "s_and_b32 s76, s75, s61\n"
    "s_add_u32 s40, s40, s76\n"
    "s_addc_u32 s41, s41, 0\n"
    "s_and_b32 s76, s75, s63\n"
    "s_add_u32 s44, s44, s76\n"
    "s_addc_u32 s45, s45, 0\n"
    ".LrLN0_LOOPB_%=:\n"
    ".LrLN0_top_%=:\n"
    "s_add_u32 s68, s68, 1\n"
    "s_add_u32 s69, s68, 4\n"
    "s_and_b32 s75, s68, s58\n"
    "s_mul_i32 s77, s75, 81920\n"
    "s_add_u32 s77, s77, s59\n"
    "s_add_u32 s78, s77, s60\n"
    "s_and_b32 s73, s69, s55\n"
    "s_mul_i32 s73, s73, s54\n"
    "s_add_u32 s73, s73, s53\n"
    "s_add_u32 s74, s73, s56\n"
    "s_waitcnt vmcnt(27)\n"
    "v_mfma_f32_16x16x32_bf16 v[80:83], v[24:27], v[40:43], 0\n"
    "v_mfma_f32_16x16x32_bf16 v[84:87], v[28:31], v[40:43], 0\n"
    "s_waitcnt vmcnt(26)\n"
    "v_mfma_f32_16x16x32_bf16 v[88:91], v[24:27], v[44:47], 0\n"
    "v_mfma_f32_16x16x32_bf16 v[92:95], v[28:31], v[44:47], 0\n"
    "s_waitcnt vmcnt(25)\n"
    "v_mfma_f32_16x16x32_bf16 v[96:99], v[24:27], v[48:51], 0\n"
    "buffer_load_dwordx4 v[40:43], v6, s[40:43], s73 offen sc1\n"
    "buffer_load_dwordx4 v[44:47], v6, s[40:43], s74 offen sc1\n"
    "buffer_load_dwordx4 v[48:51], v7, s[40:43], s73 offen sc1\n"
    "v_pk_add_f32 v[112:113], v[80:81], v[82:83]\n"
    "v_pk_add_f32 v[114:115], v[84:85], v[86:87]\n"
    "v_pk_add_f32 v[112:113], v[112:113], v[88:89]\n"
    "v_pk_add_f32 v[114:115], v[114:115], v[90:91]\n"
    "v_pk_add_f32 v[112:113], v[112:113], v[92:93]\n"
    "v_pk_add_f32 v[114:115], v[114:115], v[94:95]\n"
    "v_pk_add_f32 v[112:113], v[112:113], v[96:97]\n"
    "v_pk_add_f32 v[114:115], v[114:115], v[98:99]\n"
    "v_pk_add_f32 v[112:113], v[112:113], v[114:115]\n"
    "v_add_f32 v112, v112, v113\n"
    "s_nop 1\n"
    "v_add_f32_dpp v14, v112, v112 row_ror:8 row_mask:0xf bank_mask:0xf\n"
    "v_add_f32_dpp v15, v112, v112 row_ror:8 row_mask:0xf bank_mask:0xf\n"
    "s_nop 1\n"
    "v_permlane16_swap_b32 v14, v15\n"
    "v_add_f32 v112, v14, v15\n"
    "v_add_f32 v14, v14, v15\n"
    "s_nop 1\n"
    "v_permlane32_swap_b32 v112, v14\n"
    "v_add_f32 v112, v112, v14\n"
    "v_mul_f32 v112, 0x3be38e39, v112\n"
    "v_mul_f32 v114, v112, v12\n"
    "v_pk_add_f32 v[80:81], v[80:81], v[112:113] op_sel_hi:[1,0] neg_lo:[0,1] neg_hi:[0,1]\n"
    "v_pk_add_f32 v[82:83], v[82:83], v[112:113] op_sel_hi:[1,0] neg_lo:[0,1] neg_hi:[0,1]\n"
    "v_pk_add_f32 v[84:85], v[84:85], v[112:113] op_sel_hi:[1,0] neg_lo:[0,1] neg_hi:[0,1]\n"
    "v_pk_add_f32 v[86:87], v[86:87], v[112:113] op_sel_hi:[1,0] neg_lo:[0,1] neg_hi:[0,1]\n"
    "v_pk_add_f32 v[88:89], v[88:89], v[112:113] op_sel_hi:[1,0] neg_lo:[0,1] neg_hi:[0,1]\n"
    "v_pk_add_f32 v[90:91], v[90:91], v[112:113] op_sel_hi:[1,0] neg_lo:[0,1] neg_hi:[0,1]\n"
    "v_pk_add_f32 v[92:93], v[92:93], v[112:113] op_sel_hi:[1,0] neg_lo:[0,1] neg_hi:[0,1]\n"
    "v_pk_add_f32 v[94:95], v[94:95], v[112:113] op_sel_hi:[1,0] neg_lo:[0,1] neg_hi:[0,1]\n"
    "v_pk_add_f32 v[96:97], v[96:97], v[114:115] op_sel_hi:[1,0] neg_lo:[0,1] neg_hi:[0,1]\n"
    "v_pk_add_f32 v[98:99], v[98:99], v[114:115] op_sel_hi:[1,0] neg_lo:[0,1] neg_hi:[0,1]\n"
    "v_pk_mul_f32 v[116:117], v[80:81], v[80:81]\n"
    "v_pk_mul_f32 v[118:119], v[82:83], v[82:83]\n"
    "v_pk_fma_f32 v[116:117], v[84:85], v[84:85], v[116:117]\n"
    "v_pk_fma_f32 v[118:119], v[86:87], v[86:87], v[118:119]\n"
    "v_pk_fma_f32 v[116:117], v[88:89], v[88:89], v[116:117]\n"
    "v_pk_fma_f32 v[118:119], v[90:91], v[90:91], v[118:119]\n"
    "v_pk_fma_f32 v[116:117], v[92:93], v[92:93], v[116:117]\n"
    "v_pk_fma_f32 v[118:119], v[94:95], v[94:95], v[118:119]\n"
    "v_pk_fma_f32 v[116:117], v[96:97], v[96:97], v[116:117]\n"
    "v_pk_fma_f32 v[118:119], v[98:99], v[98:99], v[118:119]\n"
    "v_pk_add_f32 v[116:117], v[116:117], v[118:119]\n"
    "v_add_f32 v113, v116, v117\n"
    "s_nop 1\n"
    "v_add_f32_dpp v14, v113, v113 row_ror:8 row_mask:0xf bank_mask:0xf\n"
    "v_add_f32_dpp v15, v113, v113 row_ror:8 row_mask:0xf bank_mask:0xf\n"
    "s_nop 1\n"
    "v_permlane16_swap_b32 v14, v15\n"
    "v_add_f32 v113, v14, v15\n"
    "v_add_f32 v14, v14, v15\n"
    "s_nop 1\n"
    "v_permlane32_swap_b32 v113, v14\n"
    "v_add_f32 v113, v113, v14\n"
    "ds_write_b64 v10, v[112:113]\n"
    "s_waitcnt vmcnt(17)\n"
    "s_add_u32 s75, s66, 1\n"
    "s_min_i32 s75, s75, s68\n"
    "s_sub_u32 s76, s68, 2\n"
    "s_max_i32 s75, s75, s76\n"
    "s_sub_u32 s75, s75, 1\n"
    "v_lshl_or_b32 v14, s75, 1, 1\n"
    "s_mov_b64 exec, s[64:65]\n"
    "buffer_atomic_smax v14, v13, s[36:39], 0 offen sc1\n"
    "s_mov_b64 exec, -1\n"
    "s_waitcnt lgkmcnt(0)\n"
    "s_barrier\n"
    "ds_read_b128 v[32:35], v11\n"
    "ds_read_b128 v[36:39], v11 offset:16\n"
    "v_xor_b32 v10, 256, v10\n"
    "v_xor_b32 v11, 256, v11\n"
    "s_waitcnt lgkmcnt(0)\n"
    "v_pk_add_f32 v[114:115], v[32:33], v[34:35]\n"
    "v_pk_add_f32 v[116:117], v[36:37], v[38:39]\n"
    "v_pk_add_f32 v[114:115], v[114:115], v[116:117]\n"
    "v_fmac_f32 v32, 0xbe800000, v114\n"
    "v_fmac_f32 v34, 0xbe800000, v114\n"
    "v_fmac_f32 v36, 0xbe800000, v114\n"
    "v_fmac_f32 v38, 0xbe800000, v114\n"
    "v_fmac_f32 v112, 0xbe800000, v114\n"
    "v_mul_f32 v116, v32, v32\n"
    "v_fmac_f32 v116, v34, v34\n"
    "v_fmac_f32 v116, v36, v36\n"
    "v_fmac_f32 v116, v38, v38\n"
    "v_fmac_f32 v115, 0x43100000, v116\n"
    "v_mov_b32 v116, 0x3727c5ac\n"
    "v_fmac_f32 v116, 0x3ae38e39, v115\n"
    "v_rsq_f32 v116, v116\n"
    "s_nop 0\n"
    "v_mul_f32 v114, v112, v116\n"
    "v_pk_fma_f32 v[80:81], v[80:81], v[116:117], v[114:115] op_sel_hi:[1,0,0]\n"
    "v_pk_fma_f32 v[82:83], v[82:83], v[116:117], v[114:115] op_sel_hi:[1,0,0]\n"
    "v_pk_fma_f32 v[84:85], v[84:85], v[116:117], v[114:115] op_sel_hi:[1,0,0]\n"
    "v_pk_fma_f32 v[86:87], v[86:87], v[116:117], v[114:115] op_sel_hi:[1,0,0]\n"
    "v_pk_fma_f32 v[88:89], v[88:89], v[116:117], v[114:115] op_sel_hi:[1,0,0]\n"
    "v_pk_fma_f32 v[90:91], v[90:91], v[116:117], v[114:115] op_sel_hi:[1,0,0]\n"
    "v_pk_fma_f32 v[92:93], v[92:93], v[116:117], v[114:115] op_sel_hi:[1,0,0]\n"
    "v_pk_fma_f32 v[94:95], v[94:95], v[116:117], v[114:115] op_sel_hi:[1,0,0]\n"
    "v_pk_fma_f32 v[96:97], v[96:97], v[116:117], v[114:115] op_sel_hi:[1,0,0]\n"
    "v_pk_fma_f32 v[98:99], v[98:99], v[116:117], v[114:115] op_sel_hi:[1,0,0]\n"
    "v_cvt_pk_bf16_f32 v100, v80, v81\n"
    "v_cvt_pk_bf16_f32 v101, v82, v83\n"
    "v_cvt_pk_bf16_f32 v102, v84, v85\n"
    "v_cvt_pk_bf16_f32 v103, v86, v87\n"
    "v_cvt_pk_bf16_f32 v104, v88, v89\n"
    "v_cvt_pk_bf16_f32 v105, v90, v91\n"
    "v_cvt_pk_bf16_f32 v106, v92, v93\n"
    "v_cvt_pk_bf16_f32 v107, v94, v95\n"
    "v_cvt_pk_bf16_f32 v108, v96, v97\n"
    "v_cvt_pk_bf16_f32 v109, v98, v99\n"
    "v_permlane16_swap_b32 v100, v102\n"
    "v_permlane16_swap_b32 v101, v103\n"
    "buffer_store_dwordx4 v[100:103], v4, s[44:47], s77 offen sc1\n"
    "v_permlane16_swap_b32 v104, v106\n"
    "v_permlane16_swap_b32 v105, v107\n"
    "buffer_store_dwordx4 v[104:107], v4, s[44:47], s78 offen sc1\n"
    "v_permlane16_swap_b32 v108, v110\n"
    "v_permlane16_swap_b32 v109, v111\n"
    "buffer_store_dwordx4 v[108:111], v5, s[44:47], s77 offen sc1\n"
    "s_cmp_eq_u32 s68, s66\n"
    "s_cbranch_scc1 .LrLN0_first_%=\n"
    ".LrLN0_first1_%=:\n"
    "s_add_u32 s68, s68, 1\n"
    "s_add_u32 s69, s68, 4\n"
    "s_and_b32 s75, s68, s58\n"
    "s_mul_i32 s77, s75, 81920\n"
    "s_add_u32 s77, s77, s59\n"
    "s_add_u32 s78, s77, s60\n"
    "s_and_b32 s73, s69, s55\n"
    "s_mul_i32 s73, s73, s54\n"
    "s_add_u32 s73, s73, s53\n"
    "s_add_u32 s74, s73, s56\n"
    "s_waitcnt vmcnt(27)\n"
    "v_mfma_f32_16x16x32_bf16 v[80:83], v[24:27], v[52:55], 0\n"
    "v_mfma_f32_16x16x32_bf16 v[84:87], v[28:31], v[52:55], 0\n"
    "s_waitcnt vmcnt(26)\n"
    "v_mfma_f32_16x16x32_bf16 v[88:91], v[24:27], v[56:59], 0\n"
    "v_mfma_f32_16x16x32_bf16 v[92:95], v[28:31], v[56:59], 0\n"
    "s_waitcnt vmcnt(25)\n"
    "v_mfma_f32_16x16x32_bf16 v[96:99], v[24:27], v[60:63], 0\n"
    "buffer_load_dwordx4 v[52:55], v6, s[40:43], s73 offen sc1\n"
    "buffer_load_dwordx4 v[56:59], v6, s[40:43], s74 offen sc1\n"
    "buffer_load_dwordx4 v[60:63], v7, s[40:43], s73 offen sc1\n"
    "v_pk_add_f32 v[112:113], v[80:81], v[82:83]\n"
    "v_pk_add_f32 v[114:115], v[84:85], v[86:87]\n"
    "v_pk_add_f32 v[112:113], v[112:113], v[88:89]\n"
    "v_pk_add_f32 v[114:115], v[114:115], v[90:91]\n"
    "v_pk_add_f32 v[112:113], v[112:113], v[92:93]\n"
    "v_pk_add_f32 v[114:115], v[114:115], v[94:95]\n"
    "v_pk_add_f32 v[112:113], v[112:113], v[96:97]\n"
    "v_pk_add_f32 v[114:115], v[114:115], v[98:99]\n"
    "v_pk_add_f32 v[112:113], v[112:113], v[114:115]\n"
    "v_add_f32 v112, v112, v113\n"
    "s_nop 1\n"
    "v_add_f32_dpp v14, v112, v112 row_ror:8 row_mask:0xf bank_mask:0xf\n"
    "v_add_f32_dpp v15, v112, v112 row_ror:8 row_mask:0xf bank_mask:0xf\n"
    "s_nop 1\n"
    "v_permlane16_swap_b32 v14, v15\n"
    "v_add_f32 v112, v14, v15\n"
    "v_add_f32 v14, v14, v15\n"
    "s_nop 1\n"
    "v_permlane32_swap_b32 v112, v14\n"
    "v_add_f32 v112, v112, v14\n"
    "v_mul_f32 v112, 0x3be38e39, v112\n"
    "v_mul_f32 v114, v112, v12\n"
    "v_pk_add_f32 v[80:81], v[80:81], v[112:113] op_sel_hi:[1,0] neg_lo:[0,1] neg_hi:[0,1]\n"
    "v_pk_add_f32 v[82:83], v[82:83], v[112:113] op_sel_hi:[1,0] neg_lo:[0,1] neg_hi:[0,1]\n"
    "v_pk_add_f32 v[84:85], v[84:85], v[112:113] op_sel_hi:[1,0] neg_lo:[0,1] neg_hi:[0,1]\n"
    "v_pk_add_f32 v[86:87], v[86:87], v[112:113] op_sel_hi:[1,0] neg_lo:[0,1] neg_hi:[0,1]\n"
    "v_pk_add_f32 v[88:89], v[88:89], v[112:113] op_sel_hi:[1,0] neg_lo:[0,1] neg_hi:[0,1]\n"
    "v_pk_add_f32 v[90:91], v[90:91], v[112:113] op_sel_hi:[1,0] neg_lo:[0,1] neg_hi:[0,1]\n"
    "v_pk_add_f32 v[92:93], v[92:93], v[112:113] op_sel_hi:[1,0] neg_lo:[0,1] neg_hi:[0,1]\n"
    "v_pk_add_f32 v[94:95], v[94:95], v[112:113] op_sel_hi:[1,0] neg_lo:[0,1] neg_hi:[0,1]\n"
    "v_pk_add_f32 v[96:97], v[96:97], v[114:115] op_sel_hi:[1,0] neg_lo:[0,1] neg_hi:[0,1]\n"
    "v_pk_add_f32 v[98:99], v[98:99], v[114:115] op_sel_hi:[1,0] neg_lo:[0,1] neg_hi:[0,1]\n"
    "v_pk_mul_f32 v[116:117], v[80:81], v[80:81]\n"
    "v_pk_mul_f32 v[118:119], v[82:83], v[82:83]\n"
    "v_pk_fma_f32 v[116:117], v[84:85], v[84:85], v[116:117]\n"
    "v_pk_fma_f32 v[118:119], v[86:87], v[86:87], v[118:119]\n"
    "v_pk_fma_f32 v[116:117], v[88:89], v[88:89], v[116:117]\n"
    "v_pk_fma_f32 v[118:119], v[90:91], v[90:91], v[118:119]\n"
    "v_pk_fma_f32 v[116:117], v[92:93], v[92:93], v[116:117]\n"
    "v_pk_fma_f32 v[118:119], v[94:95], v[94:95], v[118:119]\n"
    "v_pk_fma_f32 v[116:117], v[96:97], v[96:97], v[116:117]\n"
    "v_pk_fma_f32 v[118:119], v[98:99], v[98:99], v[118:119]\n"
    "v_pk_add_f32 v[116:117], v[116:117], v[118:119]\n"
    "v_add_f32 v113, v116, v117\n"
    "s_nop 1\n"
    "v_add_f32_dpp v14, v113, v113 row_ror:8 row_mask:0xf bank_mask:0xf\n"
    "v_add_f32_dpp v15, v113, v113 row_ror:8 row_mask:0xf bank_mask:0xf\n"
    "s_nop 1\n"
    "v_permlane16_swap_b32 v14, v15\n"
    "v_add_f32 v113, v14, v15\n"
    "v_add_f32 v14, v14, v15\n"
    "s_nop 1\n"
    "v_permlane32_swap_b32 v113, v14\n"
    "v_add_f32 v113, v113, v14\n"
    "ds_write_b64 v10, v[112:113]\n"
    "s_waitcnt vmcnt(17)\n"
    "s_add_u32 s75, s66, 1\n"
    "s_min_i32 s75, s75, s68\n"
    "s_sub_u32 s76, s68, 2\n"
    "s_max_i32 s75, s75, s76\n"
    "s_sub_u32 s75, s75, 1\n"
    "v_lshl_or_b32 v14, s75, 1, 1\n"
    "s_mov_b64 exec, s[64:65]\n"
    "buffer_atomic_smax v14, v13, s[36:39], 0 offen sc1\n"
    "s_mov_b64 exec, -1\n"
    "s_waitcnt lgkmcnt(0)\n"
    "s_barrier\n"
    "ds_read_b128 v[32:35], v11\n"
    "ds_read_b128 v[36:39], v11 offset:16\n"
    "v_xor_b32 v10, 256, v10\n"
    "v_xor_b32 v11, 256, v11\n"
    "s_waitcnt lgkmcnt(0)\n"
    "v_pk_add_f32 v[114:115], v[32:33], v[34:35]\n"
    "v_pk_add_f32 v[116:117], v[36:37], v[38:39]\n"
    "v_pk_add_f32 v[114:115], v[114:115], v[116:117]\n"
    "v_fmac_f32 v32, 0xbe800000, v114\n"
    "v_fmac_f32 v34, 0xbe800000, v114\n"
    "v_fmac_f32 v36, 0xbe800000, v114\n"
    "v_fmac_f32 v38, 0xbe800000, v114\n"
    "v_fmac_f32 v112, 0xbe800000, v114\n"
    "v_mul_f32 v116, v32, v32\n"
    "v_fmac_f32 v116, v34, v34\n"
    "v_fmac_f32 v116, v36, v36\n"
    "v_fmac_f32 v116, v38, v38\n"
    "v_fmac_f32 v115, 0x43100000, v116\n"
    "v_mov_b32 v116, 0x3727c5ac\n"
    "v_fmac_f32 v116, 0x3ae38e39, v115\n"
    "v_rsq_f32 v116, v116\n"
    "s_nop 0\n"
    "v_mul_f32 v114, v112, v116\n"
    "v_pk_fma_f32 v[80:81], v[80:81], v[116:117], v[114:115] op_sel_hi:[1,0,0]\n"
    "v_pk_fma_f32 v[82:83], v[82:83], v[116:117], v[114:115] op_sel_hi:[1,0,0]\n"
    "v_pk_fma_f32 v[84:85], v[84:85], v[116:117], v[114:115] op_sel_hi:[1,0,0]\n"
    "v_pk_fma_f32 v[86:87], v[86:87], v[116:117], v[114:115] op_sel_hi:[1,0,0]\n"
    "v_pk_fma_f32 v[88:89], v[88:89], v[116:117], v[114:115] op_sel_hi:[1,0,0]\n"
    "v_pk_fma_f32 v[90:91], v[90:91], v[116:117], v[114:115] op_sel_hi:[1,0,0]\n"
    "v_pk_fma_f32 v[92:93], v[92:93], v[116:117], v[114:115] op_sel_hi:[1,0,0]\n"
    "v_pk_fma_f32 v[94:95], v[94:95], v[116:117], v[114:115] op_sel_hi:[1,0,0]\n"
    "v_pk_fma_f32 v[96:97], v[96:97], v[116:117], v[114:115] op_sel_hi:[1,0,0]\n"
    "v_pk_fma_f32 v[98:99], v[98:99], v[116:117], v[114:115] op_sel_hi:[1,0,0]\n"
    "v_cvt_pk_bf16_f32 v100, v80, v81\n"
    "v_cvt_pk_bf16_f32 v101, v82, v83\n"
    "v_cvt_pk_bf16_f32 v102, v84, v85\n"
    "v_cvt_pk_bf16_f32 v103, v86, v87\n"
    "v_cvt_pk_bf16_f32 v104, v88, v89\n"
    "v_cvt_pk_bf16_f32 v105, v90, v91\n"
    "v_cvt_pk_bf16_f32 v106, v92, v93\n"
    "v_cvt_pk_bf16_f32 v107, v94, v95\n"
    "v_cvt_pk_bf16_f32 v108, v96, v97\n"
    "v_cvt_pk_bf16_f32 v109, v98, v99\n"
    "v_permlane16_swap_b32 v100, v102\n"
    "v_permlane16_swap_b32 v101, v103\n"
    "buffer_store_dwordx4 v[100:103], v4, s[44:47], s77 offen sc1\n"
    "v_permlane16_swap_b32 v104, v106\n"
    "v_permlane16_swap_b32 v105, v107\n"
    "buffer_store_dwordx4 v[104:107], v4, s[44:47], s78 offen sc1\n"
    "v_permlane16_swap_b32 v108, v110\n"
    "v_permlane16_swap_b32 v109, v111\n"
    "buffer_store_dwordx4 v[108:111], v5, s[44:47], s77 offen sc1\n"
    "s_add_u32 s68, s68, 1\n"
    "s_add_u32 s69, s68, 4\n"
    "s_and_b32 s75, s68, s58\n"
    "s_mul_i32 s77, s75, 81920\n"
    "s_add_u32 s77, s77, s59\n"
    "s_add_u32 s78, s77, s60\n"
    "s_and_b32 s73, s69, s55\n"
    "s_mul_i32 s73, s73, s54\n"
    "s_add_u32 s73, s73, s53\n"
    "s_add_u32 s74, s73, s56\n"
    "s_waitcnt vmcnt(27)\n"
    "v_mfma_f32_16x16x32_bf16 v[80:83], v[24:27], v[68:71], 0\n"
    "v_mfma_f32_16x16x32_bf16 v[84:87], v[28:31], v[68:71], 0\n"
    "s_waitcnt vmcnt(26)\n"
    "v_mfma_f32_16x16x32_bf16 v[88:91], v[24:27], v[72:75], 0\n"
    "v_mfma_f32_16x16x32_bf16 v[92:95], v[28:31], v[72:75], 0\n"
    "s_waitcnt vmcnt(25)\n"
    "v_mfma_f32_16x16x32_bf16 v[96:99], v[24:27], v[76:79], 0\n"
    "buffer_load_dwordx4 v[68:71], v6, s[40:43], s73 offen sc1\n"
    "buffer_load_dwordx4 v[72:75], v6, s[40:43], s74 offen sc1\n"
    "buffer_load_dwordx4 v[76:79], v7, s[40:43], s73 offen sc1\n"
    "v_pk_add_f32 v[112:113], v[80:81], v[82:83]\n"
    "v_pk_add_f32 v[114:115], v[84:85], v[86:87]\n"
    "v_pk_add_f32 v[112:113], v[112:113], v[88:89]\n"
    "v_pk_add_f32 v[114:115], v[114:115], v[90:91]\n"
    "v_pk_add_f32 v[112:113], v[112:113], v[92:93]\n"
    "v_pk_add_f32 v[114:115], v[114:115], v[94:95]\n"
    "v_pk_add_f32 v[112:113], v[112:113], v[96:97]\n"
    "v_pk_add_f32 v[114:115], v[114:115], v[98:99]\n"
    "v_pk_add_f32 v[112:113], v[112:113], v[114:115]\n"
    "v_add_f32 v112, v112, v113\n"
    "s_nop 1\n"
    "v_add_f32_dpp v14, v112, v112 row_ror:8 row_mask:0xf bank_mask:0xf\n"
    "v_add_f32_dpp v15, v112, v112 row_ror:8 row_mask:0xf bank_mask:0xf\n"
    "s_nop 1\n"
    "v_permlane16_swap_b32 v14, v15\n"
    "v_add_f32 v112, v14, v15\n"
    "v_add_f32 v14, v14, v15\n"
    "s_nop 1\n"
    "v_permlane32_swap_b32 v112, v14\n"
    "v_add_f32 v112, v112, v14\n"
    "v_mul_f32 v112, 0x3be38e39, v112\n"
    "v_mul_f32 v114, v112, v12\n"
    "v_pk_add_f32 v[80:81], v[80:81], v[112:113] op_sel_hi:[1,0] neg_lo:[0,1] neg_hi:[0,1]\n"
    "v_pk_add_f32 v[82:83], v[82:83], v[112:113] op_sel_hi:[1,0] neg_lo:[0,1] neg_hi:[0,1]\n"
    "v_pk_add_f32 v[84:85], v[84:85], v[112:113] op_sel_hi:[1,0] neg_lo:[0,1] neg_hi:[0,1]\n"
    "v_pk_add_f32 v[86:87], v[86:87], v[112:113] op_sel_hi:[1,0] neg_lo:[0,1] neg_hi:[0,1]\n"
    "v_pk_add_f32 v[88:89], v[88:89], v[112:113] op_sel_hi:[1,0] neg_lo:[0,1] neg_hi:[0,1]\n"
    "v_pk_add_f32 v[90:91], v[90:91], v[112:113] op_sel_hi:[1,0] neg_lo:[0,1] neg_hi:[0,1]\n"
    "v_pk_add_f32 v[92:93], v[92:93], v[112:113] op_sel_hi:[1,0] neg_lo:[0,1] neg_hi:[0,1]\n"
    "v_pk_add_f32 v[94:95], v[94:95], v[112:113] op_sel_hi:[1,0] neg_lo:[0,1] neg_hi:[0,1]\n"
    "v_pk_add_f32 v[96:97], v[96:97], v[114:115] op_sel_hi:[1,0] neg_lo:[0,1] neg_hi:[0,1]\n"
    "v_pk_add_f32 v[98:99], v[98:99], v[114:115] op_sel_hi:[1,0] neg_lo:[0,1] neg_hi:[0,1]\n"
    "v_pk_mul_f32 v[116:117], v[80:81], v[80:81]\n"
    "v_pk_mul_f32 v[118:119], v[82:83], v[82:83]\n"
    "v_pk_fma_f32 v[116:117], v[84:85], v[84:85], v[116:117]\n"
    "v_pk_fma_f32 v[118:119], v[86:87], v[86:87], v[118:119]\n"
    "v_pk_fma_f32 v[116:117], v[88:89], v[88:89], v[116:117]\n"
    "v_pk_fma_f32 v[118:119], v[90:91], v[90:91], v[118:119]\n"
    "v_pk_fma_f32 v[116:117], v[92:93], v[92:93], v[116:117]\n"
    "v_pk_fma_f32 v[118:119], v[94:95], v[94:95], v[118:119]\n"
    "v_pk_fma_f32 v[116:117], v[96:97], v[96:97], v[116:117]\n"
    "v_pk_fma_f32 v[118:119], v[98:99], v[98:99], v[118:119]\n"
    "v_pk_add_f32 v[116:117], v[116:117], v[118:119]\n"
    "v_add_f32 v113, v116, v117\n"
    "s_nop 1\n"
    "v_add_f32_dpp v14, v113, v113 row_ror:8 row_mask:0xf bank_mask:0xf\n"
    "v_add_f32_dpp v15, v113, v113 row_ror:8 row_mask:0xf bank_mask:0xf\n"
    "s_nop 1\n"
    "v_permlane16_swap_b32 v14, v15\n"
    "v_add_f32 v113, v14, v15\n"
    "v_add_f32 v14, v14, v15\n"
    "s_nop 1\n"
    "v_permlane32_swap_b32 v113, v14\n"
    "v_add_f32 v113, v113, v14\n"
    "ds_write_b64 v10, v[112:113]\n"
    "s_waitcnt vmcnt(17)\n"
    "s_add_u32 s75, s66, 1\n"
    "s_min_i32 s75, s75, s68\n"
    "s_sub_u32 s76, s68, 2\n"
    "s_max_i32 s75, s75, s76\n"
    "s_sub_u32 s75, s75, 1\n"
    "v_lshl_or_b32 v14, s75, 1, 1\n"
    "s_mov_b64 exec, s[64:65]\n"
    "buffer_atomic_smax v14, v13, s[36:39], 0 offen sc1\n"
    "s_mov_b64 exec, -1\n"
    "s_waitcnt lgkmcnt(0)\n"
    "s_barrier\n"
    "ds_read_b128 v[32:35], v11\n"
    "ds_read_b128 v[36:39], v11 offset:16\n"
    "v_xor_b32 v10, 256, v10\n"
    "v_xor_b32 v11, 256, v11\n"
    "s_waitcnt lgkmcnt(0)\n"
    "v_pk_add_f32 v[114:115], v[32:33], v[34:35]\n"
    "v_pk_add_f32 v[116:117], v[36:37], v[38:39]\n"
    "v_pk_add_f32 v[114:115], v[114:115], v[116:117]\n"
    "v_fmac_f32 v32, 0xbe800000, v114\n"
    "v_fmac_f32 v34, 0xbe800000, v114\n"
    "v_fmac_f32 v36, 0xbe800000, v114\n"
    "v_fmac_f32 v38, 0xbe800000, v114\n"
    "v_fmac_f32 v112, 0xbe800000, v114\n"
    "v_mul_f32 v116, v32, v32\n"
    "v_fmac_f32 v116, v34, v34\n"
    "v_fmac_f32 v116, v36, v36\n"
    "v_fmac_f32 v116, v38, v38\n"
    "v_fmac_f32 v115, 0x43100000, v116\n"
    "v_mov_b32 v116, 0x3727c5ac\n"
    "v_fmac_f32 v116, 0x3ae38e39, v115\n"
    "v_rsq_f32 v116, v116\n"
    "s_nop 0\n"
    "v_mul_f32 v114, v112, v116\n"
    "v_pk_fma_f32 v[80:81], v[80:81], v[116:117], v[114:115] op_sel_hi:[1,0,0]\n"
    "v_pk_fma_f32 v[82:83], v[82:83], v[116:117], v[114:115] op_sel_hi:[1,0,0]\n"
    "v_pk_fma_f32 v[84:85], v[84:85], v[116:117], v[114:115] op_sel_hi:[1,0,0]\n"
    "v_pk_fma_f32 v[86:87], v[86:87], v[116:117], v[114:115] op_sel_hi:[1,0,0]\n"
    "v_pk_fma_f32 v[88:89], v[88:89], v[116:117], v[114:115] op_sel_hi:[1,0,0]\n"
    "v_pk_fma_f32 v[90:91], v[90:91], v[116:117], v[114:115] op_sel_hi:[1,0,0]\n"
    "v_pk_fma_f32 v[92:93], v[92:93], v[116:117], v[114:115] op_sel_hi:[1,0,0]\n"
    "v_pk_fma_f32 v[94:95], v[94:95], v[116:117], v[114:115] op_sel_hi:[1,0,0]\n"
    "v_pk_fma_f32 v[96:97], v[96:97], v[116:117], v[114:115] op_sel_hi:[1,0,0]\n"
    "v_pk_fma_f32 v[98:99], v[98:99], v[116:117], v[114:115] op_sel_hi:[1,0,0]\n"
    "v_cvt_pk_bf16_f32 v100, v80, v81\n"
    "v_cvt_pk_bf16_f32 v101, v82, v83\n"
    "v_cvt_pk_bf16_f32 v102, v84, v85\n"
    "v_cvt_pk_bf16_f32 v103, v86, v87\n"
    "v_cvt_pk_bf16_f32 v104, v88, v89\n"
    "v_cvt_pk_bf16_f32 v105, v90, v91\n"
    "v_cvt_pk_bf16_f32 v106, v92, v93\n"
    "v_cvt_pk_bf16_f32 v107, v94, v95\n"
    "v_cvt_pk_bf16_f32 v108, v96, v97\n"
    "v_cvt_pk_bf16_f32 v109, v98, v99\n"
    "v_permlane16_swap_b32 v100, v102\n"
    "v_permlane16_swap_b32 v101, v103\n"
    "buffer_store_dwordx4 v[100:103], v4, s[44:47], s77 offen sc1\n"
    "v_permlane16_swap_b32 v104, v106\n"
    "v_permlane16_swap_b32 v105, v107\n"
    "buffer_store_dwordx4 v[104:107], v4, s[44:47], s78 offen sc1\n"
    "v_permlane16_swap_b32 v108, v110\n"
    "v_permlane16_swap_b32 v109, v111\n"
    "buffer_store_dwordx4 v[108:111], v5, s[44:47], s77 offen sc1\n"
    "s_add_u32 s68, s68, 1\n"
    "s_add_u32 s69, s68, 4\n"
    "s_and_b32 s75, s68, s58\n"
    "s_mul_i32 s77, s75, 81920\n"
    "s_add_u32 s77, s77, s59\n"
    "s_add_u32 s78, s77, s60\n"
    "s_and_b32 s73, s69, s55\n"
    "s_mul_i32 s73, s73, s54\n"
    "s_add_u32 s73, s73, s53\n"
    "s_add_u32 s74, s73, s56\n"
    "s_waitcnt vmcnt(27)\n"
    "v_mfma_f32_16x16x32_bf16 v[80:83], v[24:27], v[124:127], 0\n"
    "v_mfma_f32_16x16x32_bf16 v[84:87], v[28:31], v[124:127], 0\n"
    "s_waitcnt vmcnt(26)\n"
    "v_mfma_f32_16x16x32_bf16 v[88:91], v[24:27], v[128:131], 0\n"
    "v_mfma_f32_16x16x32_bf16 v[92:95], v[28:31], v[128:131], 0\n"
    "s_waitcnt vmcnt(25)\n"
    "v_mfma_f32_16x16x32_bf16 v[96:99], v[24:27], v[132:135], 0\n"
    "buffer_load_dwordx4 v[124:127], v6, s[40:43], s73 offen sc1\n"
    "buffer_load_dwordx4 v[128:131], v6, s[40:43], s74 offen sc1\n"
    "buffer_load_dwordx4 v[132:135], v7, s[40:43], s73 offen sc1\n"
    "v_pk_add_f32 v[112:113], v[80:81], v[82:83]\n"
    "v_pk_add_f32 v[114:115], v[84:85], v[86:87]\n"
    "v_pk_add_f32 v[112:113], v[112:113], v[88:89]\n"
    "v_pk_add_f32 v[114:115], v[114:115], v[90:91]\n"
    "v_pk_add_f32 v[112:113], v[112:113], v[92:93]\n"
    "v_pk_add_f32 v[114:115], v[114:115], v[94:95]\n"
    "v_pk_add_f32 v[112:113], v[112:113], v[96:97]\n"
    "v_pk_add_f32 v[114:115], v[114:115], v[98:99]\n"
    "v_pk_add_f32 v[112:113], v[112:113], v[114:115]\n"
    "v_add_f32 v112, v112, v113\n"
    "s_nop 1\n"
    "v_add_f32_dpp v14, v112, v112 row_ror:8 row_mask:0xf bank_mask:0xf\n"
    "v_add_f32_dpp v15, v112, v112 row_ror:8 row_mask:0xf bank_mask:0xf\n"
    "s_nop 1\n"
    "v_permlane16_swap_b32 v14, v15\n"
    "v_add_f32 v112, v14, v15\n"
    "v_add_f32 v14, v14, v15\n"
    "s_nop 1\n"
    "v_permlane32_swap_b32 v112, v14\n"
    "v_add_f32 v112, v112, v14\n"
    "v_mul_f32 v112, 0x3be38e39, v112\n"
    "v_mul_f32 v114, v112, v12\n"
    "v_pk_add_f32 v[80:81], v[80:81], v[112:113] op_sel_hi:[1,0] neg_lo:[0,1] neg_hi:[0,1]\n"
    "v_pk_add_f32 v[82:83], v[82:83], v[112:113] op_sel_hi:[1,0] neg_lo:[0,1] neg_hi:[0,1]\n"
    "v_pk_add_f32 v[84:85], v[84:85], v[112:113] op_sel_hi:[1,0] neg_lo:[0,1] neg_hi:[0,1]\n"
    "v_pk_add_f32 v[86:87], v[86:87], v[112:113] op_sel_hi:[1,0] neg_lo:[0,1] neg_hi:[0,1]\n"
    "v_pk_add_f32 v[88:89], v[88:89], v[112:113] op_sel_hi:[1,0] neg_lo:[0,1] neg_hi:[0,1]\n"
    "v_pk_add_f32 v[90:91], v[90:91], v[112:113] op_sel_hi:[1,0] neg_lo:[0,1] neg_hi:[0,1]\n"
    "v_pk_add_f32 v[92:93], v[92:93], v[112:113] op_sel_hi:[1,0] neg_lo:[0,1] neg_hi:[0,1]\n"
    "v_pk_add_f32 v[94:95], v[94:95], v[112:113] op_sel_hi:[1,0] neg_lo:[0,1] neg_hi:[0,1]\n"
    "v_pk_add_f32 v[96:97], v[96:97], v[114:115] op_sel_hi:[1,0] neg_lo:[0,1] neg_hi:[0,1]\n"
    "v_pk_add_f32 v[98:99], v[98:99], v[114:115] op_sel_hi:[1,0] neg_lo:[0,1] neg_hi:[0,1]\n"
    "v_pk_mul_f32 v[116:117], v[80:81], v[80:81]\n"
    "v_pk_mul_f32 v[118:119], v[82:83], v[82:83]\n"
    "v_pk_fma_f32 v[116:117], v[84:85], v[84:85], v[116:117]\n"
    "v_pk_fma_f32 v[118:119], v[86:87], v[86:87], v[118:119]\n"
    "v_pk_fma_f32 v[116:117], v[88:89], v[88:89], v[116:117]\n"
    "v_pk_fma_f32 v[118:119], v[90:91], v[90:91], v[118:119]\n"
    "v_pk_fma_f32 v[116:117], v[92:93], v[92:93], v[116:117]\n"
    "v_pk_fma_f32 v[118:119], v[94:95], v[94:95], v[118:119]\n"
    "v_pk_fma_f32 v[116:117], v[96:97], v[96:97], v[116:117]\n"
    "v_pk_fma_f32 v[118:119], v[98:99], v[98:99], v[118:119]\n"
    "v_pk_add_f32 v[116:117], v[116:117], v[118:119]\n"
    "v_add_f32 v113, v116, v117\n"
    "s_nop 1\n"
    "v_add_f32_dpp v14, v113, v113 row_ror:8 row_mask:0xf bank_mask:0xf\n"
    "v_add_f32_dpp v15, v113, v113 row_ror:8 row_mask:0xf bank_mask:0xf\n"
    "s_nop 1\n"
    "v_permlane16_swap_b32 v14, v15\n"
    "v_add_f32 v113, v14, v15\n"
    "v_add_f32 v14, v14, v15\n"
    "s_nop 1\n"
    "v_permlane32_swap_b32 v113, v14\n"
    "v_add_f32 v113, v113, v14\n"
    "ds_write_b64 v10, v[112:113]\n"
    "s_waitcnt vmcnt(17)\n"
    "s_add_u32 s75, s66, 1\n"
    "s_min_i32 s75, s75, s68\n"
    "s_sub_u32 s76, s68, 2\n"
    "s_max_i32 s75, s75, s76\n"
    "s_sub_u32 s75, s75, 1\n"
    "v_lshl_or_b32 v14, s75, 1, 1\n"
    "s_mov_b64 exec, s[64:65]\n"
    "buffer_atomic_smax v14, v13, s[36:39], 0 offen sc1\n"
    "s_mov_b64 exec, -1\n"
    "s_waitcnt lgkmcnt(0)\n"
    "s_barrier\n"
    "ds_read_b128 v[32:35], v11\n"
    "ds_read_b128 v[36:39], v11 offset:16\n"
    "v_xor_b32 v10, 256, v10\n"
    "v_xor_b32 v11, 256, v11\n"
    "s_waitcnt lgkmcnt(0)\n"
    "v_pk_add_f32 v[114:115], v[32:33], v[34:35]\n"
    "v_pk_add_f32 v[116:117], v[36:37], v[38:39]\n"
    "v_pk_add_f32 v[114:115], v[114:115], v[116:117]\n"
    "v_fmac_f32 v32, 0xbe800000, v114\n"
    "v_fmac_f32 v34, 0xbe800000, v114\n"
    "v_fmac_f32 v36, 0xbe800000, v114\n"
    "v_fmac_f32 v38, 0xbe800000, v114\n"
    "v_fmac_f32 v112, 0xbe800000, v114\n"
    "v_mul_f32 v116, v32, v32\n"
    "v_fmac_f32 v116, v34, v34\n"
    "v_fmac_f32 v116, v36, v36\n"
    "v_fmac_f32 v116, v38, v38\n"
    "v_fmac_f32 v115, 0x43100000, v116\n"
    "v_mov_b32 v116, 0x3727c5ac\n"
    "v_fmac_f32 v116, 0x3ae38e39, v115\n"
    "v_rsq_f32 v116, v116\n"
    "s_nop 0\n"
    "v_mul_f32 v114, v112, v116\n"
    "v_pk_fma_f32 v[80:81], v[80:81], v[116:117], v[114:115] op_sel_hi:[1,0,0]\n"
    "v_pk_fma_f32 v[82:83], v[82:83], v[116:117], v[114:115] op_sel_hi:[1,0,0]\n"
    "v_pk_fma_f32 v[84:85], v[84:85], v[116:117], v[114:115] op_sel_hi:[1,0,0]\n"
    "v_pk_fma_f32 v[86:87], v[86:87], v[116:117], v[114:115] op_sel_hi:[1,0,0]\n"
    "v_pk_fma_f32 v[88:89], v[88:89], v[116:117], v[114:115] op_sel_hi:[1,0,0]\n"
    "v_pk_fma_f32 v[90:91], v[90:91], v[116:117], v[114:115] op_sel_hi:[1,0,0]\n"
    "v_pk_fma_f32 v[92:93], v[92:93], v[116:117], v[114:115] op_sel_hi:[1,0,0]\n"
    "v_pk_fma_f32 v[94:95], v[94:95], v[116:117], v[114:115] op_sel_hi:[1,0,0]\n"
    "v_pk_fma_f32 v[96:97], v[96:97], v[116:117], v[114:115] op_sel_hi:[1,0,0]\n"
    "v_pk_fma_f32 v[98:99], v[98:99], v[116:117], v[114:115] op_sel_hi:[1,0,0]\n"
    "v_cvt_pk_bf16_f32 v100, v80, v81\n"
    "v_cvt_pk_bf16_f32 v101, v82, v83\n"
    "v_cvt_pk_bf16_f32 v102, v84, v85\n"
    "v_cvt_pk_bf16_f32 v103, v86, v87\n"
    "v_cvt_pk_bf16_f32 v104, v88, v89\n"
    "v_cvt_pk_bf16_f32 v105, v90, v91\n"
    "v_cvt_pk_bf16_f32 v106, v92, v93\n"
    "v_cvt_pk_bf16_f32 v107, v94, v95\n"
    "v_cvt_pk_bf16_f32 v108, v96, v97\n"
    "v_cvt_pk_bf16_f32 v109, v98, v99\n"
    "v_permlane16_swap_b32 v100, v102\n"
    "v_permlane16_swap_b32 v101, v103\n"
    "buffer_store_dwordx4 v[100:103], v4, s[44:47], s77 offen sc1\n"
    "v_permlane16_swap_b32 v104, v106\n"
    "v_permlane16_swap_b32 v105, v107\n"
    "buffer_store_dwordx4 v[104:107], v4, s[44:47], s78 offen sc1\n"
    "v_permlane16_swap_b32 v108, v110\n"
    "v_permlane16_swap_b32 v109, v111\n"
    "buffer_store_dwordx4 v[108:111], v5, s[44:47], s77 offen sc1\n"
    "s_cmp_eq_u32 s68, s67\n"
    "s_cbranch_scc1 .LrLN0_fin_%=\n"
    "s_branch .LrLN0_top_%=\n"
    ".LrLN0_LOOPE_%=:\n"
    ".LrLN0_fin_%=:\n"
    "s_waitcnt vmcnt(0)\n"
    "s_mov_b32 s68, 63\n"
    "v_lshl_or_b32 v14, s68, 1, 1\n"
    "s_mov_b64 exec, s[64:65]\n"
    "buffer_atomic_smax v14, v13, s[36:39], 0 offen sc1\n"
    "s_mov_b64 exec, -1\n"
    "s_waitcnt vmcnt(0)\n"
    "s_barrier\n"
    "s_branch .LrLN0_FIN_%=\n"
    ".LrLN0_first_%=:\n"
    "s_waitcnt vmcnt(0)\n"
    "v_lshl_or_b32 v14, s68, 1, 1\n"
    "s_mov_b64 exec, s[64:65]\n"
    "buffer_atomic_smax v14, v13, s[36:39], 0 offen sc1\n"
    "s_mov_b64 exec, -1\n"
    "s_branch .LrLN0_first1_%=\n"
    ".LrLN0_FIN_%=:\n"
  );
}
DEVI void role_mid(v16u sa, v16u sb, v16u va, v16u vb) {
  RUN_RED(
    "s_nop 4\n"
    "s_mov_b32 s50, 0\n"
    "s_mov_b32 s51, 0x80000000\n"
    "s_getpc_b64 s[84:85]\n"
    ".LrMID_PC_%=:\n"
    "s_and_b32 s85, s85, 0xffff\n"
    "s_mov_b32 s86, .LrMID_FIN_%=-.LrMID_PC_%=\n"
    "s_mov_b32 s87, 0x20000\n"
    "v_mbcnt_lo_u32_b32 v134, -1, 0\n"
    "v_mbcnt_hi_u32_b32 v134, -1, v134\n"
    "v_lshlrev_b32 v134, 7, v134\n"
    "buffer_load_dword v135, v134, s[84:87], 0 offen\n"
    "v_add_u32 v134, 8192, v134\n"
    "buffer_load_dword v135, v134, s[84:87], 0 offen\n"
    "s_mov_b32 s69, 0\n"
    "v_mov_b32 v122, -1\n"
    "v_mov_b32 v123, -1\n"
    "s_mov_b32 s70, 262144\n"
    ".LrMID_par_%=:\n"
    "buffer_load_dwordx2 v[14:15], off, s[36:39], 0 sc1\n"
    "s_waitcnt vmcnt(0)\n"
    "v_readfirstlane_b32 s75, v14\n"
    "v_readfirstlane_b32 s76, v15\n"
    "s_xor_b32 s75, s75, s48\n"
    "s_xor_b32 s76, s76, s49\n"
    "s_cselect_b32 s76, 2, 0\n"
    "s_or_b32 s76, s76, s75\n"
    "s_cmp_lt_u32 s76, 2\n"
    "s_cbranch_scc1 .LrMID_par1_%=\n"
    "s_sleep 4\n"
    "s_sub_u32 s70, s70, 1\n"
    "s_cbranch_scc0 .LrMID_par_%=\n"
    "s_mov_b32 s70, 0\n"
    ".LrMID_par1_%=:\n"
    "s_and_b32 s75, s75, 1\n"
    "s_lshl_b32 s75, s75, 13\n"
    "s_add_u32 s36, s36, s75\n"
    "s_addc_u32 s37, s37, 0\n"
    "s_and_b32 s76, s75, s61\n"
    "s_add_u32 s40, s40, s76\n"
    "s_addc_u32 s41, s41, 0\n"
    "s_and_b32 s76, s75, s63\n"
    "s_add_u32 s44, s44, s76\n"
    "s_addc_u32 s45, s45, 0\n"
    "s_branch .LrMID_block_%=\n"
    ".LrMID_LOOPB_%=:\n"
    ".LrMID_top_%=:\n"
    "s_mov_b32 s68, s69\n"
    "s_add_u32 s69, s68, 1\n"
    "s_and_b32 s75, s68, s58\n"
    "s_mul_i32 s77, s75, 4194304\n"
    "s_add_u32 s77, s77, s59\n"
    "s_add_u32 s78, s77, s60\n"
    "s_and_b32 s71, s69, 7\n"
    "s_mul_i32 s71, s71, 4194304\n"
    "s_add_u32 s71, s71, s52\n"
    "s_add_u32 s72, s71, 4096\n"
    "s_and_b32 s73, s69, s55\n"
    "s_mul_i32 s73, s73, s54\n"
    "s_add_u32 s73, s73, s53\n"
    "s_add_u32 s74, s73, s56\n"
    "s_mul_i32 s75, s75, 4194304\n"
    "s_add_u32 s75, s75, s57\n"
    "s_add_u32 s76, s75, 1024\n"
    "s_waitcnt vmcnt(12)\n"
    "v_mfma_f32_16x16x32_bf16 v[80:83], v[16:19], v[32:35], 0\n"
    "s_waitcnt vmcnt(11)\n"
    "v_mfma_f32_16x16x32_bf16 v[80:83], v[20:23], v[36:39], v[80:83]\n"
    "s_waitcnt vmcnt(10)\n"
    "v_mfma_f32_16x16x32_bf16 v[84:87], v[16:19], v[40:43], 0\n"
    "s_waitcnt vmcnt(9)\n"
    "v_mfma_f32_16x16x32_bf16 v[84:87], v[20:23], v[44:47], v[84:87]\n"
    "s_waitcnt vmcnt(8)\n"
    "v_mfma_f32_16x16x32_bf16 v[88:91], v[16:19], v[48:51], 0\n"
    "s_waitcnt vmcnt(7)\n"
    "v_mfma_f32_16x16x32_bf16 v[88:91], v[20:23], v[52:55], v[88:91]\n"
    "s_waitcnt vmcnt(6)\n"
    "v_mfma_f32_16x16x32_bf16 v[92:95], v[16:19], v[56:59], 0\n"
    "s_waitcnt vmcnt(5)\n"
    "v_mfma_f32_16x16x32_bf16 v[92:95], v[20:23], v[60:63], v[92:95]\n"
    "s_waitcnt vmcnt(4)\n"
    "v_mfma_f32_16x16x32_bf16 v[96:99], v[16:19], v[64:67], 0\n"
    "s_waitcnt vmcnt(3)\n"
    "v_mfma_f32_16x16x32_bf16 v[80:83], v[24:27], v[68:71], v[80:83]\n"
    "v_mfma_f32_16x16x32_bf16 v[84:87], v[28:31], v[68:71], v[84:87]\n"
    "s_waitcnt vmcnt(2)\n"
    "v_mfma_f32_16x16x32_bf16 v[88:91], v[24:27], v[72:75], v[88:91]\n"
    "v_mfma_f32_16x16x32_bf16 v[92:95], v[28:31], v[72:75], v[92:95]\n"
    "s_waitcnt vmcnt(1)\n"
    "v_mfma_f32_16x16x32_bf16 v[96:99], v[24:27], v[76:79], v[96:99]\n"
    "v_pk_add_f32 v[112:113], v[80:81], v[82:83]\n"
    "v_pk_add_f32 v[114:115], v[84:85], v[86:87]\n"
    "v_pk_add_f32 v[112:113], v[112:113], v[88:89]\n"
    "v_pk_add_f32 v[114:115], v[114:115], v[90:91]\n"
    "v_pk_add_f32 v[112:113], v[112:113], v[92:93]\n"
    "v_pk_add_f32 v[114:115], v[114:115], v[94:95]\n"
    "s_nop 1\n"
    "v_pk_add_f32 v[112:113], v[112:113], v[96:97]\n"
    "v_pk_add_f32 v[114:115], v[114:115], v[98:99]\n"
    "v_pk_add_f32 v[112:113], v[112:113], v[114:115]\n"
    "v_add_f32 v112, v112, v113\n"
    "v_cvt_pk_bf16_f32 v100, v80, v81\n"
    "v_cvt_pk_bf16_f32 v101, v82, v83\n"
    "v_cvt_pk_bf16_f32 v102, v84, v85\n"
    "v_cvt_pk_bf16_f32 v103, v86, v87\n"
    "v_cvt_pk_bf16_f32 v104, v88, v89\n"
    "v_cvt_pk_bf16_f32 v105, v90, v91\n"
    "v_cvt_pk_bf16_f32 v106, v92, v93\n"
    "v_cvt_pk_bf16_f32 v107, v94, v95\n"
    "v_cvt_pk_bf16_f32 v108, v96, v97\n"
    "v_cvt_pk_bf16_f32 v109, v98, v99\n"
    "v_add_f32_dpp v14, v112, v112 row_ror:8 row_mask:0xf bank_mask:0xf\n"
    "v_add_f32_dpp v15, v112, v112 row_ror:8 row_mask:0xf bank_mask:0xf\n"
    "s_nop 1\n"
    "v_permlane16_swap_b32 v14, v15\n"
    "v_add_f32 v112, v14, v15\n"
    "v_add_f32 v14, v14, v15\n"
    "s_nop 1\n"
    "v_permlane32_swap_b32 v112, v14\n"
    "v_add_f32 v112, v112, v14\n"
    "v_mul_f32 v112, 0x3be38e39, v112\n"
    "v_mul_f32 v114, v112, v12\n"
    "v_pk_add_f32 v[80:81], v[80:81], v[112:113] op_sel_hi:[1,0] neg_lo:[0,1] neg_hi:[0,1]\n"
    "v_pk_add_f32 v[82:83], v[82:83], v[112:113] op_sel_hi:[1,0] neg_lo:[0,1] neg_hi:[0,1]\n"
    "v_pk_add_f32 v[84:85], v[84:85], v[112:113] op_sel_hi:[1,0] neg_lo:[0,1] neg_hi:[0,1]\n"
    "v_pk_add_f32 v[86:87], v[86:87], v[112:113] op_sel_hi:[1,0] neg_lo:[0,1] neg_hi:[0,1]\n"
    "v_pk_add_f32 v[88:89], v[88:89], v[112:113] op_sel_hi:[1,0] neg_lo:[0,1] neg_hi:[0,1]\n"
    "v_pk_add_f32 v[90:91], v[90:91], v[112:113] op_sel_hi:[1,0] neg_lo:[0,1] neg_hi:[0,1]\n"
    "v_pk_add_f32 v[92:93], v[92:93], v[112:113] op_sel_hi:[1,0] neg_lo:[0,1] neg_hi:[0,1]\n"
    "v_pk_add_f32 v[94:95], v[94:95], v[112:113] op_sel_hi:[1,0] neg_lo:[0,1] neg_hi:[0,1]\n"
    "v_pk_add_f32 v[96:97], v[96:97], v[114:115] op_sel_hi:[1,0] neg_lo:[0,1] neg_hi:[0,1]\n"
    "v_pk_add_f32 v[98:99], v[98:99], v[114:115] op_sel_hi:[1,0] neg_lo:[0,1] neg_hi:[0,1]\n"
    "v_pk_mul_f32 v[116:117], v[80:81], v[80:81]\n"
    "v_pk_mul_f32 v[118:119], v[82:83], v[82:83]\n"
    "v_pk_fma_f32 v[116:117], v[84:85], v[84:85], v[116:117]\n"
    "v_pk_fma_f32 v[118:119], v[86:87], v[86:87], v[118:119]\n"
    "v_pk_fma_f32 v[116:117], v[88:89], v[88:89], v[116:117]\n"
    "v_pk_fma_f32 v[118:119], v[90:91], v[90:91], v[118:119]\n"
    "v_pk_fma_f32 v[116:117], v[92:93], v[92:93], v[116:117]\n"
    "v_pk_fma_f32 v[118:119], v[94:95], v[94:95], v[118:119]\n"
    "v_pk_fma_f32 v[116:117], v[96:97], v[96:97], v[116:117]\n"
    "v_pk_fma_f32 v[118:119], v[98:99], v[98:99], v[118:119]\n"
    "v_pk_add_f32 v[116:117], v[116:117], v[118:119]\n"
    "v_add_f32 v113, v116, v117\n"
    "s_nop 1\n"
    "v_add_f32_dpp v14, v113, v113 row_ror:8 row_mask:0xf bank_mask:0xf\n"
    "v_add_f32_dpp v15, v113, v113 row_ror:8 row_mask:0xf bank_mask:0xf\n"
    "s_nop 1\n"
    "v_permlane16_swap_b32 v14, v15\n"
    "v_add_f32 v113, v14, v15\n"
    "v_add_f32 v14, v14, v15\n"
    "s_nop 1\n"
    "v_permlane32_swap_b32 v113, v14\n"
    "v_add_f32 v113, v113, v14\n"
    "ds_write_b64 v10, v[112:113]\n"
    "v_add_u32 v120, v122, v1\n"
    "v_ashrrev_i32 v120, 1, v120\n"
    "v_cmp_gt_i32 vcc, s69, v120\n"
    "s_nop 0\n"
    "s_or_b64 exec, vcc, s[50:51]\n"
    "buffer_atomic_smax v122, v0, s[36:39], 0 offen sc0 sc1\n"
    "s_mov_b64 exec, -1\n"
    "s_nop 0\n"
    "v_permlane16_swap_b32 v100, v102\n"
    "v_permlane16_swap_b32 v101, v103\n"
    "v_permlane16_swap_b32 v104, v106\n"
    "v_permlane16_swap_b32 v105, v107\n"
    "v_permlane16_swap_b32 v108, v110\n"
    "v_permlane16_swap_b32 v109, v111\n"
    "s_waitcnt lgkmcnt(0)\n"
    "s_barrier\n"
    "ds_read_b128 v[32:35], v11\n"
    "ds_read_b128 v[36:39], v11 offset:16\n"
    "buffer_store_dwordx4 v[100:103], v8, s[36:39], s75 offen sc1\n"
    "buffer_store_dwordx4 v[104:107], v8, s[36:39], s76 offen sc1\n"
    "buffer_store_dwordx4 v[108:111], v9, s[36:39], s75 offen sc1\n"
    "v_xor_b32 v10, 256, v10\n"
    "v_xor_b32 v11, 256, v11\n"
    "s_waitcnt lgkmcnt(0)\n"
    "v_pk_add_f32 v[114:115], v[32:33], v[34:35]\n"
    "v_pk_add_f32 v[116:117], v[36:37], v[38:39]\n"
    "v_pk_add_f32 v[114:115], v[114:115], v[116:117]\n"
    "v_fmac_f32 v32, 0xbe800000, v114\n"
    "v_fmac_f32 v34, 0xbe800000, v114\n"
    "v_fmac_f32 v36, 0xbe800000, v114\n"
    "v_fmac_f32 v38, 0xbe800000, v114\n"
    "v_fmac_f32 v112, 0xbe800000, v114\n"
    "v_mul_f32 v116, v32, v32\n"
    "v_fmac_f32 v116, v34, v34\n"
    "v_fmac_f32 v116, v36, v36\n"
    "v_fmac_f32 v116, v38, v38\n"
    "v_fmac_f32 v115, 0x43100000, v116\n"
    "v_mov_b32 v116, 0x3727c5ac\n"
    "v_fmac_f32 v116, 0x3ae38e39, v115\n"
    "v_rsq_f32 v116, v116\n"
    "s_nop 0\n"
    "v_mul_f32 v114, v112, v116\n"
    "v_pk_fma_f32 v[80:81], v[80:81], v[116:117], v[114:115] op_sel_hi:[1,0,0]\n"
    "v_pk_fma_f32 v[82:83], v[82:83], v[116:117], v[114:115] op_sel_hi:[1,0,0]\n"
    "v_pk_fma_f32 v[84:85], v[84:85], v[116:117], v[114:115] op_sel_hi:[1,0,0]\n"
    "v_pk_fma_f32 v[86:87], v[86:87], v[116:117], v[114:115] op_sel_hi:[1,0,0]\n"
    "v_pk_fma_f32 v[88:89], v[88:89], v[116:117], v[114:115] op_sel_hi:[1,0,0]\n"
    "v_pk_fma_f32 v[90:91], v[90:91], v[116:117], v[114:115] op_sel_hi:[1,0,0]\n"
    "v_pk_fma_f32 v[92:93], v[92:93], v[116:117], v[114:115] op_sel_hi:[1,0,0]\n"
    "v_pk_fma_f32 v[94:95], v[94:95], v[116:117], v[114:115] op_sel_hi:[1,0,0]\n"
    "v_pk_fma_f32 v[96:97], v[96:97], v[116:117], v[114:115] op_sel_hi:[1,0,0]\n"
    "v_pk_fma_f32 v[98:99], v[98:99], v[116:117], v[114:115] op_sel_hi:[1,0,0]\n"
    "v_cvt_pk_bf16_f32 v100, v80, v81\n"
    "v_cvt_pk_bf16_f32 v101, v82, v83\n"
    "v_cvt_pk_bf16_f32 v102, v84, v85\n"
    "v_cvt_pk_bf16_f32 v103, v86, v87\n"
    "v_cvt_pk_bf16_f32 v104, v88, v89\n"
    "v_cvt_pk_bf16_f32 v105, v90, v91\n"
    "v_cvt_pk_bf16_f32 v106, v92, v93\n"
    "v_cvt_pk_bf16_f32 v107, v94, v95\n"
    "v_cvt_pk_bf16_f32 v108, v96, v97\n"
    "v_cvt_pk_bf16_f32 v109, v98, v99\n"
    "v_permlane16_swap_b32 v100, v102\n"
    "v_permlane16_swap_b32 v101, v103\n"
    "buffer_store_dwordx4 v[100:103], v4, s[44:47], s77 offen sc1\n"
    "v_permlane16_swap_b32 v104, v106\n"
    "v_permlane16_swap_b32 v105, v107\n"
    "buffer_store_dwordx4 v[104:107], v4, s[44:47], s78 offen sc1\n"
    "v_permlane16_swap_b32 v108, v110\n"
    "v_permlane16_swap_b32 v109, v111\n"
    "buffer_store_dwordx4 v[108:111], v5, s[44:47], s77 offen sc1\n"
    "s_cmp_eq_u32 s69, 64\n"
    "s_cbranch_scc1 .LrMID_fin_%=\n"
    "s_waitcnt vmcnt(6)\n"
    "v_add_u32 v120, v122, v1\n"
    "v_ashrrev_i32 v120, 1, v120\n"
    "v_cmp_gt_i32 vcc, s69, v120\n"
    "s_cbranch_vccnz .LrMID_idle_%=\n"
    "buffer_load_dwordx4 v[32:35], v2, s[36:39], s71 offen sc1\n"
    "buffer_load_dwordx4 v[36:39], v2, s[36:39], s71 offen offset:512 sc1\n"
    "buffer_load_dwordx4 v[40:43], v2, s[36:39], s71 offen offset:2048 sc1\n"
    "buffer_load_dwordx4 v[44:47], v2, s[36:39], s71 offen offset:2560 sc1\n"
    "buffer_load_dwordx4 v[48:51], v2, s[36:39], s72 offen sc1\n"
    "buffer_load_dwordx4 v[52:55], v2, s[36:39], s72 offen offset:512 sc1\n"
    "buffer_load_dwordx4 v[56:59], v2, s[36:39], s72 offen offset:2048 sc1\n"
    "buffer_load_dwordx4 v[60:63], v2, s[36:39], s72 offen offset:2560 sc1\n"
    "buffer_load_dwordx4 v[64:67], v3, s[36:39], s71 offen sc1\n"
    "buffer_load_dwordx4 v[68:71], v6, s[40:43], s73 offen sc1\n"
    "buffer_load_dwordx4 v[72:75], v6, s[40:43], s74 offen sc1\n"
    "buffer_load_dwordx4 v[76:79], v7, s[40:43], s73 offen sc1\n"
    "s_waitcnt vmcnt(12)\n"
    "v_lshl_or_b32 v14, s68, 1, 1\n"
    "s_mov_b64 exec, s[64:65]\n"
    "buffer_atomic_smax v14, v13, s[36:39], 0 offen sc1\n"
    "s_mov_b64 exec, -1\n"
    "s_branch .LrMID_top_%=\n"
    ".LrMID_LOOPE_%=:\n"
    ".LrMID_fin_%=:\n"
    "s_waitcnt vmcnt(0)\n"
    "s_mov_b32 s68, 63\n"
    "v_lshl_or_b32 v14, s68, 1, 1\n"
    "s_mov_b64 exec, s[64:65]\n"
    "buffer_atomic_smax v14, v13, s[36:39], 0 offen sc1\n"
    "s_mov_b64 exec, -1\n"
    "s_waitcnt vmcnt(0)\n"
    "s_barrier\n"
    "s_branch .LrMID_FIN_%=\n"
    ".LrMID_idle_%=:\n"
    "v_add_u32 v120, v123, v1\n"
    "v_ashrrev_i32 v120, 1, v120\n"
    "v_cmp_gt_i32 vcc, s69, v120\n"
    "s_nop 0\n"
    "s_or_b64 exec, vcc, s[50:51]\n"
    "buffer_atomic_smax v123, v0, s[36:39], 0 offen sc0 sc1\n"
    "s_mov_b64 exec, -1\n"
    "s_waitcnt vmcnt(1)\n"
    "v_lshl_or_b32 v14, s68, 1, 1\n"
    "s_mov_b64 exec, s[64:65]\n"
    "buffer_atomic_smax v14, v13, s[36:39], 0 offen sc1\n"
    "s_mov_b64 exec, -1\n"
    ".LrMID_block_%=:\n"
    "v_add_u32 v120, v122, v1\n"
    "v_ashrrev_i32 v120, 1, v120\n"
    "v_cmp_gt_i32 vcc, s69, v120\n"
    "s_nop 0\n"
    "s_or_b64 exec, vcc, s[50:51]\n"
    "buffer_atomic_smax v122, v0, s[36:39], 0 offen sc0 sc1\n"
    "s_mov_b64 exec, -1\n"
    "s_sleep 3\n"
    "s_waitcnt vmcnt(1)\n"
    "v_add_u32 v120, v123, v1\n"
    "v_ashrrev_i32 v120, 1, v120\n"
    "v_cmp_gt_i32 vcc, s69, v120\n"
    "s_cbranch_vccz .LrMID_go_%=\n"
    "v_add_u32 v120, v123, v1\n"
    "v_ashrrev_i32 v120, 1, v120\n"
    "v_cmp_gt_i32 vcc, s69, v120\n"
    "s_nop 0\n"
    "s_or_b64 exec, vcc, s[50:51]\n"
    "buffer_atomic_smax v123, v0, s[36:39], 0 offen sc0 sc1\n"
    "s_mov_b64 exec, -1\n"
    "s_sleep 3\n"
    "s_waitcnt vmcnt(1)\n"
    "v_add_u32 v120, v122, v1\n"
    "v_ashrrev_i32 v120, 1, v120\n"
    "v_cmp_gt_i32 vcc, s69, v120\n"
    "s_cbranch_vccz .LrMID_go_%=\n"
    "s_sub_u32 s70, s70, 1\n"
    "s_cbranch_scc0 .LrMID_block_%=\n"
    "s_mov_b32 s70, 0\n"
    ".LrMID_go_%=:\n"
    "s_and_b32 s71, s69, 7\n"
    "s_mul_i32 s71, s71, 4194304\n"
    "s_add_u32 s71, s71, s52\n"
    "s_add_u32 s72, s71, 4096\n"
    "s_and_b32 s73, s69, s55\n"
    "s_mul_i32 s73, s73, s54\n"
    "s_add_u32 s73, s73, s53\n"
    "s_add_u32 s74, s73, s56\n"
    "buffer_load_dwordx4 v[32:35], v2, s[36:39], s71 offen sc1\n"
    "buffer_load_dwordx4 v[36:39], v2, s[36:39], s71 offen offset:512 sc1\n"
    "buffer_load_dwordx4 v[40:43], v2, s[36:39], s71 offen offset:2048 sc1\n"
    "buffer_load_dwordx4 v[44:47], v2, s[36:39], s71 offen offset:2560 sc1\n"
    "buffer_load_dwordx4 v[48:51], v2, s[36:39], s72 offen sc1\n"
    "buffer_load_dwordx4 v[52:55], v2, s[36:39], s72 offen offset:512 sc1\n"
    "buffer_load_dwordx4 v[56:59], v2, s[36:39], s72 offen offset:2048 sc1\n"
    "buffer_load_dwordx4 v[60:63], v2, s[36:39], s72 offen offset:2560 sc1\n"
    "buffer_load_dwordx4 v[64:67], v3, s[36:39], s71 offen sc1\n"
    "buffer_load_dwordx4 v[68:71], v6, s[40:43], s73 offen sc1\n"
    "buffer_load_dwordx4 v[72:75], v6, s[40:43], s74 offen sc1\n"
    "buffer_load_dwordx4 v[76:79], v7, s[40:43], s73 offen sc1\n"
    "buffer_load_dword v121, v7, s[40:43], s73 offen\n"
    "s_branch .LrMID_top_%=\n"
    ".LrMID_FIN_%=:\n"
  );
}
DEVI void role_fin(const Params& p, int q, u32 dbg, u64 tag) {
  v16u sa, sb, va, vb;
  red_args<2>(p, 1, q, dbg, tag, sa, sb, va, vb);
  RUN_RED(
    "s_nop 4\n"
    "s_mov_b32 s50, 0\n"
    "s_mov_b32 s51, 0x80000000\n"
    "s_getpc_b64 s[84:85]\n"
    ".LrFIN_PC_%=:\n"
    "s_and_b32 s85, s85, 0xffff\n"
    "s_mov_b32 s86, .LrFIN_FIN_%=-.LrFIN_PC_%=\n"
    "s_mov_b32 s87, 0x20000\n"
    "v_mbcnt_lo_u32_b32 v134, -1, 0\n"
    "v_mbcnt_hi_u32_b32 v134, -1, v134\n"
    "v_lshlrev_b32 v134, 7, v134\n"
    "buffer_load_dword v135, v134, s[84:87], 0 offen\n"
    "v_add_u32 v134, 8192, v134\n"
    "buffer_load_dword v135, v134, s[84:87], 0 offen\n"
    "s_mov_b32 s69, 0\n"
    "v_mov_b32 v122, -1\n"
    "v_mov_b32 v123, -1\n"
    "s_mov_b32 s70, 262144\n"
    ".LrFIN_par_%=:\n"
    "buffer_load_dwordx2 v[14:15], off, s[36:39], 0 sc1\n"
    "s_waitcnt vmcnt(0)\n"
    "v_readfirstlane_b32 s75, v14\n"
    "v_readfirstlane_b32 s76, v15\n"
    "s_xor_b32 s75, s75, s48\n"
    "s_xor_b32 s76, s76, s49\n"
    "s_cselect_b32 s76, 2, 0\n"
    "s_or_b32 s76, s76, s75\n"
    "s_cmp_lt_u32 s76, 2\n"
    "s_cbranch_scc1 .LrFIN_par1_%=\n"
    "s_sleep 4\n"
    "s_sub_u32 s70, s70, 1\n"
    "s_cbranch_scc0 .LrFIN_par_%=\n"
    "s_mov_b32 s70, 0\n"
    ".LrFIN_par1_%=:\n"
    "s_and_b32 s75, s75, 1\n"
    "s_lshl_b32 s75, s75, 13\n"
    "s_add_u32 s36, s36, s75\n"
    "s_addc_u32 s37, s37, 0\n"
    "s_and_b32 s76, s75, s61\n"
    "s_add_u32 s40, s40, s76\n"
    "s_addc_u32 s41, s41, 0\n"
    "s_and_b32 s76, s75, s63\n"
    "s_add_u32 s44, s44, s76\n"
    "s_addc_u32 s45, s45, 0\n"
    "s_branch .LrFIN_block_%=\n"
    ".LrFIN_LOOPB_%=:\n"
    ".LrFIN_top_%=:\n"
    "s_mov_b32 s68, s69\n"
    "s_add_u32 s69, s68, 1\n"
    "s_and_b32 s75, s68, s58\n"
    "s_mul_i32 s77, s75, 36864\n"
    "s_add_u32 s77, s77, s59\n"
    "s_add_u32 s78, s77, s60\n"
    "s_and_b32 s71, s69, 7\n"
    "s_mul_i32 s71, s71, 4194304\n"
    "s_add_u32 s71, s71, s52\n"
    "s_add_u32 s72, s71, 4096\n"
    "s_and_b32 s73, s69, s55\n"
    "s_mul_i32 s73, s73, s54\n"
    "s_add_u32 s73, s73, s53\n"
    "s_add_u32 s74, s73, s56\n"
    "s_waitcnt vmcnt(11)\n"
    "v_mfma_f32_16x16x32_bf16 v[80:83], v[16:19], v[32:35], 0\n"
    "s_waitcnt vmcnt(10)\n"
    "v_mfma_f32_16x16x32_bf16 v[80:83], v[20:23], v[36:39], v[80:83]\n"
    "s_waitcnt vmcnt(9)\n"
    "v_mfma_f32_16x16x32_bf16 v[84:87], v[16:19], v[40:43], 0\n"
    "s_waitcnt vmcnt(8)\n"
    "v_mfma_f32_16x16x32_bf16 v[84:87], v[20:23], v[44:47], v[84:87]\n"
    "s_waitcnt vmcnt(7)\n"
    "v_mfma_f32_16x16x32_bf16 v[88:91], v[16:19], v[48:51], 0\n"
    "s_waitcnt vmcnt(6)\n"
    "v_mfma_f32_16x16x32_bf16 v[88:91], v[20:23], v[52:55], v[88:91]\n"
    "s_waitcnt vmcnt(5)\n"
    "v_mfma_f32_16x16x32_bf16 v[92:95], v[16:19], v[56:59], 0\n"
    "s_waitcnt vmcnt(4)\n"
    "v_mfma_f32_16x16x32_bf16 v[92:95], v[20:23], v[60:63], v[92:95]\n"
    "s_waitcnt vmcnt(3)\n"
    "v_mfma_f32_16x16x32_bf16 v[96:99], v[16:19], v[64:67], 0\n"
    "s_waitcnt vmcnt(2)\n"
    "v_mfma_f32_16x16x32_bf16 v[80:83], v[24:27], v[68:71], v[80:83]\n"
    "v_mfma_f32_16x16x32_bf16 v[84:87], v[28:31], v[68:71], v[84:87]\n"
    "s_waitcnt vmcnt(1)\n"
    "v_mfma_f32_16x16x32_bf16 v[88:91], v[24:27], v[72:75], v[88:91]\n"
    "v_mfma_f32_16x16x32_bf16 v[92:95], v[28:31], v[72:75], v[92:95]\n"
    "s_waitcnt vmcnt(0)\n"
    "v_mfma_f32_16x16x32_bf16 v[96:99], v[24:27], v[76:79], v[96:99]\n"
    "v_add_u32 v120, v122, v1\n"
    "v_ashrrev_i32 v120, 1, v120\n"
    "v_cmp_gt_i32 vcc, s69, v120\n"
    "s_nop 0\n"
    "s_or_b64 exec, vcc, s[50:51]\n"
    "buffer_atomic_smax v122, v0, s[36:39], 0 offen sc0 sc1\n"
    "s_mov_b64 exec, -1\n"
    "v_lshl_or_b32 v14, s68, 1, 1\n"
    "s_mov_b64 exec, s[64:65]\n"
    "buffer_atomic_smax v14, v13, s[36:39], 0 offen sc1\n"
    "s_mov_b64 exec, -1\n"
    "v_cvt_pk_bf16_f32 v100, v80, v81\n"
    "v_cvt_pk_bf16_f32 v101, v82, v83\n"
    "v_cvt_pk_bf16_f32 v102, v84, v85\n"
    "v_cvt_pk_bf16_f32 v103, v86, v87\n"
    "v_cvt_pk_bf16_f32 v104, v88, v89\n"
    "v_cvt_pk_bf16_f32 v105, v90, v91\n"
    "v_cvt_pk_bf16_f32 v106, v92, v93\n"
    "v_cvt_pk_bf16_f32 v107, v94, v95\n"
    "v_cvt_pk_bf16_f32 v108, v96, v97\n"
    "v_cvt_pk_bf16_f32 v109, v98, v99\n"
    "v_permlane16_swap_b32 v100, v102\n"
    "v_permlane16_swap_b32 v101, v103\n"
    "ds_write_b128 v10, v[100:103]\n"
    "v_permlane16_swap_b32 v104, v106\n"
    "v_permlane16_swap_b32 v105, v107\n"
    "ds_write_b128 v10, v[104:107] offset:128\n"
    "v_permlane16_swap_b32 v108, v110\n"
    "v_permlane16_swap_b32 v109, v111\n"
    "ds_write_b128 v12, v[108:111]\n"
    "v_xor_b32 v10, 16384, v10\n"
    "v_xor_b32 v12, 16384, v12\n"
    "s_waitcnt lgkmcnt(0)\n"
    "s_barrier\n"
    "ds_read_b128 v[100:103], v11 offset:0\n"
    "ds_read_b128 v[104:107], v11 offset:4096\n"
    "ds_read_b128 v[108:111], v11 offset:8192\n"
    "v_xor_b32 v11, 16384, v11\n"
    "s_waitcnt lgkmcnt(2)\n"
    "buffer_store_dwordx4 v[100:103], v4, s[44:47], s77 offen sc1\n"
    "s_waitcnt lgkmcnt(1)\n"
    "buffer_store_dwordx4 v[104:107], v4, s[44:47], s78 offen sc1\n"
    "s_waitcnt lgkmcnt(0)\n"
    "buffer_store_dwordx4 v[108:111], v5, s[44:47], s77 offen sc1\n"
    "s_cmp_eq_u32 s69, 64\n"
    "s_cbranch_scc1 .LrFIN_fin_%=\n"
    "s_waitcnt vmcnt(4)\n"
    "v_add_u32 v120, v122, v1\n"
    "v_ashrrev_i32 v120, 1, v120\n"
    "v_cmp_gt_i32 vcc, s69, v120\n"
    "s_cbranch_vccnz .LrFIN_idle_%=\n"
    "buffer_load_dwordx4 v[32:35], v2, s[36:39], s71 offen sc1\n"
    "buffer_load_dwordx4 v[36:39], v2, s[36:39], s71 offen offset:512 sc1\n"
    "buffer_load_dwordx4 v[40:43], v2, s[36:39], s71 offen offset:2048 sc1\n"
    "buffer_load_dwordx4 v[44:47], v2, s[36:39], s71 offen offset:2560 sc1\n"
    "buffer_load_dwordx4 v[48:51], v2, s[36:39], s72 offen sc1\n"
    "buffer_load_dwordx4 v[52:55], v2, s[36:39], s72 offen offset:512 sc1\n"
    "buffer_load_dwordx4 v[56:59], v2, s[36:39], s72 offen offset:2048 sc1\n"
    "buffer_load_dwordx4 v[60:63], v2, s[36:39], s72 offen offset:2560 sc1\n"
    "buffer_load_dwordx4 v[64:67], v3, s[36:39], s71 offen sc1\n"
    "buffer_load_dwordx4 v[68:71], v6, s[40:43], s73 offen sc1\n"
    "buffer_load_dwordx4 v[72:75], v6, s[40:43], s74 offen sc1\n"
    "buffer_load_dwordx4 v[76:79], v7, s[40:43], s73 offen sc1\n"
    "s_branch .LrFIN_top_%=\n"
    ".LrFIN_LOOPE_%=:\n"
    ".LrFIN_fin_%=:\n"
    "s_waitcnt vmcnt(0)\n"
    "s_branch .LrFIN_FIN_%=\n"
    ".LrFIN_idle_%=:\n"
    ".LrFIN_block_%=:\n"
    "v_add_u32 v120, v122, v1\n"
    "v_ashrrev_i32 v120, 1, v120\n"
    "v_cmp_gt_i32 vcc, s69, v120\n"
    "s_nop 0\n"
    "s_or_b64 exec, vcc, s[50:51]\n"
    "buffer_atomic_smax v122, v0, s[36:39], 0 offen sc0 sc1\n"
    "s_mov_b64 exec, -1\n"
    "s_sleep 3\n"
    "s_waitcnt vmcnt(1)\n"
    "v_add_u32 v120, v123, v1\n"
    "v_ashrrev_i32 v120, 1, v120\n"
    "v_cmp_gt_i32 vcc, s69, v120\n"
    "s_cbranch_vccz .LrFIN_go_%=\n"
    "v_add_u32 v120, v123, v1\n"
    "v_ashrrev_i32 v120, 1, v120\n"
    "v_cmp_gt_i32 vcc, s69, v120\n"
    "s_nop 0\n"
    "s_or_b64 exec, vcc, s[50:51]\n"
    "buffer_atomic_smax v123, v0, s[36:39], 0 offen sc0 sc1\n"
    "s_mov_b64 exec, -1\n"
    "s_sleep 3\n"
    "s_waitcnt vmcnt(1)\n"
    "v_add_u32 v120, v122, v1\n"
    "v_ashrrev_i32 v120, 1, v120\n"
    "v_cmp_gt_i32 vcc, s69, v120\n"
    "s_cbranch_vccz .LrFIN_go_%=\n"
    "s_sub_u32 s70, s70, 1\n"
    "s_cbranch_scc0 .LrFIN_block_%=\n"
    "s_mov_b32 s70, 0\n"
    ".LrFIN_go_%=:\n"
    "s_and_b32 s71, s69, 7\n"
    "s_mul_i32 s71, s71, 4194304\n"
    "s_add_u32 s71, s71, s52\n"
    "s_add_u32 s72, s71, 4096\n"
    "s_and_b32 s73, s69, s55\n"
    "s_mul_i32 s73, s73, s54\n"
    "s_add_u32 s73, s73, s53\n"
    "s_add_u32 s74, s73, s56\n"
    "buffer_load_dwordx4 v[32:35], v2, s[36:39], s71 offen sc1\n"
    "buffer_load_dwordx4 v[36:39], v2, s[36:39], s71 offen offset:512 sc1\n"
    "buffer_load_dwordx4 v[40:43], v2, s[36:39], s71 offen offset:2048 sc1\n"
    "buffer_load_dwordx4 v[44:47], v2, s[36:39], s71 offen offset:2560 sc1\n"
    "buffer_load_dwordx4 v[48:51], v2, s[36:39], s72 offen sc1\n"
    "buffer_load_dwordx4 v[52:55], v2, s[36:39], s72 offen offset:512 sc1\n"
    "buffer_load_dwordx4 v[56:59], v2, s[36:39], s72 offen offset:2048 sc1\n"
    "buffer_load_dwordx4 v[60:63], v2, s[36:39], s72 offen offset:2560 sc1\n"
    "buffer_load_dwordx4 v[64:67], v3, s[36:39], s71 offen sc1\n"
    "buffer_load_dwordx4 v[68:71], v6, s[40:43], s73 offen sc1\n"
    "buffer_load_dwordx4 v[72:75], v6, s[40:43], s74 offen sc1\n"
    "buffer_load_dwordx4 v[76:79], v7, s[40:43], s73 offen sc1\n"
    "s_branch .LrFIN_top_%=\n"
    ".LrFIN_FIN_%=:\n"
  );
}

__global__ void __launch_bounds__(256, 1) mega(Params p) {
  extern __shared__ __attribute__((aligned(16))) unsigned char smem[];
  const rsrc_t rw = __builtin_amdgcn_make_buffer_rsrc(p.ws, 0, WS_END + 8192, 0x00020000);
  const unsigned long long t00 = __builtin_amdgcn_s_memrealtime();
  if (threadIdx.x < 64) {
    const int lane = threadIdx.x, b = blockIdx.x;
    if (p.role_mask & 0x40000) st16(rw, (v4u){~0u, ~0u, ~0u, ~0u}, lane < 22 ? CHUNK + (u32)b * 352 + lane * 16 : OOB, 0);
    const u64 tg = ld8u(rw, EPOCH) + 1;
    drain();
    if (lane == 0) {
      {
        const v2u tv = {(u32)tg, (u32)(tg >> 32)};
        const u32 a = READY + (u32)b * 8;
        asm volatile("buffer_atomic_swap_x2 %0, %1, %2, 0 offen sc1" :: "v"(tv), "v"(a), "s"(rw) : "memory");
      }
      int rid = (int)(__builtin_amdgcn_s_getreg(20 | (3 << 11)) & 7u) * 32 + (b >> 3);
#pragma unroll 1
      for (int k = 0; k < 256; ++k) {
        if (__hip_atomic_exchange((u64*)(p.ws + CLAIM) + ((rid & 31) * 8 + (rid >> 5)), tg, __ATOMIC_RELAXED, __HIP_MEMORY_SCOPE_AGENT) != tg) break;
        rid = (rid + 1) & 255;
      }
      *(volatile v4u*)smem = (v4u){(u32)rid, (u32)tg, (u32)(tg >> 32), 0u};
    }
  }
  int rid = (int)(__builtin_amdgcn_s_getreg(20 | (3 << 11)) & 7u) * 32 + (int)(blockIdx.x >> 3);
  u64 tag = 0;
  bool sure = false;
  for (;;) {
  if (!sure && (rid >= 128 || (rid & 31) >= SL_ATT)) { confirm(rid, tag, sure, smem); continue; }
  const int L = rid >> 7, j = (rid >> 5) & 3, sl = rid & 31;
  const u32 dbg = OFF_DBG + (u32)rid * 66 * 256;
  auto on = [&](int r) { return ((p.role_mask >> (2 + 8 * L + r)) & 1) != 0; };
  STAMP(65, 8);
  STAMP(65 + 32, 9);
  if (WARM && threadIdx.x >= 64) {
    const u32 n = threadIdx.x - 64;
    const u32 a = n < OFF_DBG / (1u << 20) ? (n << 20) + 4096 : OOB;
    asm volatile("s_mov_b32 m0, 163584\n\ts_nop 0\n\tbuffer_load_dword %0, %1, 0 offen lds" :: "v"(a), "s"(rw) : "memory");
  }
  if (((PULL >> L) & 1) && sl >= SL_WO && sl < SL_REDA) pull_weights(p, j, sl - SL_WO);
  if (sl < SL_P) { delay_start(L ? DL_R1 : 0, t00); if (on(1)) { role_rec(p, L, j, sl, dbg, rid, tag, sure, smem); continue; } if (!confirm(rid, tag, sure, smem)) continue; }
  else if (sl < SL_ATT) { delay_start(L ? DL_R1 : 0, t00); if (on(0)) { role_p(p, L, j, sl - SL_P, dbg, rid, tag, sure, smem); continue; } if (!confirm(rid, tag, sure, smem)) continue; }
  else if (sl < SL_WO) {
    const u32 shift = find_shift(rw, rid == 128 + SL_ATT && threadIdx.x < 64, tag);
    if (p.role_mask & 2) hot_init(p, rw, L, 2 * j + sl - SL_ATT, shift);
    if (on(2)) role_att(p, L, j, sl - SL_ATT, dbg, shift);
  }
  else if (sl < SL_UP) { delay_start(L ? DL_W1 : DL_W0, t00); if (on(3)) role_wo(p, L, j, sl - SL_WO, dbg, tag); }
  else if (sl < SL_DOWN) { delay_start(L ? DL_U1 : DL_U0, t00); if (on(5)) role_up(p, L, j, sl - SL_UP, dbg, tag); }
  else if (sl < SL_REDA) { delay_start(L ? DL_D1 : DL_D0, t00); if (on(6)) role_down(p, L, j, sl - SL_DOWN, dbg, tag); }
  else {
    if (sl == SL_REDA && (p.role_mask & 2)) hot_init(p, rw, L, 8 + j, find_shift(rw, false, tag));
    if ((p.role_mask & 1) && (L == 1 || sl == SL_REDF)) role_ln0(p, L == 0 ? 2 : (sl == SL_REDA ? 1 : 0), j, dbg, tag);
    if (sl == SL_REDF && L == 1) {
      if (on(7)) role_fin(p, j, dbg, tag);
    } else {
      v16u sa, sb, va, vb;
      if (sl == SL_REDA) red_args<1>(p, L, j, dbg, tag, sa, sb, va, vb);
      else red_args<2>(p, 0, j, dbg, tag, sa, sb, va, vb);
      if (on(sl == SL_REDA ? 4 : 7)) role_mid(sa, sb, va, vb);
    }
  }
  break;
  }
}

static void fill_params(Params& p, void* const* d_in, void* d_out, void* d_ws) {
  p.x = (const u16*)d_in[0]; p.mem0 = (const u16*)d_in[1];
  p.Wq = (const u16*)d_in[2]; p.Wk = (const u16*)d_in[3]; p.Wv = (const u16*)d_in[4]; p.Wo = (const u16*)d_in[5];
  p.w1 = (const u16*)d_in[8]; p.w3 = (const u16*)d_in[9]; p.w2 = (const u16*)d_in[10];
  p.out = (u16*)d_out; p.ws = (unsigned char*)d_ws;
}

extern "C" void kernel_launch(void* const* d_in, const int* in_sizes, int n_in, void* d_out, int out_size, void* d_ws,
                              size_t ws_size, hipStream_t stream) {
  Params p;
  fill_params(p, d_in, d_out, d_ws);
  (void)hipFuncSetAttribute((const void*)mega, hipFuncAttributeMaxDynamicSharedMemorySize, LDS_BYTES);
  p.role_mask = 0x7FFFF; p.unused = 0;
  mega<<<256, 256, LDS_BYTES, stream>>>(p);
}
```

```cpp
#include <hip/hip_runtime.h>
#include <stdint.h>
#include <type_traits>
#include <utility>

typedef unsigned short u16;
typedef unsigned int u32;
typedef short bf16x8 __attribute__((ext_vector_type(8)));
typedef short bf16x4 __attribute__((ext_vector_type(4)));
typedef float f32x4 __attribute__((ext_vector_type(4)));
typedef float f32x2 __attribute__((ext_vector_type(2)));
typedef __bf16 bf2_t __attribute__((ext_vector_type(2)));
typedef unsigned v16u __attribute__((ext_vector_type(16)));
typedef unsigned v8u __attribute__((ext_vector_type(8)));
typedef unsigned v4u __attribute__((ext_vector_type(4)));
typedef unsigned v2u __attribute__((ext_vector_type(2)));
typedef __amdgpu_buffer_rsrc_t rsrc_t;
#define DEVI __device__ __forceinline__

static constexpr int NSEG = 64;
static constexpr int WARM = 1, PSLOW = 1, PSKIP = 2, PULL = 1, RQ = 8, RO = 8, RP = 8, RH = 8, RX = 8, RX2 = 16, LN_B = 28, LN_C = 52, PS_ATT = 3, PS_RED = 3, DL_R1 = 12, DL_W0 = 5, DL_W1 = 14, DL_U0 = 8, DL_U1 = 3, DL_D0 = 11, DL_D1 = 5;

static constexpr u32 PARW = 0, EPOCH = 128, READY = 2048, CLAIM = 4096;
static constexpr u32 CHUNK = 16384;
static constexpr u32 MBOX = CHUNK;
static constexpr u32 INITF = 5 * CHUNK;
static constexpr u32 CTRL_BYTES = 6 * CHUNK + 8192;
static constexpr u32 HOT = 6 * CHUNK, HOT2 = 2u << 20;
static constexpr u32 SLOT = 4u << 20;
static constexpr u32 PGA = SLOT, PGB = SLOT + (2u << 20);
static constexpr int cmax(int a, int b) { return a > b ? a : b; }
static constexpr int NSLOT = cmax(cmax(cmax(RQ, RO), cmax(RP, RH)), cmax(RX, RX2));
DEVI u32 pga(int L, int blk) { return PGA + (u32)blk * CHUNK + (u32)L * 8192; }
DEVI u32 pgb(int L, int blk) { return PGB + (u32)blk * CHUNK + (u32)(1 - L) * 8192; }
static constexpr int B_QKV = 0, B_KVM = 96;
static constexpr int B_O = 0, B_H = 32, B_PA = 68, B_PF = 88, B_XN2 = 108, B_X1 = 113, B_XN = 118, B_X = 123;
static constexpr u32 OFF_XN0 = SLOT * (1 + NSLOT);
static constexpr u32 OFF_DBG = OFF_XN0 + 320 * CHUNK;
static constexpr u32 WS_END = OFF_DBG + 256u * 66 * 256;
DEVI u32 kb_off(int kb) { return (u32)((kb >> 3) * CHUNK + (kb & 7) * 1024); }
static constexpr u32 OOB = 0x80000000u;
static constexpr int LDS_BYTES = 163840;
static constexpr unsigned SPIN_LIMIT = 1u << 18;
static constexpr int SL_REC = 0, SL_P = 8, SL_ATT = 16, SL_WO = 18, SL_UP = 21, SL_DOWN = 27, SL_REDA = 30, SL_REDF = 31;
DEVI u32 mbox(int L, int j, int role, int slot) { return MBOX + (u32)(j * CHUNK + L * 8192 + role * 256 + 4 * slot); }
DEVI u32 mbox_att(int L, int j, int hl, int slot) { return mbox(L, j, SL_ATT + (hl >> 2), 16 * (hl & 3) + slot); }

struct Params {
  const u16* x; const u16* mem0; const u16* Wq; const u16* Wk; const u16* Wv; const u16* Wo;
  const u16* w1; const u16* w3; const u16* w2; u16* out; unsigned char* ws;
  int role_mask; int unused;
};

template <class F, int... I> DEVI void static_for_impl(F&& f, std::integer_sequence<int, I...>) { (f(std::integral_constant<int, I>{}), ...); }
template <int N, class F> DEVI void static_for(F&& f) { static_for_impl(f, std::make_integer_sequence<int, N>{}); }
DEVI float bflo(u32 d) { return __uint_as_float(d << 16); }
DEVI float bfhi(u32 d) { return __uint_as_float(d & 0xffff0000u); }
DEVI u32 pk2(float a, float b) {
  f32x2 f = {a, b};
  bf2_t r = __builtin_convertvector(f, bf2_t);
  return __builtin_bit_cast(u32, r);
}
DEVI bf16x8 as_frag(v4u v) { return __builtin_bit_cast(bf16x8, v); }
DEVI bf16x4 as_frag4(v2u v) { return __builtin_bit_cast(bf16x4, v); }
DEVI f32x4 mfma32(v4u a, v4u b, f32x4 c) { return __builtin_amdgcn_mfma_f32_16x16x32_bf16(as_frag(a), as_frag(b), c, 0, 0, 0); }
DEVI f32x4 mfma16(v2u a, v2u b, f32x4 c) { return __builtin_amdgcn_mfma_f32_16x16x16bf16_1k(as_frag4(a), as_frag4(b), c, 0, 0, 0); }
DEVI v4u ld16(rsrc_t r, u32 voff, u32 soff) { return __builtin_amdgcn_raw_buffer_load_b128(r, voff, soff, 16); }
DEVI v2u ld8(rsrc_t r, u32 voff, u32 soff) { return __builtin_amdgcn_raw_buffer_load_b64(r, voff, soff, 16); }
DEVI u32 ld4(rsrc_t r, u32 voff, u32 soff) { return __builtin_amdgcn_raw_buffer_load_b32(r, voff, soff, 16); }
DEVI void st16(rsrc_t r, v4u v, u32 voff, u32 soff) { __builtin_amdgcn_raw_buffer_store_b128(v, r, voff, soff, 16); }
DEVI void st8(rsrc_t r, v2u v, u32 voff, u32 soff) { __builtin_amdgcn_raw_buffer_store_b64(v, r, voff, soff, 16); }
DEVI void st4(rsrc_t r, u32 v, u32 voff, u32 soff) { __builtin_amdgcn_raw_buffer_store_b32(v, r, voff, soff, 16); }
DEVI void drain() { asm volatile("s_waitcnt vmcnt(0)" ::: "memory"); }
DEVI void cbar() { asm volatile("" ::: "memory"); }
DEVI void sbar() { __builtin_amdgcn_sched_barrier(0); }
DEVI u32 rfl(u32 v) { return (u32)__builtin_amdgcn_readfirstlane((int)v); }
DEVI float xmax16_32(float x) {
  u32 a = __float_as_uint(x);
  auto r = __builtin_amdgcn_permlane16_swap(a, a, false, false);
  x = fmaxf(__uint_as_float(r[0]), __uint_as_float(r[1]));
  a = __float_as_uint(x);
  auto q = __builtin_amdgcn_permlane32_swap(a, a, false, false);
  return fmaxf(__uint_as_float(q[0]), __uint_as_float(q[1]));
}
DEVI float xsum16_32(float x) {
  u32 a = __float_as_uint(x);
  auto r = __builtin_amdgcn_permlane16_swap(a, a, false, false);
  x = __uint_as_float(r[0]) + __uint_as_float(r[1]);
  a = __float_as_uint(x);
  auto q = __builtin_amdgcn_permlane32_swap(a, a, false, false);
  return __uint_as_float(q[0]) + __uint_as_float(q[1]);
}
DEVI float ror8(float x) { return __int_as_float(__builtin_amdgcn_update_dpp(0, __float_as_int(x), 0x128, 0xf, 0xf, false)); }
DEVI v4u swap16(v2u a, v2u b) {
  auto s0 = __builtin_amdgcn_permlane16_swap(a.x, b.x, false, false);
  auto s1 = __builtin_amdgcn_permlane16_swap(a.y, b.y, false, false);
  return (v4u){s0[0], s1[0], s0[1], s1[1]};
}

#define STAMP(row, slot) do { } while (0)

DEVI bool flags_ok(int v, int bias, int s) {
  asm volatile("" : "+v"(v));
  return __all(v + bias >= s);
}
template <int SLP> DEVI void wait_flags(rsrc_t r, u32 addr, int bias, int s, unsigned& budget) {
  for (;;) {
    cbar();
    const int v = (int)ld4(r, addr, 0);
    if (flags_ok(v, bias, s) || budget == 0) break;
    --budget;
    __builtin_amdgcn_s_sleep(SLP);
  }
  cbar();
}
DEVI void publish(rsrc_t r, u32 flag, int s, int lane) {
  drain();
  if (lane == 0) st4(r, (u32)s, flag, 0);
  cbar();
}
DEVI void delay_start(int us, unsigned long long t0) {
  if (us == 0) return;
  while (__builtin_amdgcn_s_memrealtime() - t0 < (unsigned long long)us * 100u) __builtin_amdgcn_s_sleep(16);
}
DEVI void pull_weights(const Params& p, int j, int n) {
  const int tid = threadIdx.x;
  u32 acc = 0;
  {
    const u16* w = n % 3 == 0 ? p.Wq : (n % 3 == 1 ? p.Wk : p.Wv);
    const rsrc_t r = __builtin_amdgcn_make_buffer_rsrc((void*)(w + j * 1024), 0, 576 * 8192 - j * 2048, 0x00020000);
    if (PSLOW) {
#pragma unroll
      for (int k = 0; k < 3; ++k) {
        const int row = (k * 4 + n / 3) * 64 + (tid >> 2);
        acc ^= __builtin_amdgcn_raw_buffer_load_b32(r, row < 576 ? (u32)(row * 8192 + 256 + (tid & 1) * 128 + (tid & 2) * 512) : OOB, 0, 0);
      }
    }
#pragma unroll
    for (int k = PSKIP; k < 9; ++k) {
      const int row = (k * 4 + n / 3) * 16 + (tid >> 4);
      acc ^= __builtin_amdgcn_raw_buffer_load_b32(r, (u32)(row * 8192 + (tid & 15) * 128), 0, 0);
    }
  }
  {
    const rsrc_t r = __builtin_amdgcn_make_buffer_rsrc((void*)(p.Wo + (size_t)j * 1024 * 576), 0, 1024 * 1152, 0x00020000);
#pragma unroll
    for (int k = 0; k < 3; ++k) acc ^= __builtin_amdgcn_raw_buffer_load_b32(r, (u32)((k * 12 + n) * 256 + tid) * 128, 0, 0);
  }
  asm volatile("" :: "v"(acc));
}
DEVI v4u make_srd(const void* p, u32 bytes) {
  const unsigned long long a = (unsigned long long)p;
  return (v4u){rfl((u32)a), rfl((u32)(a >> 32) & 0xffffu), bytes, 0x00020000u};
}
DEVI v16u cat4(v4u a, v4u b, v4u c, v4u d) {
  return (v16u){a.x, a.y, a.z, a.w, b.x, b.y, b.z, b.w, c.x, c.y, c.z, c.w, d.x, d.y, d.z, d.w};
}
DEVI u32 wswz(int j, int g) { return (u32)(4 * ((j >> 1) & 1) ^ 2 * (g & 1)); }
DEVI u32 tr_base(int lane, int chunk) {
  const int g = lane >> 4, q = (lane >> 2) & 3;
  return (u32)(g * 1024 + q * 128) + (((u32)chunk ^ wswz(q, g)) << 4);
}

#define RUN_ASM(TEXT) do { for (int i_ = 0; i_ < 16; ++i_) { sa[i_] = rfl(sa[i_]); sb[i_] = rfl(sb[i_]); } \
    asm volatile(TEXT "s_endpgm\n" :: "{s[36:51]}"(sa), "{s[52:67]}"(sb), "{v[0:15]}"(va) : "v255", "a255", "s99", "vcc", "memory"); \
    __builtin_unreachable(); } while (0)

#define GEMM_DECL v4u g_w0, g_w1, g_w2; u32 g_ldw, g_in_base, g_in_stride, g_in_mask, g_out_base, g_out_kb, g_out_mask, g_flag, g_paddr; int g_pbias, g_pinit = -1, g_n0 = 2, g_n1 = 2
#define GEMM_ARGS g_w0, g_w1, g_w2, g_ldw, g_in_base, g_in_stride, g_in_mask, g_out_base, g_out_kb, g_out_mask, g_flag, g_paddr, g_pbias, g_pinit, g_n0, g_n1
DEVI void gemm_args(v4u g_w0, v4u g_w1, v4u g_w2, u32 g_ldw, u32 g_in_base, u32 g_in_stride, u32 g_in_mask, u32 g_out_base, u32 g_out_kb,
                    u32 g_out_mask, u32 g_flag, u32 g_paddr, int g_pbias, int g_pinit, int g_n0, int g_n1, const Params& p, u32 dbg, unsigned long long tag, v16u& sa, v16u& sb, v16u& va) {
  const int tid = threadIdx.x, lane = tid & 63, w = (int)rfl(tid >> 6);
  const int c = lane & 15, gg = lane >> 4, pp = lane & 3;
  sa = cat4(make_srd(p.ws, WS_END), g_w0, g_w1, g_w2);
  sb = (v16u){g_in_base, g_in_stride, g_in_mask, g_out_base, SLOT, g_out_mask, 0, (u32)w * 1024, dbg + w * 64, 0, 0, 0x80000000u, 0, 0, (u32)(2 * tag), (u32)((2 * tag) >> 32)};
  va[0] = lane * 16;
  va[1] = lane * 16 + w * 2048;
  va[2] = lane == 63 ? OOB : g_paddr;
  va[3] = (u32)g_pbias;
  va[4] = (gg & 1) ? OOB : kb_off((int)g_out_kb + 8 + 2 * (w >> 1)) + (u32)(((2 * (w & 1) + (gg >> 1)) * 16 + c) * 16);
  va[5] = (u32)(8 * w + (lane >> 3)) * g_ldw + ((((u32)lane & 7) ^ wswz(lane >> 3, w)) << 4);
  va[6] = (u32)(w >> 1) * 4096 + tr_base(lane, 4 * (w & 1) + pp);
  va[7] = 2 * 4096 + tr_base(lane, 2 * w + (pp >> 1)) + 8 * (pp & 1);
  for (int i = 8; i < 16; ++i) va[i] = 0;
  va[8] = w < 2 ? va[1] : OOB;
  va[11] = (u32)g_pinit;
  va[14] = lane < g_n0 ? g_flag : OOB;
  va[10] = lane < g_n1 ? OOB : g_flag;
  va[15] = kb_off((int)g_out_kb + 2 * w) + lane * 16;
}

DEVI bool confirm(int& rid, unsigned long long& tag, bool& sure, unsigned char* smem) {
  if (sure) return true;
  asm volatile("s_waitcnt lgkmcnt(0)\n\ts_barrier" ::: "memory");
  const v4u boot = *(volatile v4u*)smem;
  const int r = (int)rfl(boot.x);
  tag = (unsigned long long)rfl(boot.y) | ((unsigned long long)rfl(boot.z) << 32);
  asm volatile("s_waitcnt lgkmcnt(0)\n\ts_barrier" ::: "memory");
  const bool ok = r == rid;
  rid = r; sure = true;
  return ok;
}
DEVI void role_p(const Params& p, int L, int j, int i, u32 dbg, int& rid, unsigned long long& tag, bool& sure, unsigned char* smem) {
  const int lane = threadIdx.x & 63, w = threadIdx.x >> 6, h = 8 * j + i;
  const u32 wb = 576u * 4096 * 2 - h * 256;
  GEMM_DECL;
  g_w0 = make_srd(p.Wq + h * 128, wb); g_w1 = make_srd(p.Wk + h * 128, wb); g_w2 = make_srd(p.Wv + h * 128, wb);
  g_ldw = 8192;
  g_in_base = L ? pgb(1, B_XN) : OFF_XN0; g_in_stride = L ? SLOT : 5 * CHUNK; g_in_mask = L ? RX2 - 1 : 63;
  g_out_base = pga(L, B_QKV + 3 * h); g_out_kb = 0; g_out_mask = RQ - 1;
  g_flag = lane == 0 ? mbox(L, j, SL_REC + i, w) : (lane == 1 ? mbox_att(L, j, i, 8 + w) : OOB);
  g_n0 = 0; g_n1 = 0;
  g_paddr = mbox(L, j, SL_P + i, lane);
  g_pbias = lane < (L ? 16 : 48) ? (lane >> 3) & 1 : (lane == 48 ? 2 * RQ : 1000);
  g_pinit = lane < 16 || lane >= 48 ? -1 : (lane < 32 ? 2 * LN_B - 1 : 2 * LN_C - 1);
  v16u sa, sb, va;
  gemm_args(GEMM_ARGS, p, dbg, tag, sa, sb, va);
  asm volatile("" : "+v"(va));
  if (!confirm(rid, tag, sure, smem)) return;
  sb[14] = (u32)(2 * tag); sb[15] = (u32)((2 * tag) >> 32);
  RUN_ASM(
    "s_nop 4\n"
    "s_getpc_b64 s[84:85]\n"
    ".LgP_PC_%=:\n"
    "s_and_b32 s85, s85, 0xffff\n"
    "s_mov_b32 s86, .LgP_FIN_%=-.LgP_PC_%=\n"
    "s_mov_b32 s87, 0x20000\n"
    "v_mbcnt_lo_u32_b32 v40, -1, 0\n"
    "v_mbcnt_hi_u32_b32 v40, -1, v40\n"
    "v_lshlrev_b32 v40, 7, v40\n"
    "buffer_load_dword v41, v40, s[84:87], 0 offen\n"
    "v_add_u32 v40, 8192, v40\n"
    "buffer_load_dword v41, v40, s[84:87], 0 offen\n"
    "v_add_u32 v40, 8192, v40\n"
    "buffer_load_dword v41, v40, s[84:87], 0 offen\n"
    "s_mov_b32 s68, 0\n"
    "s_mov_b32 s71, 0\n"
    "v_add_u32 v9, 20480, v0\n"
    "s_mov_b32 s64, 0xbfb8aa3b\n"
    "s_mov_b32 s72, 262144\n"
    "s_add_u32 m0, s59, 0\n"
    "s_mov_b32 s74, 0\n"
    "buffer_load_dwordx4 v5, s[40:43], s74 offen lds\n"
    "s_add_u32 m0, s59, 4096\n"
    "s_mov_b32 s74, 128\n"
    "buffer_load_dwordx4 v5, s[40:43], s74 offen lds\n"
    "s_add_u32 m0, s59, 8192\n"
    "s_mov_b32 s74, 0\n"
    "buffer_load_dwordx4 v5, s[44:47], s74 offen lds\n"
    "s_add_u32 m0, s59, 12288\n"
    "s_mov_b32 s74, 128\n"
    "buffer_load_dwordx4 v5, s[44:47], s74 offen lds\n"
    "s_add_u32 m0, s59, 16384\n"
    "s_mov_b32 s74, 0\n"
    "buffer_load_dwordx4 v5, s[48:51], s74 offen lds\n"
    "s_add_u32 m0, s59, 20480\n"
    "s_mov_b32 s74, 128\n"
    "buffer_load_dwordx4 v5, s[48:51], s74 offen lds\n"
    "s_add_u32 m0, s59, 24576\n"
    "s_mov_b32 s74, 262144\n"
    "buffer_load_dwordx4 v5, s[40:43], s74 offen lds\n"
    "s_add_u32 m0, s59, 28672\n"
    "s_mov_b32 s74, 262272\n"
    "buffer_load_dwordx4 v5, s[40:43], s74 offen lds\n"
    "s_add_u32 m0, s59, 32768\n"
    "s_mov_b32 s74, 262144\n"
    "buffer_load_dwordx4 v5, s[44:47], s74 offen lds\n"
    "s_add_u32 m0, s59, 36864\n"
    "s_mov_b32 s74, 262272\n"
    "buffer_load_dwordx4 v5, s[44:47], s74 offen lds\n"
    "s_add_u32 m0, s59, 40960\n"
    "s_mov_b32 s74, 262144\n"
    "buffer_load_dwordx4 v5, s[48:51], s74 offen lds\n"
    "s_add_u32 m0, s59, 45056\n"
    "s_mov_b32 s74, 262272\n"
    "buffer_load_dwordx4 v5, s[48:51], s74 offen lds\n"
    "s_add_u32 m0, s59, 49152\n"
    "s_mov_b32 s74, 524288\n"
    "buffer_load_dwordx4 v5, s[40:43], s74 offen lds\n"
    "s_add_u32 m0, s59, 53248\n"
    "s_mov_b32 s74, 524416\n"
    "buffer_load_dwordx4 v5, s[40:43], s74 offen lds\n"
    "s_add_u32 m0, s59, 57344\n"
    "s_mov_b32 s74, 524288\n"
    "buffer_load_dwordx4 v5, s[44:47], s74 offen lds\n"
    "s_add_u32 m0, s59, 61440\n"
    "s_mov_b32 s74, 524416\n"
    "buffer_load_dwordx4 v5, s[44:47], s74 offen lds\n"
    "s_add_u32 m0, s59, 65536\n"
    "s_mov_b32 s74, 524288\n"
    "buffer_load_dwordx4 v5, s[48:51], s74 offen lds\n"
    "s_add_u32 m0, s59, 69632\n"
    "s_mov_b32 s74, 524416\n"
    "buffer_load_dwordx4 v5, s[48:51], s74 offen lds\n"
    "s_add_u32 m0, s59, 73728\n"
    "s_mov_b32 s74, 786432\n"
    "buffer_load_dwordx4 v5, s[40:43], s74 offen lds\n"
    "s_add_u32 m0, s59, 77824\n"
    "s_mov_b32 s74, 786560\n"
    "buffer_load_dwordx4 v5, s[40:43], s74 offen lds\n"
    "s_add_u32 m0, s59, 81920\n"
    "s_mov_b32 s74, 786432\n"
    "buffer_load_dwordx4 v5, s[44:47], s74 offen lds\n"
    "s_add_u32 m0, s59, 86016\n"
    "s_mov_b32 s74, 786560\n"
    "buffer_load_dwordx4 v5, s[44:47], s74 offen lds\n"
    "s_add_u32 m0, s59, 90112\n"
    "s_mov_b32 s74, 786432\n"
    "buffer_load_dwordx4 v5, s[48:51], s74 offen lds\n"
    "s_add_u32 m0, s59, 94208\n"
    "s_mov_b32 s74, 786560\n"
    "buffer_load_dwordx4 v5, s[48:51], s74 offen lds\n"
    "s_add_u32 m0, s59, 98304\n"
    "s_mov_b32 s74, 1048576\n"
    "buffer_load_dwordx4 v5, s[40:43], s74 offen lds\n"
    "s_add_u32 m0, s59, 102400\n"
    "s_mov_b32 s74, 1048704\n"
    "buffer_load_dwordx4 v5, s[40:43], s74 offen lds\n"
    "s_add_u32 m0, s59, 106496\n"
    "s_mov_b32 s74, 1048576\n"
    "buffer_load_dwordx4 v5, s[44:47], s74 offen lds\n"
    "s_add_u32 m0, s59, 110592\n"
    "s_mov_b32 s74, 1048704\n"
    "buffer_load_dwordx4 v5, s[44:47], s74 offen lds\n"
    "s_add_u32 m0, s59, 114688\n"
    "s_mov_b32 s74, 1048576\n"
    "buffer_load_dwordx4 v5, s[48:51], s74 offen lds\n"
    "s_add_u32 m0, s59, 118784\n"
    "s_mov_b32 s74, 1048704\n"
    "buffer_load_dwordx4 v5, s[48:51], s74 offen lds\n"
    "v_add_u32 v16, 49152, v6\n"
    "v_add_u32 v17, 98304, v6\n"
    "v_add_u32 v18, 49152, v7\n"
    "v_add_u32 v19, 98304, v7\n"
    "s_waitcnt vmcnt(24) lgkmcnt(0)\n"
    "s_barrier\n"
    "s_add_u32 m0, s59, 122880\n"
    "s_mov_b32 s74, 1310720\n"
    "buffer_load_dwordx4 v5, s[40:43], s74 offen lds\n"
    "s_add_u32 m0, s59, 126976\n"
    "s_mov_b32 s74, 1310848\n"
    "buffer_load_dwordx4 v5, s[40:43], s74 offen lds\n"
    "s_add_u32 m0, s59, 131072\n"
    "s_mov_b32 s74, 1310720\n"
    "buffer_load_dwordx4 v5, s[44:47], s74 offen lds\n"
    "s_add_u32 m0, s59, 135168\n"
    "s_mov_b32 s74, 1310848\n"
    "buffer_load_dwordx4 v5, s[44:47], s74 offen lds\n"
    "s_add_u32 m0, s59, 139264\n"
    "s_mov_b32 s74, 1310720\n"
    "buffer_load_dwordx4 v5, s[48:51], s74 offen lds\n"
    "s_add_u32 m0, s59, 143360\n"
    "s_mov_b32 s74, 1310848\n"
    "buffer_load_dwordx4 v5, s[48:51], s74 offen lds\n"
    "ds_read_b64_tr_b16 a[0:1], v6 offset:8192\n"
    "ds_read_b64_tr_b16 a[2:3], v6 offset:8704\n"
    "ds_read_b64_tr_b16 a[4:5], v6 offset:8200\n"
    "ds_read_b64_tr_b16 a[6:7], v6 offset:8712\n"
    "ds_read_b64_tr_b16 a[8:9], v6 offset:16384\n"
    "ds_read_b64_tr_b16 a[10:11], v6 offset:16896\n"
    "ds_read_b64_tr_b16 a[12:13], v6 offset:16392\n"
    "ds_read_b64_tr_b16 a[14:15], v6 offset:16904\n"
    "ds_read_b64_tr_b16 a[16:17], v6 offset:0\n"
    "ds_read_b64_tr_b16 a[18:19], v6 offset:512\n"
    "ds_read_b64_tr_b16 a[20:21], v6 offset:8\n"
    "ds_read_b64_tr_b16 a[22:23], v6 offset:520\n"
    "s_waitcnt vmcnt(24) lgkmcnt(0)\n"
    "s_barrier\n"
    "s_add_u32 m0, s59, 0\n"
    "s_mov_b32 s74, 1572864\n"
    "buffer_load_dwordx4 v5, s[40:43], s74 offen lds\n"
    "s_add_u32 m0, s59, 4096\n"
    "s_mov_b32 s74, 1572992\n"
    "buffer_load_dwordx4 v5, s[40:43], s74 offen lds\n"
    "s_add_u32 m0, s59, 8192\n"
    "s_mov_b32 s74, 1572864\n"
    "buffer_load_dwordx4 v5, s[44:47], s74 offen lds\n"
    "s_add_u32 m0, s59, 12288\n"
    "s_mov_b32 s74, 1572992\n"
    "buffer_load_dwordx4 v5, s[44:47], s74 offen lds\n"
    "s_add_u32 m0, s59, 16384\n"
    "s_mov_b32 s74, 1572864\n"
    "buffer_load_dwordx4 v5, s[48:51], s74 offen lds\n"
    "s_add_u32 m0, s59, 20480\n"
    "s_mov_b32 s74, 1572992\n"
    "buffer_load_dwordx4 v5, s[48:51], s74 offen lds\n"
    "ds_read_b64_tr_b16 a[24:25], v6 offset:32768\n"
    "ds_read_b64_tr_b16 a[26:27], v6 offset:33280\n"
    "ds_read_b64_tr_b16 a[28:29], v6 offset:32776\n"
    "ds_read_b64_tr_b16 a[30:31], v6 offset:33288\n"
    "ds_read_b64_tr_b16 a[32:33], v6 offset:40960\n"
    "ds_read_b64_tr_b16 a[34:35], v6 offset:41472\n"
    "ds_read_b64_tr_b16 a[36:37], v6 offset:40968\n"
    "ds_read_b64_tr_b16 a[38:39], v6 offset:41480\n"
    "ds_read_b64_tr_b16 a[40:41], v6 offset:24576\n"
    "ds_read_b64_tr_b16 a[42:43], v6 offset:25088\n"
    "ds_read_b64_tr_b16 a[44:45], v6 offset:24584\n"
    "ds_read_b64_tr_b16 a[46:47], v6 offset:25096\n"
    "s_waitcnt vmcnt(24) lgkmcnt(0)\n"
    "s_barrier\n"
    "s_add_u32 m0, s59, 24576\n"
    "s_mov_b32 s74, 1835008\n"
    "buffer_load_dwordx4 v5, s[40:43], s74 offen lds\n"
    "s_add_u32 m0, s59, 28672\n"
    "s_mov_b32 s74, 1835136\n"
    "buffer_load_dwordx4 v5, s[40:43], s74 offen lds\n"
    "s_add_u32 m0, s59, 32768\n"
    "s_mov_b32 s74, 1835008\n"
    "buffer_load_dwordx4 v5, s[44:47], s74 offen lds\n"
    "s_add_u32 m0, s59, 36864\n"
    "s_mov_b32 s74, 1835136\n"
    "buffer_load_dwordx4 v5, s[44:47], s74 offen lds\n"
    "s_add_u32 m0, s59, 40960\n"
    "s_mov_b32 s74, 1835008\n"
    "buffer_load_dwordx4 v5, s[48:51], s74 offen lds\n"
    "s_add_u32 m0, s59, 45056\n"
    "s_mov_b32 s74, 1835136\n"
    "buffer_load_dwordx4 v5, s[48:51], s74 offen lds\n"
    "ds_read_b64_tr_b16 a[48:49], v16 offset:8192\n"
    "ds_read_b64_tr_b16 a[50:51], v16 offset:8704\n"
    "ds_read_b64_tr_b16 a[52:53], v16 offset:8200\n"
    "ds_read_b64_tr_b16 a[54:55], v16 offset:8712\n"
    "ds_read_b64_tr_b16 a[56:57], v16 offset:16384\n"
    "ds_read_b64_tr_b16 a[58:59], v16 offset:16896\n"
    "ds_read_b64_tr_b16 a[60:61], v16 offset:16392\n"
    "ds_read_b64_tr_b16 a[62:63], v16 offset:16904\n"
    "ds_read_b64_tr_b16 a[64:65], v16 offset:0\n"
    "ds_read_b64_tr_b16 a[66:67], v16 offset:512\n"
    "ds_read_b64_tr_b16 a[68:69], v16 offset:8\n"
    "ds_read_b64_tr_b16 a[70:71], v16 offset:520\n"
    "s_waitcnt vmcnt(24) lgkmcnt(0)\n"
    "s_barrier\n"
    "s_add_u32 m0, s59, 49152\n"
    "s_mov_b32 s74, 2097152\n"
    "buffer_load_dwordx4 v5, s[40:43], s74 offen lds\n"
    "s_add_u32 m0, s59, 53248\n"
    "s_mov_b32 s74, 2097280\n"
    "buffer_load_dwordx4 v5, s[40:43], s74 offen lds\n"
    "s_add_u32 m0, s59, 57344\n"
    "s_mov_b32 s74, 2097152\n"
    "buffer_load_dwordx4 v5, s[44:47], s74 offen lds\n"
    "s_add_u32 m0, s59, 61440\n"
    "s_mov_b32 s74, 2097280\n"
    "buffer_load_dwordx4 v5, s[44:47], s74 offen lds\n"
    "s_add_u32 m0, s59, 65536\n"
    "s_mov_b32 s74, 2097152\n"
    "buffer_load_dwordx4 v5, s[48:51], s74 offen lds\n"
    "s_add_u32 m0, s59, 69632\n"
    "s_mov_b32 s74, 2097280\n"
    "buffer_load_dwordx4 v5, s[48:51], s74 offen lds\n"
    "ds_read_b64_tr_b16 a[72:73], v16 offset:32768\n"
    "ds_read_b64_tr_b16 a[74:75], v16 offset:33280\n"
    "ds_read_b64_tr_b16 a[76:77], v16 offset:32776\n"
    "ds_read_b64_tr_b16 a[78:79], v16 offset:33288\n"
    "ds_read_b64_tr_b16 a[80:81], v16 offset:40960\n"
    "ds_read_b64_tr_b16 a[82:83], v16 offset:41472\n"
    "ds_read_b64_tr_b16 a[84:85], v16 offset:40968\n"
    "ds_read_b64_tr_b16 a[86:87], v16 offset:41480\n"
    "ds_read_b64_tr_b16 a[88:89], v16 offset:24576\n"
    "ds_read_b64_tr_b16 a[90:91], v16 offset:25088\n"
    "ds_read_b64_tr_b16 a[92:93], v16 offset:24584\n"
    "ds_read_b64_tr_b16 a[94:95], v16 offset:25096\n"
    "s_waitcnt vmcnt(24) lgkmcnt(0)\n"
    "s_barrier\n"
    "s_add_u32 m0, s59, 73728\n"
    "s_mov_b32 s74, 2359296\n"
    "buffer_load_dwordx4 v5, s[40:43], s74 offen lds\n"
    "s_add_u32 m0, s59, 77824\n"
    "s_mov_b32 s74, 2359424\n"
    "buffer_load_dwordx4 v5, s[40:43], s74 offen lds\n"
    "s_add_u32 m0, s59, 81920\n"
    "s_mov_b32 s74, 2359296\n"
    "buffer_load_dwordx4 v5, s[44:47], s74 offen lds\n"
    "s_add_u32 m0, s59, 86016\n"
    "s_mov_b32 s74, 2359424\n"
    "buffer_load_dwordx4 v5, s[44:47], s74 offen lds\n"
    "s_add_u32 m0, s59, 90112\n"
    "s_mov_b32 s74, 2359296\n"
    "buffer_load_dwordx4 v5, s[48:51], s74 offen lds\n"
    "s_add_u32 m0, s59, 94208\n"
    "s_mov_b32 s74, 2359424\n"
    "buffer_load_dwordx4 v5, s[48:51], s74 offen lds\n"
    "ds_read_b64_tr_b16 a[96:97], v17 offset:8192\n"
    "ds_read_b64_tr_b16 a[98:99], v17 offset:8704\n"
    "ds_read_b64_tr_b16 a[100:101], v17 offset:8200\n"
    "ds_read_b64_tr_b16 a[102:103], v17 offset:8712\n"
    "ds_read_b64_tr_b16 a[104:105], v17 offset:16384\n"
    "ds_read_b64_tr_b16 a[106:107], v17 offset:16896\n"
    "ds_read_b64_tr_b16 a[108:109], v17 offset:16392\n"
    "ds_read_b64_tr_b16 a[110:111], v17 offset:16904\n"
    "ds_read_b64_tr_b16 a[112:113], v17 offset:0\n"
    "ds_read_b64_tr_b16 a[114:115], v17 offset:512\n"
    "ds_read_b64_tr_b16 a[116:117], v17 offset:8\n"
    "ds_read_b64_tr_b16 a[118:119], v17 offset:520\n"
    "s_waitcnt vmcnt(24) lgkmcnt(0)\n"
    "s_barrier\n"
    "s_add_u32 m0, s59, 98304\n"
    "s_mov_b32 s74, 2621440\n"
    "buffer_load_dwordx4 v5, s[40:43], s74 offen lds\n"
    "s_add_u32 m0, s59, 102400\n"
    "s_mov_b32 s74, 2621568\n"
    "buffer_load_dwordx4 v5, s[40:43], s74 offen lds\n"
    "s_add_u32 m0, s59, 106496\n"
    "s_mov_b32 s74, 2621440\n"
    "buffer_load_dwordx4 v5, s[44:47], s74 offen lds\n"
    "s_add_u32 m0, s59, 110592\n"
    "s_mov_b32 s74, 2621568\n"
    "buffer_load_dwordx4 v5, s[44:47], s74 offen lds\n"
    "s_add_u32 m0, s59, 114688\n"
    "s_mov_b32 s74, 2621440\n"
    "buffer_load_dwordx4 v5, s[48:51], s74 offen lds\n"
    "s_add_u32 m0, s59, 118784\n"
    "s_mov_b32 s74, 2621568\n"
    "buffer_load_dwordx4 v5, s[48:51], s74 offen lds\n"
    "ds_read_b64_tr_b16 a[120:121], v17 offset:32768\n"
    "ds_read_b64_tr_b16 a[122:123], v17 offset:33280\n"
    "ds_read_b64_tr_b16 a[124:125], v17 offset:32776\n"
    "ds_read_b64_tr_b16 a[126:127], v17 offset:33288\n"
    "ds_read_b64_tr_b16 a[128:129], v17 offset:40960\n"
    "ds_read_b64_tr_b16 a[130:131], v17 offset:41472\n"
    "ds_read_b64_tr_b16 a[132:133], v17 offset:40968\n"
    "ds_read_b64_tr_b16 a[134:135], v17 offset:41480\n"
    "ds_read_b64_tr_b16 a[136:137], v17 offset:24576\n"
    "ds_read_b64_tr_b16 a[138:139], v17 offset:25088\n"
    "ds_read_b64_tr_b16 a[140:141], v17 offset:24584\n"
    "ds_read_b64_tr_b16 a[142:143], v17 offset:25096\n"
    "s_waitcnt vmcnt(24) lgkmcnt(0)\n"
    "s_barrier\n"
    "s_add_u32 m0, s59, 122880\n"
    "s_mov_b32 s74, 2883584\n"
    "buffer_load_dwordx4 v5, s[40:43], s74 offen lds\n"
    "s_add_u32 m0, s59, 126976\n"
    "s_mov_b32 s74, 2883712\n"
    "buffer_load_dwordx4 v5, s[40:43], s74 offen lds\n"
    "s_add_u32 m0, s59, 131072\n"
    "s_mov_b32 s74, 2883584\n"
    "buffer_load_dwordx4 v5, s[44:47], s74 offen lds\n"
    "s_add_u32 m0, s59, 135168\n"
    "s_mov_b32 s74, 2883712\n"
    "buffer_load_dwordx4 v5, s[44:47], s74 offen lds\n"
    "s_add_u32 m0, s59, 139264\n"
    "s_mov_b32 s74, 2883584\n"
    "buffer_load_dwordx4 v5, s[48:51], s74 offen lds\n"
    "s_add_u32 m0, s59, 143360\n"
    "s_mov_b32 s74, 2883712\n"
    "buffer_load_dwordx4 v5, s[48:51], s74 offen lds\n"
    "ds_read_b64_tr_b16 a[144:145], v6 offset:8192\n"
    "ds_read_b64_tr_b16 a[146:147], v6 offset:8704\n"
    "ds_read_b64_tr_b16 a[148:149], v6 offset:8200\n"
    "ds_read_b64_tr_b16 a[150:151], v6 offset:8712\n"
    "ds_read_b64_tr_b16 a[152:153], v6 offset:16384\n"
    "ds_read_b64_tr_b16 a[154:155], v6 offset:16896\n"
    "ds_read_b64_tr_b16 a[156:157], v6 offset:16392\n"
    "ds_read_b64_tr_b16 a[158:159], v6 offset:16904\n"
    "ds_read_b64_tr_b16 a[160:161], v6 offset:0\n"
    "ds_read_b64_tr_b16 a[162:163], v6 offset:512\n"
    "ds_read_b64_tr_b16 a[164:165], v6 offset:8\n"
    "ds_read_b64_tr_b16 a[166:167], v6 offset:520\n"
    "s_waitcnt vmcnt(24) lgkmcnt(0)\n"
    "s_barrier\n"
    "s_add_u32 m0, s59, 0\n"
    "s_mov_b32 s74, 3145728\n"
    "buffer_load_dwordx4 v5, s[40:43], s74 offen lds\n"
    "s_add_u32 m0, s59, 4096\n"
    "s_mov_b32 s74, 3145856\n"
    "buffer_load_dwordx4 v5, s[40:43], s74 offen lds\n"
    "s_add_u32 m0, s59, 8192\n"
    "s_mov_b32 s74, 3145728\n"
    "buffer_load_dwordx4 v5, s[44:47], s74 offen lds\n"
    "s_add_u32 m0, s59, 12288\n"
    "s_mov_b32 s74, 3145856\n"
    "buffer_load_dwordx4 v5, s[44:47], s74 offen lds\n"
    "s_add_u32 m0, s59, 16384\n"
    "s_mov_b32 s74, 3145728\n"
    "buffer_load_dwordx4 v5, s[48:51], s74 offen lds\n"
    "s_add_u32 m0, s59, 20480\n"
    "s_mov_b32 s74, 3145856\n"
    "buffer_load_dwordx4 v5, s[48:51], s74 offen lds\n"
    "ds_read_b64_tr_b16 a[168:169], v6 offset:32768\n"
    "ds_read_b64_tr_b16 a[170:171], v6 offset:33280\n"
    "ds_read_b64_tr_b16 a[172:173], v6 offset:32776\n"
    "ds_read_b64_tr_b16 a[174:175], v6 offset:33288\n"
    "ds_read_b64_tr_b16 a[176:177], v6 offset:40960\n"
    "ds_read_b64_tr_b16 a[178:179], v6 offset:41472\n"
    "ds_read_b64_tr_b16 a[180:181], v6 offset:40968\n"
    "ds_read_b64_tr_b16 a[182:183], v6 offset:41480\n"
    "ds_read_b64_tr_b16 a[184:185], v6 offset:24576\n"
    "ds_read_b64_tr_b16 a[186:187], v6 offset:25088\n"
    "ds_read_b64_tr_b16 a[188:189], v6 offset:24584\n"
    "ds_read_b64_tr_b16 a[190:191], v6 offset:25096\n"
    "s_waitcnt vmcnt(24) lgkmcnt(0)\n"
    "s_barrier\n"
    "s_add_u32 m0, s59, 24576\n"
    "s_mov_b32 s74, 3407872\n"
    "buffer_load_dwordx4 v5, s[40:43], s74 offen lds\n"
    "s_add_u32 m0, s59, 28672\n"
    "s_mov_b32 s74, 3408000\n"
    "buffer_load_dwordx4 v5, s[40:43], s74 offen lds\n"
    "s_add_u32 m0, s59, 32768\n"
    "s_mov_b32 s74, 3407872\n"
    "buffer_load_dwordx4 v5, s[44:47], s74 offen lds\n"
    "s_add_u32 m0, s59, 36864\n"
    "s_mov_b32 s74, 3408000\n"
    "buffer_load_dwordx4 v5, s[44:47], s74 offen lds\n"
    "s_add_u32 m0, s59, 40960\n"
    "s_mov_b32 s74, 3407872\n"
    "buffer_load_dwordx4 v5, s[48:51], s74 offen lds\n"
    "s_add_u32 m0, s59, 45056\n"
    "s_mov_b32 s74, 3408000\n"
    "buffer_load_dwordx4 v5, s[48:51], s74 offen lds\n"
    "ds_read_b64_tr_b16 a[192:193], v16 offset:8192\n"
    "ds_read_b64_tr_b16 a[194:195], v16 offset:8704\n"
    "ds_read_b64_tr_b16 a[196:197], v16 offset:8200\n"
    "ds_read_b64_tr_b16 a[198:199], v16 offset:8712\n"
    "ds_read_b64_tr_b16 a[200:201], v16 offset:16384\n"
    "ds_read_b64_tr_b16 a[202:203], v16 offset:16896\n"
    "ds_read_b64_tr_b16 a[204:205], v16 offset:16392\n"
    "ds_read_b64_tr_b16 a[206:207], v16 offset:16904\n"
    "ds_read_b64_tr_b16 a[208:209], v16 offset:0\n"
    "ds_read_b64_tr_b16 a[210:211], v16 offset:512\n"
    "ds_read_b64_tr_b16 a[212:213], v16 offset:8\n"
    "ds_read_b64_tr_b16 a[214:215], v16 offset:520\n"
    "s_waitcnt vmcnt(24) lgkmcnt(0)\n"
    "s_barrier\n"
    "s_add_u32 m0, s59, 49152\n"
    "s_mov_b32 s74, 3670016\n"
    "buffer_load_dwordx4 v5, s[40:43], s74 offen lds\n"
    "s_add_u32 m0, s59, 53248\n"
    "s_mov_b32 s74, 3670144\n"
    "buffer_load_dwordx4 v5, s[40:43], s74 offen lds\n"
    "s_add_u32 m0, s59, 57344\n"
    "s_mov_b32 s74, 3670016\n"
    "buffer_load_dwordx4 v5, s[44:47], s74 offen lds\n"
    "s_add_u32 m0, s59, 61440\n"
    "s_mov_b32 s74, 3670144\n"
    "buffer_load_dwordx4 v5, s[44:47], s74 offen lds\n"
    "s_add_u32 m0, s59, 65536\n"
    "s_mov_b32 s74, 3670016\n"
    "buffer_load_dwordx4 v5, s[48:51], s74 offen lds\n"
    "s_add_u32 m0, s59, 69632\n"
    "s_mov_b32 s74, 3670144\n"
    "buffer_load_dwordx4 v5, s[48:51], s74 offen lds\n"
    "ds_read_b64_tr_b16 a[216:217], v16 offset:32768\n"
    "ds_read_b64_tr_b16 a[218:219], v16 offset:33280\n"
    "ds_read_b64_tr_b16 a[220:221], v16 offset:32776\n"
    "ds_read_b64_tr_b16 a[222:223], v16 offset:33288\n"
    "ds_read_b64_tr_b16 a[224:225], v16 offset:40960\n"
    "ds_read_b64_tr_b16 a[226:227], v16 offset:41472\n"
    "ds_read_b64_tr_b16 a[228:229], v16 offset:40968\n"
    "ds_read_b64_tr_b16 a[230:231], v16 offset:41480\n"
    "ds_read_b64_tr_b16 a[232:233], v16 offset:24576\n"
    "ds_read_b64_tr_b16 a[234:235], v16 offset:25088\n"
    "ds_read_b64_tr_b16 a[236:237], v16 offset:24584\n"
    "ds_read_b64_tr_b16 a[238:239], v16 offset:25096\n"
    "s_waitcnt vmcnt(24) lgkmcnt(0)\n"
    "s_barrier\n"
    "s_add_u32 m0, s59, 73728\n"
    "s_mov_b32 s74, 3932160\n"
    "buffer_load_dwordx4 v5, s[40:43], s74 offen lds\n"
    "s_add_u32 m0, s59, 77824\n"
    "s_mov_b32 s74, 3932288\n"
    "buffer_load_dwordx4 v5, s[40:43], s74 offen lds\n"
    "s_add_u32 m0, s59, 81920\n"
    "s_mov_b32 s74, 3932160\n"
    "buffer_load_dwordx4 v5, s[44:47], s74 offen lds\n"
    "s_add_u32 m0, s59, 86016\n"
    "s_mov_b32 s74, 3932288\n"
    "buffer_load_dwordx4 v5, s[44:47], s74 offen lds\n"
    "s_add_u32 m0, s59, 90112\n"
    "s_mov_b32 s74, 3932160\n"
    "buffer_load_dwordx4 v5, s[48:51], s74 offen lds\n"
    "s_add_u32 m0, s59, 94208\n"
    "s_mov_b32 s74, 3932288\n"
    "buffer_load_dwordx4 v5, s[48:51], s74 offen lds\n"
    "ds_read_b64_tr_b16 a[240:241], v17 offset:8192\n"
    "ds_read_b64_tr_b16 a[242:243], v17 offset:8704\n"
    "ds_read_b64_tr_b16 a[244:245], v17 offset:8200\n"
    "ds_read_b64_tr_b16 a[246:247], v17 offset:8712\n"
    "ds_read_b64_tr_b16 a[248:249], v17 offset:16384\n"
    "ds_read_b64_tr_b16 a[250:251], v17 offset:16896\n"
    "ds_read_b64_tr_b16 a[252:253], v17 offset:16392\n"
    "ds_read_b64_tr_b16 a[254:255], v17 offset:16904\n"
    "ds_read_b64_tr_b16 v[80:81], v17 offset:0\n"
    "ds_read_b64_tr_b16 v[82:83], v17 offset:512\n"
    "ds_read_b64_tr_b16 v[84:85], v17 offset:8\n"
    "ds_read_b64_tr_b16 v[86:87], v17 offset:520\n"
    "s_waitcnt vmcnt(24) lgkmcnt(0)\n"
    "s_barrier\n"
    "s_add_u32 m0, s59, 98304\n"
    "s_mov_b32 s74, 4194304\n"
    "buffer_load_dwordx4 v5, s[40:43], s74 offen lds\n"
    "s_add_u32 m0, s59, 102400\n"
    "s_mov_b32 s74, 4194432\n"
    "buffer_load_dwordx4 v5, s[40:43], s74 offen lds\n"
    "s_add_u32 m0, s59, 106496\n"
    "s_mov_b32 s74, 4194304\n"
    "buffer_load_dwordx4 v5, s[44:47], s74 offen lds\n"
    "s_add_u32 m0, s59, 110592\n"
    "s_mov_b32 s74, 4194432\n"
    "buffer_load_dwordx4 v5, s[44:47], s74 offen lds\n"
    "s_add_u32 m0, s59, 114688\n"
    "s_mov_b32 s74, 4194304\n"
    "buffer_load_dwordx4 v5, s[48:51], s74 offen lds\n"
    "s_add_u32 m0, s59, 118784\n"
    "s_mov_b32 s74, 4194432\n"
    "buffer_load_dwordx4 v5, s[48:51], s74 offen lds\n"
    "ds_read_b64_tr_b16 v[88:89], v17 offset:32768\n"
    "ds_read_b64_tr_b16 v[90:91], v17 offset:33280\n"
    "ds_read_b64_tr_b16 v[92:93], v17 offset:32776\n"
    "ds_read_b64_tr_b16 v[94:95], v17 offset:33288\n"
    "ds_read_b64_tr_b16 v[96:97], v17 offset:40960\n"
    "ds_read_b64_tr_b16 v[98:99], v17 offset:41472\n"
    "ds_read_b64_tr_b16 v[100:101], v17 offset:40968\n"
    "ds_read_b64_tr_b16 v[102:103], v17 offset:41480\n"
    "ds_read_b64_tr_b16 v[104:105], v17 offset:24576\n"
    "ds_read_b64_tr_b16 v[106:107], v17 offset:25088\n"
    "ds_read_b64_tr_b16 v[108:109], v17 offset:24584\n"
    "ds_read_b64_tr_b16 v[110:111], v17 offset:25096\n"
    "s_waitcnt vmcnt(24) lgkmcnt(0)\n"
    "s_barrier\n"
    "s_add_u32 m0, s59, 122880\n"
    "s_mov_b32 s74, 4456448\n"
    "buffer_load_dwordx4 v5, s[40:43], s74 offen lds\n"
    "s_add_u32 m0, s59, 126976\n"
    "s_mov_b32 s74, 4456576\n"
    "buffer_load_dwordx4 v5, s[40:43], s74 offen lds\n"
    "s_add_u32 m0, s59, 131072\n"
    "s_mov_b32 s74, 4456448\n"
    "buffer_load_dwordx4 v5, s[44:47], s74 offen lds\n"
    "s_add_u32 m0, s59, 135168\n"
    "s_mov_b32 s74, 4456576\n"
    "buffer_load_dwordx4 v5, s[44:47], s74 offen lds\n"
    "s_add_u32 m0, s59, 139264\n"
    "s_mov_b32 s74, 4456448\n"
    "buffer_load_dwordx4 v5, s[48:51], s74 offen lds\n"
    "s_add_u32 m0, s59, 143360\n"
    "s_mov_b32 s74, 4456576\n"
    "buffer_load_dwordx4 v5, s[48:51], s74 offen lds\n"
    "buffer_load_dwordx2 v[12:13], off, s[36:39], 0 sc1\n"
    "ds_read_b64_tr_b16 v[112:113], v6 offset:8192\n"
    "ds_read_b64_tr_b16 v[114:115], v6 offset:8704\n"
    "ds_read_b64_tr_b16 v[116:117], v6 offset:8200\n"
    "ds_read_b64_tr_b16 v[118:119], v6 offset:8712\n"
    "ds_read_b64_tr_b16 v[120:121], v6 offset:16384\n"
    "ds_read_b64_tr_b16 v[122:123], v6 offset:16896\n"
    "ds_read_b64_tr_b16 v[124:125], v6 offset:16392\n"
    "ds_read_b64_tr_b16 v[126:127], v6 offset:16904\n"
    "ds_read_b64_tr_b16 v[128:129], v6 offset:0\n"
    "ds_read_b64_tr_b16 v[130:131], v6 offset:512\n"
    "ds_read_b64_tr_b16 v[132:133], v6 offset:8\n"
    "ds_read_b64_tr_b16 v[134:135], v6 offset:520\n"
    "s_waitcnt vmcnt(25) lgkmcnt(0)\n"
    "s_barrier\n"
    "ds_read_b64_tr_b16 v[136:137], v6 offset:32768\n"
    "ds_read_b64_tr_b16 v[138:139], v6 offset:33280\n"
    "ds_read_b64_tr_b16 v[140:141], v6 offset:32776\n"
    "ds_read_b64_tr_b16 v[142:143], v6 offset:33288\n"
    "ds_read_b64_tr_b16 v[144:145], v6 offset:40960\n"
    "ds_read_b64_tr_b16 v[146:147], v6 offset:41472\n"
    "ds_read_b64_tr_b16 v[148:149], v6 offset:40968\n"
    "ds_read_b64_tr_b16 v[150:151], v6 offset:41480\n"
    "ds_read_b64_tr_b16 v[152:153], v6 offset:24576\n"
    "ds_read_b64_tr_b16 v[154:155], v6 offset:25088\n"
    "ds_read_b64_tr_b16 v[156:157], v6 offset:24584\n"
    "ds_read_b64_tr_b16 v[158:159], v6 offset:25096\n"
    "s_waitcnt vmcnt(19) lgkmcnt(0)\n"
    "s_barrier\n"
    "ds_read_b64_tr_b16 v[160:161], v16 offset:8192\n"
    "ds_read_b64_tr_b16 v[162:163], v16 offset:8704\n"
    "ds_read_b64_tr_b16 v[164:165], v16 offset:8200\n"
    "ds_read_b64_tr_b16 v[166:167], v16 offset:8712\n"
    "ds_read_b64_tr_b16 v[168:169], v16 offset:16384\n"
    "ds_read_b64_tr_b16 v[170:171], v16 offset:16896\n"
    "ds_read_b64_tr_b16 v[172:173], v16 offset:16392\n"
    "ds_read_b64_tr_b16 v[174:175], v16 offset:16904\n"
    "ds_read_b64_tr_b16 v[176:177], v16 offset:0\n"
    "ds_read_b64_tr_b16 v[178:179], v16 offset:512\n"
    "ds_read_b64_tr_b16 v[180:181], v16 offset:8\n"
    "ds_read_b64_tr_b16 v[182:183], v16 offset:520\n"
    "s_waitcnt vmcnt(13) lgkmcnt(0)\n"
    "s_barrier\n"
    "ds_read_b64_tr_b16 v[184:185], v16 offset:32768\n"
    "ds_read_b64_tr_b16 v[186:187], v16 offset:33280\n"
    "ds_read_b64_tr_b16 v[188:189], v16 offset:32776\n"
    "ds_read_b64_tr_b16 v[190:191], v16 offset:33288\n"
    "ds_read_b64_tr_b16 v[192:193], v16 offset:40960\n"
    "ds_read_b64_tr_b16 v[194:195], v16 offset:41472\n"
    "ds_read_b64_tr_b16 v[196:197], v16 offset:40968\n"
    "ds_read_b64_tr_b16 v[198:199], v16 offset:41480\n"
    "ds_read_b64_tr_b16 v[200:201], v16 offset:24576\n"
    "ds_read_b64_tr_b16 v[202:203], v16 offset:25088\n"
    "ds_read_b64_tr_b16 v[204:205], v16 offset:24584\n"
    "ds_read_b64_tr_b16 v[206:207], v16 offset:25096\n"
    "s_waitcnt vmcnt(7) lgkmcnt(0)\n"
    "s_barrier\n"
    "ds_read_b64_tr_b16 v[208:209], v17 offset:8192\n"
    "ds_read_b64_tr_b16 v[210:211], v17 offset:8704\n"
    "ds_read_b64_tr_b16 v[212:213], v17 offset:8200\n"
    "ds_read_b64_tr_b16 v[214:215], v17 offset:8712\n"
    "ds_read_b64_tr_b16 v[216:217], v17 offset:16384\n"
    "ds_read_b64_tr_b16 v[218:219], v17 offset:16896\n"
    "ds_read_b64_tr_b16 v[220:221], v17 offset:16392\n"
    "ds_read_b64_tr_b16 v[222:223], v17 offset:16904\n"
    "ds_read_b64_tr_b16 v[224:225], v17 offset:0\n"
    "ds_read_b64_tr_b16 v[226:227], v17 offset:512\n"
    "ds_read_b64_tr_b16 v[228:229], v17 offset:8\n"
    "ds_read_b64_tr_b16 v[230:231], v17 offset:520\n"
    "s_waitcnt vmcnt(0) lgkmcnt(0)\n"
    "s_barrier\n"
    ".LgP_par_%=:\n"
    "v_readfirstlane_b32 s74, v12\n"
    "v_readfirstlane_b32 s75, v13\n"
    "s_xor_b32 s74, s74, s66\n"
    "s_xor_b32 s75, s75, s67\n"
    "s_cselect_b32 s75, 2, 0\n"
    "s_or_b32 s75, s75, s74\n"
    "s_cmp_lt_u32 s75, 2\n"
    "s_cbranch_scc1 .LgP_par1_%=\n"
    "s_sleep 4\n"
    "buffer_load_dwordx2 v[12:13], off, s[36:39], 0 sc1\n"
    "s_waitcnt vmcnt(0)\n"
    "s_sub_u32 s72, s72, 1\n"
    "s_cbranch_scc0 .LgP_par_%=\n"
    "s_mov_b32 s72, 0\n"
    ".LgP_par1_%=:\n"
    "s_and_b32 s74, s74, 1\n"
    "s_lshl_b32 s74, s74, 13\n"
    "s_add_u32 s36, s36, s74\n"
    "s_addc_u32 s37, s37, 0\n"
    "v_add_u32 v13, v11, v3\n"
    "v_cmp_gt_i32 vcc, s71, v13\n"
    "s_nop 0\n"
    "s_or_b64 exec, vcc, s[62:63]\n"
    "buffer_atomic_smax v11, v2, s[36:39], 0 offen sc0 sc1\n"
    "s_mov_b64 exec, -1\n"
    "s_getpc_b64 s[84:85]\n"
    ".LgP_PC2_%=:\n"
    "s_and_b32 s85, s85, 0xffff\n"
    "s_mov_b32 s86, .LgP_FIN_%=-.LgP_PC2_%=\n"
    "s_mov_b32 s87, 0x20000\n"
    "v_mbcnt_lo_u32_b32 v42, -1, 0\n"
    "v_mbcnt_hi_u32_b32 v42, -1, v42\n"
    "v_lshlrev_b32 v42, 7, v42\n"
    "buffer_load_dword v43, v42, s[84:87], 0 offen\n"
    "v_add_u32 v42, 8192, v42\n"
    "buffer_load_dword v43, v42, s[84:87], 0 offen\n"
    "v_add_u32 v42, 8192, v42\n"
    "buffer_load_dword v43, v42, s[84:87], 0 offen\n"
    "ds_read_b64_tr_b16 v[232:233], v17 offset:32768\n"
    "ds_read_b64_tr_b16 v[234:235], v17 offset:33280\n"
    "ds_read_b64_tr_b16 v[236:237], v17 offset:32776\n"
    "ds_read_b64_tr_b16 v[238:239], v17 offset:33288\n"
    "ds_read_b64_tr_b16 v[240:241], v17 offset:40960\n"
    "ds_read_b64_tr_b16 v[242:243], v17 offset:41472\n"
    "ds_read_b64_tr_b16 v[244:245], v17 offset:40968\n"
    "ds_read_b64_tr_b16 v[246:247], v17 offset:41480\n"
    "ds_read_b64_tr_b16 v[248:249], v17 offset:24576\n"
    "ds_read_b64_tr_b16 v[250:251], v17 offset:25088\n"
    "ds_read_b64_tr_b16 v[252:253], v17 offset:24584\n"
    "ds_read_b64_tr_b16 v[254:255], v17 offset:25096\n"
    "s_waitcnt lgkmcnt(0)\n"
    "s_barrier\n"
    "s_waitcnt vmcnt(0)\n"
    "v_mov_b32 v5, v11\n"
    "v_mov_b32 v12, -1\n"
    "s_mov_b32 s73, 0x80000000\n"
    "s_mov_b32 s79, 0x80000000\n"
    "s_mov_b32 s80, 0x80000000\n"
    "s_branch .LgP_block1_1_%=\n"
    ".LgP_LOOPB_%=:\n"
    ".LgP_head0_%=:\n"
    "v_mfma_f32_16x16x32_bf16 v[32:35], a[0:3], v[16:19], 0\n"
    "v_mfma_f32_16x16x32_bf16 v[36:39], a[4:7], v[16:19], 0\n"
    "v_cvt_pk_bf16_f32 v40, v40, v41\n"
    "v_cvt_pk_bf16_f32 v41, v42, v43\n"
    "v_mfma_f32_16x16x32_bf16 v[48:51], v[16:19], a[8:11], 0\n"
    "v_cvt_pk_bf16_f32 v42, v44, v45\n"
    "v_cvt_pk_bf16_f32 v43, v46, v47\n"
    "v_mfma_f32_16x16x32_bf16 v[56:59], v[16:19], a[12:15], 0\n"
    "buffer_store_dwordx4 v[40:43], v1, s[36:39], s79 offen offset:1024 sc1\n"
    "v_mfma_f32_16x16x32_bf16 v[64:67], a[16:19], v[16:19], 0\n"
    "s_waitcnt lgkmcnt(1)\n"
    "v_cvt_pk_bf16_f32 v55, v54, v55\n"
    "v_cvt_pk_bf16_f32 v54, v52, v53\n"
    "v_mfma_f32_16x16x32_bf16 v[68:71], a[20:23], v[16:19], 0\n"
    "ds_read_b128 v[16:19], v0 offset:3072\n"
    "v_mov_b64 v[52:53], v[28:29]\n"
    "v_mfma_f32_16x16x32_bf16 v[32:35], a[24:27], v[20:23], v[32:35]\n"
    "buffer_store_dwordx4 v[52:55], v1, s[36:39], s80 offen sc1\n"
    "v_mfma_f32_16x16x32_bf16 v[36:39], a[28:31], v[20:23], v[36:39]\n"
    "s_waitcnt vmcnt(3)\n"
    "v_cvt_pk_bf16_f32 v63, v62, v63\n"
    "v_add_u32 v13, v11, v3\n"
    "v_mfma_f32_16x16x32_bf16 v[48:51], v[20:23], a[32:35], v[48:51]\n"
    "v_cvt_pk_bf16_f32 v62, v60, v61\n"
    "v_cmp_gt_i32 vcc, s71, v13\n"
    "v_mfma_f32_16x16x32_bf16 v[56:59], v[20:23], a[36:39], v[56:59]\n"
    "buffer_atomic_smax v7, v10, s[36:39], s69 offen sc1\n"
    "v_mov_b64 v[60:61], v[30:31]\n"
    "v_mfma_f32_16x16x32_bf16 v[64:67], a[40:43], v[20:23], v[64:67]\n"
    "s_waitcnt lgkmcnt(1)\n"
    "v_cvt_pk_bf16_f32 v72, v72, v73\n"
    "v_cvt_pk_bf16_f32 v73, v74, v75\n"
    "v_mfma_f32_16x16x32_bf16 v[68:71], a[44:47], v[20:23], v[68:71]\n"
    "ds_read_b128 v[20:23], v0 offset:4096\n"
    "s_cbranch_vccnz .LgP_poll0_%=\n"
    "v_mfma_f32_16x16x32_bf16 v[32:35], a[48:51], v[24:27], v[32:35]\n"
    "s_add_u32 m0, s59, 20480\n"
    "v_mfma_f32_16x16x32_bf16 v[36:39], a[52:55], v[24:27], v[36:39]\n"
    "buffer_store_dwordx4 v[60:63], v1, s[36:39], s80 offen offset:1024 sc1\n"
    "v_mfma_f32_16x16x32_bf16 v[48:51], v[24:27], a[56:59], v[48:51]\n"
    "buffer_load_dwordx4 v1, s[36:39], s76 offen sc1 lds\n"
    "v_cvt_pk_bf16_f32 v74, v76, v77\n"
    "v_cvt_pk_bf16_f32 v75, v78, v79\n"
    "v_mfma_f32_16x16x32_bf16 v[56:59], v[24:27], a[60:63], v[56:59]\n"
    "v_mfma_f32_16x16x32_bf16 v[64:67], a[64:67], v[24:27], v[64:67]\n"
    "s_waitcnt lgkmcnt(1)\n"
    "s_add_u32 s76, s76, 16384\n"
    "s_add_u32 m0, s59, 24576\n"
    "v_mfma_f32_16x16x32_bf16 v[68:71], a[68:71], v[24:27], v[68:71]\n"
    "ds_read_b128 v[24:27], v0 offset:5120\n"
    "buffer_load_dwordx4 v1, s[36:39], s76 offen sc1 lds\n"
    "v_mfma_f32_16x16x32_bf16 v[32:35], a[72:75], v[16:19], v[32:35]\n"
    "buffer_store_dwordx4 v[72:75], v1, s[36:39], s73 offen offset:1024 sc1\n"
    "v_mfma_f32_16x16x32_bf16 v[36:39], a[76:79], v[16:19], v[36:39]\n"
    "s_lshr_b32 s73, s68, 1\n"
    "v_mfma_f32_16x16x32_bf16 v[48:51], v[16:19], a[80:83], v[48:51]\n"
    "s_and_b32 s73, s73, s57\n"
    "v_mfma_f32_16x16x32_bf16 v[56:59], v[16:19], a[84:87], v[56:59]\n"
    "s_mul_i32 s73, s73, s56\n"
    "v_mfma_f32_16x16x32_bf16 v[64:67], a[88:91], v[16:19], v[64:67]\n"
    "s_waitcnt lgkmcnt(1)\n"
    "s_add_u32 s76, s76, 16384\n"
    "s_add_u32 s73, s73, s55\n"
    "v_mfma_f32_16x16x32_bf16 v[68:71], a[92:95], v[16:19], v[68:71]\n"
    "ds_read_b128 v[16:19], v0 offset:6144\n"
    "s_add_u32 s79, s73, 16384\n"
    "v_mfma_f32_16x16x32_bf16 v[32:35], a[96:99], v[20:23], v[32:35]\n"
    "s_add_u32 m0, s59, 28672\n"
    "s_add_u32 s80, s73, 32768\n"
    "v_mfma_f32_16x16x32_bf16 v[36:39], a[100:103], v[20:23], v[36:39]\n"
    "buffer_load_dwordx4 v1, s[36:39], s76 offen sc1 lds\n"
    "v_mfma_f32_16x16x32_bf16 v[48:51], v[20:23], a[104:107], v[48:51]\n"
    "v_mfma_f32_16x16x32_bf16 v[56:59], v[20:23], a[108:111], v[56:59]\n"
    "v_mfma_f32_16x16x32_bf16 v[64:67], a[112:115], v[20:23], v[64:67]\n"
    "s_waitcnt lgkmcnt(1)\n"
    "s_add_u32 s76, s76, 16384\n"
    "s_add_u32 m0, s59, 32768\n"
    "v_mfma_f32_16x16x32_bf16 v[68:71], a[116:119], v[20:23], v[68:71]\n"
    "ds_read_b128 v[20:23], v0 offset:7168\n"
    "buffer_load_dwordx4 v1, s[36:39], s76 offen sc1 lds\n"
    "v_mfma_f32_16x16x32_bf16 v[32:35], a[120:123], v[24:27], v[32:35]\n"
    "v_mfma_f32_16x16x32_bf16 v[36:39], a[124:127], v[24:27], v[36:39]\n"
    "v_mfma_f32_16x16x32_bf16 v[48:51], v[24:27], a[128:131], v[48:51]\n"
    "v_mfma_f32_16x16x32_bf16 v[56:59], v[24:27], a[132:135], v[56:59]\n"
    "v_mfma_f32_16x16x32_bf16 v[64:67], a[136:139], v[24:27], v[64:67]\n"
    "s_waitcnt lgkmcnt(1)\n"
    "s_add_u32 s76, s76, 16384\n"
    "s_add_u32 m0, s59, 36864\n"
    "v_mfma_f32_16x16x32_bf16 v[68:71], a[140:143], v[24:27], v[68:71]\n"
    "ds_read_b128 v[24:27], v0 offset:8192\n"
    "buffer_load_dwordx4 v8, s[36:39], s76 offen sc1 lds\n"
    "v_mfma_f32_16x16x32_bf16 v[32:35], a[144:147], v[16:19], v[32:35]\n"
    "v_mfma_f32_16x16x32_bf16 v[36:39], a[148:151], v[16:19], v[36:39]\n"
    "v_mfma_f32_16x16x32_bf16 v[48:51], v[16:19], a[152:155], v[48:51]\n"
    "v_mfma_f32_16x16x32_bf16 v[56:59], v[16:19], a[156:159], v[56:59]\n"
    "v_mfma_f32_16x16x32_bf16 v[64:67], a[160:163], v[16:19], v[64:67]\n"
    "s_waitcnt lgkmcnt(1)\n"
    "v_mfma_f32_16x16x32_bf16 v[68:71], a[164:167], v[16:19], v[68:71]\n"
    "ds_read_b128 v[16:19], v0 offset:9216\n"
    "v_mfma_f32_16x16x32_bf16 v[32:35], a[168:171], v[20:23], v[32:35]\n"
    "v_mfma_f32_16x16x32_bf16 v[36:39], a[172:175], v[20:23], v[36:39]\n"
    "v_mfma_f32_16x16x32_bf16 v[48:51], v[20:23], a[176:179], v[48:51]\n"
    "v_mfma_f32_16x16x32_bf16 v[56:59], v[20:23], a[180:183], v[56:59]\n"
    "v_mfma_f32_16x16x32_bf16 v[64:67], a[184:187], v[20:23], v[64:67]\n"
    "s_waitcnt lgkmcnt(1)\n"
    "v_mfma_f32_16x16x32_bf16 v[68:71], a[188:191], v[20:23], v[68:71]\n"
    "ds_read_b128 v[20:23], v0 offset:10240\n"
    "v_mfma_f32_16x16x32_bf16 v[32:35], a[192:195], v[24:27], v[32:35]\n"
    "v_mfma_f32_16x16x32_bf16 v[36:39], a[196:199], v[24:27], v[36:39]\n"
    "v_mfma_f32_16x16x32_bf16 v[48:51], v[24:27], a[200:203], v[48:51]\n"
    "v_mfma_f32_16x16x32_bf16 v[56:59], v[24:27], a[204:207], v[56:59]\n"
    "v_mfma_f32_16x16x32_bf16 v[64:67], a[208:211], v[24:27], v[64:67]\n"
    "s_waitcnt lgkmcnt(1)\n"
    "v_mfma_f32_16x16x32_bf16 v[68:71], a[212:215], v[24:27], v[68:71]\n"
    "ds_read_b128 v[24:27], v0 offset:11264\n"
    "v_mfma_f32_16x16x32_bf16 v[32:35], a[216:219], v[16:19], v[32:35]\n"
    "v_mfma_f32_16x16x32_bf16 v[36:39], a[220:223], v[16:19], v[36:39]\n"
    "v_mfma_f32_16x16x32_bf16 v[48:51], v[16:19], a[224:227], v[48:51]\n"
    "v_mfma_f32_16x16x32_bf16 v[56:59], v[16:19], a[228:231], v[56:59]\n"
    "v_mfma_f32_16x16x32_bf16 v[64:67], a[232:235], v[16:19], v[64:67]\n"
    "s_waitcnt lgkmcnt(1)\n"
    "s_waitcnt vmcnt(3)\n"
    "buffer_atomic_smax v12, v10, s[36:39], s70 offen sc1\n"
    "v_mfma_f32_16x16x32_bf16 v[68:71], a[236:239], v[16:19], v[68:71]\n"
    "ds_read_b128 v[16:19], v0 offset:12288\n"
    "v_mfma_f32_16x16x32_bf16 v[32:35], a[240:243], v[20:23], v[32:35]\n"
    "v_mfma_f32_16x16x32_bf16 v[36:39], a[244:247], v[20:23], v[36:39]\n"
    "v_mfma_f32_16x16x32_bf16 v[48:51], v[20:23], a[248:251], v[48:51]\n"
    "v_mfma_f32_16x16x32_bf16 v[56:59], v[20:23], a[252:255], v[56:59]\n"
    "v_mfma_f32_16x16x32_bf16 v[64:67], v[80:83], v[20:23], v[64:67]\n"
    "s_waitcnt lgkmcnt(1)\n"
    "v_mfma_f32_16x16x32_bf16 v[68:71], v[84:87], v[20:23], v[68:71]\n"
    "ds_read_b128 v[20:23], v0 offset:13312\n"
    "v_mfma_f32_16x16x32_bf16 v[32:35], v[88:91], v[24:27], v[32:35]\n"
    "v_mfma_f32_16x16x32_bf16 v[36:39], v[92:95], v[24:27], v[36:39]\n"
    "v_mfma_f32_16x16x32_bf16 v[48:51], v[24:27], v[96:99], v[48:51]\n"
    "v_mfma_f32_16x16x32_bf16 v[56:59], v[24:27], v[100:103], v[56:59]\n"
    "v_mfma_f32_16x16x32_bf16 v[64:67], v[104:107], v[24:27], v[64:67]\n"
    "s_waitcnt lgkmcnt(1)\n"
    "v_mfma_f32_16x16x32_bf16 v[68:71], v[108:111], v[24:27], v[68:71]\n"
    "ds_read_b128 v[24:27], v0 offset:14336\n"
    "v_mfma_f32_16x16x32_bf16 v[32:35], v[112:115], v[16:19], v[32:35]\n"
    "v_mfma_f32_16x16x32_bf16 v[36:39], v[116:119], v[16:19], v[36:39]\n"
    "v_mfma_f32_16x16x32_bf16 v[48:51], v[16:19], v[120:123], v[48:51]\n"
    "v_mfma_f32_16x16x32_bf16 v[56:59], v[16:19], v[124:127], v[56:59]\n"
    "v_mfma_f32_16x16x32_bf16 v[64:67], v[128:131], v[16:19], v[64:67]\n"
    "s_waitcnt lgkmcnt(1)\n"
    "v_mfma_f32_16x16x32_bf16 v[68:71], v[132:135], v[16:19], v[68:71]\n"
    "ds_read_b128 v[16:19], v0 offset:15360\n"
    ".LgP_join0_%=:\n"
    "v_mfma_f32_16x16x32_bf16 v[32:35], v[136:139], v[20:23], v[32:35]\n"
    "s_add_u32 s71, s68, 2\n"
    "s_waitcnt vmcnt(5)\n"
    "v_mfma_f32_16x16x32_bf16 v[36:39], v[140:143], v[20:23], v[36:39]\n"
    "v_add_u32 v13, v11, v3\n"
    "v_mfma_f32_16x16x32_bf16 v[48:51], v[20:23], v[144:147], v[48:51]\n"
    "v_cmp_gt_i32 vcc, s71, v13\n"
    "v_mfma_f32_16x16x32_bf16 v[56:59], v[20:23], v[148:151], v[56:59]\n"
    "v_mfma_f32_16x16x32_bf16 v[64:67], v[152:155], v[20:23], v[64:67]\n"
    "s_waitcnt lgkmcnt(1)\n"
    "v_mfma_f32_16x16x32_bf16 v[68:71], v[156:159], v[20:23], v[68:71]\n"
    "ds_read_b128 v[20:23], v0 offset:16384\n"
    "v_mfma_f32_16x16x32_bf16 v[32:35], v[160:163], v[24:27], v[32:35]\n"
    "s_or_b64 exec, vcc, s[62:63]\n"
    "buffer_atomic_smax v11, v2, s[36:39], 0 offen sc0 sc1\n"
    "s_mov_b64 exec, -1\n"
    "v_mfma_f32_16x16x32_bf16 v[36:39], v[164:167], v[24:27], v[36:39]\n"
    "s_lshr_b32 s76, s71, 1\n"
    "v_mfma_f32_16x16x32_bf16 v[48:51], v[24:27], v[168:171], v[48:51]\n"
    "s_and_b32 s76, s76, s54\n"
    "v_mfma_f32_16x16x32_bf16 v[56:59], v[24:27], v[172:175], v[56:59]\n"
    "s_mul_i32 s76, s76, s53\n"
    "v_mfma_f32_16x16x32_bf16 v[64:67], v[176:179], v[24:27], v[64:67]\n"
    "s_waitcnt lgkmcnt(1)\n"
    "s_add_u32 s76, s76, s52\n"
    "v_mfma_f32_16x16x32_bf16 v[68:71], v[180:183], v[24:27], v[68:71]\n"
    "ds_read_b128 v[24:27], v0 offset:17408\n"
    "v_mfma_f32_16x16x32_bf16 v[32:35], v[184:187], v[16:19], v[32:35]\n"
    "v_mfma_f32_16x16x32_bf16 v[36:39], v[188:191], v[16:19], v[36:39]\n"
    "v_mfma_f32_16x16x32_bf16 v[48:51], v[16:19], v[192:195], v[48:51]\n"
    "v_mfma_f32_16x16x32_bf16 v[56:59], v[16:19], v[196:199], v[56:59]\n"
    "s_waitcnt vmcnt(1) lgkmcnt(0)\n"
    "s_barrier\n"
    "v_mfma_f32_16x16x32_bf16 v[64:67], v[200:203], v[16:19], v[64:67]\n"
    "v_mfma_f32_16x16x32_bf16 v[68:71], v[204:207], v[16:19], v[68:71]\n"
    "ds_read_b128 v[16:19], v9 offset:0\n"
    "v_mfma_f32_16x16x32_bf16 v[32:35], v[208:211], v[20:23], v[32:35]\n"
    "v_mfma_f32_16x16x32_bf16 v[36:39], v[212:215], v[20:23], v[36:39]\n"
    "v_add_u32 v13, v5, v3\n"
    "v_mfma_f32_16x16x32_bf16 v[48:51], v[20:23], v[216:219], v[48:51]\n"
    "v_cmp_gt_i32 vcc, s71, v13\n"
    "v_mfma_f32_16x16x32_bf16 v[56:59], v[20:23], v[220:223], v[56:59]\n"
    "v_mfma_f32_16x16x32_bf16 v[64:67], v[224:227], v[20:23], v[64:67]\n"
    "v_mfma_f32_16x16x32_bf16 v[68:71], v[228:231], v[20:23], v[68:71]\n"
    "ds_read_b128 v[20:23], v9 offset:1024\n"
    "v_mfma_f32_16x16x32_bf16 v[32:35], v[232:235], v[24:27], v[32:35]\n"
    "s_or_b64 exec, vcc, s[62:63]\n"
    "buffer_atomic_smax v5, v2, s[36:39], 0 offen sc0 sc1\n"
    "s_mov_b64 exec, -1\n"
    "v_mfma_f32_16x16x32_bf16 v[36:39], v[236:239], v[24:27], v[36:39]\n"
    "v_mfma_f32_16x16x32_bf16 v[48:51], v[24:27], v[240:243], v[48:51]\n"
    "v_mfma_f32_16x16x32_bf16 v[56:59], v[24:27], v[244:247], v[56:59]\n"
    "s_mov_b32 s69, 0x80000000\n"
    "s_mov_b32 s70, 0\n"
    "v_mov_b32 v12, s68\n"
    "v_mfma_f32_16x16x32_bf16 v[64:67], v[248:251], v[24:27], v[64:67]\n"
    "s_waitcnt lgkmcnt(1)\n"
    "s_add_u32 s68, s68, 1\n"
    "v_mfma_f32_16x16x32_bf16 v[68:71], v[252:255], v[24:27], v[68:71]\n"
    "ds_read_b128 v[24:27], v9 offset:2048\n"
    ".LgP_head1_%=:\n"
    "v_mfma_f32_16x16x32_bf16 v[40:43], a[0:3], v[16:19], 0\n"
    "v_mfma_f32_16x16x32_bf16 v[44:47], a[4:7], v[16:19], 0\n"
    "v_cvt_pk_bf16_f32 v32, v32, v33\n"
    "v_cvt_pk_bf16_f32 v33, v34, v35\n"
    "v_mfma_f32_16x16x32_bf16 v[52:55], v[16:19], a[8:11], 0\n"
    "v_cvt_pk_bf16_f32 v34, v36, v37\n"
    "v_cvt_pk_bf16_f32 v35, v38, v39\n"
    "v_mfma_f32_16x16x32_bf16 v[60:63], v[16:19], a[12:15], 0\n"
    "buffer_store_dwordx4 v[32:35], v1, s[36:39], s79 offen sc1\n"
    "v_mfma_f32_16x16x32_bf16 v[72:75], a[16:19], v[16:19], 0\n"
    "s_waitcnt lgkmcnt(1)\n"
    "v_cvt_pk_bf16_f32 v28, v48, v49\n"
    "v_cvt_pk_bf16_f32 v29, v50, v51\n"
    "v_mfma_f32_16x16x32_bf16 v[76:79], a[20:23], v[16:19], 0\n"
    "ds_read_b128 v[16:19], v9 offset:3072\n"
    "v_cvt_pk_bf16_f32 v30, v56, v57\n"
    "v_mfma_f32_16x16x32_bf16 v[40:43], a[24:27], v[20:23], v[40:43]\n"
    "v_cvt_pk_bf16_f32 v31, v58, v59\n"
    "v_mfma_f32_16x16x32_bf16 v[44:47], a[28:31], v[20:23], v[44:47]\n"
    "s_waitcnt vmcnt(2)\n"
    "v_cvt_pk_bf16_f32 v64, v64, v65\n"
    "v_add_u32 v13, v11, v3\n"
    "v_mfma_f32_16x16x32_bf16 v[52:55], v[20:23], a[32:35], v[52:55]\n"
    "v_cvt_pk_bf16_f32 v65, v66, v67\n"
    "v_cmp_gt_i32 vcc, s71, v13\n"
    "v_mfma_f32_16x16x32_bf16 v[60:63], v[20:23], a[36:39], v[60:63]\n"
    "buffer_atomic_smax v7, v14, s[36:39], s69 offen sc1\n"
    "v_cvt_pk_bf16_f32 v66, v68, v69\n"
    "v_cvt_pk_bf16_f32 v67, v70, v71\n"
    "v_mfma_f32_16x16x32_bf16 v[72:75], a[40:43], v[20:23], v[72:75]\n"
    "s_waitcnt lgkmcnt(1)\n"
    "v_mfma_f32_16x16x32_bf16 v[76:79], a[44:47], v[20:23], v[76:79]\n"
    "ds_read_b128 v[20:23], v9 offset:4096\n"
    "s_cbranch_vccnz .LgP_poll1_%=\n"
    "v_mfma_f32_16x16x32_bf16 v[40:43], a[48:51], v[24:27], v[40:43]\n"
    "s_add_u32 m0, s59, 0\n"
    "v_mfma_f32_16x16x32_bf16 v[44:47], a[52:55], v[24:27], v[44:47]\n"
    "buffer_store_dwordx4 v[64:67], v1, s[36:39], s73 offen sc1\n"
    "v_mfma_f32_16x16x32_bf16 v[52:55], v[24:27], a[56:59], v[52:55]\n"
    "buffer_load_dwordx4 v1, s[36:39], s76 offen sc1 lds\n"
    "s_lshr_b32 s73, s68, 1\n"
    "v_mfma_f32_16x16x32_bf16 v[60:63], v[24:27], a[60:63], v[60:63]\n"
    "s_and_b32 s73, s73, s57\n"
    "v_mfma_f32_16x16x32_bf16 v[72:75], a[64:67], v[24:27], v[72:75]\n"
    "s_waitcnt lgkmcnt(1)\n"
    "s_add_u32 s76, s76, 16384\n"
    "s_mul_i32 s73, s73, s56\n"
    "v_mfma_f32_16x16x32_bf16 v[76:79], a[68:71], v[24:27], v[76:79]\n"
    "ds_read_b128 v[24:27], v9 offset:5120\n"
    "s_add_u32 s73, s73, s55\n"
    "v_mfma_f32_16x16x32_bf16 v[40:43], a[72:75], v[16:19], v[40:43]\n"
    "s_add_u32 m0, s59, 4096\n"
    "s_add_u32 s79, s73, 16384\n"
    "v_mfma_f32_16x16x32_bf16 v[44:47], a[76:79], v[16:19], v[44:47]\n"
    "buffer_load_dwordx4 v1, s[36:39], s76 offen sc1 lds\n"
    "s_add_u32 s80, s73, 32768\n"
    "v_mfma_f32_16x16x32_bf16 v[52:55], v[16:19], a[80:83], v[52:55]\n"
    "v_mfma_f32_16x16x32_bf16 v[60:63], v[16:19], a[84:87], v[60:63]\n"
    "v_mfma_f32_16x16x32_bf16 v[72:75], a[88:91], v[16:19], v[72:75]\n"
    "s_waitcnt lgkmcnt(1)\n"
    "s_add_u32 s76, s76, 16384\n"
    "s_add_u32 m0, s59, 8192\n"
    "v_mfma_f32_16x16x32_bf16 v[76:79], a[92:95], v[16:19], v[76:79]\n"
    "ds_read_b128 v[16:19], v9 offset:6144\n"
    "buffer_load_dwordx4 v1, s[36:39], s76 offen sc1 lds\n"
    "v_mfma_f32_16x16x32_bf16 v[40:43], a[96:99], v[20:23], v[40:43]\n"
    "v_mfma_f32_16x16x32_bf16 v[44:47], a[100:103], v[20:23], v[44:47]\n"
    "v_mfma_f32_16x16x32_bf16 v[52:55], v[20:23], a[104:107], v[52:55]\n"
    "v_mfma_f32_16x16x32_bf16 v[60:63], v[20:23], a[108:111], v[60:63]\n"
    "v_mfma_f32_16x16x32_bf16 v[72:75], a[112:115], v[20:23], v[72:75]\n"
    "s_waitcnt lgkmcnt(1)\n"
    "s_add_u32 s76, s76, 16384\n"
    "s_add_u32 m0, s59, 12288\n"
    "v_mfma_f32_16x16x32_bf16 v[76:79], a[116:119], v[20:23], v[76:79]\n"
    "ds_read_b128 v[20:23], v9 offset:7168\n"
    "buffer_load_dwordx4 v1, s[36:39], s76 offen sc1 lds\n"
    "v_mfma_f32_16x16x32_bf16 v[40:43], a[120:123], v[24:27], v[40:43]\n"
    "v_mfma_f32_16x16x32_bf16 v[44:47], a[124:127], v[24:27], v[44:47]\n"
    "v_mfma_f32_16x16x32_bf16 v[52:55], v[24:27], a[128:131], v[52:55]\n"
    "v_mfma_f32_16x16x32_bf16 v[60:63], v[24:27], a[132:135], v[60:63]\n"
    "v_mfma_f32_16x16x32_bf16 v[72:75], a[136:139], v[24:27], v[72:75]\n"
    "s_waitcnt lgkmcnt(1)\n"
    "s_add_u32 s76, s76, 16384\n"
    "s_add_u32 m0, s59, 16384\n"
    "v_mfma_f32_16x16x32_bf16 v[76:79], a[140:143], v[24:27], v[76:79]\n"
    "ds_read_b128 v[24:27], v9 offset:8192\n"
    "buffer_load_dwordx4 v8, s[36:39], s76 offen sc1 lds\n"
    "v_mfma_f32_16x16x32_bf16 v[40:43], a[144:147], v[16:19], v[40:43]\n"
    "v_mfma_f32_16x16x32_bf16 v[44:47], a[148:151], v[16:19], v[44:47]\n"
    "v_mfma_f32_16x16x32_bf16 v[52:55], v[16:19], a[152:155], v[52:55]\n"
    "v_mfma_f32_16x16x32_bf16 v[60:63], v[16:19], a[156:159], v[60:63]\n"
    "v_mfma_f32_16x16x32_bf16 v[72:75], a[160:163], v[16:19], v[72:75]\n"
    "s_waitcnt lgkmcnt(1)\n"
    "v_mfma_f32_16x16x32_bf16 v[76:79], a[164:167], v[16:19], v[76:79]\n"
    "ds_read_b128 v[16:19], v9 offset:9216\n"
    "v_mfma_f32_16x16x32_bf16 v[40:43], a[168:171], v[20:23], v[40:43]\n"
    "v_mfma_f32_16x16x32_bf16 v[44:47], a[172:175], v[20:23], v[44:47]\n"
    "v_mfma_f32_16x16x32_bf16 v[52:55], v[20:23], a[176:179], v[52:55]\n"
    "v_mfma_f32_16x16x32_bf16 v[60:63], v[20:23], a[180:183], v[60:63]\n"
    "v_mfma_f32_16x16x32_bf16 v[72:75], a[184:187], v[20:23], v[72:75]\n"
    "s_waitcnt lgkmcnt(1)\n"
    "v_mfma_f32_16x16x32_bf16 v[76:79], a[188:191], v[20:23], v[76:79]\n"
    "ds_read_b128 v[20:23], v9 offset:10240\n"
    "v_mfma_f32_16x16x32_bf16 v[40:43], a[192:195], v[24:27], v[40:43]\n"
    "v_mfma_f32_16x16x32_bf16 v[44:47], a[196:199], v[24:27], v[44:47]\n"
    "v_mfma_f32_16x16x32_bf16 v[52:55], v[24:27], a[200:203], v[52:55]\n"
    "v_mfma_f32_16x16x32_bf16 v[60:63], v[24:27], a[204:207], v[60:63]\n"
    "v_mfma_f32_16x16x32_bf16 v[72:75], a[208:211], v[24:27], v[72:75]\n"
    "s_waitcnt lgkmcnt(1)\n"
    "s_waitcnt vmcnt(5)\n"
    "buffer_atomic_smax v12, v14, s[36:39], s70 offen sc1\n"
    "v_mfma_f32_16x16x32_bf16 v[76:79], a[212:215], v[24:27], v[76:79]\n"
    "ds_read_b128 v[24:27], v9 offset:11264\n"
    "v_mfma_f32_16x16x32_bf16 v[40:43], a[216:219], v[16:19], v[40:43]\n"
    "v_mfma_f32_16x16x32_bf16 v[44:47], a[220:223], v[16:19], v[44:47]\n"
    "v_mfma_f32_16x16x32_bf16 v[52:55], v[16:19], a[224:227], v[52:55]\n"
    "v_mfma_f32_16x16x32_bf16 v[60:63], v[16:19], a[228:231], v[60:63]\n"
    "v_mfma_f32_16x16x32_bf16 v[72:75], a[232:235], v[16:19], v[72:75]\n"
    "s_waitcnt lgkmcnt(1)\n"
    "v_mfma_f32_16x16x32_bf16 v[76:79], a[236:239], v[16:19], v[76:79]\n"
    "ds_read_b128 v[16:19], v9 offset:12288\n"
    "v_mfma_f32_16x16x32_bf16 v[40:43], a[240:243], v[20:23], v[40:43]\n"
    "v_mfma_f32_16x16x32_bf16 v[44:47], a[244:247], v[20:23], v[44:47]\n"
    "v_mfma_f32_16x16x32_bf16 v[52:55], v[20:23], a[248:251], v[52:55]\n"
    "v_mfma_f32_16x16x32_bf16 v[60:63], v[20:23], a[252:255], v[60:63]\n"
    "v_mfma_f32_16x16x32_bf16 v[72:75], v[80:83], v[20:23], v[72:75]\n"
    "s_waitcnt lgkmcnt(1)\n"
    "v_mfma_f32_16x16x32_bf16 v[76:79], v[84:87], v[20:23], v[76:79]\n"
    "ds_read_b128 v[20:23], v9 offset:13312\n"
    "v_mfma_f32_16x16x32_bf16 v[40:43], v[88:91], v[24:27], v[40:43]\n"
    "v_mfma_f32_16x16x32_bf16 v[44:47], v[92:95], v[24:27], v[44:47]\n"
    "v_mfma_f32_16x16x32_bf16 v[52:55], v[24:27], v[96:99], v[52:55]\n"
    "v_mfma_f32_16x16x32_bf16 v[60:63], v[24:27], v[100:103], v[60:63]\n"
    "v_mfma_f32_16x16x32_bf16 v[72:75], v[104:107], v[24:27], v[72:75]\n"
    "s_waitcnt lgkmcnt(1)\n"
    "v_mfma_f32_16x16x32_bf16 v[76:79], v[108:111], v[24:27], v[76:79]\n"
    "ds_read_b128 v[24:27], v9 offset:14336\n"
    "v_mfma_f32_16x16x32_bf16 v[40:43], v[112:115], v[16:19], v[40:43]\n"
    "v_mfma_f32_16x16x32_bf16 v[44:47], v[116:119], v[16:19], v[44:47]\n"
    "v_mfma_f32_16x16x32_bf16 v[52:55], v[16:19], v[120:123], v[52:55]\n"
    "v_mfma_f32_16x16x32_bf16 v[60:63], v[16:19], v[124:127], v[60:63]\n"
    "v_mfma_f32_16x16x32_bf16 v[72:75], v[128:131], v[16:19], v[72:75]\n"
    "s_waitcnt lgkmcnt(1)\n"
    "v_mfma_f32_16x16x32_bf16 v[76:79], v[132:135], v[16:19], v[76:79]\n"
    "ds_read_b128 v[16:19], v9 offset:15360\n"
    ".LgP_join1_%=:\n"
    "v_mfma_f32_16x16x32_bf16 v[40:43], v[136:139], v[20:23], v[40:43]\n"
    "s_add_u32 s71, s68, 2\n"
    "s_waitcnt vmcnt(5)\n"
    "v_mfma_f32_16x16x32_bf16 v[44:47], v[140:143], v[20:23], v[44:47]\n"
    "v_add_u32 v13, v11, v3\n"
    "v_mfma_f32_16x16x32_bf16 v[52:55], v[20:23], v[144:147], v[52:55]\n"
    "v_cmp_gt_i32 vcc, s71, v13\n"
    "v_mfma_f32_16x16x32_bf16 v[60:63], v[20:23], v[148:151], v[60:63]\n"
    "v_mfma_f32_16x16x32_bf16 v[72:75], v[152:155], v[20:23], v[72:75]\n"
    "s_waitcnt lgkmcnt(1)\n"
    "v_mfma_f32_16x16x32_bf16 v[76:79], v[156:159], v[20:23], v[76:79]\n"
    "ds_read_b128 v[20:23], v9 offset:16384\n"
    "v_mfma_f32_16x16x32_bf16 v[40:43], v[160:163], v[24:27], v[40:43]\n"
    "s_or_b64 exec, vcc, s[62:63]\n"
    "buffer_atomic_smax v11, v2, s[36:39], 0 offen sc0 sc1\n"
    "s_mov_b64 exec, -1\n"
    "v_mfma_f32_16x16x32_bf16 v[44:47], v[164:167], v[24:27], v[44:47]\n"
    "s_lshr_b32 s76, s71, 1\n"
    "v_mfma_f32_16x16x32_bf16 v[52:55], v[24:27], v[168:171], v[52:55]\n"
    "s_and_b32 s76, s76, s54\n"
    "v_mfma_f32_16x16x32_bf16 v[60:63], v[24:27], v[172:175], v[60:63]\n"
    "s_mul_i32 s76, s76, s53\n"
    "v_mfma_f32_16x16x32_bf16 v[72:75], v[176:179], v[24:27], v[72:75]\n"
    "s_waitcnt lgkmcnt(1)\n"
    "s_add_u32 s76, s76, s52\n"
    "v_mfma_f32_16x16x32_bf16 v[76:79], v[180:183], v[24:27], v[76:79]\n"
    "ds_read_b128 v[24:27], v9 offset:17408\n"
    "s_add_u32 s76, s76, 1024\n"
    "v_mfma_f32_16x16x32_bf16 v[40:43], v[184:187], v[16:19], v[40:43]\n"
    "v_mfma_f32_16x16x32_bf16 v[44:47], v[188:191], v[16:19], v[44:47]\n"
    "v_mfma_f32_16x16x32_bf16 v[52:55], v[16:19], v[192:195], v[52:55]\n"
    "v_mfma_f32_16x16x32_bf16 v[60:63], v[16:19], v[196:199], v[60:63]\n"
    "s_waitcnt vmcnt(1) lgkmcnt(0)\n"
    "s_barrier\n"
    "v_mfma_f32_16x16x32_bf16 v[72:75], v[200:203], v[16:19], v[72:75]\n"
    "v_mfma_f32_16x16x32_bf16 v[76:79], v[204:207], v[16:19], v[76:79]\n"
    "ds_read_b128 v[16:19], v0 offset:0\n"
    "v_mfma_f32_16x16x32_bf16 v[40:43], v[208:211], v[20:23], v[40:43]\n"
    "v_mfma_f32_16x16x32_bf16 v[44:47], v[212:215], v[20:23], v[44:47]\n"
    "v_add_u32 v13, v5, v3\n"
    "v_mfma_f32_16x16x32_bf16 v[52:55], v[20:23], v[216:219], v[52:55]\n"
    "v_cmp_gt_i32 vcc, s71, v13\n"
    "v_mfma_f32_16x16x32_bf16 v[60:63], v[20:23], v[220:223], v[60:63]\n"
    "v_mfma_f32_16x16x32_bf16 v[72:75], v[224:227], v[20:23], v[72:75]\n"
    "v_mfma_f32_16x16x32_bf16 v[76:79], v[228:231], v[20:23], v[76:79]\n"
    "ds_read_b128 v[20:23], v0 offset:1024\n"
    "v_mfma_f32_16x16x32_bf16 v[40:43], v[232:235], v[24:27], v[40:43]\n"
    "s_or_b64 exec, vcc, s[62:63]\n"
    "buffer_atomic_smax v5, v2, s[36:39], 0 offen sc0 sc1\n"
    "s_mov_b64 exec, -1\n"
    "v_mfma_f32_16x16x32_bf16 v[44:47], v[236:239], v[24:27], v[44:47]\n"
    "v_mfma_f32_16x16x32_bf16 v[52:55], v[24:27], v[240:243], v[52:55]\n"
    "v_mfma_f32_16x16x32_bf16 v[60:63], v[24:27], v[244:247], v[60:63]\n"
    "s_mov_b32 s69, 0x80000000\n"
    "s_mov_b32 s70, 0\n"
    "v_mov_b32 v12, s68\n"
    "v_mfma_f32_16x16x32_bf16 v[72:75], v[248:251], v[24:27], v[72:75]\n"
    "s_waitcnt lgkmcnt(1)\n"
    "s_add_u32 s68, s68, 1\n"
    "v_mfma_f32_16x16x32_bf16 v[76:79], v[252:255], v[24:27], v[76:79]\n"
    "ds_read_b128 v[24:27], v0 offset:2048\n"
    "s_branch .LgP_head0_%=\n"
    ".LgP_LOOPE_%=:\n"
    ".LgP_done_%=:\n"
    "s_waitcnt vmcnt(0)\n"
    "buffer_atomic_smax v12, v10, s[36:39], 0 offen sc1\n"
    "s_waitcnt vmcnt(0)\n"
    "s_branch .LgP_FIN_%=\n"
    ".LgP_poll0_%=:\n"
    "v_mfma_f32_16x16x32_bf16 v[32:35], a[48:51], v[24:27], v[32:35]\n"
    "s_or_b64 exec, vcc, s[62:63]\n"
    "buffer_atomic_smax v11, v2, s[36:39], 0 offen sc0 sc1\n"
    "s_mov_b64 exec, -1\n"
    "v_mfma_f32_16x16x32_bf16 v[36:39], a[52:55], v[24:27], v[36:39]\n"
    "buffer_store_dwordx4 v[60:63], v1, s[36:39], s80 offen offset:1024 sc1\n"
    "v_mfma_f32_16x16x32_bf16 v[48:51], v[24:27], a[56:59], v[48:51]\n"
    "v_cvt_pk_bf16_f32 v74, v76, v77\n"
    "v_cvt_pk_bf16_f32 v75, v78, v79\n"
    "v_mfma_f32_16x16x32_bf16 v[56:59], v[24:27], a[60:63], v[56:59]\n"
    "v_mfma_f32_16x16x32_bf16 v[64:67], a[64:67], v[24:27], v[64:67]\n"
    "s_waitcnt lgkmcnt(1)\n"
    "v_mfma_f32_16x16x32_bf16 v[68:71], a[68:71], v[24:27], v[68:71]\n"
    "ds_read_b128 v[24:27], v0 offset:5120\n"
    "v_mfma_f32_16x16x32_bf16 v[32:35], a[72:75], v[16:19], v[32:35]\n"
    "buffer_store_dwordx4 v[72:75], v1, s[36:39], s73 offen offset:1024 sc1\n"
    "v_mfma_f32_16x16x32_bf16 v[36:39], a[76:79], v[16:19], v[36:39]\n"
    "s_lshr_b32 s73, s68, 1\n"
    "v_mfma_f32_16x16x32_bf16 v[48:51], v[16:19], a[80:83], v[48:51]\n"
    "s_and_b32 s73, s73, s57\n"
    "v_mfma_f32_16x16x32_bf16 v[56:59], v[16:19], a[84:87], v[56:59]\n"
    "s_mul_i32 s73, s73, s56\n"
    "v_mfma_f32_16x16x32_bf16 v[64:67], a[88:91], v[16:19], v[64:67]\n"
    "s_waitcnt lgkmcnt(1)\n"
    "s_add_u32 s73, s73, s55\n"
    "v_mfma_f32_16x16x32_bf16 v[68:71], a[92:95], v[16:19], v[68:71]\n"
    "ds_read_b128 v[16:19], v0 offset:6144\n"
    "s_add_u32 s79, s73, 16384\n"
    "v_mfma_f32_16x16x32_bf16 v[32:35], a[96:99], v[20:23], v[32:35]\n"
    "s_add_u32 s80, s73, 32768\n"
    "v_mfma_f32_16x16x32_bf16 v[36:39], a[100:103], v[20:23], v[36:39]\n"
    "s_waitcnt vmcnt(6)\n"
    "v_add_u32 v13, v5, v3\n"
    "v_mfma_f32_16x16x32_bf16 v[48:51], v[20:23], a[104:107], v[48:51]\n"
    "v_cmp_gt_i32 vcc, s71, v13\n"
    "v_mfma_f32_16x16x32_bf16 v[56:59], v[20:23], a[108:111], v[56:59]\n"
    "v_mfma_f32_16x16x32_bf16 v[64:67], a[112:115], v[20:23], v[64:67]\n"
    "s_waitcnt lgkmcnt(1)\n"
    "v_mfma_f32_16x16x32_bf16 v[68:71], a[116:119], v[20:23], v[68:71]\n"
    "ds_read_b128 v[20:23], v0 offset:7168\n"
    "s_cbranch_vccz .LgP_dma0_30_%=\n"
    "v_mfma_f32_16x16x32_bf16 v[32:35], a[120:123], v[24:27], v[32:35]\n"
    "s_or_b64 exec, vcc, s[62:63]\n"
    "buffer_atomic_smax v5, v2, s[36:39], 0 offen sc0 sc1\n"
    "s_mov_b64 exec, -1\n"
    "v_mfma_f32_16x16x32_bf16 v[36:39], a[124:127], v[24:27], v[36:39]\n"
    "v_mfma_f32_16x16x32_bf16 v[48:51], v[24:27], a[128:131], v[48:51]\n"
    "v_mfma_f32_16x16x32_bf16 v[56:59], v[24:27], a[132:135], v[56:59]\n"
    "v_mfma_f32_16x16x32_bf16 v[64:67], a[136:139], v[24:27], v[64:67]\n"
    "s_waitcnt lgkmcnt(1)\n"
    "v_mfma_f32_16x16x32_bf16 v[68:71], a[140:143], v[24:27], v[68:71]\n"
    "ds_read_b128 v[24:27], v0 offset:8192\n"
    "v_mfma_f32_16x16x32_bf16 v[32:35], a[144:147], v[16:19], v[32:35]\n"
    "v_mfma_f32_16x16x32_bf16 v[36:39], a[148:151], v[16:19], v[36:39]\n"
    "v_mfma_f32_16x16x32_bf16 v[48:51], v[16:19], a[152:155], v[48:51]\n"
    "v_mfma_f32_16x16x32_bf16 v[56:59], v[16:19], a[156:159], v[56:59]\n"
    "v_mfma_f32_16x16x32_bf16 v[64:67], a[160:163], v[16:19], v[64:67]\n"
    "s_waitcnt lgkmcnt(1)\n"
    "v_mfma_f32_16x16x32_bf16 v[68:71], a[164:167], v[16:19], v[68:71]\n"
    "ds_read_b128 v[16:19], v0 offset:9216\n"
    "v_mfma_f32_16x16x32_bf16 v[32:35], a[168:171], v[20:23], v[32:35]\n"
    "v_mfma_f32_16x16x32_bf16 v[36:39], a[172:175], v[20:23], v[36:39]\n"
    "s_waitcnt vmcnt(3)\n"
    "v_add_u32 v13, v11, v3\n"
    "v_mfma_f32_16x16x32_bf16 v[48:51], v[20:23], a[176:179], v[48:51]\n"
    "v_cmp_gt_i32 vcc, s71, v13\n"
    "v_mfma_f32_16x16x32_bf16 v[56:59], v[20:23], a[180:183], v[56:59]\n"
    "v_mfma_f32_16x16x32_bf16 v[64:67], a[184:187], v[20:23], v[64:67]\n"
    "s_waitcnt lgkmcnt(1)\n"
    "v_mfma_f32_16x16x32_bf16 v[68:71], a[188:191], v[20:23], v[68:71]\n"
    "ds_read_b128 v[20:23], v0 offset:10240\n"
    "s_cbranch_vccz .LgP_dma0_48_%=\n"
    "v_mfma_f32_16x16x32_bf16 v[32:35], a[192:195], v[24:27], v[32:35]\n"
    "s_or_b64 exec, vcc, s[62:63]\n"
    "buffer_atomic_smax v11, v2, s[36:39], 0 offen sc0 sc1\n"
    "s_mov_b64 exec, -1\n"
    "v_mfma_f32_16x16x32_bf16 v[36:39], a[196:199], v[24:27], v[36:39]\n"
    "v_mfma_f32_16x16x32_bf16 v[48:51], v[24:27], a[200:203], v[48:51]\n"
    "v_mfma_f32_16x16x32_bf16 v[56:59], v[24:27], a[204:207], v[56:59]\n"
    "v_mfma_f32_16x16x32_bf16 v[64:67], a[208:211], v[24:27], v[64:67]\n"
    "s_waitcnt lgkmcnt(1)\n"
    "v_mfma_f32_16x16x32_bf16 v[68:71], a[212:215], v[24:27], v[68:71]\n"
    "ds_read_b128 v[24:27], v0 offset:11264\n"
    "v_mfma_f32_16x16x32_bf16 v[32:35], a[216:219], v[16:19], v[32:35]\n"
    "v_mfma_f32_16x16x32_bf16 v[36:39], a[220:223], v[16:19], v[36:39]\n"
    "v_mfma_f32_16x16x32_bf16 v[48:51], v[16:19], a[224:227], v[48:51]\n"
    "v_mfma_f32_16x16x32_bf16 v[56:59], v[16:19], a[228:231], v[56:59]\n"
    "v_mfma_f32_16x16x32_bf16 v[64:67], a[232:235], v[16:19], v[64:67]\n"
    "s_waitcnt lgkmcnt(1)\n"
    "s_waitcnt vmcnt(2)\n"
    "buffer_atomic_smax v12, v10, s[36:39], s70 offen sc1\n"
    "v_mfma_f32_16x16x32_bf16 v[68:71], a[236:239], v[16:19], v[68:71]\n"
    "ds_read_b128 v[16:19], v0 offset:12288\n"
    "v_mfma_f32_16x16x32_bf16 v[32:35], a[240:243], v[20:23], v[32:35]\n"
    "v_mfma_f32_16x16x32_bf16 v[36:39], a[244:247], v[20:23], v[36:39]\n"
    "s_waitcnt vmcnt(2)\n"
    "v_add_u32 v13, v5, v3\n"
    "v_mfma_f32_16x16x32_bf16 v[48:51], v[20:23], a[248:251], v[48:51]\n"
    "v_cmp_gt_i32 vcc, s71, v13\n"
    "v_mfma_f32_16x16x32_bf16 v[56:59], v[20:23], a[252:255], v[56:59]\n"
    "v_mfma_f32_16x16x32_bf16 v[64:67], v[80:83], v[20:23], v[64:67]\n"
    "s_waitcnt lgkmcnt(1)\n"
    "v_mfma_f32_16x16x32_bf16 v[68:71], v[84:87], v[20:23], v[68:71]\n"
    "ds_read_b128 v[20:23], v0 offset:13312\n"
    "s_cbranch_vccz .LgP_dma0_66_%=\n"
    "v_mfma_f32_16x16x32_bf16 v[32:35], v[88:91], v[24:27], v[32:35]\n"
    "s_or_b64 exec, vcc, s[62:63]\n"
    "buffer_atomic_smax v5, v2, s[36:39], 0 offen sc0 sc1\n"
    "s_mov_b64 exec, -1\n"
    "v_mfma_f32_16x16x32_bf16 v[36:39], v[92:95], v[24:27], v[36:39]\n"
    "v_mfma_f32_16x16x32_bf16 v[48:51], v[24:27], v[96:99], v[48:51]\n"
    "v_mfma_f32_16x16x32_bf16 v[56:59], v[24:27], v[100:103], v[56:59]\n"
    "v_mfma_f32_16x16x32_bf16 v[64:67], v[104:107], v[24:27], v[64:67]\n"
    "s_waitcnt lgkmcnt(1)\n"
    "v_mfma_f32_16x16x32_bf16 v[68:71], v[108:111], v[24:27], v[68:71]\n"
    "ds_read_b128 v[24:27], v0 offset:14336\n"
    "v_mfma_f32_16x16x32_bf16 v[32:35], v[112:115], v[16:19], v[32:35]\n"
    "v_mfma_f32_16x16x32_bf16 v[36:39], v[116:119], v[16:19], v[36:39]\n"
    "v_mfma_f32_16x16x32_bf16 v[48:51], v[16:19], v[120:123], v[48:51]\n"
    "v_mfma_f32_16x16x32_bf16 v[56:59], v[16:19], v[124:127], v[56:59]\n"
    "v_mfma_f32_16x16x32_bf16 v[64:67], v[128:131], v[16:19], v[64:67]\n"
    "s_waitcnt lgkmcnt(1)\n"
    "v_mfma_f32_16x16x32_bf16 v[68:71], v[132:135], v[16:19], v[68:71]\n"
    "ds_read_b128 v[16:19], v0 offset:15360\n"
    "v_mfma_f32_16x16x32_bf16 v[32:35], v[136:139], v[20:23], v[32:35]\n"
    "v_mfma_f32_16x16x32_bf16 v[36:39], v[140:143], v[20:23], v[36:39]\n"
    "s_waitcnt vmcnt(2)\n"
    "v_add_u32 v13, v11, v3\n"
    "v_mfma_f32_16x16x32_bf16 v[48:51], v[20:23], v[144:147], v[48:51]\n"
    "v_cmp_gt_i32 vcc, s71, v13\n"
    "v_mfma_f32_16x16x32_bf16 v[56:59], v[20:23], v[148:151], v[56:59]\n"
    "v_mfma_f32_16x16x32_bf16 v[64:67], v[152:155], v[20:23], v[64:67]\n"
    "s_waitcnt lgkmcnt(1)\n"
    "v_mfma_f32_16x16x32_bf16 v[68:71], v[156:159], v[20:23], v[68:71]\n"
    "ds_read_b128 v[20:23], v0 offset:16384\n"
    "s_cbranch_vccz .LgP_dma0_84_%=\n"
    "v_mfma_f32_16x16x32_bf16 v[32:35], v[160:163], v[24:27], v[32:35]\n"
    "s_or_b64 exec, vcc, s[62:63]\n"
    "buffer_atomic_smax v11, v2, s[36:39], 0 offen sc0 sc1\n"
    "s_mov_b64 exec, -1\n"
    "v_mfma_f32_16x16x32_bf16 v[36:39], v[164:167], v[24:27], v[36:39]\n"
    "v_mfma_f32_16x16x32_bf16 v[48:51], v[24:27], v[168:171], v[48:51]\n"
    "v_mfma_f32_16x16x32_bf16 v[56:59], v[24:27], v[172:175], v[56:59]\n"
    "v_mfma_f32_16x16x32_bf16 v[64:67], v[176:179], v[24:27], v[64:67]\n"
    "s_waitcnt lgkmcnt(1)\n"
    "v_mfma_f32_16x16x32_bf16 v[68:71], v[180:183], v[24:27], v[68:71]\n"
    "ds_read_b128 v[24:27], v0 offset:17408\n"
    "v_mfma_f32_16x16x32_bf16 v[32:35], v[184:187], v[16:19], v[32:35]\n"
    "v_mfma_f32_16x16x32_bf16 v[36:39], v[188:191], v[16:19], v[36:39]\n"
    "v_mfma_f32_16x16x32_bf16 v[48:51], v[16:19], v[192:195], v[48:51]\n"
    "v_mfma_f32_16x16x32_bf16 v[56:59], v[16:19], v[196:199], v[56:59]\n"
    "v_mfma_f32_16x16x32_bf16 v[64:67], v[200:203], v[16:19], v[64:67]\n"
    "s_waitcnt lgkmcnt(1)\n"
    "v_mfma_f32_16x16x32_bf16 v[68:71], v[204:207], v[16:19], v[68:71]\n"
    "v_mfma_f32_16x16x32_bf16 v[32:35], v[208:211], v[20:23], v[32:35]\n"
    "v_mfma_f32_16x16x32_bf16 v[36:39], v[212:215], v[20:23], v[36:39]\n"
    "s_waitcnt vmcnt(1)\n"
    "v_add_u32 v13, v5, v3\n"
    "v_mfma_f32_16x16x32_bf16 v[48:51], v[20:23], v[216:219], v[48:51]\n"
    "v_cmp_gt_i32 vcc, s71, v13\n"
    "v_mfma_f32_16x16x32_bf16 v[56:59], v[20:23], v[220:223], v[56:59]\n"
    "v_mfma_f32_16x16x32_bf16 v[64:67], v[224:227], v[20:23], v[64:67]\n"
    "s_waitcnt lgkmcnt(0)\n"
    "v_mfma_f32_16x16x32_bf16 v[68:71], v[228:231], v[20:23], v[68:71]\n"
    "s_cbranch_vccz .LgP_dma0_102_%=\n"
    "v_mfma_f32_16x16x32_bf16 v[32:35], v[232:235], v[24:27], v[32:35]\n"
    "s_or_b64 exec, vcc, s[62:63]\n"
    "buffer_atomic_smax v5, v2, s[36:39], 0 offen sc0 sc1\n"
    "s_mov_b64 exec, -1\n"
    "v_mfma_f32_16x16x32_bf16 v[36:39], v[236:239], v[24:27], v[36:39]\n"
    "v_mfma_f32_16x16x32_bf16 v[48:51], v[24:27], v[240:243], v[48:51]\n"
    "v_mfma_f32_16x16x32_bf16 v[56:59], v[24:27], v[244:247], v[56:59]\n"
    "v_mfma_f32_16x16x32_bf16 v[64:67], v[248:251], v[24:27], v[64:67]\n"
    "v_mfma_f32_16x16x32_bf16 v[68:71], v[252:255], v[24:27], v[68:71]\n"
    "v_cvt_pk_bf16_f32 v32, v32, v33\n"
    "v_cvt_pk_bf16_f32 v33, v34, v35\n"
    "v_cvt_pk_bf16_f32 v34, v36, v37\n"
    "v_cvt_pk_bf16_f32 v35, v38, v39\n"
    "buffer_store_dwordx4 v[32:35], v1, s[36:39], s79 offen sc1\n"
    "v_cvt_pk_bf16_f32 v28, v48, v49\n"
    "v_cvt_pk_bf16_f32 v29, v50, v51\n"
    "v_cvt_pk_bf16_f32 v30, v56, v57\n"
    "v_cvt_pk_bf16_f32 v31, v58, v59\n"
    "v_cvt_pk_bf16_f32 v64, v64, v65\n"
    "v_cvt_pk_bf16_f32 v65, v66, v67\n"
    "v_cvt_pk_bf16_f32 v66, v68, v69\n"
    "v_cvt_pk_bf16_f32 v67, v70, v71\n"
    "buffer_store_dwordx4 v[64:67], v1, s[36:39], s73 offen sc1\n"
    "v_mov_b32 v12, s68\n"
    "s_mov_b32 s73, 0x80000000\n"
    "s_mov_b32 s79, 0x80000000\n"
    "s_mov_b32 s80, 0x80000000\n"
    "s_mov_b32 s68, s71\n"
    "s_waitcnt vmcnt(3)\n"
    "v_add_u32 v13, v11, v3\n"
    "v_cmp_gt_i32 vcc, s71, v13\n"
    "s_cbranch_vccnz .LgP_idle0_%=\n"
    "s_add_u32 m0, s59, 20480\n"
    "s_nop 0\n"
    "buffer_load_dwordx4 v1, s[36:39], s76 offen sc1 lds\n"
    "s_add_u32 m0, s59, 24576\n"
    "s_add_u32 s76, s76, 16384\n"
    "buffer_load_dwordx4 v1, s[36:39], s76 offen sc1 lds\n"
    "s_add_u32 m0, s59, 28672\n"
    "s_add_u32 s76, s76, 16384\n"
    "buffer_load_dwordx4 v1, s[36:39], s76 offen sc1 lds\n"
    "s_add_u32 m0, s59, 32768\n"
    "s_add_u32 s76, s76, 16384\n"
    "buffer_load_dwordx4 v1, s[36:39], s76 offen sc1 lds\n"
    "s_add_u32 m0, s59, 36864\n"
    "s_add_u32 s76, s76, 16384\n"
    "buffer_load_dwordx4 v8, s[36:39], s76 offen sc1 lds\n"
    "s_waitcnt vmcnt(5)\n"
    "buffer_atomic_smax v12, v14, s[36:39], 0 offen sc1\n"
    "s_mov_b32 s69, 0x80000000\n"
    "s_mov_b32 s70, 0x80000000\n"
    "v_mov_b32 v7, v12\n"
    "s_waitcnt vmcnt(8)\n"
    "s_add_u32 s71, s68, 1\n"
    "v_add_u32 v13, v11, v3\n"
    "v_cmp_gt_i32 vcc, s71, v13\n"
    "s_nop 0\n"
    "s_or_b64 exec, vcc, s[62:63]\n"
    "buffer_atomic_smax v11, v2, s[36:39], 0 offen sc0 sc1\n"
    "s_mov_b64 exec, -1\n"
    "s_waitcnt vmcnt(2)\n"
    "s_branch .LgP_bar1_%=\n"
    ".LgP_dma0_30_%=:\n"
    "v_mfma_f32_16x16x32_bf16 v[32:35], a[120:123], v[24:27], v[32:35]\n"
    "s_add_u32 m0, s59, 20480\n"
    "v_mfma_f32_16x16x32_bf16 v[36:39], a[124:127], v[24:27], v[36:39]\n"
    "buffer_load_dwordx4 v1, s[36:39], s76 offen sc1 lds\n"
    "v_mfma_f32_16x16x32_bf16 v[48:51], v[24:27], a[128:131], v[48:51]\n"
    "s_add_u32 s76, s76, 16384\n"
    "s_add_u32 m0, s59, 24576\n"
    "v_mfma_f32_16x16x32_bf16 v[56:59], v[24:27], a[132:135], v[56:59]\n"
    "buffer_load_dwordx4 v1, s[36:39], s76 offen sc1 lds\n"
    "v_mfma_f32_16x16x32_bf16 v[64:67], a[136:139], v[24:27], v[64:67]\n"
    "s_waitcnt lgkmcnt(1)\n"
    "v_mfma_f32_16x16x32_bf16 v[68:71], a[140:143], v[24:27], v[68:71]\n"
    "ds_read_b128 v[24:27], v0 offset:8192\n"
    "v_mfma_f32_16x16x32_bf16 v[32:35], a[144:147], v[16:19], v[32:35]\n"
    "s_add_u32 s76, s76, 16384\n"
    "s_add_u32 m0, s59, 28672\n"
    "v_mfma_f32_16x16x32_bf16 v[36:39], a[148:151], v[16:19], v[36:39]\n"
    "buffer_load_dwordx4 v1, s[36:39], s76 offen sc1 lds\n"
    "v_mfma_f32_16x16x32_bf16 v[48:51], v[16:19], a[152:155], v[48:51]\n"
    "v_mfma_f32_16x16x32_bf16 v[56:59], v[16:19], a[156:159], v[56:59]\n"
    "v_mfma_f32_16x16x32_bf16 v[64:67], a[160:163], v[16:19], v[64:67]\n"
    "s_waitcnt lgkmcnt(1)\n"
    "s_add_u32 s76, s76, 16384\n"
    "s_add_u32 m0, s59, 32768\n"
    "v_mfma_f32_16x16x32_bf16 v[68:71], a[164:167], v[16:19], v[68:71]\n"
    "ds_read_b128 v[16:19], v0 offset:9216\n"
    "buffer_load_dwordx4 v1, s[36:39], s76 offen sc1 lds\n"
    "v_mfma_f32_16x16x32_bf16 v[32:35], a[168:171], v[20:23], v[32:35]\n"
    "v_mfma_f32_16x16x32_bf16 v[36:39], a[172:175], v[20:23], v[36:39]\n"
    "v_mfma_f32_16x16x32_bf16 v[48:51], v[20:23], a[176:179], v[48:51]\n"
    "s_add_u32 s76, s76, 16384\n"
    "s_add_u32 m0, s59, 36864\n"
    "v_mfma_f32_16x16x32_bf16 v[56:59], v[20:23], a[180:183], v[56:59]\n"
    "buffer_load_dwordx4 v8, s[36:39], s76 offen sc1 lds\n"
    "v_mfma_f32_16x16x32_bf16 v[64:67], a[184:187], v[20:23], v[64:67]\n"
    "s_waitcnt lgkmcnt(1)\n"
    "v_mfma_f32_16x16x32_bf16 v[68:71], a[188:191], v[20:23], v[68:71]\n"
    "ds_read_b128 v[20:23], v0 offset:10240\n"
    "v_mfma_f32_16x16x32_bf16 v[32:35], a[192:195], v[24:27], v[32:35]\n"
    "v_mfma_f32_16x16x32_bf16 v[36:39], a[196:199], v[24:27], v[36:39]\n"
    "v_mfma_f32_16x16x32_bf16 v[48:51], v[24:27], a[200:203], v[48:51]\n"
    "v_mfma_f32_16x16x32_bf16 v[56:59], v[24:27], a[204:207], v[56:59]\n"
    "v_mfma_f32_16x16x32_bf16 v[64:67], a[208:211], v[24:27], v[64:67]\n"
    "s_waitcnt lgkmcnt(1)\n"
    "v_mfma_f32_16x16x32_bf16 v[68:71], a[212:215], v[24:27], v[68:71]\n"
    "ds_read_b128 v[24:27], v0 offset:11264\n"
    "v_mfma_f32_16x16x32_bf16 v[32:35], a[216:219], v[16:19], v[32:35]\n"
    "v_mfma_f32_16x16x32_bf16 v[36:39], a[220:223], v[16:19], v[36:39]\n"
    "v_mfma_f32_16x16x32_bf16 v[48:51], v[16:19], a[224:227], v[48:51]\n"
    "v_mfma_f32_16x16x32_bf16 v[56:59], v[16:19], a[228:231], v[56:59]\n"
    "v_mfma_f32_16x16x32_bf16 v[64:67], a[232:235], v[16:19], v[64:67]\n"
    "s_waitcnt lgkmcnt(1)\n"
    "s_waitcnt vmcnt(5)\n"
    "buffer_atomic_smax v12, v10, s[36:39], s70 offen sc1\n"
    "v_mfma_f32_16x16x32_bf16 v[68:71], a[236:239], v[16:19], v[68:71]\n"
    "ds_read_b128 v[16:19], v0 offset:12288\n"
    "v_mfma_f32_16x16x32_bf16 v[32:35], a[240:243], v[20:23], v[32:35]\n"
    "v_mfma_f32_16x16x32_bf16 v[36:39], a[244:247], v[20:23], v[36:39]\n"
    "v_mfma_f32_16x16x32_bf16 v[48:51], v[20:23], a[248:251], v[48:51]\n"
    "v_mfma_f32_16x16x32_bf16 v[56:59], v[20:23], a[252:255], v[56:59]\n"
    "v_mfma_f32_16x16x32_bf16 v[64:67], v[80:83], v[20:23], v[64:67]\n"
    "s_waitcnt lgkmcnt(1)\n"
    "v_mfma_f32_16x16x32_bf16 v[68:71], v[84:87], v[20:23], v[68:71]\n"
    "ds_read_b128 v[20:23], v0 offset:13312\n"
    "v_mfma_f32_16x16x32_bf16 v[32:35], v[88:91], v[24:27], v[32:35]\n"
    "v_mfma_f32_16x16x32_bf16 v[36:39], v[92:95], v[24:27], v[36:39]\n"
    "v_mfma_f32_16x16x32_bf16 v[48:51], v[24:27], v[96:99], v[48:51]\n"
    "v_mfma_f32_16x16x32_bf16 v[56:59], v[24:27], v[100:103], v[56:59]\n"
    "v_mfma_f32_16x16x32_bf16 v[64:67], v[104:107], v[24:27], v[64:67]\n"
    "s_waitcnt lgkmcnt(1)\n"
    "v_mfma_f32_16x16x32_bf16 v[68:71], v[108:111], v[24:27], v[68:71]\n"
    "ds_read_b128 v[24:27], v0 offset:14336\n"
    "v_mfma_f32_16x16x32_bf16 v[32:35], v[112:115], v[16:19], v[32:35]\n"
    "v_mfma_f32_16x16x32_bf16 v[36:39], v[116:119], v[16:19], v[36:39]\n"
    "v_mfma_f32_16x16x32_bf16 v[48:51], v[16:19], v[120:123], v[48:51]\n"
    "v_mfma_f32_16x16x32_bf16 v[56:59], v[16:19], v[124:127], v[56:59]\n"
    "v_mfma_f32_16x16x32_bf16 v[64:67], v[128:131], v[16:19], v[64:67]\n"
    "s_waitcnt lgkmcnt(1)\n"
    "v_mfma_f32_16x16x32_bf16 v[68:71], v[132:135], v[16:19], v[68:71]\n"
    "ds_read_b128 v[16:19], v0 offset:15360\n"
    "s_branch .LgP_join0_%=\n"
    ".LgP_dma0_48_%=:\n"
    "v_mfma_f32_16x16x32_bf16 v[32:35], a[192:195], v[24:27], v[32:35]\n"
    "s_add_u32 m0, s59, 20480\n"
    "v_mfma_f32_16x16x32_bf16 v[36:39], a[196:199], v[24:27], v[36:39]\n"
    "buffer_load_dwordx4 v1, s[36:39], s76 offen sc1 lds\n"
    "v_mfma_f32_16x16x32_bf16 v[48:51], v[24:27], a[200:203], v[48:51]\n"
    "s_add_u32 s76, s76, 16384\n"
    "s_add_u32 m0, s59, 24576\n"
    "v_mfma_f32_16x16x32_bf16 v[56:59], v[24:27], a[204:207], v[56:59]\n"
    "buffer_load_dwordx4 v1, s[36:39], s76 offen sc1 lds\n"
    "v_mfma_f32_16x16x32_bf16 v[64:67], a[208:211], v[24:27], v[64:67]\n"
    "s_waitcnt lgkmcnt(1)\n"
    "v_mfma_f32_16x16x32_bf16 v[68:71], a[212:215], v[24:27], v[68:71]\n"
    "ds_read_b128 v[24:27], v0 offset:11264\n"
    "v_mfma_f32_16x16x32_bf16 v[32:35], a[216:219], v[16:19], v[32:35]\n"
    "s_add_u32 s76, s76, 16384\n"
    "s_add_u32 m0, s59, 28672\n"
    "v_mfma_f32_16x16x32_bf16 v[36:39], a[220:223], v[16:19], v[36:39]\n"
    "buffer_load_dwordx4 v1, s[36:39], s76 offen sc1 lds\n"
    "v_mfma_f32_16x16x32_bf16 v[48:51], v[16:19], a[224:227], v[48:51]\n"
    "v_mfma_f32_16x16x32_bf16 v[56:59], v[16:19], a[228:231], v[56:59]\n"
    "v_mfma_f32_16x16x32_bf16 v[64:67], a[232:235], v[16:19], v[64:67]\n"
    "s_waitcnt lgkmcnt(1)\n"
    "s_waitcnt vmcnt(4)\n"
    "buffer_atomic_smax v12, v10, s[36:39], s70 offen sc1\n"
    "v_mfma_f32_16x16x32_bf16 v[68:71], a[236:239], v[16:19], v[68:71]\n"
    "ds_read_b128 v[16:19], v0 offset:12288\n"
    "s_add_u32 s76, s76, 16384\n"
    "v_mfma_f32_16x16x32_bf16 v[32:35], a[240:243], v[20:23], v[32:35]\n"
    "s_add_u32 m0, s59, 32768\n"
    "v_mfma_f32_16x16x32_bf16 v[36:39], a[244:247], v[20:23], v[36:39]\n"
    "buffer_load_dwordx4 v1, s[36:39], s76 offen sc1 lds\n"
    "v_mfma_f32_16x16x32_bf16 v[48:51], v[20:23], a[248:251], v[48:51]\n"
    "s_add_u32 s76, s76, 16384\n"
    "s_add_u32 m0, s59, 36864\n"
    "v_mfma_f32_16x16x32_bf16 v[56:59], v[20:23], a[252:255], v[56:59]\n"
    "buffer_load_dwordx4 v8, s[36:39], s76 offen sc1 lds\n"
    "v_mfma_f32_16x16x32_bf16 v[64:67], v[80:83], v[20:23], v[64:67]\n"
    "s_waitcnt lgkmcnt(1)\n"
    "v_mfma_f32_16x16x32_bf16 v[68:71], v[84:87], v[20:23], v[68:71]\n"
    "ds_read_b128 v[20:23], v0 offset:13312\n"
    "v_mfma_f32_16x16x32_bf16 v[32:35], v[88:91], v[24:27], v[32:35]\n"
    "v_mfma_f32_16x16x32_bf16 v[36:39], v[92:95], v[24:27], v[36:39]\n"
    "v_mfma_f32_16x16x32_bf16 v[48:51], v[24:27], v[96:99], v[48:51]\n"
    "v_mfma_f32_16x16x32_bf16 v[56:59], v[24:27], v[100:103], v[56:59]\n"
    "v_mfma_f32_16x16x32_bf16 v[64:67], v[104:107], v[24:27], v[64:67]\n"
    "s_waitcnt lgkmcnt(1)\n"
    "v_mfma_f32_16x16x32_bf16 v[68:71], v[108:111], v[24:27], v[68:71]\n"
    "ds_read_b128 v[24:27], v0 offset:14336\n"
    "v_mfma_f32_16x16x32_bf16 v[32:35], v[112:115], v[16:19], v[32:35]\n"
    "v_mfma_f32_16x16x32_bf16 v[36:39], v[116:119], v[16:19], v[36:39]\n"
    "v_mfma_f32_16x16x32_bf16 v[48:51], v[16:19], v[120:123], v[48:51]\n"
    "v_mfma_f32_16x16x32_bf16 v[56:59], v[16:19], v[124:127], v[56:59]\n"
    "v_mfma_f32_16x16x32_bf16 v[64:67], v[128:131], v[16:19], v[64:67]\n"
    "s_waitcnt lgkmcnt(1)\n"
    "v_mfma_f32_16x16x32_bf16 v[68:71], v[132:135], v[16:19], v[68:71]\n"
    "ds_read_b128 v[16:19], v0 offset:15360\n"
    "s_branch .LgP_join0_%=\n"
    ".LgP_dma0_66_%=:\n"
    "v_mfma_f32_16x16x32_bf16 v[32:35], v[88:91], v[24:27], v[32:35]\n"
    "s_add_u32 m0, s59, 20480\n"
    "v_mfma_f32_16x16x32_bf16 v[36:39], v[92:95], v[24:27], v[36:39]\n"
    "buffer_load_dwordx4 v1, s[36:39], s76 offen sc1 lds\n"
    "v_mfma_f32_16x16x32_bf16 v[48:51], v[24:27], v[96:99], v[48:51]\n"
    "s_add_u32 s76, s76, 16384\n"
    "s_add_u32 m0, s59, 24576\n"
    "v_mfma_f32_16x16x32_bf16 v[56:59], v[24:27], v[100:103], v[56:59]\n"
    "buffer_load_dwordx4 v1, s[36:39], s76 offen sc1 lds\n"
    "v_mfma_f32_16x16x32_bf16 v[64:67], v[104:107], v[24:27], v[64:67]\n"
    "s_waitcnt lgkmcnt(1)\n"
    "v_mfma_f32_16x16x32_bf16 v[68:71], v[108:111], v[24:27], v[68:71]\n"
    "ds_read_b128 v[24:27], v0 offset:14336\n"
    "v_mfma_f32_16x16x32_bf16 v[32:35], v[112:115], v[16:19], v[32:35]\n"
    "s_add_u32 s76, s76, 16384\n"
    "s_add_u32 m0, s59, 28672\n"
    "v_mfma_f32_16x16x32_bf16 v[36:39], v[116:119], v[16:19], v[36:39]\n"
    "buffer_load_dwordx4 v1, s[36:39], s76 offen sc1 lds\n"
    "v_mfma_f32_16x16x32_bf16 v[48:51], v[16:19], v[120:123], v[48:51]\n"
    "v_mfma_f32_16x16x32_bf16 v[56:59], v[16:19], v[124:127], v[56:59]\n"
    "v_mfma_f32_16x16x32_bf16 v[64:67], v[128:131], v[16:19], v[64:67]\n"
    "s_waitcnt lgkmcnt(1)\n"
    "s_add_u32 s76, s76, 16384\n"
    "s_add_u32 m0, s59, 32768\n"
    "v_mfma_f32_16x16x32_bf16 v[68:71], v[132:135], v[16:19], v[68:71]\n"
    "ds_read_b128 v[16:19], v0 offset:15360\n"
    "buffer_load_dwordx4 v1, s[36:39], s76 offen sc1 lds\n"
    "v_mfma_f32_16x16x32_bf16 v[32:35], v[136:139], v[20:23], v[32:35]\n"
    "v_mfma_f32_16x16x32_bf16 v[36:39], v[140:143], v[20:23], v[36:39]\n"
    "v_mfma_f32_16x16x32_bf16 v[48:51], v[20:23], v[144:147], v[48:51]\n"
    "s_add_u32 s76, s76, 16384\n"
    "s_add_u32 m0, s59, 36864\n"
    "v_mfma_f32_16x16x32_bf16 v[56:59], v[20:23], v[148:151], v[56:59]\n"
    "buffer_load_dwordx4 v8, s[36:39], s76 offen sc1 lds\n"
    "v_mfma_f32_16x16x32_bf16 v[64:67], v[152:155], v[20:23], v[64:67]\n"
    "s_waitcnt lgkmcnt(1)\n"
    "v_mfma_f32_16x16x32_bf16 v[68:71], v[156:159], v[20:23], v[68:71]\n"
    "ds_read_b128 v[20:23], v0 offset:16384\n"
    "v_mfma_f32_16x16x32_bf16 v[32:35], v[160:163], v[24:27], v[32:35]\n"
    "v_mfma_f32_16x16x32_bf16 v[36:39], v[164:167], v[24:27], v[36:39]\n"
    "v_mfma_f32_16x16x32_bf16 v[48:51], v[24:27], v[168:171], v[48:51]\n"
    "v_mfma_f32_16x16x32_bf16 v[56:59], v[24:27], v[172:175], v[56:59]\n"
    "v_mfma_f32_16x16x32_bf16 v[64:67], v[176:179], v[24:27], v[64:67]\n"
    "s_waitcnt lgkmcnt(1)\n"
    "v_mfma_f32_16x16x32_bf16 v[68:71], v[180:183], v[24:27], v[68:71]\n"
    "ds_read_b128 v[24:27], v0 offset:17408\n"
    "v_mfma_f32_16x16x32_bf16 v[32:35], v[184:187], v[16:19], v[32:35]\n"
    "v_mfma_f32_16x16x32_bf16 v[36:39], v[188:191], v[16:19], v[36:39]\n"
    "v_mfma_f32_16x16x32_bf16 v[48:51], v[16:19], v[192:195], v[48:51]\n"
    "v_mfma_f32_16x16x32_bf16 v[56:59], v[16:19], v[196:199], v[56:59]\n"
    "v_mfma_f32_16x16x32_bf16 v[64:67], v[200:203], v[16:19], v[64:67]\n"
    "s_waitcnt lgkmcnt(1)\n"
    "v_mfma_f32_16x16x32_bf16 v[68:71], v[204:207], v[16:19], v[68:71]\n"
    "v_mfma_f32_16x16x32_bf16 v[32:35], v[208:211], v[20:23], v[32:35]\n"
    "v_mfma_f32_16x16x32_bf16 v[36:39], v[212:215], v[20:23], v[36:39]\n"
    "v_mfma_f32_16x16x32_bf16 v[48:51], v[20:23], v[216:219], v[48:51]\n"
    "v_mfma_f32_16x16x32_bf16 v[56:59], v[20:23], v[220:223], v[56:59]\n"
    "v_mfma_f32_16x16x32_bf16 v[64:67], v[224:227], v[20:23], v[64:67]\n"
    "s_waitcnt lgkmcnt(0)\n"
    "v_mfma_f32_16x16x32_bf16 v[68:71], v[228:231], v[20:23], v[68:71]\n"
    "v_mfma_f32_16x16x32_bf16 v[32:35], v[232:235], v[24:27], v[32:35]\n"
    "v_mfma_f32_16x16x32_bf16 v[36:39], v[236:239], v[24:27], v[36:39]\n"
    "v_mfma_f32_16x16x32_bf16 v[48:51], v[24:27], v[240:243], v[48:51]\n"
    "v_mfma_f32_16x16x32_bf16 v[56:59], v[24:27], v[244:247], v[56:59]\n"
    "v_mfma_f32_16x16x32_bf16 v[64:67], v[248:251], v[24:27], v[64:67]\n"
    "v_mfma_f32_16x16x32_bf16 v[68:71], v[252:255], v[24:27], v[68:71]\n"
    "v_cvt_pk_bf16_f32 v32, v32, v33\n"
    "v_cvt_pk_bf16_f32 v33, v34, v35\n"
    "v_cvt_pk_bf16_f32 v34, v36, v37\n"
    "v_cvt_pk_bf16_f32 v35, v38, v39\n"
    "buffer_store_dwordx4 v[32:35], v1, s[36:39], s79 offen sc1\n"
    "v_cvt_pk_bf16_f32 v28, v48, v49\n"
    "v_cvt_pk_bf16_f32 v29, v50, v51\n"
    "v_cvt_pk_bf16_f32 v30, v56, v57\n"
    "v_cvt_pk_bf16_f32 v31, v58, v59\n"
    "v_cvt_pk_bf16_f32 v64, v64, v65\n"
    "v_cvt_pk_bf16_f32 v65, v66, v67\n"
    "v_cvt_pk_bf16_f32 v66, v68, v69\n"
    "v_cvt_pk_bf16_f32 v67, v70, v71\n"
    "buffer_store_dwordx4 v[64:67], v1, s[36:39], s73 offen sc1\n"
    "v_mov_b32 v12, s68\n"
    "s_mov_b32 s73, 0x80000000\n"
    "s_mov_b32 s79, 0x80000000\n"
    "s_mov_b32 s80, 0x80000000\n"
    "s_mov_b32 s68, s71\n"
    "s_mov_b32 s69, 0\n"
    "s_mov_b32 s70, 0x80000000\n"
    "v_mov_b32 v7, v12\n"
    "s_waitcnt vmcnt(8)\n"
    "s_add_u32 s71, s68, 1\n"
    "v_add_u32 v13, v11, v3\n"
    "v_cmp_gt_i32 vcc, s71, v13\n"
    "s_nop 0\n"
    "s_or_b64 exec, vcc, s[62:63]\n"
    "buffer_atomic_smax v11, v2, s[36:39], 0 offen sc0 sc1\n"
    "s_mov_b64 exec, -1\n"
    "s_waitcnt vmcnt(3)\n"
    "s_branch .LgP_bar1_%=\n"
    ".LgP_dma0_84_%=:\n"
    "v_mfma_f32_16x16x32_bf16 v[32:35], v[160:163], v[24:27], v[32:35]\n"
    "s_add_u32 m0, s59, 20480\n"
    "v_mfma_f32_16x16x32_bf16 v[36:39], v[164:167], v[24:27], v[36:39]\n"
    "buffer_load_dwordx4 v1, s[36:39], s76 offen sc1 lds\n"
    "v_mfma_f32_16x16x32_bf16 v[48:51], v[24:27], v[168:171], v[48:51]\n"
    "s_add_u32 s76, s76, 16384\n"
    "s_add_u32 m0, s59, 24576\n"
    "v_mfma_f32_16x16x32_bf16 v[56:59], v[24:27], v[172:175], v[56:59]\n"
    "buffer_load_dwordx4 v1, s[36:39], s76 offen sc1 lds\n"
    "v_mfma_f32_16x16x32_bf16 v[64:67], v[176:179], v[24:27], v[64:67]\n"
    "s_waitcnt lgkmcnt(1)\n"
    "v_mfma_f32_16x16x32_bf16 v[68:71], v[180:183], v[24:27], v[68:71]\n"
    "ds_read_b128 v[24:27], v0 offset:17408\n"
    "v_mfma_f32_16x16x32_bf16 v[32:35], v[184:187], v[16:19], v[32:35]\n"
    "s_add_u32 s76, s76, 16384\n"
    "s_add_u32 m0, s59, 28672\n"
    "v_mfma_f32_16x16x32_bf16 v[36:39], v[188:191], v[16:19], v[36:39]\n"
    "buffer_load_dwordx4 v1, s[36:39], s76 offen sc1 lds\n"
    "v_mfma_f32_16x16x32_bf16 v[48:51], v[16:19], v[192:195], v[48:51]\n"
    "v_mfma_f32_16x16x32_bf16 v[56:59], v[16:19], v[196:199], v[56:59]\n"
    "v_mfma_f32_16x16x32_bf16 v[64:67], v[200:203], v[16:19], v[64:67]\n"
    "s_waitcnt lgkmcnt(1)\n"
    "s_add_u32 s76, s76, 16384\n"
    "s_add_u32 m0, s59, 32768\n"
    "v_mfma_f32_16x16x32_bf16 v[68:71], v[204:207], v[16:19], v[68:71]\n"
    "buffer_load_dwordx4 v1, s[36:39], s76 offen sc1 lds\n"
    "v_mfma_f32_16x16x32_bf16 v[32:35], v[208:211], v[20:23], v[32:35]\n"
    "v_mfma_f32_16x16x32_bf16 v[36:39], v[212:215], v[20:23], v[36:39]\n"
    "v_mfma_f32_16x16x32_bf16 v[48:51], v[20:23], v[216:219], v[48:51]\n"
    "s_add_u32 s76, s76, 16384\n"
    "s_add_u32 m0, s59, 36864\n"
    "v_mfma_f32_16x16x32_bf16 v[56:59], v[20:23], v[220:223], v[56:59]\n"
    "buffer_load_dwordx4 v8, s[36:39], s76 offen sc1 lds\n"
    "v_mfma_f32_16x16x32_bf16 v[64:67], v[224:227], v[20:23], v[64:67]\n"
    "s_waitcnt lgkmcnt(0)\n"
    "v_mfma_f32_16x16x32_bf16 v[68:71], v[228:231], v[20:23], v[68:71]\n"
    "v_mfma_f32_16x16x32_bf16 v[32:35], v[232:235], v[24:27], v[32:35]\n"
    "v_mfma_f32_16x16x32_bf16 v[36:39], v[236:239], v[24:27], v[36:39]\n"
    "v_mfma_f32_16x16x32_bf16 v[48:51], v[24:27], v[240:243], v[48:51]\n"
    "v_mfma_f32_16x16x32_bf16 v[56:59], v[24:27], v[244:247], v[56:59]\n"
    "v_mfma_f32_16x16x32_bf16 v[64:67], v[248:251], v[24:27], v[64:67]\n"
    "v_mfma_f32_16x16x32_bf16 v[68:71], v[252:255], v[24:27], v[68:71]\n"
    "v_cvt_pk_bf16_f32 v32, v32, v33\n"
    "v_cvt_pk_bf16_f32 v33, v34, v35\n"
    "v_cvt_pk_bf16_f32 v34, v36, v37\n"
    "v_cvt_pk_bf16_f32 v35, v38, v39\n"
    "buffer_store_dwordx4 v[32:35], v1, s[36:39], s79 offen sc1\n"
    "v_cvt_pk_bf16_f32 v28, v48, v49\n"
    "v_cvt_pk_bf16_f32 v29, v50, v51\n"
    "v_cvt_pk_bf16_f32 v30, v56, v57\n"
    "v_cvt_pk_bf16_f32 v31, v58, v59\n"
    "v_cvt_pk_bf16_f32 v64, v64, v65\n"
    "v_cvt_pk_bf16_f32 v65, v66, v67\n"
    "v_cvt_pk_bf16_f32 v66, v68, v69\n"
    "v_cvt_pk_bf16_f32 v67, v70, v71\n"
    "buffer_store_dwordx4 v[64:67], v1, s[36:39], s73 offen sc1\n"
    "v_mov_b32 v12, s68\n"
    "s_mov_b32 s73, 0x80000000\n"
    "s_mov_b32 s79, 0x80000000\n"
    "s_mov_b32 s80, 0x80000000\n"
    "s_mov_b32 s68, s71\n"
    "s_mov_b32 s69, 0\n"
    "s_mov_b32 s70, 0x80000000\n"
    "v_mov_b32 v7, v12\n"
    "s_waitcnt vmcnt(7)\n"
    "s_add_u32 s71, s68, 1\n"
    "v_add_u32 v13, v11, v3\n"
    "v_cmp_gt_i32 vcc, s71, v13\n"
    "s_nop 0\n"
    "s_or_b64 exec, vcc, s[62:63]\n"
    "buffer_atomic_smax v11, v2, s[36:39], 0 offen sc0 sc1\n"
    "s_mov_b64 exec, -1\n"
    "s_waitcnt vmcnt(3)\n"
    "s_branch .LgP_bar1_%=\n"
    ".LgP_dma0_102_%=:\n"
    "v_mfma_f32_16x16x32_bf16 v[32:35], v[232:235], v[24:27], v[32:35]\n"
    "s_add_u32 m0, s59, 20480\n"
    "v_mfma_f32_16x16x32_bf16 v[36:39], v[236:239], v[24:27], v[36:39]\n"
    "buffer_load_dwordx4 v1, s[36:39], s76 offen sc1 lds\n"
    "v_mfma_f32_16x16x32_bf16 v[48:51], v[24:27], v[240:243], v[48:51]\n"
    "s_add_u32 s76, s76, 16384\n"
    "s_add_u32 m0, s59, 24576\n"
    "v_mfma_f32_16x16x32_bf16 v[56:59], v[24:27], v[244:247], v[56:59]\n"
    "buffer_load_dwordx4 v1, s[36:39], s76 offen sc1 lds\n"
    "v_mfma_f32_16x16x32_bf16 v[64:67], v[248:251], v[24:27], v[64:67]\n"
    "v_mfma_f32_16x16x32_bf16 v[68:71], v[252:255], v[24:27], v[68:71]\n"
    "v_cvt_pk_bf16_f32 v32, v32, v33\n"
    "v_cvt_pk_bf16_f32 v33, v34, v35\n"
    "v_cvt_pk_bf16_f32 v34, v36, v37\n"
    "v_cvt_pk_bf16_f32 v35, v38, v39\n"
    "buffer_store_dwordx4 v[32:35], v1, s[36:39], s79 offen sc1\n"
    "v_cvt_pk_bf16_f32 v28, v48, v49\n"
    "v_cvt_pk_bf16_f32 v29, v50, v51\n"
    "v_cvt_pk_bf16_f32 v30, v56, v57\n"
    "v_cvt_pk_bf16_f32 v31, v58, v59\n"
    "v_cvt_pk_bf16_f32 v64, v64, v65\n"
    "v_cvt_pk_bf16_f32 v65, v66, v67\n"
    "v_cvt_pk_bf16_f32 v66, v68, v69\n"
    "v_cvt_pk_bf16_f32 v67, v70, v71\n"
    "buffer_store_dwordx4 v[64:67], v1, s[36:39], s73 offen sc1\n"
    "v_mov_b32 v12, s68\n"
    "s_mov_b32 s73, 0x80000000\n"
    "s_mov_b32 s79, 0x80000000\n"
    "s_mov_b32 s80, 0x80000000\n"
    "s_mov_b32 s68, s71\n"
    "s_add_u32 m0, s59, 28672\n"
    "s_add_u32 s76, s76, 16384\n"
    "buffer_load_dwordx4 v1, s[36:39], s76 offen sc1 lds\n"
    "s_add_u32 m0, s59, 32768\n"
    "s_add_u32 s76, s76, 16384\n"
    "buffer_load_dwordx4 v1, s[36:39], s76 offen sc1 lds\n"
    "s_add_u32 m0, s59, 36864\n"
    "s_add_u32 s76, s76, 16384\n"
    "buffer_load_dwordx4 v8, s[36:39], s76 offen sc1 lds\n"
    "s_waitcnt vmcnt(3)\n"
    "buffer_atomic_smax v12, v14, s[36:39], 0 offen sc1\n"
    "s_mov_b32 s69, 0x80000000\n"
    "s_mov_b32 s70, 0x80000000\n"
    "v_mov_b32 v7, v12\n"
    "s_waitcnt vmcnt(8)\n"
    "s_add_u32 s71, s68, 1\n"
    "v_add_u32 v13, v11, v3\n"
    "v_cmp_gt_i32 vcc, s71, v13\n"
    "s_nop 0\n"
    "s_or_b64 exec, vcc, s[62:63]\n"
    "buffer_atomic_smax v11, v2, s[36:39], 0 offen sc0 sc1\n"
    "s_mov_b64 exec, -1\n"
    "s_waitcnt vmcnt(2)\n"
    "s_branch .LgP_bar1_%=\n"
    ".LgP_idle0_%=:\n"
    "s_or_b64 exec, vcc, s[62:63]\n"
    "buffer_atomic_smax v11, v2, s[36:39], 0 offen sc0 sc1\n"
    "s_mov_b64 exec, -1\n"
    "s_waitcnt vmcnt(1)\n"
    "buffer_atomic_smax v12, v14, s[36:39], 0 offen sc1\n"
    "s_branch .LgP_block0_0_%=\n"
    ".LgP_block0_%=:\n"
    "v_add_u32 v13, v11, v3\n"
    "v_cmp_gt_i32 vcc, s71, v13\n"
    "s_nop 0\n"
    "s_or_b64 exec, vcc, s[62:63]\n"
    "buffer_atomic_smax v11, v2, s[36:39], 0 offen sc0 sc1\n"
    "s_mov_b64 exec, -1\n"
    "s_sleep 3\n"
    "s_waitcnt vmcnt(1)\n"
    ".LgP_block0_0_%=:\n"
    "v_add_u32 v13, v5, v3\n"
    "v_cmp_gt_i32 vcc, s71, v13\n"
    "s_cbranch_vccz .LgP_go0_%=\n"
    "v_add_u32 v13, v5, v3\n"
    "v_cmp_gt_i32 vcc, s71, v13\n"
    "s_nop 0\n"
    "s_or_b64 exec, vcc, s[62:63]\n"
    "buffer_atomic_smax v5, v2, s[36:39], 0 offen sc0 sc1\n"
    "s_mov_b64 exec, -1\n"
    "s_sleep 3\n"
    "s_waitcnt vmcnt(1)\n"
    ".LgP_block0_1_%=:\n"
    "v_add_u32 v13, v11, v3\n"
    "v_cmp_gt_i32 vcc, s71, v13\n"
    "s_cbranch_vccz .LgP_go0_%=\n"
    "s_sub_u32 s72, s72, 1\n"
    "s_cbranch_scc0 .LgP_block0_%=\n"
    "s_mov_b32 s72, 0\n"
    ".LgP_go0_%=:\n"
    "s_lshr_b32 s76, s71, 1\n"
    "s_and_b32 s76, s76, s54\n"
    "s_mul_i32 s76, s76, s53\n"
    "s_add_u32 s76, s76, s52\n"
    "s_add_u32 s76, s76, 1024\n"
    "s_add_u32 m0, s59, 20480\n"
    "s_nop 0\n"
    "buffer_load_dwordx4 v1, s[36:39], s76 offen sc1 lds\n"
    "s_add_u32 m0, s59, 24576\n"
    "s_add_u32 s76, s76, 16384\n"
    "buffer_load_dwordx4 v1, s[36:39], s76 offen sc1 lds\n"
    "s_add_u32 m0, s59, 28672\n"
    "s_add_u32 s76, s76, 16384\n"
    "buffer_load_dwordx4 v1, s[36:39], s76 offen sc1 lds\n"
    "s_add_u32 m0, s59, 32768\n"
    "s_add_u32 s76, s76, 16384\n"
    "buffer_load_dwordx4 v1, s[36:39], s76 offen sc1 lds\n"
    "s_add_u32 m0, s59, 36864\n"
    "s_add_u32 s76, s76, 16384\n"
    "buffer_load_dwordx4 v8, s[36:39], s76 offen sc1 lds\n"
    "s_mov_b32 s69, 0x80000000\n"
    "s_mov_b32 s70, 0x80000000\n"
    "v_mov_b32 v7, v12\n"
    "s_waitcnt vmcnt(5)\n"
    "s_add_u32 s71, s68, 1\n"
    "v_add_u32 v13, v11, v3\n"
    "v_cmp_gt_i32 vcc, s71, v13\n"
    "s_nop 0\n"
    "s_or_b64 exec, vcc, s[62:63]\n"
    "buffer_atomic_smax v11, v2, s[36:39], 0 offen sc0 sc1\n"
    "s_mov_b64 exec, -1\n"
    "s_waitcnt vmcnt(1)\n"
    "s_branch .LgP_bar1_%=\n"
    ".LgP_bar0_%=:\n"
    "s_barrier\n"
    "ds_read_b128 v[16:19], v0 offset:0\n"
    "ds_read_b128 v[20:23], v0 offset:1024\n"
    "ds_read_b128 v[24:27], v0 offset:2048\n"
    "v_add_u32 v13, v5, v3\n"
    "v_cmp_gt_i32 vcc, s71, v13\n"
    "s_nop 0\n"
    "s_or_b64 exec, vcc, s[62:63]\n"
    "buffer_atomic_smax v5, v2, s[36:39], 0 offen sc0 sc1\n"
    "s_mov_b64 exec, -1\n"
    "s_lshr_b32 s76, s71, 1\n"
    "s_and_b32 s76, s76, s54\n"
    "s_mul_i32 s76, s76, s53\n"
    "s_add_u32 s76, s76, s52\n"
    "s_add_u32 s76, s76, 1024\n"
    "s_waitcnt lgkmcnt(2)\n"
    "s_branch .LgP_head0_%=\n"
    ".LgP_poll1_%=:\n"
    "v_mfma_f32_16x16x32_bf16 v[40:43], a[48:51], v[24:27], v[40:43]\n"
    "s_or_b64 exec, vcc, s[62:63]\n"
    "buffer_atomic_smax v11, v2, s[36:39], 0 offen sc0 sc1\n"
    "s_mov_b64 exec, -1\n"
    "v_mfma_f32_16x16x32_bf16 v[44:47], a[52:55], v[24:27], v[44:47]\n"
    "buffer_store_dwordx4 v[64:67], v1, s[36:39], s73 offen sc1\n"
    "v_mfma_f32_16x16x32_bf16 v[52:55], v[24:27], a[56:59], v[52:55]\n"
    "s_lshr_b32 s73, s68, 1\n"
    "v_mfma_f32_16x16x32_bf16 v[60:63], v[24:27], a[60:63], v[60:63]\n"
    "s_and_b32 s73, s73, s57\n"
    "v_mfma_f32_16x16x32_bf16 v[72:75], a[64:67], v[24:27], v[72:75]\n"
    "s_waitcnt lgkmcnt(1)\n"
    "s_mul_i32 s73, s73, s56\n"
    "v_mfma_f32_16x16x32_bf16 v[76:79], a[68:71], v[24:27], v[76:79]\n"
    "ds_read_b128 v[24:27], v9 offset:5120\n"
    "s_add_u32 s73, s73, s55\n"
    "v_mfma_f32_16x16x32_bf16 v[40:43], a[72:75], v[16:19], v[40:43]\n"
    "s_add_u32 s79, s73, 16384\n"
    "v_mfma_f32_16x16x32_bf16 v[44:47], a[76:79], v[16:19], v[44:47]\n"
    "s_add_u32 s80, s73, 32768\n"
    "v_mfma_f32_16x16x32_bf16 v[52:55], v[16:19], a[80:83], v[52:55]\n"
    "v_mfma_f32_16x16x32_bf16 v[60:63], v[16:19], a[84:87], v[60:63]\n"
    "v_mfma_f32_16x16x32_bf16 v[72:75], a[88:91], v[16:19], v[72:75]\n"
    "s_waitcnt lgkmcnt(1)\n"
    "v_mfma_f32_16x16x32_bf16 v[76:79], a[92:95], v[16:19], v[76:79]\n"
    "ds_read_b128 v[16:19], v9 offset:6144\n"
    "v_mfma_f32_16x16x32_bf16 v[40:43], a[96:99], v[20:23], v[40:43]\n"
    "v_mfma_f32_16x16x32_bf16 v[44:47], a[100:103], v[20:23], v[44:47]\n"
    "s_waitcnt vmcnt(4)\n"
    "v_add_u32 v13, v5, v3\n"
    "v_mfma_f32_16x16x32_bf16 v[52:55], v[20:23], a[104:107], v[52:55]\n"
    "v_cmp_gt_i32 vcc, s71, v13\n"
    "v_mfma_f32_16x16x32_bf16 v[60:63], v[20:23], a[108:111], v[60:63]\n"
    "v_mfma_f32_16x16x32_bf16 v[72:75], a[112:115], v[20:23], v[72:75]\n"
    "s_waitcnt lgkmcnt(1)\n"
    "v_mfma_f32_16x16x32_bf16 v[76:79], a[116:119], v[20:23], v[76:79]\n"
    "ds_read_b128 v[20:23], v9 offset:7168\n"
    "s_cbranch_vccz .LgP_dma1_30_%=\n"
    "v_mfma_f32_16x16x32_bf16 v[40:43], a[120:123], v[24:27], v[40:43]\n"
    "s_or_b64 exec, vcc, s[62:63]\n"
    "buffer_atomic_smax v5, v2, s[36:39], 0 offen sc0 sc1\n"
    "s_mov_b64 exec, -1\n"
    "v_mfma_f32_16x16x32_bf16 v[44:47], a[124:127], v[24:27], v[44:47]\n"
    "v_mfma_f32_16x16x32_bf16 v[52:55], v[24:27], a[128:131], v[52:55]\n"
    "v_mfma_f32_16x16x32_bf16 v[60:63], v[24:27], a[132:135], v[60:63]\n"
    "v_mfma_f32_16x16x32_bf16 v[72:75], a[136:139], v[24:27], v[72:75]\n"
    "s_waitcnt lgkmcnt(1)\n"
    "v_mfma_f32_16x16x32_bf16 v[76:79], a[140:143], v[24:27], v[76:79]\n"
    "ds_read_b128 v[24:27], v9 offset:8192\n"
    "v_mfma_f32_16x16x32_bf16 v[40:43], a[144:147], v[16:19], v[40:43]\n"
    "v_mfma_f32_16x16x32_bf16 v[44:47], a[148:151], v[16:19], v[44:47]\n"
    "v_mfma_f32_16x16x32_bf16 v[52:55], v[16:19], a[152:155], v[52:55]\n"
    "v_mfma_f32_16x16x32_bf16 v[60:63], v[16:19], a[156:159], v[60:63]\n"
    "v_mfma_f32_16x16x32_bf16 v[72:75], a[160:163], v[16:19], v[72:75]\n"
    "s_waitcnt lgkmcnt(1)\n"
    "v_mfma_f32_16x16x32_bf16 v[76:79], a[164:167], v[16:19], v[76:79]\n"
    "ds_read_b128 v[16:19], v9 offset:9216\n"
    "v_mfma_f32_16x16x32_bf16 v[40:43], a[168:171], v[20:23], v[40:43]\n"
    "v_mfma_f32_16x16x32_bf16 v[44:47], a[172:175], v[20:23], v[44:47]\n"
    "s_waitcnt vmcnt(2)\n"
    "v_add_u32 v13, v11, v3\n"
    "v_mfma_f32_16x16x32_bf16 v[52:55], v[20:23], a[176:179], v[52:55]\n"
    "v_cmp_gt_i32 vcc, s71, v13\n"
    "v_mfma_f32_16x16x32_bf16 v[60:63], v[20:23], a[180:183], v[60:63]\n"
    "v_mfma_f32_16x16x32_bf16 v[72:75], a[184:187], v[20:23], v[72:75]\n"
    "s_waitcnt lgkmcnt(1)\n"
    "v_mfma_f32_16x16x32_bf16 v[76:79], a[188:191], v[20:23], v[76:79]\n"
    "ds_read_b128 v[20:23], v9 offset:10240\n"
    "s_cbranch_vccz .LgP_dma1_48_%=\n"
    "v_mfma_f32_16x16x32_bf16 v[40:43], a[192:195], v[24:27], v[40:43]\n"
    "s_or_b64 exec, vcc, s[62:63]\n"
    "buffer_atomic_smax v11, v2, s[36:39], 0 offen sc0 sc1\n"
    "s_mov_b64 exec, -1\n"
    "v_mfma_f32_16x16x32_bf16 v[44:47], a[196:199], v[24:27], v[44:47]\n"
    "v_mfma_f32_16x16x32_bf16 v[52:55], v[24:27], a[200:203], v[52:55]\n"
    "v_mfma_f32_16x16x32_bf16 v[60:63], v[24:27], a[204:207], v[60:63]\n"
    "v_mfma_f32_16x16x32_bf16 v[72:75], a[208:211], v[24:27], v[72:75]\n"
    "s_waitcnt lgkmcnt(1)\n"
    "s_waitcnt vmcnt(2)\n"
    "buffer_atomic_smax v12, v14, s[36:39], s70 offen sc1\n"
    "v_mfma_f32_16x16x32_bf16 v[76:79], a[212:215], v[24:27], v[76:79]\n"
    "ds_read_b128 v[24:27], v9 offset:11264\n"
    "v_mfma_f32_16x16x32_bf16 v[40:43], a[216:219], v[16:19], v[40:43]\n"
    "v_mfma_f32_16x16x32_bf16 v[44:47], a[220:223], v[16:19], v[44:47]\n"
    "v_mfma_f32_16x16x32_bf16 v[52:55], v[16:19], a[224:227], v[52:55]\n"
    "v_mfma_f32_16x16x32_bf16 v[60:63], v[16:19], a[228:231], v[60:63]\n"
    "v_mfma_f32_16x16x32_bf16 v[72:75], a[232:235], v[16:19], v[72:75]\n"
    "s_waitcnt lgkmcnt(1)\n"
    "v_mfma_f32_16x16x32_bf16 v[76:79], a[236:239], v[16:19], v[76:79]\n"
    "ds_read_b128 v[16:19], v9 offset:12288\n"
    "v_mfma_f32_16x16x32_bf16 v[40:43], a[240:243], v[20:23], v[40:43]\n"
    "v_mfma_f32_16x16x32_bf16 v[44:47], a[244:247], v[20:23], v[44:47]\n"
    "s_waitcnt vmcnt(2)\n"
    "v_add_u32 v13, v5, v3\n"
    "v_mfma_f32_16x16x32_bf16 v[52:55], v[20:23], a[248:251], v[52:55]\n"
    "v_cmp_gt_i32 vcc, s71, v13\n"
    "v_mfma_f32_16x16x32_bf16 v[60:63], v[20:23], a[252:255], v[60:63]\n"
    "v_mfma_f32_16x16x32_bf16 v[72:75], v[80:83], v[20:23], v[72:75]\n"
    "s_waitcnt lgkmcnt(1)\n"
    "v_mfma_f32_16x16x32_bf16 v[76:79], v[84:87], v[20:23], v[76:79]\n"
    "ds_read_b128 v[20:23], v9 offset:13312\n"
    "s_cbranch_vccz .LgP_dma1_66_%=\n"
    "v_mfma_f32_16x16x32_bf16 v[40:43], v[88:91], v[24:27], v[40:43]\n"
    "s_or_b64 exec, vcc, s[62:63]\n"
    "buffer_atomic_smax v5, v2, s[36:39], 0 offen sc0 sc1\n"
    "s_mov_b64 exec, -1\n"
    "v_mfma_f32_16x16x32_bf16 v[44:47], v[92:95], v[24:27], v[44:47]\n"
    "v_mfma_f32_16x16x32_bf16 v[52:55], v[24:27], v[96:99], v[52:55]\n"
    "v_mfma_f32_16x16x32_bf16 v[60:63], v[24:27], v[100:103], v[60:63]\n"
    "v_mfma_f32_16x16x32_bf16 v[72:75], v[104:107], v[24:27], v[72:75]\n"
    "s_waitcnt lgkmcnt(1)\n"
    "v_mfma_f32_16x16x32_bf16 v[76:79], v[108:111], v[24:27], v[76:79]\n"
    "ds_read_b128 v[24:27], v9 offset:14336\n"
    "v_mfma_f32_16x16x32_bf16 v[40:43], v[112:115], v[16:19], v[40:43]\n"
    "v_mfma_f32_16x16x32_bf16 v[44:47], v[116:119], v[16:19], v[44:47]\n"
    "v_mfma_f32_16x16x32_bf16 v[52:55], v[16:19], v[120:123], v[52:55]\n"
    "v_mfma_f32_16x16x32_bf16 v[60:63], v[16:19], v[124:127], v[60:63]\n"
    "v_mfma_f32_16x16x32_bf16 v[72:75], v[128:131], v[16:19], v[72:75]\n"
    "s_waitcnt lgkmcnt(1)\n"
    "v_mfma_f32_16x16x32_bf16 v[76:79], v[132:135], v[16:19], v[76:79]\n"
    "ds_read_b128 v[16:19], v9 offset:15360\n"
    "v_mfma_f32_16x16x32_bf16 v[40:43], v[136:139], v[20:23], v[40:43]\n"
    "v_mfma_f32_16x16x32_bf16 v[44:47], v[140:143], v[20:23], v[44:47]\n"
    "s_waitcnt vmcnt(2)\n"
    "v_add_u32 v13, v11, v3\n"
    "v_mfma_f32_16x16x32_bf16 v[52:55], v[20:23], v[144:147], v[52:55]\n"
    "v_cmp_gt_i32 vcc, s71, v13\n"
    "v_mfma_f32_16x16x32_bf16 v[60:63], v[20:23], v[148:151], v[60:63]\n"
    "v_mfma_f32_16x16x32_bf16 v[72:75], v[152:155], v[20:23], v[72:75]\n"
    "s_waitcnt lgkmcnt(1)\n"
    "v_mfma_f32_16x16x32_bf16 v[76:79], v[156:159], v[20:23], v[76:79]\n"
    "ds_read_b128 v[20:23], v9 offset:16384\n"
    "s_cbranch_vccz .LgP_dma1_84_%=\n"
    "v_mfma_f32_16x16x32_bf16 v[40:43], v[160:163], v[24:27], v[40:43]\n"
    "s_or_b64 exec, vcc, s[62:63]\n"
    "buffer_atomic_smax v11, v2, s[36:39], 0 offen sc0 sc1\n"
    "s_mov_b64 exec, -1\n"
    "v_mfma_f32_16x16x32_bf16 v[44:47], v[164:167], v[24:27], v[44:47]\n"
    "v_mfma_f32_16x16x32_bf16 v[52:55], v[24:27], v[168:171], v[52:55]\n"
    "v_mfma_f32_16x16x32_bf16 v[60:63], v[24:27], v[172:175], v[60:63]\n"
    "v_mfma_f32_16x16x32_bf16 v[72:75], v[176:179], v[24:27], v[72:75]\n"
    "s_waitcnt lgkmcnt(1)\n"
    "v_mfma_f32_16x16x32_bf16 v[76:79], v[180:183], v[24:27], v[76:79]\n"
    "ds_read_b128 v[24:27], v9 offset:17408\n"
    "v_mfma_f32_16x16x32_bf16 v[40:43], v[184:187], v[16:19], v[40:43]\n"
    "v_mfma_f32_16x16x32_bf16 v[44:47], v[188:191], v[16:19], v[44:47]\n"
    "v_mfma_f32_16x16x32_bf16 v[52:55], v[16:19], v[192:195], v[52:55]\n"
    "v_mfma_f32_16x16x32_bf16 v[60:63], v[16:19], v[196:199], v[60:63]\n"
    "v_mfma_f32_16x16x32_bf16 v[72:75], v[200:203], v[16:19], v[72:75]\n"
    "s_waitcnt lgkmcnt(1)\n"
    "v_mfma_f32_16x16x32_bf16 v[76:79], v[204:207], v[16:19], v[76:79]\n"
    "v_mfma_f32_16x16x32_bf16 v[40:43], v[208:211], v[20:23], v[40:43]\n"
    "v_mfma_f32_16x16x32_bf16 v[44:47], v[212:215], v[20:23], v[44:47]\n"
    "s_waitcnt vmcnt(1)\n"
    "v_add_u32 v13, v5, v3\n"
    "v_mfma_f32_16x16x32_bf16 v[52:55], v[20:23], v[216:219], v[52:55]\n"
    "v_cmp_gt_i32 vcc, s71, v13\n"
    "v_mfma_f32_16x16x32_bf16 v[60:63], v[20:23], v[220:223], v[60:63]\n"
    "v_mfma_f32_16x16x32_bf16 v[72:75], v[224:227], v[20:23], v[72:75]\n"
    "s_waitcnt lgkmcnt(0)\n"
    "v_mfma_f32_16x16x32_bf16 v[76:79], v[228:231], v[20:23], v[76:79]\n"
    "s_cbranch_vccz .LgP_dma1_102_%=\n"
    "v_mfma_f32_16x16x32_bf16 v[40:43], v[232:235], v[24:27], v[40:43]\n"
    "s_or_b64 exec, vcc, s[62:63]\n"
    "buffer_atomic_smax v5, v2, s[36:39], 0 offen sc0 sc1\n"
    "s_mov_b64 exec, -1\n"
    "v_mfma_f32_16x16x32_bf16 v[44:47], v[236:239], v[24:27], v[44:47]\n"
    "v_mfma_f32_16x16x32_bf16 v[52:55], v[24:27], v[240:243], v[52:55]\n"
    "v_mfma_f32_16x16x32_bf16 v[60:63], v[24:27], v[244:247], v[60:63]\n"
    "v_mfma_f32_16x16x32_bf16 v[72:75], v[248:251], v[24:27], v[72:75]\n"
    "v_mfma_f32_16x16x32_bf16 v[76:79], v[252:255], v[24:27], v[76:79]\n"
    "v_cvt_pk_bf16_f32 v40, v40, v41\n"
    "v_cvt_pk_bf16_f32 v41, v42, v43\n"
    "v_cvt_pk_bf16_f32 v42, v44, v45\n"
    "v_cvt_pk_bf16_f32 v43, v46, v47\n"
    "buffer_store_dwordx4 v[40:43], v1, s[36:39], s79 offen offset:1024 sc1\n"
    "v_cvt_pk_bf16_f32 v55, v54, v55\n"
    "v_cvt_pk_bf16_f32 v54, v52, v53\n"
    "v_mov_b64 v[52:53], v[28:29]\n"
    "buffer_store_dwordx4 v[52:55], v1, s[36:39], s80 offen sc1\n"
    "v_cvt_pk_bf16_f32 v63, v62, v63\n"
    "v_cvt_pk_bf16_f32 v62, v60, v61\n"
    "v_mov_b64 v[60:61], v[30:31]\n"
    "buffer_store_dwordx4 v[60:63], v1, s[36:39], s80 offen offset:1024 sc1\n"
    "v_cvt_pk_bf16_f32 v72, v72, v73\n"
    "v_cvt_pk_bf16_f32 v73, v74, v75\n"
    "v_cvt_pk_bf16_f32 v74, v76, v77\n"
    "v_cvt_pk_bf16_f32 v75, v78, v79\n"
    "buffer_store_dwordx4 v[72:75], v1, s[36:39], s73 offen offset:1024 sc1\n"
    "v_mov_b32 v12, s68\n"
    "s_mov_b32 s73, 0x80000000\n"
    "s_mov_b32 s79, 0x80000000\n"
    "s_mov_b32 s80, 0x80000000\n"
    "s_mov_b32 s68, s71\n"
    "s_cmp_eq_u32 s68, 128\n"
    "s_cbranch_scc1 .LgP_done_%=\n"
    "s_waitcnt vmcnt(5)\n"
    "v_add_u32 v13, v11, v3\n"
    "v_cmp_gt_i32 vcc, s71, v13\n"
    "s_cbranch_vccnz .LgP_idle1_%=\n"
    "s_add_u32 m0, s59, 0\n"
    "s_nop 0\n"
    "buffer_load_dwordx4 v1, s[36:39], s76 offen sc1 lds\n"
    "s_add_u32 m0, s59, 4096\n"
    "s_add_u32 s76, s76, 16384\n"
    "buffer_load_dwordx4 v1, s[36:39], s76 offen sc1 lds\n"
    "s_add_u32 m0, s59, 8192\n"
    "s_add_u32 s76, s76, 16384\n"
    "buffer_load_dwordx4 v1, s[36:39], s76 offen sc1 lds\n"
    "s_add_u32 m0, s59, 12288\n"
    "s_add_u32 s76, s76, 16384\n"
    "buffer_load_dwordx4 v1, s[36:39], s76 offen sc1 lds\n"
    "s_add_u32 m0, s59, 16384\n"
    "s_add_u32 s76, s76, 16384\n"
    "buffer_load_dwordx4 v8, s[36:39], s76 offen sc1 lds\n"
    "s_waitcnt vmcnt(5)\n"
    "buffer_atomic_smax v12, v10, s[36:39], 0 offen sc1\n"
    "s_mov_b32 s69, 0x80000000\n"
    "s_mov_b32 s70, 0x80000000\n"
    "v_mov_b32 v7, v12\n"
    "s_waitcnt vmcnt(10)\n"
    "s_add_u32 s71, s68, 1\n"
    "v_add_u32 v13, v11, v3\n"
    "v_cmp_gt_i32 vcc, s71, v13\n"
    "s_nop 0\n"
    "s_or_b64 exec, vcc, s[62:63]\n"
    "buffer_atomic_smax v11, v2, s[36:39], 0 offen sc0 sc1\n"
    "s_mov_b64 exec, -1\n"
    "s_waitcnt vmcnt(2)\n"
    "s_branch .LgP_bar0_%=\n"
    ".LgP_dma1_30_%=:\n"
    "v_mfma_f32_16x16x32_bf16 v[40:43], a[120:123], v[24:27], v[40:43]\n"
    "s_add_u32 m0, s59, 0\n"
    "v_mfma_f32_16x16x32_bf16 v[44:47], a[124:127], v[24:27], v[44:47]\n"
    "buffer_load_dwordx4 v1, s[36:39], s76 offen sc1 lds\n"
    "v_mfma_f32_16x16x32_bf16 v[52:55], v[24:27], a[128:131], v[52:55]\n"
    "s_add_u32 s76, s76, 16384\n"
    "s_add_u32 m0, s59, 4096\n"
    "v_mfma_f32_16x16x32_bf16 v[60:63], v[24:27], a[132:135], v[60:63]\n"
    "buffer_load_dwordx4 v1, s[36:39], s76 offen sc1 lds\n"
    "v_mfma_f32_16x16x32_bf16 v[72:75], a[136:139], v[24:27], v[72:75]\n"
    "s_waitcnt lgkmcnt(1)\n"
    "v_mfma_f32_16x16x32_bf16 v[76:79], a[140:143], v[24:27], v[76:79]\n"
    "ds_read_b128 v[24:27], v9 offset:8192\n"
    "v_mfma_f32_16x16x32_bf16 v[40:43], a[144:147], v[16:19], v[40:43]\n"
    "s_add_u32 s76, s76, 16384\n"
    "s_add_u32 m0, s59, 8192\n"
    "v_mfma_f32_16x16x32_bf16 v[44:47], a[148:151], v[16:19], v[44:47]\n"
    "buffer_load_dwordx4 v1, s[36:39], s76 offen sc1 lds\n"
    "v_mfma_f32_16x16x32_bf16 v[52:55], v[16:19], a[152:155], v[52:55]\n"
    "v_mfma_f32_16x16x32_bf16 v[60:63], v[16:19], a[156:159], v[60:63]\n"
    "v_mfma_f32_16x16x32_bf16 v[72:75], a[160:163], v[16:19], v[72:75]\n"
    "s_waitcnt lgkmcnt(1)\n"
    "s_add_u32 s76, s76, 16384\n"
    "s_add_u32 m0, s59, 12288\n"
    "v_mfma_f32_16x16x32_bf16 v[76:79], a[164:167], v[16:19], v[76:79]\n"
    "ds_read_b128 v[16:19], v9 offset:9216\n"
    "buffer_load_dwordx4 v1, s[36:39], s76 offen sc1 lds\n"
    "v_mfma_f32_16x16x32_bf16 v[40:43], a[168:171], v[20:23], v[40:43]\n"
    "v_mfma_f32_16x16x32_bf16 v[44:47], a[172:175], v[20:23], v[44:47]\n"
    "v_mfma_f32_16x16x32_bf16 v[52:55], v[20:23], a[176:179], v[52:55]\n"
    "s_add_u32 s76, s76, 16384\n"
    "s_add_u32 m0, s59, 16384\n"
    "v_mfma_f32_16x16x32_bf16 v[60:63], v[20:23], a[180:183], v[60:63]\n"
    "buffer_load_dwordx4 v8, s[36:39], s76 offen sc1 lds\n"
    "v_mfma_f32_16x16x32_bf16 v[72:75], a[184:187], v[20:23], v[72:75]\n"
    "s_waitcnt lgkmcnt(1)\n"
    "v_mfma_f32_16x16x32_bf16 v[76:79], a[188:191], v[20:23], v[76:79]\n"
    "ds_read_b128 v[20:23], v9 offset:10240\n"
    "v_mfma_f32_16x16x32_bf16 v[40:43], a[192:195], v[24:27], v[40:43]\n"
    "v_mfma_f32_16x16x32_bf16 v[44:47], a[196:199], v[24:27], v[44:47]\n"
    "v_mfma_f32_16x16x32_bf16 v[52:55], v[24:27], a[200:203], v[52:55]\n"
    "v_mfma_f32_16x16x32_bf16 v[60:63], v[24:27], a[204:207], v[60:63]\n"
    "v_mfma_f32_16x16x32_bf16 v[72:75], a[208:211], v[24:27], v[72:75]\n"
    "s_waitcnt lgkmcnt(1)\n"
    "s_waitcnt vmcnt(5)\n"
    "buffer_atomic_smax v12, v14, s[36:39], s70 offen sc1\n"
    "v_mfma_f32_16x16x32_bf16 v[76:79], a[212:215], v[24:27], v[76:79]\n"
    "ds_read_b128 v[24:27], v9 offset:11264\n"
    "v_mfma_f32_16x16x32_bf16 v[40:43], a[216:219], v[16:19], v[40:43]\n"
    "v_mfma_f32_16x16x32_bf16 v[44:47], a[220:223], v[16:19], v[44:47]\n"
    "v_mfma_f32_16x16x32_bf16 v[52:55], v[16:19], a[224:227], v[52:55]\n"
    "v_mfma_f32_16x16x32_bf16 v[60:63], v[16:19], a[228:231], v[60:63]\n"
    "v_mfma_f32_16x16x32_bf16 v[72:75], a[232:235], v[16:19], v[72:75]\n"
    "s_waitcnt lgkmcnt(1)\n"
    "v_mfma_f32_16x16x32_bf16 v[76:79], a[236:239], v[16:19], v[76:79]\n"
    "ds_read_b128 v[16:19], v9 offset:12288\n"
    "v_mfma_f32_16x16x32_bf16 v[40:43], a[240:243], v[20:23], v[40:43]\n"
    "v_mfma_f32_16x16x32_bf16 v[44:47], a[244:247], v[20:23], v[44:47]\n"
    "v_mfma_f32_16x16x32_bf16 v[52:55], v[20:23], a[248:251], v[52:55]\n"
    "v_mfma_f32_16x16x32_bf16 v[60:63], v[20:23], a[252:255], v[60:63]\n"
    "v_mfma_f32_16x16x32_bf16 v[72:75], v[80:83], v[20:23], v[72:75]\n"
    "s_waitcnt lgkmcnt(1)\n"
    "v_mfma_f32_16x16x32_bf16 v[76:79], v[84:87], v[20:23], v[76:79]\n"
    "ds_read_b128 v[20:23], v9 offset:13312\n"
    "v_mfma_f32_16x16x32_bf16 v[40:43], v[88:91], v[24:27], v[40:43]\n"
    "v_mfma_f32_16x16x32_bf16 v[44:47], v[92:95], v[24:27], v[44:47]\n"
    "v_mfma_f32_16x16x32_bf16 v[52:55], v[24:27], v[96:99], v[52:55]\n"
    "v_mfma_f32_16x16x32_bf16 v[60:63], v[24:27], v[100:103], v[60:63]\n"
    "v_mfma_f32_16x16x32_bf16 v[72:75], v[104:107], v[24:27], v[72:75]\n"
    "s_waitcnt lgkmcnt(1)\n"
    "v_mfma_f32_16x16x32_bf16 v[76:79], v[108:111], v[24:27], v[76:79]\n"
    "ds_read_b128 v[24:27], v9 offset:14336\n"
    "v_mfma_f32_16x16x32_bf16 v[40:43], v[112:115], v[16:19], v[40:43]\n"
    "v_mfma_f32_16x16x32_bf16 v[44:47], v[116:119], v[16:19], v[44:47]\n"
    "v_mfma_f32_16x16x32_bf16 v[52:55], v[16:19], v[120:123], v[52:55]\n"
    "v_mfma_f32_16x16x32_bf16 v[60:63], v[16:19], v[124:127], v[60:63]\n"
    "v_mfma_f32_16x16x32_bf16 v[72:75], v[128:131], v[16:19], v[72:75]\n"
    "s_waitcnt lgkmcnt(1)\n"
    "v_mfma_f32_16x16x32_bf16 v[76:79], v[132:135], v[16:19], v[76:79]\n"
    "ds_read_b128 v[16:19], v9 offset:15360\n"
    "s_branch .LgP_join1_%=\n"
    ".LgP_dma1_48_%=:\n"
    "v_mfma_f32_16x16x32_bf16 v[40:43], a[192:195], v[24:27], v[40:43]\n"
    "s_add_u32 m0, s59, 0\n"
    "v_mfma_f32_16x16x32_bf16 v[44:47], a[196:199], v[24:27], v[44:47]\n"
    "buffer_load_dwordx4 v1, s[36:39], s76 offen sc1 lds\n"
    "v_mfma_f32_16x16x32_bf16 v[52:55], v[24:27], a[200:203], v[52:55]\n"
    "s_add_u32 s76, s76, 16384\n"
    "s_add_u32 m0, s59, 4096\n"
    "v_mfma_f32_16x16x32_bf16 v[60:63], v[24:27], a[204:207], v[60:63]\n"
    "buffer_load_dwordx4 v1, s[36:39], s76 offen sc1 lds\n"
    "v_mfma_f32_16x16x32_bf16 v[72:75], a[208:211], v[24:27], v[72:75]\n"
    "s_waitcnt lgkmcnt(1)\n"
    "s_waitcnt vmcnt(3)\n"
    "buffer_atomic_smax v12, v14, s[36:39], s70 offen sc1\n"
    "v_mfma_f32_16x16x32_bf16 v[76:79], a[212:215], v[24:27], v[76:79]\n"
    "ds_read_b128 v[24:27], v9 offset:11264\n"
    "v_mfma_f32_16x16x32_bf16 v[40:43], a[216:219], v[16:19], v[40:43]\n"
    "s_add_u32 s76, s76, 16384\n"
    "s_add_u32 m0, s59, 8192\n"
    "v_mfma_f32_16x16x32_bf16 v[44:47], a[220:223], v[16:19], v[44:47]\n"
    "buffer_load_dwordx4 v1, s[36:39], s76 offen sc1 lds\n"
    "v_mfma_f32_16x16x32_bf16 v[52:55], v[16:19], a[224:227], v[52:55]\n"
    "v_mfma_f32_16x16x32_bf16 v[60:63], v[16:19], a[228:231], v[60:63]\n"
    "v_mfma_f32_16x16x32_bf16 v[72:75], a[232:235], v[16:19], v[72:75]\n"
    "s_waitcnt lgkmcnt(1)\n"
    "s_add_u32 s76, s76, 16384\n"
    "s_add_u32 m0, s59, 12288\n"
    "v_mfma_f32_16x16x32_bf16 v[76:79], a[236:239], v[16:19], v[76:79]\n"
    "ds_read_b128 v[16:19], v9 offset:12288\n"
    "buffer_load_dwordx4 v1, s[36:39], s76 offen sc1 lds\n"
    "v_mfma_f32_16x16x32_bf16 v[40:43], a[240:243], v[20:23], v[40:43]\n"
    "v_mfma_f32_16x16x32_bf16 v[44:47], a[244:247], v[20:23], v[44:47]\n"
    "v_mfma_f32_16x16x32_bf16 v[52:55], v[20:23], a[248:251], v[52:55]\n"
    "s_add_u32 s76, s76, 16384\n"
    "s_add_u32 m0, s59, 16384\n"
    "v_mfma_f32_16x16x32_bf16 v[60:63], v[20:23], a[252:255], v[60:63]\n"
    "buffer_load_dwordx4 v8, s[36:39], s76 offen sc1 lds\n"
    "v_mfma_f32_16x16x32_bf16 v[72:75], v[80:83], v[20:23], v[72:75]\n"
    "s_waitcnt lgkmcnt(1)\n"
    "v_mfma_f32_16x16x32_bf16 v[76:79], v[84:87], v[20:23], v[76:79]\n"
    "ds_read_b128 v[20:23], v9 offset:13312\n"
    "v_mfma_f32_16x16x32_bf16 v[40:43], v[88:91], v[24:27], v[40:43]\n"
    "v_mfma_f32_16x16x32_bf16 v[44:47], v[92:95], v[24:27], v[44:47]\n"
    "v_mfma_f32_16x16x32_bf16 v[52:55], v[24:27], v[96:99], v[52:55]\n"
    "v_mfma_f32_16x16x32_bf16 v[60:63], v[24:27], v[100:103], v[60:63]\n"
    "v_mfma_f32_16x16x32_bf16 v[72:75], v[104:107], v[24:27], v[72:75]\n"
    "s_waitcnt lgkmcnt(1)\n"
    "v_mfma_f32_16x16x32_bf16 v[76:79], v[108:111], v[24:27], v[76:79]\n"
    "ds_read_b128 v[24:27], v9 offset:14336\n"
    "v_mfma_f32_16x16x32_bf16 v[40:43], v[112:115], v[16:19], v[40:43]\n"
    "v_mfma_f32_16x16x32_bf16 v[44:47], v[116:119], v[16:19], v[44:47]\n"
    "v_mfma_f32_16x16x32_bf16 v[52:55], v[16:19], v[120:123], v[52:55]\n"
    "v_mfma_f32_16x16x32_bf16 v[60:63], v[16:19], v[124:127], v[60:63]\n"
    "v_mfma_f32_16x16x32_bf16 v[72:75], v[128:131], v[16:19], v[72:75]\n"
    "s_waitcnt lgkmcnt(1)\n"
    "v_mfma_f32_16x16x32_bf16 v[76:79], v[132:135], v[16:19], v[76:79]\n"
    "ds_read_b128 v[16:19], v9 offset:15360\n"
    "s_branch .LgP_join1_%=\n"
    ".LgP_dma1_66_%=:\n"
    "v_mfma_f32_16x16x32_bf16 v[40:43], v[88:91], v[24:27], v[40:43]\n"
    "s_add_u32 m0, s59, 0\n"
    "v_mfma_f32_16x16x32_bf16 v[44:47], v[92:95], v[24:27], v[44:47]\n"
    "buffer_load_dwordx4 v1, s[36:39], s76 offen sc1 lds\n"
    "v_mfma_f32_16x16x32_bf16 v[52:55], v[24:27], v[96:99], v[52:55]\n"
    "s_add_u32 s76, s76, 16384\n"
    "s_add_u32 m0, s59, 4096\n"
    "v_mfma_f32_16x16x32_bf16 v[60:63], v[24:27], v[100:103], v[60:63]\n"
    "buffer_load_dwordx4 v1, s[36:39], s76 offen sc1 lds\n"
    "v_mfma_f32_16x16x32_bf16 v[72:75], v[104:107], v[24:27], v[72:75]\n"
    "s_waitcnt lgkmcnt(1)\n"
    "v_mfma_f32_16x16x32_bf16 v[76:79], v[108:111], v[24:27], v[76:79]\n"
    "ds_read_b128 v[24:27], v9 offset:14336\n"
    "v_mfma_f32_16x16x32_bf16 v[40:43], v[112:115], v[16:19], v[40:43]\n"
    "s_add_u32 s76, s76, 16384\n"
    "s_add_u32 m0, s59, 8192\n"
    "v_mfma_f32_16x16x32_bf16 v[44:47], v[116:119], v[16:19], v[44:47]\n"
    "buffer_load_dwordx4 v1, s[36:39], s76 offen sc1 lds\n"
    "v_mfma_f32_16x16x32_bf16 v[52:55], v[16:19], v[120:123], v[52:55]\n"
    "v_mfma_f32_16x16x32_bf16 v[60:63], v[16:19], v[124:127], v[60:63]\n"
    "v_mfma_f32_16x16x32_bf16 v[72:75], v[128:131], v[16:19], v[72:75]\n"
    "s_waitcnt lgkmcnt(1)\n"
    "s_add_u32 s76, s76, 16384\n"
    "s_add_u32 m0, s59, 12288\n"
    "v_mfma_f32_16x16x32_bf16 v[76:79], v[132:135], v[16:19], v[76:79]\n"
    "ds_read_b128 v[16:19], v9 offset:15360\n"
    "buffer_load_dwordx4 v1, s[36:39], s76 offen sc1 lds\n"
    "v_mfma_f32_16x16x32_bf16 v[40:43], v[136:139], v[20:23], v[40:43]\n"
    "v_mfma_f32_16x16x32_bf16 v[44:47], v[140:143], v[20:23], v[44:47]\n"
    "v_mfma_f32_16x16x32_bf16 v[52:55], v[20:23], v[144:147], v[52:55]\n"
    "s_add_u32 s76, s76, 16384\n"
    "s_add_u32 m0, s59, 16384\n"
    "v_mfma_f32_16x16x32_bf16 v[60:63], v[20:23], v[148:151], v[60:63]\n"
    "buffer_load_dwordx4 v8, s[36:39], s76 offen sc1 lds\n"
    "v_mfma_f32_16x16x32_bf16 v[72:75], v[152:155], v[20:23], v[72:75]\n"
    "s_waitcnt lgkmcnt(1)\n"
    "v_mfma_f32_16x16x32_bf16 v[76:79], v[156:159], v[20:23], v[76:79]\n"
    "ds_read_b128 v[20:23], v9 offset:16384\n"
    "v_mfma_f32_16x16x32_bf16 v[40:43], v[160:163], v[24:27], v[40:43]\n"
    "v_mfma_f32_16x16x32_bf16 v[44:47], v[164:167], v[24:27], v[44:47]\n"
    "v_mfma_f32_16x16x32_bf16 v[52:55], v[24:27], v[168:171], v[52:55]\n"
    "v_mfma_f32_16x16x32_bf16 v[60:63], v[24:27], v[172:175], v[60:63]\n"
    "v_mfma_f32_16x16x32_bf16 v[72:75], v[176:179], v[24:27], v[72:75]\n"
    "s_waitcnt lgkmcnt(1)\n"
    "v_mfma_f32_16x16x32_bf16 v[76:79], v[180:183], v[24:27], v[76:79]\n"
    "ds_read_b128 v[24:27], v9 offset:17408\n"
    "v_mfma_f32_16x16x32_bf16 v[40:43], v[184:187], v[16:19], v[40:43]\n"
    "v_mfma_f32_16x16x32_bf16 v[44:47], v[188:191], v[16:19], v[44:47]\n"
    "v_mfma_f32_16x16x32_bf16 v[52:55], v[16:19], v[192:195], v[52:55]\n"
    "v_mfma_f32_16x16x32_bf16 v[60:63], v[16:19], v[196:199], v[60:63]\n"
    "v_mfma_f32_16x16x32_bf16 v[72:75], v[200:203], v[16:19], v[72:75]\n"
    "s_waitcnt lgkmcnt(1)\n"
    "v_mfma_f32_16x16x32_bf16 v[76:79], v[204:207], v[16:19], v[76:79]\n"
    "v_mfma_f32_16x16x32_bf16 v[40:43], v[208:211], v[20:23], v[40:43]\n"
    "v_mfma_f32_16x16x32_bf16 v[44:47], v[212:215], v[20:23], v[44:47]\n"
    "v_mfma_f32_16x16x32_bf16 v[52:55], v[20:23], v[216:219], v[52:55]\n"
    "v_mfma_f32_16x16x32_bf16 v[60:63], v[20:23], v[220:223], v[60:63]\n"
    "v_mfma_f32_16x16x32_bf16 v[72:75], v[224:227], v[20:23], v[72:75]\n"
    "s_waitcnt lgkmcnt(0)\n"
    "v_mfma_f32_16x16x32_bf16 v[76:79], v[228:231], v[20:23], v[76:79]\n"
    "v_mfma_f32_16x16x32_bf16 v[40:43], v[232:235], v[24:27], v[40:43]\n"
    "v_mfma_f32_16x16x32_bf16 v[44:47], v[236:239], v[24:27], v[44:47]\n"
    "v_mfma_f32_16x16x32_bf16 v[52:55], v[24:27], v[240:243], v[52:55]\n"
    "v_mfma_f32_16x16x32_bf16 v[60:63], v[24:27], v[244:247], v[60:63]\n"
    "v_mfma_f32_16x16x32_bf16 v[72:75], v[248:251], v[24:27], v[72:75]\n"
    "v_mfma_f32_16x16x32_bf16 v[76:79], v[252:255], v[24:27], v[76:79]\n"
    "v_cvt_pk_bf16_f32 v40, v40, v41\n"
    "v_cvt_pk_bf16_f32 v41, v42, v43\n"
    "v_cvt_pk_bf16_f32 v42, v44, v45\n"
    "v_cvt_pk_bf16_f32 v43, v46, v47\n"
    "buffer_store_dwordx4 v[40:43], v1, s[36:39], s79 offen offset:1024 sc1\n"
    "v_cvt_pk_bf16_f32 v55, v54, v55\n"
    "v_cvt_pk_bf16_f32 v54, v52, v53\n"
    "v_mov_b64 v[52:53], v[28:29]\n"
    "buffer_store_dwordx4 v[52:55], v1, s[36:39], s80 offen sc1\n"
    "v_cvt_pk_bf16_f32 v63, v62, v63\n"
    "v_cvt_pk_bf16_f32 v62, v60, v61\n"
    "v_mov_b64 v[60:61], v[30:31]\n"
    "buffer_store_dwordx4 v[60:63], v1, s[36:39], s80 offen offset:1024 sc1\n"
    "v_cvt_pk_bf16_f32 v72, v72, v73\n"
    "v_cvt_pk_bf16_f32 v73, v74, v75\n"
    "v_cvt_pk_bf16_f32 v74, v76, v77\n"
    "v_cvt_pk_bf16_f32 v75, v78, v79\n"
    "buffer_store_dwordx4 v[72:75], v1, s[36:39], s73 offen offset:1024 sc1\n"
    "v_mov_b32 v12, s68\n"
    "s_mov_b32 s73, 0x80000000\n"
    "s_mov_b32 s79, 0x80000000\n"
    "s_mov_b32 s80, 0x80000000\n"
    "s_mov_b32 s68, s71\n"
    "s_mov_b32 s69, 0\n"
    "s_mov_b32 s70, 0x80000000\n"
    "v_mov_b32 v7, v12\n"
    "s_waitcnt vmcnt(10)\n"
    "s_add_u32 s71, s68, 1\n"
    "v_add_u32 v13, v11, v3\n"
    "v_cmp_gt_i32 vcc, s71, v13\n"
    "s_nop 0\n"
    "s_or_b64 exec, vcc, s[62:63]\n"
    "buffer_atomic_smax v11, v2, s[36:39], 0 offen sc0 sc1\n"
    "s_mov_b64 exec, -1\n"
    "s_waitcnt vmcnt(5)\n"
    "s_branch .LgP_bar0_%=\n"
    ".LgP_dma1_84_%=:\n"
    "v_mfma_f32_16x16x32_bf16 v[40:43], v[160:163], v[24:27], v[40:43]\n"
    "s_add_u32 m0, s59, 0\n"
    "v_mfma_f32_16x16x32_bf16 v[44:47], v[164:167], v[24:27], v[44:47]\n"
    "buffer_load_dwordx4 v1, s[36:39], s76 offen sc1 lds\n"
    "v_mfma_f32_16x16x32_bf16 v[52:55], v[24:27], v[168:171], v[52:55]\n"
    "s_add_u32 s76, s76, 16384\n"
    "s_add_u32 m0, s59, 4096\n"
    "v_mfma_f32_16x16x32_bf16 v[60:63], v[24:27], v[172:175], v[60:63]\n"
    "buffer_load_dwordx4 v1, s[36:39], s76 offen sc1 lds\n"
    "v_mfma_f32_16x16x32_bf16 v[72:75], v[176:179], v[24:27], v[72:75]\n"
    "s_waitcnt lgkmcnt(1)\n"
    "v_mfma_f32_16x16x32_bf16 v[76:79], v[180:183], v[24:27], v[76:79]\n"
    "ds_read_b128 v[24:27], v9 offset:17408\n"
    "v_mfma_f32_16x16x32_bf16 v[40:43], v[184:187], v[16:19], v[40:43]\n"
    "s_add_u32 s76, s76, 16384\n"
    "s_add_u32 m0, s59, 8192\n"
    "v_mfma_f32_16x16x32_bf16 v[44:47], v[188:191], v[16:19], v[44:47]\n"
    "buffer_load_dwordx4 v1, s[36:39], s76 offen sc1 lds\n"
    "v_mfma_f32_16x16x32_bf16 v[52:55], v[16:19], v[192:195], v[52:55]\n"
    "v_mfma_f32_16x16x32_bf16 v[60:63], v[16:19], v[196:199], v[60:63]\n"
    "v_mfma_f32_16x16x32_bf16 v[72:75], v[200:203], v[16:19], v[72:75]\n"
    "s_waitcnt lgkmcnt(1)\n"
    "s_add_u32 s76, s76, 16384\n"
    "s_add_u32 m0, s59, 12288\n"
    "v_mfma_f32_16x16x32_bf16 v[76:79], v[204:207], v[16:19], v[76:79]\n"
    "buffer_load_dwordx4 v1, s[36:39], s76 offen sc1 lds\n"
    "v_mfma_f32_16x16x32_bf16 v[40:43], v[208:211], v[20:23], v[40:43]\n"
    "v_mfma_f32_16x16x32_bf16 v[44:47], v[212:215], v[20:23], v[44:47]\n"
    "v_mfma_f32_16x16x32_bf16 v[52:55], v[20:23], v[216:219], v[52:55]\n"
    "s_add_u32 s76, s76, 16384\n"
    "s_add_u32 m0, s59, 16384\n"
    "v_mfma_f32_16x16x32_bf16 v[60:63], v[20:23], v[220:223], v[60:63]\n"
    "buffer_load_dwordx4 v8, s[36:39], s76 offen sc1 lds\n"
    "v_mfma_f32_16x16x32_bf16 v[72:75], v[224:227], v[20:23], v[72:75]\n"
    "s_waitcnt lgkmcnt(0)\n"
    "v_mfma_f32_16x16x32_bf16 v[76:79], v[228:231], v[20:23], v[76:79]\n"
    "v_mfma_f32_16x16x32_bf16 v[40:43], v[232:235], v[24:27], v[40:43]\n"
    "v_mfma_f32_16x16x32_bf16 v[44:47], v[236:239], v[24:27], v[44:47]\n"
    "v_mfma_f32_16x16x32_bf16 v[52:55], v[24:27], v[240:243], v[52:55]\n"
    "v_mfma_f32_16x16x32_bf16 v[60:63], v[24:27], v[244:247], v[60:63]\n"
    "v_mfma_f32_16x16x32_bf16 v[72:75], v[248:251], v[24:27], v[72:75]\n"
    "v_mfma_f32_16x16x32_bf16 v[76:79], v[252:255], v[24:27], v[76:79]\n"
    "v_cvt_pk_bf16_f32 v40, v40, v41\n"
    "v_cvt_pk_bf16_f32 v41, v42, v43\n"
    "v_cvt_pk_bf16_f32 v42, v44, v45\n"
    "v_cvt_pk_bf16_f32 v43, v46, v47\n"
    "buffer_store_dwordx4 v[40:43], v1, s[36:39], s79 offen offset:1024 sc1\n"
    "v_cvt_pk_bf16_f32 v55, v54, v55\n"
    "v_cvt_pk_bf16_f32 v54, v52, v53\n"
    "v_mov_b64 v[52:53], v[28:29]\n"
    "buffer_store_dwordx4 v[52:55], v1, s[36:39], s80 offen sc1\n"
    "v_cvt_pk_bf16_f32 v63, v62, v63\n"
    "v_cvt_pk_bf16_f32 v62, v60, v61\n"
    "v_mov_b64 v[60:61], v[30:31]\n"
    "buffer_store_dwordx4 v[60:63], v1, s[36:39], s80 offen offset:1024 sc1\n"
    "v_cvt_pk_bf16_f32 v72, v72, v73\n"
    "v_cvt_pk_bf16_f32 v73, v74, v75\n"
    "v_cvt_pk_bf16_f32 v74, v76, v77\n"
    "v_cvt_pk_bf16_f32 v75, v78, v79\n"
    "buffer_store_dwordx4 v[72:75], v1, s[36:39], s73 offen offset:1024 sc1\n"
    "v_mov_b32 v12, s68\n"
    "s_mov_b32 s73, 0x80000000\n"
    "s_mov_b32 s79, 0x80000000\n"
    "s_mov_b32 s80, 0x80000000\n"
    "s_mov_b32 s68, s71\n"
    "s_mov_b32 s69, 0\n"
    "s_mov_b32 s70, 0x80000000\n"
    "v_mov_b32 v7, v12\n"
    "s_waitcnt vmcnt(9)\n"
    "s_add_u32 s71, s68, 1\n"
    "v_add_u32 v13, v11, v3\n"
    "v_cmp_gt_i32 vcc, s71, v13\n"
    "s_nop 0\n"
    "s_or_b64 exec, vcc, s[62:63]\n"
    "buffer_atomic_smax v11, v2, s[36:39], 0 offen sc0 sc1\n"
    "s_mov_b64 exec, -1\n"
    "s_waitcnt vmcnt(5)\n"
    "s_branch .LgP_bar0_%=\n"
    ".LgP_dma1_102_%=:\n"
    "v_mfma_f32_16x16x32_bf16 v[40:43], v[232:235], v[24:27], v[40:43]\n"
    "s_add_u32 m0, s59, 0\n"
    "v_mfma_f32_16x16x32_bf16 v[44:47], v[236:239], v[24:27], v[44:47]\n"
    "buffer_load_dwordx4 v1, s[36:39], s76 offen sc1 lds\n"
    "v_mfma_f32_16x16x32_bf16 v[52:55], v[24:27], v[240:243], v[52:55]\n"
    "s_add_u32 s76, s76, 16384\n"
    "s_add_u32 m0, s59, 4096\n"
    "v_mfma_f32_16x16x32_bf16 v[60:63], v[24:27], v[244:247], v[60:63]\n"
    "buffer_load_dwordx4 v1, s[36:39], s76 offen sc1 lds\n"
    "v_mfma_f32_16x16x32_bf16 v[72:75], v[248:251], v[24:27], v[72:75]\n"
    "v_mfma_f32_16x16x32_bf16 v[76:79], v[252:255], v[24:27], v[76:79]\n"
    "v_cvt_pk_bf16_f32 v40, v40, v41\n"
    "v_cvt_pk_bf16_f32 v41, v42, v43\n"
    "v_cvt_pk_bf16_f32 v42, v44, v45\n"
    "v_cvt_pk_bf16_f32 v43, v46, v47\n"
    "buffer_store_dwordx4 v[40:43], v1, s[36:39], s79 offen offset:1024 sc1\n"
    "v_cvt_pk_bf16_f32 v55, v54, v55\n"
    "v_cvt_pk_bf16_f32 v54, v52, v53\n"
    "v_mov_b64 v[52:53], v[28:29]\n"
    "buffer_store_dwordx4 v[52:55], v1, s[36:39], s80 offen sc1\n"
    "v_cvt_pk_bf16_f32 v63, v62, v63\n"
    "v_cvt_pk_bf16_f32 v62, v60, v61\n"
    "v_mov_b64 v[60:61], v[30:31]\n"
    "buffer_store_dwordx4 v[60:63], v1, s[36:39], s80 offen offset:1024 sc1\n"
    "v_cvt_pk_bf16_f32 v72, v72, v73\n"
    "v_cvt_pk_bf16_f32 v73, v74, v75\n"
    "v_cvt_pk_bf16_f32 v74, v76, v77\n"
    "v_cvt_pk_bf16_f32 v75, v78, v79\n"
    "buffer_store_dwordx4 v[72:75], v1, s[36:39], s73 offen offset:1024 sc1\n"
    "v_mov_b32 v12, s68\n"
    "s_mov_b32 s73, 0x80000000\n"
    "s_mov_b32 s79, 0x80000000\n"
    "s_mov_b32 s80, 0x80000000\n"
    "s_mov_b32 s68, s71\n"
    "s_add_u32 m0, s59, 8192\n"
    "s_add_u32 s76, s76, 16384\n"
    "buffer_load_dwordx4 v1, s[36:39], s76 offen sc1 lds\n"
    "s_add_u32 m0, s59, 12288\n"
    "s_add_u32 s76, s76, 16384\n"
    "buffer_load_dwordx4 v1, s[36:39], s76 offen sc1 lds\n"
    "s_add_u32 m0, s59, 16384\n"
    "s_add_u32 s76, s76, 16384\n"
    "buffer_load_dwordx4 v8, s[36:39], s76 offen sc1 lds\n"
    "s_waitcnt vmcnt(3)\n"
    "buffer_atomic_smax v12, v10, s[36:39], 0 offen sc1\n"
    "s_mov_b32 s69, 0x80000000\n"
    "s_mov_b32 s70, 0x80000000\n"
    "v_mov_b32 v7, v12\n"
    "s_waitcnt vmcnt(10)\n"
    "s_add_u32 s71, s68, 1\n"
    "v_add_u32 v13, v11, v3\n"
    "v_cmp_gt_i32 vcc, s71, v13\n"
    "s_nop 0\n"
    "s_or_b64 exec, vcc, s[62:63]\n"
    "buffer_atomic_smax v11, v2, s[36:39], 0 offen sc0 sc1\n"
    "s_mov_b64 exec, -1\n"
    "s_waitcnt vmcnt(2)\n"
    "s_branch .LgP_bar0_%=\n"
    ".LgP_idle1_%=:\n"
    "s_or_b64 exec, vcc, s[62:63]\n"
    "buffer_atomic_smax v11, v2, s[36:39], 0 offen sc0 sc1\n"
    "s_mov_b64 exec, -1\n"
    "s_waitcnt vmcnt(1)\n"
    "buffer_atomic_smax v12, v10, s[36:39], 0 offen sc1\n"
    "s_branch .LgP_block1_0_%=\n"
    ".LgP_block1_%=:\n"
    "v_add_u32 v13, v11, v3\n"
    "v_cmp_gt_i32 vcc, s71, v13\n"
    "s_nop 0\n"
    "s_or_b64 exec, vcc, s[62:63]\n"
    "buffer_atomic_smax v11, v2, s[36:39], 0 offen sc0 sc1\n"
    "s_mov_b64 exec, -1\n"
    "s_sleep 3\n"
    "s_waitcnt vmcnt(1)\n"
    ".LgP_block1_0_%=:\n"
    "v_add_u32 v13, v5, v3\n"
    "v_cmp_gt_i32 vcc, s71, v13\n"
    "s_cbranch_vccz .LgP_go1_%=\n"
    "v_add_u32 v13, v5, v3\n"
    "v_cmp_gt_i32 vcc, s71, v13\n"
    "s_nop 0\n"
    "s_or_b64 exec, vcc, s[62:63]\n"
    "buffer_atomic_smax v5, v2, s[36:39], 0 offen sc0 sc1\n"
    "s_mov_b64 exec, -1\n"
    "s_sleep 3\n"
    "s_waitcnt vmcnt(1)\n"
    ".LgP_block1_1_%=:\n"
    "v_add_u32 v13, v11, v3\n"
    "v_cmp_gt_i32 vcc, s71, v13\n"
    "s_cbranch_vccz .LgP_go1_%=\n"
    "s_sub_u32 s72, s72, 1\n"
    "s_cbranch_scc0 .LgP_block1_%=\n"
    "s_mov_b32 s72, 0\n"
    ".LgP_go1_%=:\n"
    "s_lshr_b32 s76, s71, 1\n"
    "s_and_b32 s76, s76, s54\n"
    "s_mul_i32 s76, s76, s53\n"
    "s_add_u32 s76, s76, s52\n"
    "s_add_u32 m0, s59, 0\n"
    "s_nop 0\n"
    "buffer_load_dwordx4 v1, s[36:39], s76 offen sc1 lds\n"
    "s_add_u32 m0, s59, 4096\n"
    "s_add_u32 s76, s76, 16384\n"
    "buffer_load_dwordx4 v1, s[36:39], s76 offen sc1 lds\n"
    "s_add_u32 m0, s59, 8192\n"
    "s_add_u32 s76, s76, 16384\n"
    "buffer_load_dwordx4 v1, s[36:39], s76 offen sc1 lds\n"
    "s_add_u32 m0, s59, 12288\n"
    "s_add_u32 s76, s76, 16384\n"
    "buffer_load_dwordx4 v1, s[36:39], s76 offen sc1 lds\n"
    "s_add_u32 m0, s59, 16384\n"
    "s_add_u32 s76, s76, 16384\n"
    "buffer_load_dwordx4 v8, s[36:39], s76 offen sc1 lds\n"
    "s_mov_b32 s69, 0x80000000\n"
    "s_mov_b32 s70, 0x80000000\n"
    "v_mov_b32 v7, v12\n"
    "s_waitcnt vmcnt(5)\n"
    "s_add_u32 s71, s68, 1\n"
    "v_add_u32 v13, v11, v3\n"
    "v_cmp_gt_i32 vcc, s71, v13\n"
    "s_nop 0\n"
    "s_or_b64 exec, vcc, s[62:63]\n"
    "buffer_atomic_smax v11, v2, s[36:39], 0 offen sc0 sc1\n"
    "s_mov_b64 exec, -1\n"
    "s_waitcnt vmcnt(1)\n"
    "s_branch .LgP_bar0_%=\n"
    ".LgP_bar1_%=:\n"
    "s_barrier\n"
    "ds_read_b128 v[16:19], v9 offset:0\n"
    "ds_read_b128 v[20:23], v9 offset:1024\n"
    "ds_read_b128 v[24:27], v9 offset:2048\n"
    "v_add_u32 v13, v5, v3\n"
    "v_cmp_gt_i32 vcc, s71, v13\n"
    "s_nop 0\n"
    "s_or_b64 exec, vcc, s[62:63]\n"
    "buffer_atomic_smax v5, v2, s[36:39], 0 offen sc0 sc1\n"
    "s_mov_b64 exec, -1\n"
    "s_lshr_b32 s76, s71, 1\n"
    "s_and_b32 s76, s76, s54\n"
    "s_mul_i32 s76, s76, s53\n"
    "s_add_u32 s76, s76, s52\n"
    "s_waitcnt lgkmcnt(2)\n"
    "s_branch .LgP_head1_%=\n"
    ".LgP_FIN_%=:\n"
  );
}
DEVI void role_up(const Params& p, int L, int j, int i, u32 dbg, unsigned long long tag) {
  const int lane = threadIdx.x & 63, w = threadIdx.x >> 6, col = (6 * j + i) * 192;
  const u32 wb = 576u * 4608 * 2 - col * 2;
  GEMM_DECL;
  g_w0 = make_srd(p.w1 + col, wb); g_w1 = make_srd(p.w3 + col, wb); g_w2 = g_w1;
  g_ldw = 9216;
  g_in_base = pgb(L, B_XN2); g_in_stride = SLOT; g_in_mask = RX - 1;
  g_out_base = pgb(L, B_H + 9 * j); g_out_kb = 12 * i; g_out_mask = RH - 1;
  g_flag = lane < 3 ? mbox(L, j, SL_DOWN + lane, 4 * i + w) : OOB;
  g_n0 = 64; g_n1 = 0;
  g_paddr = mbox(L, j, SL_UP + i, lane);
  g_pbias = lane < 16 ? lane >> 3 : (lane < 19 ? 2 * RH : 1000);
  v16u sa, sb, va;
  gemm_args(GEMM_ARGS, p, dbg, tag, sa, sb, va);
  RUN_ASM(
    "s_nop 4\n"
    "s_getpc_b64 s[84:85]\n"
    ".LgUP_PC_%=:\n"
    "s_and_b32 s85, s85, 0xffff\n"
    "s_mov_b32 s86, .LgUP_FIN_%=-.LgUP_PC_%=\n"
    "s_mov_b32 s87, 0x20000\n"
    "v_mbcnt_lo_u32_b32 v40, -1, 0\n"
    "v_mbcnt_hi_u32_b32 v40, -1, v40\n"
    "v_lshlrev_b32 v40, 7, v40\n"
    "buffer_load_dword v41, v40, s[84:87], 0 offen\n"
    "v_add_u32 v40, 8192, v40\n"
    "buffer_load_dword v41, v40, s[84:87], 0 offen\n"
    "v_add_u32 v40, 8192, v40\n"
    "buffer_load_dword v41, v40, s[84:87], 0 offen\n"
    "s_mov_b32 s68, 0\n"
    "s_mov_b32 s71, 0\n"
    "v_add_u32 v9, 20480, v0\n"
    "s_mov_b32 s64, 0xbfb8aa3b\n"
    "s_mov_b32 s72, 262144\n"
    "s_add_u32 m0, s59, 0\n"
    "s_mov_b32 s74, 0\n"
    "buffer_load_dwordx4 v5, s[40:43], s74 offen lds\n"
    "s_add_u32 m0, s59, 4096\n"
    "s_mov_b32 s74, 128\n"
    "buffer_load_dwordx4 v5, s[40:43], s74 offen lds\n"
    "s_add_u32 m0, s59, 8192\n"
    "s_mov_b32 s74, 256\n"
    "buffer_load_dwordx4 v5, s[40:43], s74 offen lds\n"
    "s_add_u32 m0, s59, 12288\n"
    "s_mov_b32 s74, 0\n"
    "buffer_load_dwordx4 v5, s[44:47], s74 offen lds\n"
    "s_add_u32 m0, s59, 16384\n"
    "s_mov_b32 s74, 128\n"
    "buffer_load_dwordx4 v5, s[44:47], s74 offen lds\n"
    "s_add_u32 m0, s59, 20480\n"
    "s_mov_b32 s74, 256\n"
    "buffer_load_dwordx4 v5, s[44:47], s74 offen lds\n"
    "s_add_u32 m0, s59, 24576\n"
    "s_mov_b32 s74, 294912\n"
    "buffer_load_dwordx4 v5, s[40:43], s74 offen lds\n"
    "s_add_u32 m0, s59, 28672\n"
    "s_mov_b32 s74, 295040\n"
    "buffer_load_dwordx4 v5, s[40:43], s74 offen lds\n"
    "s_add_u32 m0, s59, 32768\n"
    "s_mov_b32 s74, 295168\n"
    "buffer_load_dwordx4 v5, s[40:43], s74 offen lds\n"
    "s_add_u32 m0, s59, 36864\n"
    "s_mov_b32 s74, 294912\n"
    "buffer_load_dwordx4 v5, s[44:47], s74 offen lds\n"
    "s_add_u32 m0, s59, 40960\n"
    "s_mov_b32 s74, 295040\n"
    "buffer_load_dwordx4 v5, s[44:47], s74 offen lds\n"
    "s_add_u32 m0, s59, 45056\n"
    "s_mov_b32 s74, 295168\n"
    "buffer_load_dwordx4 v5, s[44:47], s74 offen lds\n"
    "s_add_u32 m0, s59, 49152\n"
    "s_mov_b32 s74, 589824\n"
    "buffer_load_dwordx4 v5, s[40:43], s74 offen lds\n"
    "s_add_u32 m0, s59, 53248\n"
    "s_mov_b32 s74, 589952\n"
    "buffer_load_dwordx4 v5, s[40:43], s74 offen lds\n"
    "s_add_u32 m0, s59, 57344\n"
    "s_mov_b32 s74, 590080\n"
    "buffer_load_dwordx4 v5, s[40:43], s74 offen lds\n"
    "s_add_u32 m0, s59, 61440\n"
    "s_mov_b32 s74, 589824\n"
    "buffer_load_dwordx4 v5, s[44:47], s74 offen lds\n"
    "s_add_u32 m0, s59, 65536\n"
    "s_mov_b32 s74, 589952\n"
    "buffer_load_dwordx4 v5, s[44:47], s74 offen lds\n"
    "s_add_u32 m0, s59, 69632\n"
    "s_mov_b32 s74, 590080\n"
    "buffer_load_dwordx4 v5, s[44:47], s74 offen lds\n"
    "s_add_u32 m0, s59, 73728\n"
    "s_mov_b32 s74, 884736\n"
    "buffer_load_dwordx4 v5, s[40:43], s74 offen lds\n"
    "s_add_u32 m0, s59, 77824\n"
    "s_mov_b32 s74, 884864\n"
    "buffer_load_dwordx4 v5, s[40:43], s74 offen lds\n"
    "s_add_u32 m0, s59, 81920\n"
    "s_mov_b32 s74, 884992\n"
    "buffer_load_dwordx4 v5, s[40:43], s74 offen lds\n"
    "s_add_u32 m0, s59, 86016\n"
    "s_mov_b32 s74, 884736\n"
    "buffer_load_dwordx4 v5, s[44:47], s74 offen lds\n"
    "s_add_u32 m0, s59, 90112\n"
    "s_mov_b32 s74, 884864\n"
    "buffer_load_dwordx4 v5, s[44:47], s74 offen lds\n"
    "s_add_u32 m0, s59, 94208\n"
    "s_mov_b32 s74, 884992\n"
    "buffer_load_dwordx4 v5, s[44:47], s74 offen lds\n"
    "s_add_u32 m0, s59, 98304\n"
    "s_mov_b32 s74, 1179648\n"
    "buffer_load_dwordx4 v5, s[40:43], s74 offen lds\n"
    "s_add_u32 m0, s59, 102400\n"
    "s_mov_b32 s74, 1179776\n"
    "buffer_load_dwordx4 v5, s[40:43], s74 offen lds\n"
    "s_add_u32 m0, s59, 106496\n"
    "s_mov_b32 s74, 1179904\n"
    "buffer_load_dwordx4 v5, s[40:43], s74 offen lds\n"
    "s_add_u32 m0, s59, 110592\n"
    "s_mov_b32 s74, 1179648\n"
    "buffer_load_dwordx4 v5, s[44:47], s74 offen lds\n"
    "s_add_u32 m0, s59, 114688\n"
    "s_mov_b32 s74, 1179776\n"
    "buffer_load_dwordx4 v5, s[44:47], s74 offen lds\n"
    "s_add_u32 m0, s59, 118784\n"
    "s_mov_b32 s74, 1179904\n"
    "buffer_load_dwordx4 v5, s[44:47], s74 offen lds\n"
    "v_add_u32 v16, 49152, v6\n"
    "v_add_u32 v17, 98304, v6\n"
    "v_add_u32 v18, 49152, v7\n"
    "v_add_u32 v19, 98304, v7\n"
    "s_waitcnt vmcnt(24) lgkmcnt(0)\n"
    "s_barrier\n"
    "s_add_u32 m0, s59, 122880\n"
    "s_mov_b32 s74, 1474560\n"
    "buffer_load_dwordx4 v5, s[40:43], s74 offen lds\n"
    "s_add_u32 m0, s59, 126976\n"
    "s_mov_b32 s74, 1474688\n"
    "buffer_load_dwordx4 v5, s[40:43], s74 offen lds\n"
    "s_add_u32 m0, s59, 131072\n"
    "s_mov_b32 s74, 1474816\n"
    "buffer_load_dwordx4 v5, s[40:43], s74 offen lds\n"
    "s_add_u32 m0, s59, 135168\n"
    "s_mov_b32 s74, 1474560\n"
    "buffer_load_dwordx4 v5, s[44:47], s74 offen lds\n"
    "s_add_u32 m0, s59, 139264\n"
    "s_mov_b32 s74, 1474688\n"
    "buffer_load_dwordx4 v5, s[44:47], s74 offen lds\n"
    "s_add_u32 m0, s59, 143360\n"
    "s_mov_b32 s74, 1474816\n"
    "buffer_load_dwordx4 v5, s[44:47], s74 offen lds\n"
    "ds_read_b64_tr_b16 a[0:1], v6 offset:0\n"
    "ds_read_b64_tr_b16 a[2:3], v6 offset:512\n"
    "ds_read_b64_tr_b16 a[4:5], v6 offset:8\n"
    "ds_read_b64_tr_b16 a[6:7], v6 offset:520\n"
    "ds_read_b64_tr_b16 a[8:9], v7 offset:0\n"
    "ds_read_b64_tr_b16 a[10:11], v7 offset:512\n"
    "ds_read_b64_tr_b16 a[12:13], v6 offset:12288\n"
    "ds_read_b64_tr_b16 a[14:15], v6 offset:12800\n"
    "ds_read_b64_tr_b16 a[16:17], v6 offset:12296\n"
    "ds_read_b64_tr_b16 a[18:19], v6 offset:12808\n"
    "ds_read_b64_tr_b16 a[20:21], v7 offset:12288\n"
    "ds_read_b64_tr_b16 a[22:23], v7 offset:12800\n"
    "s_waitcnt vmcnt(24) lgkmcnt(0)\n"
    "s_barrier\n"
    "s_add_u32 m0, s59, 0\n"
    "s_mov_b32 s74, 1769472\n"
    "buffer_load_dwordx4 v5, s[40:43], s74 offen lds\n"
    "s_add_u32 m0, s59, 4096\n"
    "s_mov_b32 s74, 1769600\n"
    "buffer_load_dwordx4 v5, s[40:43], s74 offen lds\n"
    "s_add_u32 m0, s59, 8192\n"
    "s_mov_b32 s74, 1769728\n"
    "buffer_load_dwordx4 v5, s[40:43], s74 offen lds\n"
    "s_add_u32 m0, s59, 12288\n"
    "s_mov_b32 s74, 1769472\n"
    "buffer_load_dwordx4 v5, s[44:47], s74 offen lds\n"
    "s_add_u32 m0, s59, 16384\n"
    "s_mov_b32 s74, 1769600\n"
    "buffer_load_dwordx4 v5, s[44:47], s74 offen lds\n"
    "s_add_u32 m0, s59, 20480\n"
    "s_mov_b32 s74, 1769728\n"
    "buffer_load_dwordx4 v5, s[44:47], s74 offen lds\n"
    "ds_read_b64_tr_b16 a[24:25], v6 offset:24576\n"
    "ds_read_b64_tr_b16 a[26:27], v6 offset:25088\n"
    "ds_read_b64_tr_b16 a[28:29], v6 offset:24584\n"
    "ds_read_b64_tr_b16 a[30:31], v6 offset:25096\n"
    "ds_read_b64_tr_b16 a[32:33], v7 offset:24576\n"
    "ds_read_b64_tr_b16 a[34:35], v7 offset:25088\n"
    "ds_read_b64_tr_b16 a[36:37], v6 offset:36864\n"
    "ds_read_b64_tr_b16 a[38:39], v6 offset:37376\n"
    "ds_read_b64_tr_b16 a[40:41], v6 offset:36872\n"
    "ds_read_b64_tr_b16 a[42:43], v6 offset:37384\n"
    "ds_read_b64_tr_b16 a[44:45], v7 offset:36864\n"
    "ds_read_b64_tr_b16 a[46:47], v7 offset:37376\n"
    "s_waitcnt vmcnt(24) lgkmcnt(0)\n"
    "s_barrier\n"
    "s_add_u32 m0, s59, 24576\n"
    "s_mov_b32 s74, 2064384\n"
    "buffer_load_dwordx4 v5, s[40:43], s74 offen lds\n"
    "s_add_u32 m0, s59, 28672\n"
    "s_mov_b32 s74, 2064512\n"
    "buffer_load_dwordx4 v5, s[40:43], s74 offen lds\n"
    "s_add_u32 m0, s59, 32768\n"
    "s_mov_b32 s74, 2064640\n"
    "buffer_load_dwordx4 v5, s[40:43], s74 offen lds\n"
    "s_add_u32 m0, s59, 36864\n"
    "s_mov_b32 s74, 2064384\n"
    "buffer_load_dwordx4 v5, s[44:47], s74 offen lds\n"
    "s_add_u32 m0, s59, 40960\n"
    "s_mov_b32 s74, 2064512\n"
    "buffer_load_dwordx4 v5, s[44:47], s74 offen lds\n"
    "s_add_u32 m0, s59, 45056\n"
    "s_mov_b32 s74, 2064640\n"
    "buffer_load_dwordx4 v5, s[44:47], s74 offen lds\n"
    "ds_read_b64_tr_b16 a[48:49], v16 offset:0\n"
    "ds_read_b64_tr_b16 a[50:51], v16 offset:512\n"
    "ds_read_b64_tr_b16 a[52:53], v16 offset:8\n"
    "ds_read_b64_tr_b16 a[54:55], v16 offset:520\n"
    "ds_read_b64_tr_b16 a[56:57], v18 offset:0\n"
    "ds_read_b64_tr_b16 a[58:59], v18 offset:512\n"
    "ds_read_b64_tr_b16 a[60:61], v16 offset:12288\n"
    "ds_read_b64_tr_b16 a[62:63], v16 offset:12800\n"
    "ds_read_b64_tr_b16 a[64:65], v16 offset:12296\n"
    "ds_read_b64_tr_b16 a[66:67], v16 offset:12808\n"
    "ds_read_b64_tr_b16 a[68:69], v18 offset:12288\n"
    "ds_read_b64_tr_b16 a[70:71], v18 offset:12800\n"
    "s_waitcnt vmcnt(24) lgkmcnt(0)\n"
    "s_barrier\n"
    "s_add_u32 m0, s59, 49152\n"
    "s_mov_b32 s74, 2359296\n"
    "buffer_load_dwordx4 v5, s[40:43], s74 offen lds\n"
    "s_add_u32 m0, s59, 53248\n"
    "s_mov_b32 s74, 2359424\n"
    "buffer_load_dwordx4 v5, s[40:43], s74 offen lds\n"
    "s_add_u32 m0, s59, 57344\n"
    "s_mov_b32 s74, 2359552\n"
    "buffer_load_dwordx4 v5, s[40:43], s74 offen lds\n"
    "s_add_u32 m0, s59, 61440\n"
    "s_mov_b32 s74, 2359296\n"
    "buffer_load_dwordx4 v5, s[44:47], s74 offen lds\n"
    "s_add_u32 m0, s59, 65536\n"
    "s_mov_b32 s74, 2359424\n"
    "buffer_load_dwordx4 v5, s[44:47], s74 offen lds\n"
    "s_add_u32 m0, s59, 69632\n"
    "s_mov_b32 s74, 2359552\n"
    "buffer_load_dwordx4 v5, s[44:47], s74 offen lds\n"
    "ds_read_b64_tr_b16 a[72:73], v16 offset:24576\n"
    "ds_read_b64_tr_b16 a[74:75], v16 offset:25088\n"
    "ds_read_b64_tr_b16 a[76:77], v16 offset:24584\n"
    "ds_read_b64_tr_b16 a[78:79], v16 offset:25096\n"
    "ds_read_b64_tr_b16 a[80:81], v18 offset:24576\n"
    "ds_read_b64_tr_b16 a[82:83], v18 offset:25088\n"
    "ds_read_b64_tr_b16 a[84:85], v16 offset:36864\n"
    "ds_read_b64_tr_b16 a[86:87], v16 offset:37376\n"
    "ds_read_b64_tr_b16 a[88:89], v16 offset:36872\n"
    "ds_read_b64_tr_b16 a[90:91], v16 offset:37384\n"
    "ds_read_b64_tr_b16 a[92:93], v18 offset:36864\n"
    "ds_read_b64_tr_b16 a[94:95], v18 offset:37376\n"
    "s_waitcnt vmcnt(24) lgkmcnt(0)\n"
    "s_barrier\n"
    "s_add_u32 m0, s59, 73728\n"
    "s_mov_b32 s74, 2654208\n"
    "buffer_load_dwordx4 v5, s[40:43], s74 offen lds\n"
    "s_add_u32 m0, s59, 77824\n"
    "s_mov_b32 s74, 2654336\n"
    "buffer_load_dwordx4 v5, s[40:43], s74 offen lds\n"
    "s_add_u32 m0, s59, 81920\n"
    "s_mov_b32 s74, 2654464\n"
    "buffer_load_dwordx4 v5, s[40:43], s74 offen lds\n"
    "s_add_u32 m0, s59, 86016\n"
    "s_mov_b32 s74, 2654208\n"
    "buffer_load_dwordx4 v5, s[44:47], s74 offen lds\n"
    "s_add_u32 m0, s59, 90112\n"
    "s_mov_b32 s74, 2654336\n"
    "buffer_load_dwordx4 v5, s[44:47], s74 offen lds\n"
    "s_add_u32 m0, s59, 94208\n"
    "s_mov_b32 s74, 2654464\n"
    "buffer_load_dwordx4 v5, s[44:47], s74 offen lds\n"
    "ds_read_b64_tr_b16 a[96:97], v17 offset:0\n"
    "ds_read_b64_tr_b16 a[98:99], v17 offset:512\n"
    "ds_read_b64_tr_b16 a[100:101], v17 offset:8\n"
    "ds_read_b64_tr_b16 a[102:103], v17 offset:520\n"
    "ds_read_b64_tr_b16 a[104:105], v19 offset:0\n"
    "ds_read_b64_tr_b16 a[106:107], v19 offset:512\n"
    "ds_read_b64_tr_b16 a[108:109], v17 offset:12288\n"
    "ds_read_b64_tr_b16 a[110:111], v17 offset:12800\n"
    "ds_read_b64_tr_b16 a[112:113], v17 offset:12296\n"
    "ds_read_b64_tr_b16 a[114:115], v17 offset:12808\n"
    "ds_read_b64_tr_b16 a[116:117], v19 offset:12288\n"
    "ds_read_b64_tr_b16 a[118:119], v19 offset:12800\n"
    "s_waitcnt vmcnt(24) lgkmcnt(0)\n"
    "s_barrier\n"
    "s_add_u32 m0, s59, 98304\n"
    "s_mov_b32 s74, 2949120\n"
    "buffer_load_dwordx4 v5, s[40:43], s74 offen lds\n"
    "s_add_u32 m0, s59, 102400\n"
    "s_mov_b32 s74, 2949248\n"
    "buffer_load_dwordx4 v5, s[40:43], s74 offen lds\n"
    "s_add_u32 m0, s59, 106496\n"
    "s_mov_b32 s74, 2949376\n"
    "buffer_load_dwordx4 v5, s[40:43], s74 offen lds\n"
    "s_add_u32 m0, s59, 110592\n"
    "s_mov_b32 s74, 2949120\n"
    "buffer_load_dwordx4 v5, s[44:47], s74 offen lds\n"
    "s_add_u32 m0, s59, 114688\n"
    "s_mov_b32 s74, 2949248\n"
    "buffer_load_dwordx4 v5, s[44:47], s74 offen lds\n"
    "s_add_u32 m0, s59, 118784\n"
    "s_mov_b32 s74, 2949376\n"
    "buffer_load_dwordx4 v5, s[44:47], s74 offen lds\n"
    "ds_read_b64_tr_b16 a[120:121], v17 offset:24576\n"
    "ds_read_b64_tr_b16 a[122:123], v17 offset:25088\n"
    "ds_read_b64_tr_b16 a[124:125], v17 offset:24584\n"
    "ds_read_b64_tr_b16 a[126:127], v17 offset:25096\n"
    "ds_read_b64_tr_b16 a[128:129], v19 offset:24576\n"
    "ds_read_b64_tr_b16 a[130:131], v19 offset:25088\n"
    "ds_read_b64_tr_b16 a[132:133], v17 offset:36864\n"
    "ds_read_b64_tr_b16 a[134:135], v17 offset:37376\n"
    "ds_read_b64_tr_b16 a[136:137], v17 offset:36872\n"
    "ds_read_b64_tr_b16 a[138:139], v17 offset:37384\n"
    "ds_read_b64_tr_b16 a[140:141], v19 offset:36864\n"
    "ds_read_b64_tr_b16 a[142:143], v19 offset:37376\n"
    "s_waitcnt vmcnt(24) lgkmcnt(0)\n"
    "s_barrier\n"
    "s_add_u32 m0, s59, 122880\n"
    "s_mov_b32 s74, 3244032\n"
    "buffer_load_dwordx4 v5, s[40:43], s74 offen lds\n"
    "s_add_u32 m0, s59, 126976\n"
    "s_mov_b32 s74, 3244160\n"
    "buffer_load_dwordx4 v5, s[40:43], s74 offen lds\n"
    "s_add_u32 m0, s59, 131072\n"
    "s_mov_b32 s74, 3244288\n"
    "buffer_load_dwordx4 v5, s[40:43], s74 offen lds\n"
    "s_add_u32 m0, s59, 135168\n"
    "s_mov_b32 s74, 3244032\n"
    "buffer_load_dwordx4 v5, s[44:47], s74 offen lds\n"
    "s_add_u32 m0, s59, 139264\n"
    "s_mov_b32 s74, 3244160\n"
    "buffer_load_dwordx4 v5, s[44:47], s74 offen lds\n"
    "s_add_u32 m0, s59, 143360\n"
    "s_mov_b32 s74, 3244288\n"
    "buffer_load_dwordx4 v5, s[44:47], s74 offen lds\n"
    "ds_read_b64_tr_b16 a[144:145], v6 offset:0\n"
    "ds_read_b64_tr_b16 a[146:147], v6 offset:512\n"
    "ds_read_b64_tr_b16 a[148:149], v6 offset:8\n"
    "ds_read_b64_tr_b16 a[150:151], v6 offset:520\n"
    "ds_read_b64_tr_b16 a[152:153], v7 offset:0\n"
    "ds_read_b64_tr_b16 a[154:155], v7 offset:512\n"
    "ds_read_b64_tr_b16 a[156:157], v6 offset:12288\n"
    "ds_read_b64_tr_b16 a[158:159], v6 offset:12800\n"
    "ds_read_b64_tr_b16 a[160:161], v6 offset:12296\n"
    "ds_read_b64_tr_b16 a[162:163], v6 offset:12808\n"
    "ds_read_b64_tr_b16 a[164:165], v7 offset:12288\n"
    "ds_read_b64_tr_b16 a[166:167], v7 offset:12800\n"
    "s_waitcnt vmcnt(24) lgkmcnt(0)\n"
    "s_barrier\n"
    "s_add_u32 m0, s59, 0\n"
    "s_mov_b32 s74, 3538944\n"
    "buffer_load_dwordx4 v5, s[40:43], s74 offen lds\n"
    "s_add_u32 m0, s59, 4096\n"
    "s_mov_b32 s74, 3539072\n"
    "buffer_load_dwordx4 v5, s[40:43], s74 offen lds\n"
    "s_add_u32 m0, s59, 8192\n"
    "s_mov_b32 s74, 3539200\n"
    "buffer_load_dwordx4 v5, s[40:43], s74 offen lds\n"
    "s_add_u32 m0, s59, 12288\n"
    "s_mov_b32 s74, 3538944\n"
    "buffer_load_dwordx4 v5, s[44:47], s74 offen lds\n"
    "s_add_u32 m0, s59, 16384\n"
    "s_mov_b32 s74, 3539072\n"
    "buffer_load_dwordx4 v5, s[44:47], s74 offen lds\n"
    "s_add_u32 m0, s59, 20480\n"
    "s_mov_b32 s74, 3539200\n"
    "buffer_load_dwordx4 v5, s[44:47], s74 offen lds\n"
    "ds_read_b64_tr_b16 a[168:169], v6 offset:24576\n"
    "ds_read_b64_tr_b16 a[170:171], v6 offset:25088\n"
    "ds_read_b64_tr_b16 a[172:173], v6 offset:24584\n"
    "ds_read_b64_tr_b16 a[174:175], v6 offset:25096\n"
    "ds_read_b64_tr_b16 a[176:177], v7 offset:24576\n"
    "ds_read_b64_tr_b16 a[178:179], v7 offset:25088\n"
    "ds_read_b64_tr_b16 a[180:181], v6 offset:36864\n"
    "ds_read_b64_tr_b16 a[182:183], v6 offset:37376\n"
    "ds_read_b64_tr_b16 a[184:185], v6 offset:36872\n"
    "ds_read_b64_tr_b16 a[186:187], v6 offset:37384\n"
    "ds_read_b64_tr_b16 a[188:189], v7 offset:36864\n"
    "ds_read_b64_tr_b16 a[190:191], v7 offset:37376\n"
    "s_waitcnt vmcnt(24) lgkmcnt(0)\n"
    "s_barrier\n"
    "s_add_u32 m0, s59, 24576\n"
    "s_mov_b32 s74, 3833856\n"
    "buffer_load_dwordx4 v5, s[40:43], s74 offen lds\n"
    "s_add_u32 m0, s59, 28672\n"
    "s_mov_b32 s74, 3833984\n"
    "buffer_load_dwordx4 v5, s[40:43], s74 offen lds\n"
    "s_add_u32 m0, s59, 32768\n"
    "s_mov_b32 s74, 3834112\n"
    "buffer_load_dwordx4 v5, s[40:43], s74 offen lds\n"
    "s_add_u32 m0, s59, 36864\n"
    "s_mov_b32 s74, 3833856\n"
    "buffer_load_dwordx4 v5, s[44:47], s74 offen lds\n"
    "s_add_u32 m0, s59, 40960\n"
    "s_mov_b32 s74, 3833984\n"
    "buffer_load_dwordx4 v5, s[44:47], s74 offen lds\n"
    "s_add_u32 m0, s59, 45056\n"
    "s_mov_b32 s74, 3834112\n"
    "buffer_load_dwordx4 v5, s[44:47], s74 offen lds\n"
    "ds_read_b64_tr_b16 a[192:193], v16 offset:0\n"
    "ds_read_b64_tr_b16 a[194:195], v16 offset:512\n"
    "ds_read_b64_tr_b16 a[196:197], v16 offset:8\n"
    "ds_read_b64_tr_b16 a[198:199], v16 offset:520\n"
    "ds_read_b64_tr_b16 a[200:201], v18 offset:0\n"
    "ds_read_b64_tr_b16 a[202:203], v18 offset:512\n"
    "ds_read_b64_tr_b16 a[204:205], v16 offset:12288\n"
    "ds_read_b64_tr_b16 a[206:207], v16 offset:12800\n"
    "ds_read_b64_tr_b16 a[208:209], v16 offset:12296\n"
    "ds_read_b64_tr_b16 a[210:211], v16 offset:12808\n"
    "ds_read_b64_tr_b16 a[212:213], v18 offset:12288\n"
    "ds_read_b64_tr_b16 a[214:215], v18 offset:12800\n"
    "s_waitcnt vmcnt(24) lgkmcnt(0)\n"
    "s_barrier\n"
    "s_add_u32 m0, s59, 49152\n"
    "s_mov_b32 s74, 4128768\n"
    "buffer_load_dwordx4 v5, s[40:43], s74 offen lds\n"
    "s_add_u32 m0, s59, 53248\n"
    "s_mov_b32 s74, 4128896\n"
    "buffer_load_dwordx4 v5, s[40:43], s74 offen lds\n"
    "s_add_u32 m0, s59, 57344\n"
    "s_mov_b32 s74, 4129024\n"
    "buffer_load_dwordx4 v5, s[40:43], s74 offen lds\n"
    "s_add_u32 m0, s59, 61440\n"
    "s_mov_b32 s74, 4128768\n"
    "buffer_load_dwordx4 v5, s[44:47], s74 offen lds\n"
    "s_add_u32 m0, s59, 65536\n"
    "s_mov_b32 s74, 4128896\n"
    "buffer_load_dwordx4 v5, s[44:47], s74 offen lds\n"
    "s_add_u32 m0, s59, 69632\n"
    "s_mov_b32 s74, 4129024\n"
    "buffer_load_dwordx4 v5, s[44:47], s74 offen lds\n"
    "ds_read_b64_tr_b16 a[216:217], v16 offset:24576\n"
    "ds_read_b64_tr_b16 a[218:219], v16 offset:25088\n"
    "ds_read_b64_tr_b16 a[220:221], v16 offset:24584\n"
    "ds_read_b64_tr_b16 a[222:223], v16 offset:25096\n"
    "ds_read_b64_tr_b16 a[224:225], v18 offset:24576\n"
    "ds_read_b64_tr_b16 a[226:227], v18 offset:25088\n"
    "ds_read_b64_tr_b16 a[228:229], v16 offset:36864\n"
    "ds_read_b64_tr_b16 a[230:231], v16 offset:37376\n"
    "ds_read_b64_tr_b16 a[232:233], v16 offset:36872\n"
    "ds_read_b64_tr_b16 a[234:235], v16 offset:37384\n"
    "ds_read_b64_tr_b16 a[236:237], v18 offset:36864\n"
    "ds_read_b64_tr_b16 a[238:239], v18 offset:37376\n"
    "s_waitcnt vmcnt(24) lgkmcnt(0)\n"
    "s_barrier\n"
    "s_add_u32 m0, s59, 73728\n"
    "s_mov_b32 s74, 4423680\n"
    "buffer_load_dwordx4 v5, s[40:43], s74 offen lds\n"
    "s_add_u32 m0, s59, 77824\n"
    "s_mov_b32 s74, 4423808\n"
    "buffer_load_dwordx4 v5, s[40:43], s74 offen lds\n"
    "s_add_u32 m0, s59, 81920\n"
    "s_mov_b32 s74, 4423936\n"
    "buffer_load_dwordx4 v5, s[40:43], s74 offen lds\n"
    "s_add_u32 m0, s59, 86016\n"
    "s_mov_b32 s74, 4423680\n"
    "buffer_load_dwordx4 v5, s[44:47], s74 offen lds\n"
    "s_add_u32 m0, s59, 90112\n"
    "s_mov_b32 s74, 4423808\n"
    "buffer_load_dwordx4 v5, s[44:47], s74 offen lds\n"
    "s_add_u32 m0, s59, 94208\n"
    "s_mov_b32 s74, 4423936\n"
    "buffer_load_dwordx4 v5, s[44:47], s74 offen lds\n"
    "ds_read_b64_tr_b16 a[240:241], v17 offset:0\n"
    "ds_read_b64_tr_b16 a[242:243], v17 offset:512\n"
    "ds_read_b64_tr_b16 a[244:245], v17 offset:8\n"
    "ds_read_b64_tr_b16 a[246:247], v17 offset:520\n"
    "ds_read_b64_tr_b16 a[248:249], v19 offset:0\n"
    "ds_read_b64_tr_b16 a[250:251], v19 offset:512\n"
    "ds_read_b64_tr_b16 a[252:253], v17 offset:12288\n"
    "ds_read_b64_tr_b16 a[254:255], v17 offset:12800\n"
    "ds_read_b64_tr_b16 v[80:81], v17 offset:12296\n"
    "ds_read_b64_tr_b16 v[82:83], v17 offset:12808\n"
    "ds_read_b64_tr_b16 v[84:85], v19 offset:12288\n"
    "ds_read_b64_tr_b16 v[86:87], v19 offset:12800\n"
    "s_waitcnt vmcnt(24) lgkmcnt(0)\n"
    "s_barrier\n"
    "s_add_u32 m0, s59, 98304\n"
    "s_mov_b32 s74, 4718592\n"
    "buffer_load_dwordx4 v5, s[40:43], s74 offen lds\n"
    "s_add_u32 m0, s59, 102400\n"
    "s_mov_b32 s74, 4718720\n"
    "buffer_load_dwordx4 v5, s[40:43], s74 offen lds\n"
    "s_add_u32 m0, s59, 106496\n"
    "s_mov_b32 s74, 4718848\n"
    "buffer_load_dwordx4 v5, s[40:43], s74 offen lds\n"
    "s_add_u32 m0, s59, 110592\n"
    "s_mov_b32 s74, 4718592\n"
    "buffer_load_dwordx4 v5, s[44:47], s74 offen lds\n"
    "s_add_u32 m0, s59, 114688\n"
    "s_mov_b32 s74, 4718720\n"
    "buffer_load_dwordx4 v5, s[44:47], s74 offen lds\n"
    "s_add_u32 m0, s59, 118784\n"
    "s_mov_b32 s74, 4718848\n"
    "buffer_load_dwordx4 v5, s[44:47], s74 offen lds\n"
    "ds_read_b64_tr_b16 v[88:89], v17 offset:24576\n"
    "ds_read_b64_tr_b16 v[90:91], v17 offset:25088\n"
    "ds_read_b64_tr_b16 v[92:93], v17 offset:24584\n"
    "ds_read_b64_tr_b16 v[94:95], v17 offset:25096\n"
    "ds_read_b64_tr_b16 v[96:97], v19 offset:24576\n"
    "ds_read_b64_tr_b16 v[98:99], v19 offset:25088\n"
    "ds_read_b64_tr_b16 v[100:101], v17 offset:36864\n"
    "ds_read_b64_tr_b16 v[102:103], v17 offset:37376\n"
    "ds_read_b64_tr_b16 v[104:105], v17 offset:36872\n"
    "ds_read_b64_tr_b16 v[106:107], v17 offset:37384\n"
    "ds_read_b64_tr_b16 v[108:109], v19 offset:36864\n"
    "ds_read_b64_tr_b16 v[110:111], v19 offset:37376\n"
    "s_waitcnt vmcnt(24) lgkmcnt(0)\n"
    "s_barrier\n"
    "s_add_u32 m0, s59, 122880\n"
    "s_mov_b32 s74, 5013504\n"
    "buffer_load_dwordx4 v5, s[40:43], s74 offen lds\n"
    "s_add_u32 m0, s59, 126976\n"
    "s_mov_b32 s74, 5013632\n"
    "buffer_load_dwordx4 v5, s[40:43], s74 offen lds\n"
    "s_add_u32 m0, s59, 131072\n"
    "s_mov_b32 s74, 5013760\n"
    "buffer_load_dwordx4 v5, s[40:43], s74 offen lds\n"
    "s_add_u32 m0, s59, 135168\n"
    "s_mov_b32 s74, 5013504\n"
    "buffer_load_dwordx4 v5, s[44:47], s74 offen lds\n"
    "s_add_u32 m0, s59, 139264\n"
    "s_mov_b32 s74, 5013632\n"
    "buffer_load_dwordx4 v5, s[44:47], s74 offen lds\n"
    "s_add_u32 m0, s59, 143360\n"
    "s_mov_b32 s74, 5013760\n"
    "buffer_load_dwordx4 v5, s[44:47], s74 offen lds\n"
    "buffer_load_dwordx2 v[12:13], off, s[36:39], 0 sc1\n"
    "ds_read_b64_tr_b16 v[112:113], v6 offset:0\n"
    "ds_read_b64_tr_b16 v[114:115], v6 offset:512\n"
    "ds_read_b64_tr_b16 v[116:117], v6 offset:8\n"
    "ds_read_b64_tr_b16 v[118:119], v6 offset:520\n"
    "ds_read_b64_tr_b16 v[120:121], v7 offset:0\n"
    "ds_read_b64_tr_b16 v[122:123], v7 offset:512\n"
    "ds_read_b64_tr_b16 v[124:125], v6 offset:12288\n"
    "ds_read_b64_tr_b16 v[126:127], v6 offset:12800\n"
    "ds_read_b64_tr_b16 v[128:129], v6 offset:12296\n"
    "ds_read_b64_tr_b16 v[130:131], v6 offset:12808\n"
    "ds_read_b64_tr_b16 v[132:133], v7 offset:12288\n"
    "ds_read_b64_tr_b16 v[134:135], v7 offset:12800\n"
    "s_waitcnt vmcnt(25) lgkmcnt(0)\n"
    "s_barrier\n"
    "ds_read_b64_tr_b16 v[136:137], v6 offset:24576\n"
    "ds_read_b64_tr_b16 v[138:139], v6 offset:25088\n"
    "ds_read_b64_tr_b16 v[140:141], v6 offset:24584\n"
    "ds_read_b64_tr_b16 v[142:143], v6 offset:25096\n"
    "ds_read_b64_tr_b16 v[144:145], v7 offset:24576\n"
    "ds_read_b64_tr_b16 v[146:147], v7 offset:25088\n"
    "ds_read_b64_tr_b16 v[148:149], v6 offset:36864\n"
    "ds_read_b64_tr_b16 v[150:151], v6 offset:37376\n"
    "ds_read_b64_tr_b16 v[152:153], v6 offset:36872\n"
    "ds_read_b64_tr_b16 v[154:155], v6 offset:37384\n"
    "ds_read_b64_tr_b16 v[156:157], v7 offset:36864\n"
    "ds_read_b64_tr_b16 v[158:159], v7 offset:37376\n"
    "s_waitcnt vmcnt(19) lgkmcnt(0)\n"
    "s_barrier\n"
    "ds_read_b64_tr_b16 v[160:161], v16 offset:0\n"
    "ds_read_b64_tr_b16 v[162:163], v16 offset:512\n"
    "ds_read_b64_tr_b16 v[164:165], v16 offset:8\n"
    "ds_read_b64_tr_b16 v[166:167], v16 offset:520\n"
    "ds_read_b64_tr_b16 v[168:169], v18 offset:0\n"
    "ds_read_b64_tr_b16 v[170:171], v18 offset:512\n"
    "ds_read_b64_tr_b16 v[172:173], v16 offset:12288\n"
    "ds_read_b64_tr_b16 v[174:175], v16 offset:12800\n"
    "ds_read_b64_tr_b16 v[176:177], v16 offset:12296\n"
    "ds_read_b64_tr_b16 v[178:179], v16 offset:12808\n"
    "ds_read_b64_tr_b16 v[180:181], v18 offset:12288\n"
    "ds_read_b64_tr_b16 v[182:183], v18 offset:12800\n"
    "s_waitcnt vmcnt(13) lgkmcnt(0)\n"
    "s_barrier\n"
    "ds_read_b64_tr_b16 v[184:185], v16 offset:24576\n"
    "ds_read_b64_tr_b16 v[186:187], v16 offset:25088\n"
    "ds_read_b64_tr_b16 v[188:189], v16 offset:24584\n"
    "ds_read_b64_tr_b16 v[190:191], v16 offset:25096\n"
    "ds_read_b64_tr_b16 v[192:193], v18 offset:24576\n"
    "ds_read_b64_tr_b16 v[194:195], v18 offset:25088\n"
    "ds_read_b64_tr_b16 v[196:197], v16 offset:36864\n"
    "ds_read_b64_tr_b16 v[198:199], v16 offset:37376\n"
    "ds_read_b64_tr_b16 v[200:201], v16 offset:36872\n"
    "ds_read_b64_tr_b16 v[202:203], v16 offset:37384\n"
    "ds_read_b64_tr_b16 v[204:205], v18 offset:36864\n"
    "ds_read_b64_tr_b16 v[206:207], v18 offset:37376\n"
    "s_waitcnt vmcnt(7) lgkmcnt(0)\n"
    "s_barrier\n"
    "ds_read_b64_tr_b16 v[208:209], v17 offset:0\n"
    "ds_read_b64_tr_b16 v[210:211], v17 offset:512\n"
    "ds_read_b64_tr_b16 v[212:213], v17 offset:8\n"
    "ds_read_b64_tr_b16 v[214:215], v17 offset:520\n"
    "ds_read_b64_tr_b16 v[216:217], v19 offset:0\n"
    "ds_read_b64_tr_b16 v[218:219], v19 offset:512\n"
    "ds_read_b64_tr_b16 v[220:221], v17 offset:12288\n"
    "ds_read_b64_tr_b16 v[222:223], v17 offset:12800\n"
    "ds_read_b64_tr_b16 v[224:225], v17 offset:12296\n"
    "ds_read_b64_tr_b16 v[226:227], v17 offset:12808\n"
    "ds_read_b64_tr_b16 v[228:229], v19 offset:12288\n"
    "ds_read_b64_tr_b16 v[230:231], v19 offset:12800\n"
    "s_waitcnt vmcnt(0) lgkmcnt(0)\n"
    "s_barrier\n"
    ".LgUP_par_%=:\n"
    "v_readfirstlane_b32 s74, v12\n"
    "v_readfirstlane_b32 s75, v13\n"
    "s_xor_b32 s74, s74, s66\n"
    "s_xor_b32 s75, s75, s67\n"
    "s_cselect_b32 s75, 2, 0\n"
    "s_or_b32 s75, s75, s74\n"
    "s_cmp_lt_u32 s75, 2\n"
    "s_cbranch_scc1 .LgUP_par1_%=\n"
    "s_sleep 4\n"
    "buffer_load_dwordx2 v[12:13], off, s[36:39], 0 sc1\n"
    "s_waitcnt vmcnt(0)\n"
    "s_sub_u32 s72, s72, 1\n"
    "s_cbranch_scc0 .LgUP_par_%=\n"
    "s_mov_b32 s72, 0\n"
    ".LgUP_par1_%=:\n"
    "s_and_b32 s74, s74, 1\n"
    "s_lshl_b32 s74, s74, 13\n"
    "s_add_u32 s36, s36, s74\n"
    "s_addc_u32 s37, s37, 0\n"
    "v_add_u32 v13, v11, v3\n"
    "v_cmp_gt_i32 vcc, s71, v13\n"
    "s_nop 0\n"
    "s_or_b64 exec, vcc, s[62:63]\n"
    "buffer_atomic_smax v11, v2, s[36:39], 0 offen sc0 sc1\n"
    "s_mov_b64 exec, -1\n"
    "s_getpc_b64 s[84:85]\n"
    ".LgUP_PC2_%=:\n"
    "s_and_b32 s85, s85, 0xffff\n"
    "s_mov_b32 s86, .LgUP_FIN_%=-.LgUP_PC2_%=\n"
    "s_mov_b32 s87, 0x20000\n"
    "v_mbcnt_lo_u32_b32 v42, -1, 0\n"
    "v_mbcnt_hi_u32_b32 v42, -1, v42\n"
    "v_lshlrev_b32 v42, 7, v42\n"
    "buffer_load_dword v43, v42, s[84:87], 0 offen\n"
    "v_add_u32 v42, 8192, v42\n"
    "buffer_load_dword v43, v42, s[84:87], 0 offen\n"
    "v_add_u32 v42, 8192, v42\n"
    "buffer_load_dword v43, v42, s[84:87], 0 offen\n"
    "ds_read_b64_tr_b16 v[232:233], v17 offset:24576\n"
    "ds_read_b64_tr_b16 v[234:235], v17 offset:25088\n"
    "ds_read_b64_tr_b16 v[236:237], v17 offset:24584\n"
    "ds_read_b64_tr_b16 v[238:239], v17 offset:25096\n"
    "ds_read_b64_tr_b16 v[240:241], v19 offset:24576\n"
    "ds_read_b64_tr_b16 v[242:243], v19 offset:25088\n"
    "ds_read_b64_tr_b16 v[244:245], v17 offset:36864\n"
    "ds_read_b64_tr_b16 v[246:247], v17 offset:37376\n"
    "ds_read_b64_tr_b16 v[248:249], v17 offset:36872\n"
    "ds_read_b64_tr_b16 v[250:251], v17 offset:37384\n"
    "ds_read_b64_tr_b16 v[252:253], v19 offset:36864\n"
    "ds_read_b64_tr_b16 v[254:255], v19 offset:37376\n"
    "s_waitcnt lgkmcnt(0)\n"
    "s_barrier\n"
    "s_waitcnt vmcnt(0)\n"
    "v_mov_b32 v5, v11\n"
    "v_mov_b32 v12, -1\n"
    "s_mov_b32 s73, 0x80000000\n"
    "s_branch .LgUP_block1_1_%=\n"
    ".LgUP_LOOPB_%=:\n"
    ".LgUP_head0_%=:\n"
    "v_mfma_f32_16x16x32_bf16 v[32:35], a[0:3], v[16:19], 0\n"
    "v_mfma_f32_16x16x32_bf16 v[36:39], a[4:7], v[16:19], 0\n"
    "v_mul_f32 v64, v40, v64\n"
    "v_mul_f32 v65, v41, v65\n"
    "v_mfma_f32_16x16x32_bf16 v[48:51], a[8:11], v[16:19], 0\n"
    "v_mul_f32 v66, v42, v66\n"
    "v_mul_f32 v67, v43, v67\n"
    "v_mfma_f32_16x16x32_bf16 v[56:59], a[12:15], v[16:19], 0\n"
    "v_mul_f32 v68, v44, v68\n"
    "v_mul_f32 v69, v45, v69\n"
    "v_mfma_f32_16x16x32_bf16 v[60:63], a[16:19], v[16:19], 0\n"
    "s_waitcnt lgkmcnt(1)\n"
    "v_mul_f32 v70, v46, v70\n"
    "v_mul_f32 v71, v47, v71\n"
    "v_mfma_f32_16x16x32_bf16 v[72:75], a[20:23], v[16:19], 0\n"
    "ds_read_b128 v[16:19], v0 offset:3072\n"
    "v_mul_f32 v40, 0xbfb8aa3b, v40\n"
    "v_mfma_f32_16x16x32_bf16 v[32:35], a[24:27], v[20:23], v[32:35]\n"
    "v_exp_f32 v40, v40\n"
    "v_mfma_f32_16x16x32_bf16 v[36:39], a[28:31], v[20:23], v[36:39]\n"
    "s_waitcnt vmcnt(1)\n"
    "v_mul_f32 v41, 0xbfb8aa3b, v41\n"
    "v_add_u32 v13, v11, v3\n"
    "v_mfma_f32_16x16x32_bf16 v[48:51], a[32:35], v[20:23], v[48:51]\n"
    "v_mul_f32 v42, 0xbfb8aa3b, v42\n"
    "v_cmp_gt_i32 vcc, s71, v13\n"
    "v_mfma_f32_16x16x32_bf16 v[56:59], a[36:39], v[20:23], v[56:59]\n"
    "buffer_atomic_smax v7, v10, s[36:39], s69 offen sc1\n"
    "v_exp_f32 v41, v41\n"
    "v_mfma_f32_16x16x32_bf16 v[60:63], a[40:43], v[20:23], v[60:63]\n"
    "s_waitcnt lgkmcnt(1)\n"
    "v_exp_f32 v42, v42\n"
    "v_mfma_f32_16x16x32_bf16 v[72:75], a[44:47], v[20:23], v[72:75]\n"
    "ds_read_b128 v[20:23], v0 offset:4096\n"
    "s_cbranch_vccnz .LgUP_poll0_%=\n"
    "v_mfma_f32_16x16x32_bf16 v[32:35], a[48:51], v[24:27], v[32:35]\n"
    "s_add_u32 m0, s59, 20480\n"
    "v_mfma_f32_16x16x32_bf16 v[36:39], a[52:55], v[24:27], v[36:39]\n"
    "buffer_load_dwordx4 v1, s[36:39], s76 offen sc1 lds\n"
    "v_mul_f32 v43, 0xbfb8aa3b, v43\n"
    "v_mul_f32 v44, 0xbfb8aa3b, v44\n"
    "v_mfma_f32_16x16x32_bf16 v[48:51], a[56:59], v[24:27], v[48:51]\n"
    "v_exp_f32 v43, v43\n"
    "v_mfma_f32_16x16x32_bf16 v[56:59], a[60:63], v[24:27], v[56:59]\n"
    "v_exp_f32 v44, v44\n"
    "v_mfma_f32_16x16x32_bf16 v[60:63], a[64:67], v[24:27], v[60:63]\n"
    "s_waitcnt lgkmcnt(1)\n"
    "v_mul_f32 v45, 0xbfb8aa3b, v45\n"
    "v_mul_f32 v46, 0xbfb8aa3b, v46\n"
    "v_mfma_f32_16x16x32_bf16 v[72:75], a[68:71], v[24:27], v[72:75]\n"
    "ds_read_b128 v[24:27], v0 offset:5120\n"
    "v_mul_f32 v47, 0xbfb8aa3b, v47\n"
    "v_mfma_f32_16x16x32_bf16 v[32:35], a[72:75], v[16:19], v[32:35]\n"
    "s_add_u32 s76, s76, 16384\n"
    "v_exp_f32 v45, v45\n"
    "v_mfma_f32_16x16x32_bf16 v[36:39], a[76:79], v[16:19], v[36:39]\n"
    "s_add_u32 m0, s59, 24576\n"
    "v_exp_f32 v46, v46\n"
    "v_mfma_f32_16x16x32_bf16 v[48:51], a[80:83], v[16:19], v[48:51]\n"
    "buffer_load_dwordx4 v1, s[36:39], s76 offen sc1 lds\n"
    "v_exp_f32 v47, v47\n"
    "v_mfma_f32_16x16x32_bf16 v[56:59], a[84:87], v[16:19], v[56:59]\n"
    "v_add_f32 v40, 1.0, v40\n"
    "v_add_f32 v41, 1.0, v41\n"
    "v_mfma_f32_16x16x32_bf16 v[60:63], a[88:91], v[16:19], v[60:63]\n"
    "s_waitcnt lgkmcnt(1)\n"
    "v_rcp_f32 v40, v40\n"
    "v_mfma_f32_16x16x32_bf16 v[72:75], a[92:95], v[16:19], v[72:75]\n"
    "ds_read_b128 v[16:19], v0 offset:6144\n"
    "v_add_f32 v42, 1.0, v42\n"
    "v_mfma_f32_16x16x32_bf16 v[32:35], a[96:99], v[20:23], v[32:35]\n"
    "s_add_u32 s76, s76, 16384\n"
    "v_rcp_f32 v41, v41\n"
    "v_mfma_f32_16x16x32_bf16 v[36:39], a[100:103], v[20:23], v[36:39]\n"
    "s_add_u32 m0, s59, 28672\n"
    "v_add_f32 v43, 1.0, v43\n"
    "v_mfma_f32_16x16x32_bf16 v[48:51], a[104:107], v[20:23], v[48:51]\n"
    "buffer_load_dwordx4 v1, s[36:39], s76 offen sc1 lds\n"
    "v_add_f32 v44, 1.0, v44\n"
    "v_mfma_f32_16x16x32_bf16 v[56:59], a[108:111], v[20:23], v[56:59]\n"
    "v_rcp_f32 v42, v42\n"
    "v_mfma_f32_16x16x32_bf16 v[60:63], a[112:115], v[20:23], v[60:63]\n"
    "s_waitcnt lgkmcnt(1)\n"
    "v_rcp_f32 v43, v43\n"
    "v_mfma_f32_16x16x32_bf16 v[72:75], a[116:119], v[20:23], v[72:75]\n"
    "ds_read_b128 v[20:23], v0 offset:7168\n"
    "s_add_u32 s76, s76, 16384\n"
    "v_mfma_f32_16x16x32_bf16 v[32:35], a[120:123], v[24:27], v[32:35]\n"
    "s_add_u32 m0, s59, 32768\n"
    "v_mfma_f32_16x16x32_bf16 v[36:39], a[124:127], v[24:27], v[36:39]\n"
    "buffer_load_dwordx4 v1, s[36:39], s76 offen sc1 lds\n"
    "v_rcp_f32 v44, v44\n"
    "v_mfma_f32_16x16x32_bf16 v[48:51], a[128:131], v[24:27], v[48:51]\n"
    "v_add_f32 v45, 1.0, v45\n"
    "v_add_f32 v46, 1.0, v46\n"
    "v_mfma_f32_16x16x32_bf16 v[56:59], a[132:135], v[24:27], v[56:59]\n"
    "v_rcp_f32 v45, v45\n"
    "v_mfma_f32_16x16x32_bf16 v[60:63], a[136:139], v[24:27], v[60:63]\n"
    "s_waitcnt lgkmcnt(1)\n"
    "v_rcp_f32 v46, v46\n"
    "v_mfma_f32_16x16x32_bf16 v[72:75], a[140:143], v[24:27], v[72:75]\n"
    "ds_read_b128 v[24:27], v0 offset:8192\n"
    "v_add_f32 v47, 1.0, v47\n"
    "v_mfma_f32_16x16x32_bf16 v[32:35], a[144:147], v[16:19], v[32:35]\n"
    "s_add_u32 s76, s76, 16384\n"
    "v_rcp_f32 v47, v47\n"
    "v_mfma_f32_16x16x32_bf16 v[36:39], a[148:151], v[16:19], v[36:39]\n"
    "s_add_u32 m0, s59, 36864\n"
    "v_mul_f32 v40, v40, v64\n"
    "v_mul_f32 v41, v41, v65\n"
    "v_mfma_f32_16x16x32_bf16 v[48:51], a[152:155], v[16:19], v[48:51]\n"
    "buffer_load_dwordx4 v8, s[36:39], s76 offen sc1 lds\n"
    "v_mul_f32 v42, v42, v66\n"
    "v_mul_f32 v43, v43, v67\n"
    "v_mfma_f32_16x16x32_bf16 v[56:59], a[156:159], v[16:19], v[56:59]\n"
    "v_mul_f32 v44, v44, v68\n"
    "v_mul_f32 v45, v45, v69\n"
    "v_mfma_f32_16x16x32_bf16 v[60:63], a[160:163], v[16:19], v[60:63]\n"
    "s_waitcnt lgkmcnt(1)\n"
    "v_mul_f32 v46, v46, v70\n"
    "v_mul_f32 v47, v47, v71\n"
    "v_mfma_f32_16x16x32_bf16 v[72:75], a[164:167], v[16:19], v[72:75]\n"
    "ds_read_b128 v[16:19], v0 offset:9216\n"
    "v_cvt_pk_bf16_f32 v40, v40, v41\n"
    "v_mfma_f32_16x16x32_bf16 v[32:35], a[168:171], v[20:23], v[32:35]\n"
    "v_cvt_pk_bf16_f32 v41, v42, v43\n"
    "v_cvt_pk_bf16_f32 v42, v44, v45\n"
    "v_mfma_f32_16x16x32_bf16 v[36:39], a[172:175], v[20:23], v[36:39]\n"
    "v_cvt_pk_bf16_f32 v43, v46, v47\n"
    "v_mfma_f32_16x16x32_bf16 v[48:51], a[176:179], v[20:23], v[48:51]\n"
    "v_mul_f32 v76, v52, v76\n"
    "v_mfma_f32_16x16x32_bf16 v[56:59], a[180:183], v[20:23], v[56:59]\n"
    "buffer_store_dwordx4 v[40:43], v15, s[36:39], s73 offen offset:1024 sc1\n"
    "v_mfma_f32_16x16x32_bf16 v[60:63], a[184:187], v[20:23], v[60:63]\n"
    "s_waitcnt lgkmcnt(1)\n"
    "v_mul_f32 v77, v53, v77\n"
    "v_mul_f32 v78, v54, v78\n"
    "v_mfma_f32_16x16x32_bf16 v[72:75], a[188:191], v[20:23], v[72:75]\n"
    "ds_read_b128 v[20:23], v0 offset:10240\n"
    "v_mfma_f32_16x16x32_bf16 v[32:35], a[192:195], v[24:27], v[32:35]\n"
    "v_mfma_f32_16x16x32_bf16 v[36:39], a[196:199], v[24:27], v[36:39]\n"
    "v_mul_f32 v79, v55, v79\n"
    "v_mul_f32 v52, 0xbfb8aa3b, v52\n"
    "v_mfma_f32_16x16x32_bf16 v[48:51], a[200:203], v[24:27], v[48:51]\n"
    "v_exp_f32 v52, v52\n"
    "v_mfma_f32_16x16x32_bf16 v[56:59], a[204:207], v[24:27], v[56:59]\n"
    "v_mul_f32 v53, 0xbfb8aa3b, v53\n"
    "v_mul_f32 v54, 0xbfb8aa3b, v54\n"
    "v_mfma_f32_16x16x32_bf16 v[60:63], a[208:211], v[24:27], v[60:63]\n"
    "s_waitcnt lgkmcnt(1)\n"
    "v_exp_f32 v53, v53\n"
    "v_mfma_f32_16x16x32_bf16 v[72:75], a[212:215], v[24:27], v[72:75]\n"
    "ds_read_b128 v[24:27], v0 offset:11264\n"
    "v_mul_f32 v55, 0xbfb8aa3b, v55\n"
    "v_mfma_f32_16x16x32_bf16 v[32:35], a[216:219], v[16:19], v[32:35]\n"
    "v_exp_f32 v54, v54\n"
    "v_mfma_f32_16x16x32_bf16 v[36:39], a[220:223], v[16:19], v[36:39]\n"
    "v_exp_f32 v55, v55\n"
    "v_mfma_f32_16x16x32_bf16 v[48:51], a[224:227], v[16:19], v[48:51]\n"
    "v_add_f32 v52, 1.0, v52\n"
    "v_add_f32 v53, 1.0, v53\n"
    "v_mfma_f32_16x16x32_bf16 v[56:59], a[228:231], v[16:19], v[56:59]\n"
    "v_rcp_f32 v52, v52\n"
    "v_mfma_f32_16x16x32_bf16 v[60:63], a[232:235], v[16:19], v[60:63]\n"
    "s_waitcnt lgkmcnt(1)\n"
    "v_rcp_f32 v53, v53\n"
    "v_mfma_f32_16x16x32_bf16 v[72:75], a[236:239], v[16:19], v[72:75]\n"
    "ds_read_b128 v[16:19], v0 offset:12288\n"
    "v_add_f32 v54, 1.0, v54\n"
    "v_mfma_f32_16x16x32_bf16 v[32:35], a[240:243], v[20:23], v[32:35]\n"
    "v_rcp_f32 v54, v54\n"
    "v_mfma_f32_16x16x32_bf16 v[36:39], a[244:247], v[20:23], v[36:39]\n"
    "v_add_f32 v55, 1.0, v55\n"
    "v_mfma_f32_16x16x32_bf16 v[48:51], a[248:251], v[20:23], v[48:51]\n"
    "v_mul_f32 v52, v52, v76\n"
    "v_mfma_f32_16x16x32_bf16 v[56:59], a[252:255], v[20:23], v[56:59]\n"
    "v_rcp_f32 v55, v55\n"
    "v_mfma_f32_16x16x32_bf16 v[60:63], v[80:83], v[20:23], v[60:63]\n"
    "s_waitcnt lgkmcnt(1)\n"
    "v_mul_f32 v53, v53, v77\n"
    "v_mul_f32 v54, v54, v78\n"
    "v_mfma_f32_16x16x32_bf16 v[72:75], v[84:87], v[20:23], v[72:75]\n"
    "ds_read_b128 v[20:23], v0 offset:13312\n"
    "v_mfma_f32_16x16x32_bf16 v[32:35], v[88:91], v[24:27], v[32:35]\n"
    "v_mfma_f32_16x16x32_bf16 v[36:39], v[92:95], v[24:27], v[36:39]\n"
    "v_mul_f32 v55, v55, v79\n"
    "v_cvt_pk_bf16_f32 v52, v52, v53\n"
    "v_mfma_f32_16x16x32_bf16 v[48:51], v[96:99], v[24:27], v[48:51]\n"
    "v_cvt_pk_bf16_f32 v53, v54, v55\n"
    "s_nop 0\n"
    "v_mfma_f32_16x16x32_bf16 v[56:59], v[100:103], v[24:27], v[56:59]\n"
    "v_permlane16_swap_b32 v52, v54\n"
    "v_mfma_f32_16x16x32_bf16 v[60:63], v[104:107], v[24:27], v[60:63]\n"
    "s_waitcnt lgkmcnt(1)\n"
    "v_permlane16_swap_b32 v53, v55\n"
    "v_mfma_f32_16x16x32_bf16 v[72:75], v[108:111], v[24:27], v[72:75]\n"
    "ds_read_b128 v[24:27], v0 offset:14336\n"
    ".LgUP_join0_%=:\n"
    "v_mfma_f32_16x16x32_bf16 v[32:35], v[112:115], v[16:19], v[32:35]\n"
    "buffer_store_dwordx4 v[52:55], v4, s[36:39], s73 offen offset:1024 sc1\n"
    "v_mfma_f32_16x16x32_bf16 v[36:39], v[116:119], v[16:19], v[36:39]\n"
    "s_lshr_b32 s73, s68, 1\n"
    "v_mfma_f32_16x16x32_bf16 v[48:51], v[120:123], v[16:19], v[48:51]\n"
    "s_and_b32 s73, s73, s57\n"
    "v_mfma_f32_16x16x32_bf16 v[56:59], v[124:127], v[16:19], v[56:59]\n"
    "s_mul_i32 s73, s73, s56\n"
    "v_mfma_f32_16x16x32_bf16 v[60:63], v[128:131], v[16:19], v[60:63]\n"
    "s_waitcnt lgkmcnt(1)\n"
    "s_add_u32 s73, s73, s55\n"
    "v_mfma_f32_16x16x32_bf16 v[72:75], v[132:135], v[16:19], v[72:75]\n"
    "ds_read_b128 v[16:19], v0 offset:15360\n"
    "v_mfma_f32_16x16x32_bf16 v[32:35], v[136:139], v[20:23], v[32:35]\n"
    "s_add_u32 s71, s68, 2\n"
    "s_waitcnt vmcnt(6)\n"
    "v_mfma_f32_16x16x32_bf16 v[36:39], v[140:143], v[20:23], v[36:39]\n"
    "v_add_u32 v13, v11, v3\n"
    "v_mfma_f32_16x16x32_bf16 v[48:51], v[144:147], v[20:23], v[48:51]\n"
    "v_cmp_gt_i32 vcc, s71, v13\n"
    "v_mfma_f32_16x16x32_bf16 v[56:59], v[148:151], v[20:23], v[56:59]\n"
    "v_mfma_f32_16x16x32_bf16 v[60:63], v[152:155], v[20:23], v[60:63]\n"
    "s_waitcnt lgkmcnt(1)\n"
    "v_mfma_f32_16x16x32_bf16 v[72:75], v[156:159], v[20:23], v[72:75]\n"
    "ds_read_b128 v[20:23], v0 offset:16384\n"
    "v_mfma_f32_16x16x32_bf16 v[32:35], v[160:163], v[24:27], v[32:35]\n"
    "s_or_b64 exec, vcc, s[62:63]\n"
    "buffer_atomic_smax v11, v2, s[36:39], 0 offen sc0 sc1\n"
    "s_mov_b64 exec, -1\n"
    "v_mfma_f32_16x16x32_bf16 v[36:39], v[164:167], v[24:27], v[36:39]\n"
    "s_lshr_b32 s76, s71, 1\n"
    "v_mfma_f32_16x16x32_bf16 v[48:51], v[168:171], v[24:27], v[48:51]\n"
    "s_and_b32 s76, s76, s54\n"
    "v_mfma_f32_16x16x32_bf16 v[56:59], v[172:175], v[24:27], v[56:59]\n"
    "s_mul_i32 s76, s76, s53\n"
    "v_mfma_f32_16x16x32_bf16 v[60:63], v[176:179], v[24:27], v[60:63]\n"
    "s_waitcnt lgkmcnt(1)\n"
    "s_add_u32 s76, s76, s52\n"
    "v_mfma_f32_16x16x32_bf16 v[72:75], v[180:183], v[24:27], v[72:75]\n"
    "ds_read_b128 v[24:27], v0 offset:17408\n"
    "v_mfma_f32_16x16x32_bf16 v[32:35], v[184:187], v[16:19], v[32:35]\n"
    "v_mfma_f32_16x16x32_bf16 v[36:39], v[188:191], v[16:19], v[36:39]\n"
    "v_mfma_f32_16x16x32_bf16 v[48:51], v[192:195], v[16:19], v[48:51]\n"
    "v_mfma_f32_16x16x32_bf16 v[56:59], v[196:199], v[16:19], v[56:59]\n"
    "s_waitcnt vmcnt(2) lgkmcnt(0)\n"
    "s_barrier\n"
    "v_mfma_f32_16x16x32_bf16 v[60:63], v[200:203], v[16:19], v[60:63]\n"
    "v_mfma_f32_16x16x32_bf16 v[72:75], v[204:207], v[16:19], v[72:75]\n"
    "ds_read_b128 v[16:19], v9 offset:0\n"
    "v_mfma_f32_16x16x32_bf16 v[32:35], v[208:211], v[20:23], v[32:35]\n"
    "v_mfma_f32_16x16x32_bf16 v[36:39], v[212:215], v[20:23], v[36:39]\n"
    "v_add_u32 v13, v5, v3\n"
    "v_mfma_f32_16x16x32_bf16 v[48:51], v[216:219], v[20:23], v[48:51]\n"
    "v_cmp_gt_i32 vcc, s71, v13\n"
    "v_mfma_f32_16x16x32_bf16 v[56:59], v[220:223], v[20:23], v[56:59]\n"
    "v_mfma_f32_16x16x32_bf16 v[60:63], v[224:227], v[20:23], v[60:63]\n"
    "v_mfma_f32_16x16x32_bf16 v[72:75], v[228:231], v[20:23], v[72:75]\n"
    "ds_read_b128 v[20:23], v9 offset:1024\n"
    "v_mfma_f32_16x16x32_bf16 v[32:35], v[232:235], v[24:27], v[32:35]\n"
    "s_or_b64 exec, vcc, s[62:63]\n"
    "buffer_atomic_smax v5, v2, s[36:39], 0 offen sc0 sc1\n"
    "s_mov_b64 exec, -1\n"
    "v_mfma_f32_16x16x32_bf16 v[36:39], v[236:239], v[24:27], v[36:39]\n"
    "v_mfma_f32_16x16x32_bf16 v[48:51], v[240:243], v[24:27], v[48:51]\n"
    "v_mov_b32 v7, v12\n"
    "v_mfma_f32_16x16x32_bf16 v[56:59], v[244:247], v[24:27], v[56:59]\n"
    "s_mov_b32 s69, s70\n"
    "s_mov_b32 s70, 0\n"
    "v_mov_b32 v12, s68\n"
    "v_mfma_f32_16x16x32_bf16 v[60:63], v[248:251], v[24:27], v[60:63]\n"
    "s_waitcnt lgkmcnt(1)\n"
    "s_add_u32 s68, s68, 1\n"
    "v_mfma_f32_16x16x32_bf16 v[72:75], v[252:255], v[24:27], v[72:75]\n"
    "ds_read_b128 v[24:27], v9 offset:2048\n"
    ".LgUP_head1_%=:\n"
    "v_mfma_f32_16x16x32_bf16 v[40:43], a[0:3], v[16:19], 0\n"
    "v_mfma_f32_16x16x32_bf16 v[44:47], a[4:7], v[16:19], 0\n"
    "v_mul_f32 v56, v32, v56\n"
    "v_mul_f32 v57, v33, v57\n"
    "v_mfma_f32_16x16x32_bf16 v[52:55], a[8:11], v[16:19], 0\n"
    "v_mul_f32 v58, v34, v58\n"
    "v_mul_f32 v59, v35, v59\n"
    "v_mfma_f32_16x16x32_bf16 v[64:67], a[12:15], v[16:19], 0\n"
    "v_mul_f32 v60, v36, v60\n"
    "v_mul_f32 v61, v37, v61\n"
    "v_mfma_f32_16x16x32_bf16 v[68:71], a[16:19], v[16:19], 0\n"
    "s_waitcnt lgkmcnt(1)\n"
    "v_mul_f32 v62, v38, v62\n"
    "v_mul_f32 v63, v39, v63\n"
    "v_mfma_f32_16x16x32_bf16 v[76:79], a[20:23], v[16:19], 0\n"
    "ds_read_b128 v[16:19], v9 offset:3072\n"
    "v_mul_f32 v32, 0xbfb8aa3b, v32\n"
    "v_mfma_f32_16x16x32_bf16 v[40:43], a[24:27], v[20:23], v[40:43]\n"
    "v_exp_f32 v32, v32\n"
    "v_mfma_f32_16x16x32_bf16 v[44:47], a[28:31], v[20:23], v[44:47]\n"
    "s_waitcnt vmcnt(1)\n"
    "v_mul_f32 v33, 0xbfb8aa3b, v33\n"
    "v_add_u32 v13, v11, v3\n"
    "v_mfma_f32_16x16x32_bf16 v[52:55], a[32:35], v[20:23], v[52:55]\n"
    "v_mul_f32 v34, 0xbfb8aa3b, v34\n"
    "v_cmp_gt_i32 vcc, s71, v13\n"
    "v_mfma_f32_16x16x32_bf16 v[64:67], a[36:39], v[20:23], v[64:67]\n"
    "buffer_atomic_smax v7, v14, s[36:39], s69 offen sc1\n"
    "v_exp_f32 v33, v33\n"
    "v_mfma_f32_16x16x32_bf16 v[68:71], a[40:43], v[20:23], v[68:71]\n"
    "s_waitcnt lgkmcnt(1)\n"
    "v_exp_f32 v34, v34\n"
    "v_mfma_f32_16x16x32_bf16 v[76:79], a[44:47], v[20:23], v[76:79]\n"
    "ds_read_b128 v[20:23], v9 offset:4096\n"
    "s_cbranch_vccnz .LgUP_poll1_%=\n"
    "v_mfma_f32_16x16x32_bf16 v[40:43], a[48:51], v[24:27], v[40:43]\n"
    "s_add_u32 m0, s59, 0\n"
    "v_mfma_f32_16x16x32_bf16 v[44:47], a[52:55], v[24:27], v[44:47]\n"
    "buffer_load_dwordx4 v1, s[36:39], s76 offen sc1 lds\n"
    "v_mul_f32 v35, 0xbfb8aa3b, v35\n"
    "v_mul_f32 v36, 0xbfb8aa3b, v36\n"
    "v_mfma_f32_16x16x32_bf16 v[52:55], a[56:59], v[24:27], v[52:55]\n"
    "v_exp_f32 v35, v35\n"
    "v_mfma_f32_16x16x32_bf16 v[64:67], a[60:63], v[24:27], v[64:67]\n"
    "v_exp_f32 v36, v36\n"
    "v_mfma_f32_16x16x32_bf16 v[68:71], a[64:67], v[24:27], v[68:71]\n"
    "s_waitcnt lgkmcnt(1)\n"
    "v_mul_f32 v37, 0xbfb8aa3b, v37\n"
    "v_mul_f32 v38, 0xbfb8aa3b, v38\n"
    "v_mfma_f32_16x16x32_bf16 v[76:79], a[68:71], v[24:27], v[76:79]\n"
    "ds_read_b128 v[24:27], v9 offset:5120\n"
    "v_mul_f32 v39, 0xbfb8aa3b, v39\n"
    "v_mfma_f32_16x16x32_bf16 v[40:43], a[72:75], v[16:19], v[40:43]\n"
    "s_add_u32 s76, s76, 16384\n"
    "v_exp_f32 v37, v37\n"
    "v_mfma_f32_16x16x32_bf16 v[44:47], a[76:79], v[16:19], v[44:47]\n"
    "s_add_u32 m0, s59, 4096\n"
    "v_exp_f32 v38, v38\n"
    "v_mfma_f32_16x16x32_bf16 v[52:55], a[80:83], v[16:19], v[52:55]\n"
    "buffer_load_dwordx4 v1, s[36:39], s76 offen sc1 lds\n"
    "v_exp_f32 v39, v39\n"
    "v_mfma_f32_16x16x32_bf16 v[64:67], a[84:87], v[16:19], v[64:67]\n"
    "v_add_f32 v32, 1.0, v32\n"
    "v_add_f32 v33, 1.0, v33\n"
    "v_mfma_f32_16x16x32_bf16 v[68:71], a[88:91], v[16:19], v[68:71]\n"
    "s_waitcnt lgkmcnt(1)\n"
    "v_rcp_f32 v32, v32\n"
    "v_mfma_f32_16x16x32_bf16 v[76:79], a[92:95], v[16:19], v[76:79]\n"
    "ds_read_b128 v[16:19], v9 offset:6144\n"
    "v_add_f32 v34, 1.0, v34\n"
    "v_mfma_f32_16x16x32_bf16 v[40:43], a[96:99], v[20:23], v[40:43]\n"
    "s_add_u32 s76, s76, 16384\n"
    "v_rcp_f32 v33, v33\n"
    "v_mfma_f32_16x16x32_bf16 v[44:47], a[100:103], v[20:23], v[44:47]\n"
    "s_add_u32 m0, s59, 8192\n"
    "v_add_f32 v35, 1.0, v35\n"
    "v_mfma_f32_16x16x32_bf16 v[52:55], a[104:107], v[20:23], v[52:55]\n"
    "buffer_load_dwordx4 v1, s[36:39], s76 offen sc1 lds\n"
    "v_add_f32 v36, 1.0, v36\n"
    "v_mfma_f32_16x16x32_bf16 v[64:67], a[108:111], v[20:23], v[64:67]\n"
    "v_rcp_f32 v34, v34\n"
    "v_mfma_f32_16x16x32_bf16 v[68:71], a[112:115], v[20:23], v[68:71]\n"
    "s_waitcnt lgkmcnt(1)\n"
    "v_rcp_f32 v35, v35\n"
    "v_mfma_f32_16x16x32_bf16 v[76:79], a[116:119], v[20:23], v[76:79]\n"
    "ds_read_b128 v[20:23], v9 offset:7168\n"
    "s_add_u32 s76, s76, 16384\n"
    "v_mfma_f32_16x16x32_bf16 v[40:43], a[120:123], v[24:27], v[40:43]\n"
    "s_add_u32 m0, s59, 12288\n"
    "v_mfma_f32_16x16x32_bf16 v[44:47], a[124:127], v[24:27], v[44:47]\n"
    "buffer_load_dwordx4 v1, s[36:39], s76 offen sc1 lds\n"
    "v_rcp_f32 v36, v36\n"
    "v_mfma_f32_16x16x32_bf16 v[52:55], a[128:131], v[24:27], v[52:55]\n"
    "v_add_f32 v37, 1.0, v37\n"
    "v_add_f32 v38, 1.0, v38\n"
    "v_mfma_f32_16x16x32_bf16 v[64:67], a[132:135], v[24:27], v[64:67]\n"
    "v_rcp_f32 v37, v37\n"
    "v_mfma_f32_16x16x32_bf16 v[68:71], a[136:139], v[24:27], v[68:71]\n"
    "s_waitcnt lgkmcnt(1)\n"
    "v_rcp_f32 v38, v38\n"
    "v_mfma_f32_16x16x32_bf16 v[76:79], a[140:143], v[24:27], v[76:79]\n"
    "ds_read_b128 v[24:27], v9 offset:8192\n"
    "v_add_f32 v39, 1.0, v39\n"
    "v_mfma_f32_16x16x32_bf16 v[40:43], a[144:147], v[16:19], v[40:43]\n"
    "s_add_u32 s76, s76, 16384\n"
    "v_rcp_f32 v39, v39\n"
    "v_mfma_f32_16x16x32_bf16 v[44:47], a[148:151], v[16:19], v[44:47]\n"
    "s_add_u32 m0, s59, 16384\n"
    "v_mul_f32 v32, v32, v56\n"
    "v_mul_f32 v33, v33, v57\n"
    "v_mfma_f32_16x16x32_bf16 v[52:55], a[152:155], v[16:19], v[52:55]\n"
    "buffer_load_dwordx4 v8, s[36:39], s76 offen sc1 lds\n"
    "v_mul_f32 v34, v34, v58\n"
    "v_mul_f32 v35, v35, v59\n"
    "v_mfma_f32_16x16x32_bf16 v[64:67], a[156:159], v[16:19], v[64:67]\n"
    "v_mul_f32 v36, v36, v60\n"
    "v_mul_f32 v37, v37, v61\n"
    "v_mfma_f32_16x16x32_bf16 v[68:71], a[160:163], v[16:19], v[68:71]\n"
    "s_waitcnt lgkmcnt(1)\n"
    "v_mul_f32 v38, v38, v62\n"
    "v_mul_f32 v39, v39, v63\n"
    "v_mfma_f32_16x16x32_bf16 v[76:79], a[164:167], v[16:19], v[76:79]\n"
    "ds_read_b128 v[16:19], v9 offset:9216\n"
    "v_cvt_pk_bf16_f32 v32, v32, v33\n"
    "v_mfma_f32_16x16x32_bf16 v[40:43], a[168:171], v[20:23], v[40:43]\n"
    "v_cvt_pk_bf16_f32 v33, v34, v35\n"
    "v_cvt_pk_bf16_f32 v34, v36, v37\n"
    "v_mfma_f32_16x16x32_bf16 v[44:47], a[172:175], v[20:23], v[44:47]\n"
    "v_cvt_pk_bf16_f32 v35, v38, v39\n"
    "v_mfma_f32_16x16x32_bf16 v[52:55], a[176:179], v[20:23], v[52:55]\n"
    "v_mul_f32 v72, v48, v72\n"
    "v_mfma_f32_16x16x32_bf16 v[64:67], a[180:183], v[20:23], v[64:67]\n"
    "buffer_store_dwordx4 v[32:35], v15, s[36:39], s73 offen sc1\n"
    "v_mfma_f32_16x16x32_bf16 v[68:71], a[184:187], v[20:23], v[68:71]\n"
    "s_waitcnt lgkmcnt(1)\n"
    "v_mul_f32 v73, v49, v73\n"
    "v_mul_f32 v74, v50, v74\n"
    "v_mfma_f32_16x16x32_bf16 v[76:79], a[188:191], v[20:23], v[76:79]\n"
    "ds_read_b128 v[20:23], v9 offset:10240\n"
    "v_mfma_f32_16x16x32_bf16 v[40:43], a[192:195], v[24:27], v[40:43]\n"
    "v_mfma_f32_16x16x32_bf16 v[44:47], a[196:199], v[24:27], v[44:47]\n"
    "v_mul_f32 v75, v51, v75\n"
    "v_mul_f32 v48, 0xbfb8aa3b, v48\n"
    "v_mfma_f32_16x16x32_bf16 v[52:55], a[200:203], v[24:27], v[52:55]\n"
    "v_exp_f32 v48, v48\n"
    "v_mfma_f32_16x16x32_bf16 v[64:67], a[204:207], v[24:27], v[64:67]\n"
    "v_mul_f32 v49, 0xbfb8aa3b, v49\n"
    "v_mul_f32 v50, 0xbfb8aa3b, v50\n"
    "v_mfma_f32_16x16x32_bf16 v[68:71], a[208:211], v[24:27], v[68:71]\n"
    "s_waitcnt lgkmcnt(1)\n"
    "v_exp_f32 v49, v49\n"
    "v_mfma_f32_16x16x32_bf16 v[76:79], a[212:215], v[24:27], v[76:79]\n"
    "ds_read_b128 v[24:27], v9 offset:11264\n"
    "v_mul_f32 v51, 0xbfb8aa3b, v51\n"
    "v_mfma_f32_16x16x32_bf16 v[40:43], a[216:219], v[16:19], v[40:43]\n"
    "v_exp_f32 v50, v50\n"
    "v_mfma_f32_16x16x32_bf16 v[44:47], a[220:223], v[16:19], v[44:47]\n"
    "v_exp_f32 v51, v51\n"
    "v_mfma_f32_16x16x32_bf16 v[52:55], a[224:227], v[16:19], v[52:55]\n"
    "v_add_f32 v48, 1.0, v48\n"
    "v_add_f32 v49, 1.0, v49\n"
    "v_mfma_f32_16x16x32_bf16 v[64:67], a[228:231], v[16:19], v[64:67]\n"
    "v_rcp_f32 v48, v48\n"
    "v_mfma_f32_16x16x32_bf16 v[68:71], a[232:235], v[16:19], v[68:71]\n"
    "s_waitcnt lgkmcnt(1)\n"
    "v_rcp_f32 v49, v49\n"
    "v_mfma_f32_16x16x32_bf16 v[76:79], a[236:239], v[16:19], v[76:79]\n"
    "ds_read_b128 v[16:19], v9 offset:12288\n"
    "v_add_f32 v50, 1.0, v50\n"
    "v_mfma_f32_16x16x32_bf16 v[40:43], a[240:243], v[20:23], v[40:43]\n"
    "v_rcp_f32 v50, v50\n"
    "v_mfma_f32_16x16x32_bf16 v[44:47], a[244:247], v[20:23], v[44:47]\n"
    "v_add_f32 v51, 1.0, v51\n"
    "v_mfma_f32_16x16x32_bf16 v[52:55], a[248:251], v[20:23], v[52:55]\n"
    "v_mul_f32 v48, v48, v72\n"
    "v_mfma_f32_16x16x32_bf16 v[64:67], a[252:255], v[20:23], v[64:67]\n"
    "v_rcp_f32 v51, v51\n"
    "v_mfma_f32_16x16x32_bf16 v[68:71], v[80:83], v[20:23], v[68:71]\n"
    "s_waitcnt lgkmcnt(1)\n"
    "v_mul_f32 v49, v49, v73\n"
    "v_mul_f32 v50, v50, v74\n"
    "v_mfma_f32_16x16x32_bf16 v[76:79], v[84:87], v[20:23], v[76:79]\n"
    "ds_read_b128 v[20:23], v9 offset:13312\n"
    "v_mfma_f32_16x16x32_bf16 v[40:43], v[88:91], v[24:27], v[40:43]\n"
    "v_mfma_f32_16x16x32_bf16 v[44:47], v[92:95], v[24:27], v[44:47]\n"
    "v_mul_f32 v51, v51, v75\n"
    "v_cvt_pk_bf16_f32 v48, v48, v49\n"
    "v_mfma_f32_16x16x32_bf16 v[52:55], v[96:99], v[24:27], v[52:55]\n"
    "v_cvt_pk_bf16_f32 v49, v50, v51\n"
    "s_nop 0\n"
    "v_mfma_f32_16x16x32_bf16 v[64:67], v[100:103], v[24:27], v[64:67]\n"
    "v_permlane16_swap_b32 v48, v50\n"
    "v_mfma_f32_16x16x32_bf16 v[68:71], v[104:107], v[24:27], v[68:71]\n"
    "s_waitcnt lgkmcnt(1)\n"
    "v_permlane16_swap_b32 v49, v51\n"
    "v_mfma_f32_16x16x32_bf16 v[76:79], v[108:111], v[24:27], v[76:79]\n"
    "ds_read_b128 v[24:27], v9 offset:14336\n"
    ".LgUP_join1_%=:\n"
    "v_mfma_f32_16x16x32_bf16 v[40:43], v[112:115], v[16:19], v[40:43]\n"
    "buffer_store_dwordx4 v[48:51], v4, s[36:39], s73 offen sc1\n"
    "v_mfma_f32_16x16x32_bf16 v[44:47], v[116:119], v[16:19], v[44:47]\n"
    "s_lshr_b32 s73, s68, 1\n"
    "v_mfma_f32_16x16x32_bf16 v[52:55], v[120:123], v[16:19], v[52:55]\n"
    "s_and_b32 s73, s73, s57\n"
    "v_mfma_f32_16x16x32_bf16 v[64:67], v[124:127], v[16:19], v[64:67]\n"
    "s_mul_i32 s73, s73, s56\n"
    "v_mfma_f32_16x16x32_bf16 v[68:71], v[128:131], v[16:19], v[68:71]\n"
    "s_waitcnt lgkmcnt(1)\n"
    "s_add_u32 s73, s73, s55\n"
    "v_mfma_f32_16x16x32_bf16 v[76:79], v[132:135], v[16:19], v[76:79]\n"
    "ds_read_b128 v[16:19], v9 offset:15360\n"
    "v_mfma_f32_16x16x32_bf16 v[40:43], v[136:139], v[20:23], v[40:43]\n"
    "s_add_u32 s71, s68, 2\n"
    "s_waitcnt vmcnt(6)\n"
    "v_mfma_f32_16x16x32_bf16 v[44:47], v[140:143], v[20:23], v[44:47]\n"
    "v_add_u32 v13, v11, v3\n"
    "v_mfma_f32_16x16x32_bf16 v[52:55], v[144:147], v[20:23], v[52:55]\n"
    "v_cmp_gt_i32 vcc, s71, v13\n"
    "v_mfma_f32_16x16x32_bf16 v[64:67], v[148:151], v[20:23], v[64:67]\n"
    "v_mfma_f32_16x16x32_bf16 v[68:71], v[152:155], v[20:23], v[68:71]\n"
    "s_waitcnt lgkmcnt(1)\n"
    "v_mfma_f32_16x16x32_bf16 v[76:79], v[156:159], v[20:23], v[76:79]\n"
    "ds_read_b128 v[20:23], v9 offset:16384\n"
    "v_mfma_f32_16x16x32_bf16 v[40:43], v[160:163], v[24:27], v[40:43]\n"
    "s_or_b64 exec, vcc, s[62:63]\n"
    "buffer_atomic_smax v11, v2, s[36:39], 0 offen sc0 sc1\n"
    "s_mov_b64 exec, -1\n"
    "v_mfma_f32_16x16x32_bf16 v[44:47], v[164:167], v[24:27], v[44:47]\n"
    "s_lshr_b32 s76, s71, 1\n"
    "v_mfma_f32_16x16x32_bf16 v[52:55], v[168:171], v[24:27], v[52:55]\n"
    "s_and_b32 s76, s76, s54\n"
    "v_mfma_f32_16x16x32_bf16 v[64:67], v[172:175], v[24:27], v[64:67]\n"
    "s_mul_i32 s76, s76, s53\n"
    "v_mfma_f32_16x16x32_bf16 v[68:71], v[176:179], v[24:27], v[68:71]\n"
    "s_waitcnt lgkmcnt(1)\n"
    "s_add_u32 s76, s76, s52\n"
    "v_mfma_f32_16x16x32_bf16 v[76:79], v[180:183], v[24:27], v[76:79]\n"
    "ds_read_b128 v[24:27], v9 offset:17408\n"
    "s_add_u32 s76, s76, 1024\n"
    "v_mfma_f32_16x16x32_bf16 v[40:43], v[184:187], v[16:19], v[40:43]\n"
    "v_mfma_f32_16x16x32_bf16 v[44:47], v[188:191], v[16:19], v[44:47]\n"
    "v_mfma_f32_16x16x32_bf16 v[52:55], v[192:195], v[16:19], v[52:55]\n"
    "v_mfma_f32_16x16x32_bf16 v[64:67], v[196:199], v[16:19], v[64:67]\n"
    "s_waitcnt vmcnt(2) lgkmcnt(0)\n"
    "s_barrier\n"
    "v_mfma_f32_16x16x32_bf16 v[68:71], v[200:203], v[16:19], v[68:71]\n"
    "v_mfma_f32_16x16x32_bf16 v[76:79], v[204:207], v[16:19], v[76:79]\n"
    "ds_read_b128 v[16:19], v0 offset:0\n"
    "v_mfma_f32_16x16x32_bf16 v[40:43], v[208:211], v[20:23], v[40:43]\n"
    "v_mfma_f32_16x16x32_bf16 v[44:47], v[212:215], v[20:23], v[44:47]\n"
    "v_add_u32 v13, v5, v3\n"
    "v_mfma_f32_16x16x32_bf16 v[52:55], v[216:219], v[20:23], v[52:55]\n"
    "v_cmp_gt_i32 vcc, s71, v13\n"
    "v_mfma_f32_16x16x32_bf16 v[64:67], v[220:223], v[20:23], v[64:67]\n"
    "v_mfma_f32_16x16x32_bf16 v[68:71], v[224:227], v[20:23], v[68:71]\n"
    "v_mfma_f32_16x16x32_bf16 v[76:79], v[228:231], v[20:23], v[76:79]\n"
    "ds_read_b128 v[20:23], v0 offset:1024\n"
    "v_mfma_f32_16x16x32_bf16 v[40:43], v[232:235], v[24:27], v[40:43]\n"
    "s_or_b64 exec, vcc, s[62:63]\n"
    "buffer_atomic_smax v5, v2, s[36:39], 0 offen sc0 sc1\n"
    "s_mov_b64 exec, -1\n"
    "v_mfma_f32_16x16x32_bf16 v[44:47], v[236:239], v[24:27], v[44:47]\n"
    "v_mfma_f32_16x16x32_bf16 v[52:55], v[240:243], v[24:27], v[52:55]\n"
    "v_mov_b32 v7, v12\n"
    "v_mfma_f32_16x16x32_bf16 v[64:67], v[244:247], v[24:27], v[64:67]\n"
    "s_mov_b32 s69, s70\n"
    "s_mov_b32 s70, 0\n"
    "v_mov_b32 v12, s68\n"
    "v_mfma_f32_16x16x32_bf16 v[68:71], v[248:251], v[24:27], v[68:71]\n"
    "s_waitcnt lgkmcnt(1)\n"
    "s_add_u32 s68, s68, 1\n"
    "v_mfma_f32_16x16x32_bf16 v[76:79], v[252:255], v[24:27], v[76:79]\n"
    "ds_read_b128 v[24:27], v0 offset:2048\n"
    "s_branch .LgUP_head0_%=\n"
    ".LgUP_LOOPE_%=:\n"
    ".LgUP_done_%=:\n"
    "s_waitcnt vmcnt(0)\n"
    "buffer_atomic_smax v12, v10, s[36:39], 0 offen sc1\n"
    "s_waitcnt vmcnt(0)\n"
    "s_branch .LgUP_FIN_%=\n"
    ".LgUP_poll0_%=:\n"
    "v_mfma_f32_16x16x32_bf16 v[32:35], a[48:51], v[24:27], v[32:35]\n"
    "s_or_b64 exec, vcc, s[62:63]\n"
    "buffer_atomic_smax v11, v2, s[36:39], 0 offen sc0 sc1\n"
    "s_mov_b64 exec, -1\n"
    "v_mfma_f32_16x16x32_bf16 v[36:39], a[52:55], v[24:27], v[36:39]\n"
    "v_mul_f32 v43, 0xbfb8aa3b, v43\n"
    "v_mul_f32 v44, 0xbfb8aa3b, v44\n"
    "v_mfma_f32_16x16x32_bf16 v[48:51], a[56:59], v[24:27], v[48:51]\n"
    "v_exp_f32 v43, v43\n"
    "v_mfma_f32_16x16x32_bf16 v[56:59], a[60:63], v[24:27], v[56:59]\n"
    "v_exp_f32 v44, v44\n"
    "v_mfma_f32_16x16x32_bf16 v[60:63], a[64:67], v[24:27], v[60:63]\n"
    "s_waitcnt lgkmcnt(1)\n"
    "v_mul_f32 v45, 0xbfb8aa3b, v45\n"
    "v_mul_f32 v46, 0xbfb8aa3b, v46\n"
    "v_mfma_f32_16x16x32_bf16 v[72:75], a[68:71], v[24:27], v[72:75]\n"
    "ds_read_b128 v[24:27], v0 offset:5120\n"
    "v_mul_f32 v47, 0xbfb8aa3b, v47\n"
    "v_mfma_f32_16x16x32_bf16 v[32:35], a[72:75], v[16:19], v[32:35]\n"
    "v_exp_f32 v45, v45\n"
    "v_mfma_f32_16x16x32_bf16 v[36:39], a[76:79], v[16:19], v[36:39]\n"
    "v_exp_f32 v46, v46\n"
    "v_mfma_f32_16x16x32_bf16 v[48:51], a[80:83], v[16:19], v[48:51]\n"
    "v_exp_f32 v47, v47\n"
    "v_mfma_f32_16x16x32_bf16 v[56:59], a[84:87], v[16:19], v[56:59]\n"
    "v_add_f32 v40, 1.0, v40\n"
    "v_add_f32 v41, 1.0, v41\n"
    "v_mfma_f32_16x16x32_bf16 v[60:63], a[88:91], v[16:19], v[60:63]\n"
    "s_waitcnt lgkmcnt(1)\n"
    "v_rcp_f32 v40, v40\n"
    "v_mfma_f32_16x16x32_bf16 v[72:75], a[92:95], v[16:19], v[72:75]\n"
    "ds_read_b128 v[16:19], v0 offset:6144\n"
    "v_add_f32 v42, 1.0, v42\n"
    "v_mfma_f32_16x16x32_bf16 v[32:35], a[96:99], v[20:23], v[32:35]\n"
    "v_rcp_f32 v41, v41\n"
    "v_mfma_f32_16x16x32_bf16 v[36:39], a[100:103], v[20:23], v[36:39]\n"
    "s_waitcnt vmcnt(2)\n"
    "v_add_f32 v43, 1.0, v43\n"
    "v_add_u32 v13, v5, v3\n"
    "v_mfma_f32_16x16x32_bf16 v[48:51], a[104:107], v[20:23], v[48:51]\n"
    "v_add_f32 v44, 1.0, v44\n"
    "v_cmp_gt_i32 vcc, s71, v13\n"
    "v_mfma_f32_16x16x32_bf16 v[56:59], a[108:111], v[20:23], v[56:59]\n"
    "v_rcp_f32 v42, v42\n"
    "v_mfma_f32_16x16x32_bf16 v[60:63], a[112:115], v[20:23], v[60:63]\n"
    "s_waitcnt lgkmcnt(1)\n"
    "v_rcp_f32 v43, v43\n"
    "v_mfma_f32_16x16x32_bf16 v[72:75], a[116:119], v[20:23], v[72:75]\n"
    "ds_read_b128 v[20:23], v0 offset:7168\n"
    "s_cbranch_vccz .LgUP_dma0_30_%=\n"
    "v_mfma_f32_16x16x32_bf16 v[32:35], a[120:123], v[24:27], v[32:35]\n"
    "s_or_b64 exec, vcc, s[62:63]\n"
    "buffer_atomic_smax v5, v2, s[36:39], 0 offen sc0 sc1\n"
    "s_mov_b64 exec, -1\n"
    "v_mfma_f32_16x16x32_bf16 v[36:39], a[124:127], v[24:27], v[36:39]\n"
    "v_rcp_f32 v44, v44\n"
    "v_mfma_f32_16x16x32_bf16 v[48:51], a[128:131], v[24:27], v[48:51]\n"
    "v_add_f32 v45, 1.0, v45\n"
    "v_add_f32 v46, 1.0, v46\n"
    "v_mfma_f32_16x16x32_bf16 v[56:59], a[132:135], v[24:27], v[56:59]\n"
    "v_rcp_f32 v45, v45\n"
    "v_mfma_f32_16x16x32_bf16 v[60:63], a[136:139], v[24:27], v[60:63]\n"
    "s_waitcnt lgkmcnt(1)\n"
    "v_rcp_f32 v46, v46\n"
    "v_mfma_f32_16x16x32_bf16 v[72:75], a[140:143], v[24:27], v[72:75]\n"
    "ds_read_b128 v[24:27], v0 offset:8192\n"
    "v_add_f32 v47, 1.0, v47\n"
    "v_mfma_f32_16x16x32_bf16 v[32:35], a[144:147], v[16:19], v[32:35]\n"
    "v_rcp_f32 v47, v47\n"
    "v_mfma_f32_16x16x32_bf16 v[36:39], a[148:151], v[16:19], v[36:39]\n"
    "v_mul_f32 v40, v40, v64\n"
    "v_mul_f32 v41, v41, v65\n"
    "v_mfma_f32_16x16x32_bf16 v[48:51], a[152:155], v[16:19], v[48:51]\n"
    "v_mul_f32 v42, v42, v66\n"
    "v_mul_f32 v43, v43, v67\n"
    "v_mfma_f32_16x16x32_bf16 v[56:59], a[156:159], v[16:19], v[56:59]\n"
    "v_mul_f32 v44, v44, v68\n"
    "v_mul_f32 v45, v45, v69\n"
    "v_mfma_f32_16x16x32_bf16 v[60:63], a[160:163], v[16:19], v[60:63]\n"
    "s_waitcnt lgkmcnt(1)\n"
    "v_mul_f32 v46, v46, v70\n"
    "v_mul_f32 v47, v47, v71\n"
    "v_mfma_f32_16x16x32_bf16 v[72:75], a[164:167], v[16:19], v[72:75]\n"
    "ds_read_b128 v[16:19], v0 offset:9216\n"
    "v_cvt_pk_bf16_f32 v40, v40, v41\n"
    "v_mfma_f32_16x16x32_bf16 v[32:35], a[168:171], v[20:23], v[32:35]\n"
    "v_cvt_pk_bf16_f32 v41, v42, v43\n"
    "v_cvt_pk_bf16_f32 v42, v44, v45\n"
    "v_mfma_f32_16x16x32_bf16 v[36:39], a[172:175], v[20:23], v[36:39]\n"
    "s_waitcnt vmcnt(1)\n"
    "v_cvt_pk_bf16_f32 v43, v46, v47\n"
    "v_add_u32 v13, v11, v3\n"
    "v_mfma_f32_16x16x32_bf16 v[48:51], a[176:179], v[20:23], v[48:51]\n"
    "v_mul_f32 v76, v52, v76\n"
    "v_cmp_gt_i32 vcc, s71, v13\n"
    "v_mfma_f32_16x16x32_bf16 v[56:59], a[180:183], v[20:23], v[56:59]\n"
    "buffer_store_dwordx4 v[40:43], v15, s[36:39], s73 offen offset:1024 sc1\n"
    "v_mfma_f32_16x16x32_bf16 v[60:63], a[184:187], v[20:23], v[60:63]\n"
    "s_waitcnt lgkmcnt(1)\n"
    "v_mul_f32 v77, v53, v77\n"
    "v_mul_f32 v78, v54, v78\n"
    "v_mfma_f32_16x16x32_bf16 v[72:75], a[188:191], v[20:23], v[72:75]\n"
    "ds_read_b128 v[20:23], v0 offset:10240\n"
    "s_cbranch_vccz .LgUP_dma0_48_%=\n"
    "v_mfma_f32_16x16x32_bf16 v[32:35], a[192:195], v[24:27], v[32:35]\n"
    "s_or_b64 exec, vcc, s[62:63]\n"
    "buffer_atomic_smax v11, v2, s[36:39], 0 offen sc0 sc1\n"
    "s_mov_b64 exec, -1\n"
    "v_mfma_f32_16x16x32_bf16 v[36:39], a[196:199], v[24:27], v[36:39]\n"
    "v_mul_f32 v79, v55, v79\n"
    "v_mul_f32 v52, 0xbfb8aa3b, v52\n"
    "v_mfma_f32_16x16x32_bf16 v[48:51], a[200:203], v[24:27], v[48:51]\n"
    "v_exp_f32 v52, v52\n"
    "v_mfma_f32_16x16x32_bf16 v[56:59], a[204:207], v[24:27], v[56:59]\n"
    "v_mul_f32 v53, 0xbfb8aa3b, v53\n"
    "v_mul_f32 v54, 0xbfb8aa3b, v54\n"
    "v_mfma_f32_16x16x32_bf16 v[60:63], a[208:211], v[24:27], v[60:63]\n"
    "s_waitcnt lgkmcnt(1)\n"
    "v_exp_f32 v53, v53\n"
    "v_mfma_f32_16x16x32_bf16 v[72:75], a[212:215], v[24:27], v[72:75]\n"
    "ds_read_b128 v[24:27], v0 offset:11264\n"
    "v_mul_f32 v55, 0xbfb8aa3b, v55\n"
    "v_mfma_f32_16x16x32_bf16 v[32:35], a[216:219], v[16:19], v[32:35]\n"
    "v_exp_f32 v54, v54\n"
    "v_mfma_f32_16x16x32_bf16 v[36:39], a[220:223], v[16:19], v[36:39]\n"
    "v_exp_f32 v55, v55\n"
    "v_mfma_f32_16x16x32_bf16 v[48:51], a[224:227], v[16:19], v[48:51]\n"
    "v_add_f32 v52, 1.0, v52\n"
    "v_add_f32 v53, 1.0, v53\n"
    "v_mfma_f32_16x16x32_bf16 v[56:59], a[228:231], v[16:19], v[56:59]\n"
    "v_rcp_f32 v52, v52\n"
    "v_mfma_f32_16x16x32_bf16 v[60:63], a[232:235], v[16:19], v[60:63]\n"
    "s_waitcnt lgkmcnt(1)\n"
    "v_rcp_f32 v53, v53\n"
    "v_mfma_f32_16x16x32_bf16 v[72:75], a[236:239], v[16:19], v[72:75]\n"
    "ds_read_b128 v[16:19], v0 offset:12288\n"
    "v_add_f32 v54, 1.0, v54\n"
    "v_mfma_f32_16x16x32_bf16 v[32:35], a[240:243], v[20:23], v[32:35]\n"
    "v_rcp_f32 v54, v54\n"
    "v_mfma_f32_16x16x32_bf16 v[36:39], a[244:247], v[20:23], v[36:39]\n"
    "s_waitcnt vmcnt(2)\n"
    "v_add_f32 v55, 1.0, v55\n"
    "v_add_u32 v13, v5, v3\n"
    "v_mfma_f32_16x16x32_bf16 v[48:51], a[248:251], v[20:23], v[48:51]\n"
    "v_mul_f32 v52, v52, v76\n"
    "v_cmp_gt_i32 vcc, s71, v13\n"
    "v_mfma_f32_16x16x32_bf16 v[56:59], a[252:255], v[20:23], v[56:59]\n"
    "v_rcp_f32 v55, v55\n"
    "v_mfma_f32_16x16x32_bf16 v[60:63], v[80:83], v[20:23], v[60:63]\n"
    "s_waitcnt lgkmcnt(1)\n"
    "v_mul_f32 v53, v53, v77\n"
    "v_mul_f32 v54, v54, v78\n"
    "v_mfma_f32_16x16x32_bf16 v[72:75], v[84:87], v[20:23], v[72:75]\n"
    "ds_read_b128 v[20:23], v0 offset:13312\n"
    "s_cbranch_vccz .LgUP_dma0_66_%=\n"
    "v_mfma_f32_16x16x32_bf16 v[32:35], v[88:91], v[24:27], v[32:35]\n"
    "s_or_b64 exec, vcc, s[62:63]\n"
    "buffer_atomic_smax v5, v2, s[36:39], 0 offen sc0 sc1\n"
    "s_mov_b64 exec, -1\n"
    "v_mfma_f32_16x16x32_bf16 v[36:39], v[92:95], v[24:27], v[36:39]\n"
    "v_mul_f32 v55, v55, v79\n"
    "v_cvt_pk_bf16_f32 v52, v52, v53\n"
    "v_mfma_f32_16x16x32_bf16 v[48:51], v[96:99], v[24:27], v[48:51]\n"
    "v_cvt_pk_bf16_f32 v53, v54, v55\n"
    "s_nop 0\n"
    "v_mfma_f32_16x16x32_bf16 v[56:59], v[100:103], v[24:27], v[56:59]\n"
    "v_permlane16_swap_b32 v52, v54\n"
    "v_mfma_f32_16x16x32_bf16 v[60:63], v[104:107], v[24:27], v[60:63]\n"
    "s_waitcnt lgkmcnt(1)\n"
    "v_permlane16_swap_b32 v53, v55\n"
    "v_mfma_f32_16x16x32_bf16 v[72:75], v[108:111], v[24:27], v[72:75]\n"
    "ds_read_b128 v[24:27], v0 offset:14336\n"
    "v_mfma_f32_16x16x32_bf16 v[32:35], v[112:115], v[16:19], v[32:35]\n"
    "buffer_store_dwordx4 v[52:55], v4, s[36:39], s73 offen offset:1024 sc1\n"
    "v_mfma_f32_16x16x32_bf16 v[36:39], v[116:119], v[16:19], v[36:39]\n"
    "s_lshr_b32 s73, s68, 1\n"
    "v_mfma_f32_16x16x32_bf16 v[48:51], v[120:123], v[16:19], v[48:51]\n"
    "s_and_b32 s73, s73, s57\n"
    "v_mfma_f32_16x16x32_bf16 v[56:59], v[124:127], v[16:19], v[56:59]\n"
    "s_mul_i32 s73, s73, s56\n"
    "v_mfma_f32_16x16x32_bf16 v[60:63], v[128:131], v[16:19], v[60:63]\n"
    "s_waitcnt lgkmcnt(1)\n"
    "s_add_u32 s73, s73, s55\n"
    "v_mfma_f32_16x16x32_bf16 v[72:75], v[132:135], v[16:19], v[72:75]\n"
    "ds_read_b128 v[16:19], v0 offset:15360\n"
    "v_mfma_f32_16x16x32_bf16 v[32:35], v[136:139], v[20:23], v[32:35]\n"
    "v_mfma_f32_16x16x32_bf16 v[36:39], v[140:143], v[20:23], v[36:39]\n"
    "s_waitcnt vmcnt(2)\n"
    "v_add_u32 v13, v11, v3\n"
    "v_mfma_f32_16x16x32_bf16 v[48:51], v[144:147], v[20:23], v[48:51]\n"
    "v_cmp_gt_i32 vcc, s71, v13\n"
    "v_mfma_f32_16x16x32_bf16 v[56:59], v[148:151], v[20:23], v[56:59]\n"
    "v_mfma_f32_16x16x32_bf16 v[60:63], v[152:155], v[20:23], v[60:63]\n"
    "s_waitcnt lgkmcnt(1)\n"
    "v_mfma_f32_16x16x32_bf16 v[72:75], v[156:159], v[20:23], v[72:75]\n"
    "ds_read_b128 v[20:23], v0 offset:16384\n"
    "s_cbranch_vccz .LgUP_dma0_84_%=\n"
    "v_mfma_f32_16x16x32_bf16 v[32:35], v[160:163], v[24:27], v[32:35]\n"
    "s_or_b64 exec, vcc, s[62:63]\n"
    "buffer_atomic_smax v11, v2, s[36:39], 0 offen sc0 sc1\n"
    "s_mov_b64 exec, -1\n"
    "v_mfma_f32_16x16x32_bf16 v[36:39], v[164:167], v[24:27], v[36:39]\n"
    "v_mfma_f32_16x16x32_bf16 v[48:51], v[168:171], v[24:27], v[48:51]\n"
    "v_mfma_f32_16x16x32_bf16 v[56:59], v[172:175], v[24:27], v[56:59]\n"
    "v_mfma_f32_16x16x32_bf16 v[60:63], v[176:179], v[24:27], v[60:63]\n"
    "s_waitcnt lgkmcnt(1)\n"
    "v_mfma_f32_16x16x32_bf16 v[72:75], v[180:183], v[24:27], v[72:75]\n"
    "ds_read_b128 v[24:27], v0 offset:17408\n"
    "v_mfma_f32_16x16x32_bf16 v[32:35], v[184:187], v[16:19], v[32:35]\n"
    "v_mfma_f32_16x16x32_bf16 v[36:39], v[188:191], v[16:19], v[36:39]\n"
    "v_mfma_f32_16x16x32_bf16 v[48:51], v[192:195], v[16:19], v[48:51]\n"
    "v_mfma_f32_16x16x32_bf16 v[56:59], v[196:199], v[16:19], v[56:59]\n"
    "v_mfma_f32_16x16x32_bf16 v[60:63], v[200:203], v[16:19], v[60:63]\n"
    "s_waitcnt lgkmcnt(1)\n"
    "v_mfma_f32_16x16x32_bf16 v[72:75], v[204:207], v[16:19], v[72:75]\n"
    "v_mfma_f32_16x16x32_bf16 v[32:35], v[208:211], v[20:23], v[32:35]\n"
    "v_mfma_f32_16x16x32_bf16 v[36:39], v[212:215], v[20:23], v[36:39]\n"
    "s_waitcnt vmcnt(2)\n"
    "v_add_u32 v13, v5, v3\n"
    "v_mfma_f32_16x16x32_bf16 v[48:51], v[216:219], v[20:23], v[48:51]\n"
    "v_cmp_gt_i32 vcc, s71, v13\n"
    "v_mfma_f32_16x16x32_bf16 v[56:59], v[220:223], v[20:23], v[56:59]\n"
    "v_mfma_f32_16x16x32_bf16 v[60:63], v[224:227], v[20:23], v[60:63]\n"
    "s_waitcnt lgkmcnt(0)\n"
    "v_mfma_f32_16x16x32_bf16 v[72:75], v[228:231], v[20:23], v[72:75]\n"
    "s_cbranch_vccz .LgUP_dma0_102_%=\n"
    "v_mfma_f32_16x16x32_bf16 v[32:35], v[232:235], v[24:27], v[32:35]\n"
    "s_or_b64 exec, vcc, s[62:63]\n"
    "buffer_atomic_smax v5, v2, s[36:39], 0 offen sc0 sc1\n"
    "s_mov_b64 exec, -1\n"
    "v_mfma_f32_16x16x32_bf16 v[36:39], v[236:239], v[24:27], v[36:39]\n"
    "v_mfma_f32_16x16x32_bf16 v[48:51], v[240:243], v[24:27], v[48:51]\n"
    "s_waitcnt vmcnt(2)\n"
    "buffer_atomic_smax v12, v10, s[36:39], s70 offen sc1\n"
    "v_mfma_f32_16x16x32_bf16 v[56:59], v[244:247], v[24:27], v[56:59]\n"
    "v_mfma_f32_16x16x32_bf16 v[60:63], v[248:251], v[24:27], v[60:63]\n"
    "v_mfma_f32_16x16x32_bf16 v[72:75], v[252:255], v[24:27], v[72:75]\n"
    "v_pk_mul_f32 v[56:57], v[32:33], v[56:57]\n"
    "v_pk_mul_f32 v[58:59], v[34:35], v[58:59]\n"
    "s_nop 1\n"
    "v_pk_mul_f32 v[60:61], v[36:37], v[60:61]\n"
    "v_pk_mul_f32 v[62:63], v[38:39], v[62:63]\n"
    "v_pk_mul_f32 v[32:33], v[32:33], s[64:65] op_sel_hi:[1,0]\n"
    "v_pk_mul_f32 v[34:35], v[34:35], s[64:65] op_sel_hi:[1,0]\n"
    "v_pk_mul_f32 v[36:37], v[36:37], s[64:65] op_sel_hi:[1,0]\n"
    "v_pk_mul_f32 v[38:39], v[38:39], s[64:65] op_sel_hi:[1,0]\n"
    "v_exp_f32 v32, v32\n"
    "v_exp_f32 v33, v33\n"
    "v_exp_f32 v34, v34\n"
    "v_exp_f32 v35, v35\n"
    "v_exp_f32 v36, v36\n"
    "v_exp_f32 v37, v37\n"
    "v_exp_f32 v38, v38\n"
    "v_exp_f32 v39, v39\n"
    "v_pk_add_f32 v[32:33], v[32:33], 1.0 op_sel_hi:[1,0]\n"
    "v_pk_add_f32 v[34:35], v[34:35], 1.0 op_sel_hi:[1,0]\n"
    "v_pk_add_f32 v[36:37], v[36:37], 1.0 op_sel_hi:[1,0]\n"
    "v_pk_add_f32 v[38:39], v[38:39], 1.0 op_sel_hi:[1,0]\n"
    "v_rcp_f32 v32, v32\n"
    "v_rcp_f32 v33, v33\n"
    "v_rcp_f32 v34, v34\n"
    "v_rcp_f32 v35, v35\n"
    "v_rcp_f32 v36, v36\n"
    "v_rcp_f32 v37, v37\n"
    "v_rcp_f32 v38, v38\n"
    "v_rcp_f32 v39, v39\n"
    "v_pk_mul_f32 v[32:33], v[32:33], v[56:57]\n"
    "v_pk_mul_f32 v[34:35], v[34:35], v[58:59]\n"
    "v_pk_mul_f32 v[36:37], v[36:37], v[60:61]\n"
    "v_pk_mul_f32 v[38:39], v[38:39], v[62:63]\n"
    "v_cvt_pk_bf16_f32 v32, v32, v33\n"
    "v_cvt_pk_bf16_f32 v33, v34, v35\n"
    "v_cvt_pk_bf16_f32 v34, v36, v37\n"
    "v_cvt_pk_bf16_f32 v35, v38, v39\n"
    "buffer_store_dwordx4 v[32:35], v15, s[36:39], s73 offen sc1\n"
    "v_pk_mul_f32 v[72:73], v[48:49], v[72:73]\n"
    "v_pk_mul_f32 v[74:75], v[50:51], v[74:75]\n"
    "v_pk_mul_f32 v[48:49], v[48:49], s[64:65] op_sel_hi:[1,0]\n"
    "v_pk_mul_f32 v[50:51], v[50:51], s[64:65] op_sel_hi:[1,0]\n"
    "v_exp_f32 v48, v48\n"
    "v_exp_f32 v49, v49\n"
    "v_exp_f32 v50, v50\n"
    "v_exp_f32 v51, v51\n"
    "v_pk_add_f32 v[48:49], v[48:49], 1.0 op_sel_hi:[1,0]\n"
    "v_pk_add_f32 v[50:51], v[50:51], 1.0 op_sel_hi:[1,0]\n"
    "v_rcp_f32 v48, v48\n"
    "v_rcp_f32 v49, v49\n"
    "v_rcp_f32 v50, v50\n"
    "v_rcp_f32 v51, v51\n"
    "v_pk_mul_f32 v[48:49], v[48:49], v[72:73]\n"
    "v_pk_mul_f32 v[50:51], v[50:51], v[74:75]\n"
    "v_cvt_pk_bf16_f32 v48, v48, v49\n"
    "v_cvt_pk_bf16_f32 v49, v50, v51\n"
    "s_nop 0\n"
    "v_permlane16_swap_b32 v48, v50\n"
    "v_permlane16_swap_b32 v49, v51\n"
    "buffer_store_dwordx4 v[48:51], v4, s[36:39], s73 offen sc1\n"
    "v_mov_b32 v12, s68\n"
    "s_mov_b32 s73, 0x80000000\n"
    "s_mov_b32 s68, s71\n"
    "s_waitcnt vmcnt(4)\n"
    "v_add_u32 v13, v11, v3\n"
    "v_cmp_gt_i32 vcc, s71, v13\n"
    "s_cbranch_vccnz .LgUP_idle0_%=\n"
    "s_add_u32 m0, s59, 20480\n"
    "s_nop 0\n"
    "buffer_load_dwordx4 v1, s[36:39], s76 offen sc1 lds\n"
    "s_add_u32 m0, s59, 24576\n"
    "s_add_u32 s76, s76, 16384\n"
    "buffer_load_dwordx4 v1, s[36:39], s76 offen sc1 lds\n"
    "s_add_u32 m0, s59, 28672\n"
    "s_add_u32 s76, s76, 16384\n"
    "buffer_load_dwordx4 v1, s[36:39], s76 offen sc1 lds\n"
    "s_add_u32 m0, s59, 32768\n"
    "s_add_u32 s76, s76, 16384\n"
    "buffer_load_dwordx4 v1, s[36:39], s76 offen sc1 lds\n"
    "s_add_u32 m0, s59, 36864\n"
    "s_add_u32 s76, s76, 16384\n"
    "buffer_load_dwordx4 v8, s[36:39], s76 offen sc1 lds\n"
    "s_waitcnt vmcnt(5)\n"
    "buffer_atomic_smax v12, v14, s[36:39], 0 offen sc1\n"
    "s_mov_b32 s69, 0x80000000\n"
    "s_mov_b32 s70, 0x80000000\n"
    "v_mov_b32 v7, v12\n"
    "s_waitcnt vmcnt(9)\n"
    "s_add_u32 s71, s68, 1\n"
    "v_add_u32 v13, v11, v3\n"
    "v_cmp_gt_i32 vcc, s71, v13\n"
    "s_nop 0\n"
    "s_or_b64 exec, vcc, s[62:63]\n"
    "buffer_atomic_smax v11, v2, s[36:39], 0 offen sc0 sc1\n"
    "s_mov_b64 exec, -1\n"
    "s_waitcnt vmcnt(2)\n"
    "s_branch .LgUP_bar1_%=\n"
    ".LgUP_dma0_30_%=:\n"
    "v_mfma_f32_16x16x32_bf16 v[32:35], a[120:123], v[24:27], v[32:35]\n"
    "s_add_u32 m0, s59, 20480\n"
    "v_mfma_f32_16x16x32_bf16 v[36:39], a[124:127], v[24:27], v[36:39]\n"
    "buffer_load_dwordx4 v1, s[36:39], s76 offen sc1 lds\n"
    "v_rcp_f32 v44, v44\n"
    "v_mfma_f32_16x16x32_bf16 v[48:51], a[128:131], v[24:27], v[48:51]\n"
    "s_add_u32 s76, s76, 16384\n"
    "v_add_f32 v45, 1.0, v45\n"
    "v_add_f32 v46, 1.0, v46\n"
    "v_mfma_f32_16x16x32_bf16 v[56:59], a[132:135], v[24:27], v[56:59]\n"
    "s_add_u32 m0, s59, 24576\n"
    "v_rcp_f32 v45, v45\n"
    "v_mfma_f32_16x16x32_bf16 v[60:63], a[136:139], v[24:27], v[60:63]\n"
    "s_waitcnt lgkmcnt(1)\n"
    "v_rcp_f32 v46, v46\n"
    "v_mfma_f32_16x16x32_bf16 v[72:75], a[140:143], v[24:27], v[72:75]\n"
    "ds_read_b128 v[24:27], v0 offset:8192\n"
    "v_add_f32 v47, 1.0, v47\n"
    "v_mfma_f32_16x16x32_bf16 v[32:35], a[144:147], v[16:19], v[32:35]\n"
    "buffer_load_dwordx4 v1, s[36:39], s76 offen sc1 lds\n"
    "v_rcp_f32 v47, v47\n"
    "v_mfma_f32_16x16x32_bf16 v[36:39], a[148:151], v[16:19], v[36:39]\n"
    "s_add_u32 s76, s76, 16384\n"
    "v_mul_f32 v40, v40, v64\n"
    "v_mul_f32 v41, v41, v65\n"
    "v_mfma_f32_16x16x32_bf16 v[48:51], a[152:155], v[16:19], v[48:51]\n"
    "s_add_u32 m0, s59, 28672\n"
    "v_mul_f32 v42, v42, v66\n"
    "v_mul_f32 v43, v43, v67\n"
    "v_mfma_f32_16x16x32_bf16 v[56:59], a[156:159], v[16:19], v[56:59]\n"
    "buffer_load_dwordx4 v1, s[36:39], s76 offen sc1 lds\n"
    "v_mul_f32 v44, v44, v68\n"
    "v_mul_f32 v45, v45, v69\n"
    "v_mfma_f32_16x16x32_bf16 v[60:63], a[160:163], v[16:19], v[60:63]\n"
    "s_waitcnt lgkmcnt(1)\n"
    "v_mul_f32 v46, v46, v70\n"
    "v_mul_f32 v47, v47, v71\n"
    "v_mfma_f32_16x16x32_bf16 v[72:75], a[164:167], v[16:19], v[72:75]\n"
    "ds_read_b128 v[16:19], v0 offset:9216\n"
    "v_cvt_pk_bf16_f32 v40, v40, v41\n"
    "v_mfma_f32_16x16x32_bf16 v[32:35], a[168:171], v[20:23], v[32:35]\n"
    "s_add_u32 s76, s76, 16384\n"
    "v_cvt_pk_bf16_f32 v41, v42, v43\n"
    "v_cvt_pk_bf16_f32 v42, v44, v45\n"
    "v_mfma_f32_16x16x32_bf16 v[36:39], a[172:175], v[20:23], v[36:39]\n"
    "s_add_u32 m0, s59, 32768\n"
    "v_cvt_pk_bf16_f32 v43, v46, v47\n"
    "v_mfma_f32_16x16x32_bf16 v[48:51], a[176:179], v[20:23], v[48:51]\n"
    "buffer_load_dwordx4 v1, s[36:39], s76 offen sc1 lds\n"
    "s_add_u32 s76, s76, 16384\n"
    "v_mul_f32 v76, v52, v76\n"
    "v_mfma_f32_16x16x32_bf16 v[56:59], a[180:183], v[20:23], v[56:59]\n"
    "buffer_store_dwordx4 v[40:43], v15, s[36:39], s73 offen offset:1024 sc1\n"
    "v_mfma_f32_16x16x32_bf16 v[60:63], a[184:187], v[20:23], v[60:63]\n"
    "s_waitcnt lgkmcnt(1)\n"
    "s_add_u32 m0, s59, 36864\n"
    "v_mul_f32 v77, v53, v77\n"
    "v_mul_f32 v78, v54, v78\n"
    "v_mfma_f32_16x16x32_bf16 v[72:75], a[188:191], v[20:23], v[72:75]\n"
    "ds_read_b128 v[20:23], v0 offset:10240\n"
    "buffer_load_dwordx4 v8, s[36:39], s76 offen sc1 lds\n"
    "v_mfma_f32_16x16x32_bf16 v[32:35], a[192:195], v[24:27], v[32:35]\n"
    "v_mfma_f32_16x16x32_bf16 v[36:39], a[196:199], v[24:27], v[36:39]\n"
    "v_mul_f32 v79, v55, v79\n"
    "v_mul_f32 v52, 0xbfb8aa3b, v52\n"
    "v_mfma_f32_16x16x32_bf16 v[48:51], a[200:203], v[24:27], v[48:51]\n"
    "v_exp_f32 v52, v52\n"
    "v_mfma_f32_16x16x32_bf16 v[56:59], a[204:207], v[24:27], v[56:59]\n"
    "v_mul_f32 v53, 0xbfb8aa3b, v53\n"
    "v_mul_f32 v54, 0xbfb8aa3b, v54\n"
    "v_mfma_f32_16x16x32_bf16 v[60:63], a[208:211], v[24:27], v[60:63]\n"
    "s_waitcnt lgkmcnt(1)\n"
    "v_exp_f32 v53, v53\n"
    "v_mfma_f32_16x16x32_bf16 v[72:75], a[212:215], v[24:27], v[72:75]\n"
    "ds_read_b128 v[24:27], v0 offset:11264\n"
    "v_mul_f32 v55, 0xbfb8aa3b, v55\n"
    "v_mfma_f32_16x16x32_bf16 v[32:35], a[216:219], v[16:19], v[32:35]\n"
    "v_exp_f32 v54, v54\n"
    "v_mfma_f32_16x16x32_bf16 v[36:39], a[220:223], v[16:19], v[36:39]\n"
    "v_exp_f32 v55, v55\n"
    "v_mfma_f32_16x16x32_bf16 v[48:51], a[224:227], v[16:19], v[48:51]\n"
    "v_add_f32 v52, 1.0, v52\n"
    "v_add_f32 v53, 1.0, v53\n"
    "v_mfma_f32_16x16x32_bf16 v[56:59], a[228:231], v[16:19], v[56:59]\n"
    "v_rcp_f32 v52, v52\n"
    "v_mfma_f32_16x16x32_bf16 v[60:63], a[232:235], v[16:19], v[60:63]\n"
    "s_waitcnt lgkmcnt(1)\n"
    "v_rcp_f32 v53, v53\n"
    "v_mfma_f32_16x16x32_bf16 v[72:75], a[236:239], v[16:19], v[72:75]\n"
    "ds_read_b128 v[16:19], v0 offset:12288\n"
    "v_add_f32 v54, 1.0, v54\n"
    "v_mfma_f32_16x16x32_bf16 v[32:35], a[240:243], v[20:23], v[32:35]\n"
    "v_rcp_f32 v54, v54\n"
    "v_mfma_f32_16x16x32_bf16 v[36:39], a[244:247], v[20:23], v[36:39]\n"
    "v_add_f32 v55, 1.0, v55\n"
    "v_mfma_f32_16x16x32_bf16 v[48:51], a[248:251], v[20:23], v[48:51]\n"
    "v_mul_f32 v52, v52, v76\n"
    "v_mfma_f32_16x16x32_bf16 v[56:59], a[252:255], v[20:23], v[56:59]\n"
    "v_rcp_f32 v55, v55\n"
    "v_mfma_f32_16x16x32_bf16 v[60:63], v[80:83], v[20:23], v[60:63]\n"
    "s_waitcnt lgkmcnt(1)\n"
    "v_mul_f32 v53, v53, v77\n"
    "v_mul_f32 v54, v54, v78\n"
    "v_mfma_f32_16x16x32_bf16 v[72:75], v[84:87], v[20:23], v[72:75]\n"
    "ds_read_b128 v[20:23], v0 offset:13312\n"
    "v_mfma_f32_16x16x32_bf16 v[32:35], v[88:91], v[24:27], v[32:35]\n"
    "v_mfma_f32_16x16x32_bf16 v[36:39], v[92:95], v[24:27], v[36:39]\n"
    "v_mul_f32 v55, v55, v79\n"
    "v_cvt_pk_bf16_f32 v52, v52, v53\n"
    "v_mfma_f32_16x16x32_bf16 v[48:51], v[96:99], v[24:27], v[48:51]\n"
    "v_cvt_pk_bf16_f32 v53, v54, v55\n"
    "s_nop 0\n"
    "v_mfma_f32_16x16x32_bf16 v[56:59], v[100:103], v[24:27], v[56:59]\n"
    "v_permlane16_swap_b32 v52, v54\n"
    "v_mfma_f32_16x16x32_bf16 v[60:63], v[104:107], v[24:27], v[60:63]\n"
    "s_waitcnt lgkmcnt(1)\n"
    "v_permlane16_swap_b32 v53, v55\n"
    "v_mfma_f32_16x16x32_bf16 v[72:75], v[108:111], v[24:27], v[72:75]\n"
    "ds_read_b128 v[24:27], v0 offset:14336\n"
    "s_branch .LgUP_join0_%=\n"
    ".LgUP_dma0_48_%=:\n"
    "v_mfma_f32_16x16x32_bf16 v[32:35], a[192:195], v[24:27], v[32:35]\n"
    "s_add_u32 m0, s59, 20480\n"
    "v_mfma_f32_16x16x32_bf16 v[36:39], a[196:199], v[24:27], v[36:39]\n"
    "buffer_load_dwordx4 v1, s[36:39], s76 offen sc1 lds\n"
    "v_mul_f32 v79, v55, v79\n"
    "v_mul_f32 v52, 0xbfb8aa3b, v52\n"
    "v_mfma_f32_16x16x32_bf16 v[48:51], a[200:203], v[24:27], v[48:51]\n"
    "s_add_u32 s76, s76, 16384\n"
    "v_exp_f32 v52, v52\n"
    "v_mfma_f32_16x16x32_bf16 v[56:59], a[204:207], v[24:27], v[56:59]\n"
    "s_add_u32 m0, s59, 24576\n"
    "v_mul_f32 v53, 0xbfb8aa3b, v53\n"
    "v_mul_f32 v54, 0xbfb8aa3b, v54\n"
    "v_mfma_f32_16x16x32_bf16 v[60:63], a[208:211], v[24:27], v[60:63]\n"
    "s_waitcnt lgkmcnt(1)\n"
    "v_exp_f32 v53, v53\n"
    "v_mfma_f32_16x16x32_bf16 v[72:75], a[212:215], v[24:27], v[72:75]\n"
    "ds_read_b128 v[24:27], v0 offset:11264\n"
    "v_mul_f32 v55, 0xbfb8aa3b, v55\n"
    "v_mfma_f32_16x16x32_bf16 v[32:35], a[216:219], v[16:19], v[32:35]\n"
    "buffer_load_dwordx4 v1, s[36:39], s76 offen sc1 lds\n"
    "v_exp_f32 v54, v54\n"
    "v_mfma_f32_16x16x32_bf16 v[36:39], a[220:223], v[16:19], v[36:39]\n"
    "s_add_u32 s76, s76, 16384\n"
    "v_exp_f32 v55, v55\n"
    "v_mfma_f32_16x16x32_bf16 v[48:51], a[224:227], v[16:19], v[48:51]\n"
    "s_add_u32 m0, s59, 28672\n"
    "v_add_f32 v52, 1.0, v52\n"
    "v_add_f32 v53, 1.0, v53\n"
    "v_mfma_f32_16x16x32_bf16 v[56:59], a[228:231], v[16:19], v[56:59]\n"
    "buffer_load_dwordx4 v1, s[36:39], s76 offen sc1 lds\n"
    "v_rcp_f32 v52, v52\n"
    "v_mfma_f32_16x16x32_bf16 v[60:63], a[232:235], v[16:19], v[60:63]\n"
    "s_waitcnt lgkmcnt(1)\n"
    "v_rcp_f32 v53, v53\n"
    "v_mfma_f32_16x16x32_bf16 v[72:75], a[236:239], v[16:19], v[72:75]\n"
    "ds_read_b128 v[16:19], v0 offset:12288\n"
    "v_add_f32 v54, 1.0, v54\n"
    "v_mfma_f32_16x16x32_bf16 v[32:35], a[240:243], v[20:23], v[32:35]\n"
    "s_add_u32 s76, s76, 16384\n"
    "v_rcp_f32 v54, v54\n"
    "v_mfma_f32_16x16x32_bf16 v[36:39], a[244:247], v[20:23], v[36:39]\n"
    "s_add_u32 m0, s59, 32768\n"
    "v_add_f32 v55, 1.0, v55\n"
    "v_mfma_f32_16x16x32_bf16 v[48:51], a[248:251], v[20:23], v[48:51]\n"
    "buffer_load_dwordx4 v1, s[36:39], s76 offen sc1 lds\n"
    "s_add_u32 s76, s76, 16384\n"
    "v_mul_f32 v52, v52, v76\n"
    "v_mfma_f32_16x16x32_bf16 v[56:59], a[252:255], v[20:23], v[56:59]\n"
    "s_add_u32 m0, s59, 36864\n"
    "v_rcp_f32 v55, v55\n"
    "v_mfma_f32_16x16x32_bf16 v[60:63], v[80:83], v[20:23], v[60:63]\n"
    "s_waitcnt lgkmcnt(1)\n"
    "v_mul_f32 v53, v53, v77\n"
    "v_mul_f32 v54, v54, v78\n"
    "v_mfma_f32_16x16x32_bf16 v[72:75], v[84:87], v[20:23], v[72:75]\n"
    "ds_read_b128 v[20:23], v0 offset:13312\n"
    "buffer_load_dwordx4 v8, s[36:39], s76 offen sc1 lds\n"
    "v_mfma_f32_16x16x32_bf16 v[32:35], v[88:91], v[24:27], v[32:35]\n"
    "v_mfma_f32_16x16x32_bf16 v[36:39], v[92:95], v[24:27], v[36:39]\n"
    "v_mul_f32 v55, v55, v79\n"
    "v_cvt_pk_bf16_f32 v52, v52, v53\n"
    "v_mfma_f32_16x16x32_bf16 v[48:51], v[96:99], v[24:27], v[48:51]\n"
    "v_cvt_pk_bf16_f32 v53, v54, v55\n"
    "s_nop 0\n"
    "v_mfma_f32_16x16x32_bf16 v[56:59], v[100:103], v[24:27], v[56:59]\n"
    "v_permlane16_swap_b32 v52, v54\n"
    "v_mfma_f32_16x16x32_bf16 v[60:63], v[104:107], v[24:27], v[60:63]\n"
    "s_waitcnt lgkmcnt(1)\n"
    "v_permlane16_swap_b32 v53, v55\n"
    "v_mfma_f32_16x16x32_bf16 v[72:75], v[108:111], v[24:27], v[72:75]\n"
    "ds_read_b128 v[24:27], v0 offset:14336\n"
    "s_branch .LgUP_join0_%=\n"
    ".LgUP_dma0_66_%=:\n"
    "v_mfma_f32_16x16x32_bf16 v[32:35], v[88:91], v[24:27], v[32:35]\n"
    "s_add_u32 m0, s59, 20480\n"
    "v_mfma_f32_16x16x32_bf16 v[36:39], v[92:95], v[24:27], v[36:39]\n"
    "buffer_load_dwordx4 v1, s[36:39], s76 offen sc1 lds\n"
    "v_mul_f32 v55, v55, v79\n"
    "v_cvt_pk_bf16_f32 v52, v52, v53\n"
    "v_mfma_f32_16x16x32_bf16 v[48:51], v[96:99], v[24:27], v[48:51]\n"
    "s_add_u32 s76, s76, 16384\n"
    "v_cvt_pk_bf16_f32 v53, v54, v55\n"
    "s_nop 0\n"
    "v_mfma_f32_16x16x32_bf16 v[56:59], v[100:103], v[24:27], v[56:59]\n"
    "s_add_u32 m0, s59, 24576\n"
    "v_permlane16_swap_b32 v52, v54\n"
    "v_mfma_f32_16x16x32_bf16 v[60:63], v[104:107], v[24:27], v[60:63]\n"
    "s_waitcnt lgkmcnt(1)\n"
    "v_permlane16_swap_b32 v53, v55\n"
    "v_mfma_f32_16x16x32_bf16 v[72:75], v[108:111], v[24:27], v[72:75]\n"
    "ds_read_b128 v[24:27], v0 offset:14336\n"
    "buffer_load_dwordx4 v1, s[36:39], s76 offen sc1 lds\n"
    "v_mfma_f32_16x16x32_bf16 v[32:35], v[112:115], v[16:19], v[32:35]\n"
    "buffer_store_dwordx4 v[52:55], v4, s[36:39], s73 offen offset:1024 sc1\n"
    "v_mfma_f32_16x16x32_bf16 v[36:39], v[116:119], v[16:19], v[36:39]\n"
    "s_add_u32 s76, s76, 16384\n"
    "s_add_u32 m0, s59, 28672\n"
    "s_lshr_b32 s73, s68, 1\n"
    "v_mfma_f32_16x16x32_bf16 v[48:51], v[120:123], v[16:19], v[48:51]\n"
    "buffer_load_dwordx4 v1, s[36:39], s76 offen sc1 lds\n"
    "s_and_b32 s73, s73, s57\n"
    "v_mfma_f32_16x16x32_bf16 v[56:59], v[124:127], v[16:19], v[56:59]\n"
    "s_mul_i32 s73, s73, s56\n"
    "v_mfma_f32_16x16x32_bf16 v[60:63], v[128:131], v[16:19], v[60:63]\n"
    "s_waitcnt lgkmcnt(1)\n"
    "s_add_u32 s76, s76, 16384\n"
    "s_add_u32 s73, s73, s55\n"
    "v_mfma_f32_16x16x32_bf16 v[72:75], v[132:135], v[16:19], v[72:75]\n"
    "ds_read_b128 v[16:19], v0 offset:15360\n"
    "s_add_u32 m0, s59, 32768\n"
    "v_mfma_f32_16x16x32_bf16 v[32:35], v[136:139], v[20:23], v[32:35]\n"
    "buffer_load_dwordx4 v1, s[36:39], s76 offen sc1 lds\n"
    "v_mfma_f32_16x16x32_bf16 v[36:39], v[140:143], v[20:23], v[36:39]\n"
    "v_mfma_f32_16x16x32_bf16 v[48:51], v[144:147], v[20:23], v[48:51]\n"
    "s_add_u32 s76, s76, 16384\n"
    "s_add_u32 m0, s59, 36864\n"
    "v_mfma_f32_16x16x32_bf16 v[56:59], v[148:151], v[20:23], v[56:59]\n"
    "buffer_load_dwordx4 v8, s[36:39], s76 offen sc1 lds\n"
    "v_mfma_f32_16x16x32_bf16 v[60:63], v[152:155], v[20:23], v[60:63]\n"
    "s_waitcnt lgkmcnt(1)\n"
    "v_mfma_f32_16x16x32_bf16 v[72:75], v[156:159], v[20:23], v[72:75]\n"
    "ds_read_b128 v[20:23], v0 offset:16384\n"
    "v_mfma_f32_16x16x32_bf16 v[32:35], v[160:163], v[24:27], v[32:35]\n"
    "v_mfma_f32_16x16x32_bf16 v[36:39], v[164:167], v[24:27], v[36:39]\n"
    "v_mfma_f32_16x16x32_bf16 v[48:51], v[168:171], v[24:27], v[48:51]\n"
    "v_mfma_f32_16x16x32_bf16 v[56:59], v[172:175], v[24:27], v[56:59]\n"
    "v_mfma_f32_16x16x32_bf16 v[60:63], v[176:179], v[24:27], v[60:63]\n"
    "s_waitcnt lgkmcnt(1)\n"
    "v_mfma_f32_16x16x32_bf16 v[72:75], v[180:183], v[24:27], v[72:75]\n"
    "ds_read_b128 v[24:27], v0 offset:17408\n"
    "v_mfma_f32_16x16x32_bf16 v[32:35], v[184:187], v[16:19], v[32:35]\n"
    "v_mfma_f32_16x16x32_bf16 v[36:39], v[188:191], v[16:19], v[36:39]\n"
    "v_mfma_f32_16x16x32_bf16 v[48:51], v[192:195], v[16:19], v[48:51]\n"
    "v_mfma_f32_16x16x32_bf16 v[56:59], v[196:199], v[16:19], v[56:59]\n"
    "v_mfma_f32_16x16x32_bf16 v[60:63], v[200:203], v[16:19], v[60:63]\n"
    "s_waitcnt lgkmcnt(1)\n"
    "v_mfma_f32_16x16x32_bf16 v[72:75], v[204:207], v[16:19], v[72:75]\n"
    "v_mfma_f32_16x16x32_bf16 v[32:35], v[208:211], v[20:23], v[32:35]\n"
    "v_mfma_f32_16x16x32_bf16 v[36:39], v[212:215], v[20:23], v[36:39]\n"
    "v_mfma_f32_16x16x32_bf16 v[48:51], v[216:219], v[20:23], v[48:51]\n"
    "v_mfma_f32_16x16x32_bf16 v[56:59], v[220:223], v[20:23], v[56:59]\n"
    "v_mfma_f32_16x16x32_bf16 v[60:63], v[224:227], v[20:23], v[60:63]\n"
    "s_waitcnt lgkmcnt(0)\n"
    "v_mfma_f32_16x16x32_bf16 v[72:75], v[228:231], v[20:23], v[72:75]\n"
    "v_mfma_f32_16x16x32_bf16 v[32:35], v[232:235], v[24:27], v[32:35]\n"
    "v_mfma_f32_16x16x32_bf16 v[36:39], v[236:239], v[24:27], v[36:39]\n"
    "v_mfma_f32_16x16x32_bf16 v[48:51], v[240:243], v[24:27], v[48:51]\n"
    "s_waitcnt vmcnt(3)\n"
    "buffer_atomic_smax v12, v10, s[36:39], s70 offen sc1\n"
    "v_mfma_f32_16x16x32_bf16 v[56:59], v[244:247], v[24:27], v[56:59]\n"
    "v_mfma_f32_16x16x32_bf16 v[60:63], v[248:251], v[24:27], v[60:63]\n"
    "v_mfma_f32_16x16x32_bf16 v[72:75], v[252:255], v[24:27], v[72:75]\n"
    "v_pk_mul_f32 v[56:57], v[32:33], v[56:57]\n"
    "v_pk_mul_f32 v[58:59], v[34:35], v[58:59]\n"
    "s_nop 1\n"
    "v_pk_mul_f32 v[60:61], v[36:37], v[60:61]\n"
    "v_pk_mul_f32 v[62:63], v[38:39], v[62:63]\n"
    "v_pk_mul_f32 v[32:33], v[32:33], s[64:65] op_sel_hi:[1,0]\n"
    "v_pk_mul_f32 v[34:35], v[34:35], s[64:65] op_sel_hi:[1,0]\n"
    "v_pk_mul_f32 v[36:37], v[36:37], s[64:65] op_sel_hi:[1,0]\n"
    "v_pk_mul_f32 v[38:39], v[38:39], s[64:65] op_sel_hi:[1,0]\n"
    "v_exp_f32 v32, v32\n"
    "v_exp_f32 v33, v33\n"
    "v_exp_f32 v34, v34\n"
    "v_exp_f32 v35, v35\n"
    "v_exp_f32 v36, v36\n"
    "v_exp_f32 v37, v37\n"
    "v_exp_f32 v38, v38\n"
    "v_exp_f32 v39, v39\n"
    "v_pk_add_f32 v[32:33], v[32:33], 1.0 op_sel_hi:[1,0]\n"
    "v_pk_add_f32 v[34:35], v[34:35], 1.0 op_sel_hi:[1,0]\n"
    "v_pk_add_f32 v[36:37], v[36:37], 1.0 op_sel_hi:[1,0]\n"
    "v_pk_add_f32 v[38:39], v[38:39], 1.0 op_sel_hi:[1,0]\n"
    "v_rcp_f32 v32, v32\n"
    "v_rcp_f32 v33, v33\n"
    "v_rcp_f32 v34, v34\n"
    "v_rcp_f32 v35, v35\n"
    "v_rcp_f32 v36, v36\n"
    "v_rcp_f32 v37, v37\n"
    "v_rcp_f32 v38, v38\n"
    "v_rcp_f32 v39, v39\n"
    "v_pk_mul_f32 v[32:33], v[32:33], v[56:57]\n"
    "v_pk_mul_f32 v[34:35], v[34:35], v[58:59]\n"
    "v_pk_mul_f32 v[36:37], v[36:37], v[60:61]\n"
    "v_pk_mul_f32 v[38:39], v[38:39], v[62:63]\n"
    "v_cvt_pk_bf16_f32 v32, v32, v33\n"
    "v_cvt_pk_bf16_f32 v33, v34, v35\n"
    "v_cvt_pk_bf16_f32 v34, v36, v37\n"
    "v_cvt_pk_bf16_f32 v35, v38, v39\n"
    "buffer_store_dwordx4 v[32:35], v15, s[36:39], s73 offen sc1\n"
    "v_pk_mul_f32 v[72:73], v[48:49], v[72:73]\n"
    "v_pk_mul_f32 v[74:75], v[50:51], v[74:75]\n"
    "v_pk_mul_f32 v[48:49], v[48:49], s[64:65] op_sel_hi:[1,0]\n"
    "v_pk_mul_f32 v[50:51], v[50:51], s[64:65] op_sel_hi:[1,0]\n"
    "v_exp_f32 v48, v48\n"
    "v_exp_f32 v49, v49\n"
    "v_exp_f32 v50, v50\n"
    "v_exp_f32 v51, v51\n"
    "v_pk_add_f32 v[48:49], v[48:49], 1.0 op_sel_hi:[1,0]\n"
    "v_pk_add_f32 v[50:51], v[50:51], 1.0 op_sel_hi:[1,0]\n"
    "v_rcp_f32 v48, v48\n"
    "v_rcp_f32 v49, v49\n"
    "v_rcp_f32 v50, v50\n"
    "v_rcp_f32 v51, v51\n"
    "v_pk_mul_f32 v[48:49], v[48:49], v[72:73]\n"
    "v_pk_mul_f32 v[50:51], v[50:51], v[74:75]\n"
    "v_cvt_pk_bf16_f32 v48, v48, v49\n"
    "v_cvt_pk_bf16_f32 v49, v50, v51\n"
    "s_nop 0\n"
    "v_permlane16_swap_b32 v48, v50\n"
    "v_permlane16_swap_b32 v49, v51\n"
    "buffer_store_dwordx4 v[48:51], v4, s[36:39], s73 offen sc1\n"
    "v_mov_b32 v12, s68\n"
    "s_mov_b32 s73, 0x80000000\n"
    "s_mov_b32 s68, s71\n"
    "s_mov_b32 s69, 0\n"
    "s_mov_b32 s70, 0x80000000\n"
    "v_mov_b32 v7, v12\n"
    "s_waitcnt vmcnt(9)\n"
    "s_add_u32 s71, s68, 1\n"
    "v_add_u32 v13, v11, v3\n"
    "v_cmp_gt_i32 vcc, s71, v13\n"
    "s_nop 0\n"
    "s_or_b64 exec, vcc, s[62:63]\n"
    "buffer_atomic_smax v11, v2, s[36:39], 0 offen sc0 sc1\n"
    "s_mov_b64 exec, -1\n"
    "s_waitcnt vmcnt(4)\n"
    "s_branch .LgUP_bar1_%=\n"
    ".LgUP_dma0_84_%=:\n"
    "v_mfma_f32_16x16x32_bf16 v[32:35], v[160:163], v[24:27], v[32:35]\n"
    "s_add_u32 m0, s59, 20480\n"
    "v_mfma_f32_16x16x32_bf16 v[36:39], v[164:167], v[24:27], v[36:39]\n"
    "buffer_load_dwordx4 v1, s[36:39], s76 offen sc1 lds\n"
    "v_mfma_f32_16x16x32_bf16 v[48:51], v[168:171], v[24:27], v[48:51]\n"
    "s_add_u32 s76, s76, 16384\n"
    "s_add_u32 m0, s59, 24576\n"
    "v_mfma_f32_16x16x32_bf16 v[56:59], v[172:175], v[24:27], v[56:59]\n"
    "buffer_load_dwordx4 v1, s[36:39], s76 offen sc1 lds\n"
    "v_mfma_f32_16x16x32_bf16 v[60:63], v[176:179], v[24:27], v[60:63]\n"
    "s_waitcnt lgkmcnt(1)\n"
    "v_mfma_f32_16x16x32_bf16 v[72:75], v[180:183], v[24:27], v[72:75]\n"
    "ds_read_b128 v[24:27], v0 offset:17408\n"
    "v_mfma_f32_16x16x32_bf16 v[32:35], v[184:187], v[16:19], v[32:35]\n"
    "s_add_u32 s76, s76, 16384\n"
    "s_add_u32 m0, s59, 28672\n"
    "v_mfma_f32_16x16x32_bf16 v[36:39], v[188:191], v[16:19], v[36:39]\n"
    "buffer_load_dwordx4 v1, s[36:39], s76 offen sc1 lds\n"
    "v_mfma_f32_16x16x32_bf16 v[48:51], v[192:195], v[16:19], v[48:51]\n"
    "v_mfma_f32_16x16x32_bf16 v[56:59], v[196:199], v[16:19], v[56:59]\n"
    "v_mfma_f32_16x16x32_bf16 v[60:63], v[200:203], v[16:19], v[60:63]\n"
    "s_waitcnt lgkmcnt(1)\n"
    "s_add_u32 s76, s76, 16384\n"
    "s_add_u32 m0, s59, 32768\n"
    "v_mfma_f32_16x16x32_bf16 v[72:75], v[204:207], v[16:19], v[72:75]\n"
    "buffer_load_dwordx4 v1, s[36:39], s76 offen sc1 lds\n"
    "v_mfma_f32_16x16x32_bf16 v[32:35], v[208:211], v[20:23], v[32:35]\n"
    "v_mfma_f32_16x16x32_bf16 v[36:39], v[212:215], v[20:23], v[36:39]\n"
    "v_mfma_f32_16x16x32_bf16 v[48:51], v[216:219], v[20:23], v[48:51]\n"
    "s_add_u32 s76, s76, 16384\n"
    "s_add_u32 m0, s59, 36864\n"
    "v_mfma_f32_16x16x32_bf16 v[56:59], v[220:223], v[20:23], v[56:59]\n"
    "buffer_load_dwordx4 v8, s[36:39], s76 offen sc1 lds\n"
    "v_mfma_f32_16x16x32_bf16 v[60:63], v[224:227], v[20:23], v[60:63]\n"
    "s_waitcnt lgkmcnt(0)\n"
    "v_mfma_f32_16x16x32_bf16 v[72:75], v[228:231], v[20:23], v[72:75]\n"
    "v_mfma_f32_16x16x32_bf16 v[32:35], v[232:235], v[24:27], v[32:35]\n"
    "v_mfma_f32_16x16x32_bf16 v[36:39], v[236:239], v[24:27], v[36:39]\n"
    "v_mfma_f32_16x16x32_bf16 v[48:51], v[240:243], v[24:27], v[48:51]\n"
    "s_waitcnt vmcnt(5)\n"
    "buffer_atomic_smax v12, v10, s[36:39], s70 offen sc1\n"
    "v_mfma_f32_16x16x32_bf16 v[56:59], v[244:247], v[24:27], v[56:59]\n"
    "v_mfma_f32_16x16x32_bf16 v[60:63], v[248:251], v[24:27], v[60:63]\n"
    "v_mfma_f32_16x16x32_bf16 v[72:75], v[252:255], v[24:27], v[72:75]\n"
    "v_pk_mul_f32 v[56:57], v[32:33], v[56:57]\n"
    "v_pk_mul_f32 v[58:59], v[34:35], v[58:59]\n"
    "s_nop 1\n"
    "v_pk_mul_f32 v[60:61], v[36:37], v[60:61]\n"
    "v_pk_mul_f32 v[62:63], v[38:39], v[62:63]\n"
    "v_pk_mul_f32 v[32:33], v[32:33], s[64:65] op_sel_hi:[1,0]\n"
    "v_pk_mul_f32 v[34:35], v[34:35], s[64:65] op_sel_hi:[1,0]\n"
    "v_pk_mul_f32 v[36:37], v[36:37], s[64:65] op_sel_hi:[1,0]\n"
    "v_pk_mul_f32 v[38:39], v[38:39], s[64:65] op_sel_hi:[1,0]\n"
    "v_exp_f32 v32, v32\n"
    "v_exp_f32 v33, v33\n"
    "v_exp_f32 v34, v34\n"
    "v_exp_f32 v35, v35\n"
    "v_exp_f32 v36, v36\n"
    "v_exp_f32 v37, v37\n"
    "v_exp_f32 v38, v38\n"
    "v_exp_f32 v39, v39\n"
    "v_pk_add_f32 v[32:33], v[32:33], 1.0 op_sel_hi:[1,0]\n"
    "v_pk_add_f32 v[34:35], v[34:35], 1.0 op_sel_hi:[1,0]\n"
    "v_pk_add_f32 v[36:37], v[36:37], 1.0 op_sel_hi:[1,0]\n"
    "v_pk_add_f32 v[38:39], v[38:39], 1.0 op_sel_hi:[1,0]\n"
    "v_rcp_f32 v32, v32\n"
    "v_rcp_f32 v33, v33\n"
    "v_rcp_f32 v34, v34\n"
    "v_rcp_f32 v35, v35\n"
    "v_rcp_f32 v36, v36\n"
    "v_rcp_f32 v37, v37\n"
    "v_rcp_f32 v38, v38\n"
    "v_rcp_f32 v39, v39\n"
    "v_pk_mul_f32 v[32:33], v[32:33], v[56:57]\n"
    "v_pk_mul_f32 v[34:35], v[34:35], v[58:59]\n"
    "v_pk_mul_f32 v[36:37], v[36:37], v[60:61]\n"
    "v_pk_mul_f32 v[38:39], v[38:39], v[62:63]\n"
    "v_cvt_pk_bf16_f32 v32, v32, v33\n"
    "v_cvt_pk_bf16_f32 v33, v34, v35\n"
    "v_cvt_pk_bf16_f32 v34, v36, v37\n"
    "v_cvt_pk_bf16_f32 v35, v38, v39\n"
    "buffer_store_dwordx4 v[32:35], v15, s[36:39], s73 offen sc1\n"
    "v_pk_mul_f32 v[72:73], v[48:49], v[72:73]\n"
    "v_pk_mul_f32 v[74:75], v[50:51], v[74:75]\n"
    "v_pk_mul_f32 v[48:49], v[48:49], s[64:65] op_sel_hi:[1,0]\n"
    "v_pk_mul_f32 v[50:51], v[50:51], s[64:65] op_sel_hi:[1,0]\n"
    "v_exp_f32 v48, v48\n"
    "v_exp_f32 v49, v49\n"
    "v_exp_f32 v50, v50\n"
    "v_exp_f32 v51, v51\n"
    "v_pk_add_f32 v[48:49], v[48:49], 1.0 op_sel_hi:[1,0]\n"
    "v_pk_add_f32 v[50:51], v[50:51], 1.0 op_sel_hi:[1,0]\n"
    "v_rcp_f32 v48, v48\n"
    "v_rcp_f32 v49, v49\n"
    "v_rcp_f32 v50, v50\n"
    "v_rcp_f32 v51, v51\n"
    "v_pk_mul_f32 v[48:49], v[48:49], v[72:73]\n"
    "v_pk_mul_f32 v[50:51], v[50:51], v[74:75]\n"
    "v_cvt_pk_bf16_f32 v48, v48, v49\n"
    "v_cvt_pk_bf16_f32 v49, v50, v51\n"
    "s_nop 0\n"
    "v_permlane16_swap_b32 v48, v50\n"
    "v_permlane16_swap_b32 v49, v51\n"
    "buffer_store_dwordx4 v[48:51], v4, s[36:39], s73 offen sc1\n"
    "v_mov_b32 v12, s68\n"
    "s_mov_b32 s73, 0x80000000\n"
    "s_mov_b32 s68, s71\n"
    "s_mov_b32 s69, 0\n"
    "s_mov_b32 s70, 0x80000000\n"
    "v_mov_b32 v7, v12\n"
    "s_waitcnt vmcnt(9)\n"
    "s_add_u32 s71, s68, 1\n"
    "v_add_u32 v13, v11, v3\n"
    "v_cmp_gt_i32 vcc, s71, v13\n"
    "s_nop 0\n"
    "s_or_b64 exec, vcc, s[62:63]\n"
    "buffer_atomic_smax v11, v2, s[36:39], 0 offen sc0 sc1\n"
    "s_mov_b64 exec, -1\n"
    "s_waitcnt vmcnt(4)\n"
    "s_branch .LgUP_bar1_%=\n"
    ".LgUP_dma0_102_%=:\n"
    "v_mfma_f32_16x16x32_bf16 v[32:35], v[232:235], v[24:27], v[32:35]\n"
    "s_add_u32 m0, s59, 20480\n"
    "v_mfma_f32_16x16x32_bf16 v[36:39], v[236:239], v[24:27], v[36:39]\n"
    "buffer_load_dwordx4 v1, s[36:39], s76 offen sc1 lds\n"
    "v_mfma_f32_16x16x32_bf16 v[48:51], v[240:243], v[24:27], v[48:51]\n"
    "s_add_u32 s76, s76, 16384\n"
    "s_waitcnt vmcnt(2)\n"
    "buffer_atomic_smax v12, v10, s[36:39], s70 offen sc1\n"
    "v_mfma_f32_16x16x32_bf16 v[56:59], v[244:247], v[24:27], v[56:59]\n"
    "s_add_u32 m0, s59, 24576\n"
    "v_mfma_f32_16x16x32_bf16 v[60:63], v[248:251], v[24:27], v[60:63]\n"
    "buffer_load_dwordx4 v1, s[36:39], s76 offen sc1 lds\n"
    "v_mfma_f32_16x16x32_bf16 v[72:75], v[252:255], v[24:27], v[72:75]\n"
    "v_pk_mul_f32 v[56:57], v[32:33], v[56:57]\n"
    "v_pk_mul_f32 v[58:59], v[34:35], v[58:59]\n"
    "s_nop 1\n"
    "v_pk_mul_f32 v[60:61], v[36:37], v[60:61]\n"
    "v_pk_mul_f32 v[62:63], v[38:39], v[62:63]\n"
    "v_pk_mul_f32 v[32:33], v[32:33], s[64:65] op_sel_hi:[1,0]\n"
    "v_pk_mul_f32 v[34:35], v[34:35], s[64:65] op_sel_hi:[1,0]\n"
    "v_pk_mul_f32 v[36:37], v[36:37], s[64:65] op_sel_hi:[1,0]\n"
    "v_pk_mul_f32 v[38:39], v[38:39], s[64:65] op_sel_hi:[1,0]\n"
    "v_exp_f32 v32, v32\n"
    "v_exp_f32 v33, v33\n"
    "v_exp_f32 v34, v34\n"
    "v_exp_f32 v35, v35\n"
    "v_exp_f32 v36, v36\n"
    "v_exp_f32 v37, v37\n"
    "v_exp_f32 v38, v38\n"
    "v_exp_f32 v39, v39\n"
    "v_pk_add_f32 v[32:33], v[32:33], 1.0 op_sel_hi:[1,0]\n"
    "v_pk_add_f32 v[34:35], v[34:35], 1.0 op_sel_hi:[1,0]\n"
    "v_pk_add_f32 v[36:37], v[36:37], 1.0 op_sel_hi:[1,0]\n"
    "v_pk_add_f32 v[38:39], v[38:39], 1.0 op_sel_hi:[1,0]\n"
    "v_rcp_f32 v32, v32\n"
    "v_rcp_f32 v33, v33\n"
    "v_rcp_f32 v34, v34\n"
    "v_rcp_f32 v35, v35\n"
    "v_rcp_f32 v36, v36\n"
    "v_rcp_f32 v37, v37\n"
    "v_rcp_f32 v38, v38\n"
    "v_rcp_f32 v39, v39\n"
    "v_pk_mul_f32 v[32:33], v[32:33], v[56:57]\n"
    "v_pk_mul_f32 v[34:35], v[34:35], v[58:59]\n"
    "v_pk_mul_f32 v[36:37], v[36:37], v[60:61]\n"
    "v_pk_mul_f32 v[38:39], v[38:39], v[62:63]\n"
    "v_cvt_pk_bf16_f32 v32, v32, v33\n"
    "v_cvt_pk_bf16_f32 v33, v34, v35\n"
    "v_cvt_pk_bf16_f32 v34, v36, v37\n"
    "v_cvt_pk_bf16_f32 v35, v38, v39\n"
    "buffer_store_dwordx4 v[32:35], v15, s[36:39], s73 offen sc1\n"
    "v_pk_mul_f32 v[72:73], v[48:49], v[72:73]\n"
    "v_pk_mul_f32 v[74:75], v[50:51], v[74:75]\n"
    "v_pk_mul_f32 v[48:49], v[48:49], s[64:65] op_sel_hi:[1,0]\n"
    "v_pk_mul_f32 v[50:51], v[50:51], s[64:65] op_sel_hi:[1,0]\n"
    "v_exp_f32 v48, v48\n"
    "v_exp_f32 v49, v49\n"
    "v_exp_f32 v50, v50\n"
    "v_exp_f32 v51, v51\n"
    "v_pk_add_f32 v[48:49], v[48:49], 1.0 op_sel_hi:[1,0]\n"
    "v_pk_add_f32 v[50:51], v[50:51], 1.0 op_sel_hi:[1,0]\n"
    "v_rcp_f32 v48, v48\n"
    "v_rcp_f32 v49, v49\n"
    "v_rcp_f32 v50, v50\n"
    "v_rcp_f32 v51, v51\n"
    "v_pk_mul_f32 v[48:49], v[48:49], v[72:73]\n"
    "v_pk_mul_f32 v[50:51], v[50:51], v[74:75]\n"
    "v_cvt_pk_bf16_f32 v48, v48, v49\n"
    "v_cvt_pk_bf16_f32 v49, v50, v51\n"
    "s_nop 0\n"
    "v_permlane16_swap_b32 v48, v50\n"
    "v_permlane16_swap_b32 v49, v51\n"
    "buffer_store_dwordx4 v[48:51], v4, s[36:39], s73 offen sc1\n"
    "v_mov_b32 v12, s68\n"
    "s_mov_b32 s73, 0x80000000\n"
    "s_mov_b32 s68, s71\n"
    "s_add_u32 m0, s59, 28672\n"
    "s_add_u32 s76, s76, 16384\n"
    "buffer_load_dwordx4 v1, s[36:39], s76 offen sc1 lds\n"
    "s_add_u32 m0, s59, 32768\n"
    "s_add_u32 s76, s76, 16384\n"
    "buffer_load_dwordx4 v1, s[36:39], s76 offen sc1 lds\n"
    "s_add_u32 m0, s59, 36864\n"
    "s_add_u32 s76, s76, 16384\n"
    "buffer_load_dwordx4 v8, s[36:39], s76 offen sc1 lds\n"
    "s_waitcnt vmcnt(3)\n"
    "buffer_atomic_smax v12, v14, s[36:39], 0 offen sc1\n"
    "s_mov_b32 s69, 0x80000000\n"
    "s_mov_b32 s70, 0x80000000\n"
    "v_mov_b32 v7, v12\n"
    "s_waitcnt vmcnt(9)\n"
    "s_add_u32 s71, s68, 1\n"
    "v_add_u32 v13, v11, v3\n"
    "v_cmp_gt_i32 vcc, s71, v13\n"
    "s_nop 0\n"
    "s_or_b64 exec, vcc, s[62:63]\n"
    "buffer_atomic_smax v11, v2, s[36:39], 0 offen sc0 sc1\n"
    "s_mov_b64 exec, -1\n"
    "s_waitcnt vmcnt(2)\n"
    "s_branch .LgUP_bar1_%=\n"
    ".LgUP_idle0_%=:\n"
    "s_or_b64 exec, vcc, s[62:63]\n"
    "buffer_atomic_smax v11, v2, s[36:39], 0 offen sc0 sc1\n"
    "s_mov_b64 exec, -1\n"
    "s_waitcnt vmcnt(1)\n"
    "buffer_atomic_smax v12, v14, s[36:39], 0 offen sc1\n"
    "s_branch .LgUP_block0_0_%=\n"
    ".LgUP_block0_%=:\n"
    "v_add_u32 v13, v11, v3\n"
    "v_cmp_gt_i32 vcc, s71, v13\n"
    "s_nop 0\n"
    "s_or_b64 exec, vcc, s[62:63]\n"
    "buffer_atomic_smax v11, v2, s[36:39], 0 offen sc0 sc1\n"
    "s_mov_b64 exec, -1\n"
    "s_sleep 3\n"
    "s_waitcnt vmcnt(1)\n"
    ".LgUP_block0_0_%=:\n"
    "v_add_u32 v13, v5, v3\n"
    "v_cmp_gt_i32 vcc, s71, v13\n"
    "s_cbranch_vccz .LgUP_go0_%=\n"
    "v_add_u32 v13, v5, v3\n"
    "v_cmp_gt_i32 vcc, s71, v13\n"
    "s_nop 0\n"
    "s_or_b64 exec, vcc, s[62:63]\n"
    "buffer_atomic_smax v5, v2, s[36:39], 0 offen sc0 sc1\n"
    "s_mov_b64 exec, -1\n"
    "s_sleep 3\n"
    "s_waitcnt vmcnt(1)\n"
    ".LgUP_block0_1_%=:\n"
    "v_add_u32 v13, v11, v3\n"
    "v_cmp_gt_i32 vcc, s71, v13\n"
    "s_cbranch_vccz .LgUP_go0_%=\n"
    "s_sub_u32 s72, s72, 1\n"
    "s_cbranch_scc0 .LgUP_block0_%=\n"
    "s_mov_b32 s72, 0\n"
    ".LgUP_go0_%=:\n"
    "s_lshr_b32 s76, s71, 1\n"
    "s_and_b32 s76, s76, s54\n"
    "s_mul_i32 s76, s76, s53\n"
    "s_add_u32 s76, s76, s52\n"
    "s_add_u32 s76, s76, 1024\n"
    "s_add_u32 m0, s59, 20480\n"
    "s_nop 0\n"
    "buffer_load_dwordx4 v1, s[36:39], s76 offen sc1 lds\n"
    "s_add_u32 m0, s59, 24576\n"
    "s_add_u32 s76, s76, 16384\n"
    "buffer_load_dwordx4 v1, s[36:39], s76 offen sc1 lds\n"
    "s_add_u32 m0, s59, 28672\n"
    "s_add_u32 s76, s76, 16384\n"
    "buffer_load_dwordx4 v1, s[36:39], s76 offen sc1 lds\n"
    "s_add_u32 m0, s59, 32768\n"
    "s_add_u32 s76, s76, 16384\n"
    "buffer_load_dwordx4 v1, s[36:39], s76 offen sc1 lds\n"
    "s_add_u32 m0, s59, 36864\n"
    "s_add_u32 s76, s76, 16384\n"
    "buffer_load_dwordx4 v8, s[36:39], s76 offen sc1 lds\n"
    "s_mov_b32 s69, 0x80000000\n"
    "s_mov_b32 s70, 0x80000000\n"
    "v_mov_b32 v7, v12\n"
    "s_waitcnt vmcnt(5)\n"
    "s_add_u32 s71, s68, 1\n"
    "v_add_u32 v13, v11, v3\n"
    "v_cmp_gt_i32 vcc, s71, v13\n"
    "s_nop 0\n"
    "s_or_b64 exec, vcc, s[62:63]\n"
    "buffer_atomic_smax v11, v2, s[36:39], 0 offen sc0 sc1\n"
    "s_mov_b64 exec, -1\n"
    "s_waitcnt vmcnt(1)\n"
    "s_branch .LgUP_bar1_%=\n"
    ".LgUP_bar0_%=:\n"
    "s_barrier\n"
    "ds_read_b128 v[16:19], v0 offset:0\n"
    "ds_read_b128 v[20:23], v0 offset:1024\n"
    "ds_read_b128 v[24:27], v0 offset:2048\n"
    "v_add_u32 v13, v5, v3\n"
    "v_cmp_gt_i32 vcc, s71, v13\n"
    "s_nop 0\n"
    "s_or_b64 exec, vcc, s[62:63]\n"
    "buffer_atomic_smax v5, v2, s[36:39], 0 offen sc0 sc1\n"
    "s_mov_b64 exec, -1\n"
    "s_lshr_b32 s76, s71, 1\n"
    "s_and_b32 s76, s76, s54\n"
    "s_mul_i32 s76, s76, s53\n"
    "s_add_u32 s76, s76, s52\n"
    "s_add_u32 s76, s76, 1024\n"
    "s_waitcnt lgkmcnt(2)\n"
    "s_branch .LgUP_head0_%=\n"
    ".LgUP_poll1_%=:\n"
    "v_mfma_f32_16x16x32_bf16 v[40:43], a[48:51], v[24:27], v[40:43]\n"
    "s_or_b64 exec, vcc, s[62:63]\n"
    "buffer_atomic_smax v11, v2, s[36:39], 0 offen sc0 sc1\n"
    "s_mov_b64 exec, -1\n"
    "v_mfma_f32_16x16x32_bf16 v[44:47], a[52:55], v[24:27], v[44:47]\n"
    "v_mul_f32 v35, 0xbfb8aa3b, v35\n"
    "v_mul_f32 v36, 0xbfb8aa3b, v36\n"
    "v_mfma_f32_16x16x32_bf16 v[52:55], a[56:59], v[24:27], v[52:55]\n"
    "v_exp_f32 v35, v35\n"
    "v_mfma_f32_16x16x32_bf16 v[64:67], a[60:63], v[24:27], v[64:67]\n"
    "v_exp_f32 v36, v36\n"
    "v_mfma_f32_16x16x32_bf16 v[68:71], a[64:67], v[24:27], v[68:71]\n"
    "s_waitcnt lgkmcnt(1)\n"
    "v_mul_f32 v37, 0xbfb8aa3b, v37\n"
    "v_mul_f32 v38, 0xbfb8aa3b, v38\n"
    "v_mfma_f32_16x16x32_bf16 v[76:79], a[68:71], v[24:27], v[76:79]\n"
    "ds_read_b128 v[24:27], v9 offset:5120\n"
    "v_mul_f32 v39, 0xbfb8aa3b, v39\n"
    "v_mfma_f32_16x16x32_bf16 v[40:43], a[72:75], v[16:19], v[40:43]\n"
    "v_exp_f32 v37, v37\n"
    "v_mfma_f32_16x16x32_bf16 v[44:47], a[76:79], v[16:19], v[44:47]\n"
    "v_exp_f32 v38, v38\n"
    "v_mfma_f32_16x16x32_bf16 v[52:55], a[80:83], v[16:19], v[52:55]\n"
    "v_exp_f32 v39, v39\n"
    "v_mfma_f32_16x16x32_bf16 v[64:67], a[84:87], v[16:19], v[64:67]\n"
    "v_add_f32 v32, 1.0, v32\n"
    "v_add_f32 v33, 1.0, v33\n"
    "v_mfma_f32_16x16x32_bf16 v[68:71], a[88:91], v[16:19], v[68:71]\n"
    "s_waitcnt lgkmcnt(1)\n"
    "v_rcp_f32 v32, v32\n"
    "v_mfma_f32_16x16x32_bf16 v[76:79], a[92:95], v[16:19], v[76:79]\n"
    "ds_read_b128 v[16:19], v9 offset:6144\n"
    "v_add_f32 v34, 1.0, v34\n"
    "v_mfma_f32_16x16x32_bf16 v[40:43], a[96:99], v[20:23], v[40:43]\n"
    "v_rcp_f32 v33, v33\n"
    "v_mfma_f32_16x16x32_bf16 v[44:47], a[100:103], v[20:23], v[44:47]\n"
    "s_waitcnt vmcnt(2)\n"
    "v_add_f32 v35, 1.0, v35\n"
    "v_add_u32 v13, v5, v3\n"
    "v_mfma_f32_16x16x32_bf16 v[52:55], a[104:107], v[20:23], v[52:55]\n"
    "v_add_f32 v36, 1.0, v36\n"
    "v_cmp_gt_i32 vcc, s71, v13\n"
    "v_mfma_f32_16x16x32_bf16 v[64:67], a[108:111], v[20:23], v[64:67]\n"
    "v_rcp_f32 v34, v34\n"
    "v_mfma_f32_16x16x32_bf16 v[68:71], a[112:115], v[20:23], v[68:71]\n"
    "s_waitcnt lgkmcnt(1)\n"
    "v_rcp_f32 v35, v35\n"
    "v_mfma_f32_16x16x32_bf16 v[76:79], a[116:119], v[20:23], v[76:79]\n"
    "ds_read_b128 v[20:23], v9 offset:7168\n"
    "s_cbranch_vccz .LgUP_dma1_30_%=\n"
    "v_mfma_f32_16x16x32_bf16 v[40:43], a[120:123], v[24:27], v[40:43]\n"
    "s_or_b64 exec, vcc, s[62:63]\n"
    "buffer_atomic_smax v5, v2, s[36:39], 0 offen sc0 sc1\n"
    "s_mov_b64 exec, -1\n"
    "v_mfma_f32_16x16x32_bf16 v[44:47], a[124:127], v[24:27], v[44:47]\n"
    "v_rcp_f32 v36, v36\n"
    "v_mfma_f32_16x16x32_bf16 v[52:55], a[128:131], v[24:27], v[52:55]\n"
    "v_add_f32 v37, 1.0, v37\n"
    "v_add_f32 v38, 1.0, v38\n"
    "v_mfma_f32_16x16x32_bf16 v[64:67], a[132:135], v[24:27], v[64:67]\n"
    "v_rcp_f32 v37, v37\n"
    "v_mfma_f32_16x16x32_bf16 v[68:71], a[136:139], v[24:27], v[68:71]\n"
    "s_waitcnt lgkmcnt(1)\n"
    "v_rcp_f32 v38, v38\n"
    "v_mfma_f32_16x16x32_bf16 v[76:79], a[140:143], v[24:27], v[76:79]\n"
    "ds_read_b128 v[24:27], v9 offset:8192\n"
    "v_add_f32 v39, 1.0, v39\n"
    "v_mfma_f32_16x16x32_bf16 v[40:43], a[144:147], v[16:19], v[40:43]\n"
    "v_rcp_f32 v39, v39\n"
    "v_mfma_f32_16x16x32_bf16 v[44:47], a[148:151], v[16:19], v[44:47]\n"
    "v_mul_f32 v32, v32, v56\n"
    "v_mul_f32 v33, v33, v57\n"
    "v_mfma_f32_16x16x32_bf16 v[52:55], a[152:155], v[16:19], v[52:55]\n"
    "v_mul_f32 v34, v34, v58\n"
    "v_mul_f32 v35, v35, v59\n"
    "v_mfma_f32_16x16x32_bf16 v[64:67], a[156:159], v[16:19], v[64:67]\n"
    "v_mul_f32 v36, v36, v60\n"
    "v_mul_f32 v37, v37, v61\n"
    "v_mfma_f32_16x16x32_bf16 v[68:71], a[160:163], v[16:19], v[68:71]\n"
    "s_waitcnt lgkmcnt(1)\n"
    "v_mul_f32 v38, v38, v62\n"
    "v_mul_f32 v39, v39, v63\n"
    "v_mfma_f32_16x16x32_bf16 v[76:79], a[164:167], v[16:19], v[76:79]\n"
    "ds_read_b128 v[16:19], v9 offset:9216\n"
    "v_cvt_pk_bf16_f32 v32, v32, v33\n"
    "v_mfma_f32_16x16x32_bf16 v[40:43], a[168:171], v[20:23], v[40:43]\n"
    "v_cvt_pk_bf16_f32 v33, v34, v35\n"
    "v_cvt_pk_bf16_f32 v34, v36, v37\n"
    "v_mfma_f32_16x16x32_bf16 v[44:47], a[172:175], v[20:23], v[44:47]\n"
    "s_waitcnt vmcnt(1)\n"
    "v_cvt_pk_bf16_f32 v35, v38, v39\n"
    "v_add_u32 v13, v11, v3\n"
    "v_mfma_f32_16x16x32_bf16 v[52:55], a[176:179], v[20:23], v[52:55]\n"
    "v_mul_f32 v72, v48, v72\n"
    "v_cmp_gt_i32 vcc, s71, v13\n"
    "v_mfma_f32_16x16x32_bf16 v[64:67], a[180:183], v[20:23], v[64:67]\n"
    "buffer_store_dwordx4 v[32:35], v15, s[36:39], s73 offen sc1\n"
    "v_mfma_f32_16x16x32_bf16 v[68:71], a[184:187], v[20:23], v[68:71]\n"
    "s_waitcnt lgkmcnt(1)\n"
    "v_mul_f32 v73, v49, v73\n"
    "v_mul_f32 v74, v50, v74\n"
    "v_mfma_f32_16x16x32_bf16 v[76:79], a[188:191], v[20:23], v[76:79]\n"
    "ds_read_b128 v[20:23], v9 offset:10240\n"
    "s_cbranch_vccz .LgUP_dma1_48_%=\n"
    "v_mfma_f32_16x16x32_bf16 v[40:43], a[192:195], v[24:27], v[40:43]\n"
    "s_or_b64 exec, vcc, s[62:63]\n"
    "buffer_atomic_smax v11, v2, s[36:39], 0 offen sc0 sc1\n"
    "s_mov_b64 exec, -1\n"
    "v_mfma_f32_16x16x32_bf16 v[44:47], a[196:199], v[24:27], v[44:47]\n"
    "v_mul_f32 v75, v51, v75\n"
    "v_mul_f32 v48, 0xbfb8aa3b, v48\n"
    "v_mfma_f32_16x16x32_bf16 v[52:55], a[200:203], v[24:27], v[52:55]\n"
    "v_exp_f32 v48, v48\n"
    "v_mfma_f32_16x16x32_bf16 v[64:67], a[204:207], v[24:27], v[64:67]\n"
    "v_mul_f32 v49, 0xbfb8aa3b, v49\n"
    "v_mul_f32 v50, 0xbfb8aa3b, v50\n"
    "v_mfma_f32_16x16x32_bf16 v[68:71], a[208:211], v[24:27], v[68:71]\n"
    "s_waitcnt lgkmcnt(1)\n"
    "v_exp_f32 v49, v49\n"
    "v_mfma_f32_16x16x32_bf16 v[76:79], a[212:215], v[24:27], v[76:79]\n"
    "ds_read_b128 v[24:27], v9 offset:11264\n"
    "v_mul_f32 v51, 0xbfb8aa3b, v51\n"
    "v_mfma_f32_16x16x32_bf16 v[40:43], a[216:219], v[16:19], v[40:43]\n"
    "v_exp_f32 v50, v50\n"
    "v_mfma_f32_16x16x32_bf16 v[44:47], a[220:223], v[16:19], v[44:47]\n"
    "v_exp_f32 v51, v51\n"
    "v_mfma_f32_16x16x32_bf16 v[52:55], a[224:227], v[16:19], v[52:55]\n"
    "v_add_f32 v48, 1.0, v48\n"
    "v_add_f32 v49, 1.0, v49\n"
    "v_mfma_f32_16x16x32_bf16 v[64:67], a[228:231], v[16:19], v[64:67]\n"
    "v_rcp_f32 v48, v48\n"
    "v_mfma_f32_16x16x32_bf16 v[68:71], a[232:235], v[16:19], v[68:71]\n"
    "s_waitcnt lgkmcnt(1)\n"
    "v_rcp_f32 v49, v49\n"
    "v_mfma_f32_16x16x32_bf16 v[76:79], a[236:239], v[16:19], v[76:79]\n"
    "ds_read_b128 v[16:19], v9 offset:12288\n"
    "v_add_f32 v50, 1.0, v50\n"
    "v_mfma_f32_16x16x32_bf16 v[40:43], a[240:243], v[20:23], v[40:43]\n"
    "v_rcp_f32 v50, v50\n"
    "v_mfma_f32_16x16x32_bf16 v[44:47], a[244:247], v[20:23], v[44:47]\n"
    "s_waitcnt vmcnt(2)\n"
    "v_add_f32 v51, 1.0, v51\n"
    "v_add_u32 v13, v5, v3\n"
    "v_mfma_f32_16x16x32_bf16 v[52:55], a[248:251], v[20:23], v[52:55]\n"
    "v_mul_f32 v48, v48, v72\n"
    "v_cmp_gt_i32 vcc, s71, v13\n"
    "v_mfma_f32_16x16x32_bf16 v[64:67], a[252:255], v[20:23], v[64:67]\n"
    "v_rcp_f32 v51, v51\n"
    "v_mfma_f32_16x16x32_bf16 v[68:71], v[80:83], v[20:23], v[68:71]\n"
    "s_waitcnt lgkmcnt(1)\n"
    "v_mul_f32 v49, v49, v73\n"
    "v_mul_f32 v50, v50, v74\n"
    "v_mfma_f32_16x16x32_bf16 v[76:79], v[84:87], v[20:23], v[76:79]\n"
    "ds_read_b128 v[20:23], v9 offset:13312\n"
    "s_cbranch_vccz .LgUP_dma1_66_%=\n"
    "v_mfma_f32_16x16x32_bf16 v[40:43], v[88:91], v[24:27], v[40:43]\n"
    "s_or_b64 exec, vcc, s[62:63]\n"
    "buffer_atomic_smax v5, v2, s[36:39], 0 offen sc0 sc1\n"
    "s_mov_b64 exec, -1\n"
    "v_mfma_f32_16x16x32_bf16 v[44:47], v[92:95], v[24:27], v[44:47]\n"
    "v_mul_f32 v51, v51, v75\n"
    "v_cvt_pk_bf16_f32 v48, v48, v49\n"
    "v_mfma_f32_16x16x32_bf16 v[52:55], v[96:99], v[24:27], v[52:55]\n"
    "v_cvt_pk_bf16_f32 v49, v50, v51\n"
    "s_nop 0\n"
    "v_mfma_f32_16x16x32_bf16 v[64:67], v[100:103], v[24:27], v[64:67]\n"
    "v_permlane16_swap_b32 v48, v50\n"
    "v_mfma_f32_16x16x32_bf16 v[68:71], v[104:107], v[24:27], v[68:71]\n"
    "s_waitcnt lgkmcnt(1)\n"
    "v_permlane16_swap_b32 v49, v51\n"
    "v_mfma_f32_16x16x32_bf16 v[76:79], v[108:111], v[24:27], v[76:79]\n"
    "ds_read_b128 v[24:27], v9 offset:14336\n"
    "v_mfma_f32_16x16x32_bf16 v[40:43], v[112:115], v[16:19], v[40:43]\n"
    "buffer_store_dwordx4 v[48:51], v4, s[36:39], s73 offen sc1\n"
    "v_mfma_f32_16x16x32_bf16 v[44:47], v[116:119], v[16:19], v[44:47]\n"
    "s_lshr_b32 s73, s68, 1\n"
    "v_mfma_f32_16x16x32_bf16 v[52:55], v[120:123], v[16:19], v[52:55]\n"
    "s_and_b32 s73, s73, s57\n"
    "v_mfma_f32_16x16x32_bf16 v[64:67], v[124:127], v[16:19], v[64:67]\n"
    "s_mul_i32 s73, s73, s56\n"
    "v_mfma_f32_16x16x32_bf16 v[68:71], v[128:131], v[16:19], v[68:71]\n"
    "s_waitcnt lgkmcnt(1)\n"
    "s_add_u32 s73, s73, s55\n"
    "v_mfma_f32_16x16x32_bf16 v[76:79], v[132:135], v[16:19], v[76:79]\n"
    "ds_read_b128 v[16:19], v9 offset:15360\n"
    "v_mfma_f32_16x16x32_bf16 v[40:43], v[136:139], v[20:23], v[40:43]\n"
    "v_mfma_f32_16x16x32_bf16 v[44:47], v[140:143], v[20:23], v[44:47]\n"
    "s_waitcnt vmcnt(2)\n"
    "v_add_u32 v13, v11, v3\n"
    "v_mfma_f32_16x16x32_bf16 v[52:55], v[144:147], v[20:23], v[52:55]\n"
    "v_cmp_gt_i32 vcc, s71, v13\n"
    "v_mfma_f32_16x16x32_bf16 v[64:67], v[148:151], v[20:23], v[64:67]\n"
    "v_mfma_f32_16x16x32_bf16 v[68:71], v[152:155], v[20:23], v[68:71]\n"
    "s_waitcnt lgkmcnt(1)\n"
    "v_mfma_f32_16x16x32_bf16 v[76:79], v[156:159], v[20:23], v[76:79]\n"
    "ds_read_b128 v[20:23], v9 offset:16384\n"
    "s_cbranch_vccz .LgUP_dma1_84_%=\n"
    "v_mfma_f32_16x16x32_bf16 v[40:43], v[160:163], v[24:27], v[40:43]\n"
    "s_or_b64 exec, vcc, s[62:63]\n"
    "buffer_atomic_smax v11, v2, s[36:39], 0 offen sc0 sc1\n"
    "s_mov_b64 exec, -1\n"
    "v_mfma_f32_16x16x32_bf16 v[44:47], v[164:167], v[24:27], v[44:47]\n"
    "v_mfma_f32_16x16x32_bf16 v[52:55], v[168:171], v[24:27], v[52:55]\n"
    "v_mfma_f32_16x16x32_bf16 v[64:67], v[172:175], v[24:27], v[64:67]\n"
    "v_mfma_f32_16x16x32_bf16 v[68:71], v[176:179], v[24:27], v[68:71]\n"
    "s_waitcnt lgkmcnt(1)\n"
    "v_mfma_f32_16x16x32_bf16 v[76:79], v[180:183], v[24:27], v[76:79]\n"
    "ds_read_b128 v[24:27], v9 offset:17408\n"
    "v_mfma_f32_16x16x32_bf16 v[40:43], v[184:187], v[16:19], v[40:43]\n"
    "v_mfma_f32_16x16x32_bf16 v[44:47], v[188:191], v[16:19], v[44:47]\n"
    "v_mfma_f32_16x16x32_bf16 v[52:55], v[192:195], v[16:19], v[52:55]\n"
    "v_mfma_f32_16x16x32_bf16 v[64:67], v[196:199], v[16:19], v[64:67]\n"
    "v_mfma_f32_16x16x32_bf16 v[68:71], v[200:203], v[16:19], v[68:71]\n"
    "s_waitcnt lgkmcnt(1)\n"
    "v_mfma_f32_16x16x32_bf16 v[76:79], v[204:207], v[16:19], v[76:79]\n"
    "v_mfma_f32_16x16x32_bf16 v[40:43], v[208:211], v[20:23], v[40:43]\n"
    "v_mfma_f32_16x16x32_bf16 v[44:47], v[212:215], v[20:23], v[44:47]\n"
    "s_waitcnt vmcnt(2)\n"
    "v_add_u32 v13, v5, v3\n"
    "v_mfma_f32_16x16x32_bf16 v[52:55], v[216:219], v[20:23], v[52:55]\n"
    "v_cmp_gt_i32 vcc, s71, v13\n"
    "v_mfma_f32_16x16x32_bf16 v[64:67], v[220:223], v[20:23], v[64:67]\n"
    "v_mfma_f32_16x16x32_bf16 v[68:71], v[224:227], v[20:23], v[68:71]\n"
    "s_waitcnt lgkmcnt(0)\n"
    "v_mfma_f32_16x16x32_bf16 v[76:79], v[228:231], v[20:23], v[76:79]\n"
    "s_cbranch_vccz .LgUP_dma1_102_%=\n"
    "v_mfma_f32_16x16x32_bf16 v[40:43], v[232:235], v[24:27], v[40:43]\n"
    "s_or_b64 exec, vcc, s[62:63]\n"
    "buffer_atomic_smax v5, v2, s[36:39], 0 offen sc0 sc1\n"
    "s_mov_b64 exec, -1\n"
    "v_mfma_f32_16x16x32_bf16 v[44:47], v[236:239], v[24:27], v[44:47]\n"
    "v_mfma_f32_16x16x32_bf16 v[52:55], v[240:243], v[24:27], v[52:55]\n"
    "s_waitcnt vmcnt(2)\n"
    "buffer_atomic_smax v12, v14, s[36:39], s70 offen sc1\n"
    "v_mfma_f32_16x16x32_bf16 v[64:67], v[244:247], v[24:27], v[64:67]\n"
    "v_mfma_f32_16x16x32_bf16 v[68:71], v[248:251], v[24:27], v[68:71]\n"
    "v_mfma_f32_16x16x32_bf16 v[76:79], v[252:255], v[24:27], v[76:79]\n"
    "v_pk_mul_f32 v[64:65], v[40:41], v[64:65]\n"
    "v_pk_mul_f32 v[66:67], v[42:43], v[66:67]\n"
    "s_nop 1\n"
    "v_pk_mul_f32 v[68:69], v[44:45], v[68:69]\n"
    "v_pk_mul_f32 v[70:71], v[46:47], v[70:71]\n"
    "v_pk_mul_f32 v[40:41], v[40:41], s[64:65] op_sel_hi:[1,0]\n"
    "v_pk_mul_f32 v[42:43], v[42:43], s[64:65] op_sel_hi:[1,0]\n"
    "v_pk_mul_f32 v[44:45], v[44:45], s[64:65] op_sel_hi:[1,0]\n"
    "v_pk_mul_f32 v[46:47], v[46:47], s[64:65] op_sel_hi:[1,0]\n"
    "v_exp_f32 v40, v40\n"
    "v_exp_f32 v41, v41\n"
    "v_exp_f32 v42, v42\n"
    "v_exp_f32 v43, v43\n"
    "v_exp_f32 v44, v44\n"
    "v_exp_f32 v45, v45\n"
    "v_exp_f32 v46, v46\n"
    "v_exp_f32 v47, v47\n"
    "v_pk_add_f32 v[40:41], v[40:41], 1.0 op_sel_hi:[1,0]\n"
    "v_pk_add_f32 v[42:43], v[42:43], 1.0 op_sel_hi:[1,0]\n"
    "v_pk_add_f32 v[44:45], v[44:45], 1.0 op_sel_hi:[1,0]\n"
    "v_pk_add_f32 v[46:47], v[46:47], 1.0 op_sel_hi:[1,0]\n"
    "v_rcp_f32 v40, v40\n"
    "v_rcp_f32 v41, v41\n"
    "v_rcp_f32 v42, v42\n"
    "v_rcp_f32 v43, v43\n"
    "v_rcp_f32 v44, v44\n"
    "v_rcp_f32 v45, v45\n"
    "v_rcp_f32 v46, v46\n"
    "v_rcp_f32 v47, v47\n"
    "v_pk_mul_f32 v[40:41], v[40:41], v[64:65]\n"
    "v_pk_mul_f32 v[42:43], v[42:43], v[66:67]\n"
    "v_pk_mul_f32 v[44:45], v[44:45], v[68:69]\n"
    "v_pk_mul_f32 v[46:47], v[46:47], v[70:71]\n"
    "v_cvt_pk_bf16_f32 v40, v40, v41\n"
    "v_cvt_pk_bf16_f32 v41, v42, v43\n"
    "v_cvt_pk_bf16_f32 v42, v44, v45\n"
    "v_cvt_pk_bf16_f32 v43, v46, v47\n"
    "buffer_store_dwordx4 v[40:43], v15, s[36:39], s73 offen offset:1024 sc1\n"
    "v_pk_mul_f32 v[76:77], v[52:53], v[76:77]\n"
    "v_pk_mul_f32 v[78:79], v[54:55], v[78:79]\n"
    "v_pk_mul_f32 v[52:53], v[52:53], s[64:65] op_sel_hi:[1,0]\n"
    "v_pk_mul_f32 v[54:55], v[54:55], s[64:65] op_sel_hi:[1,0]\n"
    "v_exp_f32 v52, v52\n"
    "v_exp_f32 v53, v53\n"
    "v_exp_f32 v54, v54\n"
    "v_exp_f32 v55, v55\n"
    "v_pk_add_f32 v[52:53], v[52:53], 1.0 op_sel_hi:[1,0]\n"
    "v_pk_add_f32 v[54:55], v[54:55], 1.0 op_sel_hi:[1,0]\n"
    "v_rcp_f32 v52, v52\n"
    "v_rcp_f32 v53, v53\n"
    "v_rcp_f32 v54, v54\n"
    "v_rcp_f32 v55, v55\n"
    "v_pk_mul_f32 v[52:53], v[52:53], v[76:77]\n"
    "v_pk_mul_f32 v[54:55], v[54:55], v[78:79]\n"
    "v_cvt_pk_bf16_f32 v52, v52, v53\n"
    "v_cvt_pk_bf16_f32 v53, v54, v55\n"
    "s_nop 0\n"
    "v_permlane16_swap_b32 v52, v54\n"
    "v_permlane16_swap_b32 v53, v55\n"
    "buffer_store_dwordx4 v[52:55], v4, s[36:39], s73 offen offset:1024 sc1\n"
    "v_mov_b32 v12, s68\n"
    "s_mov_b32 s73, 0x80000000\n"
    "s_mov_b32 s68, s71\n"
    "s_cmp_eq_u32 s68, 128\n"
    "s_cbranch_scc1 .LgUP_done_%=\n"
    "s_waitcnt vmcnt(4)\n"
    "v_add_u32 v13, v11, v3\n"
    "v_cmp_gt_i32 vcc, s71, v13\n"
    "s_cbranch_vccnz .LgUP_idle1_%=\n"
    "s_add_u32 m0, s59, 0\n"
    "s_nop 0\n"
    "buffer_load_dwordx4 v1, s[36:39], s76 offen sc1 lds\n"
    "s_add_u32 m0, s59, 4096\n"
    "s_add_u32 s76, s76, 16384\n"
    "buffer_load_dwordx4 v1, s[36:39], s76 offen sc1 lds\n"
    "s_add_u32 m0, s59, 8192\n"
    "s_add_u32 s76, s76, 16384\n"
    "buffer_load_dwordx4 v1, s[36:39], s76 offen sc1 lds\n"
    "s_add_u32 m0, s59, 12288\n"
    "s_add_u32 s76, s76, 16384\n"
    "buffer_load_dwordx4 v1, s[36:39], s76 offen sc1 lds\n"
    "s_add_u32 m0, s59, 16384\n"
    "s_add_u32 s76, s76, 16384\n"
    "buffer_load_dwordx4 v8, s[36:39], s76 offen sc1 lds\n"
    "s_waitcnt vmcnt(5)\n"
    "buffer_atomic_smax v12, v10, s[36:39], 0 offen sc1\n"
    "s_mov_b32 s69, 0x80000000\n"
    "s_mov_b32 s70, 0x80000000\n"
    "v_mov_b32 v7, v12\n"
    "s_waitcnt vmcnt(9)\n"
    "s_add_u32 s71, s68, 1\n"
    "v_add_u32 v13, v11, v3\n"
    "v_cmp_gt_i32 vcc, s71, v13\n"
    "s_nop 0\n"
    "s_or_b64 exec, vcc, s[62:63]\n"
    "buffer_atomic_smax v11, v2, s[36:39], 0 offen sc0 sc1\n"
    "s_mov_b64 exec, -1\n"
    "s_waitcnt vmcnt(2)\n"
    "s_branch .LgUP_bar0_%=\n"
    ".LgUP_dma1_30_%=:\n"
    "v_mfma_f32_16x16x32_bf16 v[40:43], a[120:123], v[24:27], v[40:43]\n"
    "s_add_u32 m0, s59, 0\n"
    "v_mfma_f32_16x16x32_bf16 v[44:47], a[124:127], v[24:27], v[44:47]\n"
    "buffer_load_dwordx4 v1, s[36:39], s76 offen sc1 lds\n"
    "v_rcp_f32 v36, v36\n"
    "v_mfma_f32_16x16x32_bf16 v[52:55], a[128:131], v[24:27], v[52:55]\n"
    "s_add_u32 s76, s76, 16384\n"
    "v_add_f32 v37, 1.0, v37\n"
    "v_add_f32 v38, 1.0, v38\n"
    "v_mfma_f32_16x16x32_bf16 v[64:67], a[132:135], v[24:27], v[64:67]\n"
    "s_add_u32 m0, s59, 4096\n"
    "v_rcp_f32 v37, v37\n"
    "v_mfma_f32_16x16x32_bf16 v[68:71], a[136:139], v[24:27], v[68:71]\n"
    "s_waitcnt lgkmcnt(1)\n"
    "v_rcp_f32 v38, v38\n"
    "v_mfma_f32_16x16x32_bf16 v[76:79], a[140:143], v[24:27], v[76:79]\n"
    "ds_read_b128 v[24:27], v9 offset:8192\n"
    "v_add_f32 v39, 1.0, v39\n"
    "v_mfma_f32_16x16x32_bf16 v[40:43], a[144:147], v[16:19], v[40:43]\n"
    "buffer_load_dwordx4 v1, s[36:39], s76 offen sc1 lds\n"
    "v_rcp_f32 v39, v39\n"
    "v_mfma_f32_16x16x32_bf16 v[44:47], a[148:151], v[16:19], v[44:47]\n"
    "s_add_u32 s76, s76, 16384\n"
    "v_mul_f32 v32, v32, v56\n"
    "v_mul_f32 v33, v33, v57\n"
    "v_mfma_f32_16x16x32_bf16 v[52:55], a[152:155], v[16:19], v[52:55]\n"
    "s_add_u32 m0, s59, 8192\n"
    "v_mul_f32 v34, v34, v58\n"
    "v_mul_f32 v35, v35, v59\n"
    "v_mfma_f32_16x16x32_bf16 v[64:67], a[156:159], v[16:19], v[64:67]\n"
    "buffer_load_dwordx4 v1, s[36:39], s76 offen sc1 lds\n"
    "v_mul_f32 v36, v36, v60\n"
    "v_mul_f32 v37, v37, v61\n"
    "v_mfma_f32_16x16x32_bf16 v[68:71], a[160:163], v[16:19], v[68:71]\n"
    "s_waitcnt lgkmcnt(1)\n"
    "v_mul_f32 v38, v38, v62\n"
    "v_mul_f32 v39, v39, v63\n"
    "v_mfma_f32_16x16x32_bf16 v[76:79], a[164:167], v[16:19], v[76:79]\n"
    "ds_read_b128 v[16:19], v9 offset:9216\n"
    "v_cvt_pk_bf16_f32 v32, v32, v33\n"
    "v_mfma_f32_16x16x32_bf16 v[40:43], a[168:171], v[20:23], v[40:43]\n"
    "s_add_u32 s76, s76, 16384\n"
    "v_cvt_pk_bf16_f32 v33, v34, v35\n"
    "v_cvt_pk_bf16_f32 v34, v36, v37\n"
    "v_mfma_f32_16x16x32_bf16 v[44:47], a[172:175], v[20:23], v[44:47]\n"
    "s_add_u32 m0, s59, 12288\n"
    "v_cvt_pk_bf16_f32 v35, v38, v39\n"
    "v_mfma_f32_16x16x32_bf16 v[52:55], a[176:179], v[20:23], v[52:55]\n"
    "buffer_load_dwordx4 v1, s[36:39], s76 offen sc1 lds\n"
    "s_add_u32 s76, s76, 16384\n"
    "v_mul_f32 v72, v48, v72\n"
    "v_mfma_f32_16x16x32_bf16 v[64:67], a[180:183], v[20:23], v[64:67]\n"
    "buffer_store_dwordx4 v[32:35], v15, s[36:39], s73 offen sc1\n"
    "v_mfma_f32_16x16x32_bf16 v[68:71], a[184:187], v[20:23], v[68:71]\n"
    "s_waitcnt lgkmcnt(1)\n"
    "s_add_u32 m0, s59, 16384\n"
    "v_mul_f32 v73, v49, v73\n"
    "v_mul_f32 v74, v50, v74\n"
    "v_mfma_f32_16x16x32_bf16 v[76:79], a[188:191], v[20:23], v[76:79]\n"
    "ds_read_b128 v[20:23], v9 offset:10240\n"
    "buffer_load_dwordx4 v8, s[36:39], s76 offen sc1 lds\n"
    "v_mfma_f32_16x16x32_bf16 v[40:43], a[192:195], v[24:27], v[40:43]\n"
    "v_mfma_f32_16x16x32_bf16 v[44:47], a[196:199], v[24:27], v[44:47]\n"
    "v_mul_f32 v75, v51, v75\n"
    "v_mul_f32 v48, 0xbfb8aa3b, v48\n"
    "v_mfma_f32_16x16x32_bf16 v[52:55], a[200:203], v[24:27], v[52:55]\n"
    "v_exp_f32 v48, v48\n"
    "v_mfma_f32_16x16x32_bf16 v[64:67], a[204:207], v[24:27], v[64:67]\n"
    "v_mul_f32 v49, 0xbfb8aa3b, v49\n"
    "v_mul_f32 v50, 0xbfb8aa3b, v50\n"
    "v_mfma_f32_16x16x32_bf16 v[68:71], a[208:211], v[24:27], v[68:71]\n"
    "s_waitcnt lgkmcnt(1)\n"
    "v_exp_f32 v49, v49\n"
    "v_mfma_f32_16x16x32_bf16 v[76:79], a[212:215], v[24:27], v[76:79]\n"
    "ds_read_b128 v[24:27], v9 offset:11264\n"
    "v_mul_f32 v51, 0xbfb8aa3b, v51\n"
    "v_mfma_f32_16x16x32_bf16 v[40:43], a[216:219], v[16:19], v[40:43]\n"
    "v_exp_f32 v50, v50\n"
    "v_mfma_f32_16x16x32_bf16 v[44:47], a[220:223], v[16:19], v[44:47]\n"
    "v_exp_f32 v51, v51\n"
    "v_mfma_f32_16x16x32_bf16 v[52:55], a[224:227], v[16:19], v[52:55]\n"
    "v_add_f32 v48, 1.0, v48\n"
    "v_add_f32 v49, 1.0, v49\n"
    "v_mfma_f32_16x16x32_bf16 v[64:67], a[228:231], v[16:19], v[64:67]\n"
    "v_rcp_f32 v48, v48\n"
    "v_mfma_f32_16x16x32_bf16 v[68:71], a[232:235], v[16:19], v[68:71]\n"
    "s_waitcnt lgkmcnt(1)\n"
    "v_rcp_f32 v49, v49\n"
    "v_mfma_f32_16x16x32_bf16 v[76:79], a[236:239], v[16:19], v[76:79]\n"
    "ds_read_b128 v[16:19], v9 offset:12288\n"
    "v_add_f32 v50, 1.0, v50\n"
    "v_mfma_f32_16x16x32_bf16 v[40:43], a[240:243], v[20:23], v[40:43]\n"
    "v_rcp_f32 v50, v50\n"
    "v_mfma_f32_16x16x32_bf16 v[44:47], a[244:247], v[20:23], v[44:47]\n"
    "v_add_f32 v51, 1.0, v51\n"
    "v_mfma_f32_16x16x32_bf16 v[52:55], a[248:251], v[20:23], v[52:55]\n"
    "v_mul_f32 v48, v48, v72\n"
    "v_mfma_f32_16x16x32_bf16 v[64:67], a[252:255], v[20:23], v[64:67]\n"
    "v_rcp_f32 v51, v51\n"
    "v_mfma_f32_16x16x32_bf16 v[68:71], v[80:83], v[20:23], v[68:71]\n"
    "s_waitcnt lgkmcnt(1)\n"
    "v_mul_f32 v49, v49, v73\n"
    "v_mul_f32 v50, v50, v74\n"
    "v_mfma_f32_16x16x32_bf16 v[76:79], v[84:87], v[20:23], v[76:79]\n"
    "ds_read_b128 v[20:23], v9 offset:13312\n"
    "v_mfma_f32_16x16x32_bf16 v[40:43], v[88:91], v[24:27], v[40:43]\n"
    "v_mfma_f32_16x16x32_bf16 v[44:47], v[92:95], v[24:27], v[44:47]\n"
    "v_mul_f32 v51, v51, v75\n"
    "v_cvt_pk_bf16_f32 v48, v48, v49\n"
    "v_mfma_f32_16x16x32_bf16 v[52:55], v[96:99], v[24:27], v[52:55]\n"
    "v_cvt_pk_bf16_f32 v49, v50, v51\n"
    "s_nop 0\n"
    "v_mfma_f32_16x16x32_bf16 v[64:67], v[100:103], v[24:27], v[64:67]\n"
    "v_permlane16_swap_b32 v48, v50\n"
    "v_mfma_f32_16x16x32_bf16 v[68:71], v[104:107], v[24:27], v[68:71]\n"
    "s_waitcnt lgkmcnt(1)\n"
    "v_permlane16_swap_b32 v49, v51\n"
    "v_mfma_f32_16x16x32_bf16 v[76:79], v[108:111], v[24:27], v[76:79]\n"
    "ds_read_b128 v[24:27], v9 offset:14336\n"
    "s_branch .LgUP_join1_%=\n"
    ".LgUP_dma1_48_%=:\n"
    "v_mfma_f32_16x16x32_bf16 v[40:43], a[192:195], v[24:27], v[40:43]\n"
    "s_add_u32 m0, s59, 0\n"
    "v_mfma_f32_16x16x32_bf16 v[44:47], a[196:199], v[24:27], v[44:47]\n"
    "buffer_load_dwordx4 v1, s[36:39], s76 offen sc1 lds\n"
    "v_mul_f32 v75, v51, v75\n"
    "v_mul_f32 v48, 0xbfb8aa3b, v48\n"
    "v_mfma_f32_16x16x32_bf16 v[52:55], a[200:203], v[24:27], v[52:55]\n"
    "s_add_u32 s76, s76, 16384\n"
    "v_exp_f32 v48, v48\n"
    "v_mfma_f32_16x16x32_bf16 v[64:67], a[204:207], v[24:27], v[64:67]\n"
    "s_add_u32 m0, s59, 4096\n"
    "v_mul_f32 v49, 0xbfb8aa3b, v49\n"
    "v_mul_f32 v50, 0xbfb8aa3b, v50\n"
    "v_mfma_f32_16x16x32_bf16 v[68:71], a[208:211], v[24:27], v[68:71]\n"
    "s_waitcnt lgkmcnt(1)\n"
    "v_exp_f32 v49, v49\n"
    "v_mfma_f32_16x16x32_bf16 v[76:79], a[212:215], v[24:27], v[76:79]\n"
    "ds_read_b128 v[24:27], v9 offset:11264\n"
    "v_mul_f32 v51, 0xbfb8aa3b, v51\n"
    "v_mfma_f32_16x16x32_bf16 v[40:43], a[216:219], v[16:19], v[40:43]\n"
    "buffer_load_dwordx4 v1, s[36:39], s76 offen sc1 lds\n"
    "v_exp_f32 v50, v50\n"
    "v_mfma_f32_16x16x32_bf16 v[44:47], a[220:223], v[16:19], v[44:47]\n"
    "s_add_u32 s76, s76, 16384\n"
    "v_exp_f32 v51, v51\n"
    "v_mfma_f32_16x16x32_bf16 v[52:55], a[224:227], v[16:19], v[52:55]\n"
    "s_add_u32 m0, s59, 8192\n"
    "v_add_f32 v48, 1.0, v48\n"
    "v_add_f32 v49, 1.0, v49\n"
    "v_mfma_f32_16x16x32_bf16 v[64:67], a[228:231], v[16:19], v[64:67]\n"
    "buffer_load_dwordx4 v1, s[36:39], s76 offen sc1 lds\n"
    "v_rcp_f32 v48, v48\n"
    "v_mfma_f32_16x16x32_bf16 v[68:71], a[232:235], v[16:19], v[68:71]\n"
    "s_waitcnt lgkmcnt(1)\n"
    "v_rcp_f32 v49, v49\n"
    "v_mfma_f32_16x16x32_bf16 v[76:79], a[236:239], v[16:19], v[76:79]\n"
    "ds_read_b128 v[16:19], v9 offset:12288\n"
    "v_add_f32 v50, 1.0, v50\n"
    "v_mfma_f32_16x16x32_bf16 v[40:43], a[240:243], v[20:23], v[40:43]\n"
    "s_add_u32 s76, s76, 16384\n"
    "v_rcp_f32 v50, v50\n"
    "v_mfma_f32_16x16x32_bf16 v[44:47], a[244:247], v[20:23], v[44:47]\n"
    "s_add_u32 m0, s59, 12288\n"
    "v_add_f32 v51, 1.0, v51\n"
    "v_mfma_f32_16x16x32_bf16 v[52:55], a[248:251], v[20:23], v[52:55]\n"
    "buffer_load_dwordx4 v1, s[36:39], s76 offen sc1 lds\n"
    "s_add_u32 s76, s76, 16384\n"
    "v_mul_f32 v48, v48, v72\n"
    "v_mfma_f32_16x16x32_bf16 v[64:67], a[252:255], v[20:23], v[64:67]\n"
    "s_add_u32 m0, s59, 16384\n"
    "v_rcp_f32 v51, v51\n"
    "v_mfma_f32_16x16x32_bf16 v[68:71], v[80:83], v[20:23], v[68:71]\n"
    "s_waitcnt lgkmcnt(1)\n"
    "v_mul_f32 v49, v49, v73\n"
    "v_mul_f32 v50, v50, v74\n"
    "v_mfma_f32_16x16x32_bf16 v[76:79], v[84:87], v[20:23], v[76:79]\n"
    "ds_read_b128 v[20:23], v9 offset:13312\n"
    "buffer_load_dwordx4 v8, s[36:39], s76 offen sc1 lds\n"
    "v_mfma_f32_16x16x32_bf16 v[40:43], v[88:91], v[24:27], v[40:43]\n"
    "v_mfma_f32_16x16x32_bf16 v[44:47], v[92:95], v[24:27], v[44:47]\n"
    "v_mul_f32 v51, v51, v75\n"
    "v_cvt_pk_bf16_f32 v48, v48, v49\n"
    "v_mfma_f32_16x16x32_bf16 v[52:55], v[96:99], v[24:27], v[52:55]\n"
    "v_cvt_pk_bf16_f32 v49, v50, v51\n"
    "s_nop 0\n"
    "v_mfma_f32_16x16x32_bf16 v[64:67], v[100:103], v[24:27], v[64:67]\n"
    "v_permlane16_swap_b32 v48, v50\n"
    "v_mfma_f32_16x16x32_bf16 v[68:71], v[104:107], v[24:27], v[68:71]\n"
    "s_waitcnt lgkmcnt(1)\n"
    "v_permlane16_swap_b32 v49, v51\n"
    "v_mfma_f32_16x16x32_bf16 v[76:79], v[108:111], v[24:27], v[76:79]\n"
    "ds_read_b128 v[24:27], v9 offset:14336\n"
    "s_branch .LgUP_join1_%=\n"
    ".LgUP_dma1_66_%=:\n"
    "v_mfma_f32_16x16x32_bf16 v[40:43], v[88:91], v[24:27], v[40:43]\n"
    "s_add_u32 m0, s59, 0\n"
    "v_mfma_f32_16x16x32_bf16 v[44:47], v[92:95], v[24:27], v[44:47]\n"
    "buffer_load_dwordx4 v1, s[36:39], s76 offen sc1 lds\n"
    "v_mul_f32 v51, v51, v75\n"
    "v_cvt_pk_bf16_f32 v48, v48, v49\n"
    "v_mfma_f32_16x16x32_bf16 v[52:55], v[96:99], v[24:27], v[52:55]\n"
    "s_add_u32 s76, s76, 16384\n"
    "v_cvt_pk_bf16_f32 v49, v50, v51\n"
    "s_nop 0\n"
    "v_mfma_f32_16x16x32_bf16 v[64:67], v[100:103], v[24:27], v[64:67]\n"
    "s_add_u32 m0, s59, 4096\n"
    "v_permlane16_swap_b32 v48, v50\n"
    "v_mfma_f32_16x16x32_bf16 v[68:71], v[104:107], v[24:27], v[68:71]\n"
    "s_waitcnt lgkmcnt(1)\n"
    "v_permlane16_swap_b32 v49, v51\n"
    "v_mfma_f32_16x16x32_bf16 v[76:79], v[108:111], v[24:27], v[76:79]\n"
    "ds_read_b128 v[24:27], v9 offset:14336\n"
    "buffer_load_dwordx4 v1, s[36:39], s76 offen sc1 lds\n"
    "v_mfma_f32_16x16x32_bf16 v[40:43], v[112:115], v[16:19], v[40:43]\n"
    "buffer_store_dwordx4 v[48:51], v4, s[36:39], s73 offen sc1\n"
    "v_mfma_f32_16x16x32_bf16 v[44:47], v[116:119], v[16:19], v[44:47]\n"
    "s_add_u32 s76, s76, 16384\n"
    "s_add_u32 m0, s59, 8192\n"
    "s_lshr_b32 s73, s68, 1\n"
    "v_mfma_f32_16x16x32_bf16 v[52:55], v[120:123], v[16:19], v[52:55]\n"
    "buffer_load_dwordx4 v1, s[36:39], s76 offen sc1 lds\n"
    "s_and_b32 s73, s73, s57\n"
    "v_mfma_f32_16x16x32_bf16 v[64:67], v[124:127], v[16:19], v[64:67]\n"
    "s_mul_i32 s73, s73, s56\n"
    "v_mfma_f32_16x16x32_bf16 v[68:71], v[128:131], v[16:19], v[68:71]\n"
    "s_waitcnt lgkmcnt(1)\n"
    "s_add_u32 s76, s76, 16384\n"
    "s_add_u32 s73, s73, s55\n"
    "v_mfma_f32_16x16x32_bf16 v[76:79], v[132:135], v[16:19], v[76:79]\n"
    "ds_read_b128 v[16:19], v9 offset:15360\n"
    "s_add_u32 m0, s59, 12288\n"
    "v_mfma_f32_16x16x32_bf16 v[40:43], v[136:139], v[20:23], v[40:43]\n"
    "buffer_load_dwordx4 v1, s[36:39], s76 offen sc1 lds\n"
    "v_mfma_f32_16x16x32_bf16 v[44:47], v[140:143], v[20:23], v[44:47]\n"
    "v_mfma_f32_16x16x32_bf16 v[52:55], v[144:147], v[20:23], v[52:55]\n"
    "s_add_u32 s76, s76, 16384\n"
    "s_add_u32 m0, s59, 16384\n"
    "v_mfma_f32_16x16x32_bf16 v[64:67], v[148:151], v[20:23], v[64:67]\n"
    "buffer_load_dwordx4 v8, s[36:39], s76 offen sc1 lds\n"
    "v_mfma_f32_16x16x32_bf16 v[68:71], v[152:155], v[20:23], v[68:71]\n"
    "s_waitcnt lgkmcnt(1)\n"
    "v_mfma_f32_16x16x32_bf16 v[76:79], v[156:159], v[20:23], v[76:79]\n"
    "ds_read_b128 v[20:23], v9 offset:16384\n"
    "v_mfma_f32_16x16x32_bf16 v[40:43], v[160:163], v[24:27], v[40:43]\n"
    "v_mfma_f32_16x16x32_bf16 v[44:47], v[164:167], v[24:27], v[44:47]\n"
    "v_mfma_f32_16x16x32_bf16 v[52:55], v[168:171], v[24:27], v[52:55]\n"
    "v_mfma_f32_16x16x32_bf16 v[64:67], v[172:175], v[24:27], v[64:67]\n"
    "v_mfma_f32_16x16x32_bf16 v[68:71], v[176:179], v[24:27], v[68:71]\n"
    "s_waitcnt lgkmcnt(1)\n"
    "v_mfma_f32_16x16x32_bf16 v[76:79], v[180:183], v[24:27], v[76:79]\n"
    "ds_read_b128 v[24:27], v9 offset:17408\n"
    "v_mfma_f32_16x16x32_bf16 v[40:43], v[184:187], v[16:19], v[40:43]\n"
    "v_mfma_f32_16x16x32_bf16 v[44:47], v[188:191], v[16:19], v[44:47]\n"
    "v_mfma_f32_16x16x32_bf16 v[52:55], v[192:195], v[16:19], v[52:55]\n"
    "v_mfma_f32_16x16x32_bf16 v[64:67], v[196:199], v[16:19], v[64:67]\n"
    "v_mfma_f32_16x16x32_bf16 v[68:71], v[200:203], v[16:19], v[68:71]\n"
    "s_waitcnt lgkmcnt(1)\n"
    "v_mfma_f32_16x16x32_bf16 v[76:79], v[204:207], v[16:19], v[76:79]\n"
    "v_mfma_f32_16x16x32_bf16 v[40:43], v[208:211], v[20:23], v[40:43]\n"
    "v_mfma_f32_16x16x32_bf16 v[44:47], v[212:215], v[20:23], v[44:47]\n"
    "v_mfma_f32_16x16x32_bf16 v[52:55], v[216:219], v[20:23], v[52:55]\n"
    "v_mfma_f32_16x16x32_bf16 v[64:67], v[220:223], v[20:23], v[64:67]\n"
    "v_mfma_f32_16x16x32_bf16 v[68:71], v[224:227], v[20:23], v[68:71]\n"
    "s_waitcnt lgkmcnt(0)\n"
    "v_mfma_f32_16x16x32_bf16 v[76:79], v[228:231], v[20:23], v[76:79]\n"
    "v_mfma_f32_16x16x32_bf16 v[40:43], v[232:235], v[24:27], v[40:43]\n"
    "v_mfma_f32_16x16x32_bf16 v[44:47], v[236:239], v[24:27], v[44:47]\n"
    "v_mfma_f32_16x16x32_bf16 v[52:55], v[240:243], v[24:27], v[52:55]\n"
    "s_waitcnt vmcnt(3)\n"
    "buffer_atomic_smax v12, v14, s[36:39], s70 offen sc1\n"
    "v_mfma_f32_16x16x32_bf16 v[64:67], v[244:247], v[24:27], v[64:67]\n"
    "v_mfma_f32_16x16x32_bf16 v[68:71], v[248:251], v[24:27], v[68:71]\n"
    "v_mfma_f32_16x16x32_bf16 v[76:79], v[252:255], v[24:27], v[76:79]\n"
    "v_pk_mul_f32 v[64:65], v[40:41], v[64:65]\n"
    "v_pk_mul_f32 v[66:67], v[42:43], v[66:67]\n"
    "s_nop 1\n"
    "v_pk_mul_f32 v[68:69], v[44:45], v[68:69]\n"
    "v_pk_mul_f32 v[70:71], v[46:47], v[70:71]\n"
    "v_pk_mul_f32 v[40:41], v[40:41], s[64:65] op_sel_hi:[1,0]\n"
    "v_pk_mul_f32 v[42:43], v[42:43], s[64:65] op_sel_hi:[1,0]\n"
    "v_pk_mul_f32 v[44:45], v[44:45], s[64:65] op_sel_hi:[1,0]\n"
    "v_pk_mul_f32 v[46:47], v[46:47], s[64:65] op_sel_hi:[1,0]\n"
    "v_exp_f32 v40, v40\n"
    "v_exp_f32 v41, v41\n"
    "v_exp_f32 v42, v42\n"
    "v_exp_f32 v43, v43\n"
    "v_exp_f32 v44, v44\n"
    "v_exp_f32 v45, v45\n"
    "v_exp_f32 v46, v46\n"
    "v_exp_f32 v47, v47\n"
    "v_pk_add_f32 v[40:41], v[40:41], 1.0 op_sel_hi:[1,0]\n"
    "v_pk_add_f32 v[42:43], v[42:43], 1.0 op_sel_hi:[1,0]\n"
    "v_pk_add_f32 v[44:45], v[44:45], 1.0 op_sel_hi:[1,0]\n"
    "v_pk_add_f32 v[46:47], v[46:47], 1.0 op_sel_hi:[1,0]\n"
    "v_rcp_f32 v40, v40\n"
    "v_rcp_f32 v41, v41\n"
    "v_rcp_f32 v42, v42\n"
    "v_rcp_f32 v43, v43\n"
    "v_rcp_f32 v44, v44\n"
    "v_rcp_f32 v45, v45\n"
    "v_rcp_f32 v46, v46\n"
    "v_rcp_f32 v47, v47\n"
    "v_pk_mul_f32 v[40:41], v[40:41], v[64:65]\n"
    "v_pk_mul_f32 v[42:43], v[42:43], v[66:67]\n"
    "v_pk_mul_f32 v[44:45], v[44:45], v[68:69]\n"
    "v_pk_mul_f32 v[46:47], v[46:47], v[70:71]\n"
    "v_cvt_pk_bf16_f32 v40, v40, v41\n"
    "v_cvt_pk_bf16_f32 v41, v42, v43\n"
    "v_cvt_pk_bf16_f32 v42, v44, v45\n"
    "v_cvt_pk_bf16_f32 v43, v46, v47\n"
    "buffer_store_dwordx4 v[40:43], v15, s[36:39], s73 offen offset:1024 sc1\n"
    "v_pk_mul_f32 v[76:77], v[52:53], v[76:77]\n"
    "v_pk_mul_f32 v[78:79], v[54:55], v[78:79]\n"
    "v_pk_mul_f32 v[52:53], v[52:53], s[64:65] op_sel_hi:[1,0]\n"
    "v_pk_mul_f32 v[54:55], v[54:55], s[64:65] op_sel_hi:[1,0]\n"
    "v_exp_f32 v52, v52\n"
    "v_exp_f32 v53, v53\n"
    "v_exp_f32 v54, v54\n"
    "v_exp_f32 v55, v55\n"
    "v_pk_add_f32 v[52:53], v[52:53], 1.0 op_sel_hi:[1,0]\n"
    "v_pk_add_f32 v[54:55], v[54:55], 1.0 op_sel_hi:[1,0]\n"
    "v_rcp_f32 v52, v52\n"
    "v_rcp_f32 v53, v53\n"
    "v_rcp_f32 v54, v54\n"
    "v_rcp_f32 v55, v55\n"
    "v_pk_mul_f32 v[52:53], v[52:53], v[76:77]\n"
    "v_pk_mul_f32 v[54:55], v[54:55], v[78:79]\n"
    "v_cvt_pk_bf16_f32 v52, v52, v53\n"
    "v_cvt_pk_bf16_f32 v53, v54, v55\n"
    "s_nop 0\n"
    "v_permlane16_swap_b32 v52, v54\n"
    "v_permlane16_swap_b32 v53, v55\n"
    "buffer_store_dwordx4 v[52:55], v4, s[36:39], s73 offen offset:1024 sc1\n"
    "v_mov_b32 v12, s68\n"
    "s_mov_b32 s73, 0x80000000\n"
    "s_mov_b32 s68, s71\n"
    "s_mov_b32 s69, 0\n"
    "s_mov_b32 s70, 0x80000000\n"
    "v_mov_b32 v7, v12\n"
    "s_waitcnt vmcnt(9)\n"
    "s_add_u32 s71, s68, 1\n"
    "v_add_u32 v13, v11, v3\n"
    "v_cmp_gt_i32 vcc, s71, v13\n"
    "s_nop 0\n"
    "s_or_b64 exec, vcc, s[62:63]\n"
    "buffer_atomic_smax v11, v2, s[36:39], 0 offen sc0 sc1\n"
    "s_mov_b64 exec, -1\n"
    "s_waitcnt vmcnt(4)\n"
    "s_branch .LgUP_bar0_%=\n"
    ".LgUP_dma1_84_%=:\n"
    "v_mfma_f32_16x16x32_bf16 v[40:43], v[160:163], v[24:27], v[40:43]\n"
    "s_add_u32 m0, s59, 0\n"
    "v_mfma_f32_16x16x32_bf16 v[44:47], v[164:167], v[24:27], v[44:47]\n"
    "buffer_load_dwordx4 v1, s[36:39], s76 offen sc1 lds\n"
    "v_mfma_f32_16x16x32_bf16 v[52:55], v[168:171], v[24:27], v[52:55]\n"
    "s_add_u32 s76, s76, 16384\n"
    "s_add_u32 m0, s59, 4096\n"
    "v_mfma_f32_16x16x32_bf16 v[64:67], v[172:175], v[24:27], v[64:67]\n"
    "buffer_load_dwordx4 v1, s[36:39], s76 offen sc1 lds\n"
    "v_mfma_f32_16x16x32_bf16 v[68:71], v[176:179], v[24:27], v[68:71]\n"
    "s_waitcnt lgkmcnt(1)\n"
    "v_mfma_f32_16x16x32_bf16 v[76:79], v[180:183], v[24:27], v[76:79]\n"
    "ds_read_b128 v[24:27], v9 offset:17408\n"
    "v_mfma_f32_16x16x32_bf16 v[40:43], v[184:187], v[16:19], v[40:43]\n"
    "s_add_u32 s76, s76, 16384\n"
    "s_add_u32 m0, s59, 8192\n"
    "v_mfma_f32_16x16x32_bf16 v[44:47], v[188:191], v[16:19], v[44:47]\n"
    "buffer_load_dwordx4 v1, s[36:39], s76 offen sc1 lds\n"
    "v_mfma_f32_16x16x32_bf16 v[52:55], v[192:195], v[16:19], v[52:55]\n"
    "v_mfma_f32_16x16x32_bf16 v[64:67], v[196:199], v[16:19], v[64:67]\n"
    "v_mfma_f32_16x16x32_bf16 v[68:71], v[200:203], v[16:19], v[68:71]\n"
    "s_waitcnt lgkmcnt(1)\n"
    "s_add_u32 s76, s76, 16384\n"
    "s_add_u32 m0, s59, 12288\n"
    "v_mfma_f32_16x16x32_bf16 v[76:79], v[204:207], v[16:19], v[76:79]\n"
    "buffer_load_dwordx4 v1, s[36:39], s76 offen sc1 lds\n"
    "v_mfma_f32_16x16x32_bf16 v[40:43], v[208:211], v[20:23], v[40:43]\n"
    "v_mfma_f32_16x16x32_bf16 v[44:47], v[212:215], v[20:23], v[44:47]\n"
    "v_mfma_f32_16x16x32_bf16 v[52:55], v[216:219], v[20:23], v[52:55]\n"
    "s_add_u32 s76, s76, 16384\n"
    "s_add_u32 m0, s59, 16384\n"
    "v_mfma_f32_16x16x32_bf16 v[64:67], v[220:223], v[20:23], v[64:67]\n"
    "buffer_load_dwordx4 v8, s[36:39], s76 offen sc1 lds\n"
    "v_mfma_f32_16x16x32_bf16 v[68:71], v[224:227], v[20:23], v[68:71]\n"
    "s_waitcnt lgkmcnt(0)\n"
    "v_mfma_f32_16x16x32_bf16 v[76:79], v[228:231], v[20:23], v[76:79]\n"
    "v_mfma_f32_16x16x32_bf16 v[40:43], v[232:235], v[24:27], v[40:43]\n"
    "v_mfma_f32_16x16x32_bf16 v[44:47], v[236:239], v[24:27], v[44:47]\n"
    "v_mfma_f32_16x16x32_bf16 v[52:55], v[240:243], v[24:27], v[52:55]\n"
    "s_waitcnt vmcnt(5)\n"
    "buffer_atomic_smax v12, v14, s[36:39], s70 offen sc1\n"
    "v_mfma_f32_16x16x32_bf16 v[64:67], v[244:247], v[24:27], v[64:67]\n"
    "v_mfma_f32_16x16x32_bf16 v[68:71], v[248:251], v[24:27], v[68:71]\n"
    "v_mfma_f32_16x16x32_bf16 v[76:79], v[252:255], v[24:27], v[76:79]\n"
    "v_pk_mul_f32 v[64:65], v[40:41], v[64:65]\n"
    "v_pk_mul_f32 v[66:67], v[42:43], v[66:67]\n"
    "s_nop 1\n"
    "v_pk_mul_f32 v[68:69], v[44:45], v[68:69]\n"
    "v_pk_mul_f32 v[70:71], v[46:47], v[70:71]\n"
    "v_pk_mul_f32 v[40:41], v[40:41], s[64:65] op_sel_hi:[1,0]\n"
    "v_pk_mul_f32 v[42:43], v[42:43], s[64:65] op_sel_hi:[1,0]\n"
    "v_pk_mul_f32 v[44:45], v[44:45], s[64:65] op_sel_hi:[1,0]\n"
    "v_pk_mul_f32 v[46:47], v[46:47], s[64:65] op_sel_hi:[1,0]\n"
    "v_exp_f32 v40, v40\n"
    "v_exp_f32 v41, v41\n"
    "v_exp_f32 v42, v42\n"
    "v_exp_f32 v43, v43\n"
    "v_exp_f32 v44, v44\n"
    "v_exp_f32 v45, v45\n"
    "v_exp_f32 v46, v46\n"
    "v_exp_f32 v47, v47\n"
    "v_pk_add_f32 v[40:41], v[40:41], 1.0 op_sel_hi:[1,0]\n"
    "v_pk_add_f32 v[42:43], v[42:43], 1.0 op_sel_hi:[1,0]\n"
    "v_pk_add_f32 v[44:45], v[44:45], 1.0 op_sel_hi:[1,0]\n"
    "v_pk_add_f32 v[46:47], v[46:47], 1.0 op_sel_hi:[1,0]\n"
    "v_rcp_f32 v40, v40\n"
    "v_rcp_f32 v41, v41\n"
    "v_rcp_f32 v42, v42\n"
    "v_rcp_f32 v43, v43\n"
    "v_rcp_f32 v44, v44\n"
    "v_rcp_f32 v45, v45\n"
    "v_rcp_f32 v46, v46\n"
    "v_rcp_f32 v47, v47\n"
    "v_pk_mul_f32 v[40:41], v[40:41], v[64:65]\n"
    "v_pk_mul_f32 v[42:43], v[42:43], v[66:67]\n"
    "v_pk_mul_f32 v[44:45], v[44:45], v[68:69]\n"
    "v_pk_mul_f32 v[46:47], v[46:47], v[70:71]\n"
    "v_cvt_pk_bf16_f32 v40, v40, v41\n"
    "v_cvt_pk_bf16_f32 v41, v42, v43\n"
    "v_cvt_pk_bf16_f32 v42, v44, v45\n"
    "v_cvt_pk_bf16_f32 v43, v46, v47\n"
    "buffer_store_dwordx4 v[40:43], v15, s[36:39], s73 offen offset:1024 sc1\n"
    "v_pk_mul_f32 v[76:77], v[52:53], v[76:77]\n"
    "v_pk_mul_f32 v[78:79], v[54:55], v[78:79]\n"
    "v_pk_mul_f32 v[52:53], v[52:53], s[64:65] op_sel_hi:[1,0]\n"
    "v_pk_mul_f32 v[54:55], v[54:55], s[64:65] op_sel_hi:[1,0]\n"
    "v_exp_f32 v52, v52\n"
    "v_exp_f32 v53, v53\n"
    "v_exp_f32 v54, v54\n"
    "v_exp_f32 v55, v55\n"
    "v_pk_add_f32 v[52:53], v[52:53], 1.0 op_sel_hi:[1,0]\n"
    "v_pk_add_f32 v[54:55], v[54:55], 1.0 op_sel_hi:[1,0]\n"
    "v_rcp_f32 v52, v52\n"
    "v_rcp_f32 v53, v53\n"
    "v_rcp_f32 v54, v54\n"
    "v_rcp_f32 v55, v55\n"
    "v_pk_mul_f32 v[52:53], v[52:53], v[76:77]\n"
    "v_pk_mul_f32 v[54:55], v[54:55], v[78:79]\n"
    "v_cvt_pk_bf16_f32 v52, v52, v53\n"
    "v_cvt_pk_bf16_f32 v53, v54, v55\n"
    "s_nop 0\n"
    "v_permlane16_swap_b32 v52, v54\n"
    "v_permlane16_swap_b32 v53, v55\n"
    "buffer_store_dwordx4 v[52:55], v4, s[36:39], s73 offen offset:1024 sc1\n"
    "v_mov_b32 v12, s68\n"
    "s_mov_b32 s73, 0x80000000\n"
    "s_mov_b32 s68, s71\n"
    "s_mov_b32 s69, 0\n"
    "s_mov_b32 s70, 0x80000000\n"
    "v_mov_b32 v7, v12\n"
    "s_waitcnt vmcnt(9)\n"
    "s_add_u32 s71, s68, 1\n"
    "v_add_u32 v13, v11, v3\n"
    "v_cmp_gt_i32 vcc, s71, v13\n"
    "s_nop 0\n"
    "s_or_b64 exec, vcc, s[62:63]\n"
    "buffer_atomic_smax v11, v2, s[36:39], 0 offen sc0 sc1\n"
    "s_mov_b64 exec, -1\n"
    "s_waitcnt vmcnt(4)\n"
    "s_branch .LgUP_bar0_%=\n"
    ".LgUP_dma1_102_%=:\n"
    "v_mfma_f32_16x16x32_bf16 v[40:43], v[232:235], v[24:27], v[40:43]\n"
    "s_add_u32 m0, s59, 0\n"
    "v_mfma_f32_16x16x32_bf16 v[44:47], v[236:239], v[24:27], v[44:47]\n"
    "buffer_load_dwordx4 v1, s[36:39], s76 offen sc1 lds\n"
    "v_mfma_f32_16x16x32_bf16 v[52:55], v[240:243], v[24:27], v[52:55]\n"
    "s_add_u32 s76, s76, 16384\n"
    "s_waitcnt vmcnt(2)\n"
    "buffer_atomic_smax v12, v14, s[36:39], s70 offen sc1\n"
    "v_mfma_f32_16x16x32_bf16 v[64:67], v[244:247], v[24:27], v[64:67]\n"
    "s_add_u32 m0, s59, 4096\n"
    "v_mfma_f32_16x16x32_bf16 v[68:71], v[248:251], v[24:27], v[68:71]\n"
    "buffer_load_dwordx4 v1, s[36:39], s76 offen sc1 lds\n"
    "v_mfma_f32_16x16x32_bf16 v[76:79], v[252:255], v[24:27], v[76:79]\n"
    "v_pk_mul_f32 v[64:65], v[40:41], v[64:65]\n"
    "v_pk_mul_f32 v[66:67], v[42:43], v[66:67]\n"
    "s_nop 1\n"
    "v_pk_mul_f32 v[68:69], v[44:45], v[68:69]\n"
    "v_pk_mul_f32 v[70:71], v[46:47], v[70:71]\n"
    "v_pk_mul_f32 v[40:41], v[40:41], s[64:65] op_sel_hi:[1,0]\n"
    "v_pk_mul_f32 v[42:43], v[42:43], s[64:65] op_sel_hi:[1,0]\n"
    "v_pk_mul_f32 v[44:45], v[44:45], s[64:65] op_sel_hi:[1,0]\n"
    "v_pk_mul_f32 v[46:47], v[46:47], s[64:65] op_sel_hi:[1,0]\n"
    "v_exp_f32 v40, v40\n"
    "v_exp_f32 v41, v41\n"
    "v_exp_f32 v42, v42\n"
    "v_exp_f32 v43, v43\n"
    "v_exp_f32 v44, v44\n"
    "v_exp_f32 v45, v45\n"
    "v_exp_f32 v46, v46\n"
    "v_exp_f32 v47, v47\n"
    "v_pk_add_f32 v[40:41], v[40:41], 1.0 op_sel_hi:[1,0]\n"
    "v_pk_add_f32 v[42:43], v[42:43], 1.0 op_sel_hi:[1,0]\n"
    "v_pk_add_f32 v[44:45], v[44:45], 1.0 op_sel_hi:[1,0]\n"
    "v_pk_add_f32 v[46:47], v[46:47], 1.0 op_sel_hi:[1,0]\n"
    "v_rcp_f32 v40, v40\n"
    "v_rcp_f32 v41, v41\n"
    "v_rcp_f32 v42, v42\n"
    "v_rcp_f32 v43, v43\n"
    "v_rcp_f32 v44, v44\n"
    "v_rcp_f32 v45, v45\n"
    "v_rcp_f32 v46, v46\n"
    "v_rcp_f32 v47, v47\n"
    "v_pk_mul_f32 v[40:41], v[40:41], v[64:65]\n"
    "v_pk_mul_f32 v[42:43], v[42:43], v[66:67]\n"
    "v_pk_mul_f32 v[44:45], v[44:45], v[68:69]\n"
    "v_pk_mul_f32 v[46:47], v[46:47], v[70:71]\n"
    "v_cvt_pk_bf16_f32 v40, v40, v41\n"
    "v_cvt_pk_bf16_f32 v41, v42, v43\n"
    "v_cvt_pk_bf16_f32 v42, v44, v45\n"
    "v_cvt_pk_bf16_f32 v43, v46, v47\n"
    "buffer_store_dwordx4 v[40:43], v15, s[36:39], s73 offen offset:1024 sc1\n"
    "v_pk_mul_f32 v[76:77], v[52:53], v[76:77]\n"
    "v_pk_mul_f32 v[78:79], v[54:55], v[78:79]\n"
    "v_pk_mul_f32 v[52:53], v[52:53], s[64:65] op_sel_hi:[1,0]\n"
    "v_pk_mul_f32 v[54:55], v[54:55], s[64:65] op_sel_hi:[1,0]\n"
    "v_exp_f32 v52, v52\n"
    "v_exp_f32 v53, v53\n"
    "v_exp_f32 v54, v54\n"
    "v_exp_f32 v55, v55\n"
    "v_pk_add_f32 v[52:53], v[52:53], 1.0 op_sel_hi:[1,0]\n"
    "v_pk_add_f32 v[54:55], v[54:55], 1.0 op_sel_hi:[1,0]\n"
    "v_rcp_f32 v52, v52\n"
    "v_rcp_f32 v53, v53\n"
    "v_rcp_f32 v54, v54\n"
    "v_rcp_f32 v55, v55\n"
    "v_pk_mul_f32 v[52:53], v[52:53], v[76:77]\n"
    "v_pk_mul_f32 v[54:55], v[54:55], v[78:79]\n"
    "v_cvt_pk_bf16_f32 v52, v52, v53\n"
    "v_cvt_pk_bf16_f32 v53, v54, v55\n"
    "s_nop 0\n"
    "v_permlane16_swap_b32 v52, v54\n"
    "v_permlane16_swap_b32 v53, v55\n"
    "buffer_store_dwordx4 v[52:55], v4, s[36:39], s73 offen offset:1024 sc1\n"
    "v_mov_b32 v12, s68\n"
    "s_mov_b32 s73, 0x80000000\n"
    "s_mov_b32 s68, s71\n"
    "s_add_u32 m0, s59, 8192\n"
    "s_add_u32 s76, s76, 16384\n"
    "buffer_load_dwordx4 v1, s[36:39], s76 offen sc1 lds\n"
    "s_add_u32 m0, s59, 12288\n"
    "s_add_u32 s76, s76, 16384\n"
    "buffer_load_dwordx4 v1, s[36:39], s76 offen sc1 lds\n"
    "s_add_u32 m0, s59, 16384\n"
    "s_add_u32 s76, s76, 16384\n"
    "buffer_load_dwordx4 v8, s[36:39], s76 offen sc1 lds\n"
    "s_waitcnt vmcnt(3)\n"
    "buffer_atomic_smax v12, v10, s[36:39], 0 offen sc1\n"
    "s_mov_b32 s69, 0x80000000\n"
    "s_mov_b32 s70, 0x80000000\n"
    "v_mov_b32 v7, v12\n"
    "s_waitcnt vmcnt(9)\n"
    "s_add_u32 s71, s68, 1\n"
    "v_add_u32 v13, v11, v3\n"
    "v_cmp_gt_i32 vcc, s71, v13\n"
    "s_nop 0\n"
    "s_or_b64 exec, vcc, s[62:63]\n"
    "buffer_atomic_smax v11, v2, s[36:39], 0 offen sc0 sc1\n"
    "s_mov_b64 exec, -1\n"
    "s_waitcnt vmcnt(2)\n"
    "s_branch .LgUP_bar0_%=\n"
    ".LgUP_idle1_%=:\n"
    "s_or_b64 exec, vcc, s[62:63]\n"
    "buffer_atomic_smax v11, v2, s[36:39], 0 offen sc0 sc1\n"
    "s_mov_b64 exec, -1\n"
    "s_waitcnt vmcnt(1)\n"
    "buffer_atomic_smax v12, v10, s[36:39], 0 offen sc1\n"
    "s_branch .LgUP_block1_0_%=\n"
    ".LgUP_block1_%=:\n"
    "v_add_u32 v13, v11, v3\n"
    "v_cmp_gt_i32 vcc, s71, v13\n"
    "s_nop 0\n"
    "s_or_b64 exec, vcc, s[62:63]\n"
    "buffer_atomic_smax v11, v2, s[36:39], 0 offen sc0 sc1\n"
    "s_mov_b64 exec, -1\n"
    "s_sleep 3\n"
    "s_waitcnt vmcnt(1)\n"
    ".LgUP_block1_0_%=:\n"
    "v_add_u32 v13, v5, v3\n"
    "v_cmp_gt_i32 vcc, s71, v13\n"
    "s_cbranch_vccz .LgUP_go1_%=\n"
    "v_add_u32 v13, v5, v3\n"
    "v_cmp_gt_i32 vcc, s71, v13\n"
    "s_nop 0\n"
    "s_or_b64 exec, vcc, s[62:63]\n"
    "buffer_atomic_smax v5, v2, s[36:39], 0 offen sc0 sc1\n"
    "s_mov_b64 exec, -1\n"
    "s_sleep 3\n"
    "s_waitcnt vmcnt(1)\n"
    ".LgUP_block1_1_%=:\n"
    "v_add_u32 v13, v11, v3\n"
    "v_cmp_gt_i32 vcc, s71, v13\n"
    "s_cbranch_vccz .LgUP_go1_%=\n"
    "s_sub_u32 s72, s72, 1\n"
    "s_cbranch_scc0 .LgUP_block1_%=\n"
    "s_mov_b32 s72, 0\n"
    ".LgUP_go1_%=:\n"
    "s_lshr_b32 s76, s71, 1\n"
    "s_and_b32 s76, s76, s54\n"
    "s_mul_i32 s76, s76, s53\n"
    "s_add_u32 s76, s76, s52\n"
    "s_add_u32 m0, s59, 0\n"
    "s_nop 0\n"
    "buffer_load_dwordx4 v1, s[36:39], s76 offen sc1 lds\n"
    "s_add_u32 m0, s59, 4096\n"
    "s_add_u32 s76, s76, 16384\n"
    "buffer_load_dwordx4 v1, s[36:39], s76 offen sc1 lds\n"
    "s_add_u32 m0, s59, 8192\n"
    "s_add_u32 s76, s76, 16384\n"
    "buffer_load_dwordx4 v1, s[36:39], s76 offen sc1 lds\n"
    "s_add_u32 m0, s59, 12288\n"
    "s_add_u32 s76, s76, 16384\n"
    "buffer_load_dwordx4 v1, s[36:39], s76 offen sc1 lds\n"
    "s_add_u32 m0, s59, 16384\n"
    "s_add_u32 s76, s76, 16384\n"
    "buffer_load_dwordx4 v8, s[36:39], s76 offen sc1 lds\n"
    "s_mov_b32 s69, 0x80000000\n"
    "s_mov_b32 s70, 0x80000000\n"
    "v_mov_b32 v7, v12\n"
    "s_waitcnt vmcnt(5)\n"
    "s_add_u32 s71, s68, 1\n"
    "v_add_u32 v13, v11, v3\n"
    "v_cmp_gt_i32 vcc, s71, v13\n"
    "s_nop 0\n"
    "s_or_b64 exec, vcc, s[62:63]\n"
    "buffer_atomic_smax v11, v2, s[36:39], 0 offen sc0 sc1\n"
    "s_mov_b64 exec, -1\n"
    "s_waitcnt vmcnt(1)\n"
    "s_branch .LgUP_bar0_%=\n"
    ".LgUP_bar1_%=:\n"
    "s_barrier\n"
    "ds_read_b128 v[16:19], v9 offset:0\n"
    "ds_read_b128 v[20:23], v9 offset:1024\n"
    "ds_read_b128 v[24:27], v9 offset:2048\n"
    "v_add_u32 v13, v5, v3\n"
    "v_cmp_gt_i32 vcc, s71, v13\n"
    "s_nop 0\n"
    "s_or_b64 exec, vcc, s[62:63]\n"
    "buffer_atomic_smax v5, v2, s[36:39], 0 offen sc0 sc1\n"
    "s_mov_b64 exec, -1\n"
    "s_lshr_b32 s76, s71, 1\n"
    "s_and_b32 s76, s76, s54\n"
    "s_mul_i32 s76, s76, s53\n"
    "s_add_u32 s76, s76, s52\n"
    "s_waitcnt lgkmcnt(2)\n"
    "s_branch .LgUP_head1_%=\n"
    ".LgUP_FIN_%=:\n"
  );
}
DEVI void role_wo(const Params& p, int L, int j, int i, u32 dbg, unsigned long long tag) {
  const int lane = threadIdx.x & 63, w = threadIdx.x >> 6, e0 = j * 1024 * 576 + i * 192;
  GEMM_DECL;
  g_w0 = make_srd(p.Wo + e0, 4096u * 576 * 2 - e0 * 2); g_w1 = g_w0; g_w2 = g_w0;
  g_ldw = 1152;
  g_in_base = pgb(L, B_O + 8 * j); g_in_stride = SLOT; g_in_mask = RO - 1;
  g_out_base = pgb(L, B_PA + 5 * j); g_out_kb = 12 * i; g_out_mask = RP - 1;
  g_flag = lane < 4 ? mbox(L, lane, SL_REDA, 12 * j + 4 * i + w) : ((lane < 12 && w == 0) ? mbox_att(L, j, lane - 4, 4 + i) : OOB);
  g_paddr = mbox(L, j, SL_WO + i, lane);
  g_pbias = lane < 8 ? 0 : (lane < 24 ? 2 * RP : 1000);
  v16u sa, sb, va;
  gemm_args(GEMM_ARGS, p, dbg, tag, sa, sb, va);
  RUN_ASM(
    "s_nop 4\n"
    "s_getpc_b64 s[84:85]\n"
    ".LgWO_PC_%=:\n"
    "s_and_b32 s85, s85, 0xffff\n"
    "s_mov_b32 s86, .LgWO_FIN_%=-.LgWO_PC_%=\n"
    "s_mov_b32 s87, 0x20000\n"
    "v_mbcnt_lo_u32_b32 v40, -1, 0\n"
    "v_mbcnt_hi_u32_b32 v40, -1, v40\n"
    "v_lshlrev_b32 v40, 7, v40\n"
    "buffer_load_dword v41, v40, s[84:87], 0 offen\n"
    "v_add_u32 v40, 8192, v40\n"
    "buffer_load_dword v41, v40, s[84:87], 0 offen\n"
    "v_add_u32 v40, 8192, v40\n"
    "buffer_load_dword v41, v40, s[84:87], 0 offen\n"
    "s_mov_b32 s68, 0\n"
    "s_mov_b32 s71, 0\n"
    "v_add_u32 v9, 32768, v0\n"
    "s_mov_b32 s64, 0xbfb8aa3b\n"
    "s_mov_b32 s72, 262144\n"
    "s_add_u32 m0, s59, 0\n"
    "s_mov_b32 s74, 0\n"
    "buffer_load_dwordx4 v5, s[40:43], s74 offen lds\n"
    "s_add_u32 m0, s59, 4096\n"
    "s_mov_b32 s74, 128\n"
    "buffer_load_dwordx4 v5, s[40:43], s74 offen lds\n"
    "s_add_u32 m0, s59, 8192\n"
    "s_mov_b32 s74, 256\n"
    "buffer_load_dwordx4 v5, s[40:43], s74 offen lds\n"
    "s_add_u32 m0, s59, 12288\n"
    "s_mov_b32 s74, 36864\n"
    "buffer_load_dwordx4 v5, s[40:43], s74 offen lds\n"
    "s_add_u32 m0, s59, 16384\n"
    "s_mov_b32 s74, 36992\n"
    "buffer_load_dwordx4 v5, s[40:43], s74 offen lds\n"
    "s_add_u32 m0, s59, 20480\n"
    "s_mov_b32 s74, 37120\n"
    "buffer_load_dwordx4 v5, s[40:43], s74 offen lds\n"
    "s_add_u32 m0, s59, 24576\n"
    "s_mov_b32 s74, 73728\n"
    "buffer_load_dwordx4 v5, s[40:43], s74 offen lds\n"
    "s_add_u32 m0, s59, 28672\n"
    "s_mov_b32 s74, 73856\n"
    "buffer_load_dwordx4 v5, s[40:43], s74 offen lds\n"
    "s_add_u32 m0, s59, 32768\n"
    "s_mov_b32 s74, 73984\n"
    "buffer_load_dwordx4 v5, s[40:43], s74 offen lds\n"
    "s_add_u32 m0, s59, 36864\n"
    "s_mov_b32 s74, 110592\n"
    "buffer_load_dwordx4 v5, s[40:43], s74 offen lds\n"
    "s_add_u32 m0, s59, 40960\n"
    "s_mov_b32 s74, 110720\n"
    "buffer_load_dwordx4 v5, s[40:43], s74 offen lds\n"
    "s_add_u32 m0, s59, 45056\n"
    "s_mov_b32 s74, 110848\n"
    "buffer_load_dwordx4 v5, s[40:43], s74 offen lds\n"
    "s_add_u32 m0, s59, 49152\n"
    "s_mov_b32 s74, 147456\n"
    "buffer_load_dwordx4 v5, s[40:43], s74 offen lds\n"
    "s_add_u32 m0, s59, 53248\n"
    "s_mov_b32 s74, 147584\n"
    "buffer_load_dwordx4 v5, s[40:43], s74 offen lds\n"
    "s_add_u32 m0, s59, 57344\n"
    "s_mov_b32 s74, 147712\n"
    "buffer_load_dwordx4 v5, s[40:43], s74 offen lds\n"
    "s_add_u32 m0, s59, 61440\n"
    "s_mov_b32 s74, 184320\n"
    "buffer_load_dwordx4 v5, s[40:43], s74 offen lds\n"
    "s_add_u32 m0, s59, 65536\n"
    "s_mov_b32 s74, 184448\n"
    "buffer_load_dwordx4 v5, s[40:43], s74 offen lds\n"
    "s_add_u32 m0, s59, 69632\n"
    "s_mov_b32 s74, 184576\n"
    "buffer_load_dwordx4 v5, s[40:43], s74 offen lds\n"
    "s_add_u32 m0, s59, 73728\n"
    "s_mov_b32 s74, 221184\n"
    "buffer_load_dwordx4 v5, s[40:43], s74 offen lds\n"
    "s_add_u32 m0, s59, 77824\n"
    "s_mov_b32 s74, 221312\n"
    "buffer_load_dwordx4 v5, s[40:43], s74 offen lds\n"
    "s_add_u32 m0, s59, 81920\n"
    "s_mov_b32 s74, 221440\n"
    "buffer_load_dwordx4 v5, s[40:43], s74 offen lds\n"
    "s_add_u32 m0, s59, 86016\n"
    "s_mov_b32 s74, 258048\n"
    "buffer_load_dwordx4 v5, s[40:43], s74 offen lds\n"
    "s_add_u32 m0, s59, 90112\n"
    "s_mov_b32 s74, 258176\n"
    "buffer_load_dwordx4 v5, s[40:43], s74 offen lds\n"
    "s_add_u32 m0, s59, 94208\n"
    "s_mov_b32 s74, 258304\n"
    "buffer_load_dwordx4 v5, s[40:43], s74 offen lds\n"
    "s_add_u32 m0, s59, 98304\n"
    "s_mov_b32 s74, 294912\n"
    "buffer_load_dwordx4 v5, s[40:43], s74 offen lds\n"
    "s_add_u32 m0, s59, 102400\n"
    "s_mov_b32 s74, 295040\n"
    "buffer_load_dwordx4 v5, s[40:43], s74 offen lds\n"
    "s_add_u32 m0, s59, 106496\n"
    "s_mov_b32 s74, 295168\n"
    "buffer_load_dwordx4 v5, s[40:43], s74 offen lds\n"
    "s_add_u32 m0, s59, 110592\n"
    "s_mov_b32 s74, 331776\n"
    "buffer_load_dwordx4 v5, s[40:43], s74 offen lds\n"
    "s_add_u32 m0, s59, 114688\n"
    "s_mov_b32 s74, 331904\n"
    "buffer_load_dwordx4 v5, s[40:43], s74 offen lds\n"
    "s_add_u32 m0, s59, 118784\n"
    "s_mov_b32 s74, 332032\n"
    "buffer_load_dwordx4 v5, s[40:43], s74 offen lds\n"
    "v_add_u32 v16, 49152, v6\n"
    "v_add_u32 v17, 98304, v6\n"
    "v_add_u32 v18, 49152, v7\n"
    "v_add_u32 v19, 98304, v7\n"
    "s_waitcnt vmcnt(24) lgkmcnt(0)\n"
    "s_barrier\n"
    "s_add_u32 m0, s59, 122880\n"
    "s_mov_b32 s74, 368640\n"
    "buffer_load_dwordx4 v5, s[40:43], s74 offen lds\n"
    "s_add_u32 m0, s59, 126976\n"
    "s_mov_b32 s74, 368768\n"
    "buffer_load_dwordx4 v5, s[40:43], s74 offen lds\n"
    "s_add_u32 m0, s59, 131072\n"
    "s_mov_b32 s74, 368896\n"
    "buffer_load_dwordx4 v5, s[40:43], s74 offen lds\n"
    "s_add_u32 m0, s59, 135168\n"
    "s_mov_b32 s74, 405504\n"
    "buffer_load_dwordx4 v5, s[40:43], s74 offen lds\n"
    "s_add_u32 m0, s59, 139264\n"
    "s_mov_b32 s74, 405632\n"
    "buffer_load_dwordx4 v5, s[40:43], s74 offen lds\n"
    "s_add_u32 m0, s59, 143360\n"
    "s_mov_b32 s74, 405760\n"
    "buffer_load_dwordx4 v5, s[40:43], s74 offen lds\n"
    "ds_read_b64_tr_b16 a[0:1], v6 offset:0\n"
    "ds_read_b64_tr_b16 a[2:3], v6 offset:512\n"
    "ds_read_b64_tr_b16 a[4:5], v6 offset:8\n"
    "ds_read_b64_tr_b16 a[6:7], v6 offset:520\n"
    "ds_read_b64_tr_b16 a[8:9], v7 offset:0\n"
    "ds_read_b64_tr_b16 a[10:11], v7 offset:512\n"
    "ds_read_b64_tr_b16 a[12:13], v6 offset:12288\n"
    "ds_read_b64_tr_b16 a[14:15], v6 offset:12800\n"
    "ds_read_b64_tr_b16 a[16:17], v6 offset:12296\n"
    "ds_read_b64_tr_b16 a[18:19], v6 offset:12808\n"
    "ds_read_b64_tr_b16 a[20:21], v7 offset:12288\n"
    "ds_read_b64_tr_b16 a[22:23], v7 offset:12800\n"
    "s_waitcnt vmcnt(24) lgkmcnt(0)\n"
    "s_barrier\n"
    "s_add_u32 m0, s59, 0\n"
    "s_mov_b32 s74, 442368\n"
    "buffer_load_dwordx4 v5, s[40:43], s74 offen lds\n"
    "s_add_u32 m0, s59, 4096\n"
    "s_mov_b32 s74, 442496\n"
    "buffer_load_dwordx4 v5, s[40:43], s74 offen lds\n"
    "s_add_u32 m0, s59, 8192\n"
    "s_mov_b32 s74, 442624\n"
    "buffer_load_dwordx4 v5, s[40:43], s74 offen lds\n"
    "s_add_u32 m0, s59, 12288\n"
    "s_mov_b32 s74, 479232\n"
    "buffer_load_dwordx4 v5, s[40:43], s74 offen lds\n"
    "s_add_u32 m0, s59, 16384\n"
    "s_mov_b32 s74, 479360\n"
    "buffer_load_dwordx4 v5, s[40:43], s74 offen lds\n"
    "s_add_u32 m0, s59, 20480\n"
    "s_mov_b32 s74, 479488\n"
    "buffer_load_dwordx4 v5, s[40:43], s74 offen lds\n"
    "ds_read_b64_tr_b16 a[24:25], v6 offset:24576\n"
    "ds_read_b64_tr_b16 a[26:27], v6 offset:25088\n"
    "ds_read_b64_tr_b16 a[28:29], v6 offset:24584\n"
    "ds_read_b64_tr_b16 a[30:31], v6 offset:25096\n"
    "ds_read_b64_tr_b16 a[32:33], v7 offset:24576\n"
    "ds_read_b64_tr_b16 a[34:35], v7 offset:25088\n"
    "ds_read_b64_tr_b16 a[36:37], v6 offset:36864\n"
    "ds_read_b64_tr_b16 a[38:39], v6 offset:37376\n"
    "ds_read_b64_tr_b16 a[40:41], v6 offset:36872\n"
    "ds_read_b64_tr_b16 a[42:43], v6 offset:37384\n"
    "ds_read_b64_tr_b16 a[44:45], v7 offset:36864\n"
    "ds_read_b64_tr_b16 a[46:47], v7 offset:37376\n"
    "s_waitcnt vmcnt(24) lgkmcnt(0)\n"
    "s_barrier\n"
    "s_add_u32 m0, s59, 24576\n"
    "s_mov_b32 s74, 516096\n"
    "buffer_load_dwordx4 v5, s[40:43], s74 offen lds\n"
    "s_add_u32 m0, s59, 28672\n"
    "s_mov_b32 s74, 516224\n"
    "buffer_load_dwordx4 v5, s[40:43], s74 offen lds\n"
    "s_add_u32 m0, s59, 32768\n"
    "s_mov_b32 s74, 516352\n"
    "buffer_load_dwordx4 v5, s[40:43], s74 offen lds\n"
    "s_add_u32 m0, s59, 36864\n"
    "s_mov_b32 s74, 552960\n"
    "buffer_load_dwordx4 v5, s[40:43], s74 offen lds\n"
    "s_add_u32 m0, s59, 40960\n"
    "s_mov_b32 s74, 553088\n"
    "buffer_load_dwordx4 v5, s[40:43], s74 offen lds\n"
    "s_add_u32 m0, s59, 45056\n"
    "s_mov_b32 s74, 553216\n"
    "buffer_load_dwordx4 v5, s[40:43], s74 offen lds\n"
    "ds_read_b64_tr_b16 a[48:49], v16 offset:0\n"
    "ds_read_b64_tr_b16 a[50:51], v16 offset:512\n"
    "ds_read_b64_tr_b16 a[52:53], v16 offset:8\n"
    "ds_read_b64_tr_b16 a[54:55], v16 offset:520\n"
    "ds_read_b64_tr_b16 a[56:57], v18 offset:0\n"
    "ds_read_b64_tr_b16 a[58:59], v18 offset:512\n"
    "ds_read_b64_tr_b16 a[60:61], v16 offset:12288\n"
    "ds_read_b64_tr_b16 a[62:63], v16 offset:12800\n"
    "ds_read_b64_tr_b16 a[64:65], v16 offset:12296\n"
    "ds_read_b64_tr_b16 a[66:67], v16 offset:12808\n"
    "ds_read_b64_tr_b16 a[68:69], v18 offset:12288\n"
    "ds_read_b64_tr_b16 a[70:71], v18 offset:12800\n"
    "s_waitcnt vmcnt(24) lgkmcnt(0)\n"
    "s_barrier\n"
    "s_add_u32 m0, s59, 49152\n"
    "s_mov_b32 s74, 589824\n"
    "buffer_load_dwordx4 v5, s[40:43], s74 offen lds\n"
    "s_add_u32 m0, s59, 53248\n"
    "s_mov_b32 s74, 589952\n"
    "buffer_load_dwordx4 v5, s[40:43], s74 offen lds\n"
    "s_add_u32 m0, s59, 57344\n"
    "s_mov_b32 s74, 590080\n"
    "buffer_load_dwordx4 v5, s[40:43], s74 offen lds\n"
    "s_add_u32 m0, s59, 61440\n"
    "s_mov_b32 s74, 626688\n"
    "buffer_load_dwordx4 v5, s[40:43], s74 offen lds\n"
    "s_add_u32 m0, s59, 65536\n"
    "s_mov_b32 s74, 626816\n"
    "buffer_load_dwordx4 v5, s[40:43], s74 offen lds\n"
    "s_add_u32 m0, s59, 69632\n"
    "s_mov_b32 s74, 626944\n"
    "buffer_load_dwordx4 v5, s[40:43], s74 offen lds\n"
    "ds_read_b64_tr_b16 a[72:73], v16 offset:24576\n"
    "ds_read_b64_tr_b16 a[74:75], v16 offset:25088\n"
    "ds_read_b64_tr_b16 a[76:77], v16 offset:24584\n"
    "ds_read_b64_tr_b16 a[78:79], v16 offset:25096\n"
    "ds_read_b64_tr_b16 a[80:81], v18 offset:24576\n"
    "ds_read_b64_tr_b16 a[82:83], v18 offset:25088\n"
    "ds_read_b64_tr_b16 a[84:85], v16 offset:36864\n"
    "ds_read_b64_tr_b16 a[86:87], v16 offset:37376\n"
    "ds_read_b64_tr_b16 a[88:89], v16 offset:36872\n"
    "ds_read_b64_tr_b16 a[90:91], v16 offset:37384\n"
    "ds_read_b64_tr_b16 a[92:93], v18 offset:36864\n"
    "ds_read_b64_tr_b16 a[94:95], v18 offset:37376\n"
    "s_waitcnt vmcnt(24) lgkmcnt(0)\n"
    "s_barrier\n"
    "s_add_u32 m0, s59, 73728\n"
    "s_mov_b32 s74, 663552\n"
    "buffer_load_dwordx4 v5, s[40:43], s74 offen lds\n"
    "s_add_u32 m0, s59, 77824\n"
    "s_mov_b32 s74, 663680\n"
    "buffer_load_dwordx4 v5, s[40:43], s74 offen lds\n"
    "s_add_u32 m0, s59, 81920\n"
    "s_mov_b32 s74, 663808\n"
    "buffer_load_dwordx4 v5, s[40:43], s74 offen lds\n"
    "s_add_u32 m0, s59, 86016\n"
    "s_mov_b32 s74, 700416\n"
    "buffer_load_dwordx4 v5, s[40:43], s74 offen lds\n"
    "s_add_u32 m0, s59, 90112\n"
    "s_mov_b32 s74, 700544\n"
    "buffer_load_dwordx4 v5, s[40:43], s74 offen lds\n"
    "s_add_u32 m0, s59, 94208\n"
    "s_mov_b32 s74, 700672\n"
    "buffer_load_dwordx4 v5, s[40:43], s74 offen lds\n"
    "ds_read_b64_tr_b16 a[96:97], v17 offset:0\n"
    "ds_read_b64_tr_b16 a[98:99], v17 offset:512\n"
    "ds_read_b64_tr_b16 a[100:101], v17 offset:8\n"
    "ds_read_b64_tr_b16 a[102:103], v17 offset:520\n"
    "ds_read_b64_tr_b16 a[104:105], v19 offset:0\n"
    "ds_read_b64_tr_b16 a[106:107], v19 offset:512\n"
    "ds_read_b64_tr_b16 a[108:109], v17 offset:12288\n"
    "ds_read_b64_tr_b16 a[110:111], v17 offset:12800\n"
    "ds_read_b64_tr_b16 a[112:113], v17 offset:12296\n"
    "ds_read_b64_tr_b16 a[114:115], v17 offset:12808\n"
    "ds_read_b64_tr_b16 a[116:117], v19 offset:12288\n"
    "ds_read_b64_tr_b16 a[118:119], v19 offset:12800\n"
    "s_waitcnt vmcnt(24) lgkmcnt(0)\n"
    "s_barrier\n"
    "s_add_u32 m0, s59, 98304\n"
    "s_mov_b32 s74, 737280\n"
    "buffer_load_dwordx4 v5, s[40:43], s74 offen lds\n"
    "s_add_u32 m0, s59, 102400\n"
    "s_mov_b32 s74, 737408\n"
    "buffer_load_dwordx4 v5, s[40:43], s74 offen lds\n"
    "s_add_u32 m0, s59, 106496\n"
    "s_mov_b32 s74, 737536\n"
    "buffer_load_dwordx4 v5, s[40:43], s74 offen lds\n"
    "s_add_u32 m0, s59, 110592\n"
    "s_mov_b32 s74, 774144\n"
    "buffer_load_dwordx4 v5, s[40:43], s74 offen lds\n"
    "s_add_u32 m0, s59, 114688\n"
    "s_mov_b32 s74, 774272\n"
    "buffer_load_dwordx4 v5, s[40:43], s74 offen lds\n"
    "s_add_u32 m0, s59, 118784\n"
    "s_mov_b32 s74, 774400\n"
    "buffer_load_dwordx4 v5, s[40:43], s74 offen lds\n"
    "ds_read_b64_tr_b16 a[120:121], v17 offset:24576\n"
    "ds_read_b64_tr_b16 a[122:123], v17 offset:25088\n"
    "ds_read_b64_tr_b16 a[124:125], v17 offset:24584\n"
    "ds_read_b64_tr_b16 a[126:127], v17 offset:25096\n"
    "ds_read_b64_tr_b16 a[128:129], v19 offset:24576\n"
    "ds_read_b64_tr_b16 a[130:131], v19 offset:25088\n"
    "ds_read_b64_tr_b16 a[132:133], v17 offset:36864\n"
    "ds_read_b64_tr_b16 a[134:135], v17 offset:37376\n"
    "ds_read_b64_tr_b16 a[136:137], v17 offset:36872\n"
    "ds_read_b64_tr_b16 a[138:139], v17 offset:37384\n"
    "ds_read_b64_tr_b16 a[140:141], v19 offset:36864\n"
    "ds_read_b64_tr_b16 a[142:143], v19 offset:37376\n"
    "s_waitcnt vmcnt(24) lgkmcnt(0)\n"
    "s_barrier\n"
    "s_add_u32 m0, s59, 122880\n"
    "s_mov_b32 s74, 811008\n"
    "buffer_load_dwordx4 v5, s[40:43], s74 offen lds\n"
    "s_add_u32 m0, s59, 126976\n"
    "s_mov_b32 s74, 811136\n"
    "buffer_load_dwordx4 v5, s[40:43], s74 offen lds\n"
    "s_add_u32 m0, s59, 131072\n"
    "s_mov_b32 s74, 811264\n"
    "buffer_load_dwordx4 v5, s[40:43], s74 offen lds\n"
    "s_add_u32 m0, s59, 135168\n"
    "s_mov_b32 s74, 847872\n"
    "buffer_load_dwordx4 v5, s[40:43], s74 offen lds\n"
    "s_add_u32 m0, s59, 139264\n"
    "s_mov_b32 s74, 848000\n"
    "buffer_load_dwordx4 v5, s[40:43], s74 offen lds\n"
    "s_add_u32 m0, s59, 143360\n"
    "s_mov_b32 s74, 848128\n"
    "buffer_load_dwordx4 v5, s[40:43], s74 offen lds\n"
    "ds_read_b64_tr_b16 a[144:145], v6 offset:0\n"
    "ds_read_b64_tr_b16 a[146:147], v6 offset:512\n"
    "ds_read_b64_tr_b16 a[148:149], v6 offset:8\n"
    "ds_read_b64_tr_b16 a[150:151], v6 offset:520\n"
    "ds_read_b64_tr_b16 a[152:153], v7 offset:0\n"
    "ds_read_b64_tr_b16 a[154:155], v7 offset:512\n"
    "ds_read_b64_tr_b16 a[156:157], v6 offset:12288\n"
    "ds_read_b64_tr_b16 a[158:159], v6 offset:12800\n"
    "ds_read_b64_tr_b16 a[160:161], v6 offset:12296\n"
    "ds_read_b64_tr_b16 a[162:163], v6 offset:12808\n"
    "ds_read_b64_tr_b16 a[164:165], v7 offset:12288\n"
    "ds_read_b64_tr_b16 a[166:167], v7 offset:12800\n"
    "s_waitcnt vmcnt(24) lgkmcnt(0)\n"
    "s_barrier\n"
    "s_add_u32 m0, s59, 0\n"
    "s_mov_b32 s74, 884736\n"
    "buffer_load_dwordx4 v5, s[40:43], s74 offen lds\n"
    "s_add_u32 m0, s59, 4096\n"
    "s_mov_b32 s74, 884864\n"
    "buffer_load_dwordx4 v5, s[40:43], s74 offen lds\n"
    "s_add_u32 m0, s59, 8192\n"
    "s_mov_b32 s74, 884992\n"
    "buffer_load_dwordx4 v5, s[40:43], s74 offen lds\n"
    "s_add_u32 m0, s59, 12288\n"
    "s_mov_b32 s74, 921600\n"
    "buffer_load_dwordx4 v5, s[40:43], s74 offen lds\n"
    "s_add_u32 m0, s59, 16384\n"
    "s_mov_b32 s74, 921728\n"
    "buffer_load_dwordx4 v5, s[40:43], s74 offen lds\n"
    "s_add_u32 m0, s59, 20480\n"
    "s_mov_b32 s74, 921856\n"
    "buffer_load_dwordx4 v5, s[40:43], s74 offen lds\n"
    "ds_read_b64_tr_b16 a[168:169], v6 offset:24576\n"
    "ds_read_b64_tr_b16 a[170:171], v6 offset:25088\n"
    "ds_read_b64_tr_b16 a[172:173], v6 offset:24584\n"
    "ds_read_b64_tr_b16 a[174:175], v6 offset:25096\n"
    "ds_read_b64_tr_b16 a[176:177], v7 offset:24576\n"
    "ds_read_b64_tr_b16 a[178:179], v7 offset:25088\n"
    "ds_read_b64_tr_b16 a[180:181], v6 offset:36864\n"
    "ds_read_b64_tr_b16 a[182:183], v6 offset:37376\n"
    "ds_read_b64_tr_b16 a[184:185], v6 offset:36872\n"
    "ds_read_b64_tr_b16 a[186:187], v6 offset:37384\n"
    "ds_read_b64_tr_b16 a[188:189], v7 offset:36864\n"
    "ds_read_b64_tr_b16 a[190:191], v7 offset:37376\n"
    "s_waitcnt vmcnt(24) lgkmcnt(0)\n"
    "s_barrier\n"
    "s_add_u32 m0, s59, 24576\n"
    "s_mov_b32 s74, 958464\n"
    "buffer_load_dwordx4 v5, s[40:43], s74 offen lds\n"
    "s_add_u32 m0, s59, 28672\n"
    "s_mov_b32 s74, 958592\n"
    "buffer_load_dwordx4 v5, s[40:43], s74 offen lds\n"
    "s_add_u32 m0, s59, 32768\n"
    "s_mov_b32 s74, 958720\n"
    "buffer_load_dwordx4 v5, s[40:43], s74 offen lds\n"
    "s_add_u32 m0, s59, 36864\n"
    "s_mov_b32 s74, 995328\n"
    "buffer_load_dwordx4 v5, s[40:43], s74 offen lds\n"
    "s_add_u32 m0, s59, 40960\n"
    "s_mov_b32 s74, 995456\n"
    "buffer_load_dwordx4 v5, s[40:43], s74 offen lds\n"
    "s_add_u32 m0, s59, 45056\n"
    "s_mov_b32 s74, 995584\n"
    "buffer_load_dwordx4 v5, s[40:43], s74 offen lds\n"
    "ds_read_b64_tr_b16 a[192:193], v16 offset:0\n"
    "ds_read_b64_tr_b16 a[194:195], v16 offset:512\n"
    "ds_read_b64_tr_b16 a[196:197], v16 offset:8\n"
    "ds_read_b64_tr_b16 a[198:199], v16 offset:520\n"
    "ds_read_b64_tr_b16 a[200:201], v18 offset:0\n"
    "ds_read_b64_tr_b16 a[202:203], v18 offset:512\n"
    "ds_read_b64_tr_b16 a[204:205], v16 offset:12288\n"
    "ds_read_b64_tr_b16 a[206:207], v16 offset:12800\n"
    "ds_read_b64_tr_b16 a[208:209], v16 offset:12296\n"
    "ds_read_b64_tr_b16 a[210:211], v16 offset:12808\n"
    "ds_read_b64_tr_b16 a[212:213], v18 offset:12288\n"
    "ds_read_b64_tr_b16 a[214:215], v18 offset:12800\n"
    "s_waitcnt vmcnt(24) lgkmcnt(0)\n"
    "s_barrier\n"
    "s_add_u32 m0, s59, 49152\n"
    "s_mov_b32 s74, 1032192\n"
    "buffer_load_dwordx4 v5, s[40:43], s74 offen lds\n"
    "s_add_u32 m0, s59, 53248\n"
    "s_mov_b32 s74, 1032320\n"
    "buffer_load_dwordx4 v5, s[40:43], s74 offen lds\n"
    "s_add_u32 m0, s59, 57344\n"
    "s_mov_b32 s74, 1032448\n"
    "buffer_load_dwordx4 v5, s[40:43], s74 offen lds\n"
    "s_add_u32 m0, s59, 61440\n"
    "s_mov_b32 s74, 1069056\n"
    "buffer_load_dwordx4 v5, s[40:43], s74 offen lds\n"
    "s_add_u32 m0, s59, 65536\n"
    "s_mov_b32 s74, 1069184\n"
    "buffer_load_dwordx4 v5, s[40:43], s74 offen lds\n"
    "s_add_u32 m0, s59, 69632\n"
    "s_mov_b32 s74, 1069312\n"
    "buffer_load_dwordx4 v5, s[40:43], s74 offen lds\n"
    "ds_read_b64_tr_b16 a[216:217], v16 offset:24576\n"
    "ds_read_b64_tr_b16 a[218:219], v16 offset:25088\n"
    "ds_read_b64_tr_b16 a[220:221], v16 offset:24584\n"
    "ds_read_b64_tr_b16 a[222:223], v16 offset:25096\n"
    "ds_read_b64_tr_b16 a[224:225], v18 offset:24576\n"
    "ds_read_b64_tr_b16 a[226:227], v18 offset:25088\n"
    "ds_read_b64_tr_b16 a[228:229], v16 offset:36864\n"
    "ds_read_b64_tr_b16 a[230:231], v16 offset:37376\n"
    "ds_read_b64_tr_b16 a[232:233], v16 offset:36872\n"
    "ds_read_b64_tr_b16 a[234:235], v16 offset:37384\n"
    "ds_read_b64_tr_b16 a[236:237], v18 offset:36864\n"
    "ds_read_b64_tr_b16 a[238:239], v18 offset:37376\n"
    "s_waitcnt vmcnt(24) lgkmcnt(0)\n"
    "s_barrier\n"
    "s_add_u32 m0, s59, 73728\n"
    "s_mov_b32 s74, 1105920\n"
    "buffer_load_dwordx4 v5, s[40:43], s74 offen lds\n"
    "s_add_u32 m0, s59, 77824\n"
    "s_mov_b32 s74, 1106048\n"
    "buffer_load_dwordx4 v5, s[40:43], s74 offen lds\n"
    "s_add_u32 m0, s59, 81920\n"
    "s_mov_b32 s74, 1106176\n"
    "buffer_load_dwordx4 v5, s[40:43], s74 offen lds\n"
    "s_add_u32 m0, s59, 86016\n"
    "s_mov_b32 s74, 1142784\n"
    "buffer_load_dwordx4 v5, s[40:43], s74 offen lds\n"
    "s_add_u32 m0, s59, 90112\n"
    "s_mov_b32 s74, 1142912\n"
    "buffer_load_dwordx4 v5, s[40:43], s74 offen lds\n"
    "s_add_u32 m0, s59, 94208\n"
    "s_mov_b32 s74, 1143040\n"
    "buffer_load_dwordx4 v5, s[40:43], s74 offen lds\n"
    "buffer_load_dwordx2 v[12:13], off, s[36:39], 0 sc1\n"
    "ds_read_b64_tr_b16 a[240:241], v17 offset:0\n"
    "ds_read_b64_tr_b16 a[242:243], v17 offset:512\n"
    "ds_read_b64_tr_b16 a[244:245], v17 offset:8\n"
    "ds_read_b64_tr_b16 a[246:247], v17 offset:520\n"
    "ds_read_b64_tr_b16 a[248:249], v19 offset:0\n"
    "ds_read_b64_tr_b16 a[250:251], v19 offset:512\n"
    "ds_read_b64_tr_b16 a[252:253], v17 offset:12288\n"
    "ds_read_b64_tr_b16 a[254:255], v17 offset:12800\n"
    "ds_read_b64_tr_b16 v[128:129], v17 offset:12296\n"
    "ds_read_b64_tr_b16 v[130:131], v17 offset:12808\n"
    "ds_read_b64_tr_b16 v[132:133], v19 offset:12288\n"
    "ds_read_b64_tr_b16 v[134:135], v19 offset:12800\n"
    "s_waitcnt vmcnt(25) lgkmcnt(0)\n"
    "s_barrier\n"
    "ds_read_b64_tr_b16 v[136:137], v17 offset:24576\n"
    "ds_read_b64_tr_b16 v[138:139], v17 offset:25088\n"
    "ds_read_b64_tr_b16 v[140:141], v17 offset:24584\n"
    "ds_read_b64_tr_b16 v[142:143], v17 offset:25096\n"
    "ds_read_b64_tr_b16 v[144:145], v19 offset:24576\n"
    "ds_read_b64_tr_b16 v[146:147], v19 offset:25088\n"
    "ds_read_b64_tr_b16 v[148:149], v17 offset:36864\n"
    "ds_read_b64_tr_b16 v[150:151], v17 offset:37376\n"
    "ds_read_b64_tr_b16 v[152:153], v17 offset:36872\n"
    "ds_read_b64_tr_b16 v[154:155], v17 offset:37384\n"
    "ds_read_b64_tr_b16 v[156:157], v19 offset:36864\n"
    "ds_read_b64_tr_b16 v[158:159], v19 offset:37376\n"
    "s_waitcnt vmcnt(19) lgkmcnt(0)\n"
    "s_barrier\n"
    "ds_read_b64_tr_b16 v[160:161], v6 offset:0\n"
    "ds_read_b64_tr_b16 v[162:163], v6 offset:512\n"
    "ds_read_b64_tr_b16 v[164:165], v6 offset:8\n"
    "ds_read_b64_tr_b16 v[166:167], v6 offset:520\n"
    "ds_read_b64_tr_b16 v[168:169], v7 offset:0\n"
    "ds_read_b64_tr_b16 v[170:171], v7 offset:512\n"
    "ds_read_b64_tr_b16 v[172:173], v6 offset:12288\n"
    "ds_read_b64_tr_b16 v[174:175], v6 offset:12800\n"
    "ds_read_b64_tr_b16 v[176:177], v6 offset:12296\n"
    "ds_read_b64_tr_b16 v[178:179], v6 offset:12808\n"
    "ds_read_b64_tr_b16 v[180:181], v7 offset:12288\n"
    "ds_read_b64_tr_b16 v[182:183], v7 offset:12800\n"
    "s_waitcnt vmcnt(13) lgkmcnt(0)\n"
    "s_barrier\n"
    "ds_read_b64_tr_b16 v[184:185], v6 offset:24576\n"
    "ds_read_b64_tr_b16 v[186:187], v6 offset:25088\n"
    "ds_read_b64_tr_b16 v[188:189], v6 offset:24584\n"
    "ds_read_b64_tr_b16 v[190:191], v6 offset:25096\n"
    "ds_read_b64_tr_b16 v[192:193], v7 offset:24576\n"
    "ds_read_b64_tr_b16 v[194:195], v7 offset:25088\n"
    "ds_read_b64_tr_b16 v[196:197], v6 offset:36864\n"
    "ds_read_b64_tr_b16 v[198:199], v6 offset:37376\n"
    "ds_read_b64_tr_b16 v[200:201], v6 offset:36872\n"
    "ds_read_b64_tr_b16 v[202:203], v6 offset:37384\n"
    "ds_read_b64_tr_b16 v[204:205], v7 offset:36864\n"
    "ds_read_b64_tr_b16 v[206:207], v7 offset:37376\n"
    "s_waitcnt vmcnt(7) lgkmcnt(0)\n"
    "s_barrier\n"
    "ds_read_b64_tr_b16 v[208:209], v16 offset:0\n"
    "ds_read_b64_tr_b16 v[210:211], v16 offset:512\n"
    "ds_read_b64_tr_b16 v[212:213], v16 offset:8\n"
    "ds_read_b64_tr_b16 v[214:215], v16 offset:520\n"
    "ds_read_b64_tr_b16 v[216:217], v18 offset:0\n"
    "ds_read_b64_tr_b16 v[218:219], v18 offset:512\n"
    "ds_read_b64_tr_b16 v[220:221], v16 offset:12288\n"
    "ds_read_b64_tr_b16 v[222:223], v16 offset:12800\n"
    "ds_read_b64_tr_b16 v[224:225], v16 offset:12296\n"
    "ds_read_b64_tr_b16 v[226:227], v16 offset:12808\n"
    "ds_read_b64_tr_b16 v[228:229], v18 offset:12288\n"
    "ds_read_b64_tr_b16 v[230:231], v18 offset:12800\n"
    "s_waitcnt vmcnt(0) lgkmcnt(0)\n"
    "s_barrier\n"
    ".LgWO_par_%=:\n"
    "v_readfirstlane_b32 s74, v12\n"
    "v_readfirstlane_b32 s75, v13\n"
    "s_xor_b32 s74, s74, s66\n"
    "s_xor_b32 s75, s75, s67\n"
    "s_cselect_b32 s75, 2, 0\n"
    "s_or_b32 s75, s75, s74\n"
    "s_cmp_lt_u32 s75, 2\n"
    "s_cbranch_scc1 .LgWO_par1_%=\n"
    "s_sleep 4\n"
    "buffer_load_dwordx2 v[12:13], off, s[36:39], 0 sc1\n"
    "s_waitcnt vmcnt(0)\n"
    "s_sub_u32 s72, s72, 1\n"
    "s_cbranch_scc0 .LgWO_par_%=\n"
    "s_mov_b32 s72, 0\n"
    ".LgWO_par1_%=:\n"
    "s_and_b32 s74, s74, 1\n"
    "s_lshl_b32 s74, s74, 13\n"
    "s_add_u32 s36, s36, s74\n"
    "s_addc_u32 s37, s37, 0\n"
    "v_add_u32 v13, v11, v3\n"
    "v_cmp_gt_i32 vcc, s71, v13\n"
    "s_nop 0\n"
    "s_or_b64 exec, vcc, s[62:63]\n"
    "buffer_atomic_smax v11, v2, s[36:39], 0 offen sc0 sc1\n"
    "s_mov_b64 exec, -1\n"
    "s_getpc_b64 s[84:85]\n"
    ".LgWO_PC2_%=:\n"
    "s_and_b32 s85, s85, 0xffff\n"
    "s_mov_b32 s86, .LgWO_FIN_%=-.LgWO_PC2_%=\n"
    "s_mov_b32 s87, 0x20000\n"
    "v_mbcnt_lo_u32_b32 v42, -1, 0\n"
    "v_mbcnt_hi_u32_b32 v42, -1, v42\n"
    "v_lshlrev_b32 v42, 7, v42\n"
    "buffer_load_dword v43, v42, s[84:87], 0 offen\n"
    "v_add_u32 v42, 8192, v42\n"
    "buffer_load_dword v43, v42, s[84:87], 0 offen\n"
    "v_add_u32 v42, 8192, v42\n"
    "buffer_load_dword v43, v42, s[84:87], 0 offen\n"
    "ds_read_b64_tr_b16 v[232:233], v16 offset:24576\n"
    "ds_read_b64_tr_b16 v[234:235], v16 offset:25088\n"
    "ds_read_b64_tr_b16 v[236:237], v16 offset:24584\n"
    "ds_read_b64_tr_b16 v[238:239], v16 offset:25096\n"
    "ds_read_b64_tr_b16 v[240:241], v18 offset:24576\n"
    "ds_read_b64_tr_b16 v[242:243], v18 offset:25088\n"
    "ds_read_b64_tr_b16 v[244:245], v16 offset:36864\n"
    "ds_read_b64_tr_b16 v[246:247], v16 offset:37376\n"
    "ds_read_b64_tr_b16 v[248:249], v16 offset:36872\n"
    "ds_read_b64_tr_b16 v[250:251], v16 offset:37384\n"
    "ds_read_b64_tr_b16 v[252:253], v18 offset:36864\n"
    "ds_read_b64_tr_b16 v[254:255], v18 offset:37376\n"
    "s_waitcnt lgkmcnt(0)\n"
    "s_barrier\n"
    "s_waitcnt vmcnt(0)\n"
    "v_mov_b32 v5, v11\n"
    "v_mov_b32 v12, -1\n"
    "s_mov_b32 s73, 0x80000000\n"
    "s_branch .LgWO_block1_1_%=\n"
    ".LgWO_LOOPB_%=:\n"
    ".LgWO_head0_%=:\n"
    "v_mfma_f32_16x16x32_bf16 v[48:51], a[0:3], v[16:19], 0\n"
    "v_mfma_f32_16x16x32_bf16 v[52:55], a[4:7], v[16:19], 0\n"
    "s_waitcnt lgkmcnt(2)\n"
    "v_cvt_pk_bf16_f32 v56, v56, v57\n"
    "v_cvt_pk_bf16_f32 v57, v58, v59\n"
    "v_mfma_f32_16x16x32_bf16 v[64:67], a[8:11], v[16:19], 0\n"
    "ds_read_b128 v[16:19], v0 offset:4096\n"
    "v_cvt_pk_bf16_f32 v58, v60, v61\n"
    "v_mfma_f32_16x16x32_bf16 v[48:51], a[12:15], v[20:23], v[48:51]\n"
    "v_cvt_pk_bf16_f32 v59, v62, v63\n"
    "v_mfma_f32_16x16x32_bf16 v[52:55], a[16:19], v[20:23], v[52:55]\n"
    "s_waitcnt lgkmcnt(2)\n"
    "v_cvt_pk_bf16_f32 v68, v68, v69\n"
    "v_cvt_pk_bf16_f32 v69, v70, v71\n"
    "v_mfma_f32_16x16x32_bf16 v[64:67], a[20:23], v[20:23], v[64:67]\n"
    "ds_read_b128 v[20:23], v0 offset:5120\n"
    "s_nop 0\n"
    "v_mfma_f32_16x16x32_bf16 v[48:51], a[24:27], v[24:27], v[48:51]\n"
    "buffer_store_dwordx4 v[56:59], v15, s[36:39], s73 offen offset:1024 sc1\n"
    "v_mfma_f32_16x16x32_bf16 v[52:55], a[28:31], v[24:27], v[52:55]\n"
    "s_waitcnt lgkmcnt(2)\n"
    "s_waitcnt vmcnt(2)\n"
    "v_add_u32 v13, v11, v3\n"
    "v_mfma_f32_16x16x32_bf16 v[64:67], a[32:35], v[24:27], v[64:67]\n"
    "ds_read_b128 v[24:27], v0 offset:6144\n"
    "v_cmp_gt_i32 vcc, s71, v13\n"
    "v_mfma_f32_16x16x32_bf16 v[48:51], a[36:39], v[28:31], v[48:51]\n"
    "buffer_atomic_smax v7, v10, s[36:39], s69 offen sc1\n"
    "v_permlane16_swap_b32 v68, v70\n"
    "v_mfma_f32_16x16x32_bf16 v[52:55], a[40:43], v[28:31], v[52:55]\n"
    "s_waitcnt lgkmcnt(2)\n"
    "v_permlane16_swap_b32 v69, v71\n"
    "v_mfma_f32_16x16x32_bf16 v[64:67], a[44:47], v[28:31], v[64:67]\n"
    "ds_read_b128 v[28:31], v0 offset:7168\n"
    "s_cbranch_vccnz .LgWO_poll0_%=\n"
    "v_mfma_f32_16x16x32_bf16 v[48:51], a[48:51], v[16:19], v[48:51]\n"
    "s_add_u32 m0, s59, 32768\n"
    "v_mfma_f32_16x16x32_bf16 v[52:55], a[52:55], v[16:19], v[52:55]\n"
    "s_waitcnt lgkmcnt(2)\n"
    "buffer_load_dwordx4 v1, s[36:39], s76 offen sc1 lds\n"
    "v_mfma_f32_16x16x32_bf16 v[64:67], a[56:59], v[16:19], v[64:67]\n"
    "ds_read_b128 v[16:19], v0 offset:8192\n"
    "v_mfma_f32_16x16x32_bf16 v[48:51], a[60:63], v[20:23], v[48:51]\n"
    "buffer_store_dwordx4 v[68:71], v4, s[36:39], s73 offen offset:1024 sc1\n"
    "v_mfma_f32_16x16x32_bf16 v[52:55], a[64:67], v[20:23], v[52:55]\n"
    "s_waitcnt lgkmcnt(2)\n"
    "s_add_u32 s76, s76, 16384\n"
    "s_lshr_b32 s73, s68, 1\n"
    "v_mfma_f32_16x16x32_bf16 v[64:67], a[68:71], v[20:23], v[64:67]\n"
    "ds_read_b128 v[20:23], v0 offset:9216\n"
    "s_and_b32 s73, s73, s57\n"
    "v_mfma_f32_16x16x32_bf16 v[48:51], a[72:75], v[24:27], v[48:51]\n"
    "s_add_u32 m0, s59, 36864\n"
    "s_mul_i32 s73, s73, s56\n"
    "v_mfma_f32_16x16x32_bf16 v[52:55], a[76:79], v[24:27], v[52:55]\n"
    "s_waitcnt lgkmcnt(2)\n"
    "buffer_load_dwordx4 v1, s[36:39], s76 offen sc1 lds\n"
    "s_add_u32 s73, s73, s55\n"
    "v_mfma_f32_16x16x32_bf16 v[64:67], a[80:83], v[24:27], v[64:67]\n"
    "ds_read_b128 v[24:27], v0 offset:10240\n"
    "v_mfma_f32_16x16x32_bf16 v[48:51], a[84:87], v[28:31], v[48:51]\n"
    "s_add_u32 s76, s76, 16384\n"
    "s_add_u32 m0, s59, 40960\n"
    "v_mfma_f32_16x16x32_bf16 v[52:55], a[88:91], v[28:31], v[52:55]\n"
    "s_waitcnt lgkmcnt(2)\n"
    "buffer_load_dwordx4 v1, s[36:39], s76 offen sc1 lds\n"
    "v_mfma_f32_16x16x32_bf16 v[64:67], a[92:95], v[28:31], v[64:67]\n"
    "ds_read_b128 v[28:31], v0 offset:11264\n"
    "v_mfma_f32_16x16x32_bf16 v[48:51], a[96:99], v[16:19], v[48:51]\n"
    "v_mfma_f32_16x16x32_bf16 v[52:55], a[100:103], v[16:19], v[52:55]\n"
    "s_waitcnt lgkmcnt(2)\n"
    "v_mfma_f32_16x16x32_bf16 v[64:67], a[104:107], v[16:19], v[64:67]\n"
    "ds_read_b128 v[16:19], v0 offset:12288\n"
    "s_add_u32 s76, s76, 16384\n"
    "v_mfma_f32_16x16x32_bf16 v[48:51], a[108:111], v[20:23], v[48:51]\n"
    "s_add_u32 m0, s59, 45056\n"
    "v_mfma_f32_16x16x32_bf16 v[52:55], a[112:115], v[20:23], v[52:55]\n"
    "s_waitcnt lgkmcnt(2)\n"
    "buffer_load_dwordx4 v1, s[36:39], s76 offen sc1 lds\n"
    "v_mfma_f32_16x16x32_bf16 v[64:67], a[116:119], v[20:23], v[64:67]\n"
    "ds_read_b128 v[20:23], v0 offset:13312\n"
    "v_mfma_f32_16x16x32_bf16 v[48:51], a[120:123], v[24:27], v[48:51]\n"
    "v_mfma_f32_16x16x32_bf16 v[52:55], a[124:127], v[24:27], v[52:55]\n"
    "s_waitcnt lgkmcnt(2)\n"
    "v_mfma_f32_16x16x32_bf16 v[64:67], a[128:131], v[24:27], v[64:67]\n"
    "ds_read_b128 v[24:27], v0 offset:14336\n"
    "s_add_u32 s76, s76, 16384\n"
    "v_mfma_f32_16x16x32_bf16 v[48:51], a[132:135], v[28:31], v[48:51]\n"
    "s_add_u32 m0, s59, 49152\n"
    "v_mfma_f32_16x16x32_bf16 v[52:55], a[136:139], v[28:31], v[52:55]\n"
    "s_waitcnt lgkmcnt(2)\n"
    "buffer_load_dwordx4 v1, s[36:39], s76 offen sc1 lds\n"
    "v_mfma_f32_16x16x32_bf16 v[64:67], a[140:143], v[28:31], v[64:67]\n"
    "ds_read_b128 v[28:31], v0 offset:15360\n"
    "v_mfma_f32_16x16x32_bf16 v[48:51], a[144:147], v[16:19], v[48:51]\n"
    "v_mfma_f32_16x16x32_bf16 v[52:55], a[148:151], v[16:19], v[52:55]\n"
    "s_waitcnt lgkmcnt(2)\n"
    "v_mfma_f32_16x16x32_bf16 v[64:67], a[152:155], v[16:19], v[64:67]\n"
    "ds_read_b128 v[16:19], v0 offset:16384\n"
    "s_add_u32 s76, s76, 16384\n"
    "v_mfma_f32_16x16x32_bf16 v[48:51], a[156:159], v[20:23], v[48:51]\n"
    "s_add_u32 m0, s59, 53248\n"
    "v_mfma_f32_16x16x32_bf16 v[52:55], a[160:163], v[20:23], v[52:55]\n"
    "s_waitcnt lgkmcnt(2)\n"
    "buffer_load_dwordx4 v1, s[36:39], s76 offen sc1 lds\n"
    "v_mfma_f32_16x16x32_bf16 v[64:67], a[164:167], v[20:23], v[64:67]\n"
    "ds_read_b128 v[20:23], v0 offset:17408\n"
    "v_mfma_f32_16x16x32_bf16 v[48:51], a[168:171], v[24:27], v[48:51]\n"
    "v_mfma_f32_16x16x32_bf16 v[52:55], a[172:175], v[24:27], v[52:55]\n"
    "s_waitcnt lgkmcnt(2)\n"
    "s_add_u32 s76, s76, 16384\n"
    "s_add_u32 m0, s59, 57344\n"
    "v_mfma_f32_16x16x32_bf16 v[64:67], a[176:179], v[24:27], v[64:67]\n"
    "ds_read_b128 v[24:27], v0 offset:18432\n"
    "buffer_load_dwordx4 v1, s[36:39], s76 offen sc1 lds\n"
    "v_mfma_f32_16x16x32_bf16 v[48:51], a[180:183], v[28:31], v[48:51]\n"
    "v_mfma_f32_16x16x32_bf16 v[52:55], a[184:187], v[28:31], v[52:55]\n"
    "s_waitcnt lgkmcnt(2)\n"
    "v_mfma_f32_16x16x32_bf16 v[64:67], a[188:191], v[28:31], v[64:67]\n"
    "ds_read_b128 v[28:31], v0 offset:19456\n"
    "v_mfma_f32_16x16x32_bf16 v[48:51], a[192:195], v[16:19], v[48:51]\n"
    "v_mfma_f32_16x16x32_bf16 v[52:55], a[196:199], v[16:19], v[52:55]\n"
    "s_waitcnt lgkmcnt(2)\n"
    "s_add_u32 s76, s76, 16384\n"
    "s_add_u32 m0, s59, 61440\n"
    "v_mfma_f32_16x16x32_bf16 v[64:67], a[200:203], v[16:19], v[64:67]\n"
    "ds_read_b128 v[16:19], v0 offset:20480\n"
    "buffer_load_dwordx4 v1, s[36:39], s76 offen sc1 lds\n"
    "v_mfma_f32_16x16x32_bf16 v[48:51], a[204:207], v[20:23], v[48:51]\n"
    "v_mfma_f32_16x16x32_bf16 v[52:55], a[208:211], v[20:23], v[52:55]\n"
    "s_waitcnt lgkmcnt(2)\n"
    "v_mfma_f32_16x16x32_bf16 v[64:67], a[212:215], v[20:23], v[64:67]\n"
    "ds_read_b128 v[20:23], v0 offset:21504\n"
    "v_mfma_f32_16x16x32_bf16 v[48:51], a[216:219], v[24:27], v[48:51]\n"
    "v_mfma_f32_16x16x32_bf16 v[52:55], a[220:223], v[24:27], v[52:55]\n"
    "s_waitcnt lgkmcnt(2)\n"
    "s_waitcnt vmcnt(7)\n"
    "buffer_atomic_smax v12, v10, s[36:39], s70 offen sc1\n"
    "v_mfma_f32_16x16x32_bf16 v[64:67], a[224:227], v[24:27], v[64:67]\n"
    "ds_read_b128 v[24:27], v0 offset:22528\n"
    "v_mfma_f32_16x16x32_bf16 v[48:51], a[228:231], v[28:31], v[48:51]\n"
    "v_mfma_f32_16x16x32_bf16 v[52:55], a[232:235], v[28:31], v[52:55]\n"
    "s_waitcnt lgkmcnt(2)\n"
    "v_mfma_f32_16x16x32_bf16 v[64:67], a[236:239], v[28:31], v[64:67]\n"
    "ds_read_b128 v[28:31], v0 offset:23552\n"
    ".LgWO_join0_%=:\n"
    "v_mfma_f32_16x16x32_bf16 v[48:51], a[240:243], v[16:19], v[48:51]\n"
    "s_add_u32 s71, s68, 2\n"
    "s_waitcnt vmcnt(8)\n"
    "v_mfma_f32_16x16x32_bf16 v[52:55], a[244:247], v[16:19], v[52:55]\n"
    "s_waitcnt lgkmcnt(2)\n"
    "v_add_u32 v13, v11, v3\n"
    "v_mfma_f32_16x16x32_bf16 v[64:67], a[248:251], v[16:19], v[64:67]\n"
    "ds_read_b128 v[16:19], v0 offset:24576\n"
    "v_cmp_gt_i32 vcc, s71, v13\n"
    "v_mfma_f32_16x16x32_bf16 v[48:51], a[252:255], v[20:23], v[48:51]\n"
    "v_mfma_f32_16x16x32_bf16 v[52:55], v[128:131], v[20:23], v[52:55]\n"
    "s_waitcnt lgkmcnt(2)\n"
    "v_mfma_f32_16x16x32_bf16 v[64:67], v[132:135], v[20:23], v[64:67]\n"
    "ds_read_b128 v[20:23], v0 offset:25600\n"
    "v_mfma_f32_16x16x32_bf16 v[48:51], v[136:139], v[24:27], v[48:51]\n"
    "s_or_b64 exec, vcc, s[62:63]\n"
    "buffer_atomic_smax v11, v2, s[36:39], 0 offen sc0 sc1\n"
    "s_mov_b64 exec, -1\n"
    "v_mfma_f32_16x16x32_bf16 v[52:55], v[140:143], v[24:27], v[52:55]\n"
    "s_waitcnt lgkmcnt(2)\n"
    "s_lshr_b32 s76, s71, 1\n"
    "v_mfma_f32_16x16x32_bf16 v[64:67], v[144:147], v[24:27], v[64:67]\n"
    "ds_read_b128 v[24:27], v0 offset:26624\n"
    "s_and_b32 s76, s76, s54\n"
    "v_mfma_f32_16x16x32_bf16 v[48:51], v[148:151], v[28:31], v[48:51]\n"
    "s_mul_i32 s76, s76, s53\n"
    "v_mfma_f32_16x16x32_bf16 v[52:55], v[152:155], v[28:31], v[52:55]\n"
    "s_waitcnt lgkmcnt(2)\n"
    "s_add_u32 s76, s76, s52\n"
    "v_mfma_f32_16x16x32_bf16 v[64:67], v[156:159], v[28:31], v[64:67]\n"
    "ds_read_b128 v[28:31], v0 offset:27648\n"
    "v_mfma_f32_16x16x32_bf16 v[48:51], v[160:163], v[16:19], v[48:51]\n"
    "v_mfma_f32_16x16x32_bf16 v[52:55], v[164:167], v[16:19], v[52:55]\n"
    "s_waitcnt lgkmcnt(2)\n"
    "v_mfma_f32_16x16x32_bf16 v[64:67], v[168:171], v[16:19], v[64:67]\n"
    "ds_read_b128 v[16:19], v0 offset:28672\n"
    "v_mfma_f32_16x16x32_bf16 v[48:51], v[172:175], v[20:23], v[48:51]\n"
    "v_mfma_f32_16x16x32_bf16 v[52:55], v[176:179], v[20:23], v[52:55]\n"
    "s_waitcnt lgkmcnt(2)\n"
    "v_mfma_f32_16x16x32_bf16 v[64:67], v[180:183], v[20:23], v[64:67]\n"
    "ds_read_b128 v[20:23], v0 offset:29696\n"
    "v_mfma_f32_16x16x32_bf16 v[48:51], v[184:187], v[24:27], v[48:51]\n"
    "v_mfma_f32_16x16x32_bf16 v[52:55], v[188:191], v[24:27], v[52:55]\n"
    "s_waitcnt lgkmcnt(2)\n"
    "v_mfma_f32_16x16x32_bf16 v[64:67], v[192:195], v[24:27], v[64:67]\n"
    "ds_read_b128 v[24:27], v0 offset:30720\n"
    "v_mfma_f32_16x16x32_bf16 v[48:51], v[196:199], v[28:31], v[48:51]\n"
    "v_mfma_f32_16x16x32_bf16 v[52:55], v[200:203], v[28:31], v[52:55]\n"
    "s_waitcnt lgkmcnt(2)\n"
    "v_add_u32 v13, v5, v3\n"
    "v_mfma_f32_16x16x32_bf16 v[64:67], v[204:207], v[28:31], v[64:67]\n"
    "ds_read_b128 v[28:31], v0 offset:31744\n"
    "v_cmp_gt_i32 vcc, s71, v13\n"
    "v_mfma_f32_16x16x32_bf16 v[48:51], v[208:211], v[16:19], v[48:51]\n"
    "v_mfma_f32_16x16x32_bf16 v[52:55], v[212:215], v[16:19], v[52:55]\n"
    "s_waitcnt lgkmcnt(2)\n"
    "v_mfma_f32_16x16x32_bf16 v[64:67], v[216:219], v[16:19], v[64:67]\n"
    "v_mfma_f32_16x16x32_bf16 v[48:51], v[220:223], v[20:23], v[48:51]\n"
    "s_waitcnt vmcnt(1) lgkmcnt(0)\n"
    "s_barrier\n"
    "ds_read_b128 v[16:19], v9 offset:0\n"
    "s_or_b64 exec, vcc, s[62:63]\n"
    "buffer_atomic_smax v5, v2, s[36:39], 0 offen sc0 sc1\n"
    "s_mov_b64 exec, -1\n"
    "v_mfma_f32_16x16x32_bf16 v[52:55], v[224:227], v[20:23], v[52:55]\n"
    "v_mfma_f32_16x16x32_bf16 v[64:67], v[228:231], v[20:23], v[64:67]\n"
    "ds_read_b128 v[20:23], v9 offset:1024\n"
    "v_mfma_f32_16x16x32_bf16 v[48:51], v[232:235], v[24:27], v[48:51]\n"
    "v_mfma_f32_16x16x32_bf16 v[52:55], v[236:239], v[24:27], v[52:55]\n"
    "v_mfma_f32_16x16x32_bf16 v[64:67], v[240:243], v[24:27], v[64:67]\n"
    "ds_read_b128 v[24:27], v9 offset:2048\n"
    "v_mfma_f32_16x16x32_bf16 v[48:51], v[244:247], v[28:31], v[48:51]\n"
    "s_mov_b32 s69, 0x80000000\n"
    "s_mov_b32 s70, 0\n"
    "v_mov_b32 v12, s68\n"
    "v_mfma_f32_16x16x32_bf16 v[52:55], v[248:251], v[28:31], v[52:55]\n"
    "s_waitcnt lgkmcnt(2)\n"
    "s_add_u32 s68, s68, 1\n"
    "v_mfma_f32_16x16x32_bf16 v[64:67], v[252:255], v[28:31], v[64:67]\n"
    "ds_read_b128 v[28:31], v9 offset:3072\n"
    ".LgWO_head1_%=:\n"
    "v_mfma_f32_16x16x32_bf16 v[56:59], a[0:3], v[16:19], 0\n"
    "v_mfma_f32_16x16x32_bf16 v[60:63], a[4:7], v[16:19], 0\n"
    "s_waitcnt lgkmcnt(2)\n"
    "v_cvt_pk_bf16_f32 v48, v48, v49\n"
    "v_cvt_pk_bf16_f32 v49, v50, v51\n"
    "v_mfma_f32_16x16x32_bf16 v[68:71], a[8:11], v[16:19], 0\n"
    "ds_read_b128 v[16:19], v9 offset:4096\n"
    "v_cvt_pk_bf16_f32 v50, v52, v53\n"
    "v_mfma_f32_16x16x32_bf16 v[56:59], a[12:15], v[20:23], v[56:59]\n"
    "v_cvt_pk_bf16_f32 v51, v54, v55\n"
    "v_mfma_f32_16x16x32_bf16 v[60:63], a[16:19], v[20:23], v[60:63]\n"
    "s_waitcnt lgkmcnt(2)\n"
    "v_cvt_pk_bf16_f32 v64, v64, v65\n"
    "v_cvt_pk_bf16_f32 v65, v66, v67\n"
    "v_mfma_f32_16x16x32_bf16 v[68:71], a[20:23], v[20:23], v[68:71]\n"
    "ds_read_b128 v[20:23], v9 offset:5120\n"
    "s_nop 0\n"
    "v_mfma_f32_16x16x32_bf16 v[56:59], a[24:27], v[24:27], v[56:59]\n"
    "buffer_store_dwordx4 v[48:51], v15, s[36:39], s73 offen sc1\n"
    "v_mfma_f32_16x16x32_bf16 v[60:63], a[28:31], v[24:27], v[60:63]\n"
    "s_waitcnt lgkmcnt(2)\n"
    "s_waitcnt vmcnt(2)\n"
    "v_add_u32 v13, v11, v3\n"
    "v_mfma_f32_16x16x32_bf16 v[68:71], a[32:35], v[24:27], v[68:71]\n"
    "ds_read_b128 v[24:27], v9 offset:6144\n"
    "v_cmp_gt_i32 vcc, s71, v13\n"
    "v_mfma_f32_16x16x32_bf16 v[56:59], a[36:39], v[28:31], v[56:59]\n"
    "buffer_atomic_smax v7, v14, s[36:39], s69 offen sc1\n"
    "v_permlane16_swap_b32 v64, v66\n"
    "v_mfma_f32_16x16x32_bf16 v[60:63], a[40:43], v[28:31], v[60:63]\n"
    "s_waitcnt lgkmcnt(2)\n"
    "v_permlane16_swap_b32 v65, v67\n"
    "v_mfma_f32_16x16x32_bf16 v[68:71], a[44:47], v[28:31], v[68:71]\n"
    "ds_read_b128 v[28:31], v9 offset:7168\n"
    "s_cbranch_vccnz .LgWO_poll1_%=\n"
    "v_mfma_f32_16x16x32_bf16 v[56:59], a[48:51], v[16:19], v[56:59]\n"
    "s_add_u32 m0, s59, 0\n"
    "v_mfma_f32_16x16x32_bf16 v[60:63], a[52:55], v[16:19], v[60:63]\n"
    "s_waitcnt lgkmcnt(2)\n"
    "buffer_load_dwordx4 v1, s[36:39], s76 offen sc1 lds\n"
    "v_mfma_f32_16x16x32_bf16 v[68:71], a[56:59], v[16:19], v[68:71]\n"
    "ds_read_b128 v[16:19], v9 offset:8192\n"
    "v_mfma_f32_16x16x32_bf16 v[56:59], a[60:63], v[20:23], v[56:59]\n"
    "buffer_store_dwordx4 v[64:67], v4, s[36:39], s73 offen sc1\n"
    "v_mfma_f32_16x16x32_bf16 v[60:63], a[64:67], v[20:23], v[60:63]\n"
    "s_waitcnt lgkmcnt(2)\n"
    "s_add_u32 s76, s76, 16384\n"
    "s_lshr_b32 s73, s68, 1\n"
    "v_mfma_f32_16x16x32_bf16 v[68:71], a[68:71], v[20:23], v[68:71]\n"
    "ds_read_b128 v[20:23], v9 offset:9216\n"
    "s_and_b32 s73, s73, s57\n"
    "v_mfma_f32_16x16x32_bf16 v[56:59], a[72:75], v[24:27], v[56:59]\n"
    "s_add_u32 m0, s59, 4096\n"
    "s_mul_i32 s73, s73, s56\n"
    "v_mfma_f32_16x16x32_bf16 v[60:63], a[76:79], v[24:27], v[60:63]\n"
    "s_waitcnt lgkmcnt(2)\n"
    "buffer_load_dwordx4 v1, s[36:39], s76 offen sc1 lds\n"
    "s_add_u32 s73, s73, s55\n"
    "v_mfma_f32_16x16x32_bf16 v[68:71], a[80:83], v[24:27], v[68:71]\n"
    "ds_read_b128 v[24:27], v9 offset:10240\n"
    "v_mfma_f32_16x16x32_bf16 v[56:59], a[84:87], v[28:31], v[56:59]\n"
    "s_add_u32 s76, s76, 16384\n"
    "s_add_u32 m0, s59, 8192\n"
    "v_mfma_f32_16x16x32_bf16 v[60:63], a[88:91], v[28:31], v[60:63]\n"
    "s_waitcnt lgkmcnt(2)\n"
    "buffer_load_dwordx4 v1, s[36:39], s76 offen sc1 lds\n"
    "v_mfma_f32_16x16x32_bf16 v[68:71], a[92:95], v[28:31], v[68:71]\n"
    "ds_read_b128 v[28:31], v9 offset:11264\n"
    "v_mfma_f32_16x16x32_bf16 v[56:59], a[96:99], v[16:19], v[56:59]\n"
    "v_mfma_f32_16x16x32_bf16 v[60:63], a[100:103], v[16:19], v[60:63]\n"
    "s_waitcnt lgkmcnt(2)\n"
    "v_mfma_f32_16x16x32_bf16 v[68:71], a[104:107], v[16:19], v[68:71]\n"
    "ds_read_b128 v[16:19], v9 offset:12288\n"
    "s_add_u32 s76, s76, 16384\n"
    "v_mfma_f32_16x16x32_bf16 v[56:59], a[108:111], v[20:23], v[56:59]\n"
    "s_add_u32 m0, s59, 12288\n"
    "v_mfma_f32_16x16x32_bf16 v[60:63], a[112:115], v[20:23], v[60:63]\n"
    "s_waitcnt lgkmcnt(2)\n"
    "buffer_load_dwordx4 v1, s[36:39], s76 offen sc1 lds\n"
    "v_mfma_f32_16x16x32_bf16 v[68:71], a[116:119], v[20:23], v[68:71]\n"
    "ds_read_b128 v[20:23], v9 offset:13312\n"
    "v_mfma_f32_16x16x32_bf16 v[56:59], a[120:123], v[24:27], v[56:59]\n"
    "v_mfma_f32_16x16x32_bf16 v[60:63], a[124:127], v[24:27], v[60:63]\n"
    "s_waitcnt lgkmcnt(2)\n"
    "v_mfma_f32_16x16x32_bf16 v[68:71], a[128:131], v[24:27], v[68:71]\n"
    "ds_read_b128 v[24:27], v9 offset:14336\n"
    "s_add_u32 s76, s76, 16384\n"
    "v_mfma_f32_16x16x32_bf16 v[56:59], a[132:135], v[28:31], v[56:59]\n"
    "s_add_u32 m0, s59, 16384\n"
    "v_mfma_f32_16x16x32_bf16 v[60:63], a[136:139], v[28:31], v[60:63]\n"
    "s_waitcnt lgkmcnt(2)\n"
    "buffer_load_dwordx4 v1, s[36:39], s76 offen sc1 lds\n"
    "v_mfma_f32_16x16x32_bf16 v[68:71], a[140:143], v[28:31], v[68:71]\n"
    "ds_read_b128 v[28:31], v9 offset:15360\n"
    "v_mfma_f32_16x16x32_bf16 v[56:59], a[144:147], v[16:19], v[56:59]\n"
    "v_mfma_f32_16x16x32_bf16 v[60:63], a[148:151], v[16:19], v[60:63]\n"
    "s_waitcnt lgkmcnt(2)\n"
    "v_mfma_f32_16x16x32_bf16 v[68:71], a[152:155], v[16:19], v[68:71]\n"
    "ds_read_b128 v[16:19], v9 offset:16384\n"
    "s_add_u32 s76, s76, 16384\n"
    "v_mfma_f32_16x16x32_bf16 v[56:59], a[156:159], v[20:23], v[56:59]\n"
    "s_add_u32 m0, s59, 20480\n"
    "v_mfma_f32_16x16x32_bf16 v[60:63], a[160:163], v[20:23], v[60:63]\n"
    "s_waitcnt lgkmcnt(2)\n"
    "buffer_load_dwordx4 v1, s[36:39], s76 offen sc1 lds\n"
    "v_mfma_f32_16x16x32_bf16 v[68:71], a[164:167], v[20:23], v[68:71]\n"
    "ds_read_b128 v[20:23], v9 offset:17408\n"
    "v_mfma_f32_16x16x32_bf16 v[56:59], a[168:171], v[24:27], v[56:59]\n"
    "v_mfma_f32_16x16x32_bf16 v[60:63], a[172:175], v[24:27], v[60:63]\n"
    "s_waitcnt lgkmcnt(2)\n"
    "s_add_u32 s76, s76, 16384\n"
    "s_add_u32 m0, s59, 24576\n"
    "v_mfma_f32_16x16x32_bf16 v[68:71], a[176:179], v[24:27], v[68:71]\n"
    "ds_read_b128 v[24:27], v9 offset:18432\n"
    "buffer_load_dwordx4 v1, s[36:39], s76 offen sc1 lds\n"
    "v_mfma_f32_16x16x32_bf16 v[56:59], a[180:183], v[28:31], v[56:59]\n"
    "v_mfma_f32_16x16x32_bf16 v[60:63], a[184:187], v[28:31], v[60:63]\n"
    "s_waitcnt lgkmcnt(2)\n"
    "v_mfma_f32_16x16x32_bf16 v[68:71], a[188:191], v[28:31], v[68:71]\n"
    "ds_read_b128 v[28:31], v9 offset:19456\n"
    "v_mfma_f32_16x16x32_bf16 v[56:59], a[192:195], v[16:19], v[56:59]\n"
    "v_mfma_f32_16x16x32_bf16 v[60:63], a[196:199], v[16:19], v[60:63]\n"
    "s_waitcnt lgkmcnt(2)\n"
    "s_add_u32 s76, s76, 16384\n"
    "s_add_u32 m0, s59, 28672\n"
    "v_mfma_f32_16x16x32_bf16 v[68:71], a[200:203], v[16:19], v[68:71]\n"
    "ds_read_b128 v[16:19], v9 offset:20480\n"
    "buffer_load_dwordx4 v1, s[36:39], s76 offen sc1 lds\n"
    "v_mfma_f32_16x16x32_bf16 v[56:59], a[204:207], v[20:23], v[56:59]\n"
    "v_mfma_f32_16x16x32_bf16 v[60:63], a[208:211], v[20:23], v[60:63]\n"
    "s_waitcnt lgkmcnt(2)\n"
    "v_mfma_f32_16x16x32_bf16 v[68:71], a[212:215], v[20:23], v[68:71]\n"
    "ds_read_b128 v[20:23], v9 offset:21504\n"
    "v_mfma_f32_16x16x32_bf16 v[56:59], a[216:219], v[24:27], v[56:59]\n"
    "v_mfma_f32_16x16x32_bf16 v[60:63], a[220:223], v[24:27], v[60:63]\n"
    "s_waitcnt lgkmcnt(2)\n"
    "s_waitcnt vmcnt(7)\n"
    "buffer_atomic_smax v12, v14, s[36:39], s70 offen sc1\n"
    "v_mfma_f32_16x16x32_bf16 v[68:71], a[224:227], v[24:27], v[68:71]\n"
    "ds_read_b128 v[24:27], v9 offset:22528\n"
    "v_mfma_f32_16x16x32_bf16 v[56:59], a[228:231], v[28:31], v[56:59]\n"
    "v_mfma_f32_16x16x32_bf16 v[60:63], a[232:235], v[28:31], v[60:63]\n"
    "s_waitcnt lgkmcnt(2)\n"
    "v_mfma_f32_16x16x32_bf16 v[68:71], a[236:239], v[28:31], v[68:71]\n"
    "ds_read_b128 v[28:31], v9 offset:23552\n"
    ".LgWO_join1_%=:\n"
    "v_mfma_f32_16x16x32_bf16 v[56:59], a[240:243], v[16:19], v[56:59]\n"
    "s_add_u32 s71, s68, 2\n"
    "s_waitcnt vmcnt(8)\n"
    "v_mfma_f32_16x16x32_bf16 v[60:63], a[244:247], v[16:19], v[60:63]\n"
    "s_waitcnt lgkmcnt(2)\n"
    "v_add_u32 v13, v11, v3\n"
    "v_mfma_f32_16x16x32_bf16 v[68:71], a[248:251], v[16:19], v[68:71]\n"
    "ds_read_b128 v[16:19], v9 offset:24576\n"
    "v_cmp_gt_i32 vcc, s71, v13\n"
    "v_mfma_f32_16x16x32_bf16 v[56:59], a[252:255], v[20:23], v[56:59]\n"
    "v_mfma_f32_16x16x32_bf16 v[60:63], v[128:131], v[20:23], v[60:63]\n"
    "s_waitcnt lgkmcnt(2)\n"
    "v_mfma_f32_16x16x32_bf16 v[68:71], v[132:135], v[20:23], v[68:71]\n"
    "ds_read_b128 v[20:23], v9 offset:25600\n"
    "v_mfma_f32_16x16x32_bf16 v[56:59], v[136:139], v[24:27], v[56:59]\n"
    "s_or_b64 exec, vcc, s[62:63]\n"
    "buffer_atomic_smax v11, v2, s[36:39], 0 offen sc0 sc1\n"
    "s_mov_b64 exec, -1\n"
    "v_mfma_f32_16x16x32_bf16 v[60:63], v[140:143], v[24:27], v[60:63]\n"
    "s_waitcnt lgkmcnt(2)\n"
    "s_lshr_b32 s76, s71, 1\n"
    "v_mfma_f32_16x16x32_bf16 v[68:71], v[144:147], v[24:27], v[68:71]\n"
    "ds_read_b128 v[24:27], v9 offset:26624\n"
    "s_and_b32 s76, s76, s54\n"
    "v_mfma_f32_16x16x32_bf16 v[56:59], v[148:151], v[28:31], v[56:59]\n"
    "s_mul_i32 s76, s76, s53\n"
    "v_mfma_f32_16x16x32_bf16 v[60:63], v[152:155], v[28:31], v[60:63]\n"
    "s_waitcnt lgkmcnt(2)\n"
    "s_add_u32 s76, s76, s52\n"
    "v_mfma_f32_16x16x32_bf16 v[68:71], v[156:159], v[28:31], v[68:71]\n"
    "ds_read_b128 v[28:31], v9 offset:27648\n"
    "s_add_u32 s76, s76, 1024\n"
    "v_mfma_f32_16x16x32_bf16 v[56:59], v[160:163], v[16:19], v[56:59]\n"
    "v_mfma_f32_16x16x32_bf16 v[60:63], v[164:167], v[16:19], v[60:63]\n"
    "s_waitcnt lgkmcnt(2)\n"
    "v_mfma_f32_16x16x32_bf16 v[68:71], v[168:171], v[16:19], v[68:71]\n"
    "ds_read_b128 v[16:19], v9 offset:28672\n"
    "v_mfma_f32_16x16x32_bf16 v[56:59], v[172:175], v[20:23], v[56:59]\n"
    "v_mfma_f32_16x16x32_bf16 v[60:63], v[176:179], v[20:23], v[60:63]\n"
    "s_waitcnt lgkmcnt(2)\n"
    "v_mfma_f32_16x16x32_bf16 v[68:71], v[180:183], v[20:23], v[68:71]\n"
    "ds_read_b128 v[20:23], v9 offset:29696\n"
    "v_mfma_f32_16x16x32_bf16 v[56:59], v[184:187], v[24:27], v[56:59]\n"
    "v_mfma_f32_16x16x32_bf16 v[60:63], v[188:191], v[24:27], v[60:63]\n"
    "s_waitcnt lgkmcnt(2)\n"
    "v_mfma_f32_16x16x32_bf16 v[68:71], v[192:195], v[24:27], v[68:71]\n"
    "ds_read_b128 v[24:27], v9 offset:30720\n"
    "v_mfma_f32_16x16x32_bf16 v[56:59], v[196:199], v[28:31], v[56:59]\n"
    "v_mfma_f32_16x16x32_bf16 v[60:63], v[200:203], v[28:31], v[60:63]\n"
    "s_waitcnt lgkmcnt(2)\n"
    "v_add_u32 v13, v5, v3\n"
    "v_mfma_f32_16x16x32_bf16 v[68:71], v[204:207], v[28:31], v[68:71]\n"
    "ds_read_b128 v[28:31], v9 offset:31744\n"
    "v_cmp_gt_i32 vcc, s71, v13\n"
    "v_mfma_f32_16x16x32_bf16 v[56:59], v[208:211], v[16:19], v[56:59]\n"
    "v_mfma_f32_16x16x32_bf16 v[60:63], v[212:215], v[16:19], v[60:63]\n"
    "s_waitcnt lgkmcnt(2)\n"
    "v_mfma_f32_16x16x32_bf16 v[68:71], v[216:219], v[16:19], v[68:71]\n"
    "v_mfma_f32_16x16x32_bf16 v[56:59], v[220:223], v[20:23], v[56:59]\n"
    "s_waitcnt vmcnt(1) lgkmcnt(0)\n"
    "s_barrier\n"
    "ds_read_b128 v[16:19], v0 offset:0\n"
    "s_or_b64 exec, vcc, s[62:63]\n"
    "buffer_atomic_smax v5, v2, s[36:39], 0 offen sc0 sc1\n"
    "s_mov_b64 exec, -1\n"
    "v_mfma_f32_16x16x32_bf16 v[60:63], v[224:227], v[20:23], v[60:63]\n"
    "v_mfma_f32_16x16x32_bf16 v[68:71], v[228:231], v[20:23], v[68:71]\n"
    "ds_read_b128 v[20:23], v0 offset:1024\n"
    "v_mfma_f32_16x16x32_bf16 v[56:59], v[232:235], v[24:27], v[56:59]\n"
    "v_mfma_f32_16x16x32_bf16 v[60:63], v[236:239], v[24:27], v[60:63]\n"
    "v_mfma_f32_16x16x32_bf16 v[68:71], v[240:243], v[24:27], v[68:71]\n"
    "ds_read_b128 v[24:27], v0 offset:2048\n"
    "v_mfma_f32_16x16x32_bf16 v[56:59], v[244:247], v[28:31], v[56:59]\n"
    "s_mov_b32 s69, 0x80000000\n"
    "s_mov_b32 s70, 0\n"
    "v_mov_b32 v12, s68\n"
    "v_mfma_f32_16x16x32_bf16 v[60:63], v[248:251], v[28:31], v[60:63]\n"
    "s_waitcnt lgkmcnt(2)\n"
    "s_add_u32 s68, s68, 1\n"
    "v_mfma_f32_16x16x32_bf16 v[68:71], v[252:255], v[28:31], v[68:71]\n"
    "ds_read_b128 v[28:31], v0 offset:3072\n"
    "s_branch .LgWO_head0_%=\n"
    ".LgWO_LOOPE_%=:\n"
    ".LgWO_done_%=:\n"
    "s_waitcnt vmcnt(0)\n"
    "buffer_atomic_smax v12, v10, s[36:39], 0 offen sc1\n"
    "s_waitcnt vmcnt(0)\n"
    "s_branch .LgWO_FIN_%=\n"
    ".LgWO_poll0_%=:\n"
    "v_mfma_f32_16x16x32_bf16 v[48:51], a[48:51], v[16:19], v[48:51]\n"
    "s_or_b64 exec, vcc, s[62:63]\n"
    "buffer_atomic_smax v11, v2, s[36:39], 0 offen sc0 sc1\n"
    "s_mov_b64 exec, -1\n"
    "v_mfma_f32_16x16x32_bf16 v[52:55], a[52:55], v[16:19], v[52:55]\n"
    "s_waitcnt lgkmcnt(2)\n"
    "v_mfma_f32_16x16x32_bf16 v[64:67], a[56:59], v[16:19], v[64:67]\n"
    "ds_read_b128 v[16:19], v0 offset:8192\n"
    "v_mfma_f32_16x16x32_bf16 v[48:51], a[60:63], v[20:23], v[48:51]\n"
    "buffer_store_dwordx4 v[68:71], v4, s[36:39], s73 offen offset:1024 sc1\n"
    "v_mfma_f32_16x16x32_bf16 v[52:55], a[64:67], v[20:23], v[52:55]\n"
    "s_waitcnt lgkmcnt(2)\n"
    "s_lshr_b32 s73, s68, 1\n"
    "v_mfma_f32_16x16x32_bf16 v[64:67], a[68:71], v[20:23], v[64:67]\n"
    "ds_read_b128 v[20:23], v0 offset:9216\n"
    "s_and_b32 s73, s73, s57\n"
    "v_mfma_f32_16x16x32_bf16 v[48:51], a[72:75], v[24:27], v[48:51]\n"
    "s_mul_i32 s73, s73, s56\n"
    "v_mfma_f32_16x16x32_bf16 v[52:55], a[76:79], v[24:27], v[52:55]\n"
    "s_waitcnt lgkmcnt(2)\n"
    "s_add_u32 s73, s73, s55\n"
    "v_mfma_f32_16x16x32_bf16 v[64:67], a[80:83], v[24:27], v[64:67]\n"
    "ds_read_b128 v[24:27], v0 offset:10240\n"
    "v_mfma_f32_16x16x32_bf16 v[48:51], a[84:87], v[28:31], v[48:51]\n"
    "v_mfma_f32_16x16x32_bf16 v[52:55], a[88:91], v[28:31], v[52:55]\n"
    "s_waitcnt lgkmcnt(2)\n"
    "v_mfma_f32_16x16x32_bf16 v[64:67], a[92:95], v[28:31], v[64:67]\n"
    "ds_read_b128 v[28:31], v0 offset:11264\n"
    "v_mfma_f32_16x16x32_bf16 v[48:51], a[96:99], v[16:19], v[48:51]\n"
    "v_mfma_f32_16x16x32_bf16 v[52:55], a[100:103], v[16:19], v[52:55]\n"
    "s_waitcnt lgkmcnt(2)\n"
    "v_mfma_f32_16x16x32_bf16 v[64:67], a[104:107], v[16:19], v[64:67]\n"
    "ds_read_b128 v[16:19], v0 offset:12288\n"
    "v_mfma_f32_16x16x32_bf16 v[48:51], a[108:111], v[20:23], v[48:51]\n"
    "v_mfma_f32_16x16x32_bf16 v[52:55], a[112:115], v[20:23], v[52:55]\n"
    "s_waitcnt lgkmcnt(2)\n"
    "s_waitcnt vmcnt(4)\n"
    "v_add_u32 v13, v5, v3\n"
    "v_mfma_f32_16x16x32_bf16 v[64:67], a[116:119], v[20:23], v[64:67]\n"
    "ds_read_b128 v[20:23], v0 offset:13312\n"
    "v_cmp_gt_i32 vcc, s71, v13\n"
    "v_mfma_f32_16x16x32_bf16 v[48:51], a[120:123], v[24:27], v[48:51]\n"
    "v_mfma_f32_16x16x32_bf16 v[52:55], a[124:127], v[24:27], v[52:55]\n"
    "s_waitcnt lgkmcnt(2)\n"
    "v_mfma_f32_16x16x32_bf16 v[64:67], a[128:131], v[24:27], v[64:67]\n"
    "ds_read_b128 v[24:27], v0 offset:14336\n"
    "s_cbranch_vccz .LgWO_dma0_33_%=\n"
    "v_mfma_f32_16x16x32_bf16 v[48:51], a[132:135], v[28:31], v[48:51]\n"
    "s_or_b64 exec, vcc, s[62:63]\n"
    "buffer_atomic_smax v5, v2, s[36:39], 0 offen sc0 sc1\n"
    "s_mov_b64 exec, -1\n"
    "v_mfma_f32_16x16x32_bf16 v[52:55], a[136:139], v[28:31], v[52:55]\n"
    "s_waitcnt lgkmcnt(2)\n"
    "v_mfma_f32_16x16x32_bf16 v[64:67], a[140:143], v[28:31], v[64:67]\n"
    "ds_read_b128 v[28:31], v0 offset:15360\n"
    "v_mfma_f32_16x16x32_bf16 v[48:51], a[144:147], v[16:19], v[48:51]\n"
    "v_mfma_f32_16x16x32_bf16 v[52:55], a[148:151], v[16:19], v[52:55]\n"
    "s_waitcnt lgkmcnt(2)\n"
    "v_mfma_f32_16x16x32_bf16 v[64:67], a[152:155], v[16:19], v[64:67]\n"
    "ds_read_b128 v[16:19], v0 offset:16384\n"
    "v_mfma_f32_16x16x32_bf16 v[48:51], a[156:159], v[20:23], v[48:51]\n"
    "v_mfma_f32_16x16x32_bf16 v[52:55], a[160:163], v[20:23], v[52:55]\n"
    "s_waitcnt lgkmcnt(2)\n"
    "v_mfma_f32_16x16x32_bf16 v[64:67], a[164:167], v[20:23], v[64:67]\n"
    "ds_read_b128 v[20:23], v0 offset:17408\n"
    "v_mfma_f32_16x16x32_bf16 v[48:51], a[168:171], v[24:27], v[48:51]\n"
    "v_mfma_f32_16x16x32_bf16 v[52:55], a[172:175], v[24:27], v[52:55]\n"
    "s_waitcnt lgkmcnt(2)\n"
    "v_mfma_f32_16x16x32_bf16 v[64:67], a[176:179], v[24:27], v[64:67]\n"
    "ds_read_b128 v[24:27], v0 offset:18432\n"
    "v_mfma_f32_16x16x32_bf16 v[48:51], a[180:183], v[28:31], v[48:51]\n"
    "v_mfma_f32_16x16x32_bf16 v[52:55], a[184:187], v[28:31], v[52:55]\n"
    "s_waitcnt lgkmcnt(2)\n"
    "v_mfma_f32_16x16x32_bf16 v[64:67], a[188:191], v[28:31], v[64:67]\n"
    "ds_read_b128 v[28:31], v0 offset:19456\n"
    "v_mfma_f32_16x16x32_bf16 v[48:51], a[192:195], v[16:19], v[48:51]\n"
    "v_mfma_f32_16x16x32_bf16 v[52:55], a[196:199], v[16:19], v[52:55]\n"
    "s_waitcnt lgkmcnt(2)\n"
    "s_waitcnt vmcnt(2)\n"
    "v_add_u32 v13, v11, v3\n"
    "v_mfma_f32_16x16x32_bf16 v[64:67], a[200:203], v[16:19], v[64:67]\n"
    "ds_read_b128 v[16:19], v0 offset:20480\n"
    "v_cmp_gt_i32 vcc, s71, v13\n"
    "v_mfma_f32_16x16x32_bf16 v[48:51], a[204:207], v[20:23], v[48:51]\n"
    "v_mfma_f32_16x16x32_bf16 v[52:55], a[208:211], v[20:23], v[52:55]\n"
    "s_waitcnt lgkmcnt(2)\n"
    "v_mfma_f32_16x16x32_bf16 v[64:67], a[212:215], v[20:23], v[64:67]\n"
    "ds_read_b128 v[20:23], v0 offset:21504\n"
    "s_cbranch_vccz .LgWO_dma0_54_%=\n"
    "v_mfma_f32_16x16x32_bf16 v[48:51], a[216:219], v[24:27], v[48:51]\n"
    "s_or_b64 exec, vcc, s[62:63]\n"
    "buffer_atomic_smax v11, v2, s[36:39], 0 offen sc0 sc1\n"
    "s_mov_b64 exec, -1\n"
    "v_mfma_f32_16x16x32_bf16 v[52:55], a[220:223], v[24:27], v[52:55]\n"
    "s_waitcnt lgkmcnt(2)\n"
    "s_waitcnt vmcnt(2)\n"
    "buffer_atomic_smax v12, v10, s[36:39], s70 offen sc1\n"
    "v_mfma_f32_16x16x32_bf16 v[64:67], a[224:227], v[24:27], v[64:67]\n"
    "ds_read_b128 v[24:27], v0 offset:22528\n"
    "v_mfma_f32_16x16x32_bf16 v[48:51], a[228:231], v[28:31], v[48:51]\n"
    "v_mfma_f32_16x16x32_bf16 v[52:55], a[232:235], v[28:31], v[52:55]\n"
    "s_waitcnt lgkmcnt(2)\n"
    "v_mfma_f32_16x16x32_bf16 v[64:67], a[236:239], v[28:31], v[64:67]\n"
    "ds_read_b128 v[28:31], v0 offset:23552\n"
    "v_mfma_f32_16x16x32_bf16 v[48:51], a[240:243], v[16:19], v[48:51]\n"
    "v_mfma_f32_16x16x32_bf16 v[52:55], a[244:247], v[16:19], v[52:55]\n"
    "s_waitcnt lgkmcnt(2)\n"
    "v_mfma_f32_16x16x32_bf16 v[64:67], a[248:251], v[16:19], v[64:67]\n"
    "ds_read_b128 v[16:19], v0 offset:24576\n"
    "v_mfma_f32_16x16x32_bf16 v[48:51], a[252:255], v[20:23], v[48:51]\n"
    "v_mfma_f32_16x16x32_bf16 v[52:55], v[128:131], v[20:23], v[52:55]\n"
    "s_waitcnt lgkmcnt(2)\n"
    "v_mfma_f32_16x16x32_bf16 v[64:67], v[132:135], v[20:23], v[64:67]\n"
    "ds_read_b128 v[20:23], v0 offset:25600\n"
    "v_mfma_f32_16x16x32_bf16 v[48:51], v[136:139], v[24:27], v[48:51]\n"
    "v_mfma_f32_16x16x32_bf16 v[52:55], v[140:143], v[24:27], v[52:55]\n"
    "s_waitcnt lgkmcnt(2)\n"
    "v_mfma_f32_16x16x32_bf16 v[64:67], v[144:147], v[24:27], v[64:67]\n"
    "ds_read_b128 v[24:27], v0 offset:26624\n"
    "v_mfma_f32_16x16x32_bf16 v[48:51], v[148:151], v[28:31], v[48:51]\n"
    "v_mfma_f32_16x16x32_bf16 v[52:55], v[152:155], v[28:31], v[52:55]\n"
    "s_waitcnt lgkmcnt(2)\n"
    "s_waitcnt vmcnt(2)\n"
    "v_add_u32 v13, v5, v3\n"
    "v_mfma_f32_16x16x32_bf16 v[64:67], v[156:159], v[28:31], v[64:67]\n"
    "ds_read_b128 v[28:31], v0 offset:27648\n"
    "v_cmp_gt_i32 vcc, s71, v13\n"
    "v_mfma_f32_16x16x32_bf16 v[48:51], v[160:163], v[16:19], v[48:51]\n"
    "v_mfma_f32_16x16x32_bf16 v[52:55], v[164:167], v[16:19], v[52:55]\n"
    "s_waitcnt lgkmcnt(2)\n"
    "v_mfma_f32_16x16x32_bf16 v[64:67], v[168:171], v[16:19], v[64:67]\n"
    "ds_read_b128 v[16:19], v0 offset:28672\n"
    "s_cbranch_vccz .LgWO_dma0_75_%=\n"
    "v_mfma_f32_16x16x32_bf16 v[48:51], v[172:175], v[20:23], v[48:51]\n"
    "s_or_b64 exec, vcc, s[62:63]\n"
    "buffer_atomic_smax v5, v2, s[36:39], 0 offen sc0 sc1\n"
    "s_mov_b64 exec, -1\n"
    "v_mfma_f32_16x16x32_bf16 v[52:55], v[176:179], v[20:23], v[52:55]\n"
    "s_waitcnt lgkmcnt(2)\n"
    "v_mfma_f32_16x16x32_bf16 v[64:67], v[180:183], v[20:23], v[64:67]\n"
    "ds_read_b128 v[20:23], v0 offset:29696\n"
    "v_mfma_f32_16x16x32_bf16 v[48:51], v[184:187], v[24:27], v[48:51]\n"
    "v_mfma_f32_16x16x32_bf16 v[52:55], v[188:191], v[24:27], v[52:55]\n"
    "s_waitcnt lgkmcnt(2)\n"
    "v_mfma_f32_16x16x32_bf16 v[64:67], v[192:195], v[24:27], v[64:67]\n"
    "ds_read_b128 v[24:27], v0 offset:30720\n"
    "v_mfma_f32_16x16x32_bf16 v[48:51], v[196:199], v[28:31], v[48:51]\n"
    "v_mfma_f32_16x16x32_bf16 v[52:55], v[200:203], v[28:31], v[52:55]\n"
    "s_waitcnt lgkmcnt(2)\n"
    "v_mfma_f32_16x16x32_bf16 v[64:67], v[204:207], v[28:31], v[64:67]\n"
    "ds_read_b128 v[28:31], v0 offset:31744\n"
    "v_mfma_f32_16x16x32_bf16 v[48:51], v[208:211], v[16:19], v[48:51]\n"
    "v_mfma_f32_16x16x32_bf16 v[52:55], v[212:215], v[16:19], v[52:55]\n"
    "s_waitcnt lgkmcnt(2)\n"
    "v_mfma_f32_16x16x32_bf16 v[64:67], v[216:219], v[16:19], v[64:67]\n"
    "v_mfma_f32_16x16x32_bf16 v[48:51], v[220:223], v[20:23], v[48:51]\n"
    "v_mfma_f32_16x16x32_bf16 v[52:55], v[224:227], v[20:23], v[52:55]\n"
    "s_waitcnt lgkmcnt(1)\n"
    "v_mfma_f32_16x16x32_bf16 v[64:67], v[228:231], v[20:23], v[64:67]\n"
    "v_mfma_f32_16x16x32_bf16 v[48:51], v[232:235], v[24:27], v[48:51]\n"
    "v_mfma_f32_16x16x32_bf16 v[52:55], v[236:239], v[24:27], v[52:55]\n"
    "s_waitcnt lgkmcnt(0)\n"
    "v_mfma_f32_16x16x32_bf16 v[64:67], v[240:243], v[24:27], v[64:67]\n"
    "v_mfma_f32_16x16x32_bf16 v[48:51], v[244:247], v[28:31], v[48:51]\n"
    "v_mfma_f32_16x16x32_bf16 v[52:55], v[248:251], v[28:31], v[52:55]\n"
    "v_mfma_f32_16x16x32_bf16 v[64:67], v[252:255], v[28:31], v[64:67]\n"
    "v_cvt_pk_bf16_f32 v48, v48, v49\n"
    "v_cvt_pk_bf16_f32 v49, v50, v51\n"
    "s_nop 1\n"
    "v_cvt_pk_bf16_f32 v50, v52, v53\n"
    "v_cvt_pk_bf16_f32 v51, v54, v55\n"
    "buffer_store_dwordx4 v[48:51], v15, s[36:39], s73 offen sc1\n"
    "s_nop 0\n"
    "v_cvt_pk_bf16_f32 v64, v64, v65\n"
    "v_cvt_pk_bf16_f32 v65, v66, v67\n"
    "s_nop 0\n"
    "v_permlane16_swap_b32 v64, v66\n"
    "v_permlane16_swap_b32 v65, v67\n"
    "buffer_store_dwordx4 v[64:67], v4, s[36:39], s73 offen sc1\n"
    "v_mov_b32 v12, s68\n"
    "s_mov_b32 s73, 0x80000000\n"
    "s_mov_b32 s68, s71\n"
    "s_waitcnt vmcnt(4)\n"
    "v_add_u32 v13, v11, v3\n"
    "v_cmp_gt_i32 vcc, s71, v13\n"
    "s_cbranch_vccnz .LgWO_idle0_%=\n"
    "s_add_u32 m0, s59, 32768\n"
    "s_nop 0\n"
    "buffer_load_dwordx4 v1, s[36:39], s76 offen sc1 lds\n"
    "s_add_u32 m0, s59, 36864\n"
    "s_add_u32 s76, s76, 16384\n"
    "buffer_load_dwordx4 v1, s[36:39], s76 offen sc1 lds\n"
    "s_add_u32 m0, s59, 40960\n"
    "s_add_u32 s76, s76, 16384\n"
    "buffer_load_dwordx4 v1, s[36:39], s76 offen sc1 lds\n"
    "s_add_u32 m0, s59, 45056\n"
    "s_add_u32 s76, s76, 16384\n"
    "buffer_load_dwordx4 v1, s[36:39], s76 offen sc1 lds\n"
    "s_add_u32 m0, s59, 49152\n"
    "s_add_u32 s76, s76, 16384\n"
    "buffer_load_dwordx4 v1, s[36:39], s76 offen sc1 lds\n"
    "s_add_u32 m0, s59, 53248\n"
    "s_add_u32 s76, s76, 16384\n"
    "buffer_load_dwordx4 v1, s[36:39], s76 offen sc1 lds\n"
    "s_add_u32 m0, s59, 57344\n"
    "s_add_u32 s76, s76, 16384\n"
    "buffer_load_dwordx4 v1, s[36:39], s76 offen sc1 lds\n"
    "s_add_u32 m0, s59, 61440\n"
    "s_add_u32 s76, s76, 16384\n"
    "buffer_load_dwordx4 v1, s[36:39], s76 offen sc1 lds\n"
    "s_waitcnt vmcnt(8)\n"
    "buffer_atomic_smax v12, v14, s[36:39], 0 offen sc1\n"
    "s_mov_b32 s69, 0x80000000\n"
    "s_mov_b32 s70, 0x80000000\n"
    "v_mov_b32 v7, v12\n"
    "s_waitcnt vmcnt(11)\n"
    "s_add_u32 s71, s68, 1\n"
    "v_add_u32 v13, v11, v3\n"
    "v_cmp_gt_i32 vcc, s71, v13\n"
    "s_nop 0\n"
    "s_or_b64 exec, vcc, s[62:63]\n"
    "buffer_atomic_smax v11, v2, s[36:39], 0 offen sc0 sc1\n"
    "s_mov_b64 exec, -1\n"
    "s_waitcnt vmcnt(2)\n"
    "s_branch .LgWO_bar1_%=\n"
    ".LgWO_dma0_33_%=:\n"
    "v_mfma_f32_16x16x32_bf16 v[48:51], a[132:135], v[28:31], v[48:51]\n"
    "s_add_u32 m0, s59, 32768\n"
    "v_mfma_f32_16x16x32_bf16 v[52:55], a[136:139], v[28:31], v[52:55]\n"
    "s_waitcnt lgkmcnt(2)\n"
    "buffer_load_dwordx4 v1, s[36:39], s76 offen sc1 lds\n"
    "v_mfma_f32_16x16x32_bf16 v[64:67], a[140:143], v[28:31], v[64:67]\n"
    "ds_read_b128 v[28:31], v0 offset:15360\n"
    "s_add_u32 s76, s76, 16384\n"
    "v_mfma_f32_16x16x32_bf16 v[48:51], a[144:147], v[16:19], v[48:51]\n"
    "s_add_u32 m0, s59, 36864\n"
    "v_mfma_f32_16x16x32_bf16 v[52:55], a[148:151], v[16:19], v[52:55]\n"
    "s_waitcnt lgkmcnt(2)\n"
    "buffer_load_dwordx4 v1, s[36:39], s76 offen sc1 lds\n"
    "v_mfma_f32_16x16x32_bf16 v[64:67], a[152:155], v[16:19], v[64:67]\n"
    "ds_read_b128 v[16:19], v0 offset:16384\n"
    "v_mfma_f32_16x16x32_bf16 v[48:51], a[156:159], v[20:23], v[48:51]\n"
    "s_add_u32 s76, s76, 16384\n"
    "s_add_u32 m0, s59, 40960\n"
    "v_mfma_f32_16x16x32_bf16 v[52:55], a[160:163], v[20:23], v[52:55]\n"
    "s_waitcnt lgkmcnt(2)\n"
    "buffer_load_dwordx4 v1, s[36:39], s76 offen sc1 lds\n"
    "v_mfma_f32_16x16x32_bf16 v[64:67], a[164:167], v[20:23], v[64:67]\n"
    "ds_read_b128 v[20:23], v0 offset:17408\n"
    "v_mfma_f32_16x16x32_bf16 v[48:51], a[168:171], v[24:27], v[48:51]\n"
    "v_mfma_f32_16x16x32_bf16 v[52:55], a[172:175], v[24:27], v[52:55]\n"
    "s_waitcnt lgkmcnt(2)\n"
    "s_add_u32 s76, s76, 16384\n"
    "s_add_u32 m0, s59, 45056\n"
    "v_mfma_f32_16x16x32_bf16 v[64:67], a[176:179], v[24:27], v[64:67]\n"
    "ds_read_b128 v[24:27], v0 offset:18432\n"
    "buffer_load_dwordx4 v1, s[36:39], s76 offen sc1 lds\n"
    "v_mfma_f32_16x16x32_bf16 v[48:51], a[180:183], v[28:31], v[48:51]\n"
    "v_mfma_f32_16x16x32_bf16 v[52:55], a[184:187], v[28:31], v[52:55]\n"
    "s_waitcnt lgkmcnt(2)\n"
    "v_mfma_f32_16x16x32_bf16 v[64:67], a[188:191], v[28:31], v[64:67]\n"
    "ds_read_b128 v[28:31], v0 offset:19456\n"
    "s_add_u32 s76, s76, 16384\n"
    "v_mfma_f32_16x16x32_bf16 v[48:51], a[192:195], v[16:19], v[48:51]\n"
    "s_add_u32 m0, s59, 49152\n"
    "v_mfma_f32_16x16x32_bf16 v[52:55], a[196:199], v[16:19], v[52:55]\n"
    "s_waitcnt lgkmcnt(2)\n"
    "buffer_load_dwordx4 v1, s[36:39], s76 offen sc1 lds\n"
    "v_mfma_f32_16x16x32_bf16 v[64:67], a[200:203], v[16:19], v[64:67]\n"
    "ds_read_b128 v[16:19], v0 offset:20480\n"
    "v_mfma_f32_16x16x32_bf16 v[48:51], a[204:207], v[20:23], v[48:51]\n"
    "s_add_u32 s76, s76, 16384\n"
    "s_add_u32 m0, s59, 53248\n"
    "v_mfma_f32_16x16x32_bf16 v[52:55], a[208:211], v[20:23], v[52:55]\n"
    "s_waitcnt lgkmcnt(2)\n"
    "buffer_load_dwordx4 v1, s[36:39], s76 offen sc1 lds\n"
    "v_mfma_f32_16x16x32_bf16 v[64:67], a[212:215], v[20:23], v[64:67]\n"
    "ds_read_b128 v[20:23], v0 offset:21504\n"
    "v_mfma_f32_16x16x32_bf16 v[48:51], a[216:219], v[24:27], v[48:51]\n"
    "v_mfma_f32_16x16x32_bf16 v[52:55], a[220:223], v[24:27], v[52:55]\n"
    "s_waitcnt lgkmcnt(2)\n"
    "s_waitcnt vmcnt(6)\n"
    "buffer_atomic_smax v12, v10, s[36:39], s70 offen sc1\n"
    "v_mfma_f32_16x16x32_bf16 v[64:67], a[224:227], v[24:27], v[64:67]\n"
    "ds_read_b128 v[24:27], v0 offset:22528\n"
    "s_add_u32 s76, s76, 16384\n"
    "v_mfma_f32_16x16x32_bf16 v[48:51], a[228:231], v[28:31], v[48:51]\n"
    "s_add_u32 m0, s59, 57344\n"
    "v_mfma_f32_16x16x32_bf16 v[52:55], a[232:235], v[28:31], v[52:55]\n"
    "s_waitcnt lgkmcnt(2)\n"
    "buffer_load_dwordx4 v1, s[36:39], s76 offen sc1 lds\n"
    "v_mfma_f32_16x16x32_bf16 v[64:67], a[236:239], v[28:31], v[64:67]\n"
    "ds_read_b128 v[28:31], v0 offset:23552\n"
    "s_add_u32 s76, s76, 16384\n"
    "v_mfma_f32_16x16x32_bf16 v[48:51], a[240:243], v[16:19], v[48:51]\n"
    "s_add_u32 m0, s59, 61440\n"
    "v_mfma_f32_16x16x32_bf16 v[52:55], a[244:247], v[16:19], v[52:55]\n"
    "s_waitcnt lgkmcnt(2)\n"
    "buffer_load_dwordx4 v1, s[36:39], s76 offen sc1 lds\n"
    "v_mfma_f32_16x16x32_bf16 v[64:67], a[248:251], v[16:19], v[64:67]\n"
    "ds_read_b128 v[16:19], v0 offset:24576\n"
    "v_mfma_f32_16x16x32_bf16 v[48:51], a[252:255], v[20:23], v[48:51]\n"
    "v_mfma_f32_16x16x32_bf16 v[52:55], v[128:131], v[20:23], v[52:55]\n"
    "s_waitcnt lgkmcnt(2)\n"
    "v_mfma_f32_16x16x32_bf16 v[64:67], v[132:135], v[20:23], v[64:67]\n"
    "ds_read_b128 v[20:23], v0 offset:25600\n"
    "v_mfma_f32_16x16x32_bf16 v[48:51], v[136:139], v[24:27], v[48:51]\n"
    "v_mfma_f32_16x16x32_bf16 v[52:55], v[140:143], v[24:27], v[52:55]\n"
    "s_waitcnt lgkmcnt(2)\n"
    "v_mfma_f32_16x16x32_bf16 v[64:67], v[144:147], v[24:27], v[64:67]\n"
    "ds_read_b128 v[24:27], v0 offset:26624\n"
    "v_mfma_f32_16x16x32_bf16 v[48:51], v[148:151], v[28:31], v[48:51]\n"
    "v_mfma_f32_16x16x32_bf16 v[52:55], v[152:155], v[28:31], v[52:55]\n"
    "s_waitcnt lgkmcnt(2)\n"
    "v_mfma_f32_16x16x32_bf16 v[64:67], v[156:159], v[28:31], v[64:67]\n"
    "ds_read_b128 v[28:31], v0 offset:27648\n"
    "v_mfma_f32_16x16x32_bf16 v[48:51], v[160:163], v[16:19], v[48:51]\n"
    "v_mfma_f32_16x16x32_bf16 v[52:55], v[164:167], v[16:19], v[52:55]\n"
    "s_waitcnt lgkmcnt(2)\n"
    "v_mfma_f32_16x16x32_bf16 v[64:67], v[168:171], v[16:19], v[64:67]\n"
    "ds_read_b128 v[16:19], v0 offset:28672\n"
    "v_mfma_f32_16x16x32_bf16 v[48:51], v[172:175], v[20:23], v[48:51]\n"
    "v_mfma_f32_16x16x32_bf16 v[52:55], v[176:179], v[20:23], v[52:55]\n"
    "s_waitcnt lgkmcnt(2)\n"
    "v_mfma_f32_16x16x32_bf16 v[64:67], v[180:183], v[20:23], v[64:67]\n"
    "ds_read_b128 v[20:23], v0 offset:29696\n"
    "v_mfma_f32_16x16x32_bf16 v[48:51], v[184:187], v[24:27], v[48:51]\n"
    "v_mfma_f32_16x16x32_bf16 v[52:55], v[188:191], v[24:27], v[52:55]\n"
    "s_waitcnt lgkmcnt(2)\n"
    "v_mfma_f32_16x16x32_bf16 v[64:67], v[192:195], v[24:27], v[64:67]\n"
    "ds_read_b128 v[24:27], v0 offset:30720\n"
    "v_mfma_f32_16x16x32_bf16 v[48:51], v[196:199], v[28:31], v[48:51]\n"
    "v_mfma_f32_16x16x32_bf16 v[52:55], v[200:203], v[28:31], v[52:55]\n"
    "s_waitcnt lgkmcnt(2)\n"
    "v_mfma_f32_16x16x32_bf16 v[64:67], v[204:207], v[28:31], v[64:67]\n"
    "ds_read_b128 v[28:31], v0 offset:31744\n"
    "v_mfma_f32_16x16x32_bf16 v[48:51], v[208:211], v[16:19], v[48:51]\n"
    "v_mfma_f32_16x16x32_bf16 v[52:55], v[212:215], v[16:19], v[52:55]\n"
    "s_waitcnt lgkmcnt(2)\n"
    "v_mfma_f32_16x16x32_bf16 v[64:67], v[216:219], v[16:19], v[64:67]\n"
    "v_mfma_f32_16x16x32_bf16 v[48:51], v[220:223], v[20:23], v[48:51]\n"
    "v_mfma_f32_16x16x32_bf16 v[52:55], v[224:227], v[20:23], v[52:55]\n"
    "s_waitcnt lgkmcnt(1)\n"
    "v_mfma_f32_16x16x32_bf16 v[64:67], v[228:231], v[20:23], v[64:67]\n"
    "v_mfma_f32_16x16x32_bf16 v[48:51], v[232:235], v[24:27], v[48:51]\n"
    "v_mfma_f32_16x16x32_bf16 v[52:55], v[236:239], v[24:27], v[52:55]\n"
    "s_waitcnt lgkmcnt(0)\n"
    "v_mfma_f32_16x16x32_bf16 v[64:67], v[240:243], v[24:27], v[64:67]\n"
    "v_mfma_f32_16x16x32_bf16 v[48:51], v[244:247], v[28:31], v[48:51]\n"
    "v_mfma_f32_16x16x32_bf16 v[52:55], v[248:251], v[28:31], v[52:55]\n"
    "v_mfma_f32_16x16x32_bf16 v[64:67], v[252:255], v[28:31], v[64:67]\n"
    "v_cvt_pk_bf16_f32 v48, v48, v49\n"
    "v_cvt_pk_bf16_f32 v49, v50, v51\n"
    "s_nop 1\n"
    "v_cvt_pk_bf16_f32 v50, v52, v53\n"
    "v_cvt_pk_bf16_f32 v51, v54, v55\n"
    "buffer_store_dwordx4 v[48:51], v15, s[36:39], s73 offen sc1\n"
    "s_nop 0\n"
    "v_cvt_pk_bf16_f32 v64, v64, v65\n"
    "v_cvt_pk_bf16_f32 v65, v66, v67\n"
    "s_nop 0\n"
    "v_permlane16_swap_b32 v64, v66\n"
    "v_permlane16_swap_b32 v65, v67\n"
    "buffer_store_dwordx4 v[64:67], v4, s[36:39], s73 offen sc1\n"
    "v_mov_b32 v12, s68\n"
    "s_mov_b32 s73, 0x80000000\n"
    "s_mov_b32 s68, s71\n"
    "s_mov_b32 s69, 0\n"
    "s_mov_b32 s70, 0x80000000\n"
    "v_mov_b32 v7, v12\n"
    "s_waitcnt vmcnt(12)\n"
    "s_add_u32 s71, s68, 1\n"
    "v_add_u32 v13, v11, v3\n"
    "v_cmp_gt_i32 vcc, s71, v13\n"
    "s_nop 0\n"
    "s_or_b64 exec, vcc, s[62:63]\n"
    "buffer_atomic_smax v11, v2, s[36:39], 0 offen sc0 sc1\n"
    "s_mov_b64 exec, -1\n"
    "s_waitcnt vmcnt(3)\n"
    "s_branch .LgWO_bar1_%=\n"
    ".LgWO_dma0_54_%=:\n"
    "v_mfma_f32_16x16x32_bf16 v[48:51], a[216:219], v[24:27], v[48:51]\n"
    "s_add_u32 m0, s59, 32768\n"
    "v_mfma_f32_16x16x32_bf16 v[52:55], a[220:223], v[24:27], v[52:55]\n"
    "s_waitcnt lgkmcnt(2)\n"
    "s_waitcnt vmcnt(1)\n"
    "buffer_atomic_smax v12, v10, s[36:39], s70 offen sc1\n"
    "v_mfma_f32_16x16x32_bf16 v[64:67], a[224:227], v[24:27], v[64:67]\n"
    "ds_read_b128 v[24:27], v0 offset:22528\n"
    "buffer_load_dwordx4 v1, s[36:39], s76 offen sc1 lds\n"
    "v_mfma_f32_16x16x32_bf16 v[48:51], a[228:231], v[28:31], v[48:51]\n"
    "s_add_u32 s76, s76, 16384\n"
    "s_add_u32 m0, s59, 36864\n"
    "v_mfma_f32_16x16x32_bf16 v[52:55], a[232:235], v[28:31], v[52:55]\n"
    "s_waitcnt lgkmcnt(2)\n"
    "buffer_load_dwordx4 v1, s[36:39], s76 offen sc1 lds\n"
    "v_mfma_f32_16x16x32_bf16 v[64:67], a[236:239], v[28:31], v[64:67]\n"
    "ds_read_b128 v[28:31], v0 offset:23552\n"
    "v_mfma_f32_16x16x32_bf16 v[48:51], a[240:243], v[16:19], v[48:51]\n"
    "s_add_u32 s76, s76, 16384\n"
    "s_add_u32 m0, s59, 40960\n"
    "v_mfma_f32_16x16x32_bf16 v[52:55], a[244:247], v[16:19], v[52:55]\n"
    "s_waitcnt lgkmcnt(2)\n"
    "buffer_load_dwordx4 v1, s[36:39], s76 offen sc1 lds\n"
    "v_mfma_f32_16x16x32_bf16 v[64:67], a[248:251], v[16:19], v[64:67]\n"
    "ds_read_b128 v[16:19], v0 offset:24576\n"
    "v_mfma_f32_16x16x32_bf16 v[48:51], a[252:255], v[20:23], v[48:51]\n"
    "v_mfma_f32_16x16x32_bf16 v[52:55], v[128:131], v[20:23], v[52:55]\n"
    "s_waitcnt lgkmcnt(2)\n"
    "s_add_u32 s76, s76, 16384\n"
    "s_add_u32 m0, s59, 45056\n"
    "v_mfma_f32_16x16x32_bf16 v[64:67], v[132:135], v[20:23], v[64:67]\n"
    "ds_read_b128 v[20:23], v0 offset:25600\n"
    "buffer_load_dwordx4 v1, s[36:39], s76 offen sc1 lds\n"
    "v_mfma_f32_16x16x32_bf16 v[48:51], v[136:139], v[24:27], v[48:51]\n"
    "v_mfma_f32_16x16x32_bf16 v[52:55], v[140:143], v[24:27], v[52:55]\n"
    "s_waitcnt lgkmcnt(2)\n"
    "v_mfma_f32_16x16x32_bf16 v[64:67], v[144:147], v[24:27], v[64:67]\n"
    "ds_read_b128 v[24:27], v0 offset:26624\n"
    "s_add_u32 s76, s76, 16384\n"
    "v_mfma_f32_16x16x32_bf16 v[48:51], v[148:151], v[28:31], v[48:51]\n"
    "s_add_u32 m0, s59, 49152\n"
    "v_mfma_f32_16x16x32_bf16 v[52:55], v[152:155], v[28:31], v[52:55]\n"
    "s_waitcnt lgkmcnt(2)\n"
    "buffer_load_dwordx4 v1, s[36:39], s76 offen sc1 lds\n"
    "v_mfma_f32_16x16x32_bf16 v[64:67], v[156:159], v[28:31], v[64:67]\n"
    "ds_read_b128 v[28:31], v0 offset:27648\n"
    "v_mfma_f32_16x16x32_bf16 v[48:51], v[160:163], v[16:19], v[48:51]\n"
    "s_add_u32 s76, s76, 16384\n"
    "s_add_u32 m0, s59, 53248\n"
    "v_mfma_f32_16x16x32_bf16 v[52:55], v[164:167], v[16:19], v[52:55]\n"
    "s_waitcnt lgkmcnt(2)\n"
    "buffer_load_dwordx4 v1, s[36:39], s76 offen sc1 lds\n"
    "v_mfma_f32_16x16x32_bf16 v[64:67], v[168:171], v[16:19], v[64:67]\n"
    "ds_read_b128 v[16:19], v0 offset:28672\n"
    "v_mfma_f32_16x16x32_bf16 v[48:51], v[172:175], v[20:23], v[48:51]\n"
    "v_mfma_f32_16x16x32_bf16 v[52:55], v[176:179], v[20:23], v[52:55]\n"
    "s_waitcnt lgkmcnt(2)\n"
    "s_add_u32 s76, s76, 16384\n"
    "s_add_u32 m0, s59, 57344\n"
    "v_mfma_f32_16x16x32_bf16 v[64:67], v[180:183], v[20:23], v[64:67]\n"
    "ds_read_b128 v[20:23], v0 offset:29696\n"
    "buffer_load_dwordx4 v1, s[36:39], s76 offen sc1 lds\n"
    "v_mfma_f32_16x16x32_bf16 v[48:51], v[184:187], v[24:27], v[48:51]\n"
    "v_mfma_f32_16x16x32_bf16 v[52:55], v[188:191], v[24:27], v[52:55]\n"
    "s_waitcnt lgkmcnt(2)\n"
    "v_mfma_f32_16x16x32_bf16 v[64:67], v[192:195], v[24:27], v[64:67]\n"
    "ds_read_b128 v[24:27], v0 offset:30720\n"
    "s_add_u32 s76, s76, 16384\n"
    "v_mfma_f32_16x16x32_bf16 v[48:51], v[196:199], v[28:31], v[48:51]\n"
    "s_add_u32 m0, s59, 61440\n"
    "v_mfma_f32_16x16x32_bf16 v[52:55], v[200:203], v[28:31], v[52:55]\n"
    "s_waitcnt lgkmcnt(2)\n"
    "buffer_load_dwordx4 v1, s[36:39], s76 offen sc1 lds\n"
    "v_mfma_f32_16x16x32_bf16 v[64:67], v[204:207], v[28:31], v[64:67]\n"
    "ds_read_b128 v[28:31], v0 offset:31744\n"
    "v_mfma_f32_16x16x32_bf16 v[48:51], v[208:211], v[16:19], v[48:51]\n"
    "v_mfma_f32_16x16x32_bf16 v[52:55], v[212:215], v[16:19], v[52:55]\n"
    "s_waitcnt lgkmcnt(2)\n"
    "v_mfma_f32_16x16x32_bf16 v[64:67], v[216:219], v[16:19], v[64:67]\n"
    "v_mfma_f32_16x16x32_bf16 v[48:51], v[220:223], v[20:23], v[48:51]\n"
    "v_mfma_f32_16x16x32_bf16 v[52:55], v[224:227], v[20:23], v[52:55]\n"
    "s_waitcnt lgkmcnt(1)\n"
    "v_mfma_f32_16x16x32_bf16 v[64:67], v[228:231], v[20:23], v[64:67]\n"
    "v_mfma_f32_16x16x32_bf16 v[48:51], v[232:235], v[24:27], v[48:51]\n"
    "v_mfma_f32_16x16x32_bf16 v[52:55], v[236:239], v[24:27], v[52:55]\n"
    "s_waitcnt lgkmcnt(0)\n"
    "v_mfma_f32_16x16x32_bf16 v[64:67], v[240:243], v[24:27], v[64:67]\n"
    "v_mfma_f32_16x16x32_bf16 v[48:51], v[244:247], v[28:31], v[48:51]\n"
    "v_mfma_f32_16x16x32_bf16 v[52:55], v[248:251], v[28:31], v[52:55]\n"
    "v_mfma_f32_16x16x32_bf16 v[64:67], v[252:255], v[28:31], v[64:67]\n"
    "v_cvt_pk_bf16_f32 v48, v48, v49\n"
    "v_cvt_pk_bf16_f32 v49, v50, v51\n"
    "s_nop 1\n"
    "v_cvt_pk_bf16_f32 v50, v52, v53\n"
    "v_cvt_pk_bf16_f32 v51, v54, v55\n"
    "buffer_store_dwordx4 v[48:51], v15, s[36:39], s73 offen sc1\n"
    "s_nop 0\n"
    "v_cvt_pk_bf16_f32 v64, v64, v65\n"
    "v_cvt_pk_bf16_f32 v65, v66, v67\n"
    "s_nop 0\n"
    "v_permlane16_swap_b32 v64, v66\n"
    "v_permlane16_swap_b32 v65, v67\n"
    "buffer_store_dwordx4 v[64:67], v4, s[36:39], s73 offen sc1\n"
    "v_mov_b32 v12, s68\n"
    "s_mov_b32 s73, 0x80000000\n"
    "s_mov_b32 s68, s71\n"
    "s_mov_b32 s69, 0\n"
    "s_mov_b32 s70, 0x80000000\n"
    "v_mov_b32 v7, v12\n"
    "s_waitcnt vmcnt(11)\n"
    "s_add_u32 s71, s68, 1\n"
    "v_add_u32 v13, v11, v3\n"
    "v_cmp_gt_i32 vcc, s71, v13\n"
    "s_nop 0\n"
    "s_or_b64 exec, vcc, s[62:63]\n"
    "buffer_atomic_smax v11, v2, s[36:39], 0 offen sc0 sc1\n"
    "s_mov_b64 exec, -1\n"
    "s_waitcnt vmcnt(3)\n"
    "s_branch .LgWO_bar1_%=\n"
    ".LgWO_dma0_75_%=:\n"
    "v_mfma_f32_16x16x32_bf16 v[48:51], v[172:175], v[20:23], v[48:51]\n"
    "s_add_u32 m0, s59, 32768\n"
    "v_mfma_f32_16x16x32_bf16 v[52:55], v[176:179], v[20:23], v[52:55]\n"
    "s_waitcnt lgkmcnt(2)\n"
    "buffer_load_dwordx4 v1, s[36:39], s76 offen sc1 lds\n"
    "v_mfma_f32_16x16x32_bf16 v[64:67], v[180:183], v[20:23], v[64:67]\n"
    "ds_read_b128 v[20:23], v0 offset:29696\n"
    "s_add_u32 s76, s76, 16384\n"
    "v_mfma_f32_16x16x32_bf16 v[48:51], v[184:187], v[24:27], v[48:51]\n"
    "s_add_u32 m0, s59, 36864\n"
    "v_mfma_f32_16x16x32_bf16 v[52:55], v[188:191], v[24:27], v[52:55]\n"
    "s_waitcnt lgkmcnt(2)\n"
    "buffer_load_dwordx4 v1, s[36:39], s76 offen sc1 lds\n"
    "v_mfma_f32_16x16x32_bf16 v[64:67], v[192:195], v[24:27], v[64:67]\n"
    "ds_read_b128 v[24:27], v0 offset:30720\n"
    "v_mfma_f32_16x16x32_bf16 v[48:51], v[196:199], v[28:31], v[48:51]\n"
    "s_add_u32 s76, s76, 16384\n"
    "s_add_u32 m0, s59, 40960\n"
    "v_mfma_f32_16x16x32_bf16 v[52:55], v[200:203], v[28:31], v[52:55]\n"
    "s_waitcnt lgkmcnt(2)\n"
    "buffer_load_dwordx4 v1, s[36:39], s76 offen sc1 lds\n"
    "v_mfma_f32_16x16x32_bf16 v[64:67], v[204:207], v[28:31], v[64:67]\n"
    "ds_read_b128 v[28:31], v0 offset:31744\n"
    "v_mfma_f32_16x16x32_bf16 v[48:51], v[208:211], v[16:19], v[48:51]\n"
    "v_mfma_f32_16x16x32_bf16 v[52:55], v[212:215], v[16:19], v[52:55]\n"
    "s_waitcnt lgkmcnt(2)\n"
    "s_add_u32 s76, s76, 16384\n"
    "s_add_u32 m0, s59, 45056\n"
    "v_mfma_f32_16x16x32_bf16 v[64:67], v[216:219], v[16:19], v[64:67]\n"
    "buffer_load_dwordx4 v1, s[36:39], s76 offen sc1 lds\n"
    "v_mfma_f32_16x16x32_bf16 v[48:51], v[220:223], v[20:23], v[48:51]\n"
    "v_mfma_f32_16x16x32_bf16 v[52:55], v[224:227], v[20:23], v[52:55]\n"
    "s_waitcnt lgkmcnt(1)\n"
    "v_mfma_f32_16x16x32_bf16 v[64:67], v[228:231], v[20:23], v[64:67]\n"
    "s_add_u32 s76, s76, 16384\n"
    "v_mfma_f32_16x16x32_bf16 v[48:51], v[232:235], v[24:27], v[48:51]\n"
    "s_add_u32 m0, s59, 49152\n"
    "v_mfma_f32_16x16x32_bf16 v[52:55], v[236:239], v[24:27], v[52:55]\n"
    "s_waitcnt lgkmcnt(0)\n"
    "buffer_load_dwordx4 v1, s[36:39], s76 offen sc1 lds\n"
    "v_mfma_f32_16x16x32_bf16 v[64:67], v[240:243], v[24:27], v[64:67]\n"
    "v_mfma_f32_16x16x32_bf16 v[48:51], v[244:247], v[28:31], v[48:51]\n"
    "s_add_u32 s76, s76, 16384\n"
    "s_add_u32 m0, s59, 53248\n"
    "v_mfma_f32_16x16x32_bf16 v[52:55], v[248:251], v[28:31], v[52:55]\n"
    "buffer_load_dwordx4 v1, s[36:39], s76 offen sc1 lds\n"
    "v_mfma_f32_16x16x32_bf16 v[64:67], v[252:255], v[28:31], v[64:67]\n"
    "v_cvt_pk_bf16_f32 v48, v48, v49\n"
    "v_cvt_pk_bf16_f32 v49, v50, v51\n"
    "s_nop 1\n"
    "v_cvt_pk_bf16_f32 v50, v52, v53\n"
    "v_cvt_pk_bf16_f32 v51, v54, v55\n"
    "buffer_store_dwordx4 v[48:51], v15, s[36:39], s73 offen sc1\n"
    "s_nop 0\n"
    "v_cvt_pk_bf16_f32 v64, v64, v65\n"
    "v_cvt_pk_bf16_f32 v65, v66, v67\n"
    "s_nop 0\n"
    "v_permlane16_swap_b32 v64, v66\n"
    "v_permlane16_swap_b32 v65, v67\n"
    "buffer_store_dwordx4 v[64:67], v4, s[36:39], s73 offen sc1\n"
    "v_mov_b32 v12, s68\n"
    "s_mov_b32 s73, 0x80000000\n"
    "s_mov_b32 s68, s71\n"
    "s_add_u32 m0, s59, 57344\n"
    "s_add_u32 s76, s76, 16384\n"
    "buffer_load_dwordx4 v1, s[36:39], s76 offen sc1 lds\n"
    "s_add_u32 m0, s59, 61440\n"
    "s_add_u32 s76, s76, 16384\n"
    "buffer_load_dwordx4 v1, s[36:39], s76 offen sc1 lds\n"
    "s_waitcnt vmcnt(2)\n"
    "buffer_atomic_smax v12, v14, s[36:39], 0 offen sc1\n"
    "s_mov_b32 s69, 0x80000000\n"
    "s_mov_b32 s70, 0x80000000\n"
    "v_mov_b32 v7, v12\n"
    "s_waitcnt vmcnt(12)\n"
    "s_add_u32 s71, s68, 1\n"
    "v_add_u32 v13, v11, v3\n"
    "v_cmp_gt_i32 vcc, s71, v13\n"
    "s_nop 0\n"
    "s_or_b64 exec, vcc, s[62:63]\n"
    "buffer_atomic_smax v11, v2, s[36:39], 0 offen sc0 sc1\n"
    "s_mov_b64 exec, -1\n"
    "s_waitcnt vmcnt(2)\n"
    "s_branch .LgWO_bar1_%=\n"
    ".LgWO_idle0_%=:\n"
    "s_or_b64 exec, vcc, s[62:63]\n"
    "buffer_atomic_smax v11, v2, s[36:39], 0 offen sc0 sc1\n"
    "s_mov_b64 exec, -1\n"
    "s_waitcnt vmcnt(1)\n"
    "buffer_atomic_smax v12, v14, s[36:39], 0 offen sc1\n"
    "s_branch .LgWO_block0_0_%=\n"
    ".LgWO_block0_%=:\n"
    "v_add_u32 v13, v11, v3\n"
    "v_cmp_gt_i32 vcc, s71, v13\n"
    "s_nop 0\n"
    "s_or_b64 exec, vcc, s[62:63]\n"
    "buffer_atomic_smax v11, v2, s[36:39], 0 offen sc0 sc1\n"
    "s_mov_b64 exec, -1\n"
    "s_sleep 3\n"
    "s_waitcnt vmcnt(1)\n"
    ".LgWO_block0_0_%=:\n"
    "v_add_u32 v13, v5, v3\n"
    "v_cmp_gt_i32 vcc, s71, v13\n"
    "s_cbranch_vccz .LgWO_go0_%=\n"
    "v_add_u32 v13, v5, v3\n"
    "v_cmp_gt_i32 vcc, s71, v13\n"
    "s_nop 0\n"
    "s_or_b64 exec, vcc, s[62:63]\n"
    "buffer_atomic_smax v5, v2, s[36:39], 0 offen sc0 sc1\n"
    "s_mov_b64 exec, -1\n"
    "s_sleep 3\n"
    "s_waitcnt vmcnt(1)\n"
    ".LgWO_block0_1_%=:\n"
    "v_add_u32 v13, v11, v3\n"
    "v_cmp_gt_i32 vcc, s71, v13\n"
    "s_cbranch_vccz .LgWO_go0_%=\n"
    "s_sub_u32 s72, s72, 1\n"
    "s_cbranch_scc0 .LgWO_block0_%=\n"
    "s_mov_b32 s72, 0\n"
    ".LgWO_go0_%=:\n"
    "s_lshr_b32 s76, s71, 1\n"
    "s_and_b32 s76, s76, s54\n"
    "s_mul_i32 s76, s76, s53\n"
    "s_add_u32 s76, s76, s52\n"
    "s_add_u32 s76, s76, 1024\n"
    "s_add_u32 m0, s59, 32768\n"
    "s_nop 0\n"
    "buffer_load_dwordx4 v1, s[36:39], s76 offen sc1 lds\n"
    "s_add_u32 m0, s59, 36864\n"
    "s_add_u32 s76, s76, 16384\n"
    "buffer_load_dwordx4 v1, s[36:39], s76 offen sc1 lds\n"
    "s_add_u32 m0, s59, 40960\n"
    "s_add_u32 s76, s76, 16384\n"
    "buffer_load_dwordx4 v1, s[36:39], s76 offen sc1 lds\n"
    "s_add_u32 m0, s59, 45056\n"
    "s_add_u32 s76, s76, 16384\n"
    "buffer_load_dwordx4 v1, s[36:39], s76 offen sc1 lds\n"
    "s_add_u32 m0, s59, 49152\n"
    "s_add_u32 s76, s76, 16384\n"
    "buffer_load_dwordx4 v1, s[36:39], s76 offen sc1 lds\n"
    "s_add_u32 m0, s59, 53248\n"
    "s_add_u32 s76, s76, 16384\n"
    "buffer_load_dwordx4 v1, s[36:39], s76 offen sc1 lds\n"
    "s_add_u32 m0, s59, 57344\n"
    "s_add_u32 s76, s76, 16384\n"
    "buffer_load_dwordx4 v1, s[36:39], s76 offen sc1 lds\n"
    "s_add_u32 m0, s59, 61440\n"
    "s_add_u32 s76, s76, 16384\n"
    "buffer_load_dwordx4 v1, s[36:39], s76 offen sc1 lds\n"
    "s_mov_b32 s69, 0x80000000\n"
    "s_mov_b32 s70, 0x80000000\n"
    "v_mov_b32 v7, v12\n"
    "s_waitcnt vmcnt(8)\n"
    "s_add_u32 s71, s68, 1\n"
    "v_add_u32 v13, v11, v3\n"
    "v_cmp_gt_i32 vcc, s71, v13\n"
    "s_nop 0\n"
    "s_or_b64 exec, vcc, s[62:63]\n"
    "buffer_atomic_smax v11, v2, s[36:39], 0 offen sc0 sc1\n"
    "s_mov_b64 exec, -1\n"
    "s_waitcnt vmcnt(1)\n"
    "s_branch .LgWO_bar1_%=\n"
    ".LgWO_bar0_%=:\n"
    "s_barrier\n"
    "ds_read_b128 v[16:19], v0 offset:0\n"
    "ds_read_b128 v[20:23], v0 offset:1024\n"
    "ds_read_b128 v[24:27], v0 offset:2048\n"
    "ds_read_b128 v[28:31], v0 offset:3072\n"
    "v_add_u32 v13, v5, v3\n"
    "v_cmp_gt_i32 vcc, s71, v13\n"
    "s_nop 0\n"
    "s_or_b64 exec, vcc, s[62:63]\n"
    "buffer_atomic_smax v5, v2, s[36:39], 0 offen sc0 sc1\n"
    "s_mov_b64 exec, -1\n"
    "s_lshr_b32 s76, s71, 1\n"
    "s_and_b32 s76, s76, s54\n"
    "s_mul_i32 s76, s76, s53\n"
    "s_add_u32 s76, s76, s52\n"
    "s_add_u32 s76, s76, 1024\n"
    "s_waitcnt lgkmcnt(3)\n"
    "s_branch .LgWO_head0_%=\n"
    ".LgWO_poll1_%=:\n"
    "v_mfma_f32_16x16x32_bf16 v[56:59], a[48:51], v[16:19], v[56:59]\n"
    "s_or_b64 exec, vcc, s[62:63]\n"
    "buffer_atomic_smax v11, v2, s[36:39], 0 offen sc0 sc1\n"
    "s_mov_b64 exec, -1\n"
    "v_mfma_f32_16x16x32_bf16 v[60:63], a[52:55], v[16:19], v[60:63]\n"
    "s_waitcnt lgkmcnt(2)\n"
    "v_mfma_f32_16x16x32_bf16 v[68:71], a[56:59], v[16:19], v[68:71]\n"
    "ds_read_b128 v[16:19], v9 offset:8192\n"
    "v_mfma_f32_16x16x32_bf16 v[56:59], a[60:63], v[20:23], v[56:59]\n"
    "buffer_store_dwordx4 v[64:67], v4, s[36:39], s73 offen sc1\n"
    "v_mfma_f32_16x16x32_bf16 v[60:63], a[64:67], v[20:23], v[60:63]\n"
    "s_waitcnt lgkmcnt(2)\n"
    "s_lshr_b32 s73, s68, 1\n"
    "v_mfma_f32_16x16x32_bf16 v[68:71], a[68:71], v[20:23], v[68:71]\n"
    "ds_read_b128 v[20:23], v9 offset:9216\n"
    "s_and_b32 s73, s73, s57\n"
    "v_mfma_f32_16x16x32_bf16 v[56:59], a[72:75], v[24:27], v[56:59]\n"
    "s_mul_i32 s73, s73, s56\n"
    "v_mfma_f32_16x16x32_bf16 v[60:63], a[76:79], v[24:27], v[60:63]\n"
    "s_waitcnt lgkmcnt(2)\n"
    "s_add_u32 s73, s73, s55\n"
    "v_mfma_f32_16x16x32_bf16 v[68:71], a[80:83], v[24:27], v[68:71]\n"
    "ds_read_b128 v[24:27], v9 offset:10240\n"
    "v_mfma_f32_16x16x32_bf16 v[56:59], a[84:87], v[28:31], v[56:59]\n"
    "v_mfma_f32_16x16x32_bf16 v[60:63], a[88:91], v[28:31], v[60:63]\n"
    "s_waitcnt lgkmcnt(2)\n"
    "v_mfma_f32_16x16x32_bf16 v[68:71], a[92:95], v[28:31], v[68:71]\n"
    "ds_read_b128 v[28:31], v9 offset:11264\n"
    "v_mfma_f32_16x16x32_bf16 v[56:59], a[96:99], v[16:19], v[56:59]\n"
    "v_mfma_f32_16x16x32_bf16 v[60:63], a[100:103], v[16:19], v[60:63]\n"
    "s_waitcnt lgkmcnt(2)\n"
    "v_mfma_f32_16x16x32_bf16 v[68:71], a[104:107], v[16:19], v[68:71]\n"
    "ds_read_b128 v[16:19], v9 offset:12288\n"
    "v_mfma_f32_16x16x32_bf16 v[56:59], a[108:111], v[20:23], v[56:59]\n"
    "v_mfma_f32_16x16x32_bf16 v[60:63], a[112:115], v[20:23], v[60:63]\n"
    "s_waitcnt lgkmcnt(2)\n"
    "s_waitcnt vmcnt(4)\n"
    "v_add_u32 v13, v5, v3\n"
    "v_mfma_f32_16x16x32_bf16 v[68:71], a[116:119], v[20:23], v[68:71]\n"
    "ds_read_b128 v[20:23], v9 offset:13312\n"
    "v_cmp_gt_i32 vcc, s71, v13\n"
    "v_mfma_f32_16x16x32_bf16 v[56:59], a[120:123], v[24:27], v[56:59]\n"
    "v_mfma_f32_16x16x32_bf16 v[60:63], a[124:127], v[24:27], v[60:63]\n"
    "s_waitcnt lgkmcnt(2)\n"
    "v_mfma_f32_16x16x32_bf16 v[68:71], a[128:131], v[24:27], v[68:71]\n"
    "ds_read_b128 v[24:27], v9 offset:14336\n"
    "s_cbranch_vccz .LgWO_dma1_33_%=\n"
    "v_mfma_f32_16x16x32_bf16 v[56:59], a[132:135], v[28:31], v[56:59]\n"
    "s_or_b64 exec, vcc, s[62:63]\n"
    "buffer_atomic_smax v5, v2, s[36:39], 0 offen sc0 sc1\n"
    "s_mov_b64 exec, -1\n"
    "v_mfma_f32_16x16x32_bf16 v[60:63], a[136:139], v[28:31], v[60:63]\n"
    "s_waitcnt lgkmcnt(2)\n"
    "v_mfma_f32_16x16x32_bf16 v[68:71], a[140:143], v[28:31], v[68:71]\n"
    "ds_read_b128 v[28:31], v9 offset:15360\n"
    "v_mfma_f32_16x16x32_bf16 v[56:59], a[144:147], v[16:19], v[56:59]\n"
    "v_mfma_f32_16x16x32_bf16 v[60:63], a[148:151], v[16:19], v[60:63]\n"
    "s_waitcnt lgkmcnt(2)\n"
    "v_mfma_f32_16x16x32_bf16 v[68:71], a[152:155], v[16:19], v[68:71]\n"
    "ds_read_b128 v[16:19], v9 offset:16384\n"
    "v_mfma_f32_16x16x32_bf16 v[56:59], a[156:159], v[20:23], v[56:59]\n"
    "v_mfma_f32_16x16x32_bf16 v[60:63], a[160:163], v[20:23], v[60:63]\n"
    "s_waitcnt lgkmcnt(2)\n"
    "v_mfma_f32_16x16x32_bf16 v[68:71], a[164:167], v[20:23], v[68:71]\n"
    "ds_read_b128 v[20:23], v9 offset:17408\n"
    "v_mfma_f32_16x16x32_bf16 v[56:59], a[168:171], v[24:27], v[56:59]\n"
    "v_mfma_f32_16x16x32_bf16 v[60:63], a[172:175], v[24:27], v[60:63]\n"
    "s_waitcnt lgkmcnt(2)\n"
    "v_mfma_f32_16x16x32_bf16 v[68:71], a[176:179], v[24:27], v[68:71]\n"
    "ds_read_b128 v[24:27], v9 offset:18432\n"
    "v_mfma_f32_16x16x32_bf16 v[56:59], a[180:183], v[28:31], v[56:59]\n"
    "v_mfma_f32_16x16x32_bf16 v[60:63], a[184:187], v[28:31], v[60:63]\n"
    "s_waitcnt lgkmcnt(2)\n"
    "v_mfma_f32_16x16x32_bf16 v[68:71], a[188:191], v[28:31], v[68:71]\n"
    "ds_read_b128 v[28:31], v9 offset:19456\n"
    "v_mfma_f32_16x16x32_bf16 v[56:59], a[192:195], v[16:19], v[56:59]\n"
    "v_mfma_f32_16x16x32_bf16 v[60:63], a[196:199], v[16:19], v[60:63]\n"
    "s_waitcnt lgkmcnt(2)\n"
    "s_waitcnt vmcnt(2)\n"
    "v_add_u32 v13, v11, v3\n"
    "v_mfma_f32_16x16x32_bf16 v[68:71], a[200:203], v[16:19], v[68:71]\n"
    "ds_read_b128 v[16:19], v9 offset:20480\n"
    "v_cmp_gt_i32 vcc, s71, v13\n"
    "v_mfma_f32_16x16x32_bf16 v[56:59], a[204:207], v[20:23], v[56:59]\n"
    "v_mfma_f32_16x16x32_bf16 v[60:63], a[208:211], v[20:23], v[60:63]\n"
    "s_waitcnt lgkmcnt(2)\n"
    "v_mfma_f32_16x16x32_bf16 v[68:71], a[212:215], v[20:23], v[68:71]\n"
    "ds_read_b128 v[20:23], v9 offset:21504\n"
    "s_cbranch_vccz .LgWO_dma1_54_%=\n"
    "v_mfma_f32_16x16x32_bf16 v[56:59], a[216:219], v[24:27], v[56:59]\n"
    "s_or_b64 exec, vcc, s[62:63]\n"
    "buffer_atomic_smax v11, v2, s[36:39], 0 offen sc0 sc1\n"
    "s_mov_b64 exec, -1\n"
    "v_mfma_f32_16x16x32_bf16 v[60:63], a[220:223], v[24:27], v[60:63]\n"
    "s_waitcnt lgkmcnt(2)\n"
    "s_waitcnt vmcnt(2)\n"
    "buffer_atomic_smax v12, v14, s[36:39], s70 offen sc1\n"
    "v_mfma_f32_16x16x32_bf16 v[68:71], a[224:227], v[24:27], v[68:71]\n"
    "ds_read_b128 v[24:27], v9 offset:22528\n"
    "v_mfma_f32_16x16x32_bf16 v[56:59], a[228:231], v[28:31], v[56:59]\n"
    "v_mfma_f32_16x16x32_bf16 v[60:63], a[232:235], v[28:31], v[60:63]\n"
    "s_waitcnt lgkmcnt(2)\n"
    "v_mfma_f32_16x16x32_bf16 v[68:71], a[236:239], v[28:31], v[68:71]\n"
    "ds_read_b128 v[28:31], v9 offset:23552\n"
    "v_mfma_f32_16x16x32_bf16 v[56:59], a[240:243], v[16:19], v[56:59]\n"
    "v_mfma_f32_16x16x32_bf16 v[60:63], a[244:247], v[16:19], v[60:63]\n"
    "s_waitcnt lgkmcnt(2)\n"
    "v_mfma_f32_16x16x32_bf16 v[68:71], a[248:251], v[16:19], v[68:71]\n"
    "ds_read_b128 v[16:19], v9 offset:24576\n"
    "v_mfma_f32_16x16x32_bf16 v[56:59], a[252:255], v[20:23], v[56:59]\n"
    "v_mfma_f32_16x16x32_bf16 v[60:63], v[128:131], v[20:23], v[60:63]\n"
    "s_waitcnt lgkmcnt(2)\n"
    "v_mfma_f32_16x16x32_bf16 v[68:71], v[132:135], v[20:23], v[68:71]\n"
    "ds_read_b128 v[20:23], v9 offset:25600\n"
    "v_mfma_f32_16x16x32_bf16 v[56:59], v[136:139], v[24:27], v[56:59]\n"
    "v_mfma_f32_16x16x32_bf16 v[60:63], v[140:143], v[24:27], v[60:63]\n"
    "s_waitcnt lgkmcnt(2)\n"
    "v_mfma_f32_16x16x32_bf16 v[68:71], v[144:147], v[24:27], v[68:71]\n"
    "ds_read_b128 v[24:27], v9 offset:26624\n"
    "v_mfma_f32_16x16x32_bf16 v[56:59], v[148:151], v[28:31], v[56:59]\n"
    "v_mfma_f32_16x16x32_bf16 v[60:63], v[152:155], v[28:31], v[60:63]\n"
    "s_waitcnt lgkmcnt(2)\n"
    "s_waitcnt vmcnt(2)\n"
    "v_add_u32 v13, v5, v3\n"
    "v_mfma_f32_16x16x32_bf16 v[68:71], v[156:159], v[28:31], v[68:71]\n"
    "ds_read_b128 v[28:31], v9 offset:27648\n"
    "v_cmp_gt_i32 vcc, s71, v13\n"
    "v_mfma_f32_16x16x32_bf16 v[56:59], v[160:163], v[16:19], v[56:59]\n"
    "v_mfma_f32_16x16x32_bf16 v[60:63], v[164:167], v[16:19], v[60:63]\n"
    "s_waitcnt lgkmcnt(2)\n"
    "v_mfma_f32_16x16x32_bf16 v[68:71], v[168:171], v[16:19], v[68:71]\n"
    "ds_read_b128 v[16:19], v9 offset:28672\n"
    "s_cbranch_vccz .LgWO_dma1_75_%=\n"
    "v_mfma_f32_16x16x32_bf16 v[56:59], v[172:175], v[20:23], v[56:59]\n"
    "s_or_b64 exec, vcc, s[62:63]\n"
    "buffer_atomic_smax v5, v2, s[36:39], 0 offen sc0 sc1\n"
    "s_mov_b64 exec, -1\n"
    "v_mfma_f32_16x16x32_bf16 v[60:63], v[176:179], v[20:23], v[60:63]\n"
    "s_waitcnt lgkmcnt(2)\n"
    "v_mfma_f32_16x16x32_bf16 v[68:71], v[180:183], v[20:23], v[68:71]\n"
    "ds_read_b128 v[20:23], v9 offset:29696\n"
    "v_mfma_f32_16x16x32_bf16 v[56:59], v[184:187], v[24:27], v[56:59]\n"
    "v_mfma_f32_16x16x32_bf16 v[60:63], v[188:191], v[24:27], v[60:63]\n"
    "s_waitcnt lgkmcnt(2)\n"
    "v_mfma_f32_16x16x32_bf16 v[68:71], v[192:195], v[24:27], v[68:71]\n"
    "ds_read_b128 v[24:27], v9 offset:30720\n"
    "v_mfma_f32_16x16x32_bf16 v[56:59], v[196:199], v[28:31], v[56:59]\n"
    "v_mfma_f32_16x16x32_bf16 v[60:63], v[200:203], v[28:31], v[60:63]\n"
    "s_waitcnt lgkmcnt(2)\n"
    "v_mfma_f32_16x16x32_bf16 v[68:71], v[204:207], v[28:31], v[68:71]\n"
    "ds_read_b128 v[28:31], v9 offset:31744\n"
    "v_mfma_f32_16x16x32_bf16 v[56:59], v[208:211], v[16:19], v[56:59]\n"
    "v_mfma_f32_16x16x32_bf16 v[60:63], v[212:215], v[16:19], v[60:63]\n"
    "s_waitcnt lgkmcnt(2)\n"
    "v_mfma_f32_16x16x32_bf16 v[68:71], v[216:219], v[16:19], v[68:71]\n"
    "v_mfma_f32_16x16x32_bf16 v[56:59], v[220:223], v[20:23], v[56:59]\n"
    "v_mfma_f32_16x16x32_bf16 v[60:63], v[224:227], v[20:23], v[60:63]\n"
    "s_waitcnt lgkmcnt(1)\n"
    "v_mfma_f32_16x16x32_bf16 v[68:71], v[228:231], v[20:23], v[68:71]\n"
    "v_mfma_f32_16x16x32_bf16 v[56:59], v[232:235], v[24:27], v[56:59]\n"
    "v_mfma_f32_16x16x32_bf16 v[60:63], v[236:239], v[24:27], v[60:63]\n"
    "s_waitcnt lgkmcnt(0)\n"
    "v_mfma_f32_16x16x32_bf16 v[68:71], v[240:243], v[24:27], v[68:71]\n"
    "v_mfma_f32_16x16x32_bf16 v[56:59], v[244:247], v[28:31], v[56:59]\n"
    "v_mfma_f32_16x16x32_bf16 v[60:63], v[248:251], v[28:31], v[60:63]\n"
    "v_mfma_f32_16x16x32_bf16 v[68:71], v[252:255], v[28:31], v[68:71]\n"
    "v_cvt_pk_bf16_f32 v56, v56, v57\n"
    "v_cvt_pk_bf16_f32 v57, v58, v59\n"
    "s_nop 1\n"
    "v_cvt_pk_bf16_f32 v58, v60, v61\n"
    "v_cvt_pk_bf16_f32 v59, v62, v63\n"
    "buffer_store_dwordx4 v[56:59], v15, s[36:39], s73 offen offset:1024 sc1\n"
    "s_nop 0\n"
    "v_cvt_pk_bf16_f32 v68, v68, v69\n"
    "v_cvt_pk_bf16_f32 v69, v70, v71\n"
    "s_nop 0\n"
    "v_permlane16_swap_b32 v68, v70\n"
    "v_permlane16_swap_b32 v69, v71\n"
    "buffer_store_dwordx4 v[68:71], v4, s[36:39], s73 offen offset:1024 sc1\n"
    "v_mov_b32 v12, s68\n"
    "s_mov_b32 s73, 0x80000000\n"
    "s_mov_b32 s68, s71\n"
    "s_cmp_eq_u32 s68, 128\n"
    "s_cbranch_scc1 .LgWO_done_%=\n"
    "s_waitcnt vmcnt(4)\n"
    "v_add_u32 v13, v11, v3\n"
    "v_cmp_gt_i32 vcc, s71, v13\n"
    "s_cbranch_vccnz .LgWO_idle1_%=\n"
    "s_add_u32 m0, s59, 0\n"
    "s_nop 0\n"
    "buffer_load_dwordx4 v1, s[36:39], s76 offen sc1 lds\n"
    "s_add_u32 m0, s59, 4096\n"
    "s_add_u32 s76, s76, 16384\n"
    "buffer_load_dwordx4 v1, s[36:39], s76 offen sc1 lds\n"
    "s_add_u32 m0, s59, 8192\n"
    "s_add_u32 s76, s76, 16384\n"
    "buffer_load_dwordx4 v1, s[36:39], s76 offen sc1 lds\n"
    "s_add_u32 m0, s59, 12288\n"
    "s_add_u32 s76, s76, 16384\n"
    "buffer_load_dwordx4 v1, s[36:39], s76 offen sc1 lds\n"
    "s_add_u32 m0, s59, 16384\n"
    "s_add_u32 s76, s76, 16384\n"
    "buffer_load_dwordx4 v1, s[36:39], s76 offen sc1 lds\n"
    "s_add_u32 m0, s59, 20480\n"
    "s_add_u32 s76, s76, 16384\n"
    "buffer_load_dwordx4 v1, s[36:39], s76 offen sc1 lds\n"
    "s_add_u32 m0, s59, 24576\n"
    "s_add_u32 s76, s76, 16384\n"
    "buffer_load_dwordx4 v1, s[36:39], s76 offen sc1 lds\n"
    "s_add_u32 m0, s59, 28672\n"
    "s_add_u32 s76, s76, 16384\n"
    "buffer_load_dwordx4 v1, s[36:39], s76 offen sc1 lds\n"
    "s_waitcnt vmcnt(8)\n"
    "buffer_atomic_smax v12, v10, s[36:39], 0 offen sc1\n"
    "s_mov_b32 s69, 0x80000000\n"
    "s_mov_b32 s70, 0x80000000\n"
    "v_mov_b32 v7, v12\n"
    "s_waitcnt vmcnt(11)\n"
    "s_add_u32 s71, s68, 1\n"
    "v_add_u32 v13, v11, v3\n"
    "v_cmp_gt_i32 vcc, s71, v13\n"
    "s_nop 0\n"
    "s_or_b64 exec, vcc, s[62:63]\n"
    "buffer_atomic_smax v11, v2, s[36:39], 0 offen sc0 sc1\n"
    "s_mov_b64 exec, -1\n"
    "s_waitcnt vmcnt(2)\n"
    "s_branch .LgWO_bar0_%=\n"
    ".LgWO_dma1_33_%=:\n"
    "v_mfma_f32_16x16x32_bf16 v[56:59], a[132:135], v[28:31], v[56:59]\n"
    "s_add_u32 m0, s59, 0\n"
    "v_mfma_f32_16x16x32_bf16 v[60:63], a[136:139], v[28:31], v[60:63]\n"
    "s_waitcnt lgkmcnt(2)\n"
    "buffer_load_dwordx4 v1, s[36:39], s76 offen sc1 lds\n"
    "v_mfma_f32_16x16x32_bf16 v[68:71], a[140:143], v[28:31], v[68:71]\n"
    "ds_read_b128 v[28:31], v9 offset:15360\n"
    "s_add_u32 s76, s76, 16384\n"
    "v_mfma_f32_16x16x32_bf16 v[56:59], a[144:147], v[16:19], v[56:59]\n"
    "s_add_u32 m0, s59, 4096\n"
    "v_mfma_f32_16x16x32_bf16 v[60:63], a[148:151], v[16:19], v[60:63]\n"
    "s_waitcnt lgkmcnt(2)\n"
    "buffer_load_dwordx4 v1, s[36:39], s76 offen sc1 lds\n"
    "v_mfma_f32_16x16x32_bf16 v[68:71], a[152:155], v[16:19], v[68:71]\n"
    "ds_read_b128 v[16:19], v9 offset:16384\n"
    "v_mfma_f32_16x16x32_bf16 v[56:59], a[156:159], v[20:23], v[56:59]\n"
    "s_add_u32 s76, s76, 16384\n"
    "s_add_u32 m0, s59, 8192\n"
    "v_mfma_f32_16x16x32_bf16 v[60:63], a[160:163], v[20:23], v[60:63]\n"
    "s_waitcnt lgkmcnt(2)\n"
    "buffer_load_dwordx4 v1, s[36:39], s76 offen sc1 lds\n"
    "v_mfma_f32_16x16x32_bf16 v[68:71], a[164:167], v[20:23], v[68:71]\n"
    "ds_read_b128 v[20:23], v9 offset:17408\n"
    "v_mfma_f32_16x16x32_bf16 v[56:59], a[168:171], v[24:27], v[56:59]\n"
    "v_mfma_f32_16x16x32_bf16 v[60:63], a[172:175], v[24:27], v[60:63]\n"
    "s_waitcnt lgkmcnt(2)\n"
    "s_add_u32 s76, s76, 16384\n"
    "s_add_u32 m0, s59, 12288\n"
    "v_mfma_f32_16x16x32_bf16 v[68:71], a[176:179], v[24:27], v[68:71]\n"
    "ds_read_b128 v[24:27], v9 offset:18432\n"
    "buffer_load_dwordx4 v1, s[36:39], s76 offen sc1 lds\n"
    "v_mfma_f32_16x16x32_bf16 v[56:59], a[180:183], v[28:31], v[56:59]\n"
    "v_mfma_f32_16x16x32_bf16 v[60:63], a[184:187], v[28:31], v[60:63]\n"
    "s_waitcnt lgkmcnt(2)\n"
    "v_mfma_f32_16x16x32_bf16 v[68:71], a[188:191], v[28:31], v[68:71]\n"
    "ds_read_b128 v[28:31], v9 offset:19456\n"
    "s_add_u32 s76, s76, 16384\n"
    "v_mfma_f32_16x16x32_bf16 v[56:59], a[192:195], v[16:19], v[56:59]\n"
    "s_add_u32 m0, s59, 16384\n"
    "v_mfma_f32_16x16x32_bf16 v[60:63], a[196:199], v[16:19], v[60:63]\n"
    "s_waitcnt lgkmcnt(2)\n"
    "buffer_load_dwordx4 v1, s[36:39], s76 offen sc1 lds\n"
    "v_mfma_f32_16x16x32_bf16 v[68:71], a[200:203], v[16:19], v[68:71]\n"
    "ds_read_b128 v[16:19], v9 offset:20480\n"
    "v_mfma_f32_16x16x32_bf16 v[56:59], a[204:207], v[20:23], v[56:59]\n"
    "s_add_u32 s76, s76, 16384\n"
    "s_add_u32 m0, s59, 20480\n"
    "v_mfma_f32_16x16x32_bf16 v[60:63], a[208:211], v[20:23], v[60:63]\n"
    "s_waitcnt lgkmcnt(2)\n"
    "buffer_load_dwordx4 v1, s[36:39], s76 offen sc1 lds\n"
    "v_mfma_f32_16x16x32_bf16 v[68:71], a[212:215], v[20:23], v[68:71]\n"
    "ds_read_b128 v[20:23], v9 offset:21504\n"
    "v_mfma_f32_16x16x32_bf16 v[56:59], a[216:219], v[24:27], v[56:59]\n"
    "v_mfma_f32_16x16x32_bf16 v[60:63], a[220:223], v[24:27], v[60:63]\n"
    "s_waitcnt lgkmcnt(2)\n"
    "s_waitcnt vmcnt(6)\n"
    "buffer_atomic_smax v12, v14, s[36:39], s70 offen sc1\n"
    "v_mfma_f32_16x16x32_bf16 v[68:71], a[224:227], v[24:27], v[68:71]\n"
    "ds_read_b128 v[24:27], v9 offset:22528\n"
    "s_add_u32 s76, s76, 16384\n"
    "v_mfma_f32_16x16x32_bf16 v[56:59], a[228:231], v[28:31], v[56:59]\n"
    "s_add_u32 m0, s59, 24576\n"
    "v_mfma_f32_16x16x32_bf16 v[60:63], a[232:235], v[28:31], v[60:63]\n"
    "s_waitcnt lgkmcnt(2)\n"
    "buffer_load_dwordx4 v1, s[36:39], s76 offen sc1 lds\n"
    "v_mfma_f32_16x16x32_bf16 v[68:71], a[236:239], v[28:31], v[68:71]\n"
    "ds_read_b128 v[28:31], v9 offset:23552\n"
    "s_add_u32 s76, s76, 16384\n"
    "v_mfma_f32_16x16x32_bf16 v[56:59], a[240:243], v[16:19], v[56:59]\n"
    "s_add_u32 m0, s59, 28672\n"
    "v_mfma_f32_16x16x32_bf16 v[60:63], a[244:247], v[16:19], v[60:63]\n"
    "s_waitcnt lgkmcnt(2)\n"
    "buffer_load_dwordx4 v1, s[36:39], s76 offen sc1 lds\n"
    "v_mfma_f32_16x16x32_bf16 v[68:71], a[248:251], v[16:19], v[68:71]\n"
    "ds_read_b128 v[16:19], v9 offset:24576\n"
    "v_mfma_f32_16x16x32_bf16 v[56:59], a[252:255], v[20:23], v[56:59]\n"
    "v_mfma_f32_16x16x32_bf16 v[60:63], v[128:131], v[20:23], v[60:63]\n"
    "s_waitcnt lgkmcnt(2)\n"
    "v_mfma_f32_16x16x32_bf16 v[68:71], v[132:135], v[20:23], v[68:71]\n"
    "ds_read_b128 v[20:23], v9 offset:25600\n"
    "v_mfma_f32_16x16x32_bf16 v[56:59], v[136:139], v[24:27], v[56:59]\n"
    "v_mfma_f32_16x16x32_bf16 v[60:63], v[140:143], v[24:27], v[60:63]\n"
    "s_waitcnt lgkmcnt(2)\n"
    "v_mfma_f32_16x16x32_bf16 v[68:71], v[144:147], v[24:27], v[68:71]\n"
    "ds_read_b128 v[24:27], v9 offset:26624\n"
    "v_mfma_f32_16x16x32_bf16 v[56:59], v[148:151], v[28:31], v[56:59]\n"
    "v_mfma_f32_16x16x32_bf16 v[60:63], v[152:155], v[28:31], v[60:63]\n"
    "s_waitcnt lgkmcnt(2)\n"
    "v_mfma_f32_16x16x32_bf16 v[68:71], v[156:159], v[28:31], v[68:71]\n"
    "ds_read_b128 v[28:31], v9 offset:27648\n"
    "v_mfma_f32_16x16x32_bf16 v[56:59], v[160:163], v[16:19], v[56:59]\n"
    "v_mfma_f32_16x16x32_bf16 v[60:63], v[164:167], v[16:19], v[60:63]\n"
    "s_waitcnt lgkmcnt(2)\n"
    "v_mfma_f32_16x16x32_bf16 v[68:71], v[168:171], v[16:19], v[68:71]\n"
    "ds_read_b128 v[16:19], v9 offset:28672\n"
    "v_mfma_f32_16x16x32_bf16 v[56:59], v[172:175], v[20:23], v[56:59]\n"
    "v_mfma_f32_16x16x32_bf16 v[60:63], v[176:179], v[20:23], v[60:63]\n"
    "s_waitcnt lgkmcnt(2)\n"
    "v_mfma_f32_16x16x32_bf16 v[68:71], v[180:183], v[20:23], v[68:71]\n"
    "ds_read_b128 v[20:23], v9 offset:29696\n"
    "v_mfma_f32_16x16x32_bf16 v[56:59], v[184:187], v[24:27], v[56:59]\n"
    "v_mfma_f32_16x16x32_bf16 v[60:63], v[188:191], v[24:27], v[60:63]\n"
    "s_waitcnt lgkmcnt(2)\n"
    "v_mfma_f32_16x16x32_bf16 v[68:71], v[192:195], v[24:27], v[68:71]\n"
    "ds_read_b128 v[24:27], v9 offset:30720\n"
    "v_mfma_f32_16x16x32_bf16 v[56:59], v[196:199], v[28:31], v[56:59]\n"
    "v_mfma_f32_16x16x32_bf16 v[60:63], v[200:203], v[28:31], v[60:63]\n"
    "s_waitcnt lgkmcnt(2)\n"
    "v_mfma_f32_16x16x32_bf16 v[68:71], v[204:207], v[28:31], v[68:71]\n"
    "ds_read_b128 v[28:31], v9 offset:31744\n"
    "v_mfma_f32_16x16x32_bf16 v[56:59], v[208:211], v[16:19], v[56:59]\n"
    "v_mfma_f32_16x16x32_bf16 v[60:63], v[212:215], v[16:19], v[60:63]\n"
    "s_waitcnt lgkmcnt(2)\n"
    "v_mfma_f32_16x16x32_bf16 v[68:71], v[216:219], v[16:19], v[68:71]\n"
    "v_mfma_f32_16x16x32_bf16 v[56:59], v[220:223], v[20:23], v[56:59]\n"
    "v_mfma_f32_16x16x32_bf16 v[60:63], v[224:227], v[20:23], v[60:63]\n"
    "s_waitcnt lgkmcnt(1)\n"
    "v_mfma_f32_16x16x32_bf16 v[68:71], v[228:231], v[20:23], v[68:71]\n"
    "v_mfma_f32_16x16x32_bf16 v[56:59], v[232:235], v[24:27], v[56:59]\n"
    "v_mfma_f32_16x16x32_bf16 v[60:63], v[236:239], v[24:27], v[60:63]\n"
    "s_waitcnt lgkmcnt(0)\n"
    "v_mfma_f32_16x16x32_bf16 v[68:71], v[240:243], v[24:27], v[68:71]\n"
    "v_mfma_f32_16x16x32_bf16 v[56:59], v[244:247], v[28:31], v[56:59]\n"
    "v_mfma_f32_16x16x32_bf16 v[60:63], v[248:251], v[28:31], v[60:63]\n"
    "v_mfma_f32_16x16x32_bf16 v[68:71], v[252:255], v[28:31], v[68:71]\n"
    "v_cvt_pk_bf16_f32 v56, v56, v57\n"
    "v_cvt_pk_bf16_f32 v57, v58, v59\n"
    "s_nop 1\n"
    "v_cvt_pk_bf16_f32 v58, v60, v61\n"
    "v_cvt_pk_bf16_f32 v59, v62, v63\n"
    "buffer_store_dwordx4 v[56:59], v15, s[36:39], s73 offen offset:1024 sc1\n"
    "s_nop 0\n"
    "v_cvt_pk_bf16_f32 v68, v68, v69\n"
    "v_cvt_pk_bf16_f32 v69, v70, v71\n"
    "s_nop 0\n"
    "v_permlane16_swap_b32 v68, v70\n"
    "v_permlane16_swap_b32 v69, v71\n"
    "buffer_store_dwordx4 v[68:71], v4, s[36:39], s73 offen offset:1024 sc1\n"
    "v_mov_b32 v12, s68\n"
    "s_mov_b32 s73, 0x80000000\n"
    "s_mov_b32 s68, s71\n"
    "s_mov_b32 s69, 0\n"
    "s_mov_b32 s70, 0x80000000\n"
    "v_mov_b32 v7, v12\n"
    "s_waitcnt vmcnt(12)\n"
    "s_add_u32 s71, s68, 1\n"
    "v_add_u32 v13, v11, v3\n"
    "v_cmp_gt_i32 vcc, s71, v13\n"
    "s_nop 0\n"
    "s_or_b64 exec, vcc, s[62:63]\n"
    "buffer_atomic_smax v11, v2, s[36:39], 0 offen sc0 sc1\n"
    "s_mov_b64 exec, -1\n"
    "s_waitcnt vmcnt(3)\n"
    "s_branch .LgWO_bar0_%=\n"
    ".LgWO_dma1_54_%=:\n"
    "v_mfma_f32_16x16x32_bf16 v[56:59], a[216:219], v[24:27], v[56:59]\n"
    "s_add_u32 m0, s59, 0\n"
    "v_mfma_f32_16x16x32_bf16 v[60:63], a[220:223], v[24:27], v[60:63]\n"
    "s_waitcnt lgkmcnt(2)\n"
    "s_waitcnt vmcnt(1)\n"
    "buffer_atomic_smax v12, v14, s[36:39], s70 offen sc1\n"
    "v_mfma_f32_16x16x32_bf16 v[68:71], a[224:227], v[24:27], v[68:71]\n"
    "ds_read_b128 v[24:27], v9 offset:22528\n"
    "buffer_load_dwordx4 v1, s[36:39], s76 offen sc1 lds\n"
    "v_mfma_f32_16x16x32_bf16 v[56:59], a[228:231], v[28:31], v[56:59]\n"
    "s_add_u32 s76, s76, 16384\n"
    "s_add_u32 m0, s59, 4096\n"
    "v_mfma_f32_16x16x32_bf16 v[60:63], a[232:235], v[28:31], v[60:63]\n"
    "s_waitcnt lgkmcnt(2)\n"
    "buffer_load_dwordx4 v1, s[36:39], s76 offen sc1 lds\n"
    "v_mfma_f32_16x16x32_bf16 v[68:71], a[236:239], v[28:31], v[68:71]\n"
    "ds_read_b128 v[28:31], v9 offset:23552\n"
    "v_mfma_f32_16x16x32_bf16 v[56:59], a[240:243], v[16:19], v[56:59]\n"
    "s_add_u32 s76, s76, 16384\n"
    "s_add_u32 m0, s59, 8192\n"
    "v_mfma_f32_16x16x32_bf16 v[60:63], a[244:247], v[16:19], v[60:63]\n"
    "s_waitcnt lgkmcnt(2)\n"
    "buffer_load_dwordx4 v1, s[36:39], s76 offen sc1 lds\n"
    "v_mfma_f32_16x16x32_bf16 v[68:71], a[248:251], v[16:19], v[68:71]\n"
    "ds_read_b128 v[16:19], v9 offset:24576\n"
    "v_mfma_f32_16x16x32_bf16 v[56:59], a[252:255], v[20:23], v[56:59]\n"
    "v_mfma_f32_16x16x32_bf16 v[60:63], v[128:131], v[20:23], v[60:63]\n"
    "s_waitcnt lgkmcnt(2)\n"
    "s_add_u32 s76, s76, 16384\n"
    "s_add_u32 m0, s59, 12288\n"
    "v_mfma_f32_16x16x32_bf16 v[68:71], v[132:135], v[20:23], v[68:71]\n"
    "ds_read_b128 v[20:23], v9 offset:25600\n"
    "buffer_load_dwordx4 v1, s[36:39], s76 offen sc1 lds\n"
    "v_mfma_f32_16x16x32_bf16 v[56:59], v[136:139], v[24:27], v[56:59]\n"
    "v_mfma_f32_16x16x32_bf16 v[60:63], v[140:143], v[24:27], v[60:63]\n"
    "s_waitcnt lgkmcnt(2)\n"
    "v_mfma_f32_16x16x32_bf16 v[68:71], v[144:147], v[24:27], v[68:71]\n"
    "ds_read_b128 v[24:27], v9 offset:26624\n"
    "s_add_u32 s76, s76, 16384\n"
    "v_mfma_f32_16x16x32_bf16 v[56:59], v[148:151], v[28:31], v[56:59]\n"
    "s_add_u32 m0, s59, 16384\n"
    "v_mfma_f32_16x16x32_bf16 v[60:63], v[152:155], v[28:31], v[60:63]\n"
    "s_waitcnt lgkmcnt(2)\n"
    "buffer_load_dwordx4 v1, s[36:39], s76 offen sc1 lds\n"
    "v_mfma_f32_16x16x32_bf16 v[68:71], v[156:159], v[28:31], v[68:71]\n"
    "ds_read_b128 v[28:31], v9 offset:27648\n"
    "v_mfma_f32_16x16x32_bf16 v[56:59], v[160:163], v[16:19], v[56:59]\n"
    "s_add_u32 s76, s76, 16384\n"
    "s_add_u32 m0, s59, 20480\n"
    "v_mfma_f32_16x16x32_bf16 v[60:63], v[164:167], v[16:19], v[60:63]\n"
    "s_waitcnt lgkmcnt(2)\n"
    "buffer_load_dwordx4 v1, s[36:39], s76 offen sc1 lds\n"
    "v_mfma_f32_16x16x32_bf16 v[68:71], v[168:171], v[16:19], v[68:71]\n"
    "ds_read_b128 v[16:19], v9 offset:28672\n"
    "v_mfma_f32_16x16x32_bf16 v[56:59], v[172:175], v[20:23], v[56:59]\n"
    "v_mfma_f32_16x16x32_bf16 v[60:63], v[176:179], v[20:23], v[60:63]\n"
    "s_waitcnt lgkmcnt(2)\n"
    "s_add_u32 s76, s76, 16384\n"
    "s_add_u32 m0, s59, 24576\n"
    "v_mfma_f32_16x16x32_bf16 v[68:71], v[180:183], v[20:23], v[68:71]\n"
    "ds_read_b128 v[20:23], v9 offset:29696\n"
    "buffer_load_dwordx4 v1, s[36:39], s76 offen sc1 lds\n"
    "v_mfma_f32_16x16x32_bf16 v[56:59], v[184:187], v[24:27], v[56:59]\n"
    "v_mfma_f32_16x16x32_bf16 v[60:63], v[188:191], v[24:27], v[60:63]\n"
    "s_waitcnt lgkmcnt(2)\n"
    "v_mfma_f32_16x16x32_bf16 v[68:71], v[192:195], v[24:27], v[68:71]\n"
    "ds_read_b128 v[24:27], v9 offset:30720\n"
    "s_add_u32 s76, s76, 16384\n"
    "v_mfma_f32_16x16x32_bf16 v[56:59], v[196:199], v[28:31], v[56:59]\n"
    "s_add_u32 m0, s59, 28672\n"
    "v_mfma_f32_16x16x32_bf16 v[60:63], v[200:203], v[28:31], v[60:63]\n"
    "s_waitcnt lgkmcnt(2)\n"
    "buffer_load_dwordx4 v1, s[36:39], s76 offen sc1 lds\n"
    "v_mfma_f32_16x16x32_bf16 v[68:71], v[204:207], v[28:31], v[68:71]\n"
    "ds_read_b128 v[28:31], v9 offset:31744\n"
    "v_mfma_f32_16x16x32_bf16 v[56:59], v[208:211], v[16:19], v[56:59]\n"
    "v_mfma_f32_16x16x32_bf16 v[60:63], v[212:215], v[16:19], v[60:63]\n"
    "s_waitcnt lgkmcnt(2)\n"
    "v_mfma_f32_16x16x32_bf16 v[68:71], v[216:219], v[16:19], v[68:71]\n"
    "v_mfma_f32_16x16x32_bf16 v[56:59], v[220:223], v[20:23], v[56:59]\n"
    "v_mfma_f32_16x16x32_bf16 v[60:63], v[224:227], v[20:23], v[60:63]\n"
    "s_waitcnt lgkmcnt(1)\n"
    "v_mfma_f32_16x16x32_bf16 v[68:71], v[228:231], v[20:23], v[68:71]\n"
    "v_mfma_f32_16x16x32_bf16 v[56:59], v[232:235], v[24:27], v[56:59]\n"
    "v_mfma_f32_16x16x32_bf16 v[60:63], v[236:239], v[24:27], v[60:63]\n"
    "s_waitcnt lgkmcnt(0)\n"
    "v_mfma_f32_16x16x32_bf16 v[68:71], v[240:243], v[24:27], v[68:71]\n"
    "v_mfma_f32_16x16x32_bf16 v[56:59], v[244:247], v[28:31], v[56:59]\n"
    "v_mfma_f32_16x16x32_bf16 v[60:63], v[248:251], v[28:31], v[60:63]\n"
    "v_mfma_f32_16x16x32_bf16 v[68:71], v[252:255], v[28:31], v[68:71]\n"
    "v_cvt_pk_bf16_f32 v56, v56, v57\n"
    "v_cvt_pk_bf16_f32 v57, v58, v59\n"
    "s_nop 1\n"
    "v_cvt_pk_bf16_f32 v58, v60, v61\n"
    "v_cvt_pk_bf16_f32 v59, v62, v63\n"
    "buffer_store_dwordx4 v[56:59], v15, s[36:39], s73 offen offset:1024 sc1\n"
    "s_nop 0\n"
    "v_cvt_pk_bf16_f32 v68, v68, v69\n"
    "v_cvt_pk_bf16_f32 v69, v70, v71\n"
    "s_nop 0\n"
    "v_permlane16_swap_b32 v68, v70\n"
    "v_permlane16_swap_b32 v69, v71\n"
    "buffer_store_dwordx4 v[68:71], v4, s[36:39], s73 offen offset:1024 sc1\n"
    "v_mov_b32 v12, s68\n"
    "s_mov_b32 s73, 0x80000000\n"
    "s_mov_b32 s68, s71\n"
    "s_mov_b32 s69, 0\n"
    "s_mov_b32 s70, 0x80000000\n"
    "v_mov_b32 v7, v12\n"
    "s_waitcnt vmcnt(11)\n"
    "s_add_u32 s71, s68, 1\n"
    "v_add_u32 v13, v11, v3\n"
    "v_cmp_gt_i32 vcc, s71, v13\n"
    "s_nop 0\n"
    "s_or_b64 exec, vcc, s[62:63]\n"
    "buffer_atomic_smax v11, v2, s[36:39], 0 offen sc0 sc1\n"
    "s_mov_b64 exec, -1\n"
    "s_waitcnt vmcnt(3)\n"
    "s_branch .LgWO_bar0_%=\n"
    ".LgWO_dma1_75_%=:\n"
    "v_mfma_f32_16x16x32_bf16 v[56:59], v[172:175], v[20:23], v[56:59]\n"
    "s_add_u32 m0, s59, 0\n"
    "v_mfma_f32_16x16x32_bf16 v[60:63], v[176:179], v[20:23], v[60:63]\n"
    "s_waitcnt lgkmcnt(2)\n"
    "buffer_load_dwordx4 v1, s[36:39], s76 offen sc1 lds\n"
    "v_mfma_f32_16x16x32_bf16 v[68:71], v[180:183], v[20:23], v[68:71]\n"
    "ds_read_b128 v[20:23], v9 offset:29696\n"
    "s_add_u32 s76, s76, 16384\n"
    "v_mfma_f32_16x16x32_bf16 v[56:59], v[184:187], v[24:27], v[56:59]\n"
    "s_add_u32 m0, s59, 4096\n"
    "v_mfma_f32_16x16x32_bf16 v[60:63], v[188:191], v[24:27], v[60:63]\n"
    "s_waitcnt lgkmcnt(2)\n"
    "buffer_load_dwordx4 v1, s[36:39], s76 offen sc1 lds\n"
    "v_mfma_f32_16x16x32_bf16 v[68:71], v[192:195], v[24:27], v[68:71]\n"
    "ds_read_b128 v[24:27], v9 offset:30720\n"
    "v_mfma_f32_16x16x32_bf16 v[56:59], v[196:199], v[28:31], v[56:59]\n"
    "s_add_u32 s76, s76, 16384\n"
    "s_add_u32 m0, s59, 8192\n"
    "v_mfma_f32_16x16x32_bf16 v[60:63], v[200:203], v[28:31], v[60:63]\n"
    "s_waitcnt lgkmcnt(2)\n"
    "buffer_load_dwordx4 v1, s[36:39], s76 offen sc1 lds\n"
    "v_mfma_f32_16x16x32_bf16 v[68:71], v[204:207], v[28:31], v[68:71]\n"
    "ds_read_b128 v[28:31], v9 offset:31744\n"
    "v_mfma_f32_16x16x32_bf16 v[56:59], v[208:211], v[16:19], v[56:59]\n"
    "v_mfma_f32_16x16x32_bf16 v[60:63], v[212:215], v[16:19], v[60:63]\n"
    "s_waitcnt lgkmcnt(2)\n"
    "s_add_u32 s76, s76, 16384\n"
    "s_add_u32 m0, s59, 12288\n"
    "v_mfma_f32_16x16x32_bf16 v[68:71], v[216:219], v[16:19], v[68:71]\n"
    "buffer_load_dwordx4 v1, s[36:39], s76 offen sc1 lds\n"
    "v_mfma_f32_16x16x32_bf16 v[56:59], v[220:223], v[20:23], v[56:59]\n"
    "v_mfma_f32_16x16x32_bf16 v[60:63], v[224:227], v[20:23], v[60:63]\n"
    "s_waitcnt lgkmcnt(1)\n"
    "v_mfma_f32_16x16x32_bf16 v[68:71], v[228:231], v[20:23], v[68:71]\n"
    "s_add_u32 s76, s76, 16384\n"
    "v_mfma_f32_16x16x32_bf16 v[56:59], v[232:235], v[24:27], v[56:59]\n"
    "s_add_u32 m0, s59, 16384\n"
    "v_mfma_f32_16x16x32_bf16 v[60:63], v[236:239], v[24:27], v[60:63]\n"
    "s_waitcnt lgkmcnt(0)\n"
    "buffer_load_dwordx4 v1, s[36:39], s76 offen sc1 lds\n"
    "v_mfma_f32_16x16x32_bf16 v[68:71], v[240:243], v[24:27], v[68:71]\n"
    "v_mfma_f32_16x16x32_bf16 v[56:59], v[244:247], v[28:31], v[56:59]\n"
    "s_add_u32 s76, s76, 16384\n"
    "s_add_u32 m0, s59, 20480\n"
    "v_mfma_f32_16x16x32_bf16 v[60:63], v[248:251], v[28:31], v[60:63]\n"
    "buffer_load_dwordx4 v1, s[36:39], s76 offen sc1 lds\n"
    "v_mfma_f32_16x16x32_bf16 v[68:71], v[252:255], v[28:31], v[68:71]\n"
    "v_cvt_pk_bf16_f32 v56, v56, v57\n"
    "v_cvt_pk_bf16_f32 v57, v58, v59\n"
    "s_nop 1\n"
    "v_cvt_pk_bf16_f32 v58, v60, v61\n"
    "v_cvt_pk_bf16_f32 v59, v62, v63\n"
    "buffer_store_dwordx4 v[56:59], v15, s[36:39], s73 offen offset:1024 sc1\n"
    "s_nop 0\n"
    "v_cvt_pk_bf16_f32 v68, v68, v69\n"
    "v_cvt_pk_bf16_f32 v69, v70, v71\n"
    "s_nop 0\n"
    "v_permlane16_swap_b32 v68, v70\n"
    "v_permlane16_swap_b32 v69, v71\n"
    "buffer_store_dwordx4 v[68:71], v4, s[36:39], s73 offen offset:1024 sc1\n"
    "v_mov_b32 v12, s68\n"
    "s_mov_b32 s73, 0x80000000\n"
    "s_mov_b32 s68, s71\n"
    "s_add_u32 m0, s59, 24576\n"
    "s_add_u32 s76, s76, 16384\n"
    "buffer_load_dwordx4 v1, s[36:39], s76 offen sc1 lds\n"
    "s_add_u32 m0, s59, 28672\n"
    "s_add_u32 s76, s76, 16384\n"
    "buffer_load_dwordx4 v1, s[36:39], s76 offen sc1 lds\n"
    "s_waitcnt vmcnt(2)\n"
    "buffer_atomic_smax v12, v10, s[36:39], 0 offen sc1\n"
    "s_mov_b32 s69, 0x80000000\n"
    "s_mov_b32 s70, 0x80000000\n"
    "v_mov_b32 v7, v12\n"
    "s_waitcnt vmcnt(12)\n"
    "s_add_u32 s71, s68, 1\n"
    "v_add_u32 v13, v11, v3\n"
    "v_cmp_gt_i32 vcc, s71, v13\n"
    "s_nop 0\n"
    "s_or_b64 exec, vcc, s[62:63]\n"
    "buffer_atomic_smax v11, v2, s[36:39], 0 offen sc0 sc1\n"
    "s_mov_b64 exec, -1\n"
    "s_waitcnt vmcnt(2)\n"
    "s_branch .LgWO_bar0_%=\n"
    ".LgWO_idle1_%=:\n"
    "s_or_b64 exec, vcc, s[62:63]\n"
    "buffer_atomic_smax v11, v2, s[36:39], 0 offen sc0 sc1\n"
    "s_mov_b64 exec, -1\n"
    "s_waitcnt vmcnt(1)\n"
    "buffer_atomic_smax v12, v10, s[36:39], 0 offen sc1\n"
    "s_branch .LgWO_block1_0_%=\n"
    ".LgWO_block1_%=:\n"
    "v_add_u32 v13, v11, v3\n"
    "v_cmp_gt_i32 vcc, s71, v13\n"
    "s_nop 0\n"
    "s_or_b64 exec, vcc, s[62:63]\n"
    "buffer_atomic_smax v11, v2, s[36:39], 0 offen sc0 sc1\n"
    "s_mov_b64 exec, -1\n"
    "s_sleep 3\n"
    "s_waitcnt vmcnt(1)\n"
    ".LgWO_block1_0_%=:\n"
    "v_add_u32 v13, v5, v3\n"
    "v_cmp_gt_i32 vcc, s71, v13\n"
    "s_cbranch_vccz .LgWO_go1_%=\n"
    "v_add_u32 v13, v5, v3\n"
    "v_cmp_gt_i32 vcc, s71, v13\n"
    "s_nop 0\n"
    "s_or_b64 exec, vcc, s[62:63]\n"
    "buffer_atomic_smax v5, v2, s[36:39], 0 offen sc0 sc1\n"
    "s_mov_b64 exec, -1\n"
    "s_sleep 3\n"
    "s_waitcnt vmcnt(1)\n"
    ".LgWO_block1_1_%=:\n"
    "v_add_u32 v13, v11, v3\n"
    "v_cmp_gt_i32 vcc, s71, v13\n"
    "s_cbranch_vccz .LgWO_go1_%=\n"
    "s_sub_u32 s72, s72, 1\n"
    "s_cbranch_scc0 .LgWO_block1_%=\n"
    "s_mov_b32 s72, 0\n"
    ".LgWO_go1_%=:\n"
    "s_lshr_b32 s76, s71, 1\n"
    "s_and_b32 s76, s76, s54\n"
    "s_mul_i32 s76, s76, s53\n"
    "s_add_u32 s76, s76, s52\n"
    "s_add_u32 m0, s59, 0\n"
    "s_nop 0\n"
    "buffer_load_dwordx4 v1, s[36:39], s76 offen sc1 lds\n"
    "s_add_u32 m0, s59, 4096\n"
    "s_add_u32 s76, s76, 16384\n"
    "buffer_load_dwordx4 v1, s[36:39], s76 offen sc1 lds\n"
    "s_add_u32 m0, s59, 8192\n"
    "s_add_u32 s76, s76, 16384\n"
    "buffer_load_dwordx4 v1, s[36:39], s76 offen sc1 lds\n"
    "s_add_u32 m0, s59, 12288\n"
    "s_add_u32 s76, s76, 16384\n"
    "buffer_load_dwordx4 v1, s[36:39], s76 offen sc1 lds\n"
    "s_add_u32 m0, s59, 16384\n"
    "s_add_u32 s76, s76, 16384\n"
    "buffer_load_dwordx4 v1, s[36:39], s76 offen sc1 lds\n"
    "s_add_u32 m0, s59, 20480\n"
    "s_add_u32 s76, s76, 16384\n"
    "buffer_load_dwordx4 v1, s[36:39], s76 offen sc1 lds\n"
    "s_add_u32 m0, s59, 24576\n"
    "s_add_u32 s76, s76, 16384\n"
    "buffer_load_dwordx4 v1, s[36:39], s76 offen sc1 lds\n"
    "s_add_u32 m0, s59, 28672\n"
    "s_add_u32 s76, s76, 16384\n"
    "buffer_load_dwordx4 v1, s[36:39], s76 offen sc1 lds\n"
    "s_mov_b32 s69, 0x80000000\n"
    "s_mov_b32 s70, 0x80000000\n"
    "v_mov_b32 v7, v12\n"
    "s_waitcnt vmcnt(8)\n"
    "s_add_u32 s71, s68, 1\n"
    "v_add_u32 v13, v11, v3\n"
    "v_cmp_gt_i32 vcc, s71, v13\n"
    "s_nop 0\n"
    "s_or_b64 exec, vcc, s[62:63]\n"
    "buffer_atomic_smax v11, v2, s[36:39], 0 offen sc0 sc1\n"
    "s_mov_b64 exec, -1\n"
    "s_waitcnt vmcnt(1)\n"
    "s_branch .LgWO_bar0_%=\n"
    ".LgWO_bar1_%=:\n"
    "s_barrier\n"
    "ds_read_b128 v[16:19], v9 offset:0\n"
    "ds_read_b128 v[20:23], v9 offset:1024\n"
    "ds_read_b128 v[24:27], v9 offset:2048\n"
    "ds_read_b128 v[28:31], v9 offset:3072\n"
    "v_add_u32 v13, v5, v3\n"
    "v_cmp_gt_i32 vcc, s71, v13\n"
    "s_nop 0\n"
    "s_or_b64 exec, vcc, s[62:63]\n"
    "buffer_atomic_smax v5, v2, s[36:39], 0 offen sc0 sc1\n"
    "s_mov_b64 exec, -1\n"
    "s_lshr_b32 s76, s71, 1\n"
    "s_and_b32 s76, s76, s54\n"
    "s_mul_i32 s76, s76, s53\n"
    "s_add_u32 s76, s76, s52\n"
    "s_waitcnt lgkmcnt(3)\n"
    "s_branch .LgWO_head1_%=\n"
    ".LgWO_FIN_%=:\n"
  );
}
DEVI void role_down(const Params& p, int L, int j, int i, u32 dbg, unsigned long long tag) {
  const int lane = threadIdx.x & 63, w = threadIdx.x >> 6, e0 = j * 1152 * 576 + i * 192;
  GEMM_DECL;
  g_w0 = make_srd(p.w2 + e0, 4608u * 576 * 2 - e0 * 2); g_w1 = g_w0; g_w2 = g_w0;
  g_ldw = 1152;
  g_in_base = pgb(L, B_H + 9 * j); g_in_stride = SLOT; g_in_mask = RH - 1;
  g_out_base = pgb(L, B_PF + 5 * j); g_out_kb = 12 * i; g_out_mask = RP - 1;
  g_flag = lane < 4 ? mbox(L, lane, SL_REDF, 12 * j + 4 * i + w) : ((lane < 10 && w == 0) ? mbox(L, j, SL_UP + lane - 4, 16 + i) : OOB);
  g_paddr = mbox(L, j, SL_DOWN + i, lane);
  g_pbias = lane < 24 ? 0 : (lane < 40 ? 2 * RP : 1000);
  v16u sa, sb, va;
  gemm_args(GEMM_ARGS, p, dbg, tag, sa, sb, va);
  RUN_ASM(
    "s_nop 4\n"
    "s_getpc_b64 s[84:85]\n"
    ".LgDOWN_PC_%=:\n"
    "s_and_b32 s85, s85, 0xffff\n"
    "s_mov_b32 s86, .LgDOWN_FIN_%=-.LgDOWN_PC_%=\n"
    "s_mov_b32 s87, 0x20000\n"
    "v_mbcnt_lo_u32_b32 v40, -1, 0\n"
    "v_mbcnt_hi_u32_b32 v40, -1, v40\n"
    "v_lshlrev_b32 v40, 7, v40\n"
    "buffer_load_dword v41, v40, s[84:87], 0 offen\n"
    "v_add_u32 v40, 8192, v40\n"
    "buffer_load_dword v41, v40, s[84:87], 0 offen\n"
    "v_add_u32 v40, 8192, v40\n"
    "buffer_load_dword v41, v40, s[84:87], 0 offen\n"
    "s_mov_b32 s68, 0\n"
    "s_mov_b32 s71, 0\n"
    "v_add_u32 v9, 36864, v0\n"
    "s_mov_b32 s64, 0xbfb8aa3b\n"
    "s_mov_b32 s72, 262144\n"
    "s_add_u32 m0, s59, 0\n"
    "s_mov_b32 s74, 0\n"
    "buffer_load_dwordx4 v5, s[40:43], s74 offen lds\n"
    "s_add_u32 m0, s59, 4096\n"
    "s_mov_b32 s74, 128\n"
    "buffer_load_dwordx4 v5, s[40:43], s74 offen lds\n"
    "s_add_u32 m0, s59, 8192\n"
    "s_mov_b32 s74, 256\n"
    "buffer_load_dwordx4 v5, s[40:43], s74 offen lds\n"
    "s_add_u32 m0, s59, 12288\n"
    "s_mov_b32 s74, 36864\n"
    "buffer_load_dwordx4 v5, s[40:43], s74 offen lds\n"
    "s_add_u32 m0, s59, 16384\n"
    "s_mov_b32 s74, 36992\n"
    "buffer_load_dwordx4 v5, s[40:43], s74 offen lds\n"
    "s_add_u32 m0, s59, 20480\n"
    "s_mov_b32 s74, 37120\n"
    "buffer_load_dwordx4 v5, s[40:43], s74 offen lds\n"
    "s_add_u32 m0, s59, 24576\n"
    "s_mov_b32 s74, 73728\n"
    "buffer_load_dwordx4 v5, s[40:43], s74 offen lds\n"
    "s_add_u32 m0, s59, 28672\n"
    "s_mov_b32 s74, 73856\n"
    "buffer_load_dwordx4 v5, s[40:43], s74 offen lds\n"
    "s_add_u32 m0, s59, 32768\n"
    "s_mov_b32 s74, 73984\n"
    "buffer_load_dwordx4 v5, s[40:43], s74 offen lds\n"
    "s_add_u32 m0, s59, 36864\n"
    "s_mov_b32 s74, 110592\n"
    "buffer_load_dwordx4 v5, s[40:43], s74 offen lds\n"
    "s_add_u32 m0, s59, 40960\n"
    "s_mov_b32 s74, 110720\n"
    "buffer_load_dwordx4 v5, s[40:43], s74 offen lds\n"
    "s_add_u32 m0, s59, 45056\n"
    "s_mov_b32 s74, 110848\n"
    "buffer_load_dwordx4 v5, s[40:43], s74 offen lds\n"
    "s_add_u32 m0, s59, 49152\n"
    "s_mov_b32 s74, 147456\n"
    "buffer_load_dwordx4 v5, s[40:43], s74 offen lds\n"
    "s_add_u32 m0, s59, 53248\n"
    "s_mov_b32 s74, 147584\n"
    "buffer_load_dwordx4 v5, s[40:43], s74 offen lds\n"
    "s_add_u32 m0, s59, 57344\n"
    "s_mov_b32 s74, 147712\n"
    "buffer_load_dwordx4 v5, s[40:43], s74 offen lds\n"
    "s_add_u32 m0, s59, 61440\n"
    "s_mov_b32 s74, 184320\n"
    "buffer_load_dwordx4 v5, s[40:43], s74 offen lds\n"
    "s_add_u32 m0, s59, 65536\n"
    "s_mov_b32 s74, 184448\n"
    "buffer_load_dwordx4 v5, s[40:43], s74 offen lds\n"
    "s_add_u32 m0, s59, 69632\n"
    "s_mov_b32 s74, 184576\n"
    "buffer_load_dwordx4 v5, s[40:43], s74 offen lds\n"
    "s_add_u32 m0, s59, 73728\n"
    "s_mov_b32 s74, 221184\n"
    "buffer_load_dwordx4 v5, s[40:43], s74 offen lds\n"
    "s_add_u32 m0, s59, 77824\n"
    "s_mov_b32 s74, 221312\n"
    "buffer_load_dwordx4 v5, s[40:43], s74 offen lds\n"
    "s_add_u32 m0, s59, 81920\n"
    "s_mov_b32 s74, 221440\n"
    "buffer_load_dwordx4 v5, s[40:43], s74 offen lds\n"
    "s_add_u32 m0, s59, 86016\n"
    "s_mov_b32 s74, 258048\n"
    "buffer_load_dwordx4 v5, s[40:43], s74 offen lds\n"
    "s_add_u32 m0, s59, 90112\n"
    "s_mov_b32 s74, 258176\n"
    "buffer_load_dwordx4 v5, s[40:43], s74 offen lds\n"
    "s_add_u32 m0, s59, 94208\n"
    "s_mov_b32 s74, 258304\n"
    "buffer_load_dwordx4 v5, s[40:43], s74 offen lds\n"
    "s_add_u32 m0, s59, 98304\n"
    "s_mov_b32 s74, 294912\n"
    "buffer_load_dwordx4 v5, s[40:43], s74 offen lds\n"
    "s_add_u32 m0, s59, 102400\n"
    "s_mov_b32 s74, 295040\n"
    "buffer_load_dwordx4 v5, s[40:43], s74 offen lds\n"
    "s_add_u32 m0, s59, 106496\n"
    "s_mov_b32 s74, 295168\n"
    "buffer_load_dwordx4 v5, s[40:43], s74 offen lds\n"
    "s_add_u32 m0, s59, 110592\n"
    "s_mov_b32 s74, 331776\n"
    "buffer_load_dwordx4 v5, s[40:43], s74 offen lds\n"
    "s_add_u32 m0, s59, 114688\n"
    "s_mov_b32 s74, 331904\n"
    "buffer_load_dwordx4 v5, s[40:43], s74 offen lds\n"
    "s_add_u32 m0, s59, 118784\n"
    "s_mov_b32 s74, 332032\n"
    "buffer_load_dwordx4 v5, s[40:43], s74 offen lds\n"
    "v_add_u32 v16, 49152, v6\n"
    "v_add_u32 v17, 98304, v6\n"
    "v_add_u32 v18, 49152, v7\n"
    "v_add_u32 v19, 98304, v7\n"
    "s_waitcnt vmcnt(24) lgkmcnt(0)\n"
    "s_barrier\n"
    "s_add_u32 m0, s59, 122880\n"
    "s_mov_b32 s74, 368640\n"
    "buffer_load_dwordx4 v5, s[40:43], s74 offen lds\n"
    "s_add_u32 m0, s59, 126976\n"
    "s_mov_b32 s74, 368768\n"
    "buffer_load_dwordx4 v5, s[40:43], s74 offen lds\n"
    "s_add_u32 m0, s59, 131072\n"
    "s_mov_b32 s74, 368896\n"
    "buffer_load_dwordx4 v5, s[40:43], s74 offen lds\n"
    "s_add_u32 m0, s59, 135168\n"
    "s_mov_b32 s74, 405504\n"
    "buffer_load_dwordx4 v5, s[40:43], s74 offen lds\n"
    "s_add_u32 m0, s59, 139264\n"
    "s_mov_b32 s74, 405632\n"
    "buffer_load_dwordx4 v5, s[40:43], s74 offen lds\n"
    "s_add_u32 m0, s59, 143360\n"
    "s_mov_b32 s74, 405760\n"
    "buffer_load_dwordx4 v5, s[40:43], s74 offen lds\n"
    "ds_read_b64_tr_b16 a[0:1], v6 offset:0\n"
    "ds_read_b64_tr_b16 a[2:3], v6 offset:512\n"
    "ds_read_b64_tr_b16 a[4:5], v6 offset:8\n"
    "ds_read_b64_tr_b16 a[6:7], v6 offset:520\n"
    "ds_read_b64_tr_b16 a[8:9], v7 offset:0\n"
    "ds_read_b64_tr_b16 a[10:11], v7 offset:512\n"
    "ds_read_b64_tr_b16 a[12:13], v6 offset:12288\n"
    "ds_read_b64_tr_b16 a[14:15], v6 offset:12800\n"
    "ds_read_b64_tr_b16 a[16:17], v6 offset:12296\n"
    "ds_read_b64_tr_b16 a[18:19], v6 offset:12808\n"
    "ds_read_b64_tr_b16 a[20:21], v7 offset:12288\n"
    "ds_read_b64_tr_b16 a[22:23], v7 offset:12800\n"
    "s_waitcnt vmcnt(24) lgkmcnt(0)\n"
    "s_barrier\n"
    "s_add_u32 m0, s59, 0\n"
    "s_mov_b32 s74, 442368\n"
    "buffer_load_dwordx4 v5, s[40:43], s74 offen lds\n"
    "s_add_u32 m0, s59, 4096\n"
    "s_mov_b32 s74, 442496\n"
    "buffer_load_dwordx4 v5, s[40:43], s74 offen lds\n"
    "s_add_u32 m0, s59, 8192\n"
    "s_mov_b32 s74, 442624\n"
    "buffer_load_dwordx4 v5, s[40:43], s74 offen lds\n"
    "s_add_u32 m0, s59, 12288\n"
    "s_mov_b32 s74, 479232\n"
    "buffer_load_dwordx4 v5, s[40:43], s74 offen lds\n"
    "s_add_u32 m0, s59, 16384\n"
    "s_mov_b32 s74, 479360\n"
    "buffer_load_dwordx4 v5, s[40:43], s74 offen lds\n"
    "s_add_u32 m0, s59, 20480\n"
    "s_mov_b32 s74, 479488\n"
    "buffer_load_dwordx4 v5, s[40:43], s74 offen lds\n"
    "ds_read_b64_tr_b16 a[24:25], v6 offset:24576\n"
    "ds_read_b64_tr_b16 a[26:27], v6 offset:25088\n"
    "ds_read_b64_tr_b16 a[28:29], v6 offset:24584\n"
    "ds_read_b64_tr_b16 a[30:31], v6 offset:25096\n"
    "ds_read_b64_tr_b16 a[32:33], v7 offset:24576\n"
    "ds_read_b64_tr_b16 a[34:35], v7 offset:25088\n"
    "ds_read_b64_tr_b16 a[36:37], v6 offset:36864\n"
    "ds_read_b64_tr_b16 a[38:39], v6 offset:37376\n"
    "ds_read_b64_tr_b16 a[40:41], v6 offset:36872\n"
    "ds_read_b64_tr_b16 a[42:43], v6 offset:37384\n"
    "ds_read_b64_tr_b16 a[44:45], v7 offset:36864\n"
    "ds_read_b64_tr_b16 a[46:47], v7 offset:37376\n"
    "s_waitcnt vmcnt(24) lgkmcnt(0)\n"
    "s_barrier\n"
    "s_add_u32 m0, s59, 24576\n"
    "s_mov_b32 s74, 516096\n"
    "buffer_load_dwordx4 v5, s[40:43], s74 offen lds\n"
    "s_add_u32 m0, s59, 28672\n"
    "s_mov_b32 s74, 516224\n"
    "buffer_load_dwordx4 v5, s[40:43], s74 offen lds\n"
    "s_add_u32 m0, s59, 32768\n"
    "s_mov_b32 s74, 516352\n"
    "buffer_load_dwordx4 v5, s[40:43], s74 offen lds\n"
    "s_add_u32 m0, s59, 36864\n"
    "s_mov_b32 s74, 552960\n"
    "buffer_load_dwordx4 v5, s[40:43], s74 offen lds\n"
    "s_add_u32 m0, s59, 40960\n"
    "s_mov_b32 s74, 553088\n"
    "buffer_load_dwordx4 v5, s[40:43], s74 offen lds\n"
    "s_add_u32 m0, s59, 45056\n"
    "s_mov_b32 s74, 553216\n"
    "buffer_load_dwordx4 v5, s[40:43], s74 offen lds\n"
    "ds_read_b64_tr_b16 a[48:49], v16 offset:0\n"
    "ds_read_b64_tr_b16 a[50:51], v16 offset:512\n"
    "ds_read_b64_tr_b16 a[52:53], v16 offset:8\n"
    "ds_read_b64_tr_b16 a[54:55], v16 offset:520\n"
    "ds_read_b64_tr_b16 a[56:57], v18 offset:0\n"
    "ds_read_b64_tr_b16 a[58:59], v18 offset:512\n"
    "ds_read_b64_tr_b16 a[60:61], v16 offset:12288\n"
    "ds_read_b64_tr_b16 a[62:63], v16 offset:12800\n"
    "ds_read_b64_tr_b16 a[64:65], v16 offset:12296\n"
    "ds_read_b64_tr_b16 a[66:67], v16 offset:12808\n"
    "ds_read_b64_tr_b16 a[68:69], v18 offset:12288\n"
    "ds_read_b64_tr_b16 a[70:71], v18 offset:12800\n"
    "s_waitcnt vmcnt(24) lgkmcnt(0)\n"
    "s_barrier\n"
    "s_add_u32 m0, s59, 49152\n"
    "s_mov_b32 s74, 589824\n"
    "buffer_load_dwordx4 v5, s[40:43], s74 offen lds\n"
    "s_add_u32 m0, s59, 53248\n"
    "s_mov_b32 s74, 589952\n"
    "buffer_load_dwordx4 v5, s[40:43], s74 offen lds\n"
    "s_add_u32 m0, s59, 57344\n"
    "s_mov_b32 s74, 590080\n"
    "buffer_load_dwordx4 v5, s[40:43], s74 offen lds\n"
    "s_add_u32 m0, s59, 61440\n"
    "s_mov_b32 s74, 626688\n"
    "buffer_load_dwordx4 v5, s[40:43], s74 offen lds\n"
    "s_add_u32 m0, s59, 65536\n"
    "s_mov_b32 s74, 626816\n"
    "buffer_load_dwordx4 v5, s[40:43], s74 offen lds\n"
    "s_add_u32 m0, s59, 69632\n"
    "s_mov_b32 s74, 626944\n"
    "buffer_load_dwordx4 v5, s[40:43], s74 offen lds\n"
    "ds_read_b64_tr_b16 a[72:73], v16 offset:24576\n"
    "ds_read_b64_tr_b16 a[74:75], v16 offset:25088\n"
    "ds_read_b64_tr_b16 a[76:77], v16 offset:24584\n"
    "ds_read_b64_tr_b16 a[78:79], v16 offset:25096\n"
    "ds_read_b64_tr_b16 a[80:81], v18 offset:24576\n"
    "ds_read_b64_tr_b16 a[82:83], v18 offset:25088\n"
    "ds_read_b64_tr_b16 a[84:85], v16 offset:36864\n"
    "ds_read_b64_tr_b16 a[86:87], v16 offset:37376\n"
    "ds_read_b64_tr_b16 a[88:89], v16 offset:36872\n"
    "ds_read_b64_tr_b16 a[90:91], v16 offset:37384\n"
    "ds_read_b64_tr_b16 a[92:93], v18 offset:36864\n"
    "ds_read_b64_tr_b16 a[94:95], v18 offset:37376\n"
    "s_waitcnt vmcnt(24) lgkmcnt(0)\n"
    "s_barrier\n"
    "s_add_u32 m0, s59, 73728\n"
    "s_mov_b32 s74, 663552\n"
    "buffer_load_dwordx4 v5, s[40:43], s74 offen lds\n"
    "s_add_u32 m0, s59, 77824\n"
    "s_mov_b32 s74, 663680\n"
    "buffer_load_dwordx4 v5, s[40:43], s74 offen lds\n"
    "s_add_u32 m0, s59, 81920\n"
    "s_mov_b32 s74, 663808\n"
    "buffer_load_dwordx4 v5, s[40:43], s74 offen lds\n"
    "s_add_u32 m0, s59, 86016\n"
    "s_mov_b32 s74, 700416\n"
    "buffer_load_dwordx4 v5, s[40:43], s74 offen lds\n"
    "s_add_u32 m0, s59, 90112\n"
    "s_mov_b32 s74, 700544\n"
    "buffer_load_dwordx4 v5, s[40:43], s74 offen lds\n"
    "s_add_u32 m0, s59, 94208\n"
    "s_mov_b32 s74, 700672\n"
    "buffer_load_dwordx4 v5, s[40:43], s74 offen lds\n"
    "ds_read_b64_tr_b16 a[96:97], v17 offset:0\n"
    "ds_read_b64_tr_b16 a[98:99], v17 offset:512\n"
    "ds_read_b64_tr_b16 a[100:101], v17 offset:8\n"
    "ds_read_b64_tr_b16 a[102:103], v17 offset:520\n"
    "ds_read_b64_tr_b16 a[104:105], v19 offset:0\n"
    "ds_read_b64_tr_b16 a[106:107], v19 offset:512\n"
    "ds_read_b64_tr_b16 a[108:109], v17 offset:12288\n"
    "ds_read_b64_tr_b16 a[110:111], v17 offset:12800\n"
    "ds_read_b64_tr_b16 a[112:113], v17 offset:12296\n"
    "ds_read_b64_tr_b16 a[114:115], v17 offset:12808\n"
    "ds_read_b64_tr_b16 a[116:117], v19 offset:12288\n"
    "ds_read_b64_tr_b16 a[118:119], v19 offset:12800\n"
    "s_waitcnt vmcnt(24) lgkmcnt(0)\n"
    "s_barrier\n"
    "s_add_u32 m0, s59, 98304\n"
    "s_mov_b32 s74, 737280\n"
    "buffer_load_dwordx4 v5, s[40:43], s74 offen lds\n"
    "s_add_u32 m0, s59, 102400\n"
    "s_mov_b32 s74, 737408\n"
    "buffer_load_dwordx4 v5, s[40:43], s74 offen lds\n"
    "s_add_u32 m0, s59, 106496\n"
    "s_mov_b32 s74, 737536\n"
    "buffer_load_dwordx4 v5, s[40:43], s74 offen lds\n"
    "s_add_u32 m0, s59, 110592\n"
    "s_mov_b32 s74, 774144\n"
    "buffer_load_dwordx4 v5, s[40:43], s74 offen lds\n"
    "s_add_u32 m0, s59, 114688\n"
    "s_mov_b32 s74, 774272\n"
    "buffer_load_dwordx4 v5, s[40:43], s74 offen lds\n"
    "s_add_u32 m0, s59, 118784\n"
    "s_mov_b32 s74, 774400\n"
    "buffer_load_dwordx4 v5, s[40:43], s74 offen lds\n"
    "ds_read_b64_tr_b16 a[120:121], v17 offset:24576\n"
    "ds_read_b64_tr_b16 a[122:123], v17 offset:25088\n"
    "ds_read_b64_tr_b16 a[124:125], v17 offset:24584\n"
    "ds_read_b64_tr_b16 a[126:127], v17 offset:25096\n"
    "ds_read_b64_tr_b16 a[128:129], v19 offset:24576\n"
    "ds_read_b64_tr_b16 a[130:131], v19 offset:25088\n"
    "ds_read_b64_tr_b16 a[132:133], v17 offset:36864\n"
    "ds_read_b64_tr_b16 a[134:135], v17 offset:37376\n"
    "ds_read_b64_tr_b16 a[136:137], v17 offset:36872\n"
    "ds_read_b64_tr_b16 a[138:139], v17 offset:37384\n"
    "ds_read_b64_tr_b16 a[140:141], v19 offset:36864\n"
    "ds_read_b64_tr_b16 a[142:143], v19 offset:37376\n"
    "s_waitcnt vmcnt(24) lgkmcnt(0)\n"
    "s_barrier\n"
    "s_add_u32 m0, s59, 122880\n"
    "s_mov_b32 s74, 811008\n"
    "buffer_load_dwordx4 v5, s[40:43], s74 offen lds\n"
    "s_add_u32 m0, s59, 126976\n"
    "s_mov_b32 s74, 811136\n"
    "buffer_load_dwordx4 v5, s[40:43], s74 offen lds\n"
    "s_add_u32 m0, s59, 131072\n"
    "s_mov_b32 s74, 811264\n"
    "buffer_load_dwordx4 v5, s[40:43], s74 offen lds\n"
    "s_add_u32 m0, s59, 135168\n"
    "s_mov_b32 s74, 847872\n"
    "buffer_load_dwordx4 v5, s[40:43], s74 offen lds\n"
    "s_add_u32 m0, s59, 139264\n"
    "s_mov_b32 s74, 848000\n"
    "buffer_load_dwordx4 v5, s[40:43], s74 offen lds\n"
    "s_add_u32 m0, s59, 143360\n"
    "s_mov_b32 s74, 848128\n"
    "buffer_load_dwordx4 v5, s[40:43], s74 offen lds\n"
    "ds_read_b64_tr_b16 a[144:145], v6 offset:0\n"
    "ds_read_b64_tr_b16 a[146:147], v6 offset:512\n"
    "ds_read_b64_tr_b16 a[148:149], v6 offset:8\n"
    "ds_read_b64_tr_b16 a[150:151], v6 offset:520\n"
    "ds_read_b64_tr_b16 a[152:153], v7 offset:0\n"
    "ds_read_b64_tr_b16 a[154:155], v7 offset:512\n"
    "ds_read_b64_tr_b16 a[156:157], v6 offset:12288\n"
    "ds_read_b64_tr_b16 a[158:159], v6 offset:12800\n"
    "ds_read_b64_tr_b16 a[160:161], v6 offset:12296\n"
    "ds_read_b64_tr_b16 a[162:163], v6 offset:12808\n"
    "ds_read_b64_tr_b16 a[164:165], v7 offset:12288\n"
    "ds_read_b64_tr_b16 a[166:167], v7 offset:12800\n"
    "s_waitcnt vmcnt(24) lgkmcnt(0)\n"
    "s_barrier\n"
    "s_add_u32 m0, s59, 0\n"
    "s_mov_b32 s74, 884736\n"
    "buffer_load_dwordx4 v5, s[40:43], s74 offen lds\n"
    "s_add_u32 m0, s59, 4096\n"
    "s_mov_b32 s74, 884864\n"
    "buffer_load_dwordx4 v5, s[40:43], s74 offen lds\n"
    "s_add_u32 m0, s59, 8192\n"
    "s_mov_b32 s74, 884992\n"
    "buffer_load_dwordx4 v5, s[40:43], s74 offen lds\n"
    "s_add_u32 m0, s59, 12288\n"
    "s_mov_b32 s74, 921600\n"
    "buffer_load_dwordx4 v5, s[40:43], s74 offen lds\n"
    "s_add_u32 m0, s59, 16384\n"
    "s_mov_b32 s74, 921728\n"
    "buffer_load_dwordx4 v5, s[40:43], s74 offen lds\n"
    "s_add_u32 m0, s59, 20480\n"
    "s_mov_b32 s74, 921856\n"
    "buffer_load_dwordx4 v5, s[40:43], s74 offen lds\n"
    "ds_read_b64_tr_b16 a[168:169], v6 offset:24576\n"
    "ds_read_b64_tr_b16 a[170:171], v6 offset:25088\n"
    "ds_read_b64_tr_b16 a[172:173], v6 offset:24584\n"
    "ds_read_b64_tr_b16 a[174:175], v6 offset:25096\n"
    "ds_read_b64_tr_b16 a[176:177], v7 offset:24576\n"
    "ds_read_b64_tr_b16 a[178:179], v7 offset:25088\n"
    "ds_read_b64_tr_b16 a[180:181], v6 offset:36864\n"
    "ds_read_b64_tr_b16 a[182:183], v6 offset:37376\n"
    "ds_read_b64_tr_b16 a[184:185], v6 offset:36872\n"
    "ds_read_b64_tr_b16 a[186:187], v6 offset:37384\n"
    "ds_read_b64_tr_b16 a[188:189], v7 offset:36864\n"
    "ds_read_b64_tr_b16 a[190:191], v7 offset:37376\n"
    "s_waitcnt vmcnt(24) lgkmcnt(0)\n"
    "s_barrier\n"
    "s_add_u32 m0, s59, 24576\n"
    "s_mov_b32 s74, 958464\n"
    "buffer_load_dwordx4 v5, s[40:43], s74 offen lds\n"
    "s_add_u32 m0, s59, 28672\n"
    "s_mov_b32 s74, 958592\n"
    "buffer_load_dwordx4 v5, s[40:43], s74 offen lds\n"
    "s_add_u32 m0, s59, 32768\n"
    "s_mov_b32 s74, 958720\n"
    "buffer_load_dwordx4 v5, s[40:43], s74 offen lds\n"
    "s_add_u32 m0, s59, 36864\n"
    "s_mov_b32 s74, 995328\n"
    "buffer_load_dwordx4 v5, s[40:43], s74 offen lds\n"
    "s_add_u32 m0, s59, 40960\n"
    "s_mov_b32 s74, 995456\n"
    "buffer_load_dwordx4 v5, s[40:43], s74 offen lds\n"
    "s_add_u32 m0, s59, 45056\n"
    "s_mov_b32 s74, 995584\n"
    "buffer_load_dwordx4 v5, s[40:43], s74 offen lds\n"
    "ds_read_b64_tr_b16 a[192:193], v16 offset:0\n"
    "ds_read_b64_tr_b16 a[194:195], v16 offset:512\n"
    "ds_read_b64_tr_b16 a[196:197], v16 offset:8\n"
    "ds_read_b64_tr_b16 a[198:199], v16 offset:520\n"
    "ds_read_b64_tr_b16 a[200:201], v18 offset:0\n"
    "ds_read_b64_tr_b16 a[202:203], v18 offset:512\n"
    "ds_read_b64_tr_b16 a[204:205], v16 offset:12288\n"
    "ds_read_b64_tr_b16 a[206:207], v16 offset:12800\n"
    "ds_read_b64_tr_b16 a[208:209], v16 offset:12296\n"
    "ds_read_b64_tr_b16 a[210:211], v16 offset:12808\n"
    "ds_read_b64_tr_b16 a[212:213], v18 offset:12288\n"
    "ds_read_b64_tr_b16 a[214:215], v18 offset:12800\n"
    "s_waitcnt vmcnt(24) lgkmcnt(0)\n"
    "s_barrier\n"
    "s_add_u32 m0, s59, 49152\n"
    "s_mov_b32 s74, 1032192\n"
    "buffer_load_dwordx4 v5, s[40:43], s74 offen lds\n"
    "s_add_u32 m0, s59, 53248\n"
    "s_mov_b32 s74, 1032320\n"
    "buffer_load_dwordx4 v5, s[40:43], s74 offen lds\n"
    "s_add_u32 m0, s59, 57344\n"
    "s_mov_b32 s74, 1032448\n"
    "buffer_load_dwordx4 v5, s[40:43], s74 offen lds\n"
    "s_add_u32 m0, s59, 61440\n"
    "s_mov_b32 s74, 1069056\n"
    "buffer_load_dwordx4 v5, s[40:43], s74 offen lds\n"
    "s_add_u32 m0, s59, 65536\n"
    "s_mov_b32 s74, 1069184\n"
    "buffer_load_dwordx4 v5, s[40:43], s74 offen lds\n"
    "s_add_u32 m0, s59, 69632\n"
    "s_mov_b32 s74, 1069312\n"
    "buffer_load_dwordx4 v5, s[40:43], s74 offen lds\n"
    "ds_read_b64_tr_b16 a[216:217], v16 offset:24576\n"
    "ds_read_b64_tr_b16 a[218:219], v16 offset:25088\n"
    "ds_read_b64_tr_b16 a[220:221], v16 offset:24584\n"
    "ds_read_b64_tr_b16 a[222:223], v16 offset:25096\n"
    "ds_read_b64_tr_b16 a[224:225], v18 offset:24576\n"
    "ds_read_b64_tr_b16 a[226:227], v18 offset:25088\n"
    "ds_read_b64_tr_b16 a[228:229], v16 offset:36864\n"
    "ds_read_b64_tr_b16 a[230:231], v16 offset:37376\n"
    "ds_read_b64_tr_b16 a[232:233], v16 offset:36872\n"
    "ds_read_b64_tr_b16 a[234:235], v16 offset:37384\n"
    "ds_read_b64_tr_b16 a[236:237], v18 offset:36864\n"
    "ds_read_b64_tr_b16 a[238:239], v18 offset:37376\n"
    "s_waitcnt vmcnt(24) lgkmcnt(0)\n"
    "s_barrier\n"
    "s_add_u32 m0, s59, 73728\n"
    "s_mov_b32 s74, 1105920\n"
    "buffer_load_dwordx4 v5, s[40:43], s74 offen lds\n"
    "s_add_u32 m0, s59, 77824\n"
    "s_mov_b32 s74, 1106048\n"
    "buffer_load_dwordx4 v5, s[40:43], s74 offen lds\n"
    "s_add_u32 m0, s59, 81920\n"
    "s_mov_b32 s74, 1106176\n"
    "buffer_load_dwordx4 v5, s[40:43], s74 offen lds\n"
    "s_add_u32 m0, s59, 86016\n"
    "s_mov_b32 s74, 1142784\n"
    "buffer_load_dwordx4 v5, s[40:43], s74 offen lds\n"
    "s_add_u32 m0, s59, 90112\n"
    "s_mov_b32 s74, 1142912\n"
    "buffer_load_dwordx4 v5, s[40:43], s74 offen lds\n"
    "s_add_u32 m0, s59, 94208\n"
    "s_mov_b32 s74, 1143040\n"
    "buffer_load_dwordx4 v5, s[40:43], s74 offen lds\n"
    "ds_read_b64_tr_b16 a[240:241], v17 offset:0\n"
    "ds_read_b64_tr_b16 a[242:243], v17 offset:512\n"
    "ds_read_b64_tr_b16 a[244:245], v17 offset:8\n"
    "ds_read_b64_tr_b16 a[246:247], v17 offset:520\n"
    "ds_read_b64_tr_b16 a[248:249], v19 offset:0\n"
    "ds_read_b64_tr_b16 a[250:251], v19 offset:512\n"
    "ds_read_b64_tr_b16 a[252:253], v17 offset:12288\n"
    "ds_read_b64_tr_b16 a[254:255], v17 offset:12800\n"
    "ds_read_b64_tr_b16 v[80:81], v17 offset:12296\n"
    "ds_read_b64_tr_b16 v[82:83], v17 offset:12808\n"
    "ds_read_b64_tr_b16 v[84:85], v19 offset:12288\n"
    "ds_read_b64_tr_b16 v[86:87], v19 offset:12800\n"
    "s_waitcnt vmcnt(24) lgkmcnt(0)\n"
    "s_barrier\n"
    "s_add_u32 m0, s59, 98304\n"
    "s_mov_b32 s74, 1179648\n"
    "buffer_load_dwordx4 v5, s[40:43], s74 offen lds\n"
    "s_add_u32 m0, s59, 102400\n"
    "s_mov_b32 s74, 1179776\n"
    "buffer_load_dwordx4 v5, s[40:43], s74 offen lds\n"
    "s_add_u32 m0, s59, 106496\n"
    "s_mov_b32 s74, 1179904\n"
    "buffer_load_dwordx4 v5, s[40:43], s74 offen lds\n"
    "s_add_u32 m0, s59, 110592\n"
    "s_mov_b32 s74, 1216512\n"
    "buffer_load_dwordx4 v5, s[40:43], s74 offen lds\n"
    "s_add_u32 m0, s59, 114688\n"
    "s_mov_b32 s74, 1216640\n"
    "buffer_load_dwordx4 v5, s[40:43], s74 offen lds\n"
    "s_add_u32 m0, s59, 118784\n"
    "s_mov_b32 s74, 1216768\n"
    "buffer_load_dwordx4 v5, s[40:43], s74 offen lds\n"
    "ds_read_b64_tr_b16 v[88:89], v17 offset:24576\n"
    "ds_read_b64_tr_b16 v[90:91], v17 offset:25088\n"
    "ds_read_b64_tr_b16 v[92:93], v17 offset:24584\n"
    "ds_read_b64_tr_b16 v[94:95], v17 offset:25096\n"
    "ds_read_b64_tr_b16 v[96:97], v19 offset:24576\n"
    "ds_read_b64_tr_b16 v[98:99], v19 offset:25088\n"
    "ds_read_b64_tr_b16 v[100:101], v17 offset:36864\n"
    "ds_read_b64_tr_b16 v[102:103], v17 offset:37376\n"
    "ds_read_b64_tr_b16 v[104:105], v17 offset:36872\n"
    "ds_read_b64_tr_b16 v[106:107], v17 offset:37384\n"
    "ds_read_b64_tr_b16 v[108:109], v19 offset:36864\n"
    "ds_read_b64_tr_b16 v[110:111], v19 offset:37376\n"
    "s_waitcnt vmcnt(24) lgkmcnt(0)\n"
    "s_barrier\n"
    "s_add_u32 m0, s59, 122880\n"
    "s_mov_b32 s74, 1253376\n"
    "buffer_load_dwordx4 v5, s[40:43], s74 offen lds\n"
    "s_add_u32 m0, s59, 126976\n"
    "s_mov_b32 s74, 1253504\n"
    "buffer_load_dwordx4 v5, s[40:43], s74 offen lds\n"
    "s_add_u32 m0, s59, 131072\n"
    "s_mov_b32 s74, 1253632\n"
    "buffer_load_dwordx4 v5, s[40:43], s74 offen lds\n"
    "s_add_u32 m0, s59, 135168\n"
    "s_mov_b32 s74, 1290240\n"
    "buffer_load_dwordx4 v5, s[40:43], s74 offen lds\n"
    "s_add_u32 m0, s59, 139264\n"
    "s_mov_b32 s74, 1290368\n"
    "buffer_load_dwordx4 v5, s[40:43], s74 offen lds\n"
    "s_add_u32 m0, s59, 143360\n"
    "s_mov_b32 s74, 1290496\n"
    "buffer_load_dwordx4 v5, s[40:43], s74 offen lds\n"
    "buffer_load_dwordx2 v[12:13], off, s[36:39], 0 sc1\n"
    "ds_read_b64_tr_b16 v[112:113], v6 offset:0\n"
    "ds_read_b64_tr_b16 v[114:115], v6 offset:512\n"
    "ds_read_b64_tr_b16 v[116:117], v6 offset:8\n"
    "ds_read_b64_tr_b16 v[118:119], v6 offset:520\n"
    "ds_read_b64_tr_b16 v[120:121], v7 offset:0\n"
    "ds_read_b64_tr_b16 v[122:123], v7 offset:512\n"
    "ds_read_b64_tr_b16 v[124:125], v6 offset:12288\n"
    "ds_read_b64_tr_b16 v[126:127], v6 offset:12800\n"
    "ds_read_b64_tr_b16 v[128:129], v6 offset:12296\n"
    "ds_read_b64_tr_b16 v[130:131], v6 offset:12808\n"
    "ds_read_b64_tr_b16 v[132:133], v7 offset:12288\n"
    "ds_read_b64_tr_b16 v[134:135], v7 offset:12800\n"
    "s_waitcnt vmcnt(25) lgkmcnt(0)\n"
    "s_barrier\n"
    "ds_read_b64_tr_b16 v[136:137], v6 offset:24576\n"
    "ds_read_b64_tr_b16 v[138:139], v6 offset:25088\n"
    "ds_read_b64_tr_b16 v[140:141], v6 offset:24584\n"
    "ds_read_b64_tr_b16 v[142:143], v6 offset:25096\n"
    "ds_read_b64_tr_b16 v[144:145], v7 offset:24576\n"
    "ds_read_b64_tr_b16 v[146:147], v7 offset:25088\n"
    "ds_read_b64_tr_b16 v[148:149], v6 offset:36864\n"
    "ds_read_b64_tr_b16 v[150:151], v6 offset:37376\n"
    "ds_read_b64_tr_b16 v[152:153], v6 offset:36872\n"
    "ds_read_b64_tr_b16 v[154:155], v6 offset:37384\n"
    "ds_read_b64_tr_b16 v[156:157], v7 offset:36864\n"
    "ds_read_b64_tr_b16 v[158:159], v7 offset:37376\n"
    "s_waitcnt vmcnt(19) lgkmcnt(0)\n"
    "s_barrier\n"
    "ds_read_b64_tr_b16 v[160:161], v16 offset:0\n"
    "ds_read_b64_tr_b16 v[162:163], v16 offset:512\n"
    "ds_read_b64_tr_b16 v[164:165], v16 offset:8\n"
    "ds_read_b64_tr_b16 v[166:167], v16 offset:520\n"
    "ds_read_b64_tr_b16 v[168:169], v18 offset:0\n"
    "ds_read_b64_tr_b16 v[170:171], v18 offset:512\n"
    "ds_read_b64_tr_b16 v[172:173], v16 offset:12288\n"
    "ds_read_b64_tr_b16 v[174:175], v16 offset:12800\n"
    "ds_read_b64_tr_b16 v[176:177], v16 offset:12296\n"
    "ds_read_b64_tr_b16 v[178:179], v16 offset:12808\n"
    "ds_read_b64_tr_b16 v[180:181], v18 offset:12288\n"
    "ds_read_b64_tr_b16 v[182:183], v18 offset:12800\n"
    "s_waitcnt vmcnt(13) lgkmcnt(0)\n"
    "s_barrier\n"
    "ds_read_b64_tr_b16 v[184:185], v16 offset:24576\n"
    "ds_read_b64_tr_b16 v[186:187], v16 offset:25088\n"
    "ds_read_b64_tr_b16 v[188:189], v16 offset:24584\n"
    "ds_read_b64_tr_b16 v[190:191], v16 offset:25096\n"
    "ds_read_b64_tr_b16 v[192:193], v18 offset:24576\n"
    "ds_read_b64_tr_b16 v[194:195], v18 offset:25088\n"
    "ds_read_b64_tr_b16 v[196:197], v16 offset:36864\n"
    "ds_read_b64_tr_b16 v[198:199], v16 offset:37376\n"
    "ds_read_b64_tr_b16 v[200:201], v16 offset:36872\n"
    "ds_read_b64_tr_b16 v[202:203], v16 offset:37384\n"
    "ds_read_b64_tr_b16 v[204:205], v18 offset:36864\n"
    "ds_read_b64_tr_b16 v[206:207], v18 offset:37376\n"
    "s_waitcnt vmcnt(7) lgkmcnt(0)\n"
    "s_barrier\n"
    "ds_read_b64_tr_b16 v[208:209], v17 offset:0\n"
    "ds_read_b64_tr_b16 v[210:211], v17 offset:512\n"
    "ds_read_b64_tr_b16 v[212:213], v17 offset:8\n"
    "ds_read_b64_tr_b16 v[214:215], v17 offset:520\n"
    "ds_read_b64_tr_b16 v[216:217], v19 offset:0\n"
    "ds_read_b64_tr_b16 v[218:219], v19 offset:512\n"
    "ds_read_b64_tr_b16 v[220:221], v17 offset:12288\n"
    "ds_read_b64_tr_b16 v[222:223], v17 offset:12800\n"
    "ds_read_b64_tr_b16 v[224:225], v17 offset:12296\n"
    "ds_read_b64_tr_b16 v[226:227], v17 offset:12808\n"
    "ds_read_b64_tr_b16 v[228:229], v19 offset:12288\n"
    "ds_read_b64_tr_b16 v[230:231], v19 offset:12800\n"
    "s_waitcnt vmcnt(0) lgkmcnt(0)\n"
    "s_barrier\n"
    ".LgDOWN_par_%=:\n"
    "v_readfirstlane_b32 s74, v12\n"
    "v_readfirstlane_b32 s75, v13\n"
    "s_xor_b32 s74, s74, s66\n"
    "s_xor_b32 s75, s75, s67\n"
    "s_cselect_b32 s75, 2, 0\n"
    "s_or_b32 s75, s75, s74\n"
    "s_cmp_lt_u32 s75, 2\n"
    "s_cbranch_scc1 .LgDOWN_par1_%=\n"
    "s_sleep 4\n"
    "buffer_load_dwordx2 v[12:13], off, s[36:39], 0 sc1\n"
    "s_waitcnt vmcnt(0)\n"
    "s_sub_u32 s72, s72, 1\n"
    "s_cbranch_scc0 .LgDOWN_par_%=\n"
    "s_mov_b32 s72, 0\n"
    ".LgDOWN_par1_%=:\n"
    "s_and_b32 s74, s74, 1\n"
    "s_lshl_b32 s74, s74, 13\n"
    "s_add_u32 s36, s36, s74\n"
    "s_addc_u32 s37, s37, 0\n"
    "v_add_u32 v13, v11, v3\n"
    "v_cmp_gt_i32 vcc, s71, v13\n"
    "s_nop 0\n"
    "s_or_b64 exec, vcc, s[62:63]\n"
    "buffer_atomic_smax v11, v2, s[36:39], 0 offen sc0 sc1\n"
    "s_mov_b64 exec, -1\n"
    "s_getpc_b64 s[84:85]\n"
    ".LgDOWN_PC2_%=:\n"
    "s_and_b32 s85, s85, 0xffff\n"
    "s_mov_b32 s86, .LgDOWN_FIN_%=-.LgDOWN_PC2_%=\n"
    "s_mov_b32 s87, 0x20000\n"
    "v_mbcnt_lo_u32_b32 v42, -1, 0\n"
    "v_mbcnt_hi_u32_b32 v42, -1, v42\n"
    "v_lshlrev_b32 v42, 7, v42\n"
    "buffer_load_dword v43, v42, s[84:87], 0 offen\n"
    "v_add_u32 v42, 8192, v42\n"
    "buffer_load_dword v43, v42, s[84:87], 0 offen\n"
    "v_add_u32 v42, 8192, v42\n"
    "buffer_load_dword v43, v42, s[84:87], 0 offen\n"
    "ds_read_b64_tr_b16 v[232:233], v17 offset:24576\n"
    "ds_read_b64_tr_b16 v[234:235], v17 offset:25088\n"
    "ds_read_b64_tr_b16 v[236:237], v17 offset:24584\n"
    "ds_read_b64_tr_b16 v[238:239], v17 offset:25096\n"
    "ds_read_b64_tr_b16 v[240:241], v19 offset:24576\n"
    "ds_read_b64_tr_b16 v[242:243], v19 offset:25088\n"
    "ds_read_b64_tr_b16 v[244:245], v17 offset:36864\n"
    "ds_read_b64_tr_b16 v[246:247], v17 offset:37376\n"
    "ds_read_b64_tr_b16 v[248:249], v17 offset:36872\n"
    "ds_read_b64_tr_b16 v[250:251], v17 offset:37384\n"
    "ds_read_b64_tr_b16 v[252:253], v19 offset:36864\n"
    "ds_read_b64_tr_b16 v[254:255], v19 offset:37376\n"
    "s_waitcnt lgkmcnt(0)\n"
    "s_barrier\n"
    "s_waitcnt vmcnt(0)\n"
    "v_mov_b32 v5, v11\n"
    "v_mov_b32 v12, -1\n"
    "s_mov_b32 s73, 0x80000000\n"
    "s_branch .LgDOWN_block1_1_%=\n"
    ".LgDOWN_LOOPB_%=:\n"
    ".LgDOWN_head0_%=:\n"
    "v_mfma_f32_16x16x32_bf16 v[48:51], a[0:3], v[16:19], 0\n"
    "v_mfma_f32_16x16x32_bf16 v[52:55], a[4:7], v[16:19], 0\n"
    "s_waitcnt lgkmcnt(2)\n"
    "v_cvt_pk_bf16_f32 v56, v56, v57\n"
    "v_cvt_pk_bf16_f32 v57, v58, v59\n"
    "v_mfma_f32_16x16x32_bf16 v[64:67], a[8:11], v[16:19], 0\n"
    "ds_read_b128 v[16:19], v0 offset:4096\n"
    "v_cvt_pk_bf16_f32 v58, v60, v61\n"
    "v_mfma_f32_16x16x32_bf16 v[48:51], a[12:15], v[20:23], v[48:51]\n"
    "v_cvt_pk_bf16_f32 v59, v62, v63\n"
    "v_mfma_f32_16x16x32_bf16 v[52:55], a[16:19], v[20:23], v[52:55]\n"
    "s_waitcnt lgkmcnt(2)\n"
    "v_cvt_pk_bf16_f32 v68, v68, v69\n"
    "v_cvt_pk_bf16_f32 v69, v70, v71\n"
    "v_mfma_f32_16x16x32_bf16 v[64:67], a[20:23], v[20:23], v[64:67]\n"
    "ds_read_b128 v[20:23], v0 offset:5120\n"
    "s_nop 0\n"
    "v_mfma_f32_16x16x32_bf16 v[48:51], a[24:27], v[24:27], v[48:51]\n"
    "buffer_store_dwordx4 v[56:59], v15, s[36:39], s73 offen offset:1024 sc1\n"
    "v_mfma_f32_16x16x32_bf16 v[52:55], a[28:31], v[24:27], v[52:55]\n"
    "s_waitcnt lgkmcnt(2)\n"
    "s_waitcnt vmcnt(2)\n"
    "v_add_u32 v13, v11, v3\n"
    "v_mfma_f32_16x16x32_bf16 v[64:67], a[32:35], v[24:27], v[64:67]\n"
    "ds_read_b128 v[24:27], v0 offset:6144\n"
    "v_cmp_gt_i32 vcc, s71, v13\n"
    "v_mfma_f32_16x16x32_bf16 v[48:51], a[36:39], v[28:31], v[48:51]\n"
    "buffer_atomic_smax v7, v10, s[36:39], s69 offen sc1\n"
    "v_permlane16_swap_b32 v68, v70\n"
    "v_mfma_f32_16x16x32_bf16 v[52:55], a[40:43], v[28:31], v[52:55]\n"
    "s_waitcnt lgkmcnt(2)\n"
    "v_permlane16_swap_b32 v69, v71\n"
    "v_mfma_f32_16x16x32_bf16 v[64:67], a[44:47], v[28:31], v[64:67]\n"
    "ds_read_b128 v[28:31], v0 offset:7168\n"
    "s_cbranch_vccnz .LgDOWN_poll0_%=\n"
    "v_mfma_f32_16x16x32_bf16 v[48:51], a[48:51], v[16:19], v[48:51]\n"
    "s_add_u32 m0, s59, 36864\n"
    "v_mfma_f32_16x16x32_bf16 v[52:55], a[52:55], v[16:19], v[52:55]\n"
    "s_waitcnt lgkmcnt(2)\n"
    "buffer_load_dwordx4 v1, s[36:39], s76 offen sc1 lds\n"
    "v_mfma_f32_16x16x32_bf16 v[64:67], a[56:59], v[16:19], v[64:67]\n"
    "ds_read_b128 v[16:19], v0 offset:8192\n"
    "v_mfma_f32_16x16x32_bf16 v[48:51], a[60:63], v[20:23], v[48:51]\n"
    "buffer_store_dwordx4 v[68:71], v4, s[36:39], s73 offen offset:1024 sc1\n"
    "v_mfma_f32_16x16x32_bf16 v[52:55], a[64:67], v[20:23], v[52:55]\n"
    "s_waitcnt lgkmcnt(2)\n"
    "s_add_u32 s76, s76, 16384\n"
    "s_lshr_b32 s73, s68, 1\n"
    "v_mfma_f32_16x16x32_bf16 v[64:67], a[68:71], v[20:23], v[64:67]\n"
    "ds_read_b128 v[20:23], v0 offset:9216\n"
    "s_and_b32 s73, s73, s57\n"
    "v_mfma_f32_16x16x32_bf16 v[48:51], a[72:75], v[24:27], v[48:51]\n"
    "s_add_u32 m0, s59, 40960\n"
    "s_mul_i32 s73, s73, s56\n"
    "v_mfma_f32_16x16x32_bf16 v[52:55], a[76:79], v[24:27], v[52:55]\n"
    "s_waitcnt lgkmcnt(2)\n"
    "buffer_load_dwordx4 v1, s[36:39], s76 offen sc1 lds\n"
    "s_add_u32 s73, s73, s55\n"
    "v_mfma_f32_16x16x32_bf16 v[64:67], a[80:83], v[24:27], v[64:67]\n"
    "ds_read_b128 v[24:27], v0 offset:10240\n"
    "v_mfma_f32_16x16x32_bf16 v[48:51], a[84:87], v[28:31], v[48:51]\n"
    "v_mfma_f32_16x16x32_bf16 v[52:55], a[88:91], v[28:31], v[52:55]\n"
    "s_waitcnt lgkmcnt(2)\n"
    "s_add_u32 s76, s76, 16384\n"
    "s_add_u32 m0, s59, 45056\n"
    "v_mfma_f32_16x16x32_bf16 v[64:67], a[92:95], v[28:31], v[64:67]\n"
    "ds_read_b128 v[28:31], v0 offset:11264\n"
    "buffer_load_dwordx4 v1, s[36:39], s76 offen sc1 lds\n"
    "v_mfma_f32_16x16x32_bf16 v[48:51], a[96:99], v[16:19], v[48:51]\n"
    "v_mfma_f32_16x16x32_bf16 v[52:55], a[100:103], v[16:19], v[52:55]\n"
    "s_waitcnt lgkmcnt(2)\n"
    "v_mfma_f32_16x16x32_bf16 v[64:67], a[104:107], v[16:19], v[64:67]\n"
    "ds_read_b128 v[16:19], v0 offset:12288\n"
    "v_mfma_f32_16x16x32_bf16 v[48:51], a[108:111], v[20:23], v[48:51]\n"
    "v_mfma_f32_16x16x32_bf16 v[52:55], a[112:115], v[20:23], v[52:55]\n"
    "s_waitcnt lgkmcnt(2)\n"
    "v_mfma_f32_16x16x32_bf16 v[64:67], a[116:119], v[20:23], v[64:67]\n"
    "ds_read_b128 v[20:23], v0 offset:13312\n"
    "s_add_u32 s76, s76, 16384\n"
    "v_mfma_f32_16x16x32_bf16 v[48:51], a[120:123], v[24:27], v[48:51]\n"
    "s_add_u32 m0, s59, 49152\n"
    "v_mfma_f32_16x16x32_bf16 v[52:55], a[124:127], v[24:27], v[52:55]\n"
    "s_waitcnt lgkmcnt(2)\n"
    "buffer_load_dwordx4 v1, s[36:39], s76 offen sc1 lds\n"
    "v_mfma_f32_16x16x32_bf16 v[64:67], a[128:131], v[24:27], v[64:67]\n"
    "ds_read_b128 v[24:27], v0 offset:14336\n"
    "v_mfma_f32_16x16x32_bf16 v[48:51], a[132:135], v[28:31], v[48:51]\n"
    "v_mfma_f32_16x16x32_bf16 v[52:55], a[136:139], v[28:31], v[52:55]\n"
    "s_waitcnt lgkmcnt(2)\n"
    "v_mfma_f32_16x16x32_bf16 v[64:67], a[140:143], v[28:31], v[64:67]\n"
    "ds_read_b128 v[28:31], v0 offset:15360\n"
    "s_add_u32 s76, s76, 16384\n"
    "v_mfma_f32_16x16x32_bf16 v[48:51], a[144:147], v[16:19], v[48:51]\n"
    "s_add_u32 m0, s59, 53248\n"
    "v_mfma_f32_16x16x32_bf16 v[52:55], a[148:151], v[16:19], v[52:55]\n"
    "s_waitcnt lgkmcnt(2)\n"
    "buffer_load_dwordx4 v1, s[36:39], s76 offen sc1 lds\n"
    "v_mfma_f32_16x16x32_bf16 v[64:67], a[152:155], v[16:19], v[64:67]\n"
    "ds_read_b128 v[16:19], v0 offset:16384\n"
    "v_mfma_f32_16x16x32_bf16 v[48:51], a[156:159], v[20:23], v[48:51]\n"
    "v_mfma_f32_16x16x32_bf16 v[52:55], a[160:163], v[20:23], v[52:55]\n"
    "s_waitcnt lgkmcnt(2)\n"
    "v_mfma_f32_16x16x32_bf16 v[64:67], a[164:167], v[20:23], v[64:67]\n"
    "ds_read_b128 v[20:23], v0 offset:17408\n"
    "s_add_u32 s76, s76, 16384\n"
    "v_mfma_f32_16x16x32_bf16 v[48:51], a[168:171], v[24:27], v[48:51]\n"
    "s_add_u32 m0, s59, 57344\n"
    "v_mfma_f32_16x16x32_bf16 v[52:55], a[172:175], v[24:27], v[52:55]\n"
    "s_waitcnt lgkmcnt(2)\n"
    "buffer_load_dwordx4 v1, s[36:39], s76 offen sc1 lds\n"
    "v_mfma_f32_16x16x32_bf16 v[64:67], a[176:179], v[24:27], v[64:67]\n"
    "ds_read_b128 v[24:27], v0 offset:18432\n"
    "v_mfma_f32_16x16x32_bf16 v[48:51], a[180:183], v[28:31], v[48:51]\n"
    "v_mfma_f32_16x16x32_bf16 v[52:55], a[184:187], v[28:31], v[52:55]\n"
    "s_waitcnt lgkmcnt(2)\n"
    "v_mfma_f32_16x16x32_bf16 v[64:67], a[188:191], v[28:31], v[64:67]\n"
    "ds_read_b128 v[28:31], v0 offset:19456\n"
    "v_mfma_f32_16x16x32_bf16 v[48:51], a[192:195], v[16:19], v[48:51]\n"
    "s_add_u32 s76, s76, 16384\n"
    "s_add_u32 m0, s59, 61440\n"
    "v_mfma_f32_16x16x32_bf16 v[52:55], a[196:199], v[16:19], v[52:55]\n"
    "s_waitcnt lgkmcnt(2)\n"
    "buffer_load_dwordx4 v1, s[36:39], s76 offen sc1 lds\n"
    "v_mfma_f32_16x16x32_bf16 v[64:67], a[200:203], v[16:19], v[64:67]\n"
    "ds_read_b128 v[16:19], v0 offset:20480\n"
    "v_mfma_f32_16x16x32_bf16 v[48:51], a[204:207], v[20:23], v[48:51]\n"
    "v_mfma_f32_16x16x32_bf16 v[52:55], a[208:211], v[20:23], v[52:55]\n"
    "s_waitcnt lgkmcnt(2)\n"
    "v_mfma_f32_16x16x32_bf16 v[64:67], a[212:215], v[20:23], v[64:67]\n"
    "ds_read_b128 v[20:23], v0 offset:21504\n"
    "v_mfma_f32_16x16x32_bf16 v[48:51], a[216:219], v[24:27], v[48:51]\n"
    "s_add_u32 s76, s76, 16384\n"
    "s_add_u32 m0, s59, 65536\n"
    "v_mfma_f32_16x16x32_bf16 v[52:55], a[220:223], v[24:27], v[52:55]\n"
    "s_waitcnt lgkmcnt(2)\n"
    "s_waitcnt vmcnt(6)\n"
    "buffer_atomic_smax v12, v10, s[36:39], s70 offen sc1\n"
    "v_mfma_f32_16x16x32_bf16 v[64:67], a[224:227], v[24:27], v[64:67]\n"
    "ds_read_b128 v[24:27], v0 offset:22528\n"
    "buffer_load_dwordx4 v1, s[36:39], s76 offen sc1 lds\n"
    "v_mfma_f32_16x16x32_bf16 v[48:51], a[228:231], v[28:31], v[48:51]\n"
    "v_mfma_f32_16x16x32_bf16 v[52:55], a[232:235], v[28:31], v[52:55]\n"
    "s_waitcnt lgkmcnt(2)\n"
    "v_mfma_f32_16x16x32_bf16 v[64:67], a[236:239], v[28:31], v[64:67]\n"
    "ds_read_b128 v[28:31], v0 offset:23552\n"
    "v_mfma_f32_16x16x32_bf16 v[48:51], a[240:243], v[16:19], v[48:51]\n"
    "s_add_u32 s76, s76, 16384\n"
    "s_add_u32 m0, s59, 69632\n"
    "v_mfma_f32_16x16x32_bf16 v[52:55], a[244:247], v[16:19], v[52:55]\n"
    "s_waitcnt lgkmcnt(2)\n"
    "buffer_load_dwordx4 v1, s[36:39], s76 offen sc1 lds\n"
    "v_mfma_f32_16x16x32_bf16 v[64:67], a[248:251], v[16:19], v[64:67]\n"
    "ds_read_b128 v[16:19], v0 offset:24576\n"
    "v_mfma_f32_16x16x32_bf16 v[48:51], a[252:255], v[20:23], v[48:51]\n"
    "v_mfma_f32_16x16x32_bf16 v[52:55], v[80:83], v[20:23], v[52:55]\n"
    "s_waitcnt lgkmcnt(2)\n"
    "v_mfma_f32_16x16x32_bf16 v[64:67], v[84:87], v[20:23], v[64:67]\n"
    "ds_read_b128 v[20:23], v0 offset:25600\n"
    "v_mfma_f32_16x16x32_bf16 v[48:51], v[88:91], v[24:27], v[48:51]\n"
    "v_mfma_f32_16x16x32_bf16 v[52:55], v[92:95], v[24:27], v[52:55]\n"
    "s_waitcnt lgkmcnt(2)\n"
    "v_mfma_f32_16x16x32_bf16 v[64:67], v[96:99], v[24:27], v[64:67]\n"
    "ds_read_b128 v[24:27], v0 offset:26624\n"
    "v_mfma_f32_16x16x32_bf16 v[48:51], v[100:103], v[28:31], v[48:51]\n"
    "v_mfma_f32_16x16x32_bf16 v[52:55], v[104:107], v[28:31], v[52:55]\n"
    "s_waitcnt lgkmcnt(2)\n"
    "v_mfma_f32_16x16x32_bf16 v[64:67], v[108:111], v[28:31], v[64:67]\n"
    "ds_read_b128 v[28:31], v0 offset:27648\n"
    ".LgDOWN_join0_%=:\n"
    "v_mfma_f32_16x16x32_bf16 v[48:51], v[112:115], v[16:19], v[48:51]\n"
    "s_add_u32 s71, s68, 2\n"
    "s_waitcnt vmcnt(9)\n"
    "v_mfma_f32_16x16x32_bf16 v[52:55], v[116:119], v[16:19], v[52:55]\n"
    "s_waitcnt lgkmcnt(2)\n"
    "v_add_u32 v13, v11, v3\n"
    "v_mfma_f32_16x16x32_bf16 v[64:67], v[120:123], v[16:19], v[64:67]\n"
    "ds_read_b128 v[16:19], v0 offset:28672\n"
    "v_cmp_gt_i32 vcc, s71, v13\n"
    "v_mfma_f32_16x16x32_bf16 v[48:51], v[124:127], v[20:23], v[48:51]\n"
    "v_mfma_f32_16x16x32_bf16 v[52:55], v[128:131], v[20:23], v[52:55]\n"
    "s_waitcnt lgkmcnt(2)\n"
    "v_mfma_f32_16x16x32_bf16 v[64:67], v[132:135], v[20:23], v[64:67]\n"
    "ds_read_b128 v[20:23], v0 offset:29696\n"
    "v_mfma_f32_16x16x32_bf16 v[48:51], v[136:139], v[24:27], v[48:51]\n"
    "s_or_b64 exec, vcc, s[62:63]\n"
    "buffer_atomic_smax v11, v2, s[36:39], 0 offen sc0 sc1\n"
    "s_mov_b64 exec, -1\n"
    "v_mfma_f32_16x16x32_bf16 v[52:55], v[140:143], v[24:27], v[52:55]\n"
    "s_waitcnt lgkmcnt(2)\n"
    "s_lshr_b32 s76, s71, 1\n"
    "v_mfma_f32_16x16x32_bf16 v[64:67], v[144:147], v[24:27], v[64:67]\n"
    "ds_read_b128 v[24:27], v0 offset:30720\n"
    "s_and_b32 s76, s76, s54\n"
    "v_mfma_f32_16x16x32_bf16 v[48:51], v[148:151], v[28:31], v[48:51]\n"
    "s_mul_i32 s76, s76, s53\n"
    "v_mfma_f32_16x16x32_bf16 v[52:55], v[152:155], v[28:31], v[52:55]\n"
    "s_waitcnt lgkmcnt(2)\n"
    "s_add_u32 s76, s76, s52\n"
    "v_mfma_f32_16x16x32_bf16 v[64:67], v[156:159], v[28:31], v[64:67]\n"
    "ds_read_b128 v[28:31], v0 offset:31744\n"
    "v_mfma_f32_16x16x32_bf16 v[48:51], v[160:163], v[16:19], v[48:51]\n"
    "v_mfma_f32_16x16x32_bf16 v[52:55], v[164:167], v[16:19], v[52:55]\n"
    "s_waitcnt lgkmcnt(2)\n"
    "v_mfma_f32_16x16x32_bf16 v[64:67], v[168:171], v[16:19], v[64:67]\n"
    "ds_read_b128 v[16:19], v0 offset:32768\n"
    "v_mfma_f32_16x16x32_bf16 v[48:51], v[172:175], v[20:23], v[48:51]\n"
    "v_mfma_f32_16x16x32_bf16 v[52:55], v[176:179], v[20:23], v[52:55]\n"
    "s_waitcnt lgkmcnt(2)\n"
    "v_mfma_f32_16x16x32_bf16 v[64:67], v[180:183], v[20:23], v[64:67]\n"
    "ds_read_b128 v[20:23], v0 offset:33792\n"
    "v_mfma_f32_16x16x32_bf16 v[48:51], v[184:187], v[24:27], v[48:51]\n"
    "v_mfma_f32_16x16x32_bf16 v[52:55], v[188:191], v[24:27], v[52:55]\n"
    "s_waitcnt lgkmcnt(2)\n"
    "v_mfma_f32_16x16x32_bf16 v[64:67], v[192:195], v[24:27], v[64:67]\n"
    "ds_read_b128 v[24:27], v0 offset:34816\n"
    "v_mfma_f32_16x16x32_bf16 v[48:51], v[196:199], v[28:31], v[48:51]\n"
    "v_mfma_f32_16x16x32_bf16 v[52:55], v[200:203], v[28:31], v[52:55]\n"
    "s_waitcnt lgkmcnt(2)\n"
    "v_add_u32 v13, v5, v3\n"
    "v_mfma_f32_16x16x32_bf16 v[64:67], v[204:207], v[28:31], v[64:67]\n"
    "ds_read_b128 v[28:31], v0 offset:35840\n"
    "v_cmp_gt_i32 vcc, s71, v13\n"
    "v_mfma_f32_16x16x32_bf16 v[48:51], v[208:211], v[16:19], v[48:51]\n"
    "v_mfma_f32_16x16x32_bf16 v[52:55], v[212:215], v[16:19], v[52:55]\n"
    "s_waitcnt lgkmcnt(2)\n"
    "v_mfma_f32_16x16x32_bf16 v[64:67], v[216:219], v[16:19], v[64:67]\n"
    "v_mfma_f32_16x16x32_bf16 v[48:51], v[220:223], v[20:23], v[48:51]\n"
    "s_waitcnt vmcnt(1) lgkmcnt(0)\n"
    "s_barrier\n"
    "ds_read_b128 v[16:19], v9 offset:0\n"
    "s_or_b64 exec, vcc, s[62:63]\n"
    "buffer_atomic_smax v5, v2, s[36:39], 0 offen sc0 sc1\n"
    "s_mov_b64 exec, -1\n"
    "v_mfma_f32_16x16x32_bf16 v[52:55], v[224:227], v[20:23], v[52:55]\n"
    "v_mfma_f32_16x16x32_bf16 v[64:67], v[228:231], v[20:23], v[64:67]\n"
    "ds_read_b128 v[20:23], v9 offset:1024\n"
    "v_mfma_f32_16x16x32_bf16 v[48:51], v[232:235], v[24:27], v[48:51]\n"
    "v_mfma_f32_16x16x32_bf16 v[52:55], v[236:239], v[24:27], v[52:55]\n"
    "v_mfma_f32_16x16x32_bf16 v[64:67], v[240:243], v[24:27], v[64:67]\n"
    "ds_read_b128 v[24:27], v9 offset:2048\n"
    "v_mfma_f32_16x16x32_bf16 v[48:51], v[244:247], v[28:31], v[48:51]\n"
    "s_mov_b32 s69, 0x80000000\n"
    "s_mov_b32 s70, 0\n"
    "v_mov_b32 v12, s68\n"
    "v_mfma_f32_16x16x32_bf16 v[52:55], v[248:251], v[28:31], v[52:55]\n"
    "s_waitcnt lgkmcnt(2)\n"
    "s_add_u32 s68, s68, 1\n"
    "v_mfma_f32_16x16x32_bf16 v[64:67], v[252:255], v[28:31], v[64:67]\n"
    "ds_read_b128 v[28:31], v9 offset:3072\n"
    ".LgDOWN_head1_%=:\n"
    "v_mfma_f32_16x16x32_bf16 v[56:59], a[0:3], v[16:19], 0\n"
    "v_mfma_f32_16x16x32_bf16 v[60:63], a[4:7], v[16:19], 0\n"
    "s_waitcnt lgkmcnt(2)\n"
    "v_cvt_pk_bf16_f32 v48, v48, v49\n"
    "v_cvt_pk_bf16_f32 v49, v50, v51\n"
    "v_mfma_f32_16x16x32_bf16 v[68:71], a[8:11], v[16:19], 0\n"
    "ds_read_b128 v[16:19], v9 offset:4096\n"
    "v_cvt_pk_bf16_f32 v50, v52, v53\n"
    "v_mfma_f32_16x16x32_bf16 v[56:59], a[12:15], v[20:23], v[56:59]\n"
    "v_cvt_pk_bf16_f32 v51, v54, v55\n"
    "v_mfma_f32_16x16x32_bf16 v[60:63], a[16:19], v[20:23], v[60:63]\n"
    "s_waitcnt lgkmcnt(2)\n"
    "v_cvt_pk_bf16_f32 v64, v64, v65\n"
    "v_cvt_pk_bf16_f32 v65, v66, v67\n"
    "v_mfma_f32_16x16x32_bf16 v[68:71], a[20:23], v[20:23], v[68:71]\n"
    "ds_read_b128 v[20:23], v9 offset:5120\n"
    "s_nop 0\n"
    "v_mfma_f32_16x16x32_bf16 v[56:59], a[24:27], v[24:27], v[56:59]\n"
    "buffer_store_dwordx4 v[48:51], v15, s[36:39], s73 offen sc1\n"
    "v_mfma_f32_16x16x32_bf16 v[60:63], a[28:31], v[24:27], v[60:63]\n"
    "s_waitcnt lgkmcnt(2)\n"
    "s_waitcnt vmcnt(2)\n"
    "v_add_u32 v13, v11, v3\n"
    "v_mfma_f32_16x16x32_bf16 v[68:71], a[32:35], v[24:27], v[68:71]\n"
    "ds_read_b128 v[24:27], v9 offset:6144\n"
    "v_cmp_gt_i32 vcc, s71, v13\n"
    "v_mfma_f32_16x16x32_bf16 v[56:59], a[36:39], v[28:31], v[56:59]\n"
    "buffer_atomic_smax v7, v14, s[36:39], s69 offen sc1\n"
    "v_permlane16_swap_b32 v64, v66\n"
    "v_mfma_f32_16x16x32_bf16 v[60:63], a[40:43], v[28:31], v[60:63]\n"
    "s_waitcnt lgkmcnt(2)\n"
    "v_permlane16_swap_b32 v65, v67\n"
    "v_mfma_f32_16x16x32_bf16 v[68:71], a[44:47], v[28:31], v[68:71]\n"
    "ds_read_b128 v[28:31], v9 offset:7168\n"
    "s_cbranch_vccnz .LgDOWN_poll1_%=\n"
    "v_mfma_f32_16x16x32_bf16 v[56:59], a[48:51], v[16:19], v[56:59]\n"
    "s_add_u32 m0, s59, 0\n"
    "v_mfma_f32_16x16x32_bf16 v[60:63], a[52:55], v[16:19], v[60:63]\n"
    "s_waitcnt lgkmcnt(2)\n"
    "buffer_load_dwordx4 v1, s[36:39], s76 offen sc1 lds\n"
    "v_mfma_f32_16x16x32_bf16 v[68:71], a[56:59], v[16:19], v[68:71]\n"
    "ds_read_b128 v[16:19], v9 offset:8192\n"
    "v_mfma_f32_16x16x32_bf16 v[56:59], a[60:63], v[20:23], v[56:59]\n"
    "buffer_store_dwordx4 v[64:67], v4, s[36:39], s73 offen sc1\n"
    "v_mfma_f32_16x16x32_bf16 v[60:63], a[64:67], v[20:23], v[60:63]\n"
    "s_waitcnt lgkmcnt(2)\n"
    "s_add_u32 s76, s76, 16384\n"
    "s_lshr_b32 s73, s68, 1\n"
    "v_mfma_f32_16x16x32_bf16 v[68:71], a[68:71], v[20:23], v[68:71]\n"
    "ds_read_b128 v[20:23], v9 offset:9216\n"
    "s_and_b32 s73, s73, s57\n"
    "v_mfma_f32_16x16x32_bf16 v[56:59], a[72:75], v[24:27], v[56:59]\n"
    "s_add_u32 m0, s59, 4096\n"
    "s_mul_i32 s73, s73, s56\n"
    "v_mfma_f32_16x16x32_bf16 v[60:63], a[76:79], v[24:27], v[60:63]\n"
    "s_waitcnt lgkmcnt(2)\n"
    "buffer_load_dwordx4 v1, s[36:39], s76 offen sc1 lds\n"
    "s_add_u32 s73, s73, s55\n"
    "v_mfma_f32_16x16x32_bf16 v[68:71], a[80:83], v[24:27], v[68:71]\n"
    "ds_read_b128 v[24:27], v9 offset:10240\n"
    "v_mfma_f32_16x16x32_bf16 v[56:59], a[84:87], v[28:31], v[56:59]\n"
    "v_mfma_f32_16x16x32_bf16 v[60:63], a[88:91], v[28:31], v[60:63]\n"
    "s_waitcnt lgkmcnt(2)\n"
    "s_add_u32 s76, s76, 16384\n"
    "s_add_u32 m0, s59, 8192\n"
    "v_mfma_f32_16x16x32_bf16 v[68:71], a[92:95], v[28:31], v[68:71]\n"
    "ds_read_b128 v[28:31], v9 offset:11264\n"
    "buffer_load_dwordx4 v1, s[36:39], s76 offen sc1 lds\n"
    "v_mfma_f32_16x16x32_bf16 v[56:59], a[96:99], v[16:19], v[56:59]\n"
    "v_mfma_f32_16x16x32_bf16 v[60:63], a[100:103], v[16:19], v[60:63]\n"
    "s_waitcnt lgkmcnt(2)\n"
    "v_mfma_f32_16x16x32_bf16 v[68:71], a[104:107], v[16:19], v[68:71]\n"
    "ds_read_b128 v[16:19], v9 offset:12288\n"
    "v_mfma_f32_16x16x32_bf16 v[56:59], a[108:111], v[20:23], v[56:59]\n"
    "v_mfma_f32_16x16x32_bf16 v[60:63], a[112:115], v[20:23], v[60:63]\n"
    "s_waitcnt lgkmcnt(2)\n"
    "v_mfma_f32_16x16x32_bf16 v[68:71], a[116:119], v[20:23], v[68:71]\n"
    "ds_read_b128 v[20:23], v9 offset:13312\n"
    "s_add_u32 s76, s76, 16384\n"
    "v_mfma_f32_16x16x32_bf16 v[56:59], a[120:123], v[24:27], v[56:59]\n"
    "s_add_u32 m0, s59, 12288\n"
    "v_mfma_f32_16x16x32_bf16 v[60:63], a[124:127], v[24:27], v[60:63]\n"
    "s_waitcnt lgkmcnt(2)\n"
    "buffer_load_dwordx4 v1, s[36:39], s76 offen sc1 lds\n"
    "v_mfma_f32_16x16x32_bf16 v[68:71], a[128:131], v[24:27], v[68:71]\n"
    "ds_read_b128 v[24:27], v9 offset:14336\n"
    "v_mfma_f32_16x16x32_bf16 v[56:59], a[132:135], v[28:31], v[56:59]\n"
    "v_mfma_f32_16x16x32_bf16 v[60:63], a[136:139], v[28:31], v[60:63]\n"
    "s_waitcnt lgkmcnt(2)\n"
    "v_mfma_f32_16x16x32_bf16 v[68:71], a[140:143], v[28:31], v[68:71]\n"
    "ds_read_b128 v[28:31], v9 offset:15360\n"
    "s_add_u32 s76, s76, 16384\n"
    "v_mfma_f32_16x16x32_bf16 v[56:59], a[144:147], v[16:19], v[56:59]\n"
    "s_add_u32 m0, s59, 16384\n"
    "v_mfma_f32_16x16x32_bf16 v[60:63], a[148:151], v[16:19], v[60:63]\n"
    "s_waitcnt lgkmcnt(2)\n"
    "buffer_load_dwordx4 v1, s[36:39], s76 offen sc1 lds\n"
    "v_mfma_f32_16x16x32_bf16 v[68:71], a[152:155], v[16:19], v[68:71]\n"
    "ds_read_b128 v[16:19], v9 offset:16384\n"
    "v_mfma_f32_16x16x32_bf16 v[56:59], a[156:159], v[20:23], v[56:59]\n"
    "v_mfma_f32_16x16x32_bf16 v[60:63], a[160:163], v[20:23], v[60:63]\n"
    "s_waitcnt lgkmcnt(2)\n"
    "v_mfma_f32_16x16x32_bf16 v[68:71], a[164:167], v[20:23], v[68:71]\n"
    "ds_read_b128 v[20:23], v9 offset:17408\n"
    "s_add_u32 s76, s76, 16384\n"
    "v_mfma_f32_16x16x32_bf16 v[56:59], a[168:171], v[24:27], v[56:59]\n"
    "s_add_u32 m0, s59, 20480\n"
    "v_mfma_f32_16x16x32_bf16 v[60:63], a[172:175], v[24:27], v[60:63]\n"
    "s_waitcnt lgkmcnt(2)\n"
    "buffer_load_dwordx4 v1, s[36:39], s76 offen sc1 lds\n"
    "v_mfma_f32_16x16x32_bf16 v[68:71], a[176:179], v[24:27], v[68:71]\n"
    "ds_read_b128 v[24:27], v9 offset:18432\n"
    "v_mfma_f32_16x16x32_bf16 v[56:59], a[180:183], v[28:31], v[56:59]\n"
    "v_mfma_f32_16x16x32_bf16 v[60:63], a[184:187], v[28:31], v[60:63]\n"
    "s_waitcnt lgkmcnt(2)\n"
    "v_mfma_f32_16x16x32_bf16 v[68:71], a[188:191], v[28:31], v[68:71]\n"
    "ds_read_b128 v[28:31], v9 offset:19456\n"
    "v_mfma_f32_16x16x32_bf16 v[56:59], a[192:195], v[16:19], v[56:59]\n"
    "s_add_u32 s76, s76, 16384\n"
    "s_add_u32 m0, s59, 24576\n"
    "v_mfma_f32_16x16x32_bf16 v[60:63], a[196:199], v[16:19], v[60:63]\n"
    "s_waitcnt lgkmcnt(2)\n"
    "buffer_load_dwordx4 v1, s[36:39], s76 offen sc1 lds\n"
    "v_mfma_f32_16x16x32_bf16 v[68:71], a[200:203], v[16:19], v[68:71]\n"
    "ds_read_b128 v[16:19], v9 offset:20480\n"
    "v_mfma_f32_16x16x32_bf16 v[56:59], a[204:207], v[20:23], v[56:59]\n"
    "v_mfma_f32_16x16x32_bf16 v[60:63], a[208:211], v[20:23], v[60:63]\n"
    "s_waitcnt lgkmcnt(2)\n"
    "v_mfma_f32_16x16x32_bf16 v[68:71], a[212:215], v[20:23], v[68:71]\n"
    "ds_read_b128 v[20:23], v9 offset:21504\n"
    "v_mfma_f32_16x16x32_bf16 v[56:59], a[216:219], v[24:27], v[56:59]\n"
    "s_add_u32 s76, s76, 16384\n"
    "s_add_u32 m0, s59, 28672\n"
    "v_mfma_f32_16x16x32_bf16 v[60:63], a[220:223], v[24:27], v[60:63]\n"
    "s_waitcnt lgkmcnt(2)\n"
    "s_waitcnt vmcnt(6)\n"
    "buffer_atomic_smax v12, v14, s[36:39], s70 offen sc1\n"
    "v_mfma_f32_16x16x32_bf16 v[68:71], a[224:227], v[24:27], v[68:71]\n"
    "ds_read_b128 v[24:27], v9 offset:22528\n"
    "buffer_load_dwordx4 v1, s[36:39], s76 offen sc1 lds\n"
    "v_mfma_f32_16x16x32_bf16 v[56:59], a[228:231], v[28:31], v[56:59]\n"
    "v_mfma_f32_16x16x32_bf16 v[60:63], a[232:235], v[28:31], v[60:63]\n"
    "s_waitcnt lgkmcnt(2)\n"
    "v_mfma_f32_16x16x32_bf16 v[68:71], a[236:239], v[28:31], v[68:71]\n"
    "ds_read_b128 v[28:31], v9 offset:23552\n"
    "v_mfma_f32_16x16x32_bf16 v[56:59], a[240:243], v[16:19], v[56:59]\n"
    "s_add_u32 s76, s76, 16384\n"
    "s_add_u32 m0, s59, 32768\n"
    "v_mfma_f32_16x16x32_bf16 v[60:63], a[244:247], v[16:19], v[60:63]\n"
    "s_waitcnt lgkmcnt(2)\n"
    "buffer_load_dwordx4 v1, s[36:39], s76 offen sc1 lds\n"
    "v_mfma_f32_16x16x32_bf16 v[68:71], a[248:251], v[16:19], v[68:71]\n"
    "ds_read_b128 v[16:19], v9 offset:24576\n"
    "v_mfma_f32_16x16x32_bf16 v[56:59], a[252:255], v[20:23], v[56:59]\n"
    "v_mfma_f32_16x16x32_bf16 v[60:63], v[80:83], v[20:23], v[60:63]\n"
    "s_waitcnt lgkmcnt(2)\n"
    "v_mfma_f32_16x16x32_bf16 v[68:71], v[84:87], v[20:23], v[68:71]\n"
    "ds_read_b128 v[20:23], v9 offset:25600\n"
    "v_mfma_f32_16x16x32_bf16 v[56:59], v[88:91], v[24:27], v[56:59]\n"
    "v_mfma_f32_16x16x32_bf16 v[60:63], v[92:95], v[24:27], v[60:63]\n"
    "s_waitcnt lgkmcnt(2)\n"
    "v_mfma_f32_16x16x32_bf16 v[68:71], v[96:99], v[24:27], v[68:71]\n"
    "ds_read_b128 v[24:27], v9 offset:26624\n"
    "v_mfma_f32_16x16x32_bf16 v[56:59], v[100:103], v[28:31], v[56:59]\n"
    "v_mfma_f32_16x16x32_bf16 v[60:63], v[104:107], v[28:31], v[60:63]\n"
    "s_waitcnt lgkmcnt(2)\n"
    "v_mfma_f32_16x16x32_bf16 v[68:71], v[108:111], v[28:31], v[68:71]\n"
    "ds_read_b128 v[28:31], v9 offset:27648\n"
    ".LgDOWN_join1_%=:\n"
    "v_mfma_f32_16x16x32_bf16 v[56:59], v[112:115], v[16:19], v[56:59]\n"
    "s_add_u32 s71, s68, 2\n"
    "s_waitcnt vmcnt(9)\n"
    "v_mfma_f32_16x16x32_bf16 v[60:63], v[116:119], v[16:19], v[60:63]\n"
    "s_waitcnt lgkmcnt(2)\n"
    "v_add_u32 v13, v11, v3\n"
    "v_mfma_f32_16x16x32_bf16 v[68:71], v[120:123], v[16:19], v[68:71]\n"
    "ds_read_b128 v[16:19], v9 offset:28672\n"
    "v_cmp_gt_i32 vcc, s71, v13\n"
    "v_mfma_f32_16x16x32_bf16 v[56:59], v[124:127], v[20:23], v[56:59]\n"
    "v_mfma_f32_16x16x32_bf16 v[60:63], v[128:131], v[20:23], v[60:63]\n"
    "s_waitcnt lgkmcnt(2)\n"
    "v_mfma_f32_16x16x32_bf16 v[68:71], v[132:135], v[20:23], v[68:71]\n"
    "ds_read_b128 v[20:23], v9 offset:29696\n"
    "v_mfma_f32_16x16x32_bf16 v[56:59], v[136:139], v[24:27], v[56:59]\n"
    "s_or_b64 exec, vcc, s[62:63]\n"
    "buffer_atomic_smax v11, v2, s[36:39], 0 offen sc0 sc1\n"
    "s_mov_b64 exec, -1\n"
    "v_mfma_f32_16x16x32_bf16 v[60:63], v[140:143], v[24:27], v[60:63]\n"
    "s_waitcnt lgkmcnt(2)\n"
    "s_lshr_b32 s76, s71, 1\n"
    "v_mfma_f32_16x16x32_bf16 v[68:71], v[144:147], v[24:27], v[68:71]\n"
    "ds_read_b128 v[24:27], v9 offset:30720\n"
    "s_and_b32 s76, s76, s54\n"
    "v_mfma_f32_16x16x32_bf16 v[56:59], v[148:151], v[28:31], v[56:59]\n"
    "s_mul_i32 s76, s76, s53\n"
    "v_mfma_f32_16x16x32_bf16 v[60:63], v[152:155], v[28:31], v[60:63]\n"
    "s_waitcnt lgkmcnt(2)\n"
    "s_add_u32 s76, s76, s52\n"
    "v_mfma_f32_16x16x32_bf16 v[68:71], v[156:159], v[28:31], v[68:71]\n"
    "ds_read_b128 v[28:31], v9 offset:31744\n"
    "s_add_u32 s76, s76, 1024\n"
    "v_mfma_f32_16x16x32_bf16 v[56:59], v[160:163], v[16:19], v[56:59]\n"
    "v_mfma_f32_16x16x32_bf16 v[60:63], v[164:167], v[16:19], v[60:63]\n"
    "s_waitcnt lgkmcnt(2)\n"
    "v_mfma_f32_16x16x32_bf16 v[68:71], v[168:171], v[16:19], v[68:71]\n"
    "ds_read_b128 v[16:19], v9 offset:32768\n"
    "v_mfma_f32_16x16x32_bf16 v[56:59], v[172:175], v[20:23], v[56:59]\n"
    "v_mfma_f32_16x16x32_bf16 v[60:63], v[176:179], v[20:23], v[60:63]\n"
    "s_waitcnt lgkmcnt(2)\n"
    "v_mfma_f32_16x16x32_bf16 v[68:71], v[180:183], v[20:23], v[68:71]\n"
    "ds_read_b128 v[20:23], v9 offset:33792\n"
    "v_mfma_f32_16x16x32_bf16 v[56:59], v[184:187], v[24:27], v[56:59]\n"
    "v_mfma_f32_16x16x32_bf16 v[60:63], v[188:191], v[24:27], v[60:63]\n"
    "s_waitcnt lgkmcnt(2)\n"
    "v_mfma_f32_16x16x32_bf16 v[68:71], v[192:195], v[24:27], v[68:71]\n"
    "ds_read_b128 v[24:27], v9 offset:34816\n"
    "v_mfma_f32_16x16x32_bf16 v[56:59], v[196:199], v[28:31], v[56:59]\n"
    "v_mfma_f32_16x16x32_bf16 v[60:63], v[200:203], v[28:31], v[60:63]\n"
    "s_waitcnt lgkmcnt(2)\n"
    "v_add_u32 v13, v5, v3\n"
    "v_mfma_f32_16x16x32_bf16 v[68:71], v[204:207], v[28:31], v[68:71]\n"
    "ds_read_b128 v[28:31], v9 offset:35840\n"
    "v_cmp_gt_i32 vcc, s71, v13\n"
    "v_mfma_f32_16x16x32_bf16 v[56:59], v[208:211], v[16:19], v[56:59]\n"
    "v_mfma_f32_16x16x32_bf16 v[60:63], v[212:215], v[16:19], v[60:63]\n"
    "s_waitcnt lgkmcnt(2)\n"
    "v_mfma_f32_16x16x32_bf16 v[68:71], v[216:219], v[16:19], v[68:71]\n"
    "v_mfma_f32_16x16x32_bf16 v[56:59], v[220:223], v[20:23], v[56:59]\n"
    "s_waitcnt vmcnt(1) lgkmcnt(0)\n"
    "s_barrier\n"
    "ds_read_b128 v[16:19], v0 offset:0\n"
    "s_or_b64 exec, vcc, s[62:63]\n"
    "buffer_atomic_smax v5, v2, s[36:39], 0 offen sc0 sc1\n"
    "s_mov_b64 exec, -1\n"
    "v_mfma_f32_16x16x32_bf16 v[60:63], v[224:227], v[20:23], v[60:63]\n"
    "v_mfma_f32_16x16x32_bf16 v[68:71], v[228:231], v[20:23], v[68:71]\n"
    "ds_read_b128 v[20:23], v0 offset:1024\n"
    "v_mfma_f32_16x16x32_bf16 v[56:59], v[232:235], v[24:27], v[56:59]\n"
    "v_mfma_f32_16x16x32_bf16 v[60:63], v[236:239], v[24:27], v[60:63]\n"
    "v_mfma_f32_16x16x32_bf16 v[68:71], v[240:243], v[24:27], v[68:71]\n"
    "ds_read_b128 v[24:27], v0 offset:2048\n"
    "v_mfma_f32_16x16x32_bf16 v[56:59], v[244:247], v[28:31], v[56:59]\n"
    "s_mov_b32 s69, 0x80000000\n"
    "s_mov_b32 s70, 0\n"
    "v_mov_b32 v12, s68\n"
    "v_mfma_f32_16x16x32_bf16 v[60:63], v[248:251], v[28:31], v[60:63]\n"
    "s_waitcnt lgkmcnt(2)\n"
    "s_add_u32 s68, s68, 1\n"
    "v_mfma_f32_16x16x32_bf16 v[68:71], v[252:255], v[28:31], v[68:71]\n"
    "ds_read_b128 v[28:31], v0 offset:3072\n"
    "s_branch .LgDOWN_head0_%=\n"
    ".LgDOWN_LOOPE_%=:\n"
    ".LgDOWN_done_%=:\n"
    "s_waitcnt vmcnt(0)\n"
    "buffer_atomic_smax v12, v10, s[36:39], 0 offen sc1\n"
    "s_waitcnt vmcnt(0)\n"
    "s_branch .LgDOWN_FIN_%=\n"
    ".LgDOWN_poll0_%=:\n"
    "v_mfma_f32_16x16x32_bf16 v[48:51], a[48:51], v[16:19], v[48:51]\n"
    "s_or_b64 exec, vcc, s[62:63]\n"
    "buffer_atomic_smax v11, v2, s[36:39], 0 offen sc0 sc1\n"
    "s_mov_b64 exec, -1\n"
    "v_mfma_f32_16x16x32_bf16 v[52:55], a[52:55], v[16:19], v[52:55]\n"
    "s_waitcnt lgkmcnt(2)\n"
    "v_mfma_f32_16x16x32_bf16 v[64:67], a[56:59], v[16:19], v[64:67]\n"
    "ds_read_b128 v[16:19], v0 offset:8192\n"
    "v_mfma_f32_16x16x32_bf16 v[48:51], a[60:63], v[20:23], v[48:51]\n"
    "buffer_store_dwordx4 v[68:71], v4, s[36:39], s73 offen offset:1024 sc1\n"
    "v_mfma_f32_16x16x32_bf16 v[52:55], a[64:67], v[20:23], v[52:55]\n"
    "s_waitcnt lgkmcnt(2)\n"
    "s_lshr_b32 s73, s68, 1\n"
    "v_mfma_f32_16x16x32_bf16 v[64:67], a[68:71], v[20:23], v[64:67]\n"
    "ds_read_b128 v[20:23], v0 offset:9216\n"
    "s_and_b32 s73, s73, s57\n"
    "v_mfma_f32_16x16x32_bf16 v[48:51], a[72:75], v[24:27], v[48:51]\n"
    "s_mul_i32 s73, s73, s56\n"
    "v_mfma_f32_16x16x32_bf16 v[52:55], a[76:79], v[24:27], v[52:55]\n"
    "s_waitcnt lgkmcnt(2)\n"
    "s_add_u32 s73, s73, s55\n"
    "v_mfma_f32_16x16x32_bf16 v[64:67], a[80:83], v[24:27], v[64:67]\n"
    "ds_read_b128 v[24:27], v0 offset:10240\n"
    "v_mfma_f32_16x16x32_bf16 v[48:51], a[84:87], v[28:31], v[48:51]\n"
    "v_mfma_f32_16x16x32_bf16 v[52:55], a[88:91], v[28:31], v[52:55]\n"
    "s_waitcnt lgkmcnt(2)\n"
    "v_mfma_f32_16x16x32_bf16 v[64:67], a[92:95], v[28:31], v[64:67]\n"
    "ds_read_b128 v[28:31], v0 offset:11264\n"
    "v_mfma_f32_16x16x32_bf16 v[48:51], a[96:99], v[16:19], v[48:51]\n"
    "v_mfma_f32_16x16x32_bf16 v[52:55], a[100:103], v[16:19], v[52:55]\n"
    "s_waitcnt lgkmcnt(2)\n"
    "v_mfma_f32_16x16x32_bf16 v[64:67], a[104:107], v[16:19], v[64:67]\n"
    "ds_read_b128 v[16:19], v0 offset:12288\n"
    "v_mfma_f32_16x16x32_bf16 v[48:51], a[108:111], v[20:23], v[48:51]\n"
    "v_mfma_f32_16x16x32_bf16 v[52:55], a[112:115], v[20:23], v[52:55]\n"
    "s_waitcnt lgkmcnt(2)\n"
    "s_waitcnt vmcnt(4)\n"
    "v_add_u32 v13, v5, v3\n"
    "v_mfma_f32_16x16x32_bf16 v[64:67], a[116:119], v[20:23], v[64:67]\n"
    "ds_read_b128 v[20:23], v0 offset:13312\n"
    "v_cmp_gt_i32 vcc, s71, v13\n"
    "v_mfma_f32_16x16x32_bf16 v[48:51], a[120:123], v[24:27], v[48:51]\n"
    "v_mfma_f32_16x16x32_bf16 v[52:55], a[124:127], v[24:27], v[52:55]\n"
    "s_waitcnt lgkmcnt(2)\n"
    "v_mfma_f32_16x16x32_bf16 v[64:67], a[128:131], v[24:27], v[64:67]\n"
    "ds_read_b128 v[24:27], v0 offset:14336\n"
    "s_cbranch_vccz .LgDOWN_dma0_33_%=\n"
    "v_mfma_f32_16x16x32_bf16 v[48:51], a[132:135], v[28:31], v[48:51]\n"
    "s_or_b64 exec, vcc, s[62:63]\n"
    "buffer_atomic_smax v5, v2, s[36:39], 0 offen sc0 sc1\n"
    "s_mov_b64 exec, -1\n"
    "v_mfma_f32_16x16x32_bf16 v[52:55], a[136:139], v[28:31], v[52:55]\n"
    "s_waitcnt lgkmcnt(2)\n"
    "v_mfma_f32_16x16x32_bf16 v[64:67], a[140:143], v[28:31], v[64:67]\n"
    "ds_read_b128 v[28:31], v0 offset:15360\n"
    "v_mfma_f32_16x16x32_bf16 v[48:51], a[144:147], v[16:19], v[48:51]\n"
    "v_mfma_f32_16x16x32_bf16 v[52:55], a[148:151], v[16:19], v[52:55]\n"
    "s_waitcnt lgkmcnt(2)\n"
    "v_mfma_f32_16x16x32_bf16 v[64:67], a[152:155], v[16:19], v[64:67]\n"
    "ds_read_b128 v[16:19], v0 offset:16384\n"
    "v_mfma_f32_16x16x32_bf16 v[48:51], a[156:159], v[20:23], v[48:51]\n"
    "v_mfma_f32_16x16x32_bf16 v[52:55], a[160:163], v[20:23], v[52:55]\n"
    "s_waitcnt lgkmcnt(2)\n"
    "v_mfma_f32_16x16x32_bf16 v[64:67], a[164:167], v[20:23], v[64:67]\n"
    "ds_read_b128 v[20:23], v0 offset:17408\n"
    "v_mfma_f32_16x16x32_bf16 v[48:51], a[168:171], v[24:27], v[48:51]\n"
    "v_mfma_f32_16x16x32_bf16 v[52:55], a[172:175], v[24:27], v[52:55]\n"
    "s_waitcnt lgkmcnt(2)\n"
    "v_mfma_f32_16x16x32_bf16 v[64:67], a[176:179], v[24:27], v[64:67]\n"
    "ds_read_b128 v[24:27], v0 offset:18432\n"
    "v_mfma_f32_16x16x32_bf16 v[48:51], a[180:183], v[28:31], v[48:51]\n"
    "v_mfma_f32_16x16x32_bf16 v[52:55], a[184:187], v[28:31], v[52:55]\n"
    "s_waitcnt lgkmcnt(2)\n"
    "v_mfma_f32_16x16x32_bf16 v[64:67], a[188:191], v[28:31], v[64:67]\n"
    "ds_read_b128 v[28:31], v0 offset:19456\n"
    "v_mfma_f32_16x16x32_bf16 v[48:51], a[192:195], v[16:19], v[48:51]\n"
    "v_mfma_f32_16x16x32_bf16 v[52:55], a[196:199], v[16:19], v[52:55]\n"
    "s_waitcnt lgkmcnt(2)\n"
    "s_waitcnt vmcnt(2)\n"
    "v_add_u32 v13, v11, v3\n"
    "v_mfma_f32_16x16x32_bf16 v[64:67], a[200:203], v[16:19], v[64:67]\n"
    "ds_read_b128 v[16:19], v0 offset:20480\n"
    "v_cmp_gt_i32 vcc, s71, v13\n"
    "v_mfma_f32_16x16x32_bf16 v[48:51], a[204:207], v[20:23], v[48:51]\n"
    "v_mfma_f32_16x16x32_bf16 v[52:55], a[208:211], v[20:23], v[52:55]\n"
    "s_waitcnt lgkmcnt(2)\n"
    "v_mfma_f32_16x16x32_bf16 v[64:67], a[212:215], v[20:23], v[64:67]\n"
    "ds_read_b128 v[20:23], v0 offset:21504\n"
    "s_cbranch_vccz .LgDOWN_dma0_54_%=\n"
    "v_mfma_f32_16x16x32_bf16 v[48:51], a[216:219], v[24:27], v[48:51]\n"
    "s_or_b64 exec, vcc, s[62:63]\n"
    "buffer_atomic_smax v11, v2, s[36:39], 0 offen sc0 sc1\n"
    "s_mov_b64 exec, -1\n"
    "v_mfma_f32_16x16x32_bf16 v[52:55], a[220:223], v[24:27], v[52:55]\n"
    "s_waitcnt lgkmcnt(2)\n"
    "s_waitcnt vmcnt(2)\n"
    "buffer_atomic_smax v12, v10, s[36:39], s70 offen sc1\n"
    "v_mfma_f32_16x16x32_bf16 v[64:67], a[224:227], v[24:27], v[64:67]\n"
    "ds_read_b128 v[24:27], v0 offset:22528\n"
    "v_mfma_f32_16x16x32_bf16 v[48:51], a[228:231], v[28:31], v[48:51]\n"
    "v_mfma_f32_16x16x32_bf16 v[52:55], a[232:235], v[28:31], v[52:55]\n"
    "s_waitcnt lgkmcnt(2)\n"
    "v_mfma_f32_16x16x32_bf16 v[64:67], a[236:239], v[28:31], v[64:67]\n"
    "ds_read_b128 v[28:31], v0 offset:23552\n"
    "v_mfma_f32_16x16x32_bf16 v[48:51], a[240:243], v[16:19], v[48:51]\n"
    "v_mfma_f32_16x16x32_bf16 v[52:55], a[244:247], v[16:19], v[52:55]\n"
    "s_waitcnt lgkmcnt(2)\n"
    "v_mfma_f32_16x16x32_bf16 v[64:67], a[248:251], v[16:19], v[64:67]\n"
    "ds_read_b128 v[16:19], v0 offset:24576\n"
    "v_mfma_f32_16x16x32_bf16 v[48:51], a[252:255], v[20:23], v[48:51]\n"
    "v_mfma_f32_16x16x32_bf16 v[52:55], v[80:83], v[20:23], v[52:55]\n"
    "s_waitcnt lgkmcnt(2)\n"
    "v_mfma_f32_16x16x32_bf16 v[64:67], v[84:87], v[20:23], v[64:67]\n"
    "ds_read_b128 v[20:23], v0 offset:25600\n"
    "v_mfma_f32_16x16x32_bf16 v[48:51], v[88:91], v[24:27], v[48:51]\n"
    "v_mfma_f32_16x16x32_bf16 v[52:55], v[92:95], v[24:27], v[52:55]\n"
    "s_waitcnt lgkmcnt(2)\n"
    "v_mfma_f32_16x16x32_bf16 v[64:67], v[96:99], v[24:27], v[64:67]\n"
    "ds_read_b128 v[24:27], v0 offset:26624\n"
    "v_mfma_f32_16x16x32_bf16 v[48:51], v[100:103], v[28:31], v[48:51]\n"
    "v_mfma_f32_16x16x32_bf16 v[52:55], v[104:107], v[28:31], v[52:55]\n"
    "s_waitcnt lgkmcnt(2)\n"
    "s_waitcnt vmcnt(2)\n"
    "v_add_u32 v13, v5, v3\n"
    "v_mfma_f32_16x16x32_bf16 v[64:67], v[108:111], v[28:31], v[64:67]\n"
    "ds_read_b128 v[28:31], v0 offset:27648\n"
    "v_cmp_gt_i32 vcc, s71, v13\n"
    "v_mfma_f32_16x16x32_bf16 v[48:51], v[112:115], v[16:19], v[48:51]\n"
    "v_mfma_f32_16x16x32_bf16 v[52:55], v[116:119], v[16:19], v[52:55]\n"
    "s_waitcnt lgkmcnt(2)\n"
    "v_mfma_f32_16x16x32_bf16 v[64:67], v[120:123], v[16:19], v[64:67]\n"
    "ds_read_b128 v[16:19], v0 offset:28672\n"
    "s_cbranch_vccz .LgDOWN_dma0_75_%=\n"
    "v_mfma_f32_16x16x32_bf16 v[48:51], v[124:127], v[20:23], v[48:51]\n"
    "s_or_b64 exec, vcc, s[62:63]\n"
    "buffer_atomic_smax v5, v2, s[36:39], 0 offen sc0 sc1\n"
    "s_mov_b64 exec, -1\n"
    "v_mfma_f32_16x16x32_bf16 v[52:55], v[128:131], v[20:23], v[52:55]\n"
    "s_waitcnt lgkmcnt(2)\n"
    "v_mfma_f32_16x16x32_bf16 v[64:67], v[132:135], v[20:23], v[64:67]\n"
    "ds_read_b128 v[20:23], v0 offset:29696\n"
    "v_mfma_f32_16x16x32_bf16 v[48:51], v[136:139], v[24:27], v[48:51]\n"
    "v_mfma_f32_16x16x32_bf16 v[52:55], v[140:143], v[24:27], v[52:55]\n"
    "s_waitcnt lgkmcnt(2)\n"
    "v_mfma_f32_16x16x32_bf16 v[64:67], v[144:147], v[24:27], v[64:67]\n"
    "ds_read_b128 v[24:27], v0 offset:30720\n"
    "v_mfma_f32_16x16x32_bf16 v[48:51], v[148:151], v[28:31], v[48:51]\n"
    "v_mfma_f32_16x16x32_bf16 v[52:55], v[152:155], v[28:31], v[52:55]\n"
    "s_waitcnt lgkmcnt(2)\n"
    "v_mfma_f32_16x16x32_bf16 v[64:67], v[156:159], v[28:31], v[64:67]\n"
    "ds_read_b128 v[28:31], v0 offset:31744\n"
    "v_mfma_f32_16x16x32_bf16 v[48:51], v[160:163], v[16:19], v[48:51]\n"
    "v_mfma_f32_16x16x32_bf16 v[52:55], v[164:167], v[16:19], v[52:55]\n"
    "s_waitcnt lgkmcnt(2)\n"
    "v_mfma_f32_16x16x32_bf16 v[64:67], v[168:171], v[16:19], v[64:67]\n"
    "ds_read_b128 v[16:19], v0 offset:32768\n"
    "v_mfma_f32_16x16x32_bf16 v[48:51], v[172:175], v[20:23], v[48:51]\n"
    "v_mfma_f32_16x16x32_bf16 v[52:55], v[176:179], v[20:23], v[52:55]\n"
    "s_waitcnt lgkmcnt(2)\n"
    "v_mfma_f32_16x16x32_bf16 v[64:67], v[180:183], v[20:23], v[64:67]\n"
    "ds_read_b128 v[20:23], v0 offset:33792\n"
    "v_mfma_f32_16x16x32_bf16 v[48:51], v[184:187], v[24:27], v[48:51]\n"
    "v_mfma_f32_16x16x32_bf16 v[52:55], v[188:191], v[24:27], v[52:55]\n"
    "s_waitcnt lgkmcnt(2)\n"
    "s_waitcnt vmcnt(2)\n"
    "v_add_u32 v13, v11, v3\n"
    "v_mfma_f32_16x16x32_bf16 v[64:67], v[192:195], v[24:27], v[64:67]\n"
    "ds_read_b128 v[24:27], v0 offset:34816\n"
    "v_cmp_gt_i32 vcc, s71, v13\n"
    "v_mfma_f32_16x16x32_bf16 v[48:51], v[196:199], v[28:31], v[48:51]\n"
    "v_mfma_f32_16x16x32_bf16 v[52:55], v[200:203], v[28:31], v[52:55]\n"
    "s_waitcnt lgkmcnt(2)\n"
    "v_mfma_f32_16x16x32_bf16 v[64:67], v[204:207], v[28:31], v[64:67]\n"
    "ds_read_b128 v[28:31], v0 offset:35840\n"
    "s_cbranch_vccz .LgDOWN_dma0_96_%=\n"
    "v_mfma_f32_16x16x32_bf16 v[48:51], v[208:211], v[16:19], v[48:51]\n"
    "s_or_b64 exec, vcc, s[62:63]\n"
    "buffer_atomic_smax v11, v2, s[36:39], 0 offen sc0 sc1\n"
    "s_mov_b64 exec, -1\n"
    "v_mfma_f32_16x16x32_bf16 v[52:55], v[212:215], v[16:19], v[52:55]\n"
    "s_waitcnt lgkmcnt(2)\n"
    "v_mfma_f32_16x16x32_bf16 v[64:67], v[216:219], v[16:19], v[64:67]\n"
    "v_mfma_f32_16x16x32_bf16 v[48:51], v[220:223], v[20:23], v[48:51]\n"
    "v_mfma_f32_16x16x32_bf16 v[52:55], v[224:227], v[20:23], v[52:55]\n"
    "s_waitcnt lgkmcnt(1)\n"
    "v_mfma_f32_16x16x32_bf16 v[64:67], v[228:231], v[20:23], v[64:67]\n"
    "v_mfma_f32_16x16x32_bf16 v[48:51], v[232:235], v[24:27], v[48:51]\n"
    "v_mfma_f32_16x16x32_bf16 v[52:55], v[236:239], v[24:27], v[52:55]\n"
    "s_waitcnt lgkmcnt(0)\n"
    "v_mfma_f32_16x16x32_bf16 v[64:67], v[240:243], v[24:27], v[64:67]\n"
    "v_mfma_f32_16x16x32_bf16 v[48:51], v[244:247], v[28:31], v[48:51]\n"
    "v_mfma_f32_16x16x32_bf16 v[52:55], v[248:251], v[28:31], v[52:55]\n"
    "v_mfma_f32_16x16x32_bf16 v[64:67], v[252:255], v[28:31], v[64:67]\n"
    "v_cvt_pk_bf16_f32 v48, v48, v49\n"
    "v_cvt_pk_bf16_f32 v49, v50, v51\n"
    "s_nop 1\n"
    "v_cvt_pk_bf16_f32 v50, v52, v53\n"
    "v_cvt_pk_bf16_f32 v51, v54, v55\n"
    "buffer_store_dwordx4 v[48:51], v15, s[36:39], s73 offen sc1\n"
    "s_nop 0\n"
    "v_cvt_pk_bf16_f32 v64, v64, v65\n"
    "v_cvt_pk_bf16_f32 v65, v66, v67\n"
    "s_nop 0\n"
    "v_permlane16_swap_b32 v64, v66\n"
    "v_permlane16_swap_b32 v65, v67\n"
    "buffer_store_dwordx4 v[64:67], v4, s[36:39], s73 offen sc1\n"
    "v_mov_b32 v12, s68\n"
    "s_mov_b32 s73, 0x80000000\n"
    "s_mov_b32 s68, s71\n"
    "s_waitcnt vmcnt(3)\n"
    "v_add_u32 v13, v5, v3\n"
    "v_cmp_gt_i32 vcc, s71, v13\n"
    "s_cbranch_vccnz .LgDOWN_idle0_%=\n"
    "s_add_u32 m0, s59, 36864\n"
    "s_nop 0\n"
    "buffer_load_dwordx4 v1, s[36:39], s76 offen sc1 lds\n"
    "s_add_u32 m0, s59, 40960\n"
    "s_add_u32 s76, s76, 16384\n"
    "buffer_load_dwordx4 v1, s[36:39], s76 offen sc1 lds\n"
    "s_add_u32 m0, s59, 45056\n"
    "s_add_u32 s76, s76, 16384\n"
    "buffer_load_dwordx4 v1, s[36:39], s76 offen sc1 lds\n"
    "s_add_u32 m0, s59, 49152\n"
    "s_add_u32 s76, s76, 16384\n"
    "buffer_load_dwordx4 v1, s[36:39], s76 offen sc1 lds\n"
    "s_add_u32 m0, s59, 53248\n"
    "s_add_u32 s76, s76, 16384\n"
    "buffer_load_dwordx4 v1, s[36:39], s76 offen sc1 lds\n"
    "s_add_u32 m0, s59, 57344\n"
    "s_add_u32 s76, s76, 16384\n"
    "buffer_load_dwordx4 v1, s[36:39], s76 offen sc1 lds\n"
    "s_add_u32 m0, s59, 61440\n"
    "s_add_u32 s76, s76, 16384\n"
    "buffer_load_dwordx4 v1, s[36:39], s76 offen sc1 lds\n"
    "s_add_u32 m0, s59, 65536\n"
    "s_add_u32 s76, s76, 16384\n"
    "buffer_load_dwordx4 v1, s[36:39], s76 offen sc1 lds\n"
    "s_add_u32 m0, s59, 69632\n"
    "s_add_u32 s76, s76, 16384\n"
    "buffer_load_dwordx4 v1, s[36:39], s76 offen sc1 lds\n"
    "s_waitcnt vmcnt(9)\n"
    "buffer_atomic_smax v12, v14, s[36:39], 0 offen sc1\n"
    "s_mov_b32 s69, 0x80000000\n"
    "s_mov_b32 s70, 0x80000000\n"
    "v_mov_b32 v7, v12\n"
    "s_waitcnt vmcnt(12)\n"
    "s_add_u32 s71, s68, 1\n"
    "v_add_u32 v13, v11, v3\n"
    "v_cmp_gt_i32 vcc, s71, v13\n"
    "s_nop 0\n"
    "s_or_b64 exec, vcc, s[62:63]\n"
    "buffer_atomic_smax v11, v2, s[36:39], 0 offen sc0 sc1\n"
    "s_mov_b64 exec, -1\n"
    "s_waitcnt vmcnt(2)\n"
    "s_branch .LgDOWN_bar1_%=\n"
    ".LgDOWN_dma0_33_%=:\n"
    "v_mfma_f32_16x16x32_bf16 v[48:51], a[132:135], v[28:31], v[48:51]\n"
    "s_add_u32 m0, s59, 36864\n"
    "v_mfma_f32_16x16x32_bf16 v[52:55], a[136:139], v[28:31], v[52:55]\n"
    "s_waitcnt lgkmcnt(2)\n"
    "buffer_load_dwordx4 v1, s[36:39], s76 offen sc1 lds\n"
    "v_mfma_f32_16x16x32_bf16 v[64:67], a[140:143], v[28:31], v[64:67]\n"
    "ds_read_b128 v[28:31], v0 offset:15360\n"
    "s_add_u32 s76, s76, 16384\n"
    "v_mfma_f32_16x16x32_bf16 v[48:51], a[144:147], v[16:19], v[48:51]\n"
    "s_add_u32 m0, s59, 40960\n"
    "v_mfma_f32_16x16x32_bf16 v[52:55], a[148:151], v[16:19], v[52:55]\n"
    "s_waitcnt lgkmcnt(2)\n"
    "buffer_load_dwordx4 v1, s[36:39], s76 offen sc1 lds\n"
    "v_mfma_f32_16x16x32_bf16 v[64:67], a[152:155], v[16:19], v[64:67]\n"
    "ds_read_b128 v[16:19], v0 offset:16384\n"
    "v_mfma_f32_16x16x32_bf16 v[48:51], a[156:159], v[20:23], v[48:51]\n"
    "s_add_u32 s76, s76, 16384\n"
    "s_add_u32 m0, s59, 45056\n"
    "v_mfma_f32_16x16x32_bf16 v[52:55], a[160:163], v[20:23], v[52:55]\n"
    "s_waitcnt lgkmcnt(2)\n"
    "buffer_load_dwordx4 v1, s[36:39], s76 offen sc1 lds\n"
    "v_mfma_f32_16x16x32_bf16 v[64:67], a[164:167], v[20:23], v[64:67]\n"
    "ds_read_b128 v[20:23], v0 offset:17408\n"
    "v_mfma_f32_16x16x32_bf16 v[48:51], a[168:171], v[24:27], v[48:51]\n"
    "v_mfma_f32_16x16x32_bf16 v[52:55], a[172:175], v[24:27], v[52:55]\n"
    "s_waitcnt lgkmcnt(2)\n"
    "s_add_u32 s76, s76, 16384\n"
    "s_add_u32 m0, s59, 49152\n"
    "v_mfma_f32_16x16x32_bf16 v[64:67], a[176:179], v[24:27], v[64:67]\n"
    "ds_read_b128 v[24:27], v0 offset:18432\n"
    "buffer_load_dwordx4 v1, s[36:39], s76 offen sc1 lds\n"
    "v_mfma_f32_16x16x32_bf16 v[48:51], a[180:183], v[28:31], v[48:51]\n"
    "v_mfma_f32_16x16x32_bf16 v[52:55], a[184:187], v[28:31], v[52:55]\n"
    "s_waitcnt lgkmcnt(2)\n"
    "v_mfma_f32_16x16x32_bf16 v[64:67], a[188:191], v[28:31], v[64:67]\n"
    "ds_read_b128 v[28:31], v0 offset:19456\n"
    "s_add_u32 s76, s76, 16384\n"
    "v_mfma_f32_16x16x32_bf16 v[48:51], a[192:195], v[16:19], v[48:51]\n"
    "s_add_u32 m0, s59, 53248\n"
    "v_mfma_f32_16x16x32_bf16 v[52:55], a[196:199], v[16:19], v[52:55]\n"
    "s_waitcnt lgkmcnt(2)\n"
    "buffer_load_dwordx4 v1, s[36:39], s76 offen sc1 lds\n"
    "v_mfma_f32_16x16x32_bf16 v[64:67], a[200:203], v[16:19], v[64:67]\n"
    "ds_read_b128 v[16:19], v0 offset:20480\n"
    "v_mfma_f32_16x16x32_bf16 v[48:51], a[204:207], v[20:23], v[48:51]\n"
    "s_add_u32 s76, s76, 16384\n"
    "s_add_u32 m0, s59, 57344\n"
    "v_mfma_f32_16x16x32_bf16 v[52:55], a[208:211], v[20:23], v[52:55]\n"
    "s_waitcnt lgkmcnt(2)\n"
    "buffer_load_dwordx4 v1, s[36:39], s76 offen sc1 lds\n"
    "v_mfma_f32_16x16x32_bf16 v[64:67], a[212:215], v[20:23], v[64:67]\n"
    "ds_read_b128 v[20:23], v0 offset:21504\n"
    "v_mfma_f32_16x16x32_bf16 v[48:51], a[216:219], v[24:27], v[48:51]\n"
    "v_mfma_f32_16x16x32_bf16 v[52:55], a[220:223], v[24:27], v[52:55]\n"
    "s_waitcnt lgkmcnt(2)\n"
    "s_waitcnt vmcnt(6)\n"
    "buffer_atomic_smax v12, v10, s[36:39], s70 offen sc1\n"
    "v_mfma_f32_16x16x32_bf16 v[64:67], a[224:227], v[24:27], v[64:67]\n"
    "ds_read_b128 v[24:27], v0 offset:22528\n"
    "s_add_u32 s76, s76, 16384\n"
    "v_mfma_f32_16x16x32_bf16 v[48:51], a[228:231], v[28:31], v[48:51]\n"
    "s_add_u32 m0, s59, 61440\n"
    "v_mfma_f32_16x16x32_bf16 v[52:55], a[232:235], v[28:31], v[52:55]\n"
    "s_waitcnt lgkmcnt(2)\n"
    "buffer_load_dwordx4 v1, s[36:39], s76 offen sc1 lds\n"
    "v_mfma_f32_16x16x32_bf16 v[64:67], a[236:239], v[28:31], v[64:67]\n"
    "ds_read_b128 v[28:31], v0 offset:23552\n"
    "s_add_u32 s76, s76, 16384\n"
    "v_mfma_f32_16x16x32_bf16 v[48:51], a[240:243], v[16:19], v[48:51]\n"
    "s_add_u32 m0, s59, 65536\n"
    "v_mfma_f32_16x16x32_bf16 v[52:55], a[244:247], v[16:19], v[52:55]\n"
    "s_waitcnt lgkmcnt(2)\n"
    "buffer_load_dwordx4 v1, s[36:39], s76 offen sc1 lds\n"
    "v_mfma_f32_16x16x32_bf16 v[64:67], a[248:251], v[16:19], v[64:67]\n"
    "ds_read_b128 v[16:19], v0 offset:24576\n"
    "v_mfma_f32_16x16x32_bf16 v[48:51], a[252:255], v[20:23], v[48:51]\n"
    "s_add_u32 s76, s76, 16384\n"
    "s_add_u32 m0, s59, 69632\n"
    "v_mfma_f32_16x16x32_bf16 v[52:55], v[80:83], v[20:23], v[52:55]\n"
    "s_waitcnt lgkmcnt(2)\n"
    "buffer_load_dwordx4 v1, s[36:39], s76 offen sc1 lds\n"
    "v_mfma_f32_16x16x32_bf16 v[64:67], v[84:87], v[20:23], v[64:67]\n"
    "ds_read_b128 v[20:23], v0 offset:25600\n"
    "v_mfma_f32_16x16x32_bf16 v[48:51], v[88:91], v[24:27], v[48:51]\n"
    "v_mfma_f32_16x16x32_bf16 v[52:55], v[92:95], v[24:27], v[52:55]\n"
    "s_waitcnt lgkmcnt(2)\n"
    "v_mfma_f32_16x16x32_bf16 v[64:67], v[96:99], v[24:27], v[64:67]\n"
    "ds_read_b128 v[24:27], v0 offset:26624\n"
    "v_mfma_f32_16x16x32_bf16 v[48:51], v[100:103], v[28:31], v[48:51]\n"
    "v_mfma_f32_16x16x32_bf16 v[52:55], v[104:107], v[28:31], v[52:55]\n"
    "s_waitcnt lgkmcnt(2)\n"
    "v_mfma_f32_16x16x32_bf16 v[64:67], v[108:111], v[28:31], v[64:67]\n"
    "ds_read_b128 v[28:31], v0 offset:27648\n"
    "s_branch .LgDOWN_join0_%=\n"
    ".LgDOWN_dma0_54_%=:\n"
    "v_mfma_f32_16x16x32_bf16 v[48:51], a[216:219], v[24:27], v[48:51]\n"
    "s_add_u32 m0, s59, 36864\n"
    "v_mfma_f32_16x16x32_bf16 v[52:55], a[220:223], v[24:27], v[52:55]\n"
    "s_waitcnt lgkmcnt(2)\n"
    "s_waitcnt vmcnt(1)\n"
    "buffer_atomic_smax v12, v10, s[36:39], s70 offen sc1\n"
    "v_mfma_f32_16x16x32_bf16 v[64:67], a[224:227], v[24:27], v[64:67]\n"
    "ds_read_b128 v[24:27], v0 offset:22528\n"
    "buffer_load_dwordx4 v1, s[36:39], s76 offen sc1 lds\n"
    "v_mfma_f32_16x16x32_bf16 v[48:51], a[228:231], v[28:31], v[48:51]\n"
    "s_add_u32 s76, s76, 16384\n"
    "s_add_u32 m0, s59, 40960\n"
    "v_mfma_f32_16x16x32_bf16 v[52:55], a[232:235], v[28:31], v[52:55]\n"
    "s_waitcnt lgkmcnt(2)\n"
    "buffer_load_dwordx4 v1, s[36:39], s76 offen sc1 lds\n"
    "v_mfma_f32_16x16x32_bf16 v[64:67], a[236:239], v[28:31], v[64:67]\n"
    "ds_read_b128 v[28:31], v0 offset:23552\n"
    "v_mfma_f32_16x16x32_bf16 v[48:51], a[240:243], v[16:19], v[48:51]\n"
    "s_add_u32 s76, s76, 16384\n"
    "s_add_u32 m0, s59, 45056\n"
    "v_mfma_f32_16x16x32_bf16 v[52:55], a[244:247], v[16:19], v[52:55]\n"
    "s_waitcnt lgkmcnt(2)\n"
    "buffer_load_dwordx4 v1, s[36:39], s76 offen sc1 lds\n"
    "v_mfma_f32_16x16x32_bf16 v[64:67], a[248:251], v[16:19], v[64:67]\n"
    "ds_read_b128 v[16:19], v0 offset:24576\n"
    "v_mfma_f32_16x16x32_bf16 v[48:51], a[252:255], v[20:23], v[48:51]\n"
    "v_mfma_f32_16x16x32_bf16 v[52:55], v[80:83], v[20:23], v[52:55]\n"
    "s_waitcnt lgkmcnt(2)\n"
    "s_add_u32 s76, s76, 16384\n"
    "s_add_u32 m0, s59, 49152\n"
    "v_mfma_f32_16x16x32_bf16 v[64:67], v[84:87], v[20:23], v[64:67]\n"
    "ds_read_b128 v[20:23], v0 offset:25600\n"
    "buffer_load_dwordx4 v1, s[36:39], s76 offen sc1 lds\n"
    "v_mfma_f32_16x16x32_bf16 v[48:51], v[88:91], v[24:27], v[48:51]\n"
    "v_mfma_f32_16x16x32_bf16 v[52:55], v[92:95], v[24:27], v[52:55]\n"
    "s_waitcnt lgkmcnt(2)\n"
    "v_mfma_f32_16x16x32_bf16 v[64:67], v[96:99], v[24:27], v[64:67]\n"
    "ds_read_b128 v[24:27], v0 offset:26624\n"
    "s_add_u32 s76, s76, 16384\n"
    "v_mfma_f32_16x16x32_bf16 v[48:51], v[100:103], v[28:31], v[48:51]\n"
    "s_add_u32 m0, s59, 53248\n"
    "v_mfma_f32_16x16x32_bf16 v[52:55], v[104:107], v[28:31], v[52:55]\n"
    "s_waitcnt lgkmcnt(2)\n"
    "buffer_load_dwordx4 v1, s[36:39], s76 offen sc1 lds\n"
    "v_mfma_f32_16x16x32_bf16 v[64:67], v[108:111], v[28:31], v[64:67]\n"
    "ds_read_b128 v[28:31], v0 offset:27648\n"
    "v_mfma_f32_16x16x32_bf16 v[48:51], v[112:115], v[16:19], v[48:51]\n"
    "s_add_u32 s76, s76, 16384\n"
    "s_add_u32 m0, s59, 57344\n"
    "v_mfma_f32_16x16x32_bf16 v[52:55], v[116:119], v[16:19], v[52:55]\n"
    "s_waitcnt lgkmcnt(2)\n"
    "buffer_load_dwordx4 v1, s[36:39], s76 offen sc1 lds\n"
    "v_mfma_f32_16x16x32_bf16 v[64:67], v[120:123], v[16:19], v[64:67]\n"
    "ds_read_b128 v[16:19], v0 offset:28672\n"
    "v_mfma_f32_16x16x32_bf16 v[48:51], v[124:127], v[20:23], v[48:51]\n"
    "v_mfma_f32_16x16x32_bf16 v[52:55], v[128:131], v[20:23], v[52:55]\n"
    "s_waitcnt lgkmcnt(2)\n"
    "s_add_u32 s76, s76, 16384\n"
    "s_add_u32 m0, s59, 61440\n"
    "v_mfma_f32_16x16x32_bf16 v[64:67], v[132:135], v[20:23], v[64:67]\n"
    "ds_read_b128 v[20:23], v0 offset:29696\n"
    "buffer_load_dwordx4 v1, s[36:39], s76 offen sc1 lds\n"
    "v_mfma_f32_16x16x32_bf16 v[48:51], v[136:139], v[24:27], v[48:51]\n"
    "v_mfma_f32_16x16x32_bf16 v[52:55], v[140:143], v[24:27], v[52:55]\n"
    "s_waitcnt lgkmcnt(2)\n"
    "v_mfma_f32_16x16x32_bf16 v[64:67], v[144:147], v[24:27], v[64:67]\n"
    "ds_read_b128 v[24:27], v0 offset:30720\n"
    "s_add_u32 s76, s76, 16384\n"
    "v_mfma_f32_16x16x32_bf16 v[48:51], v[148:151], v[28:31], v[48:51]\n"
    "s_add_u32 m0, s59, 65536\n"
    "v_mfma_f32_16x16x32_bf16 v[52:55], v[152:155], v[28:31], v[52:55]\n"
    "s_waitcnt lgkmcnt(2)\n"
    "buffer_load_dwordx4 v1, s[36:39], s76 offen sc1 lds\n"
    "v_mfma_f32_16x16x32_bf16 v[64:67], v[156:159], v[28:31], v[64:67]\n"
    "ds_read_b128 v[28:31], v0 offset:31744\n"
    "v_mfma_f32_16x16x32_bf16 v[48:51], v[160:163], v[16:19], v[48:51]\n"
    "s_add_u32 s76, s76, 16384\n"
    "s_add_u32 m0, s59, 69632\n"
    "v_mfma_f32_16x16x32_bf16 v[52:55], v[164:167], v[16:19], v[52:55]\n"
    "s_waitcnt lgkmcnt(2)\n"
    "buffer_load_dwordx4 v1, s[36:39], s76 offen sc1 lds\n"
    "v_mfma_f32_16x16x32_bf16 v[64:67], v[168:171], v[16:19], v[64:67]\n"
    "ds_read_b128 v[16:19], v0 offset:32768\n"
    "v_mfma_f32_16x16x32_bf16 v[48:51], v[172:175], v[20:23], v[48:51]\n"
    "v_mfma_f32_16x16x32_bf16 v[52:55], v[176:179], v[20:23], v[52:55]\n"
    "s_waitcnt lgkmcnt(2)\n"
    "v_mfma_f32_16x16x32_bf16 v[64:67], v[180:183], v[20:23], v[64:67]\n"
    "ds_read_b128 v[20:23], v0 offset:33792\n"
    "v_mfma_f32_16x16x32_bf16 v[48:51], v[184:187], v[24:27], v[48:51]\n"
    "v_mfma_f32_16x16x32_bf16 v[52:55], v[188:191], v[24:27], v[52:55]\n"
    "s_waitcnt lgkmcnt(2)\n"
    "v_mfma_f32_16x16x32_bf16 v[64:67], v[192:195], v[24:27], v[64:67]\n"
    "ds_read_b128 v[24:27], v0 offset:34816\n"
    "v_mfma_f32_16x16x32_bf16 v[48:51], v[196:199], v[28:31], v[48:51]\n"
    "v_mfma_f32_16x16x32_bf16 v[52:55], v[200:203], v[28:31], v[52:55]\n"
    "s_waitcnt lgkmcnt(2)\n"
    "v_mfma_f32_16x16x32_bf16 v[64:67], v[204:207], v[28:31], v[64:67]\n"
    "ds_read_b128 v[28:31], v0 offset:35840\n"
    "v_mfma_f32_16x16x32_bf16 v[48:51], v[208:211], v[16:19], v[48:51]\n"
    "v_mfma_f32_16x16x32_bf16 v[52:55], v[212:215], v[16:19], v[52:55]\n"
    "s_waitcnt lgkmcnt(2)\n"
    "v_mfma_f32_16x16x32_bf16 v[64:67], v[216:219], v[16:19], v[64:67]\n"
    "v_mfma_f32_16x16x32_bf16 v[48:51], v[220:223], v[20:23], v[48:51]\n"
    "v_mfma_f32_16x16x32_bf16 v[52:55], v[224:227], v[20:23], v[52:55]\n"
    "s_waitcnt lgkmcnt(1)\n"
    "v_mfma_f32_16x16x32_bf16 v[64:67], v[228:231], v[20:23], v[64:67]\n"
    "v_mfma_f32_16x16x32_bf16 v[48:51], v[232:235], v[24:27], v[48:51]\n"
    "v_mfma_f32_16x16x32_bf16 v[52:55], v[236:239], v[24:27], v[52:55]\n"
    "s_waitcnt lgkmcnt(0)\n"
    "v_mfma_f32_16x16x32_bf16 v[64:67], v[240:243], v[24:27], v[64:67]\n"
    "v_mfma_f32_16x16x32_bf16 v[48:51], v[244:247], v[28:31], v[48:51]\n"
    "v_mfma_f32_16x16x32_bf16 v[52:55], v[248:251], v[28:31], v[52:55]\n"
    "v_mfma_f32_16x16x32_bf16 v[64:67], v[252:255], v[28:31], v[64:67]\n"
    "v_cvt_pk_bf16_f32 v48, v48, v49\n"
    "v_cvt_pk_bf16_f32 v49, v50, v51\n"
    "s_nop 1\n"
    "v_cvt_pk_bf16_f32 v50, v52, v53\n"
    "v_cvt_pk_bf16_f32 v51, v54, v55\n"
    "buffer_store_dwordx4 v[48:51], v15, s[36:39], s73 offen sc1\n"
    "s_nop 0\n"
    "v_cvt_pk_bf16_f32 v64, v64, v65\n"
    "v_cvt_pk_bf16_f32 v65, v66, v67\n"
    "s_nop 0\n"
    "v_permlane16_swap_b32 v64, v66\n"
    "v_permlane16_swap_b32 v65, v67\n"
    "buffer_store_dwordx4 v[64:67], v4, s[36:39], s73 offen sc1\n"
    "v_mov_b32 v12, s68\n"
    "s_mov_b32 s73, 0x80000000\n"
    "s_mov_b32 s68, s71\n"
    "s_mov_b32 s69, 0\n"
    "s_mov_b32 s70, 0x80000000\n"
    "v_mov_b32 v7, v12\n"
    "s_waitcnt vmcnt(12)\n"
    "s_add_u32 s71, s68, 1\n"
    "v_add_u32 v13, v11, v3\n"
    "v_cmp_gt_i32 vcc, s71, v13\n"
    "s_nop 0\n"
    "s_or_b64 exec, vcc, s[62:63]\n"
    "buffer_atomic_smax v11, v2, s[36:39], 0 offen sc0 sc1\n"
    "s_mov_b64 exec, -1\n"
    "s_waitcnt vmcnt(3)\n"
    "s_branch .LgDOWN_bar1_%=\n"
    ".LgDOWN_dma0_75_%=:\n"
    "v_mfma_f32_16x16x32_bf16 v[48:51], v[124:127], v[20:23], v[48:51]\n"
    "s_add_u32 m0, s59, 36864\n"
    "v_mfma_f32_16x16x32_bf16 v[52:55], v[128:131], v[20:23], v[52:55]\n"
    "s_waitcnt lgkmcnt(2)\n"
    "buffer_load_dwordx4 v1, s[36:39], s76 offen sc1 lds\n"
    "v_mfma_f32_16x16x32_bf16 v[64:67], v[132:135], v[20:23], v[64:67]\n"
    "ds_read_b128 v[20:23], v0 offset:29696\n"
    "s_add_u32 s76, s76, 16384\n"
    "v_mfma_f32_16x16x32_bf16 v[48:51], v[136:139], v[24:27], v[48:51]\n"
    "s_add_u32 m0, s59, 40960\n"
    "v_mfma_f32_16x16x32_bf16 v[52:55], v[140:143], v[24:27], v[52:55]\n"
    "s_waitcnt lgkmcnt(2)\n"
    "buffer_load_dwordx4 v1, s[36:39], s76 offen sc1 lds\n"
    "v_mfma_f32_16x16x32_bf16 v[64:67], v[144:147], v[24:27], v[64:67]\n"
    "ds_read_b128 v[24:27], v0 offset:30720\n"
    "v_mfma_f32_16x16x32_bf16 v[48:51], v[148:151], v[28:31], v[48:51]\n"
    "s_add_u32 s76, s76, 16384\n"
    "s_add_u32 m0, s59, 45056\n"
    "v_mfma_f32_16x16x32_bf16 v[52:55], v[152:155], v[28:31], v[52:55]\n"
    "s_waitcnt lgkmcnt(2)\n"
    "buffer_load_dwordx4 v1, s[36:39], s76 offen sc1 lds\n"
    "v_mfma_f32_16x16x32_bf16 v[64:67], v[156:159], v[28:31], v[64:67]\n"
    "ds_read_b128 v[28:31], v0 offset:31744\n"
    "v_mfma_f32_16x16x32_bf16 v[48:51], v[160:163], v[16:19], v[48:51]\n"
    "v_mfma_f32_16x16x32_bf16 v[52:55], v[164:167], v[16:19], v[52:55]\n"
    "s_waitcnt lgkmcnt(2)\n"
    "s_add_u32 s76, s76, 16384\n"
    "s_add_u32 m0, s59, 49152\n"
    "v_mfma_f32_16x16x32_bf16 v[64:67], v[168:171], v[16:19], v[64:67]\n"
    "ds_read_b128 v[16:19], v0 offset:32768\n"
    "buffer_load_dwordx4 v1, s[36:39], s76 offen sc1 lds\n"
    "v_mfma_f32_16x16x32_bf16 v[48:51], v[172:175], v[20:23], v[48:51]\n"
    "v_mfma_f32_16x16x32_bf16 v[52:55], v[176:179], v[20:23], v[52:55]\n"
    "s_waitcnt lgkmcnt(2)\n"
    "v_mfma_f32_16x16x32_bf16 v[64:67], v[180:183], v[20:23], v[64:67]\n"
    "ds_read_b128 v[20:23], v0 offset:33792\n"
    "s_add_u32 s76, s76, 16384\n"
    "v_mfma_f32_16x16x32_bf16 v[48:51], v[184:187], v[24:27], v[48:51]\n"
    "s_add_u32 m0, s59, 53248\n"
    "v_mfma_f32_16x16x32_bf16 v[52:55], v[188:191], v[24:27], v[52:55]\n"
    "s_waitcnt lgkmcnt(2)\n"
    "buffer_load_dwordx4 v1, s[36:39], s76 offen sc1 lds\n"
    "v_mfma_f32_16x16x32_bf16 v[64:67], v[192:195], v[24:27], v[64:67]\n"
    "ds_read_b128 v[24:27], v0 offset:34816\n"
    "v_mfma_f32_16x16x32_bf16 v[48:51], v[196:199], v[28:31], v[48:51]\n"
    "s_add_u32 s76, s76, 16384\n"
    "s_add_u32 m0, s59, 57344\n"
    "v_mfma_f32_16x16x32_bf16 v[52:55], v[200:203], v[28:31], v[52:55]\n"
    "s_waitcnt lgkmcnt(2)\n"
    "buffer_load_dwordx4 v1, s[36:39], s76 offen sc1 lds\n"
    "v_mfma_f32_16x16x32_bf16 v[64:67], v[204:207], v[28:31], v[64:67]\n"
    "ds_read_b128 v[28:31], v0 offset:35840\n"
    "v_mfma_f32_16x16x32_bf16 v[48:51], v[208:211], v[16:19], v[48:51]\n"
    "v_mfma_f32_16x16x32_bf16 v[52:55], v[212:215], v[16:19], v[52:55]\n"
    "s_waitcnt lgkmcnt(2)\n"
    "s_add_u32 s76, s76, 16384\n"
    "s_add_u32 m0, s59, 61440\n"
    "v_mfma_f32_16x16x32_bf16 v[64:67], v[216:219], v[16:19], v[64:67]\n"
    "buffer_load_dwordx4 v1, s[36:39], s76 offen sc1 lds\n"
    "v_mfma_f32_16x16x32_bf16 v[48:51], v[220:223], v[20:23], v[48:51]\n"
    "v_mfma_f32_16x16x32_bf16 v[52:55], v[224:227], v[20:23], v[52:55]\n"
    "s_waitcnt lgkmcnt(1)\n"
    "v_mfma_f32_16x16x32_bf16 v[64:67], v[228:231], v[20:23], v[64:67]\n"
    "s_add_u32 s76, s76, 16384\n"
    "v_mfma_f32_16x16x32_bf16 v[48:51], v[232:235], v[24:27], v[48:51]\n"
    "s_add_u32 m0, s59, 65536\n"
    "v_mfma_f32_16x16x32_bf16 v[52:55], v[236:239], v[24:27], v[52:55]\n"
    "s_waitcnt lgkmcnt(0)\n"
    "buffer_load_dwordx4 v1, s[36:39], s76 offen sc1 lds\n"
    "v_mfma_f32_16x16x32_bf16 v[64:67], v[240:243], v[24:27], v[64:67]\n"
    "v_mfma_f32_16x16x32_bf16 v[48:51], v[244:247], v[28:31], v[48:51]\n"
    "s_add_u32 s76, s76, 16384\n"
    "s_add_u32 m0, s59, 69632\n"
    "v_mfma_f32_16x16x32_bf16 v[52:55], v[248:251], v[28:31], v[52:55]\n"
    "buffer_load_dwordx4 v1, s[36:39], s76 offen sc1 lds\n"
    "v_mfma_f32_16x16x32_bf16 v[64:67], v[252:255], v[28:31], v[64:67]\n"
    "v_cvt_pk_bf16_f32 v48, v48, v49\n"
    "v_cvt_pk_bf16_f32 v49, v50, v51\n"
    "s_nop 1\n"
    "v_cvt_pk_bf16_f32 v50, v52, v53\n"
    "v_cvt_pk_bf16_f32 v51, v54, v55\n"
    "buffer_store_dwordx4 v[48:51], v15, s[36:39], s73 offen sc1\n"
    "s_nop 0\n"
    "v_cvt_pk_bf16_f32 v64, v64, v65\n"
    "v_cvt_pk_bf16_f32 v65, v66, v67\n"
    "s_nop 0\n"
    "v_permlane16_swap_b32 v64, v66\n"
    "v_permlane16_swap_b32 v65, v67\n"
    "buffer_store_dwordx4 v[64:67], v4, s[36:39], s73 offen sc1\n"
    "v_mov_b32 v12, s68\n"
    "s_mov_b32 s73, 0x80000000\n"
    "s_mov_b32 s68, s71\n"
    "s_mov_b32 s69, 0\n"
    "s_mov_b32 s70, 0x80000000\n"
    "v_mov_b32 v7, v12\n"
    "s_waitcnt vmcnt(12)\n"
    "s_add_u32 s71, s68, 1\n"
    "v_add_u32 v13, v11, v3\n"
    "v_cmp_gt_i32 vcc, s71, v13\n"
    "s_nop 0\n"
    "s_or_b64 exec, vcc, s[62:63]\n"
    "buffer_atomic_smax v11, v2, s[36:39], 0 offen sc0 sc1\n"
    "s_mov_b64 exec, -1\n"
    "s_waitcnt vmcnt(3)\n"
    "s_branch .LgDOWN_bar1_%=\n"
    ".LgDOWN_dma0_96_%=:\n"
    "v_mfma_f32_16x16x32_bf16 v[48:51], v[208:211], v[16:19], v[48:51]\n"
    "s_add_u32 m0, s59, 36864\n"
    "v_mfma_f32_16x16x32_bf16 v[52:55], v[212:215], v[16:19], v[52:55]\n"
    "s_waitcnt lgkmcnt(2)\n"
    "buffer_load_dwordx4 v1, s[36:39], s76 offen sc1 lds\n"
    "v_mfma_f32_16x16x32_bf16 v[64:67], v[216:219], v[16:19], v[64:67]\n"
    "s_add_u32 s76, s76, 16384\n"
    "s_add_u32 m0, s59, 40960\n"
    "v_mfma_f32_16x16x32_bf16 v[48:51], v[220:223], v[20:23], v[48:51]\n"
    "buffer_load_dwordx4 v1, s[36:39], s76 offen sc1 lds\n"
    "v_mfma_f32_16x16x32_bf16 v[52:55], v[224:227], v[20:23], v[52:55]\n"
    "s_waitcnt lgkmcnt(1)\n"
    "v_mfma_f32_16x16x32_bf16 v[64:67], v[228:231], v[20:23], v[64:67]\n"
    "v_mfma_f32_16x16x32_bf16 v[48:51], v[232:235], v[24:27], v[48:51]\n"
    "s_add_u32 s76, s76, 16384\n"
    "s_add_u32 m0, s59, 45056\n"
    "v_mfma_f32_16x16x32_bf16 v[52:55], v[236:239], v[24:27], v[52:55]\n"
    "s_waitcnt lgkmcnt(0)\n"
    "buffer_load_dwordx4 v1, s[36:39], s76 offen sc1 lds\n"
    "v_mfma_f32_16x16x32_bf16 v[64:67], v[240:243], v[24:27], v[64:67]\n"
    "v_mfma_f32_16x16x32_bf16 v[48:51], v[244:247], v[28:31], v[48:51]\n"
    "v_mfma_f32_16x16x32_bf16 v[52:55], v[248:251], v[28:31], v[52:55]\n"
    "s_add_u32 s76, s76, 16384\n"
    "s_add_u32 m0, s59, 49152\n"
    "v_mfma_f32_16x16x32_bf16 v[64:67], v[252:255], v[28:31], v[64:67]\n"
    "buffer_load_dwordx4 v1, s[36:39], s76 offen sc1 lds\n"
    "v_cvt_pk_bf16_f32 v48, v48, v49\n"
    "v_cvt_pk_bf16_f32 v49, v50, v51\n"
    "s_nop 0\n"
    "v_cvt_pk_bf16_f32 v50, v52, v53\n"
    "v_cvt_pk_bf16_f32 v51, v54, v55\n"
    "buffer_store_dwordx4 v[48:51], v15, s[36:39], s73 offen sc1\n"
    "s_nop 0\n"
    "v_cvt_pk_bf16_f32 v64, v64, v65\n"
    "v_cvt_pk_bf16_f32 v65, v66, v67\n"
    "s_nop 0\n"
    "v_permlane16_swap_b32 v64, v66\n"
    "v_permlane16_swap_b32 v65, v67\n"
    "buffer_store_dwordx4 v[64:67], v4, s[36:39], s73 offen sc1\n"
    "v_mov_b32 v12, s68\n"
    "s_mov_b32 s73, 0x80000000\n"
    "s_mov_b32 s68, s71\n"
    "s_add_u32 m0, s59, 53248\n"
    "s_add_u32 s76, s76, 16384\n"
    "buffer_load_dwordx4 v1, s[36:39], s76 offen sc1 lds\n"
    "s_add_u32 m0, s59, 57344\n"
    "s_add_u32 s76, s76, 16384\n"
    "buffer_load_dwordx4 v1, s[36:39], s76 offen sc1 lds\n"
    "s_add_u32 m0, s59, 61440\n"
    "s_add_u32 s76, s76, 16384\n"
    "buffer_load_dwordx4 v1, s[36:39], s76 offen sc1 lds\n"
    "s_add_u32 m0, s59, 65536\n"
    "s_add_u32 s76, s76, 16384\n"
    "buffer_load_dwordx4 v1, s[36:39], s76 offen sc1 lds\n"
    "s_add_u32 m0, s59, 69632\n"
    "s_add_u32 s76, s76, 16384\n"
    "buffer_load_dwordx4 v1, s[36:39], s76 offen sc1 lds\n"
    "s_waitcnt vmcnt(5)\n"
    "buffer_atomic_smax v12, v14, s[36:39], 0 offen sc1\n"
    "s_mov_b32 s69, 0x80000000\n"
    "s_mov_b32 s70, 0x80000000\n"
    "v_mov_b32 v7, v12\n"
    "s_waitcnt vmcnt(12)\n"
    "s_add_u32 s71, s68, 1\n"
    "v_add_u32 v13, v11, v3\n"
    "v_cmp_gt_i32 vcc, s71, v13\n"
    "s_nop 0\n"
    "s_or_b64 exec, vcc, s[62:63]\n"
    "buffer_atomic_smax v11, v2, s[36:39], 0 offen sc0 sc1\n"
    "s_mov_b64 exec, -1\n"
    "s_waitcnt vmcnt(2)\n"
    "s_branch .LgDOWN_bar1_%=\n"
    ".LgDOWN_idle0_%=:\n"
    "s_or_b64 exec, vcc, s[62:63]\n"
    "buffer_atomic_smax v5, v2, s[36:39], 0 offen sc0 sc1\n"
    "s_mov_b64 exec, -1\n"
    "s_waitcnt vmcnt(1)\n"
    "buffer_atomic_smax v12, v14, s[36:39], 0 offen sc1\n"
    "s_branch .LgDOWN_block0_1_%=\n"
    ".LgDOWN_block0_%=:\n"
    "v_add_u32 v13, v11, v3\n"
    "v_cmp_gt_i32 vcc, s71, v13\n"
    "s_nop 0\n"
    "s_or_b64 exec, vcc, s[62:63]\n"
    "buffer_atomic_smax v11, v2, s[36:39], 0 offen sc0 sc1\n"
    "s_mov_b64 exec, -1\n"
    "s_sleep 3\n"
    "s_waitcnt vmcnt(1)\n"
    ".LgDOWN_block0_0_%=:\n"
    "v_add_u32 v13, v5, v3\n"
    "v_cmp_gt_i32 vcc, s71, v13\n"
    "s_cbranch_vccz .LgDOWN_go0_%=\n"
    "v_add_u32 v13, v5, v3\n"
    "v_cmp_gt_i32 vcc, s71, v13\n"
    "s_nop 0\n"
    "s_or_b64 exec, vcc, s[62:63]\n"
    "buffer_atomic_smax v5, v2, s[36:39], 0 offen sc0 sc1\n"
    "s_mov_b64 exec, -1\n"
    "s_sleep 3\n"
    "s_waitcnt vmcnt(1)\n"
    ".LgDOWN_block0_1_%=:\n"
    "v_add_u32 v13, v11, v3\n"
    "v_cmp_gt_i32 vcc, s71, v13\n"
    "s_cbranch_vccz .LgDOWN_go0_%=\n"
    "s_sub_u32 s72, s72, 1\n"
    "s_cbranch_scc0 .LgDOWN_block0_%=\n"
    "s_mov_b32 s72, 0\n"
    ".LgDOWN_go0_%=:\n"
    "s_lshr_b32 s76, s71, 1\n"
    "s_and_b32 s76, s76, s54\n"
    "s_mul_i32 s76, s76, s53\n"
    "s_add_u32 s76, s76, s52\n"
    "s_add_u32 s76, s76, 1024\n"
    "s_add_u32 m0, s59, 36864\n"
    "s_nop 0\n"
    "buffer_load_dwordx4 v1, s[36:39], s76 offen sc1 lds\n"
    "s_add_u32 m0, s59, 40960\n"
    "s_add_u32 s76, s76, 16384\n"
    "buffer_load_dwordx4 v1, s[36:39], s76 offen sc1 lds\n"
    "s_add_u32 m0, s59, 45056\n"
    "s_add_u32 s76, s76, 16384\n"
    "buffer_load_dwordx4 v1, s[36:39], s76 offen sc1 lds\n"
    "s_add_u32 m0, s59, 49152\n"
    "s_add_u32 s76, s76, 16384\n"
    "buffer_load_dwordx4 v1, s[36:39], s76 offen sc1 lds\n"
    "s_add_u32 m0, s59, 53248\n"
    "s_add_u32 s76, s76, 16384\n"
    "buffer_load_dwordx4 v1, s[36:39], s76 offen sc1 lds\n"
    "s_add_u32 m0, s59, 57344\n"
    "s_add_u32 s76, s76, 16384\n"
    "buffer_load_dwordx4 v1, s[36:39], s76 offen sc1 lds\n"
    "s_add_u32 m0, s59, 61440\n"
    "s_add_u32 s76, s76, 16384\n"
    "buffer_load_dwordx4 v1, s[36:39], s76 offen sc1 lds\n"
    "s_add_u32 m0, s59, 65536\n"
    "s_add_u32 s76, s76, 16384\n"
    "buffer_load_dwordx4 v1, s[36:39], s76 offen sc1 lds\n"
    "s_add_u32 m0, s59, 69632\n"
    "s_add_u32 s76, s76, 16384\n"
    "buffer_load_dwordx4 v1, s[36:39], s76 offen sc1 lds\n"
    "s_mov_b32 s69, 0x80000000\n"
    "s_mov_b32 s70, 0x80000000\n"
    "v_mov_b32 v7, v12\n"
    "s_waitcnt vmcnt(9)\n"
    "s_add_u32 s71, s68, 1\n"
    "v_add_u32 v13, v11, v3\n"
    "v_cmp_gt_i32 vcc, s71, v13\n"
    "s_nop 0\n"
    "s_or_b64 exec, vcc, s[62:63]\n"
    "buffer_atomic_smax v11, v2, s[36:39], 0 offen sc0 sc1\n"
    "s_mov_b64 exec, -1\n"
    "s_waitcnt vmcnt(1)\n"
    "s_branch .LgDOWN_bar1_%=\n"
    ".LgDOWN_bar0_%=:\n"
    "s_barrier\n"
    "ds_read_b128 v[16:19], v0 offset:0\n"
    "ds_read_b128 v[20:23], v0 offset:1024\n"
    "ds_read_b128 v[24:27], v0 offset:2048\n"
    "ds_read_b128 v[28:31], v0 offset:3072\n"
    "v_add_u32 v13, v5, v3\n"
    "v_cmp_gt_i32 vcc, s71, v13\n"
    "s_nop 0\n"
    "s_or_b64 exec, vcc, s[62:63]\n"
    "buffer_atomic_smax v5, v2, s[36:39], 0 offen sc0 sc1\n"
    "s_mov_b64 exec, -1\n"
    "s_lshr_b32 s76, s71, 1\n"
    "s_and_b32 s76, s76, s54\n"
    "s_mul_i32 s76, s76, s53\n"
    "s_add_u32 s76, s76, s52\n"
    "s_add_u32 s76, s76, 1024\n"
    "s_waitcnt lgkmcnt(3)\n"
    "s_branch .LgDOWN_head0_%=\n"
    ".LgDOWN_poll1_%=:\n"
    "v_mfma_f32_16x16x32_bf16 v[56:59], a[48:51], v[16:19], v[56:59]\n"
    "s_or_b64 exec, vcc, s[62:63]\n"
    "buffer_atomic_smax v11, v2, s[36:39], 0 offen sc0 sc1\n"
    "s_mov_b64 exec, -1\n"
    "v_mfma_f32_16x16x32_bf16 v[60:63], a[52:55], v[16:19], v[60:63]\n"
    "s_waitcnt lgkmcnt(2)\n"
    "v_mfma_f32_16x16x32_bf16 v[68:71], a[56:59], v[16:19], v[68:71]\n"
    "ds_read_b128 v[16:19], v9 offset:8192\n"
    "v_mfma_f32_16x16x32_bf16 v[56:59], a[60:63], v[20:23], v[56:59]\n"
    "buffer_store_dwordx4 v[64:67], v4, s[36:39], s73 offen sc1\n"
    "v_mfma_f32_16x16x32_bf16 v[60:63], a[64:67], v[20:23], v[60:63]\n"
    "s_waitcnt lgkmcnt(2)\n"
    "s_lshr_b32 s73, s68, 1\n"
    "v_mfma_f32_16x16x32_bf16 v[68:71], a[68:71], v[20:23], v[68:71]\n"
    "ds_read_b128 v[20:23], v9 offset:9216\n"
    "s_and_b32 s73, s73, s57\n"
    "v_mfma_f32_16x16x32_bf16 v[56:59], a[72:75], v[24:27], v[56:59]\n"
    "s_mul_i32 s73, s73, s56\n"
    "v_mfma_f32_16x16x32_bf16 v[60:63], a[76:79], v[24:27], v[60:63]\n"
    "s_waitcnt lgkmcnt(2)\n"
    "s_add_u32 s73, s73, s55\n"
    "v_mfma_f32_16x16x32_bf16 v[68:71], a[80:83], v[24:27], v[68:71]\n"
    "ds_read_b128 v[24:27], v9 offset:10240\n"
    "v_mfma_f32_16x16x32_bf16 v[56:59], a[84:87], v[28:31], v[56:59]\n"
    "v_mfma_f32_16x16x32_bf16 v[60:63], a[88:91], v[28:31], v[60:63]\n"
    "s_waitcnt lgkmcnt(2)\n"
    "v_mfma_f32_16x16x32_bf16 v[68:71], a[92:95], v[28:31], v[68:71]\n"
    "ds_read_b128 v[28:31], v9 offset:11264\n"
    "v_mfma_f32_16x16x32_bf16 v[56:59], a[96:99], v[16:19], v[56:59]\n"
    "v_mfma_f32_16x16x32_bf16 v[60:63], a[100:103], v[16:19], v[60:63]\n"
    "s_waitcnt lgkmcnt(2)\n"
    "v_mfma_f32_16x16x32_bf16 v[68:71], a[104:107], v[16:19], v[68:71]\n"
    "ds_read_b128 v[16:19], v9 offset:12288\n"
    "v_mfma_f32_16x16x32_bf16 v[56:59], a[108:111], v[20:23], v[56:59]\n"
    "v_mfma_f32_16x16x32_bf16 v[60:63], a[112:115], v[20:23], v[60:63]\n"
    "s_waitcnt lgkmcnt(2)\n"
    "s_waitcnt vmcnt(4)\n"
    "v_add_u32 v13, v5, v3\n"
    "v_mfma_f32_16x16x32_bf16 v[68:71], a[116:119], v[20:23], v[68:71]\n"
    "ds_read_b128 v[20:23], v9 offset:13312\n"
    "v_cmp_gt_i32 vcc, s71, v13\n"
    "v_mfma_f32_16x16x32_bf16 v[56:59], a[120:123], v[24:27], v[56:59]\n"
    "v_mfma_f32_16x16x32_bf16 v[60:63], a[124:127], v[24:27], v[60:63]\n"
    "s_waitcnt lgkmcnt(2)\n"
    "v_mfma_f32_16x16x32_bf16 v[68:71], a[128:131], v[24:27], v[68:71]\n"
    "ds_read_b128 v[24:27], v9 offset:14336\n"
    "s_cbranch_vccz .LgDOWN_dma1_33_%=\n"
    "v_mfma_f32_16x16x32_bf16 v[56:59], a[132:135], v[28:31], v[56:59]\n"
    "s_or_b64 exec, vcc, s[62:63]\n"
    "buffer_atomic_smax v5, v2, s[36:39], 0 offen sc0 sc1\n"
    "s_mov_b64 exec, -1\n"
    "v_mfma_f32_16x16x32_bf16 v[60:63], a[136:139], v[28:31], v[60:63]\n"
    "s_waitcnt lgkmcnt(2)\n"
    "v_mfma_f32_16x16x32_bf16 v[68:71], a[140:143], v[28:31], v[68:71]\n"
    "ds_read_b128 v[28:31], v9 offset:15360\n"
    "v_mfma_f32_16x16x32_bf16 v[56:59], a[144:147], v[16:19], v[56:59]\n"
    "v_mfma_f32_16x16x32_bf16 v[60:63], a[148:151], v[16:19], v[60:63]\n"
    "s_waitcnt lgkmcnt(2)\n"
    "v_mfma_f32_16x16x32_bf16 v[68:71], a[152:155], v[16:19], v[68:71]\n"
    "ds_read_b128 v[16:19], v9 offset:16384\n"
    "v_mfma_f32_16x16x32_bf16 v[56:59], a[156:159], v[20:23], v[56:59]\n"
    "v_mfma_f32_16x16x32_bf16 v[60:63], a[160:163], v[20:23], v[60:63]\n"
    "s_waitcnt lgkmcnt(2)\n"
    "v_mfma_f32_16x16x32_bf16 v[68:71], a[164:167], v[20:23], v[68:71]\n"
    "ds_read_b128 v[20:23], v9 offset:17408\n"
    "v_mfma_f32_16x16x32_bf16 v[56:59], a[168:171], v[24:27], v[56:59]\n"
    "v_mfma_f32_16x16x32_bf16 v[60:63], a[172:175], v[24:27], v[60:63]\n"
    "s_waitcnt lgkmcnt(2)\n"
    "v_mfma_f32_16x16x32_bf16 v[68:71], a[176:179], v[24:27], v[68:71]\n"
    "ds_read_b128 v[24:27], v9 offset:18432\n"
    "v_mfma_f32_16x16x32_bf16 v[56:59], a[180:183], v[28:31], v[56:59]\n"
    "v_mfma_f32_16x16x32_bf16 v[60:63], a[184:187], v[28:31], v[60:63]\n"
    "s_waitcnt lgkmcnt(2)\n"
    "v_mfma_f32_16x16x32_bf16 v[68:71], a[188:191], v[28:31], v[68:71]\n"
    "ds_read_b128 v[28:31], v9 offset:19456\n"
    "v_mfma_f32_16x16x32_bf16 v[56:59], a[192:195], v[16:19], v[56:59]\n"
    "v_mfma_f32_16x16x32_bf16 v[60:63], a[196:199], v[16:19], v[60:63]\n"
    "s_waitcnt lgkmcnt(2)\n"
    "s_waitcnt vmcnt(2)\n"
    "v_add_u32 v13, v11, v3\n"
    "v_mfma_f32_16x16x32_bf16 v[68:71], a[200:203], v[16:19], v[68:71]\n"
    "ds_read_b128 v[16:19], v9 offset:20480\n"
    "v_cmp_gt_i32 vcc, s71, v13\n"
    "v_mfma_f32_16x16x32_bf16 v[56:59], a[204:207], v[20:23], v[56:59]\n"
    "v_mfma_f32_16x16x32_bf16 v[60:63], a[208:211], v[20:23], v[60:63]\n"
    "s_waitcnt lgkmcnt(2)\n"
    "v_mfma_f32_16x16x32_bf16 v[68:71], a[212:215], v[20:23], v[68:71]\n"
    "ds_read_b128 v[20:23], v9 offset:21504\n"
    "s_cbranch_vccz .LgDOWN_dma1_54_%=\n"
    "v_mfma_f32_16x16x32_bf16 v[56:59], a[216:219], v[24:27], v[56:59]\n"
    "s_or_b64 exec, vcc, s[62:63]\n"
    "buffer_atomic_smax v11, v2, s[36:39], 0 offen sc0 sc1\n"
    "s_mov_b64 exec, -1\n"
    "v_mfma_f32_16x16x32_bf16 v[60:63], a[220:223], v[24:27], v[60:63]\n"
    "s_waitcnt lgkmcnt(2)\n"
    "s_waitcnt vmcnt(2)\n"
    "buffer_atomic_smax v12, v14, s[36:39], s70 offen sc1\n"
    "v_mfma_f32_16x16x32_bf16 v[68:71], a[224:227], v[24:27], v[68:71]\n"
    "ds_read_b128 v[24:27], v9 offset:22528\n"
    "v_mfma_f32_16x16x32_bf16 v[56:59], a[228:231], v[28:31], v[56:59]\n"
    "v_mfma_f32_16x16x32_bf16 v[60:63], a[232:235], v[28:31], v[60:63]\n"
    "s_waitcnt lgkmcnt(2)\n"
    "v_mfma_f32_16x16x32_bf16 v[68:71], a[236:239], v[28:31], v[68:71]\n"
    "ds_read_b128 v[28:31], v9 offset:23552\n"
    "v_mfma_f32_16x16x32_bf16 v[56:59], a[240:243], v[16:19], v[56:59]\n"
    "v_mfma_f32_16x16x32_bf16 v[60:63], a[244:247], v[16:19], v[60:63]\n"
    "s_waitcnt lgkmcnt(2)\n"
    "v_mfma_f32_16x16x32_bf16 v[68:71], a[248:251], v[16:19], v[68:71]\n"
    "ds_read_b128 v[16:19], v9 offset:24576\n"
    "v_mfma_f32_16x16x32_bf16 v[56:59], a[252:255], v[20:23], v[56:59]\n"
    "v_mfma_f32_16x16x32_bf16 v[60:63], v[80:83], v[20:23], v[60:63]\n"
    "s_waitcnt lgkmcnt(2)\n"
    "v_mfma_f32_16x16x32_bf16 v[68:71], v[84:87], v[20:23], v[68:71]\n"
    "ds_read_b128 v[20:23], v9 offset:25600\n"
    "v_mfma_f32_16x16x32_bf16 v[56:59], v[88:91], v[24:27], v[56:59]\n"
    "v_mfma_f32_16x16x32_bf16 v[60:63], v[92:95], v[24:27], v[60:63]\n"
    "s_waitcnt lgkmcnt(2)\n"
    "v_mfma_f32_16x16x32_bf16 v[68:71], v[96:99], v[24:27], v[68:71]\n"
    "ds_read_b128 v[24:27], v9 offset:26624\n"
    "v_mfma_f32_16x16x32_bf16 v[56:59], v[100:103], v[28:31], v[56:59]\n"
    "v_mfma_f32_16x16x32_bf16 v[60:63], v[104:107], v[28:31], v[60:63]\n"
    "s_waitcnt lgkmcnt(2)\n"
    "s_waitcnt vmcnt(2)\n"
    "v_add_u32 v13, v5, v3\n"
    "v_mfma_f32_16x16x32_bf16 v[68:71], v[108:111], v[28:31], v[68:71]\n"
    "ds_read_b128 v[28:31], v9 offset:27648\n"
    "v_cmp_gt_i32 vcc, s71, v13\n"
    "v_mfma_f32_16x16x32_bf16 v[56:59], v[112:115], v[16:19], v[56:59]\n"
    "v_mfma_f32_16x16x32_bf16 v[60:63], v[116:119], v[16:19], v[60:63]\n"
    "s_waitcnt lgkmcnt(2)\n"
    "v_mfma_f32_16x16x32_bf16 v[68:71], v[120:123], v[16:19], v[68:71]\n"
    "ds_read_b128 v[16:19], v9 offset:28672\n"
    "s_cbranch_vccz .LgDOWN_dma1_75_%=\n"
    "v_mfma_f32_16x16x32_bf16 v[56:59], v[124:127], v[20:23], v[56:59]\n"
    "s_or_b64 exec, vcc, s[62:63]\n"
    "buffer_atomic_smax v5, v2, s[36:39], 0 offen sc0 sc1\n"
    "s_mov_b64 exec, -1\n"
    "v_mfma_f32_16x16x32_bf16 v[60:63], v[128:131], v[20:23], v[60:63]\n"
    "s_waitcnt lgkmcnt(2)\n"
    "v_mfma_f32_16x16x32_bf16 v[68:71], v[132:135], v[20:23], v[68:71]\n"
    "ds_read_b128 v[20:23], v9 offset:29696\n"
    "v_mfma_f32_16x16x32_bf16 v[56:59], v[136:139], v[24:27], v[56:59]\n"
    "v_mfma_f32_16x16x32_bf16 v[60:63], v[140:143], v[24:27], v[60:63]\n"
    "s_waitcnt lgkmcnt(2)\n"
    "v_mfma_f32_16x16x32_bf16 v[68:71], v[144:147], v[24:27], v[68:71]\n"
    "ds_read_b128 v[24:27], v9 offset:30720\n"
    "v_mfma_f32_16x16x32_bf16 v[56:59], v[148:151], v[28:31], v[56:59]\n"
    "v_mfma_f32_16x16x32_bf16 v[60:63], v[152:155], v[28:31], v[60:63]\n"
    "s_waitcnt lgkmcnt(2)\n"
    "v_mfma_f32_16x16x32_bf16 v[68:71], v[156:159], v[28:31], v[68:71]\n"
    "ds_read_b128 v[28:31], v9 offset:31744\n"
    "v_mfma_f32_16x16x32_bf16 v[56:59], v[160:163], v[16:19], v[56:59]\n"
    "v_mfma_f32_16x16x32_bf16 v[60:63], v[164:167], v[16:19], v[60:63]\n"
    "s_waitcnt lgkmcnt(2)\n"
    "v_mfma_f32_16x16x32_bf16 v[68:71], v[168:171], v[16:19], v[68:71]\n"
    "ds_read_b128 v[16:19], v9 offset:32768\n"
    "v_mfma_f32_16x16x32_bf16 v[56:59], v[172:175], v[20:23], v[56:59]\n"
    "v_mfma_f32_16x16x32_bf16 v[60:63], v[176:179], v[20:23], v[60:63]\n"
    "s_waitcnt lgkmcnt(2)\n"
    "v_mfma_f32_16x16x32_bf16 v[68:71], v[180:183], v[20:23], v[68:71]\n"
    "ds_read_b128 v[20:23], v9 offset:33792\n"
    "v_mfma_f32_16x16x32_bf16 v[56:59], v[184:187], v[24:27], v[56:59]\n"
    "v_mfma_f32_16x16x32_bf16 v[60:63], v[188:191], v[24:27], v[60:63]\n"
    "s_waitcnt lgkmcnt(2)\n"
    "s_waitcnt vmcnt(2)\n"
    "v_add_u32 v13, v11, v3\n"
    "v_mfma_f32_16x16x32_bf16 v[68:71], v[192:195], v[24:27], v[68:71]\n"
    "ds_read_b128 v[24:27], v9 offset:34816\n"
    "v_cmp_gt_i32 vcc, s71, v13\n"
    "v_mfma_f32_16x16x32_bf16 v[56:59], v[196:199], v[28:31], v[56:59]\n"
    "v_mfma_f32_16x16x32_bf16 v[60:63], v[200:203], v[28:31], v[60:63]\n"
    "s_waitcnt lgkmcnt(2)\n"
    "v_mfma_f32_16x16x32_bf16 v[68:71], v[204:207], v[28:31], v[68:71]\n"
    "ds_read_b128 v[28:31], v9 offset:35840\n"
    "s_cbranch_vccz .LgDOWN_dma1_96_%=\n"
    "v_mfma_f32_16x16x32_bf16 v[56:59], v[208:211], v[16:19], v[56:59]\n"
    "s_or_b64 exec, vcc, s[62:63]\n"
    "buffer_atomic_smax v11, v2, s[36:39], 0 offen sc0 sc1\n"
    "s_mov_b64 exec, -1\n"
    "v_mfma_f32_16x16x32_bf16 v[60:63], v[212:215], v[16:19], v[60:63]\n"
    "s_waitcnt lgkmcnt(2)\n"
    "v_mfma_f32_16x16x32_bf16 v[68:71], v[216:219], v[16:19], v[68:71]\n"
    "v_mfma_f32_16x16x32_bf16 v[56:59], v[220:223], v[20:23], v[56:59]\n"
    "v_mfma_f32_16x16x32_bf16 v[60:63], v[224:227], v[20:23], v[60:63]\n"
    "s_waitcnt lgkmcnt(1)\n"
    "v_mfma_f32_16x16x32_bf16 v[68:71], v[228:231], v[20:23], v[68:71]\n"
    "v_mfma_f32_16x16x32_bf16 v[56:59], v[232:235], v[24:27], v[56:59]\n"
    "v_mfma_f32_16x16x32_bf16 v[60:63], v[236:239], v[24:27], v[60:63]\n"
    "s_waitcnt lgkmcnt(0)\n"
    "v_mfma_f32_16x16x32_bf16 v[68:71], v[240:243], v[24:27], v[68:71]\n"
    "v_mfma_f32_16x16x32_bf16 v[56:59], v[244:247], v[28:31], v[56:59]\n"
    "v_mfma_f32_16x16x32_bf16 v[60:63], v[248:251], v[28:31], v[60:63]\n"
    "v_mfma_f32_16x16x32_bf16 v[68:71], v[252:255], v[28:31], v[68:71]\n"
    "v_cvt_pk_bf16_f32 v56, v56, v57\n"
    "v_cvt_pk_bf16_f32 v57, v58, v59\n"
    "s_nop 1\n"
    "v_cvt_pk_bf16_f32 v58, v60, v61\n"
    "v_cvt_pk_bf16_f32 v59, v62, v63\n"
    "buffer_store_dwordx4 v[56:59], v15, s[36:39], s73 offen offset:1024 sc1\n"
    "s_nop 0\n"
    "v_cvt_pk_bf16_f32 v68, v68, v69\n"
    "v_cvt_pk_bf16_f32 v69, v70, v71\n"
    "s_nop 0\n"
    "v_permlane16_swap_b32 v68, v70\n"
    "v_permlane16_swap_b32 v69, v71\n"
    "buffer_store_dwordx4 v[68:71], v4, s[36:39], s73 offen offset:1024 sc1\n"
    "v_mov_b32 v12, s68\n"
    "s_mov_b32 s73, 0x80000000\n"
    "s_mov_b32 s68, s71\n"
    "s_cmp_eq_u32 s68, 128\n"
    "s_cbranch_scc1 .LgDOWN_done_%=\n"
    "s_waitcnt vmcnt(3)\n"
    "v_add_u32 v13, v5, v3\n"
    "v_cmp_gt_i32 vcc, s71, v13\n"
    "s_cbranch_vccnz .LgDOWN_idle1_%=\n"
    "s_add_u32 m0, s59, 0\n"
    "s_nop 0\n"
    "buffer_load_dwordx4 v1, s[36:39], s76 offen sc1 lds\n"
    "s_add_u32 m0, s59, 4096\n"
    "s_add_u32 s76, s76, 16384\n"
    "buffer_load_dwordx4 v1, s[36:39], s76 offen sc1 lds\n"
    "s_add_u32 m0, s59, 8192\n"
    "s_add_u32 s76, s76, 16384\n"
    "buffer_load_dwordx4 v1, s[36:39], s76 offen sc1 lds\n"
    "s_add_u32 m0, s59, 12288\n"
    "s_add_u32 s76, s76, 16384\n"
    "buffer_load_dwordx4 v1, s[36:39], s76 offen sc1 lds\n"
    "s_add_u32 m0, s59, 16384\n"
    "s_add_u32 s76, s76, 16384\n"
    "buffer_load_dwordx4 v1, s[36:39], s76 offen sc1 lds\n"
    "s_add_u32 m0, s59, 20480\n"
    "s_add_u32 s76, s76, 16384\n"
    "buffer_load_dwordx4 v1, s[36:39], s76 offen sc1 lds\n"
    "s_add_u32 m0, s59, 24576\n"
    "s_add_u32 s76, s76, 16384\n"
    "buffer_load_dwordx4 v1, s[36:39], s76 offen sc1 lds\n"
    "s_add_u32 m0, s59, 28672\n"
    "s_add_u32 s76, s76, 16384\n"
    "buffer_load_dwordx4 v1, s[36:39], s76 offen sc1 lds\n"
    "s_add_u32 m0, s59, 32768\n"
    "s_add_u32 s76, s76, 16384\n"
    "buffer_load_dwordx4 v1, s[36:39], s76 offen sc1 lds\n"
    "s_waitcnt vmcnt(9)\n"
    "buffer_atomic_smax v12, v10, s[36:39], 0 offen sc1\n"
    "s_mov_b32 s69, 0x80000000\n"
    "s_mov_b32 s70, 0x80000000\n"
    "v_mov_b32 v7, v12\n"
    "s_waitcnt vmcnt(12)\n"
    "s_add_u32 s71, s68, 1\n"
    "v_add_u32 v13, v11, v3\n"
    "v_cmp_gt_i32 vcc, s71, v13\n"
    "s_nop 0\n"
    "s_or_b64 exec, vcc, s[62:63]\n"
    "buffer_atomic_smax v11, v2, s[36:39], 0 offen sc0 sc1\n"
    "s_mov_b64 exec, -1\n"
    "s_waitcnt vmcnt(2)\n"
    "s_branch .LgDOWN_bar0_%=\n"
    ".LgDOWN_dma1_33_%=:\n"
    "v_mfma_f32_16x16x32_bf16 v[56:59], a[132:135], v[28:31], v[56:59]\n"
    "s_add_u32 m0, s59, 0\n"
    "v_mfma_f32_16x16x32_bf16 v[60:63], a[136:139], v[28:31], v[60:63]\n"
    "s_waitcnt lgkmcnt(2)\n"
    "buffer_load_dwordx4 v1, s[36:39], s76 offen sc1 lds\n"
    "v_mfma_f32_16x16x32_bf16 v[68:71], a[140:143], v[28:31], v[68:71]\n"
    "ds_read_b128 v[28:31], v9 offset:15360\n"
    "s_add_u32 s76, s76, 16384\n"
    "v_mfma_f32_16x16x32_bf16 v[56:59], a[144:147], v[16:19], v[56:59]\n"
    "s_add_u32 m0, s59, 4096\n"
    "v_mfma_f32_16x16x32_bf16 v[60:63], a[148:151], v[16:19], v[60:63]\n"
    "s_waitcnt lgkmcnt(2)\n"
    "buffer_load_dwordx4 v1, s[36:39], s76 offen sc1 lds\n"
    "v_mfma_f32_16x16x32_bf16 v[68:71], a[152:155], v[16:19], v[68:71]\n"
    "ds_read_b128 v[16:19], v9 offset:16384\n"
    "v_mfma_f32_16x16x32_bf16 v[56:59], a[156:159], v[20:23], v[56:59]\n"
    "s_add_u32 s76, s76, 16384\n"
    "s_add_u32 m0, s59, 8192\n"
    "v_mfma_f32_16x16x32_bf16 v[60:63], a[160:163], v[20:23], v[60:63]\n"
    "s_waitcnt lgkmcnt(2)\n"
    "buffer_load_dwordx4 v1, s[36:39], s76 offen sc1 lds\n"
    "v_mfma_f32_16x16x32_bf16 v[68:71], a[164:167], v[20:23], v[68:71]\n"
    "ds_read_b128 v[20:23], v9 offset:17408\n"
    "v_mfma_f32_16x16x32_bf16 v[56:59], a[168:171], v[24:27], v[56:59]\n"
    "v_mfma_f32_16x16x32_bf16 v[60:63], a[172:175], v[24:27], v[60:63]\n"
    "s_waitcnt lgkmcnt(2)\n"
    "s_add_u32 s76, s76, 16384\n"
    "s_add_u32 m0, s59, 12288\n"
    "v_mfma_f32_16x16x32_bf16 v[68:71], a[176:179], v[24:27], v[68:71]\n"
    "ds_read_b128 v[24:27], v9 offset:18432\n"
    "buffer_load_dwordx4 v1, s[36:39], s76 offen sc1 lds\n"
    "v_mfma_f32_16x16x32_bf16 v[56:59], a[180:183], v[28:31], v[56:59]\n"
    "v_mfma_f32_16x16x32_bf16 v[60:63], a[184:187], v[28:31], v[60:63]\n"
    "s_waitcnt lgkmcnt(2)\n"
    "v_mfma_f32_16x16x32_bf16 v[68:71], a[188:191], v[28:31], v[68:71]\n"
    "ds_read_b128 v[28:31], v9 offset:19456\n"
    "s_add_u32 s76, s76, 16384\n"
    "v_mfma_f32_16x16x32_bf16 v[56:59], a[192:195], v[16:19], v[56:59]\n"
    "s_add_u32 m0, s59, 16384\n"
    "v_mfma_f32_16x16x32_bf16 v[60:63], a[196:199], v[16:19], v[60:63]\n"
    "s_waitcnt lgkmcnt(2)\n"
    "buffer_load_dwordx4 v1, s[36:39], s76 offen sc1 lds\n"
    "v_mfma_f32_16x16x32_bf16 v[68:71], a[200:203], v[16:19], v[68:71]\n"
    "ds_read_b128 v[16:19], v9 offset:20480\n"
    "v_mfma_f32_16x16x32_bf16 v[56:59], a[204:207], v[20:23], v[56:59]\n"
    "s_add_u32 s76, s76, 16384\n"
    "s_add_u32 m0, s59, 20480\n"
    "v_mfma_f32_16x16x32_bf16 v[60:63], a[208:211], v[20:23], v[60:63]\n"
    "s_waitcnt lgkmcnt(2)\n"
    "buffer_load_dwordx4 v1, s[36:39], s76 offen sc1 lds\n"
    "v_mfma_f32_16x16x32_bf16 v[68:71], a[212:215], v[20:23], v[68:71]\n"
    "ds_read_b128 v[20:23], v9 offset:21504\n"
    "v_mfma_f32_16x16x32_bf16 v[56:59], a[216:219], v[24:27], v[56:59]\n"
    "v_mfma_f32_16x16x32_bf16 v[60:63], a[220:223], v[24:27], v[60:63]\n"
    "s_waitcnt lgkmcnt(2)\n"
    "s_waitcnt vmcnt(6)\n"
    "buffer_atomic_smax v12, v14, s[36:39], s70 offen sc1\n"
    "v_mfma_f32_16x16x32_bf16 v[68:71], a[224:227], v[24:27], v[68:71]\n"
    "ds_read_b128 v[24:27], v9 offset:22528\n"
    "s_add_u32 s76, s76, 16384\n"
    "v_mfma_f32_16x16x32_bf16 v[56:59], a[228:231], v[28:31], v[56:59]\n"
    "s_add_u32 m0, s59, 24576\n"
    "v_mfma_f32_16x16x32_bf16 v[60:63], a[232:235], v[28:31], v[60:63]\n"
    "s_waitcnt lgkmcnt(2)\n"
    "buffer_load_dwordx4 v1, s[36:39], s76 offen sc1 lds\n"
    "v_mfma_f32_16x16x32_bf16 v[68:71], a[236:239], v[28:31], v[68:71]\n"
    "ds_read_b128 v[28:31], v9 offset:23552\n"
    "s_add_u32 s76, s76, 16384\n"
    "v_mfma_f32_16x16x32_bf16 v[56:59], a[240:243], v[16:19], v[56:59]\n"
    "s_add_u32 m0, s59, 28672\n"
    "v_mfma_f32_16x16x32_bf16 v[60:63], a[244:247], v[16:19], v[60:63]\n"
    "s_waitcnt lgkmcnt(2)\n"
    "buffer_load_dwordx4 v1, s[36:39], s76 offen sc1 lds\n"
    "v_mfma_f32_16x16x32_bf16 v[68:71], a[248:251], v[16:19], v[68:71]\n"
    "ds_read_b128 v[16:19], v9 offset:24576\n"
    "v_mfma_f32_16x16x32_bf16 v[56:59], a[252:255], v[20:23], v[56:59]\n"
    "s_add_u32 s76, s76, 16384\n"
    "s_add_u32 m0, s59, 32768\n"
    "v_mfma_f32_16x16x32_bf16 v[60:63], v[80:83], v[20:23], v[60:63]\n"
    "s_waitcnt lgkmcnt(2)\n"
    "buffer_load_dwordx4 v1, s[36:39], s76 offen sc1 lds\n"
    "v_mfma_f32_16x16x32_bf16 v[68:71], v[84:87], v[20:23], v[68:71]\n"
    "ds_read_b128 v[20:23], v9 offset:25600\n"
    "v_mfma_f32_16x16x32_bf16 v[56:59], v[88:91], v[24:27], v[56:59]\n"
    "v_mfma_f32_16x16x32_bf16 v[60:63], v[92:95], v[24:27], v[60:63]\n"
    "s_waitcnt lgkmcnt(2)\n"
    "v_mfma_f32_16x16x32_bf16 v[68:71], v[96:99], v[24:27], v[68:71]\n"
    "ds_read_b128 v[24:27], v9 offset:26624\n"
    "v_mfma_f32_16x16x32_bf16 v[56:59], v[100:103], v[28:31], v[56:59]\n"
    "v_mfma_f32_16x16x32_bf16 v[60:63], v[104:107], v[28:31], v[60:63]\n"
    "s_waitcnt lgkmcnt(2)\n"
    "v_mfma_f32_16x16x32_bf16 v[68:71], v[108:111], v[28:31], v[68:71]\n"
    "ds_read_b128 v[28:31], v9 offset:27648\n"
    "s_branch .LgDOWN_join1_%=\n"
    ".LgDOWN_dma1_54_%=:\n"
    "v_mfma_f32_16x16x32_bf16 v[56:59], a[216:219], v[24:27], v[56:59]\n"
    "s_add_u32 m0, s59, 0\n"
    "v_mfma_f32_16x16x32_bf16 v[60:63], a[220:223], v[24:27], v[60:63]\n"
    "s_waitcnt lgkmcnt(2)\n"
    "s_waitcnt vmcnt(1)\n"
    "buffer_atomic_smax v12, v14, s[36:39], s70 offen sc1\n"
    "v_mfma_f32_16x16x32_bf16 v[68:71], a[224:227], v[24:27], v[68:71]\n"
    "ds_read_b128 v[24:27], v9 offset:22528\n"
    "buffer_load_dwordx4 v1, s[36:39], s76 offen sc1 lds\n"
    "v_mfma_f32_16x16x32_bf16 v[56:59], a[228:231], v[28:31], v[56:59]\n"
    "s_add_u32 s76, s76, 16384\n"
    "s_add_u32 m0, s59, 4096\n"
    "v_mfma_f32_16x16x32_bf16 v[60:63], a[232:235], v[28:31], v[60:63]\n"
    "s_waitcnt lgkmcnt(2)\n"
    "buffer_load_dwordx4 v1, s[36:39], s76 offen sc1 lds\n"
    "v_mfma_f32_16x16x32_bf16 v[68:71], a[236:239], v[28:31], v[68:71]\n"
    "ds_read_b128 v[28:31], v9 offset:23552\n"
    "v_mfma_f32_16x16x32_bf16 v[56:59], a[240:243], v[16:19], v[56:59]\n"
    "s_add_u32 s76, s76, 16384\n"
    "s_add_u32 m0, s59, 8192\n"
    "v_mfma_f32_16x16x32_bf16 v[60:63], a[244:247], v[16:19], v[60:63]\n"
    "s_waitcnt lgkmcnt(2)\n"
    "buffer_load_dwordx4 v1, s[36:39], s76 offen sc1 lds\n"
    "v_mfma_f32_16x16x32_bf16 v[68:71], a[248:251], v[16:19], v[68:71]\n"
    "ds_read_b128 v[16:19], v9 offset:24576\n"
    "v_mfma_f32_16x16x32_bf16 v[56:59], a[252:255], v[20:23], v[56:59]\n"
    "v_mfma_f32_16x16x32_bf16 v[60:63], v[80:83], v[20:23], v[60:63]\n"
    "s_waitcnt lgkmcnt(2)\n"
    "s_add_u32 s76, s76, 16384\n"
    "s_add_u32 m0, s59, 12288\n"
    "v_mfma_f32_16x16x32_bf16 v[68:71], v[84:87], v[20:23], v[68:71]\n"
    "ds_read_b128 v[20:23], v9 offset:25600\n"
    "buffer_load_dwordx4 v1, s[36:39], s76 offen sc1 lds\n"
    "v_mfma_f32_16x16x32_bf16 v[56:59], v[88:91], v[24:27], v[56:59]\n"
    "v_mfma_f32_16x16x32_bf16 v[60:63], v[92:95], v[24:27], v[60:63]\n"
    "s_waitcnt lgkmcnt(2)\n"
    "v_mfma_f32_16x16x32_bf16 v[68:71], v[96:99], v[24:27], v[68:71]\n"
    "ds_read_b128 v[24:27], v9 offset:26624\n"
    "s_add_u32 s76, s76, 16384\n"
    "v_mfma_f32_16x16x32_bf16 v[56:59], v[100:103], v[28:31], v[56:59]\n"
    "s_add_u32 m0, s59, 16384\n"
    "v_mfma_f32_16x16x32_bf16 v[60:63], v[104:107], v[28:31], v[60:63]\n"
    "s_waitcnt lgkmcnt(2)\n"
    "buffer_load_dwordx4 v1, s[36:39], s76 offen sc1 lds\n"
    "v_mfma_f32_16x16x32_bf16 v[68:71], v[108:111], v[28:31], v[68:71]\n"
    "ds_read_b128 v[28:31], v9 offset:27648\n"
    "v_mfma_f32_16x16x32_bf16 v[56:59], v[112:115], v[16:19], v[56:59]\n"
    "s_add_u32 s76, s76, 16384\n"
    "s_add_u32 m0, s59, 20480\n"
    "v_mfma_f32_16x16x32_bf16 v[60:63], v[116:119], v[16:19], v[60:63]\n"
    "s_waitcnt lgkmcnt(2)\n"
    "buffer_load_dwordx4 v1, s[36:39], s76 offen sc1 lds\n"
    "v_mfma_f32_16x16x32_bf16 v[68:71], v[120:123], v[16:19], v[68:71]\n"
    "ds_read_b128 v[16:19], v9 offset:28672\n"
    "v_mfma_f32_16x16x32_bf16 v[56:59], v[124:127], v[20:23], v[56:59]\n"
    "v_mfma_f32_16x16x32_bf16 v[60:63], v[128:131], v[20:23], v[60:63]\n"
    "s_waitcnt lgkmcnt(2)\n"
    "s_add_u32 s76, s76, 16384\n"
    "s_add_u32 m0, s59, 24576\n"
    "v_mfma_f32_16x16x32_bf16 v[68:71], v[132:135], v[20:23], v[68:71]\n"
    "ds_read_b128 v[20:23], v9 offset:29696\n"
    "buffer_load_dwordx4 v1, s[36:39], s76 offen sc1 lds\n"
    "v_mfma_f32_16x16x32_bf16 v[56:59], v[136:139], v[24:27], v[56:59]\n"
    "v_mfma_f32_16x16x32_bf16 v[60:63], v[140:143], v[24:27], v[60:63]\n"
    "s_waitcnt lgkmcnt(2)\n"
    "v_mfma_f32_16x16x32_bf16 v[68:71], v[144:147], v[24:27], v[68:71]\n"
    "ds_read_b128 v[24:27], v9 offset:30720\n"
    "s_add_u32 s76, s76, 16384\n"
    "v_mfma_f32_16x16x32_bf16 v[56:59], v[148:151], v[28:31], v[56:59]\n"
    "s_add_u32 m0, s59, 28672\n"
    "v_mfma_f32_16x16x32_bf16 v[60:63], v[152:155], v[28:31], v[60:63]\n"
    "s_waitcnt lgkmcnt(2)\n"
    "buffer_load_dwordx4 v1, s[36:39], s76 offen sc1 lds\n"
    "v_mfma_f32_16x16x32_bf16 v[68:71], v[156:159], v[28:31], v[68:71]\n"
    "ds_read_b128 v[28:31], v9 offset:31744\n"
    "v_mfma_f32_16x16x32_bf16 v[56:59], v[160:163], v[16:19], v[56:59]\n"
    "s_add_u32 s76, s76, 16384\n"
    "s_add_u32 m0, s59, 32768\n"
    "v_mfma_f32_16x16x32_bf16 v[60:63], v[164:167], v[16:19], v[60:63]\n"
    "s_waitcnt lgkmcnt(2)\n"
    "buffer_load_dwordx4 v1, s[36:39], s76 offen sc1 lds\n"
    "v_mfma_f32_16x16x32_bf16 v[68:71], v[168:171], v[16:19], v[68:71]\n"
    "ds_read_b128 v[16:19], v9 offset:32768\n"
    "v_mfma_f32_16x16x32_bf16 v[56:59], v[172:175], v[20:23], v[56:59]\n"
    "v_mfma_f32_16x16x32_bf16 v[60:63], v[176:179], v[20:23], v[60:63]\n"
    "s_waitcnt lgkmcnt(2)\n"
    "v_mfma_f32_16x16x32_bf16 v[68:71], v[180:183], v[20:23], v[68:71]\n"
    "ds_read_b128 v[20:23], v9 offset:33792\n"
    "v_mfma_f32_16x16x32_bf16 v[56:59], v[184:187], v[24:27], v[56:59]\n"
    "v_mfma_f32_16x16x32_bf16 v[60:63], v[188:191], v[24:27], v[60:63]\n"
    "s_waitcnt lgkmcnt(2)\n"
    "v_mfma_f32_16x16x32_bf16 v[68:71], v[192:195], v[24:27], v[68:71]\n"
    "ds_read_b128 v[24:27], v9 offset:34816\n"
    "v_mfma_f32_16x16x32_bf16 v[56:59], v[196:199], v[28:31], v[56:59]\n"
    "v_mfma_f32_16x16x32_bf16 v[60:63], v[200:203], v[28:31], v[60:63]\n"
    "s_waitcnt lgkmcnt(2)\n"
    "v_mfma_f32_16x16x32_bf16 v[68:71], v[204:207], v[28:31], v[68:71]\n"
    "ds_read_b128 v[28:31], v9 offset:35840\n"
    "v_mfma_f32_16x16x32_bf16 v[56:59], v[208:211], v[16:19], v[56:59]\n"
    "v_mfma_f32_16x16x32_bf16 v[60:63], v[212:215], v[16:19], v[60:63]\n"
    "s_waitcnt lgkmcnt(2)\n"
    "v_mfma_f32_16x16x32_bf16 v[68:71], v[216:219], v[16:19], v[68:71]\n"
    "v_mfma_f32_16x16x32_bf16 v[56:59], v[220:223], v[20:23], v[56:59]\n"
    "v_mfma_f32_16x16x32_bf16 v[60:63], v[224:227], v[20:23], v[60:63]\n"
    "s_waitcnt lgkmcnt(1)\n"
    "v_mfma_f32_16x16x32_bf16 v[68:71], v[228:231], v[20:23], v[68:71]\n"
    "v_mfma_f32_16x16x32_bf16 v[56:59], v[232:235], v[24:27], v[56:59]\n"
    "v_mfma_f32_16x16x32_bf16 v[60:63], v[236:239], v[24:27], v[60:63]\n"
    "s_waitcnt lgkmcnt(0)\n"
    "v_mfma_f32_16x16x32_bf16 v[68:71], v[240:243], v[24:27], v[68:71]\n"
    "v_mfma_f32_16x16x32_bf16 v[56:59], v[244:247], v[28:31], v[56:59]\n"
    "v_mfma_f32_16x16x32_bf16 v[60:63], v[248:251], v[28:31], v[60:63]\n"
    "v_mfma_f32_16x16x32_bf16 v[68:71], v[252:255], v[28:31], v[68:71]\n"
    "v_cvt_pk_bf16_f32 v56, v56, v57\n"
    "v_cvt_pk_bf16_f32 v57, v58, v59\n"
    "s_nop 1\n"
    "v_cvt_pk_bf16_f32 v58, v60, v61\n"
    "v_cvt_pk_bf16_f32 v59, v62, v63\n"
    "buffer_store_dwordx4 v[56:59], v15, s[36:39], s73 offen offset:1024 sc1\n"
    "s_nop 0\n"
    "v_cvt_pk_bf16_f32 v68, v68, v69\n"
    "v_cvt_pk_bf16_f32 v69, v70, v71\n"
    "s_nop 0\n"
    "v_permlane16_swap_b32 v68, v70\n"
    "v_permlane16_swap_b32 v69, v71\n"
    "buffer_store_dwordx4 v[68:71], v4, s[36:39], s73 offen offset:1024 sc1\n"
    "v_mov_b32 v12, s68\n"
    "s_mov_b32 s73, 0x80000000\n"
    "s_mov_b32 s68, s71\n"
    "s_mov_b32 s69, 0\n"
    "s_mov_b32 s70, 0x80000000\n"
    "v_mov_b32 v7, v12\n"
    "s_waitcnt vmcnt(12)\n"
    "s_add_u32 s71, s68, 1\n"
    "v_add_u32 v13, v11, v3\n"
    "v_cmp_gt_i32 vcc, s71, v13\n"
    "s_nop 0\n"
    "s_or_b64 exec, vcc, s[62:63]\n"
    "buffer_atomic_smax v11, v2, s[36:39], 0 offen sc0 sc1\n"
    "s_mov_b64 exec, -1\n"
    "s_waitcnt vmcnt(3)\n"
    "s_branch .LgDOWN_bar0_%=\n"
    ".LgDOWN_dma1_75_%=:\n"
    "v_mfma_f32_16x16x32_bf16 v[56:59], v[124:127], v[20:23], v[56:59]\n"
    "s_add_u32 m0, s59, 0\n"
    "v_mfma_f32_16x16x32_bf16 v[60:63], v[128:131], v[20:23], v[60:63]\n"
    "s_waitcnt lgkmcnt(2)\n"
    "buffer_load_dwordx4 v1, s[36:39], s76 offen sc1 lds\n"
    "v_mfma_f32_16x16x32_bf16 v[68:71], v[132:135], v[20:23], v[68:71]\n"
    "ds_read_b128 v[20:23], v9 offset:29696\n"
    "s_add_u32 s76, s76, 16384\n"
    "v_mfma_f32_16x16x32_bf16 v[56:59], v[136:139], v[24:27], v[56:59]\n"
    "s_add_u32 m0, s59, 4096\n"
    "v_mfma_f32_16x16x32_bf16 v[60:63], v[140:143], v[24:27], v[60:63]\n"
    "s_waitcnt lgkmcnt(2)\n"
    "buffer_load_dwordx4 v1, s[36:39], s76 offen sc1 lds\n"
    "v_mfma_f32_16x16x32_bf16 v[68:71], v[144:147], v[24:27], v[68:71]\n"
    "ds_read_b128 v[24:27], v9 offset:30720\n"
    "v_mfma_f32_16x16x32_bf16 v[56:59], v[148:151], v[28:31], v[56:59]\n"
    "s_add_u32 s76, s76, 16384\n"
    "s_add_u32 m0, s59, 8192\n"
    "v_mfma_f32_16x16x32_bf16 v[60:63], v[152:155], v[28:31], v[60:63]\n"
    "s_waitcnt lgkmcnt(2)\n"
    "buffer_load_dwordx4 v1, s[36:39], s76 offen sc1 lds\n"
    "v_mfma_f32_16x16x32_bf16 v[68:71], v[156:159], v[28:31], v[68:71]\n"
    "ds_read_b128 v[28:31], v9 offset:31744\n"
    "v_mfma_f32_16x16x32_bf16 v[56:59], v[160:163], v[16:19], v[56:59]\n"
    "v_mfma_f32_16x16x32_bf16 v[60:63], v[164:167], v[16:19], v[60:63]\n"
    "s_waitcnt lgkmcnt(2)\n"
    "s_add_u32 s76, s76, 16384\n"
    "s_add_u32 m0, s59, 12288\n"
    "v_mfma_f32_16x16x32_bf16 v[68:71], v[168:171], v[16:19], v[68:71]\n"
    "ds_read_b128 v[16:19], v9 offset:32768\n"
    "buffer_load_dwordx4 v1, s[36:39], s76 offen sc1 lds\n"
    "v_mfma_f32_16x16x32_bf16 v[56:59], v[172:175], v[20:23], v[56:59]\n"
    "v_mfma_f32_16x16x32_bf16 v[60:63], v[176:179], v[20:23], v[60:63]\n"
    "s_waitcnt lgkmcnt(2)\n"
    "v_mfma_f32_16x16x32_bf16 v[68:71], v[180:183], v[20:23], v[68:71]\n"
    "ds_read_b128 v[20:23], v9 offset:33792\n"
    "s_add_u32 s76, s76, 16384\n"
    "v_mfma_f32_16x16x32_bf16 v[56:59], v[184:187], v[24:27], v[56:59]\n"
    "s_add_u32 m0, s59, 16384\n"
    "v_mfma_f32_16x16x32_bf16 v[60:63], v[188:191], v[24:27], v[60:63]\n"
    "s_waitcnt lgkmcnt(2)\n"
    "buffer_load_dwordx4 v1, s[36:39], s76 offen sc1 lds\n"
    "v_mfma_f32_16x16x32_bf16 v[68:71], v[192:195], v[24:27], v[68:71]\n"
    "ds_read_b128 v[24:27], v9 offset:34816\n"
    "v_mfma_f32_16x16x32_bf16 v[56:59], v[196:199], v[28:31], v[56:59]\n"
    "s_add_u32 s76, s76, 16384\n"
    "s_add_u32 m0, s59, 20480\n"
    "v_mfma_f32_16x16x32_bf16 v[60:63], v[200:203], v[28:31], v[60:63]\n"
    "s_waitcnt lgkmcnt(2)\n"
    "buffer_load_dwordx4 v1, s[36:39], s76 offen sc1 lds\n"
    "v_mfma_f32_16x16x32_bf16 v[68:71], v[204:207], v[28:31], v[68:71]\n"
    "ds_read_b128 v[28:31], v9 offset:35840\n"
    "v_mfma_f32_16x16x32_bf16 v[56:59], v[208:211], v[16:19], v[56:59]\n"
    "v_mfma_f32_16x16x32_bf16 v[60:63], v[212:215], v[16:19], v[60:63]\n"
    "s_waitcnt lgkmcnt(2)\n"
    "s_add_u32 s76, s76, 16384\n"
    "s_add_u32 m0, s59, 24576\n"
    "v_mfma_f32_16x16x32_bf16 v[68:71], v[216:219], v[16:19], v[68:71]\n"
    "buffer_load_dwordx4 v1, s[36:39], s76 offen sc1 lds\n"
    "v_mfma_f32_16x16x32_bf16 v[56:59], v[220:223], v[20:23], v[56:59]\n"
    "v_mfma_f32_16x16x32_bf16 v[60:63], v[224:227], v[20:23], v[60:63]\n"
    "s_waitcnt lgkmcnt(1)\n"
    "v_mfma_f32_16x16x32_bf16 v[68:71], v[228:231], v[20:23], v[68:71]\n"
    "s_add_u32 s76, s76, 16384\n"
    "v_mfma_f32_16x16x32_bf16 v[56:59], v[232:235], v[24:27], v[56:59]\n"
    "s_add_u32 m0, s59, 28672\n"
    "v_mfma_f32_16x16x32_bf16 v[60:63], v[236:239], v[24:27], v[60:63]\n"
    "s_waitcnt lgkmcnt(0)\n"
    "buffer_load_dwordx4 v1, s[36:39], s76 offen sc1 lds\n"
    "v_mfma_f32_16x16x32_bf16 v[68:71], v[240:243], v[24:27], v[68:71]\n"
    "v_mfma_f32_16x16x32_bf16 v[56:59], v[244:247], v[28:31], v[56:59]\n"
    "s_add_u32 s76, s76, 16384\n"
    "s_add_u32 m0, s59, 32768\n"
    "v_mfma_f32_16x16x32_bf16 v[60:63], v[248:251], v[28:31], v[60:63]\n"
    "buffer_load_dwordx4 v1, s[36:39], s76 offen sc1 lds\n"
    "v_mfma_f32_16x16x32_bf16 v[68:71], v[252:255], v[28:31], v[68:71]\n"
    "v_cvt_pk_bf16_f32 v56, v56, v57\n"
    "v_cvt_pk_bf16_f32 v57, v58, v59\n"
    "s_nop 1\n"
    "v_cvt_pk_bf16_f32 v58, v60, v61\n"
    "v_cvt_pk_bf16_f32 v59, v62, v63\n"
    "buffer_store_dwordx4 v[56:59], v15, s[36:39], s73 offen offset:1024 sc1\n"
    "s_nop 0\n"
    "v_cvt_pk_bf16_f32 v68, v68, v69\n"
    "v_cvt_pk_bf16_f32 v69, v70, v71\n"
    "s_nop 0\n"
    "v_permlane16_swap_b32 v68, v70\n"
    "v_permlane16_swap_b32 v69, v71\n"
    "buffer_store_dwordx4 v[68:71], v4, s[36:39], s73 offen offset:1024 sc1\n"
    "v_mov_b32 v12, s68\n"
    "s_mov_b32 s73, 0x80000000\n"
    "s_mov_b32 s68, s71\n"
    "s_mov_b32 s69, 0\n"
    "s_mov_b32 s70, 0x80000000\n"
    "v_mov_b32 v7, v12\n"
    "s_waitcnt vmcnt(12)\n"
    "s_add_u32 s71, s68, 1\n"
    "v_add_u32 v13, v11, v3\n"
    "v_cmp_gt_i32 vcc, s71, v13\n"
    "s_nop 0\n"
    "s_or_b64 exec, vcc, s[62:63]\n"
    "buffer_atomic_smax v11, v2, s[36:39], 0 offen sc0 sc1\n"
    "s_mov_b64 exec, -1\n"
    "s_waitcnt vmcnt(3)\n"
    "s_branch .LgDOWN_bar0_%=\n"
    ".LgDOWN_dma1_96_%=:\n"
    "v_mfma_f32_16x16x32_bf16 v[56:59], v[208:211], v[16:19], v[56:59]\n"
    "s_add_u32 m0, s59, 0\n"
    "v_mfma_f32_16x16x32_bf16 v[60:63], v[212:215], v[16:19], v[60:63]\n"
    "s_waitcnt lgkmcnt(2)\n"
    "buffer_load_dwordx4 v1, s[36:39], s76 offen sc1 lds\n"
    "v_mfma_f32_16x16x32_bf16 v[68:71], v[216:219], v[16:19], v[68:71]\n"
    "s_add_u32 s76, s76, 16384\n"
    "s_add_u32 m0, s59, 4096\n"
    "v_mfma_f32_16x16x32_bf16 v[56:59], v[220:223], v[20:23], v[56:59]\n"
    "buffer_load_dwordx4 v1, s[36:39], s76 offen sc1 lds\n"
    "v_mfma_f32_16x16x32_bf16 v[60:63], v[224:227], v[20:23], v[60:63]\n"
    "s_waitcnt lgkmcnt(1)\n"
    "v_mfma_f32_16x16x32_bf16 v[68:71], v[228:231], v[20:23], v[68:71]\n"
    "v_mfma_f32_16x16x32_bf16 v[56:59], v[232:235], v[24:27], v[56:59]\n"
    "s_add_u32 s76, s76, 16384\n"
    "s_add_u32 m0, s59, 8192\n"
    "v_mfma_f32_16x16x32_bf16 v[60:63], v[236:239], v[24:27], v[60:63]\n"
    "s_waitcnt lgkmcnt(0)\n"
    "buffer_load_dwordx4 v1, s[36:39], s76 offen sc1 lds\n"
    "v_mfma_f32_16x16x32_bf16 v[68:71], v[240:243], v[24:27], v[68:71]\n"
    "v_mfma_f32_16x16x32_bf16 v[56:59], v[244:247], v[28:31], v[56:59]\n"
    "v_mfma_f32_16x16x32_bf16 v[60:63], v[248:251], v[28:31], v[60:63]\n"
    "s_add_u32 s76, s76, 16384\n"
    "s_add_u32 m0, s59, 12288\n"
    "v_mfma_f32_16x16x32_bf16 v[68:71], v[252:255], v[28:31], v[68:71]\n"
    "buffer_load_dwordx4 v1, s[36:39], s76 offen sc1 lds\n"
    "v_cvt_pk_bf16_f32 v56, v56, v57\n"
    "v_cvt_pk_bf16_f32 v57, v58, v59\n"
    "s_nop 0\n"
    "v_cvt_pk_bf16_f32 v58, v60, v61\n"
    "v_cvt_pk_bf16_f32 v59, v62, v63\n"
    "buffer_store_dwordx4 v[56:59], v15, s[36:39], s73 offen offset:1024 sc1\n"
    "s_nop 0\n"
    "v_cvt_pk_bf16_f32 v68, v68, v69\n"
    "v_cvt_pk_bf16_f32 v69, v70, v71\n"
    "s_nop 0\n"
    "v_permlane16_swap_b32 v68, v70\n"
    "v_permlane16_swap_b32 v69, v71\n"
    "buffer_store_dwordx4 v[68:71], v4, s[36:39], s73 offen offset:1024 sc1\n"
    "v_mov_b32 v12, s68\n"
    "s_mov_b32 s73, 0x80000000\n"
    "s_mov_b32 s68, s71\n"
    "s_add_u32 m0, s59, 16384\n"
    "s_add_u32 s76, s76, 16384\n"
    "buffer_load_dwordx4 v1, s[36:39], s76 offen sc1 lds\n"
    "s_add_u32 m0, s59, 20480\n"
    "s_add_u32 s76, s76, 16384\n"
    "buffer_load_dwordx4 v1, s[36:39], s76 offen sc1 lds\n"
    "s_add_u32 m0, s59, 24576\n"
    "s_add_u32 s76, s76, 16384\n"
    "buffer_load_dwordx4 v1, s[36:39], s76 offen sc1 lds\n"
    "s_add_u32 m0, s59, 28672\n"
    "s_add_u32 s76, s76, 16384\n"
    "buffer_load_dwordx4 v1, s[36:39], s76 offen sc1 lds\n"
    "s_add_u32 m0, s59, 32768\n"
    "s_add_u32 s76, s76, 16384\n"
    "buffer_load_dwordx4 v1, s[36:39], s76 offen sc1 lds\n"
    "s_waitcnt vmcnt(5)\n"
    "buffer_atomic_smax v12, v10, s[36:39], 0 offen sc1\n"
    "s_mov_b32 s69, 0x80000000\n"
    "s_mov_b32 s70, 0x80000000\n"
    "v_mov_b32 v7, v12\n"
    "s_waitcnt vmcnt(12)\n"
    "s_add_u32 s71, s68, 1\n"
    "v_add_u32 v13, v11, v3\n"
    "v_cmp_gt_i32 vcc, s71, v13\n"
    "s_nop 0\n"
    "s_or_b64 exec, vcc, s[62:63]\n"
    "buffer_atomic_smax v11, v2, s[36:39], 0 offen sc0 sc1\n"
    "s_mov_b64 exec, -1\n"
    "s_waitcnt vmcnt(2)\n"
    "s_branch .LgDOWN_bar0_%=\n"
    ".LgDOWN_idle1_%=:\n"
    "s_or_b64 exec, vcc, s[62:63]\n"
    "buffer_atomic_smax v5, v2, s[36:39], 0 offen sc0 sc1\n"
    "s_mov_b64 exec, -1\n"
    "s_waitcnt vmcnt(1)\n"
    "buffer_atomic_smax v12, v10, s[36:39], 0 offen sc1\n"
    "s_branch .LgDOWN_block1_1_%=\n"
    ".LgDOWN_block1_%=:\n"
    "v_add_u32 v13, v11, v3\n"
    "v_cmp_gt_i32 vcc, s71, v13\n"
    "s_nop 0\n"
    "s_or_b64 exec, vcc, s[62:63]\n"
    "buffer_atomic_smax v11, v2, s[36:39], 0 offen sc0 sc1\n"
    "s_mov_b64 exec, -1\n"
    "s_sleep 3\n"
    "s_waitcnt vmcnt(1)\n"
    ".LgDOWN_block1_0_%=:\n"
    "v_add_u32 v13, v5, v3\n"
    "v_cmp_gt_i32 vcc, s71, v13\n"
    "s_cbranch_vccz .LgDOWN_go1_%=\n"
    "v_add_u32 v13, v5, v3\n"
    "v_cmp_gt_i32 vcc, s71, v13\n"
    "s_nop 0\n"
    "s_or_b64 exec, vcc, s[62:63]\n"
    "buffer_atomic_smax v5, v2, s[36:39], 0 offen sc0 sc1\n"
    "s_mov_b64 exec, -1\n"
    "s_sleep 3\n"
    "s_waitcnt vmcnt(1)\n"
    ".LgDOWN_block1_1_%=:\n"
    "v_add_u32 v13, v11, v3\n"
    "v_cmp_gt_i32 vcc, s71, v13\n"
    "s_cbranch_vccz .LgDOWN_go1_%=\n"
    "s_sub_u32 s72, s72, 1\n"
    "s_cbranch_scc0 .LgDOWN_block1_%=\n"
    "s_mov_b32 s72, 0\n"
    ".LgDOWN_go1_%=:\n"
    "s_lshr_b32 s76, s71, 1\n"
    "s_and_b32 s76, s76, s54\n"
    "s_mul_i32 s76, s76, s53\n"
    "s_add_u32 s76, s76, s52\n"
    "s_add_u32 m0, s59, 0\n"
    "s_nop 0\n"
    "buffer_load_dwordx4 v1, s[36:39], s76 offen sc1 lds\n"
    "s_add_u32 m0, s59, 4096\n"
    "s_add_u32 s76, s76, 16384\n"
    "buffer_load_dwordx4 v1, s[36:39], s76 offen sc1 lds\n"
    "s_add_u32 m0, s59, 8192\n"
    "s_add_u32 s76, s76, 16384\n"
    "buffer_load_dwordx4 v1, s[36:39], s76 offen sc1 lds\n"
    "s_add_u32 m0, s59, 12288\n"
    "s_add_u32 s76, s76, 16384\n"
    "buffer_load_dwordx4 v1, s[36:39], s76 offen sc1 lds\n"
    "s_add_u32 m0, s59, 16384\n"
    "s_add_u32 s76, s76, 16384\n"
    "buffer_load_dwordx4 v1, s[36:39], s76 offen sc1 lds\n"
    "s_add_u32 m0, s59, 20480\n"
    "s_add_u32 s76, s76, 16384\n"
    "buffer_load_dwordx4 v1, s[36:39], s76 offen sc1 lds\n"
    "s_add_u32 m0, s59, 24576\n"
    "s_add_u32 s76, s76, 16384\n"
    "buffer_load_dwordx4 v1, s[36:39], s76 offen sc1 lds\n"
    "s_add_u32 m0, s59, 28672\n"
    "s_add_u32 s76, s76, 16384\n"
    "buffer_load_dwordx4 v1, s[36:39], s76 offen sc1 lds\n"
    "s_add_u32 m0, s59, 32768\n"
    "s_add_u32 s76, s76, 16384\n"
    "buffer_load_dwordx4 v1, s[36:39], s76 offen sc1 lds\n"
    "s_mov_b32 s69, 0x80000000\n"
    "s_mov_b32 s70, 0x80000000\n"
    "v_mov_b32 v7, v12\n"
    "s_waitcnt vmcnt(9)\n"
    "s_add_u32 s71, s68, 1\n"
    "v_add_u32 v13, v11, v3\n"
    "v_cmp_gt_i32 vcc, s71, v13\n"
    "s_nop 0\n"
    "s_or_b64 exec, vcc, s[62:63]\n"
    "buffer_atomic_smax v11, v2, s[36:39], 0 offen sc0 sc1\n"
    "s_mov_b64 exec, -1\n"
    "s_waitcnt vmcnt(1)\n"
    "s_branch .LgDOWN_bar0_%=\n"
    ".LgDOWN_bar1_%=:\n"
    "s_barrier\n"
    "ds_read_b128 v[16:19], v9 offset:0\n"
    "ds_read_b128 v[20:23], v9 offset:1024\n"
    "ds_read_b128 v[24:27], v9 offset:2048\n"
    "ds_read_b128 v[28:31], v9 offset:3072\n"
    "v_add_u32 v13, v5, v3\n"
    "v_cmp_gt_i32 vcc, s71, v13\n"
    "s_nop 0\n"
    "s_or_b64 exec, vcc, s[62:63]\n"
    "buffer_atomic_smax v5, v2, s[36:39], 0 offen sc0 sc1\n"
    "s_mov_b64 exec, -1\n"
    "s_lshr_b32 s76, s71, 1\n"
    "s_and_b32 s76, s76, s54\n"
    "s_mul_i32 s76, s76, s53\n"
    "s_add_u32 s76, s76, s52\n"
    "s_waitcnt lgkmcnt(3)\n"
    "s_branch .LgDOWN_head1_%=\n"
    ".LgDOWN_FIN_%=:\n"
  );
}

DEVI void role_rec(const Params& p, int L, int j, int i, u32 dbg, int& rid, unsigned long long& tag, bool& sure, unsigned char* smem) {
  const int tid = threadIdx.x, lane = tid & 63, w = (int)rfl(tid >> 6), h = 8 * j + i;
  const int c = lane & 15, g = lane >> 4, pp = lane & 3;
  {
    v4u t;
    const int q0 = w == 0 ? 4 * (c >> 3) + (c & 3) : (c & 7);
    const u32 one = (w == 0 && ((c >> 2) & 1) != (g & 1)) ? 0u : 0x3F80u << (16 * (q0 & 1));
    const v4u t1 = {(u32)(((g & 1) * 16 + c) * 8), (u32)(g * 128 + (c & 7) * 16 + (c >> 3) * 8), c < 8 ? (u32)(g * 128 + c * 16) : OOB, (u32)((lane & 31) * 64 + (lane >> 5) * 8)};
    const v4u t2 = {(u32)(g * 16384 + c * 16), g == 0 ? (u32)(c * 16) : OOB,
                    (u32)((g >> 1) * 128 + (c & 7) * 16 + (2 * (g & 1) + (c >> 3)) * 32768), t1.w};
#pragma unroll
    for (int d = 0; d < 4; ++d) t[d] = w == 1 ? t1[d] : (w == 2 ? t2[d] : ((q0 >> 1) == d ? one : 0u));
    *(v4u*)(smem + 12288 + tid * 16) = t;
    asm volatile("s_waitcnt lgkmcnt(0)" ::: "memory");
  }
  const u32 wb = 576u * 4096 * 2 - h * 256;
  v16u sa = cat4(make_srd(p.ws, WS_END), make_srd(p.Wq + h * 128, wb), make_srd(p.Wk + h * 128, wb), make_srd(p.Wv + h * 128, wb));
  const v4u so = make_srd(p.Wo + h * 128 * 576, 128u * 1152);
  const int mine = 4 * i + j, unit = w == 0 ? mine : ((w == 1 && i == 3) ? 32 + j : -1);
  const u32 hot = HOT + L * 8192, hot2 = HOT2 + (1 - L) * 8192, img = hot2 + j * 2 * CHUNK;
  v16u sb = {so.x, so.y, so.z, so.w,
             pga(L, B_QKV + 3 * h + 1) + w * 2048, pga(L, B_KVM + h) + w * 1536, mbox_att(L, j, i, w), mbox(L, j, SL_REC + i, w),
             (u32)w * 1024, dbg + w * 64, INITF + L * 8192, hot + 4 * w * CHUNK + h * 256,
             unit < 0 ? OOB : hot + unit * CHUNK, unit < 0 ? OOB : hot2 + (unit >> 5) * CHUNK + (unit & 31) * 256, img, hot + (32 + w) * CHUNK + h * 256};
  const int jr = lane >> 3;
  const u32 row = (u32)(16 * (w >> 1) + 8 * ((jr >> 1) & 1) + 4 * (w & 1) + 2 * (jr >> 2) + (jr & 1));
  const u32 ch = (((u32)lane & 7) ^ wswz(jr, w)) << 4;
  v16u va = {0, 0, 0, 0, 0, 0, 0, 0, 0, 0, 0, 0, 0, 0, 0, 0};
  va[0] = lane * 16;
  va[1] = 16384 + w * 2048 + lane * 16;
  va[2] = (u32)((c >> 3) * 0xAAAA) | 0x4444u;
  const int qq = (lane >> 2) & 3;
  va[3] = (u32)((g >> 1) * 4096 + (g & 1) * 2048 + (4 * (qq >> 1) + (qq & 1)) * 128 + ((2 * w + (pp >> 1)) << 4) + 8 * (pp & 1));
  va[4] = row * 8192 + ch;
  va[5] = row * 1152 + ch;
  va[6] = (u32)(w >> 1) * 4096 + tr_base(lane, 4 * (w & 1) + pp);
  va[7] = (u32)w * 4096 + tr_base(lane, 4 * (qq >> 1) + (pp >> 1)) + 8 * (pp & 1);
  va[8] = hot2 + 32 * CHUNK + ((w == 0 && i < 4) ? 0 : 1);
  va[9] = (u32)(h * 64 + 4 * w);
  va[10] = hot + mine * CHUNK + w * 2048;
  va[11] = w == 3 ? hot + (32 + j) * CHUNK + i * 1024 : (w == 2 ? img + i * 1024 : ((w == 1 && i == 1) ? img + CHUNK : OOB));
  va[12] = w == 3 ? ~0u : 0u;
  va[13] = w == 3 ? 0u : ~0u;
  asm volatile("" : "+v"(va));
  if (!confirm(rid, tag, sure, smem)) return;
  va[14] = (u32)(2 * tag); va[15] = (u32)((2 * tag) >> 32);
  RUN_ASM(
    "s_nop 4\n"
    "s_getpc_b64 s[88:89]\n"
    ".Lrec_PC_%=:\n"
    "s_and_b32 s89, s89, 0xffff\n"
    "s_mov_b32 s90, .Lrec_FIN_%=-.Lrec_PC_%=\n"
    "s_mov_b32 s91, 0x20000\n"
    "v_mbcnt_lo_u32_b32 v78, -1, 0\n"
    "v_mbcnt_hi_u32_b32 v78, -1, v78\n"
    "v_lshlrev_b32 v78, 7, v78\n"
    "buffer_load_dword v79, v78, s[88:91], 0 offen\n"
    "v_add_u32 v78, 8192, v78\n"
    "buffer_load_dword v79, v78, s[88:91], 0 offen\n"
    "v_readfirstlane_b32 s76, v8\n"
    "v_readfirstlane_b32 s77, v9\n"
    "v_readfirstlane_b32 s78, v10\n"
    "v_readfirstlane_b32 s79, v11\n"
    "v_readfirstlane_b32 s80, v12\n"
    "v_readfirstlane_b32 s81, v13\n"
    "v_readfirstlane_b32 s30, v14\n"
    "v_readfirstlane_b32 s31, v15\n"
    "s_memtime s[34:35]\n"
    "s_memrealtime s[92:93]\n"
    "s_mov_b32 s69, 262144\n"
    "s_add_u32 m0, s60, 16384\n"
    "s_mov_b32 s73, 0\n"
    "buffer_load_dwordx4 v4, s[40:43], s73 offen lds\n"
    "s_add_u32 m0, s60, 20480\n"
    "s_mov_b32 s73, 128\n"
    "buffer_load_dwordx4 v4, s[40:43], s73 offen lds\n"
    "s_add_u32 m0, s60, 24576\n"
    "s_mov_b32 s73, 0\n"
    "buffer_load_dwordx4 v4, s[44:47], s73 offen lds\n"
    "s_add_u32 m0, s60, 28672\n"
    "s_mov_b32 s73, 128\n"
    "buffer_load_dwordx4 v4, s[44:47], s73 offen lds\n"
    "s_add_u32 m0, s60, 32768\n"
    "s_mov_b32 s73, 0\n"
    "buffer_load_dwordx4 v4, s[48:51], s73 offen lds\n"
    "s_add_u32 m0, s60, 36864\n"
    "s_mov_b32 s73, 128\n"
    "buffer_load_dwordx4 v4, s[48:51], s73 offen lds\n"
    "s_add_u32 m0, s60, 40960\n"
    "s_mov_b32 s73, 262144\n"
    "buffer_load_dwordx4 v4, s[40:43], s73 offen lds\n"
    "s_add_u32 m0, s60, 45056\n"
    "s_mov_b32 s73, 262272\n"
    "buffer_load_dwordx4 v4, s[40:43], s73 offen lds\n"
    "s_add_u32 m0, s60, 49152\n"
    "s_mov_b32 s73, 262144\n"
    "buffer_load_dwordx4 v4, s[44:47], s73 offen lds\n"
    "s_add_u32 m0, s60, 53248\n"
    "s_mov_b32 s73, 262272\n"
    "buffer_load_dwordx4 v4, s[44:47], s73 offen lds\n"
    "s_add_u32 m0, s60, 57344\n"
    "s_mov_b32 s73, 262144\n"
    "buffer_load_dwordx4 v4, s[48:51], s73 offen lds\n"
    "s_add_u32 m0, s60, 61440\n"
    "s_mov_b32 s73, 262272\n"
    "buffer_load_dwordx4 v4, s[48:51], s73 offen lds\n"
    "s_add_u32 m0, s60, 65536\n"
    "s_mov_b32 s73, 524288\n"
    "buffer_load_dwordx4 v4, s[40:43], s73 offen lds\n"
    "s_add_u32 m0, s60, 69632\n"
    "s_mov_b32 s73, 524416\n"
    "buffer_load_dwordx4 v4, s[40:43], s73 offen lds\n"
    "s_add_u32 m0, s60, 73728\n"
    "s_mov_b32 s73, 524288\n"
    "buffer_load_dwordx4 v4, s[44:47], s73 offen lds\n"
    "s_add_u32 m0, s60, 77824\n"
    "s_mov_b32 s73, 524416\n"
    "buffer_load_dwordx4 v4, s[44:47], s73 offen lds\n"
    "s_add_u32 m0, s60, 81920\n"
    "s_mov_b32 s73, 524288\n"
    "buffer_load_dwordx4 v4, s[48:51], s73 offen lds\n"
    "s_add_u32 m0, s60, 86016\n"
    "s_mov_b32 s73, 524416\n"
    "buffer_load_dwordx4 v4, s[48:51], s73 offen lds\n"
    "s_add_u32 m0, s60, 90112\n"
    "s_mov_b32 s73, 786432\n"
    "buffer_load_dwordx4 v4, s[40:43], s73 offen lds\n"
    "s_add_u32 m0, s60, 94208\n"
    "s_mov_b32 s73, 786560\n"
    "buffer_load_dwordx4 v4, s[40:43], s73 offen lds\n"
    "s_add_u32 m0, s60, 98304\n"
    "s_mov_b32 s73, 786432\n"
    "buffer_load_dwordx4 v4, s[44:47], s73 offen lds\n"
    "s_add_u32 m0, s60, 102400\n"
    "s_mov_b32 s73, 786560\n"
    "buffer_load_dwordx4 v4, s[44:47], s73 offen lds\n"
    "s_add_u32 m0, s60, 106496\n"
    "s_mov_b32 s73, 786432\n"
    "buffer_load_dwordx4 v4, s[48:51], s73 offen lds\n"
    "s_add_u32 m0, s60, 110592\n"
    "s_mov_b32 s73, 786560\n"
    "buffer_load_dwordx4 v4, s[48:51], s73 offen lds\n"
    "s_add_u32 m0, s60, 114688\n"
    "s_mov_b32 s73, 1048576\n"
    "buffer_load_dwordx4 v4, s[40:43], s73 offen lds\n"
    "s_add_u32 m0, s60, 118784\n"
    "s_mov_b32 s73, 1048704\n"
    "buffer_load_dwordx4 v4, s[40:43], s73 offen lds\n"
    "s_add_u32 m0, s60, 122880\n"
    "s_mov_b32 s73, 1048576\n"
    "buffer_load_dwordx4 v4, s[44:47], s73 offen lds\n"
    "s_add_u32 m0, s60, 126976\n"
    "s_mov_b32 s73, 1048704\n"
    "buffer_load_dwordx4 v4, s[44:47], s73 offen lds\n"
    "s_add_u32 m0, s60, 131072\n"
    "s_mov_b32 s73, 1048576\n"
    "buffer_load_dwordx4 v4, s[48:51], s73 offen lds\n"
    "s_add_u32 m0, s60, 135168\n"
    "s_mov_b32 s73, 1048704\n"
    "buffer_load_dwordx4 v4, s[48:51], s73 offen lds\n"
    "s_add_u32 m0, s60, 139264\n"
    "s_mov_b32 s73, 1310720\n"
    "buffer_load_dwordx4 v4, s[40:43], s73 offen lds\n"
    "s_add_u32 m0, s60, 143360\n"
    "s_mov_b32 s73, 1310848\n"
    "buffer_load_dwordx4 v4, s[40:43], s73 offen lds\n"
    "s_add_u32 m0, s60, 147456\n"
    "s_mov_b32 s73, 1310720\n"
    "buffer_load_dwordx4 v4, s[44:47], s73 offen lds\n"
    "s_add_u32 m0, s60, 151552\n"
    "s_mov_b32 s73, 1310848\n"
    "buffer_load_dwordx4 v4, s[44:47], s73 offen lds\n"
    "s_add_u32 m0, s60, 155648\n"
    "s_mov_b32 s73, 1310720\n"
    "buffer_load_dwordx4 v4, s[48:51], s73 offen lds\n"
    "s_add_u32 m0, s60, 159744\n"
    "s_mov_b32 s73, 1310848\n"
    "buffer_load_dwordx4 v4, s[48:51], s73 offen lds\n"
    "s_waitcnt vmcnt(30) lgkmcnt(0)\n"
    "s_barrier\n"
    "v_add_u32 v8, 16384, v6\n"
    "ds_read_b64_tr_b16 a[0:1], v8 offset:0\n"
    "ds_read_b64_tr_b16 a[2:3], v8 offset:512\n"
    "ds_read_b64_tr_b16 a[8:9], v8 offset:8\n"
    "ds_read_b64_tr_b16 a[10:11], v8 offset:520\n"
    "ds_read_b64_tr_b16 a[144:145], v8 offset:8192\n"
    "ds_read_b64_tr_b16 a[146:147], v8 offset:8704\n"
    "ds_read_b64_tr_b16 a[152:153], v8 offset:8200\n"
    "ds_read_b64_tr_b16 a[154:155], v8 offset:8712\n"
    "ds_read_b64_tr_b16 v[112:113], v8 offset:16384\n"
    "ds_read_b64_tr_b16 v[114:115], v8 offset:16896\n"
    "ds_read_b64_tr_b16 v[224:225], v8 offset:16392\n"
    "ds_read_b64_tr_b16 v[226:227], v8 offset:16904\n"
    "s_waitcnt vmcnt(24) lgkmcnt(0)\n"
    "s_barrier\n"
    "s_add_u32 m0, s60, 16384\n"
    "s_mov_b32 s73, 1572864\n"
    "buffer_load_dwordx4 v4, s[40:43], s73 offen lds\n"
    "s_add_u32 m0, s60, 20480\n"
    "s_mov_b32 s73, 1572992\n"
    "buffer_load_dwordx4 v4, s[40:43], s73 offen lds\n"
    "s_add_u32 m0, s60, 24576\n"
    "s_mov_b32 s73, 1572864\n"
    "buffer_load_dwordx4 v4, s[44:47], s73 offen lds\n"
    "s_add_u32 m0, s60, 28672\n"
    "s_mov_b32 s73, 1572992\n"
    "buffer_load_dwordx4 v4, s[44:47], s73 offen lds\n"
    "s_add_u32 m0, s60, 32768\n"
    "s_mov_b32 s73, 1572864\n"
    "buffer_load_dwordx4 v4, s[48:51], s73 offen lds\n"
    "s_add_u32 m0, s60, 36864\n"
    "s_mov_b32 s73, 1572992\n"
    "buffer_load_dwordx4 v4, s[48:51], s73 offen lds\n"
    "v_add_u32 v8, 40960, v6\n"
    "ds_read_b64_tr_b16 a[4:5], v8 offset:0\n"
    "ds_read_b64_tr_b16 a[6:7], v8 offset:512\n"
    "ds_read_b64_tr_b16 a[12:13], v8 offset:8\n"
    "ds_read_b64_tr_b16 a[14:15], v8 offset:520\n"
    "ds_read_b64_tr_b16 a[148:149], v8 offset:8192\n"
    "ds_read_b64_tr_b16 a[150:151], v8 offset:8704\n"
    "ds_read_b64_tr_b16 a[156:157], v8 offset:8200\n"
    "ds_read_b64_tr_b16 a[158:159], v8 offset:8712\n"
    "ds_read_b64_tr_b16 v[116:117], v8 offset:16384\n"
    "ds_read_b64_tr_b16 v[118:119], v8 offset:16896\n"
    "ds_read_b64_tr_b16 v[228:229], v8 offset:16392\n"
    "ds_read_b64_tr_b16 v[230:231], v8 offset:16904\n"
    "s_waitcnt vmcnt(24) lgkmcnt(0)\n"
    "s_barrier\n"
    "s_add_u32 m0, s60, 40960\n"
    "s_mov_b32 s73, 1835008\n"
    "buffer_load_dwordx4 v4, s[40:43], s73 offen lds\n"
    "s_add_u32 m0, s60, 45056\n"
    "s_mov_b32 s73, 1835136\n"
    "buffer_load_dwordx4 v4, s[40:43], s73 offen lds\n"
    "s_add_u32 m0, s60, 49152\n"
    "s_mov_b32 s73, 1835008\n"
    "buffer_load_dwordx4 v4, s[44:47], s73 offen lds\n"
    "s_add_u32 m0, s60, 53248\n"
    "s_mov_b32 s73, 1835136\n"
    "buffer_load_dwordx4 v4, s[44:47], s73 offen lds\n"
    "s_add_u32 m0, s60, 57344\n"
    "s_mov_b32 s73, 1835008\n"
    "buffer_load_dwordx4 v4, s[48:51], s73 offen lds\n"
    "s_add_u32 m0, s60, 61440\n"
    "s_mov_b32 s73, 1835136\n"
    "buffer_load_dwordx4 v4, s[48:51], s73 offen lds\n"
    "v_add_u32 v8, 65536, v6\n"
    "ds_read_b64_tr_b16 a[16:17], v8 offset:0\n"
    "ds_read_b64_tr_b16 a[18:19], v8 offset:512\n"
    "ds_read_b64_tr_b16 a[24:25], v8 offset:8\n"
    "ds_read_b64_tr_b16 a[26:27], v8 offset:520\n"
    "ds_read_b64_tr_b16 a[160:161], v8 offset:8192\n"
    "ds_read_b64_tr_b16 a[162:163], v8 offset:8704\n"
    "ds_read_b64_tr_b16 a[168:169], v8 offset:8200\n"
    "ds_read_b64_tr_b16 a[170:171], v8 offset:8712\n"
    "ds_read_b64_tr_b16 v[120:121], v8 offset:16384\n"
    "ds_read_b64_tr_b16 v[122:123], v8 offset:16896\n"
    "ds_read_b64_tr_b16 v[232:233], v8 offset:16392\n"
    "ds_read_b64_tr_b16 v[234:235], v8 offset:16904\n"
    "s_waitcnt vmcnt(24) lgkmcnt(0)\n"
    "s_barrier\n"
    "s_add_u32 m0, s60, 65536\n"
    "s_mov_b32 s73, 2097152\n"
    "buffer_load_dwordx4 v4, s[40:43], s73 offen lds\n"
    "s_add_u32 m0, s60, 69632\n"
    "s_mov_b32 s73, 2097280\n"
    "buffer_load_dwordx4 v4, s[40:43], s73 offen lds\n"
    "s_add_u32 m0, s60, 73728\n"
    "s_mov_b32 s73, 2097152\n"
    "buffer_load_dwordx4 v4, s[44:47], s73 offen lds\n"
    "s_add_u32 m0, s60, 77824\n"
    "s_mov_b32 s73, 2097280\n"
    "buffer_load_dwordx4 v4, s[44:47], s73 offen lds\n"
    "s_add_u32 m0, s60, 81920\n"
    "s_mov_b32 s73, 2097152\n"
    "buffer_load_dwordx4 v4, s[48:51], s73 offen lds\n"
    "s_add_u32 m0, s60, 86016\n"
    "s_mov_b32 s73, 2097280\n"
    "buffer_load_dwordx4 v4, s[48:51], s73 offen lds\n"
    "v_add_u32 v8, 90112, v6\n"
    "ds_read_b64_tr_b16 a[20:21], v8 offset:0\n"
    "ds_read_b64_tr_b16 a[22:23], v8 offset:512\n"
    "ds_read_b64_tr_b16 a[28:29], v8 offset:8\n"
    "ds_read_b64_tr_b16 a[30:31], v8 offset:520\n"
    "ds_read_b64_tr_b16 a[164:165], v8 offset:8192\n"
    "ds_read_b64_tr_b16 a[166:167], v8 offset:8704\n"
    "ds_read_b64_tr_b16 a[172:173], v8 offset:8200\n"
    "ds_read_b64_tr_b16 a[174:175], v8 offset:8712\n"
    "ds_read_b64_tr_b16 v[124:125], v8 offset:16384\n"
    "ds_read_b64_tr_b16 v[126:127], v8 offset:16896\n"
    "ds_read_b64_tr_b16 v[236:237], v8 offset:16392\n"
    "ds_read_b64_tr_b16 v[238:239], v8 offset:16904\n"
    "s_waitcnt vmcnt(24) lgkmcnt(0)\n"
    "s_barrier\n"
    "s_add_u32 m0, s60, 90112\n"
    "s_mov_b32 s73, 2359296\n"
    "buffer_load_dwordx4 v4, s[40:43], s73 offen lds\n"
    "s_add_u32 m0, s60, 94208\n"
    "s_mov_b32 s73, 2359424\n"
    "buffer_load_dwordx4 v4, s[40:43], s73 offen lds\n"
    "s_add_u32 m0, s60, 98304\n"
    "s_mov_b32 s73, 2359296\n"
    "buffer_load_dwordx4 v4, s[44:47], s73 offen lds\n"
    "s_add_u32 m0, s60, 102400\n"
    "s_mov_b32 s73, 2359424\n"
    "buffer_load_dwordx4 v4, s[44:47], s73 offen lds\n"
    "s_add_u32 m0, s60, 106496\n"
    "s_mov_b32 s73, 2359296\n"
    "buffer_load_dwordx4 v4, s[48:51], s73 offen lds\n"
    "s_add_u32 m0, s60, 110592\n"
    "s_mov_b32 s73, 2359424\n"
    "buffer_load_dwordx4 v4, s[48:51], s73 offen lds\n"
    "v_add_u32 v8, 114688, v6\n"
    "ds_read_b64_tr_b16 a[32:33], v8 offset:0\n"
    "ds_read_b64_tr_b16 a[34:35], v8 offset:512\n"
    "ds_read_b64_tr_b16 a[40:41], v8 offset:8\n"
    "ds_read_b64_tr_b16 a[42:43], v8 offset:520\n"
    "ds_read_b64_tr_b16 a[176:177], v8 offset:8192\n"
    "ds_read_b64_tr_b16 a[178:179], v8 offset:8704\n"
    "ds_read_b64_tr_b16 a[184:185], v8 offset:8200\n"
    "ds_read_b64_tr_b16 a[186:187], v8 offset:8712\n"
    "ds_read_b64_tr_b16 v[128:129], v8 offset:16384\n"
    "ds_read_b64_tr_b16 v[130:131], v8 offset:16896\n"
    "ds_read_b64_tr_b16 v[240:241], v8 offset:16392\n"
    "ds_read_b64_tr_b16 v[242:243], v8 offset:16904\n"
    "s_waitcnt vmcnt(24) lgkmcnt(0)\n"
    "s_barrier\n"
    "s_add_u32 m0, s60, 114688\n"
    "s_mov_b32 s73, 2621440\n"
    "buffer_load_dwordx4 v4, s[40:43], s73 offen lds\n"
    "s_add_u32 m0, s60, 118784\n"
    "s_mov_b32 s73, 2621568\n"
    "buffer_load_dwordx4 v4, s[40:43], s73 offen lds\n"
    "s_add_u32 m0, s60, 122880\n"
    "s_mov_b32 s73, 2621440\n"
    "buffer_load_dwordx4 v4, s[44:47], s73 offen lds\n"
    "s_add_u32 m0, s60, 126976\n"
    "s_mov_b32 s73, 2621568\n"
    "buffer_load_dwordx4 v4, s[44:47], s73 offen lds\n"
    "s_add_u32 m0, s60, 131072\n"
    "s_mov_b32 s73, 2621440\n"
    "buffer_load_dwordx4 v4, s[48:51], s73 offen lds\n"
    "s_add_u32 m0, s60, 135168\n"
    "s_mov_b32 s73, 2621568\n"
    "buffer_load_dwordx4 v4, s[48:51], s73 offen lds\n"
    "v_add_u32 v8, 139264, v6\n"
    "ds_read_b64_tr_b16 a[36:37], v8 offset:0\n"
    "ds_read_b64_tr_b16 a[38:39], v8 offset:512\n"
    "ds_read_b64_tr_b16 a[44:45], v8 offset:8\n"
    "ds_read_b64_tr_b16 a[46:47], v8 offset:520\n"
    "ds_read_b64_tr_b16 a[180:181], v8 offset:8192\n"
    "ds_read_b64_tr_b16 a[182:183], v8 offset:8704\n"
    "ds_read_b64_tr_b16 a[188:189], v8 offset:8200\n"
    "ds_read_b64_tr_b16 a[190:191], v8 offset:8712\n"
    "ds_read_b64_tr_b16 v[132:133], v8 offset:16384\n"
    "ds_read_b64_tr_b16 v[134:135], v8 offset:16896\n"
    "ds_read_b64_tr_b16 v[244:245], v8 offset:16392\n"
    "ds_read_b64_tr_b16 v[246:247], v8 offset:16904\n"
    "s_waitcnt vmcnt(24) lgkmcnt(0)\n"
    "s_barrier\n"
    "s_add_u32 m0, s60, 139264\n"
    "s_mov_b32 s73, 2883584\n"
    "buffer_load_dwordx4 v4, s[40:43], s73 offen lds\n"
    "s_add_u32 m0, s60, 143360\n"
    "s_mov_b32 s73, 2883712\n"
    "buffer_load_dwordx4 v4, s[40:43], s73 offen lds\n"
    "s_add_u32 m0, s60, 147456\n"
    "s_mov_b32 s73, 2883584\n"
    "buffer_load_dwordx4 v4, s[44:47], s73 offen lds\n"
    "s_add_u32 m0, s60, 151552\n"
    "s_mov_b32 s73, 2883712\n"
    "buffer_load_dwordx4 v4, s[44:47], s73 offen lds\n"
    "s_add_u32 m0, s60, 155648\n"
    "s_mov_b32 s73, 2883584\n"
    "buffer_load_dwordx4 v4, s[48:51], s73 offen lds\n"
    "s_add_u32 m0, s60, 159744\n"
    "s_mov_b32 s73, 2883712\n"
    "buffer_load_dwordx4 v4, s[48:51], s73 offen lds\n"
    "v_add_u32 v8, 16384, v6\n"
    "ds_read_b64_tr_b16 a[48:49], v8 offset:0\n"
    "ds_read_b64_tr_b16 a[50:51], v8 offset:512\n"
    "ds_read_b64_tr_b16 a[56:57], v8 offset:8\n"
    "ds_read_b64_tr_b16 a[58:59], v8 offset:520\n"
    "ds_read_b64_tr_b16 a[192:193], v8 offset:8192\n"
    "ds_read_b64_tr_b16 a[194:195], v8 offset:8704\n"
    "ds_read_b64_tr_b16 a[200:201], v8 offset:8200\n"
    "ds_read_b64_tr_b16 a[202:203], v8 offset:8712\n"
    "ds_read_b64_tr_b16 v[136:137], v8 offset:16384\n"
    "ds_read_b64_tr_b16 v[138:139], v8 offset:16896\n"
    "ds_read_b64_tr_b16 v[248:249], v8 offset:16392\n"
    "ds_read_b64_tr_b16 v[250:251], v8 offset:16904\n"
    "s_waitcnt vmcnt(24) lgkmcnt(0)\n"
    "s_barrier\n"
    "s_add_u32 m0, s60, 16384\n"
    "s_mov_b32 s73, 3145728\n"
    "buffer_load_dwordx4 v4, s[40:43], s73 offen lds\n"
    "s_add_u32 m0, s60, 20480\n"
    "s_mov_b32 s73, 3145856\n"
    "buffer_load_dwordx4 v4, s[40:43], s73 offen lds\n"
    "s_add_u32 m0, s60, 24576\n"
    "s_mov_b32 s73, 3145728\n"
    "buffer_load_dwordx4 v4, s[44:47], s73 offen lds\n"
    "s_add_u32 m0, s60, 28672\n"
    "s_mov_b32 s73, 3145856\n"
    "buffer_load_dwordx4 v4, s[44:47], s73 offen lds\n"
    "s_add_u32 m0, s60, 32768\n"
    "s_mov_b32 s73, 3145728\n"
    "buffer_load_dwordx4 v4, s[48:51], s73 offen lds\n"
    "s_add_u32 m0, s60, 36864\n"
    "s_mov_b32 s73, 3145856\n"
    "buffer_load_dwordx4 v4, s[48:51], s73 offen lds\n"
    "v_add_u32 v8, 40960, v6\n"
    "ds_read_b64_tr_b16 a[52:53], v8 offset:0\n"
    "ds_read_b64_tr_b16 a[54:55], v8 offset:512\n"
    "ds_read_b64_tr_b16 a[60:61], v8 offset:8\n"
    "ds_read_b64_tr_b16 a[62:63], v8 offset:520\n"
    "ds_read_b64_tr_b16 a[196:197], v8 offset:8192\n"
    "ds_read_b64_tr_b16 a[198:199], v8 offset:8704\n"
    "ds_read_b64_tr_b16 a[204:205], v8 offset:8200\n"
    "ds_read_b64_tr_b16 a[206:207], v8 offset:8712\n"
    "ds_read_b64_tr_b16 v[140:141], v8 offset:16384\n"
    "ds_read_b64_tr_b16 v[142:143], v8 offset:16896\n"
    "ds_read_b64_tr_b16 v[252:253], v8 offset:16392\n"
    "ds_read_b64_tr_b16 v[254:255], v8 offset:16904\n"
    "s_waitcnt vmcnt(24) lgkmcnt(0)\n"
    "s_barrier\n"
    "s_add_u32 m0, s60, 40960\n"
    "s_mov_b32 s73, 3407872\n"
    "buffer_load_dwordx4 v4, s[40:43], s73 offen lds\n"
    "s_add_u32 m0, s60, 45056\n"
    "s_mov_b32 s73, 3408000\n"
    "buffer_load_dwordx4 v4, s[40:43], s73 offen lds\n"
    "s_add_u32 m0, s60, 49152\n"
    "s_mov_b32 s73, 3407872\n"
    "buffer_load_dwordx4 v4, s[44:47], s73 offen lds\n"
    "s_add_u32 m0, s60, 53248\n"
    "s_mov_b32 s73, 3408000\n"
    "buffer_load_dwordx4 v4, s[44:47], s73 offen lds\n"
    "s_add_u32 m0, s60, 57344\n"
    "s_mov_b32 s73, 3407872\n"
    "buffer_load_dwordx4 v4, s[48:51], s73 offen lds\n"
    "s_add_u32 m0, s60, 61440\n"
    "s_mov_b32 s73, 3408000\n"
    "buffer_load_dwordx4 v4, s[48:51], s73 offen lds\n"
    "v_add_u32 v8, 65536, v6\n"
    "ds_read_b64_tr_b16 a[64:65], v8 offset:0\n"
    "ds_read_b64_tr_b16 a[66:67], v8 offset:512\n"
    "ds_read_b64_tr_b16 a[72:73], v8 offset:8\n"
    "ds_read_b64_tr_b16 a[74:75], v8 offset:520\n"
    "ds_read_b64_tr_b16 a[208:209], v8 offset:8192\n"
    "ds_read_b64_tr_b16 a[210:211], v8 offset:8704\n"
    "ds_read_b64_tr_b16 a[216:217], v8 offset:8200\n"
    "ds_read_b64_tr_b16 a[218:219], v8 offset:8712\n"
    "ds_read_b64_tr_b16 v[144:145], v8 offset:16384\n"
    "ds_read_b64_tr_b16 v[146:147], v8 offset:16896\n"
    "ds_read_b64_tr_b16 v[208:209], v8 offset:16392\n"
    "ds_read_b64_tr_b16 v[210:211], v8 offset:16904\n"
    "s_waitcnt vmcnt(24) lgkmcnt(0)\n"
    "s_barrier\n"
    "s_add_u32 m0, s60, 65536\n"
    "s_mov_b32 s73, 3670016\n"
    "buffer_load_dwordx4 v4, s[40:43], s73 offen lds\n"
    "s_add_u32 m0, s60, 69632\n"
    "s_mov_b32 s73, 3670144\n"
    "buffer_load_dwordx4 v4, s[40:43], s73 offen lds\n"
    "s_add_u32 m0, s60, 73728\n"
    "s_mov_b32 s73, 3670016\n"
    "buffer_load_dwordx4 v4, s[44:47], s73 offen lds\n"
    "s_add_u32 m0, s60, 77824\n"
    "s_mov_b32 s73, 3670144\n"
    "buffer_load_dwordx4 v4, s[44:47], s73 offen lds\n"
    "s_add_u32 m0, s60, 81920\n"
    "s_mov_b32 s73, 3670016\n"
    "buffer_load_dwordx4 v4, s[48:51], s73 offen lds\n"
    "s_add_u32 m0, s60, 86016\n"
    "s_mov_b32 s73, 3670144\n"
    "buffer_load_dwordx4 v4, s[48:51], s73 offen lds\n"
    "v_add_u32 v8, 90112, v6\n"
    "ds_read_b64_tr_b16 a[68:69], v8 offset:0\n"
    "ds_read_b64_tr_b16 a[70:71], v8 offset:512\n"
    "ds_read_b64_tr_b16 a[76:77], v8 offset:8\n"
    "ds_read_b64_tr_b16 a[78:79], v8 offset:520\n"
    "ds_read_b64_tr_b16 a[212:213], v8 offset:8192\n"
    "ds_read_b64_tr_b16 a[214:215], v8 offset:8704\n"
    "ds_read_b64_tr_b16 a[220:221], v8 offset:8200\n"
    "ds_read_b64_tr_b16 a[222:223], v8 offset:8712\n"
    "ds_read_b64_tr_b16 v[148:149], v8 offset:16384\n"
    "ds_read_b64_tr_b16 v[150:151], v8 offset:16896\n"
    "ds_read_b64_tr_b16 v[212:213], v8 offset:16392\n"
    "ds_read_b64_tr_b16 v[214:215], v8 offset:16904\n"
    "s_waitcnt vmcnt(24) lgkmcnt(0)\n"
    "s_barrier\n"
    "s_add_u32 m0, s60, 90112\n"
    "s_mov_b32 s73, 3932160\n"
    "buffer_load_dwordx4 v4, s[40:43], s73 offen lds\n"
    "s_add_u32 m0, s60, 94208\n"
    "s_mov_b32 s73, 3932288\n"
    "buffer_load_dwordx4 v4, s[40:43], s73 offen lds\n"
    "s_add_u32 m0, s60, 98304\n"
    "s_mov_b32 s73, 3932160\n"
    "buffer_load_dwordx4 v4, s[44:47], s73 offen lds\n"
    "s_add_u32 m0, s60, 102400\n"
    "s_mov_b32 s73, 3932288\n"
    "buffer_load_dwordx4 v4, s[44:47], s73 offen lds\n"
    "s_add_u32 m0, s60, 106496\n"
    "s_mov_b32 s73, 3932160\n"
    "buffer_load_dwordx4 v4, s[48:51], s73 offen lds\n"
    "s_add_u32 m0, s60, 110592\n"
    "s_mov_b32 s73, 3932288\n"
    "buffer_load_dwordx4 v4, s[48:51], s73 offen lds\n"
    "v_add_u32 v8, 114688, v6\n"
    "ds_read_b64_tr_b16 a[80:81], v8 offset:0\n"
    "ds_read_b64_tr_b16 a[82:83], v8 offset:512\n"
    "ds_read_b64_tr_b16 a[88:89], v8 offset:8\n"
    "ds_read_b64_tr_b16 a[90:91], v8 offset:520\n"
    "ds_read_b64_tr_b16 a[224:225], v8 offset:8192\n"
    "ds_read_b64_tr_b16 a[226:227], v8 offset:8704\n"
    "ds_read_b64_tr_b16 a[232:233], v8 offset:8200\n"
    "ds_read_b64_tr_b16 a[234:235], v8 offset:8712\n"
    "ds_read_b64_tr_b16 v[152:153], v8 offset:16384\n"
    "ds_read_b64_tr_b16 v[154:155], v8 offset:16896\n"
    "ds_read_b64_tr_b16 v[216:217], v8 offset:16392\n"
    "ds_read_b64_tr_b16 v[218:219], v8 offset:16904\n"
    "s_waitcnt vmcnt(24) lgkmcnt(0)\n"
    "s_barrier\n"
    "s_add_u32 m0, s60, 114688\n"
    "s_mov_b32 s73, 4194304\n"
    "buffer_load_dwordx4 v4, s[40:43], s73 offen lds\n"
    "s_add_u32 m0, s60, 118784\n"
    "s_mov_b32 s73, 4194432\n"
    "buffer_load_dwordx4 v4, s[40:43], s73 offen lds\n"
    "s_add_u32 m0, s60, 122880\n"
    "s_mov_b32 s73, 4194304\n"
    "buffer_load_dwordx4 v4, s[44:47], s73 offen lds\n"
    "s_add_u32 m0, s60, 126976\n"
    "s_mov_b32 s73, 4194432\n"
    "buffer_load_dwordx4 v4, s[44:47], s73 offen lds\n"
    "s_add_u32 m0, s60, 131072\n"
    "s_mov_b32 s73, 4194304\n"
    "buffer_load_dwordx4 v4, s[48:51], s73 offen lds\n"
    "s_add_u32 m0, s60, 135168\n"
    "s_mov_b32 s73, 4194432\n"
    "buffer_load_dwordx4 v4, s[48:51], s73 offen lds\n"
    "v_add_u32 v8, 139264, v6\n"
    "ds_read_b64_tr_b16 a[84:85], v8 offset:0\n"
    "ds_read_b64_tr_b16 a[86:87], v8 offset:512\n"
    "ds_read_b64_tr_b16 a[92:93], v8 offset:8\n"
    "ds_read_b64_tr_b16 a[94:95], v8 offset:520\n"
    "ds_read_b64_tr_b16 a[228:229], v8 offset:8192\n"
    "ds_read_b64_tr_b16 a[230:231], v8 offset:8704\n"
    "ds_read_b64_tr_b16 a[236:237], v8 offset:8200\n"
    "ds_read_b64_tr_b16 a[238:239], v8 offset:8712\n"
    "ds_read_b64_tr_b16 v[156:157], v8 offset:16384\n"
    "ds_read_b64_tr_b16 v[158:159], v8 offset:16896\n"
    "ds_read_b64_tr_b16 v[220:221], v8 offset:16392\n"
    "ds_read_b64_tr_b16 v[222:223], v8 offset:16904\n"
    "s_waitcnt vmcnt(24) lgkmcnt(0)\n"
    "s_barrier\n"
    "s_add_u32 m0, s60, 139264\n"
    "s_mov_b32 s73, 4456448\n"
    "buffer_load_dwordx4 v4, s[40:43], s73 offen lds\n"
    "s_add_u32 m0, s60, 143360\n"
    "s_mov_b32 s73, 4456576\n"
    "buffer_load_dwordx4 v4, s[40:43], s73 offen lds\n"
    "s_add_u32 m0, s60, 147456\n"
    "s_mov_b32 s73, 4456448\n"
    "buffer_load_dwordx4 v4, s[44:47], s73 offen lds\n"
    "s_add_u32 m0, s60, 151552\n"
    "s_mov_b32 s73, 4456576\n"
    "buffer_load_dwordx4 v4, s[44:47], s73 offen lds\n"
    "s_add_u32 m0, s60, 155648\n"
    "s_mov_b32 s73, 4456448\n"
    "buffer_load_dwordx4 v4, s[48:51], s73 offen lds\n"
    "s_add_u32 m0, s60, 159744\n"
    "s_mov_b32 s73, 4456576\n"
    "buffer_load_dwordx4 v4, s[48:51], s73 offen lds\n"
    "v_add_u32 v8, 16384, v6\n"
    "ds_read_b64_tr_b16 a[96:97], v8 offset:0\n"
    "ds_read_b64_tr_b16 a[98:99], v8 offset:512\n"
    "ds_read_b64_tr_b16 a[104:105], v8 offset:8\n"
    "ds_read_b64_tr_b16 a[106:107], v8 offset:520\n"
    "ds_read_b64_tr_b16 a[240:241], v8 offset:8192\n"
    "ds_read_b64_tr_b16 a[242:243], v8 offset:8704\n"
    "ds_read_b64_tr_b16 a[248:249], v8 offset:8200\n"
    "ds_read_b64_tr_b16 a[250:251], v8 offset:8712\n"
    "ds_read_b64_tr_b16 v[160:161], v8 offset:16384\n"
    "ds_read_b64_tr_b16 v[162:163], v8 offset:16896\n"
    "ds_read_b64_tr_b16 v[168:169], v8 offset:16392\n"
    "ds_read_b64_tr_b16 v[170:171], v8 offset:16904\n"
    "s_waitcnt vmcnt(24) lgkmcnt(0)\n"
    "s_barrier\n"
    "v_add_u32 v8, 40960, v6\n"
    "ds_read_b64_tr_b16 a[100:101], v8 offset:0\n"
    "ds_read_b64_tr_b16 a[102:103], v8 offset:512\n"
    "ds_read_b64_tr_b16 a[108:109], v8 offset:8\n"
    "ds_read_b64_tr_b16 a[110:111], v8 offset:520\n"
    "ds_read_b64_tr_b16 a[244:245], v8 offset:8192\n"
    "ds_read_b64_tr_b16 a[246:247], v8 offset:8704\n"
    "ds_read_b64_tr_b16 a[252:253], v8 offset:8200\n"
    "ds_read_b64_tr_b16 a[254:255], v8 offset:8712\n"
    "ds_read_b64_tr_b16 v[164:165], v8 offset:16384\n"
    "ds_read_b64_tr_b16 v[166:167], v8 offset:16896\n"
    "ds_read_b64_tr_b16 v[172:173], v8 offset:16392\n"
    "ds_read_b64_tr_b16 v[174:175], v8 offset:16904\n"
    "s_waitcnt vmcnt(18) lgkmcnt(0)\n"
    "s_barrier\n"
    "s_add_u32 m0, s60, 16384\n"
    "s_mov_b32 s73, 74240\n"
    "buffer_load_dwordx4 v5, s[52:55], s73 offen lds\n"
    "s_add_u32 m0, s60, 20480\n"
    "s_mov_b32 s73, 74368\n"
    "buffer_load_dwordx4 v5, s[52:55], s73 offen lds\n"
    "s_add_u32 m0, s60, 24576\n"
    "s_mov_b32 s73, 74496\n"
    "buffer_load_dwordx4 v5, s[52:55], s73 offen lds\n"
    "s_add_u32 m0, s60, 28672\n"
    "s_mov_b32 s73, 74624\n"
    "buffer_load_dwordx4 v5, s[52:55], s73 offen lds\n"
    "s_add_u32 m0, s60, 32768\n"
    "s_mov_b32 s73, 111104\n"
    "buffer_load_dwordx4 v5, s[52:55], s73 offen lds\n"
    "s_add_u32 m0, s60, 36864\n"
    "s_mov_b32 s73, 111232\n"
    "buffer_load_dwordx4 v5, s[52:55], s73 offen lds\n"
    "s_add_u32 m0, s60, 40960\n"
    "s_mov_b32 s73, 111360\n"
    "buffer_load_dwordx4 v5, s[52:55], s73 offen lds\n"
    "s_add_u32 m0, s60, 45056\n"
    "s_mov_b32 s73, 111488\n"
    "buffer_load_dwordx4 v5, s[52:55], s73 offen lds\n"
    "v_add_u32 v8, 65536, v6\n"
    "ds_read_b64_tr_b16 a[112:113], v8 offset:0\n"
    "ds_read_b64_tr_b16 a[114:115], v8 offset:512\n"
    "ds_read_b64_tr_b16 a[120:121], v8 offset:8\n"
    "ds_read_b64_tr_b16 a[122:123], v8 offset:520\n"
    "ds_read_b64_tr_b16 v[80:81], v8 offset:8192\n"
    "ds_read_b64_tr_b16 v[82:83], v8 offset:8704\n"
    "ds_read_b64_tr_b16 v[88:89], v8 offset:8200\n"
    "ds_read_b64_tr_b16 v[90:91], v8 offset:8712\n"
    "ds_read_b64_tr_b16 v[176:177], v8 offset:16384\n"
    "ds_read_b64_tr_b16 v[178:179], v8 offset:16896\n"
    "ds_read_b64_tr_b16 v[184:185], v8 offset:16392\n"
    "ds_read_b64_tr_b16 v[186:187], v8 offset:16904\n"
    "s_waitcnt vmcnt(20) lgkmcnt(0)\n"
    "s_barrier\n"
    "s_add_u32 m0, s60, 49152\n"
    "s_mov_b32 s73, 73728\n"
    "buffer_load_dwordx4 v5, s[52:55], s73 offen lds\n"
    "s_add_u32 m0, s60, 53248\n"
    "s_mov_b32 s73, 73856\n"
    "buffer_load_dwordx4 v5, s[52:55], s73 offen lds\n"
    "s_add_u32 m0, s60, 57344\n"
    "s_mov_b32 s73, 73984\n"
    "buffer_load_dwordx4 v5, s[52:55], s73 offen lds\n"
    "s_add_u32 m0, s60, 61440\n"
    "s_mov_b32 s73, 74112\n"
    "buffer_load_dwordx4 v5, s[52:55], s73 offen lds\n"
    "s_add_u32 m0, s60, 65536\n"
    "s_mov_b32 s73, 110592\n"
    "buffer_load_dwordx4 v5, s[52:55], s73 offen lds\n"
    "s_add_u32 m0, s60, 69632\n"
    "s_mov_b32 s73, 110720\n"
    "buffer_load_dwordx4 v5, s[52:55], s73 offen lds\n"
    "s_add_u32 m0, s60, 73728\n"
    "s_mov_b32 s73, 110848\n"
    "buffer_load_dwordx4 v5, s[52:55], s73 offen lds\n"
    "s_add_u32 m0, s60, 77824\n"
    "s_mov_b32 s73, 110976\n"
    "buffer_load_dwordx4 v5, s[52:55], s73 offen lds\n"
    "v_add_u32 v8, 90112, v6\n"
    "ds_read_b64_tr_b16 a[116:117], v8 offset:0\n"
    "ds_read_b64_tr_b16 a[118:119], v8 offset:512\n"
    "ds_read_b64_tr_b16 a[124:125], v8 offset:8\n"
    "ds_read_b64_tr_b16 a[126:127], v8 offset:520\n"
    "ds_read_b64_tr_b16 v[84:85], v8 offset:8192\n"
    "ds_read_b64_tr_b16 v[86:87], v8 offset:8704\n"
    "ds_read_b64_tr_b16 v[92:93], v8 offset:8200\n"
    "ds_read_b64_tr_b16 v[94:95], v8 offset:8712\n"
    "ds_read_b64_tr_b16 v[180:181], v8 offset:16384\n"
    "ds_read_b64_tr_b16 v[182:183], v8 offset:16896\n"
    "ds_read_b64_tr_b16 v[188:189], v8 offset:16392\n"
    "ds_read_b64_tr_b16 v[190:191], v8 offset:16904\n"
    "s_waitcnt vmcnt(22) lgkmcnt(0)\n"
    "s_barrier\n"
    "s_add_u32 m0, s60, 81920\n"
    "s_mov_b32 s73, 0\n"
    "buffer_load_dwordx4 v5, s[52:55], s73 offen lds\n"
    "s_add_u32 m0, s60, 86016\n"
    "s_mov_b32 s73, 128\n"
    "buffer_load_dwordx4 v5, s[52:55], s73 offen lds\n"
    "s_add_u32 m0, s60, 90112\n"
    "s_mov_b32 s73, 256\n"
    "buffer_load_dwordx4 v5, s[52:55], s73 offen lds\n"
    "s_add_u32 m0, s60, 94208\n"
    "s_mov_b32 s73, 384\n"
    "buffer_load_dwordx4 v5, s[52:55], s73 offen lds\n"
    "s_add_u32 m0, s60, 98304\n"
    "s_mov_b32 s73, 36864\n"
    "buffer_load_dwordx4 v5, s[52:55], s73 offen lds\n"
    "s_add_u32 m0, s60, 102400\n"
    "s_mov_b32 s73, 36992\n"
    "buffer_load_dwordx4 v5, s[52:55], s73 offen lds\n"
    "s_add_u32 m0, s60, 106496\n"
    "s_mov_b32 s73, 37120\n"
    "buffer_load_dwordx4 v5, s[52:55], s73 offen lds\n"
    "s_add_u32 m0, s60, 110592\n"
    "s_mov_b32 s73, 37248\n"
    "buffer_load_dwordx4 v5, s[52:55], s73 offen lds\n"
    "v_add_u32 v8, 114688, v6\n"
    "ds_read_b64_tr_b16 a[128:129], v8 offset:0\n"
    "ds_read_b64_tr_b16 a[130:131], v8 offset:512\n"
    "ds_read_b64_tr_b16 a[136:137], v8 offset:8\n"
    "ds_read_b64_tr_b16 a[138:139], v8 offset:520\n"
    "ds_read_b64_tr_b16 v[96:97], v8 offset:8192\n"
    "ds_read_b64_tr_b16 v[98:99], v8 offset:8704\n"
    "ds_read_b64_tr_b16 v[104:105], v8 offset:8200\n"
    "ds_read_b64_tr_b16 v[106:107], v8 offset:8712\n"
    "ds_read_b64_tr_b16 v[192:193], v8 offset:16384\n"
    "ds_read_b64_tr_b16 v[194:195], v8 offset:16896\n"
    "ds_read_b64_tr_b16 v[200:201], v8 offset:16392\n"
    "ds_read_b64_tr_b16 v[202:203], v8 offset:16904\n"
    "s_waitcnt vmcnt(24) lgkmcnt(0)\n"
    "s_barrier\n"
    "v_add_u32 v8, 139264, v6\n"
    "ds_read_b64_tr_b16 a[132:133], v8 offset:0\n"
    "ds_read_b64_tr_b16 a[134:135], v8 offset:512\n"
    "ds_read_b64_tr_b16 a[140:141], v8 offset:8\n"
    "ds_read_b64_tr_b16 a[142:143], v8 offset:520\n"
    "ds_read_b64_tr_b16 v[100:101], v8 offset:8192\n"
    "ds_read_b64_tr_b16 v[102:103], v8 offset:8704\n"
    "ds_read_b64_tr_b16 v[108:109], v8 offset:8200\n"
    "ds_read_b64_tr_b16 v[110:111], v8 offset:8712\n"
    "ds_read_b64_tr_b16 v[196:197], v8 offset:16384\n"
    "ds_read_b64_tr_b16 v[198:199], v8 offset:16896\n"
    "ds_read_b64_tr_b16 v[204:205], v8 offset:16392\n"
    "ds_read_b64_tr_b16 v[206:207], v8 offset:16904\n"
    "s_waitcnt vmcnt(16) lgkmcnt(0)\n"
    "s_barrier\n"
    "s_add_u32 m0, s60, 114688\n"
    "s_mov_b32 s73, 512\n"
    "buffer_load_dwordx4 v5, s[52:55], s73 offen lds\n"
    "s_add_u32 m0, s60, 118784\n"
    "s_mov_b32 s73, 640\n"
    "buffer_load_dwordx4 v5, s[52:55], s73 offen lds\n"
    "s_add_u32 m0, s60, 122880\n"
    "s_mov_b32 s73, 768\n"
    "buffer_load_dwordx4 v5, s[52:55], s73 offen lds\n"
    "s_add_u32 m0, s60, 126976\n"
    "s_mov_b32 s73, 896\n"
    "buffer_load_dwordx4 v5, s[52:55], s73 offen lds\n"
    "s_add_u32 m0, s60, 131072\n"
    "s_mov_b32 s73, 37376\n"
    "buffer_load_dwordx4 v5, s[52:55], s73 offen lds\n"
    "s_add_u32 m0, s60, 135168\n"
    "s_mov_b32 s73, 37504\n"
    "buffer_load_dwordx4 v5, s[52:55], s73 offen lds\n"
    "s_add_u32 m0, s60, 139264\n"
    "s_mov_b32 s73, 37632\n"
    "buffer_load_dwordx4 v5, s[52:55], s73 offen lds\n"
    "s_add_u32 m0, s60, 143360\n"
    "s_mov_b32 s73, 37760\n"
    "buffer_load_dwordx4 v5, s[52:55], s73 offen lds\n"
    "s_add_u32 m0, s60, 147456\n"
    "s_mov_b32 s73, 1024\n"
    "buffer_load_dwordx4 v5, s[52:55], s73 offen lds\n"
    "s_add_u32 m0, s60, 151552\n"
    "s_mov_b32 s73, 37888\n"
    "buffer_load_dwordx4 v5, s[52:55], s73 offen lds\n"
    "s_add_u32 m0, s60, 155648\n"
    "s_mov_b32 s73, 74752\n"
    "buffer_load_dwordx4 v5, s[52:55], s73 offen lds\n"
    "s_add_u32 m0, s60, 159744\n"
    "s_mov_b32 s73, 111616\n"
    "buffer_load_dwordx4 v5, s[52:55], s73 offen lds\n"
    "buffer_load_dwordx2 v[12:13], off, s[36:39], 0 sc1\n"
    "v_add_u32 v8, 16384, v7\n"
    "v_xor_b32 v9, 32, v8\n"
    "v_xor_b32 v10, 320, v8\n"
    "v_xor_b32 v11, 32, v10\n"
    "ds_read_b64_tr_b16 v[16:17], v8 offset:0\n"
    "ds_read_b64_tr_b16 v[18:19], v8 offset:512\n"
    "ds_read_b64_tr_b16 v[20:21], v8 offset:16384\n"
    "ds_read_b64_tr_b16 v[22:23], v8 offset:16896\n"
    "ds_read_b64_tr_b16 v[24:25], v9 offset:0\n"
    "ds_read_b64_tr_b16 v[26:27], v9 offset:512\n"
    "ds_read_b64_tr_b16 v[28:29], v9 offset:16384\n"
    "ds_read_b64_tr_b16 v[30:31], v9 offset:16896\n"
    "s_waitcnt lgkmcnt(4)\n"
    "ds_read_b64_tr_b16 v[32:33], v10 offset:0\n"
    "ds_read_b64_tr_b16 v[34:35], v10 offset:512\n"
    "ds_read_b64_tr_b16 v[36:37], v10 offset:16384\n"
    "ds_read_b64_tr_b16 v[38:39], v10 offset:16896\n"
    "ds_read_b64_tr_b16 v[40:41], v11 offset:0\n"
    "ds_read_b64_tr_b16 v[42:43], v11 offset:512\n"
    "ds_read_b64_tr_b16 v[44:45], v11 offset:16384\n"
    "ds_read_b64_tr_b16 v[46:47], v11 offset:16896\n"
    "s_waitcnt vmcnt(21) lgkmcnt(0)\n"
    "s_barrier\n"
    "v_add_u32 v14, 0, v1\n"
    "ds_write_b128 v14, v[224:227] offset:0\n"
    "ds_write_b128 v14, v[228:231] offset:1024\n"
    "ds_write_b128 v14, v[232:235] offset:8192\n"
    "ds_write_b128 v14, v[236:239] offset:9216\n"
    "ds_write_b128 v14, v[240:243] offset:16384\n"
    "ds_write_b128 v14, v[244:247] offset:17408\n"
    "ds_write_b128 v14, v[248:251] offset:24576\n"
    "ds_write_b128 v14, v[252:255] offset:25600\n"
    "s_waitcnt lgkmcnt(4)\n"
    "v_add_u32 v8, 49152, v7\n"
    "v_xor_b32 v9, 32, v8\n"
    "v_xor_b32 v10, 320, v8\n"
    "v_xor_b32 v11, 32, v10\n"
    "ds_read_b64_tr_b16 v[48:49], v10 offset:0\n"
    "ds_read_b64_tr_b16 v[50:51], v10 offset:512\n"
    "ds_read_b64_tr_b16 v[52:53], v10 offset:16384\n"
    "ds_read_b64_tr_b16 v[54:55], v10 offset:16896\n"
    "ds_read_b64_tr_b16 v[56:57], v11 offset:0\n"
    "ds_read_b64_tr_b16 v[58:59], v11 offset:512\n"
    "ds_read_b64_tr_b16 v[60:61], v11 offset:16384\n"
    "ds_read_b64_tr_b16 v[62:63], v11 offset:16896\n"
    "s_waitcnt lgkmcnt(4)\n"
    "ds_read_b64_tr_b16 v[64:65], v8 offset:0\n"
    "ds_read_b64_tr_b16 v[66:67], v8 offset:512\n"
    "ds_read_b64_tr_b16 v[68:69], v8 offset:16384\n"
    "ds_read_b64_tr_b16 v[70:71], v8 offset:16896\n"
    "ds_read_b64_tr_b16 v[72:73], v9 offset:0\n"
    "ds_read_b64_tr_b16 v[74:75], v9 offset:512\n"
    "ds_read_b64_tr_b16 v[76:77], v9 offset:16384\n"
    "ds_read_b64_tr_b16 v[78:79], v9 offset:16896\n"
    "s_waitcnt vmcnt(13) lgkmcnt(0)\n"
    "s_barrier\n"
    "v_mov_b64 v[224:225], v[16:17]\n"
    "v_mov_b64 v[226:227], v[18:19]\n"
    "v_mov_b64 v[228:229], v[20:21]\n"
    "v_mov_b64 v[230:231], v[22:23]\n"
    "v_mov_b64 v[232:233], v[24:25]\n"
    "v_mov_b64 v[234:235], v[26:27]\n"
    "v_mov_b64 v[236:237], v[28:29]\n"
    "v_mov_b64 v[238:239], v[30:31]\n"
    "v_mov_b64 v[240:241], v[32:33]\n"
    "v_mov_b64 v[242:243], v[34:35]\n"
    "v_mov_b64 v[244:245], v[36:37]\n"
    "v_mov_b64 v[246:247], v[38:39]\n"
    "v_mov_b64 v[248:249], v[40:41]\n"
    "v_mov_b64 v[250:251], v[42:43]\n"
    "v_mov_b64 v[252:253], v[44:45]\n"
    "v_mov_b64 v[254:255], v[46:47]\n"
    "v_add_u32 v14, 32768, v1\n"
    "ds_write_b128 v14, v[208:211] offset:0\n"
    "ds_write_b128 v14, v[212:215] offset:1024\n"
    "ds_write_b128 v14, v[216:219] offset:8192\n"
    "ds_write_b128 v14, v[220:223] offset:9216\n"
    "ds_write_b128 v14, v[64:67] offset:16384\n"
    "ds_write_b128 v14, v[68:71] offset:17408\n"
    "ds_write_b128 v14, v[72:75] offset:24576\n"
    "ds_write_b128 v14, v[76:79] offset:25600\n"
    "s_waitcnt lgkmcnt(4)\n"
    "v_add_u32 v8, 81920, v7\n"
    "v_xor_b32 v9, 32, v8\n"
    "v_xor_b32 v10, 320, v8\n"
    "v_xor_b32 v11, 32, v10\n"
    "ds_read_b64_tr_b16 v[16:17], v8 offset:0\n"
    "ds_read_b64_tr_b16 v[18:19], v8 offset:512\n"
    "ds_read_b64_tr_b16 v[20:21], v8 offset:16384\n"
    "ds_read_b64_tr_b16 v[22:23], v8 offset:16896\n"
    "ds_read_b64_tr_b16 v[24:25], v9 offset:0\n"
    "ds_read_b64_tr_b16 v[26:27], v9 offset:512\n"
    "ds_read_b64_tr_b16 v[28:29], v9 offset:16384\n"
    "ds_read_b64_tr_b16 v[30:31], v9 offset:16896\n"
    "s_waitcnt lgkmcnt(4)\n"
    "ds_read_b64_tr_b16 v[32:33], v10 offset:0\n"
    "ds_read_b64_tr_b16 v[34:35], v10 offset:512\n"
    "ds_read_b64_tr_b16 v[36:37], v10 offset:16384\n"
    "ds_read_b64_tr_b16 v[38:39], v10 offset:16896\n"
    "ds_read_b64_tr_b16 v[40:41], v11 offset:0\n"
    "ds_read_b64_tr_b16 v[42:43], v11 offset:512\n"
    "ds_read_b64_tr_b16 v[44:45], v11 offset:16384\n"
    "ds_read_b64_tr_b16 v[46:47], v11 offset:16896\n"
    "s_waitcnt vmcnt(5) lgkmcnt(0)\n"
    "s_barrier\n"
    "v_mov_b64 v[208:209], v[48:49]\n"
    "v_mov_b64 v[210:211], v[50:51]\n"
    "v_mov_b64 v[212:213], v[52:53]\n"
    "v_mov_b64 v[214:215], v[54:55]\n"
    "v_mov_b64 v[216:217], v[56:57]\n"
    "v_mov_b64 v[218:219], v[58:59]\n"
    "v_mov_b64 v[220:221], v[60:61]\n"
    "v_mov_b64 v[222:223], v[62:63]\n"
    "v_add_u32 v14, 65536, v1\n"
    "ds_write_b128 v14, v[16:19] offset:0\n"
    "ds_write_b128 v14, v[20:23] offset:1024\n"
    "ds_write_b128 v14, v[24:27] offset:8192\n"
    "ds_write_b128 v14, v[28:31] offset:9216\n"
    "ds_write_b128 v14, v[32:35] offset:16384\n"
    "ds_write_b128 v14, v[36:39] offset:17408\n"
    "ds_write_b128 v14, v[40:43] offset:24576\n"
    "ds_write_b128 v14, v[44:47] offset:25600\n"
    "s_waitcnt lgkmcnt(4)\n"
    "v_add_u32 v8, 114688, v7\n"
    "v_xor_b32 v9, 32, v8\n"
    "v_xor_b32 v10, 320, v8\n"
    "v_xor_b32 v11, 32, v10\n"
    "ds_read_b64_tr_b16 v[48:49], v8 offset:0\n"
    "ds_read_b64_tr_b16 v[50:51], v8 offset:512\n"
    "ds_read_b64_tr_b16 v[52:53], v8 offset:16384\n"
    "ds_read_b64_tr_b16 v[54:55], v8 offset:16896\n"
    "ds_read_b64_tr_b16 v[56:57], v9 offset:0\n"
    "ds_read_b64_tr_b16 v[58:59], v9 offset:512\n"
    "ds_read_b64_tr_b16 v[60:61], v9 offset:16384\n"
    "ds_read_b64_tr_b16 v[62:63], v9 offset:16896\n"
    "s_waitcnt lgkmcnt(4)\n"
    "ds_read_b64_tr_b16 v[64:65], v10 offset:0\n"
    "ds_read_b64_tr_b16 v[66:67], v10 offset:512\n"
    "ds_read_b64_tr_b16 v[68:69], v10 offset:16384\n"
    "ds_read_b64_tr_b16 v[70:71], v10 offset:16896\n"
    "ds_read_b64_tr_b16 v[72:73], v11 offset:0\n"
    "ds_read_b64_tr_b16 v[74:75], v11 offset:512\n"
    "ds_read_b64_tr_b16 v[76:77], v11 offset:16384\n"
    "ds_read_b64_tr_b16 v[78:79], v11 offset:16896\n"
    "s_waitcnt vmcnt(0) lgkmcnt(0)\n"
    "s_barrier\n"
    ".Lrec_par_%=:\n"
    "v_readfirstlane_b32 s73, v12\n"
    "v_readfirstlane_b32 s74, v13\n"
    "s_xor_b32 s73, s73, s30\n"
    "s_xor_b32 s74, s74, s31\n"
    "s_cselect_b32 s74, 2, 0\n"
    "s_or_b32 s74, s74, s73\n"
    "s_cmp_lt_u32 s74, 2\n"
    "s_cbranch_scc1 .Lrec_par1_%=\n"
    "s_sleep 4\n"
    "buffer_load_dwordx2 v[12:13], off, s[36:39], 0 sc1\n"
    "s_waitcnt vmcnt(0)\n"
    "s_sub_u32 s69, s69, 1\n"
    "s_cbranch_scc0 .Lrec_par_%=\n"
    "s_mov_b32 s69, 0\n"
    ".Lrec_par1_%=:\n"
    "s_and_b32 s73, s73, 1\n"
    "s_lshl_b32 s73, s73, 13\n"
    "s_add_u32 s36, s36, s73\n"
    "s_addc_u32 s37, s37, 0\n"
    "v_lshlrev_b32 v44, 3, v0\n"
    "v_mov_b32 v45, 0x80000000\n"
    "v_cmp_gt_u32 vcc, 192, v0\n"
    "s_nop 1\n"
    "v_cndmask_b32 v44, v45, v44, vcc\n"
    "v_mov_b32 v46, -1\n"
    "buffer_load_dword v45, v44, s[36:39], s62 offen sc1\n"
    "s_mov_b64 exec, 1\n"
    "buffer_atomic_smax v46, off, s[36:39], s59 sc0 sc1\n"
    "s_mov_b64 exec, -1\n"
    "v_add_u32 v14, 98304, v1\n"
    "ds_write_b128 v14, v[48:51] offset:0\n"
    "ds_write_b128 v14, v[52:55] offset:1024\n"
    "ds_write_b128 v14, v[56:59] offset:8192\n"
    "ds_write_b128 v14, v[60:63] offset:9216\n"
    "ds_write_b128 v14, v[64:67] offset:16384\n"
    "ds_write_b128 v14, v[68:71] offset:17408\n"
    "ds_write_b128 v14, v[72:75] offset:24576\n"
    "ds_write_b128 v14, v[76:79] offset:25600\n"
    "s_waitcnt lgkmcnt(4)\n"
    "v_add_u32 v8, 147456, v3\n"
    "v_xor_b32 v9, 32, v8\n"
    "v_xor_b32 v10, 320, v8\n"
    "v_xor_b32 v11, 32, v10\n"
    "ds_read_b64_tr_b16 v[16:17], v8 offset:0\n"
    "ds_read_b64_tr_b16 v[18:19], v9 offset:1024\n"
    "ds_read_b64_tr_b16 v[20:21], v10 offset:0\n"
    "ds_read_b64_tr_b16 v[22:23], v11 offset:1024\n"
    "ds_read_b64_tr_b16 v[24:25], v8 offset:8192\n"
    "ds_read_b64_tr_b16 v[26:27], v9 offset:9216\n"
    "ds_read_b64_tr_b16 v[28:29], v10 offset:8192\n"
    "ds_read_b64_tr_b16 v[30:31], v11 offset:9216\n"
    "s_waitcnt lgkmcnt(4)\n"
    "s_waitcnt lgkmcnt(0)\n"
    "s_barrier\n"
    "v_add_u32 v14, 131072, v1\n"
    "ds_write_b128 v14, v[16:19] offset:0\n"
    "ds_write_b128 v14, v[20:23] offset:1024\n"
    "ds_write_b128 v14, v[24:27] offset:8192\n"
    "ds_write_b128 v14, v[28:31] offset:9216\n"
    "s_memtime s[32:33]\n"
    "s_memrealtime s[28:29]\n"
    "s_mov_b32 s84, 0x3e0293ee\n"
    "s_waitcnt vmcnt(0)\n"
    ".Lrec_init_%=:\n"
    "v_cmp_gt_i32 vcc, 0, v45\n"
    "s_cbranch_vccz .Lrec_init1_%=\n"
    "s_sleep 2\n"
    "buffer_load_dword v45, v44, s[36:39], s62 offen sc1\n"
    "s_waitcnt vmcnt(0)\n"
    "s_sub_u32 s69, s69, 1\n"
    "s_cbranch_scc0 .Lrec_init_%=\n"
    "s_mov_b32 s69, 0\n"
    ".Lrec_init1_%=:\n"
    "v_mov_b32 v3, v46\n"
    "s_mov_b32 s40, s76\n"
    "s_mov_b32 s41, s77\n"
    "s_mov_b32 s42, s78\n"
    "s_mov_b32 s43, s79\n"
    "s_mov_b32 s44, s80\n"
    "s_mov_b32 s45, s81\n"
    "s_mov_b32 s50, 0\n"
    "s_waitcnt lgkmcnt(0)\n"
    "s_mov_b32 s46, s34\n"
    "s_lshl_b32 s47, s92, 2\n"
    "s_mov_b32 s33, s32\n"
    "s_lshl_b32 s28, s28, 2\n"
    "s_mov_b32 s52, 1160\n"
    "s_and_b32 s35, s40, 1\n"
    "s_and_b32 s40, s40, -2\n"
    "s_mul_i32 s35, s35, 20\n"
    "s_add_u32 s34, s35, 38\n"
    "s_lshl_b32 s35, s35, 2\n"
    "s_mov_b32 s94, 40\n"
    "s_mov_b32 s68, -1\n"
    "s_add_u32 s68, s68, 1\n"
    "s_and_b32 s70, s68, 3\n"
    "s_lshl_b32 s70, s70, 17\n"
    "s_add_u32 s70, s70, s66\n"
    "s_add_u32 s71, s70, 4096\n"
    "s_add_u32 s72, s70, 16384\n"
    "s_sub_u32 s46, s33, s46\n"
    "s_sub_u32 s47, s28, s47\n"
    "v_cvt_f32_u32 v40, s46\n"
    "v_cvt_f32_u32 v41, s47\n"
    "v_rcp_f32 v41, v41\n"
    "s_mov_b32 s46, s33\n"
    "s_mov_b32 s47, s28\n"
    "v_mul_f32 v40, v40, v41\n"
    "v_mov_b32 v42, 0x40c80000\n"
    "v_med3_f32 v40, v40, 4.0, v42\n"
    "v_rcp_f32 v41, v40\n"
    "v_mul_f32 v40, 0x43800000, v40\n"
    "v_mul_f32 v41, 0x44b54000, v41\n"
    "v_cvt_u32_f32 v40, v40\n"
    "v_cvt_u32_f32 v41, v41\n"
    "v_readfirstlane_b32 s48, v40\n"
    "v_readfirstlane_b32 s49, v41\n"
    "v_mov_b64 v[40:41], 0\n"
    "v_mov_b64 v[42:43], 0\n"
    "v_mov_b64 v[44:45], 0\n"
    "v_mov_b64 v[46:47], 0\n"
    "v_mov_b64 v[48:49], 0\n"
    "v_mov_b64 v[50:51], 0\n"
    "v_mov_b64 v[52:53], 0\n"
    "v_mov_b64 v[54:55], 0\n"
    "v_mov_b64 v[56:57], 0\n"
    "v_mov_b64 v[58:59], 0\n"
    "v_mov_b64 v[60:61], 0\n"
    "v_mov_b64 v[62:63], 0\n"
    "s_waitcnt lgkmcnt(0)\n"
    "s_barrier\n"
    "s_branch .Lrec_entry_%=\n"
    ".Lrec_LOOPB_%=:\n"
    ".Lrec_top_%=:\n"
    "buffer_load_dwordx4 v[4:7], v0, s[36:39], s70 offen sc1\n"
    "s_mov_b32 s82, s50\n"
    "s_add_u32 s50, s50, 589824\n"
    "s_cmp_eq_u32 s50, 1769472\n"
    "s_cselect_b32 s50, 0, s50\n"
    "s_add_u32 s80, s68, 2\n"
    "s_and_b32 s80, s80, 3\n"
    "s_lshl_b32 s80, s80, 17\n"
    "buffer_load_dwordx4 v[8:11], v0, s[36:39], s70 offen offset:1024 sc1\n"
    "s_and_b32 s80, s80, s45\n"
    "s_and_b32 s91, s50, s44\n"
    "s_add_u32 s80, s80, s91\n"
    "s_add_u32 s80, s80, s43\n"
    "s_add_u32 s89, s42, s50\n"
    "s_lshr_b32 s90, s68, 2\n"
    "s_lshl_b32 s90, s90, 14\n"
    "buffer_load_dwordx4 v[12:15], v0, s[36:39], s70 offen offset:2048 sc1\n"
    "s_and_b32 s91, s68, 3\n"
    "s_lshl_b32 s91, s91, 11\n"
    "s_add_u32 s90, s90, s91\n"
    "s_add_u32 s90, s90, s40\n"
    "s_add_u32 s51, s90, s41\n"
    "s_and_b32 s76, s68, 7\n"
    "s_mul_i32 s76, s76, 4194304\n"
    "buffer_load_dwordx4 v[16:19], v0, s[36:39], s70 offen offset:3072 sc1\n"
    "s_add_u32 s75, s76, s56\n"
    "s_add_u32 s83, s75, 16384\n"
    "s_add_u32 s76, s76, s57\n"
    "s_add_u32 s77, s63, s82\n"
    "s_add_u32 s86, s77, 262144\n"
    "s_add_u32 s87, s67, s82\n"
    "s_add_u32 s91, s68, 1\n"
    "buffer_load_dwordx4 v[20:23], v0, s[36:39], s71 offen sc1\n"
    "s_and_b32 s79, s91, 3\n"
    "s_lshl_b32 s79, s79, 17\n"
    "s_add_u32 s79, s79, s65\n"
    "s_add_u32 s78, s64, s82\n"
    "s_add_u32 s88, s78, 4096\n"
    "s_nop 1\n"
    "buffer_load_dwordx4 v[24:27], v0, s[36:39], s71 offen offset:1024 sc1\n"
    "s_nop 6\n"
    "buffer_load_dwordx4 v[28:31], v0, s[36:39], s71 offen offset:2048 sc1\n"
    "s_nop 6\n"
    "buffer_load_dwordx4 v[32:35], v0, s[36:39], s71 offen offset:3072 sc1\n"
    "ds_read_b128 v[72:75], v1 offset:0\n"
    "ds_read_b128 v[76:79], v1 offset:1024\n"
    "s_waitcnt vmcnt(9)\n"
    "v_readfirstlane_b32 s81, v3\n"
    "s_lshl_b32 s73, s68, 1\n"
    "s_cmp_le_i32 s81, s73\n"
    "s_cbranch_scc1 .Lrec_waitp_%=\n"
    ".Lrec_haveP_%=:\n"
    "s_waitcnt vmcnt(7)\n"
    ".Lrec_start_%=:\n"
    "v_smfmac_f32_16x16x64_bf16 v[40:43], v[4:7], a[0:7], v2\n"
    "v_max3_u32 v3, v4, v5, v6\n"
    "v_smfmac_f32_16x16x64_bf16 v[44:47], v[4:7], a[8:15], v2\n"
    "v_max_u32 v3, v3, v7\n"
    "v_smfmac_f32_16x16x64_bf16 v[48:51], v[4:7], a[144:151], v2\n"
    "v_cmp_eq_u32 vcc, -1, v3\n"
    "v_smfmac_f32_16x16x64_bf16 v[52:55], v[4:7], a[152:159], v2\n"
    "buffer_load_dwordx4 v[36:39], v0, s[36:39], s72 offen sc1\n"
    "v_smfmac_f32_16x16x64_bf16 v[56:59], v[4:7], v[112:119], v2\n"
    "s_cbranch_vccnz .Lrec_retry0_%=\n"
    ".Lrec_back0_%=:\n"
    "s_waitcnt lgkmcnt(0)\n"
    "v_smfmac_f32_16x16x64_bf16 v[60:63], v[4:7], v[72:79], v2\n"
    "ds_read_b128 v[72:75], v1 offset:8192\n"
    "s_waitcnt vmcnt(7)\n"
    "v_smfmac_f32_16x16x64_bf16 v[40:43], v[8:11], a[16:23], v2\n"
    "ds_read_b128 v[76:79], v1 offset:9216\n"
    "v_max3_u32 v3, v3, v8, v9\n"
    "v_smfmac_f32_16x16x64_bf16 v[44:47], v[8:11], a[24:31], v2\n"
    "buffer_load_dwordx4 v[68:71], v0, s[36:39], s75 offen offset:1024 sc1\n"
    "v_smfmac_f32_16x16x64_bf16 v[48:51], v[8:11], a[160:167], v2\n"
    "v_max3_u32 v3, v3, v10, v11\n"
    "v_smfmac_f32_16x16x64_bf16 v[52:55], v[8:11], a[168:175], v2\n"
    "buffer_load_dwordx4 v[64:67], v0, s[36:39], s75 offen sc1\n"
    "v_smfmac_f32_16x16x64_bf16 v[56:59], v[8:11], v[120:127], v2\n"
    "s_waitcnt vmcnt(8) lgkmcnt(0)\n"
    "v_smfmac_f32_16x16x64_bf16 v[60:63], v[8:11], v[72:79], v2\n"
    "ds_read_b128 v[72:75], v1 offset:16384\n"
    "v_smfmac_f32_16x16x64_bf16 v[40:43], v[12:15], a[32:39], v2\n"
    "ds_read_b128 v[76:79], v1 offset:17408\n"
    "v_max3_u32 v3, v3, v12, v13\n"
    "v_smfmac_f32_16x16x64_bf16 v[44:47], v[12:15], a[40:47], v2\n"
    "v_max3_u32 v3, v3, v14, v15\n"
    "v_smfmac_f32_16x16x64_bf16 v[48:51], v[12:15], a[176:183], v2\n"
    "buffer_load_dwordx4 v[4:7], v0, s[36:39], s83 offen sc1\n"
    "v_smfmac_f32_16x16x64_bf16 v[52:55], v[12:15], a[184:191], v2\n"
    "buffer_load_dwordx4 v[8:11], v0, s[36:39], s83 offen offset:1024 sc1\n"
    "v_smfmac_f32_16x16x64_bf16 v[56:59], v[12:15], v[128:135], v2\n"
    "s_waitcnt vmcnt(9) lgkmcnt(0)\n"
    "v_max3_u32 v3, v3, v16, v17\n"
    "v_smfmac_f32_16x16x64_bf16 v[60:63], v[12:15], v[72:79], v2\n"
    "ds_read_b128 v[72:75], v1 offset:24576\n"
    "v_max3_u32 v3, v3, v18, v19\n"
    "v_smfmac_f32_16x16x64_bf16 v[40:43], v[16:19], a[48:55], v2\n"
    "ds_read_b128 v[76:79], v1 offset:25600\n"
    "v_cmp_eq_u32 vcc, -1, v3\n"
    "v_smfmac_f32_16x16x64_bf16 v[44:47], v[16:19], a[56:63], v2\n"
    "v_mov_b64 v[12:13], -1\n"
    "v_smfmac_f32_16x16x64_bf16 v[48:51], v[16:19], a[192:199], v2\n"
    "s_cbranch_vccnz .Lrec_retry1_%=\n"
    ".Lrec_back1_%=:\n"
    "v_mov_b64 v[14:15], -1\n"
    "v_smfmac_f32_16x16x64_bf16 v[52:55], v[16:19], a[200:207], v2\n"
    "s_waitcnt vmcnt(8) lgkmcnt(0)\n"
    "v_max3_u32 v3, v3, v20, v21\n"
    "v_smfmac_f32_16x16x64_bf16 v[60:63], v[16:19], v[72:79], v2\n"
    "ds_read_b128 v[72:75], v1 offset:32768\n"
    "v_max3_u32 v3, v3, v22, v23\n"
    "v_smfmac_f32_16x16x64_bf16 v[40:43], v[20:23], a[64:71], v2\n"
    "ds_read_b128 v[76:79], v1 offset:33792\n"
    "v_smfmac_f32_16x16x64_bf16 v[44:47], v[20:23], a[72:79], v2\n"
    "buffer_store_dwordx4 v[12:15], v0, s[36:39], s89 offen sc1\n"
    "v_smfmac_f32_16x16x64_bf16 v[48:51], v[20:23], a[208:215], v2\n"
    "v_smfmac_f32_16x16x64_bf16 v[52:55], v[20:23], a[216:223], v2\n"
    "s_waitcnt vmcnt(7) lgkmcnt(0)\n"
    "v_max3_u32 v3, v3, v24, v25\n"
    "v_smfmac_f32_16x16x64_bf16 v[60:63], v[20:23], v[72:79], v2\n"
    "ds_read_b128 v[72:75], v1 offset:40960\n"
    "v_max3_u32 v3, v3, v26, v27\n"
    "v_smfmac_f32_16x16x64_bf16 v[40:43], v[24:27], a[80:87], v2\n"
    "ds_read_b128 v[76:79], v1 offset:41984\n"
    "v_max3_u32 v3, v3, v28, v29\n"
    "v_smfmac_f32_16x16x64_bf16 v[44:47], v[24:27], a[88:95], v2\n"
    "v_max3_u32 v3, v3, v30, v31\n"
    "v_smfmac_f32_16x16x64_bf16 v[48:51], v[24:27], a[224:231], v2\n"
    "buffer_store_dwordx4 v[12:15], v0, s[36:39], s89 offen offset:1024 sc1\n"
    "v_smfmac_f32_16x16x64_bf16 v[52:55], v[24:27], a[232:239], v2\n"
    "s_waitcnt vmcnt(7) lgkmcnt(0)\n"
    "v_max3_u32 v3, v3, v32, v33\n"
    "v_smfmac_f32_16x16x64_bf16 v[60:63], v[24:27], v[72:79], v2\n"
    "ds_read_b128 v[72:75], v0 offset:13312\n"
    "v_max3_u32 v3, v3, v34, v35\n"
    "v_smfmac_f32_16x16x64_bf16 v[40:43], v[28:31], a[96:103], v2\n"
    "s_waitcnt vmcnt(6)\n"
    "buffer_store_dwordx4 v[12:15], v0, s[36:39], s80 offen sc1\n"
    "v_max3_u32 v3, v3, v36, v37\n"
    "v_smfmac_f32_16x16x64_bf16 v[44:47], v[28:31], a[104:111], v2\n"
    "ds_read_b128 v[12:15], v0 offset:12288\n"
    "v_max3_u32 v3, v3, v38, v39\n"
    "v_smfmac_f32_16x16x64_bf16 v[40:43], v[32:35], a[112:119], v2\n"
    "s_lshr_b32 s73, s60, 1\n"
    "v_cmp_eq_u32 vcc, -1, v3\n"
    "v_smfmac_f32_16x16x64_bf16 v[44:47], v[32:35], a[120:127], v2\n"
    "v_add_u32 v79, s60, v0\n"
    "v_smfmac_f32_16x16x64_bf16 v[48:51], v[28:31], a[240:247], v2\n"
    "s_cbranch_vccnz .Lrec_retry2_%=\n"
    ".Lrec_back2_%=:\n"
    "v_xor_b32 v76, 1024, v79\n"
    "v_smfmac_f32_16x16x64_bf16 v[40:43], v[36:39], a[128:135], v2\n"
    "s_waitcnt lgkmcnt(0)\n"
    "s_memrealtime s[28:29]\n"
    "v_xor_b32 v77, 2048, v79\n"
    "v_smfmac_f32_16x16x64_bf16 v[44:47], v[36:39], a[136:143], v2\n"
    "s_memrealtime s[30:31]\n"
    "v_xor_b32 v78, 3072, v79\n"
    "v_smfmac_f32_16x16x64_bf16 v[52:55], v[28:31], a[248:255], v2\n"
    "s_memtime s[32:33]\n"
    "v_cvt_pk_bf16_f32 v40, v40, v41\n"
    "v_smfmac_f32_16x16x64_bf16 v[48:51], v[32:35], v[80:87], v2\n"
    "s_memrealtime s[96:97]\n"
    "v_cvt_pk_bf16_f32 v41, v42, v43\n"
    "v_smfmac_f32_16x16x64_bf16 v[52:55], v[32:35], v[88:95], v2\n"
    "s_memrealtime s[98:99]\n"
    "v_cvt_pk_bf16_f32 v42, v44, v45\n"
    "v_smfmac_f32_16x16x64_bf16 v[48:51], v[36:39], v[96:103], v2\n"
    "v_cvt_pk_bf16_f32 v43, v46, v47\n"
    "v_smfmac_f32_16x16x64_bf16 v[52:55], v[36:39], v[104:111], v2\n"
    "v_add_u32 v1, 49152, v1\n"
    "v_mfma_f32_16x16x32_bf16 v[44:47], v[40:43], v[12:15], 0\n"
    "s_waitcnt lgkmcnt(0)\n"
    "v_cvt_pk_bf16_f32 v48, v48, v49\n"
    "v_cvt_pk_bf16_f32 v49, v50, v51\n"
    "v_lshrrev_b32 v3, 1, v79\n"
    "v_cvt_pk_bf16_f32 v50, v52, v53\n"
    "v_cvt_pk_bf16_f32 v51, v54, v55\n"
    "s_add_u32 s28, s28, s30\n"
    "s_add_u32 s96, s96, s98\n"
    "v_mfma_f32_16x16x32_bf16 v[52:55], v[48:51], v[12:15], 0\n"
    "s_add_u32 s28, s28, s96\n"
    "v_cvt_pk_bf16_f32 v40, v44, v45\n"
    "v_cvt_pk_bf16_f32 v41, v46, v47\n"
    "v_mov_b32 v44, s28\n"
    "v_mov_b32_dpp v42, v40 row_ror:8 row_mask:0xf bank_mask:0xf\n"
    "v_mov_b32_dpp v43, v41 row_ror:8 row_mask:0xf bank_mask:0xf\n"
    "v_add_u32 v44, s49, v44\n"
    "buffer_store_dword v44, off, s[36:39], s51 sc1\n"
    "s_waitcnt vmcnt(6)\n"
    "v_mfma_f32_16x16x32_bf16 v[44:47], v[68:71], v[40:43], 0\n"
    "v_cvt_pk_bf16_f32 v48, v52, v53\n"
    "v_cvt_pk_bf16_f32 v49, v54, v55\n"
    "v_mfma_f32_16x16x32_bf16 v[68:71], v[64:67], v[40:43], 0\n"
    "v_mov_b32_dpp v50, v48 row_ror:8 row_mask:0xf bank_mask:0xf\n"
    "v_mov_b32_dpp v51, v49 row_ror:8 row_mask:0xf bank_mask:0xf\n"
    "s_nop 1\n"
    "v_mfma_f32_16x16x32_bf16 v[52:55], v[48:51], v[40:43], 0\n"
    "s_nop 2\n"
    "v_mov_b32_dpp v68, v44 row_ror:8 row_mask:0xf bank_mask:0xc\n"
    "v_mov_b32_dpp v69, v45 row_ror:8 row_mask:0xf bank_mask:0xc\n"
    "v_mov_b32_dpp v70, v46 row_ror:8 row_mask:0xf bank_mask:0xc\n"
    "v_mov_b32_dpp v71, v47 row_ror:8 row_mask:0xf bank_mask:0xc\n"
    "ds_write_b128 v79, v[68:71] offset:0\n"
    "v_mov_b32_dpp v52, v54 row_ror:8 row_mask:0xf bank_mask:0xc\n"
    "v_mov_b32_dpp v53, v55 row_ror:8 row_mask:0xf bank_mask:0xc\n"
    "ds_write_b64 v3, v[52:53] offset:4096\n"
    "v_smfmac_f32_16x16x64_bf16 v[56:59], v[16:19], v[136:143], v2\n"
    "v_smfmac_f32_16x16x64_bf16 v[56:59], v[20:23], v[144:151], v2\n"
    "ds_read_b128 v[16:19], v1 offset:16384\n"
    "v_smfmac_f32_16x16x64_bf16 v[56:59], v[24:27], v[152:159], v2\n"
    "ds_read_b128 v[20:23], v1 offset:17408\n"
    "v_smfmac_f32_16x16x64_bf16 v[56:59], v[28:31], v[160:167], v2\n"
    "s_waitcnt lgkmcnt(2)\n"
    "s_barrier\n"
    "ds_read_b128 v[64:67], v76 offset:0\n"
    "v_smfmac_f32_16x16x64_bf16 v[60:63], v[28:31], v[168:175], v2\n"
    "ds_read_b128 v[40:43], v77 offset:0\n"
    "v_smfmac_f32_16x16x64_bf16 v[56:59], v[32:35], v[176:183], v2\n"
    "ds_read_b128 v[44:47], v78 offset:0\n"
    "v_smfmac_f32_16x16x64_bf16 v[60:63], v[32:35], v[184:191], v2\n"
    "ds_read_b64 v[12:13], v72 offset:4096\n"
    "ds_read_b64 v[14:15], v72 offset:4608\n"
    "v_smfmac_f32_16x16x64_bf16 v[56:59], v[36:39], v[192:199], v2\n"
    "ds_read_b64 v[52:53], v72 offset:5120\n"
    "ds_read_b64 v[54:55], v72 offset:5632\n"
    "v_smfmac_f32_16x16x64_bf16 v[60:63], v[36:39], v[200:207], v2\n"
    "buffer_store_dwordx4 v[48:51], v74, s[36:39], s76 offen sc1\n"
    "s_cmp_eq_u32 s68, 63\n"
    "s_waitcnt lgkmcnt(4)\n"
    "v_add_f32 v68, v68, v64\n"
    "v_add_f32 v69, v69, v65\n"
    "v_add_f32 v70, v70, v66\n"
    "v_add_f32 v71, v71, v67\n"
    "v_pk_add_f32 v[40:41], v[40:41], v[44:45]\n"
    "v_pk_add_f32 v[42:43], v[42:43], v[46:47]\n"
    "s_waitcnt lgkmcnt(0)\n"
    "v_pk_add_f32 v[12:13], v[12:13], v[14:15]\n"
    "v_pk_add_f32 v[52:53], v[52:53], v[54:55]\n"
    "v_pk_add_f32 v[68:69], v[68:69], v[40:41]\n"
    "v_pk_add_f32 v[70:71], v[70:71], v[42:43]\n"
    "v_pk_add_f32 v[12:13], v[12:13], v[52:53]\n"
    "v_max3_f32 v46, v68, v69, v70\n"
    "v_max3_f32 v47, v71, v12, v13\n"
    "v_max_f32 v3, v46, v47\n"
    "v_cvt_pk_bf16_f32 v56, v56, v57\n"
    "v_cvt_pk_bf16_f32 v57, v58, v59\n"
    "v_max_f32_dpp v46, v3, v3 row_ror:8 row_mask:0xf bank_mask:0xf\n"
    "v_max_f32_dpp v47, v3, v3 row_ror:8 row_mask:0xf bank_mask:0xf\n"
    "v_cvt_pk_bf16_f32 v58, v60, v61\n"
    "v_cvt_pk_bf16_f32 v59, v62, v63\n"
    "v_permlane16_swap_b32 v46, v47\n"
    "v_max_f32 v3, v46, v47\n"
    "v_max_f32 v46, v46, v47\n"
    "v_add_u32 v14, s73, v73\n"
    "v_xor_b32 v15, 128, v0\n"
    "v_permlane32_swap_b32 v3, v46\n"
    "v_max_f32 v3, v3, v46\n"
    "v_mul_f32 v46, 0xbe0293ee, v3\n"
    "v_pk_fma_f32 v[68:69], v[68:69], s[84:85], v[46:47] op_sel_hi:[1,0,0]\n"
    "v_pk_fma_f32 v[70:71], v[70:71], s[84:85], v[46:47] op_sel_hi:[1,0,0]\n"
    "v_pk_fma_f32 v[12:13], v[12:13], s[84:85], v[46:47] op_sel_hi:[1,0,0]\n"
    "v_exp_f32 v68, v68\n"
    "v_exp_f32 v69, v69\n"
    "v_exp_f32 v70, v70\n"
    "v_exp_f32 v71, v71\n"
    "v_exp_f32 v12, v12\n"
    "v_cvt_pk_bf16_f32 v40, v68, v69\n"
    "v_cvt_pk_bf16_f32 v41, v70, v71\n"
    "v_exp_f32 v13, v13\n"
    "v_mov_b32_dpp v42, v40 row_ror:8 row_mask:0xf bank_mask:0xf\n"
    "v_mov_b32_dpp v43, v41 row_ror:8 row_mask:0xf bank_mask:0xf\n"
    "v_cvt_pk_bf16_f32 v44, v12, v13\n"
    "v_pk_add_f32 v[68:69], v[68:69], v[70:71]\n"
    "s_waitcnt vmcnt(5)\n"
    "v_mfma_f32_16x16x32_bf16 v[48:51], v[4:7], v[40:43], 0\n"
    "ds_read_b128 v[28:31], v1 offset:1024\n"
    "v_mov_b32_dpp v45, v44 row_ror:8 row_mask:0xf bank_mask:0xf\n"
    "v_add_f32 v3, v12, v13\n"
    "v_mfma_f32_16x16x32_bf16 v[52:55], v[8:11], v[40:43], 0\n"
    "ds_read_b128 v[24:27], v1 offset:0\n"
    "v_add_f32 v46, v68, v69\n"
    "v_fma_f32 v3, v3, 0.5, v46\n"
    "v_mfma_f32_16x16x16_bf16 v[48:51], v[56:57], v[44:45], v[48:51]\n"
    "ds_read_b128 v[32:35], v1 offset:24576\n"
    "v_add_f32_dpp v46, v3, v3 row_ror:8 row_mask:0xf bank_mask:0xf\n"
    "v_add_f32_dpp v47, v3, v3 row_ror:8 row_mask:0xf bank_mask:0xf\n"
    "v_mfma_f32_16x16x16_bf16 v[52:55], v[58:59], v[44:45], v[52:55]\n"
    "buffer_load_dwordx2 v[12:13], v75, s[36:39], s90 offen sc1\n"
    "v_permlane16_swap_b32 v46, v47\n"
    "v_add_f32 v3, v46, v47\n"
    "v_add_f32 v46, v46, v47\n"
    "v_mov_b64 v[64:65], 0\n"
    "v_mov_b64 v[66:67], 0\n"
    "v_permlane32_swap_b32 v3, v46\n"
    "v_add_f32 v3, v3, v46\n"
    "v_rcp_f32 v46, v3\n"
    "v_mov_b32_dpp v48, v52 row_ror:8 row_mask:0xf bank_mask:0xc\n"
    "v_mov_b32_dpp v49, v53 row_ror:8 row_mask:0xf bank_mask:0xc\n"
    "v_mov_b32_dpp v50, v54 row_ror:8 row_mask:0xf bank_mask:0xc\n"
    "v_mov_b32_dpp v51, v55 row_ror:8 row_mask:0xf bank_mask:0xc\n"
    "v_pk_mul_f32 v[48:49], v[48:49], v[46:47] op_sel_hi:[1,0]\n"
    "v_pk_mul_f32 v[50:51], v[50:51], v[46:47] op_sel_hi:[1,0]\n"
    "v_cvt_pk_bf16_f32 v48, v48, v49\n"
    "v_cvt_pk_bf16_f32 v49, v50, v51\n"
    "ds_write_b64 v14, v[48:49] offset:8192\n"
    "s_cbranch_scc1 .Lrec_fin_%=\n"
    "ds_read_b128 v[36:39], v1 offset:25600\n"
    "ds_read_b128 v[72:75], v1 offset:8192\n"
    "ds_read_b128 v[76:79], v1 offset:9216\n"
    "ds_read_b128 v[4:7], v1 offset:32768\n"
    "ds_read_b128 v[8:11], v1 offset:33792\n"
    "v_mov_b64 v[68:69], 0\n"
    "v_mov_b64 v[70:71], 0\n"
    "s_waitcnt vmcnt(3) lgkmcnt(5)\n"
    "s_barrier\n"
    "ds_read_b128 v[40:43], v0 offset:8192\n"
    "ds_read_b128 v[44:47], v0 offset:9216\n"
    "ds_read_b128 v[48:51], v15 offset:8192\n"
    "ds_read_b128 v[52:55], v15 offset:9216\n"
    "buffer_store_dwordx4 v[56:59], v0, s[36:39], s76 offen offset:512 sc1\n"
    "s_waitcnt lgkmcnt(3)\n"
    "v_smfmac_f32_16x16x64_bf16 v[64:67], v[40:43], v[16:23], v2\n"
    "ds_read_b128 v[16:19], v1 offset:40960\n"
    "s_waitcnt lgkmcnt(3)\n"
    "v_smfmac_f32_16x16x64_bf16 v[64:67], v[44:47], v[24:31], v2\n"
    "ds_read_b128 v[20:23], v1 offset:41984\n"
    "v_smfmac_f32_16x16x64_bf16 v[68:71], v[40:43], v[32:39], v2\n"
    "ds_read_b128 v[24:27], v1 offset:49152\n"
    "v_mov_b64 v[56:57], 0\n"
    "v_smfmac_f32_16x16x64_bf16 v[68:71], v[44:47], v[72:79], v2\n"
    "ds_read_b128 v[28:31], v1 offset:50176\n"
    "v_mov_b64 v[58:59], 0\n"
    "s_waitcnt lgkmcnt(5)\n"
    "v_smfmac_f32_16x16x64_bf16 v[64:67], v[48:51], v[4:11], v2\n"
    "ds_read_b128 v[32:35], v1 offset:57344\n"
    "v_mov_b64 v[60:61], 0\n"
    "s_waitcnt lgkmcnt(5)\n"
    "v_smfmac_f32_16x16x64_bf16 v[64:67], v[52:55], v[208:215], v2\n"
    "ds_read_b128 v[36:39], v1 offset:58368\n"
    "ds_read_b128 v[8:11], v0 offset:14336\n"
    "s_waitcnt lgkmcnt(5)\n"
    "v_smfmac_f32_16x16x64_bf16 v[68:71], v[48:51], v[16:23], v2\n"
    "v_add_u32 v1, 32768, v1\n"
    "ds_read_b128 v[72:75], v1 offset:32768\n"
    "v_mov_b64 v[62:63], 0\n"
    "v_smfmac_f32_16x16x64_bf16 v[68:71], v[52:55], v[216:223], v2\n"
    "ds_read_b128 v[76:79], v1 offset:33792\n"
    "v_cvt_pk_bf16_f32 v64, v64, v65\n"
    "s_waitcnt lgkmcnt(5)\n"
    "v_smfmac_f32_16x16x64_bf16 v[56:59], v[40:43], v[24:31], v2\n"
    "ds_read_b128 v[16:19], v1 offset:40960\n"
    "v_cvt_pk_bf16_f32 v65, v66, v67\n"
    "v_smfmac_f32_16x16x64_bf16 v[56:59], v[44:47], v[224:231], v2\n"
    "ds_read_b128 v[20:23], v1 offset:41984\n"
    "v_cvt_pk_bf16_f32 v66, v68, v69\n"
    "v_cvt_pk_bf16_f32 v67, v70, v71\n"
    "s_waitcnt lgkmcnt(5)\n"
    "v_smfmac_f32_16x16x64_bf16 v[60:63], v[40:43], v[32:39], v2\n"
    "ds_read_b128 v[24:27], v1 offset:49152\n"
    "v_permlane16_swap_b32 v64, v66\n"
    "v_smfmac_f32_16x16x64_bf16 v[60:63], v[44:47], v[232:239], v2\n"
    "ds_read_b128 v[28:31], v1 offset:50176\n"
    "v_permlane16_swap_b32 v65, v67\n"
    "s_waitcnt lgkmcnt(4)\n"
    "v_smfmac_f32_16x16x64_bf16 v[56:59], v[48:51], v[72:79], v2\n"
    "ds_read_b128 v[32:35], v1 offset:57344\n"
    "buffer_store_dwordx4 v[64:67], v8, s[36:39], s77 offen sc1\n"
    "v_smfmac_f32_16x16x64_bf16 v[56:59], v[52:55], v[240:247], v2\n"
    "ds_read_b128 v[36:39], v1 offset:58368\n"
    "s_waitcnt lgkmcnt(4)\n"
    "v_smfmac_f32_16x16x64_bf16 v[60:63], v[48:51], v[16:23], v2\n"
    "v_smfmac_f32_16x16x64_bf16 v[60:63], v[52:55], v[248:255], v2\n"
    "v_cvt_pk_bf16_f32 v56, v56, v57\n"
    "s_waitcnt lgkmcnt(2)\n"
    "v_mfma_f32_16x16x32_bf16 v[4:7], v[40:43], v[24:27], 0\n"
    "v_cvt_pk_bf16_f32 v57, v58, v59\n"
    "v_mfma_f32_16x16x32_bf16 v[4:7], v[48:51], v[28:31], v[4:7]\n"
    "v_cvt_pk_bf16_f32 v58, v60, v61\n"
    "v_cvt_pk_bf16_f32 v59, v62, v63\n"
    "s_waitcnt lgkmcnt(0)\n"
    "v_mfma_f32_16x16x32_bf16 v[4:7], v[44:47], v[32:35], v[4:7]\n"
    "v_permlane16_swap_b32 v56, v58\n"
    "v_mfma_f32_16x16x32_bf16 v[4:7], v[52:55], v[36:39], v[4:7]\n"
    "v_permlane16_swap_b32 v57, v59\n"
    "buffer_store_dwordx4 v[56:59], v8, s[36:39], s86 offen sc1\n"
    "s_waitcnt vmcnt(3)\n"
    "v_max_u32 v12, v12, v13\n"
    "s_mov_b32 s33, s32\n"
    "s_nop 0\n"
    "v_max_u32_dpp v12, v12, v12 row_ror:8 row_mask:0xf bank_mask:0xf\n"
    "s_nop 1\n"
    "v_max_u32_dpp v12, v12, v12 row_ror:4 row_mask:0xf bank_mask:0xf\n"
    "v_cvt_pk_bf16_f32 v4, v4, v5\n"
    "v_cvt_pk_bf16_f32 v5, v6, v7\n"
    "v_max_u32_dpp v12, v12, v12 row_ror:2 row_mask:0xf bank_mask:0xf\n"
    "s_nop 0\n"
    "v_permlane16_swap_b32 v4, v6\n"
    "v_max_u32_dpp v12, v12, v12 row_ror:1 row_mask:0xf bank_mask:0xf\n"
    "v_permlane16_swap_b32 v5, v7\n"
    "buffer_store_dwordx4 v[4:7], v9, s[36:39], s87 offen sc1\n"
    "v_readlane_b32 s95, v12, 0\n"
    "v_readlane_b32 s99, v12, 16\n"
    "v_readlane_b32 s30, v12, 32\n"
    "v_readlane_b32 s31, v12, 48\n"
    "s_max_u32 s95, s95, s99\n"
    "s_max_u32 s30, s30, s31\n"
    "s_max_u32 s95, s95, s30\n"
    "s_cmp_eq_u32 s95, -1\n"
    "s_cbranch_scc1 .Lrec_tsretry_%=\n"
    ".Lrec_tsgo_%=:\n"
    "s_memtime s[96:97]\n"
    "v_mov_b32 v76, v10\n"
    "s_sub_u32 s95, s95, s28\n"
    "s_min_u32 s95, s95, 4000\n"
    "s_add_u32 s95, s95, s34\n"
    "s_add_u32 s95, s95, s94\n"
    "s_mul_i32 s95, s95, s48\n"
    "s_lshr_b32 s95, s95, 8\n"
    "s_add_u32 s32, s32, s95\n"
    "s_waitcnt lgkmcnt(0)\n"
    "s_sub_u32 s95, s32, s96\n"
    "s_mul_i32 s95, s95, 43\n"
    "s_ashr_i32 s95, s95, 10\n"
    "s_max_i32 s95, s95, 0\n"
    "s_min_i32 s95, s95, 100\n"
    ".Lrec_tsloop_%=:\n"
    "s_sub_u32 s95, s95, 1\n"
    "s_cbranch_scc0 .Lrec_tsloop_%=\n"
    "s_cmp_lg_u32 s60, 0\n"
    "s_cbranch_scc1 .Lrec_nored_%=\n"
    ".Lrec_red_%=:\n"
    "buffer_load_dwordx4 v[4:7], v0, s[36:39], s78 offen sc1\n"
    "buffer_load_dwordx4 v[8:11], v0, s[36:39], s78 offen offset:1024 sc1\n"
    "buffer_load_dwordx4 v[12:15], v0, s[36:39], s78 offen offset:2048 sc1\n"
    "buffer_load_dwordx4 v[16:19], v0, s[36:39], s78 offen offset:3072 sc1\n"
    "buffer_load_dwordx4 v[20:23], v0, s[36:39], s88 offen sc1\n"
    "buffer_load_dwordx4 v[24:27], v0, s[36:39], s88 offen offset:1024 sc1\n"
    "buffer_load_dwordx4 v[28:31], v0, s[36:39], s88 offen offset:2048 sc1\n"
    "buffer_load_dwordx4 v[32:35], v0, s[36:39], s88 offen offset:3072 sc1\n"
    "ds_read_b128 v[72:75], v0 offset:15360\n"
    "v_lshl_or_b32 v77, s68, 1, 1\n"
    "s_waitcnt vmcnt(11)\n"
    "s_mov_b64 exec, 1\n"
    "buffer_atomic_smax v77, off, s[36:39], s58 sc1\n"
    "s_mov_b64 exec, -1\n"
    "v_mov_b32 v3, -1\n"
    "v_subrev_u32 v1, 81920, v1\n"
    "s_sub_u32 s52, s52, 24\n"
    "s_max_u32 s52, s52, 860\n"
    "s_sub_u32 s94, s52, 860\n"
    "s_lshr_b32 s94, s94, 3\n"
    "s_sub_u32 s95, s52, s35\n"
    "s_mul_i32 s95, s48, s95\n"
    "s_lshr_b32 s95, s95, 10\n"
    "s_add_u32 s32, s32, s95\n"
    "s_add_u32 s68, s68, 1\n"
    "s_and_b32 s70, s68, 3\n"
    "s_lshl_b32 s70, s70, 17\n"
    "s_add_u32 s70, s70, s66\n"
    "s_add_u32 s71, s70, 4096\n"
    "s_add_u32 s72, s70, 16384\n"
    "s_sub_u32 s46, s33, s46\n"
    "s_sub_u32 s47, s28, s47\n"
    "v_cvt_f32_u32 v40, s46\n"
    "v_cvt_f32_u32 v41, s47\n"
    "v_rcp_f32 v41, v41\n"
    "s_mov_b32 s46, s33\n"
    "s_mov_b32 s47, s28\n"
    "v_mul_f32 v40, v40, v41\n"
    "v_mov_b32 v42, 0x40c80000\n"
    "v_med3_f32 v40, v40, 4.0, v42\n"
    "v_rcp_f32 v41, v40\n"
    "v_mul_f32 v40, 0x43800000, v40\n"
    "v_mul_f32 v41, 0x44b54000, v41\n"
    "v_cvt_u32_f32 v40, v40\n"
    "v_cvt_u32_f32 v41, v41\n"
    "v_readfirstlane_b32 s48, v40\n"
    "v_readfirstlane_b32 s49, v41\n"
    "v_mov_b64 v[40:41], 0\n"
    "v_mov_b64 v[42:43], 0\n"
    "v_mov_b64 v[44:45], 0\n"
    "v_mov_b64 v[46:47], 0\n"
    "v_mov_b64 v[48:49], 0\n"
    "v_mov_b64 v[50:51], 0\n"
    "v_mov_b64 v[52:53], 0\n"
    "v_mov_b64 v[54:55], 0\n"
    "v_mov_b64 v[56:57], 0\n"
    "v_mov_b64 v[58:59], 0\n"
    "v_mov_b64 v[60:61], 0\n"
    "v_mov_b64 v[62:63], 0\n"
    "s_mov_b64 exec, 1\n"
    "buffer_atomic_smax v3, off, s[36:39], s59 sc0 sc1\n"
    "s_mov_b64 exec, -1\n"
    "s_waitcnt lgkmcnt(0)\n"
    ".Lrec_rsum_%=:\n"
    "s_waitcnt vmcnt(9)\n"
    "v_mfma_f32_16x16x32_bf16 v[36:39], v[4:7], v[72:75], 0\n"
    "v_max3_u32 v78, v4, v5, v6\n"
    "v_max_u32 v78, v78, v7\n"
    "s_waitcnt vmcnt(8)\n"
    "v_mfma_f32_16x16x32_bf16 v[36:39], v[8:11], v[72:75], v[36:39]\n"
    "v_max3_u32 v78, v78, v8, v9\n"
    "v_max3_u32 v78, v78, v10, v11\n"
    "s_waitcnt vmcnt(7)\n"
    "v_mfma_f32_16x16x32_bf16 v[36:39], v[12:15], v[72:75], v[36:39]\n"
    "v_max3_u32 v78, v78, v12, v13\n"
    "v_max3_u32 v78, v78, v14, v15\n"
    "s_waitcnt vmcnt(6)\n"
    "v_mfma_f32_16x16x32_bf16 v[36:39], v[16:19], v[72:75], v[36:39]\n"
    "v_max3_u32 v78, v78, v16, v17\n"
    "v_max3_u32 v78, v78, v18, v19\n"
    "s_waitcnt vmcnt(5)\n"
    "v_mfma_f32_16x16x32_bf16 v[36:39], v[20:23], v[72:75], v[36:39]\n"
    "v_max3_u32 v78, v78, v20, v21\n"
    "v_max3_u32 v78, v78, v22, v23\n"
    "s_waitcnt vmcnt(4)\n"
    "v_mfma_f32_16x16x32_bf16 v[36:39], v[24:27], v[72:75], v[36:39]\n"
    "v_max3_u32 v78, v78, v24, v25\n"
    "v_max3_u32 v78, v78, v26, v27\n"
    "s_waitcnt vmcnt(3)\n"
    "v_mfma_f32_16x16x32_bf16 v[36:39], v[28:31], v[72:75], v[36:39]\n"
    "v_max3_u32 v78, v78, v28, v29\n"
    "v_max3_u32 v78, v78, v30, v31\n"
    "s_waitcnt vmcnt(2)\n"
    "v_mfma_f32_16x16x32_bf16 v[36:39], v[32:35], v[72:75], v[36:39]\n"
    "v_max3_u32 v78, v78, v32, v33\n"
    "v_max3_u32 v78, v78, v34, v35\n"
    "v_cmp_eq_u32 vcc, -1, v78\n"
    "s_nop 4\n"
    "v_cvt_pk_bf16_f32 v4, v36, v37\n"
    "v_cvt_pk_bf16_f32 v6, v36, v37\n"
    "v_cvt_pk_bf16_f32 v5, v38, v39\n"
    "v_cvt_pk_bf16_f32 v7, v38, v39\n"
    "s_cbranch_vccnz .Lrec_rretry_%=\n"
    ".Lrec_rgo_%=:\n"
    "s_memtime s[96:97]\n"
    "v_permlane16_swap_b32 v4, v6\n"
    "v_permlane16_swap_b32 v5, v7\n"
    "buffer_store_dwordx4 v[4:7], v76, s[36:39], s79 offen sc1\n"
    ".Lrec_b3_%=:\n"
    "s_waitcnt lgkmcnt(0)\n"
    "s_sub_u32 s95, s32, s96\n"
    "s_mul_i32 s95, s95, 43\n"
    "s_ashr_i32 s95, s95, 10\n"
    "s_max_i32 s95, s95, 0\n"
    "s_min_i32 s95, s95, 100\n"
    ".Lrec_gloop_%=:\n"
    "s_sub_u32 s95, s95, 1\n"
    "s_cbranch_scc0 .Lrec_gloop_%=\n"
    "s_branch .Lrec_top_%=\n"
    ".Lrec_LOOPE_%=:\n"
    ".Lrec_fin_%=:\n"
    "buffer_store_dwordx4 v[56:59], v0, s[36:39], s76 offen offset:512 sc1\n"
    "s_waitcnt vmcnt(0) lgkmcnt(0)\n"
    "v_lshl_or_b32 v46, s68, 1, 1\n"
    "s_mov_b64 exec, 1\n"
    "buffer_atomic_smax v46, off, s[36:39], s58 sc1\n"
    "s_mov_b64 exec, -1\n"
    "s_waitcnt vmcnt(0)\n"
    "s_branch .Lrec_FIN_%=\n"
    ".Lrec_again_%=:\n"
    "buffer_load_dwordx4 v[4:7], v0, s[36:39], s70 offen sc1\n"
    "buffer_load_dwordx4 v[8:11], v0, s[36:39], s70 offen offset:1024 sc1\n"
    "buffer_load_dwordx4 v[12:15], v0, s[36:39], s70 offen offset:2048 sc1\n"
    "buffer_load_dwordx4 v[16:19], v0, s[36:39], s70 offen offset:3072 sc1\n"
    "buffer_load_dwordx4 v[20:23], v0, s[36:39], s71 offen sc1\n"
    "buffer_load_dwordx4 v[24:27], v0, s[36:39], s71 offen offset:1024 sc1\n"
    "buffer_load_dwordx4 v[28:31], v0, s[36:39], s71 offen offset:2048 sc1\n"
    "buffer_load_dwordx4 v[32:35], v0, s[36:39], s71 offen offset:3072 sc1\n"
    "buffer_load_dwordx4 v[36:39], v0, s[36:39], s72 offen sc1\n"
    ".Lrec_look_%=:\n"
    "s_nop 7\n"
    "v_mov_b64 v[40:41], 0\n"
    "v_mov_b64 v[42:43], 0\n"
    "v_mov_b64 v[44:45], 0\n"
    "v_mov_b64 v[46:47], 0\n"
    "v_mov_b64 v[48:49], 0\n"
    "v_mov_b64 v[50:51], 0\n"
    "v_mov_b64 v[52:53], 0\n"
    "v_mov_b64 v[54:55], 0\n"
    "v_mov_b64 v[56:57], 0\n"
    "v_mov_b64 v[58:59], 0\n"
    "v_mov_b64 v[60:61], 0\n"
    "v_mov_b64 v[62:63], 0\n"
    "s_waitcnt vmcnt(0)\n"
    "v_max3_u32 v3, v4, v5, v6\n"
    "v_max3_u32 v3, v3, v7, v8\n"
    "v_max3_u32 v3, v3, v9, v10\n"
    "v_max3_u32 v3, v3, v11, v12\n"
    "v_max3_u32 v3, v3, v13, v14\n"
    "v_max3_u32 v3, v3, v15, v16\n"
    "v_max3_u32 v3, v3, v17, v18\n"
    "v_max3_u32 v3, v3, v19, v20\n"
    "v_max3_u32 v3, v3, v21, v22\n"
    "v_max3_u32 v3, v3, v23, v24\n"
    "v_max3_u32 v3, v3, v25, v26\n"
    "v_max3_u32 v3, v3, v27, v28\n"
    "v_max3_u32 v3, v3, v29, v30\n"
    "v_max3_u32 v3, v3, v31, v32\n"
    "v_max3_u32 v3, v3, v33, v34\n"
    "v_max3_u32 v3, v3, v35, v36\n"
    "v_max3_u32 v3, v3, v37, v38\n"
    "v_max_u32 v3, v3, v39\n"
    "v_cmp_eq_u32 vcc, -1, v3\n"
    "s_cbranch_vccz .Lrec_start_%=\n"
    "s_sub_u32 s69, s69, 1\n"
    "s_cbranch_scc0 .Lrec_again_%=\n"
    "s_mov_b32 s69, 0\n"
    "s_branch .Lrec_start_%=\n"
    ".Lrec_retry0_%=:\n"
    "s_cmp_eq_u32 s69, 0\n"
    "s_cbranch_scc1 .Lrec_back0_%=\n"
    "s_waitcnt lgkmcnt(0)\n"
    "ds_read_b128 v[72:75], v1 offset:0\n"
    "ds_read_b128 v[76:79], v1 offset:1024\n"
    "buffer_load_dwordx4 v[4:7], v0, s[36:39], s70 offen sc1\n"
    "s_branch .Lrec_look_%=\n"
    ".Lrec_retry1_%=:\n"
    "s_cmp_eq_u32 s69, 0\n"
    "s_cbranch_scc1 .Lrec_back1_%=\n"
    "s_waitcnt lgkmcnt(0)\n"
    "ds_read_b128 v[72:75], v1 offset:0\n"
    "ds_read_b128 v[76:79], v1 offset:1024\n"
    "buffer_load_dwordx4 v[4:7], v0, s[36:39], s70 offen sc1\n"
    "buffer_load_dwordx4 v[8:11], v0, s[36:39], s70 offen offset:1024 sc1\n"
    "buffer_load_dwordx4 v[12:15], v0, s[36:39], s70 offen offset:2048 sc1\n"
    "buffer_load_dwordx4 v[16:19], v0, s[36:39], s70 offen offset:3072 sc1\n"
    "s_branch .Lrec_look_%=\n"
    ".Lrec_retry2_%=:\n"
    "s_cmp_eq_u32 s69, 0\n"
    "s_cbranch_scc1 .Lrec_back2_%=\n"
    "s_waitcnt lgkmcnt(0)\n"
    "ds_read_b128 v[72:75], v1 offset:0\n"
    "ds_read_b128 v[76:79], v1 offset:1024\n"
    "s_branch .Lrec_again_%=\n"
    ".Lrec_tsretry_%=:\n"
    "s_mov_b32 s98, 24\n"
    ".Lrec_tsretry1_%=:\n"
    "s_mov_b32 s95, s28\n"
    "s_sub_u32 s98, s98, 1\n"
    "s_cbranch_scc1 .Lrec_tsgo_%=\n"
    "s_sleep 2\n"
    "buffer_load_dwordx2 v[12:13], v11, s[36:39], s90 offen sc1\n"
    "s_waitcnt vmcnt(0)\n"
    "v_max_u32 v12, v12, v13\n"
    "s_nop 1\n"
    "v_max_u32_dpp v12, v12, v12 row_ror:8 row_mask:0xf bank_mask:0xf\n"
    "s_nop 1\n"
    "v_max_u32_dpp v12, v12, v12 row_ror:4 row_mask:0xf bank_mask:0xf\n"
    "s_nop 1\n"
    "v_max_u32_dpp v12, v12, v12 row_ror:2 row_mask:0xf bank_mask:0xf\n"
    "s_nop 1\n"
    "v_max_u32_dpp v12, v12, v12 row_ror:1 row_mask:0xf bank_mask:0xf\n"
    "s_nop 0\n"
    "v_readlane_b32 s95, v12, 0\n"
    "v_readlane_b32 s99, v12, 16\n"
    "v_readlane_b32 s30, v12, 32\n"
    "v_readlane_b32 s31, v12, 48\n"
    "s_max_u32 s95, s95, s99\n"
    "s_max_u32 s30, s30, s31\n"
    "s_max_u32 s95, s95, s30\n"
    "s_cmp_eq_u32 s95, -1\n"
    "s_cbranch_scc1 .Lrec_tsretry1_%=\n"
    "s_branch .Lrec_tsgo_%=\n"
    ".Lrec_rretry_%=:\n"
    "s_sub_u32 s69, s69, 1\n"
    "s_cbranch_scc1 .Lrec_rdead_%=\n"
    "s_nop 7\n"
    "buffer_load_dwordx4 v[4:7], v0, s[36:39], s78 offen sc1\n"
    "buffer_load_dwordx4 v[8:11], v0, s[36:39], s78 offen offset:1024 sc1\n"
    "buffer_load_dwordx4 v[12:15], v0, s[36:39], s78 offen offset:2048 sc1\n"
    "buffer_load_dwordx4 v[16:19], v0, s[36:39], s78 offen offset:3072 sc1\n"
    "buffer_load_dwordx4 v[20:23], v0, s[36:39], s88 offen sc1\n"
    "buffer_load_dwordx4 v[24:27], v0, s[36:39], s88 offen offset:1024 sc1\n"
    "buffer_load_dwordx4 v[28:31], v0, s[36:39], s88 offen offset:2048 sc1\n"
    "buffer_load_dwordx4 v[32:35], v0, s[36:39], s88 offen offset:3072 sc1\n"
    "s_waitcnt vmcnt(0)\n"
    "s_branch .Lrec_rsum_%=\n"
    ".Lrec_rdead_%=:\n"
    "s_mov_b32 s69, 0\n"
    "s_nop 7\n"
    "s_branch .Lrec_rgo_%=\n"
    ".Lrec_nored_%=:\n"
    "s_nop 15\n"
    "s_cmp_lt_i32 s64, 0\n"
    "s_cbranch_scc0 .Lrec_red_%=\n"
    "v_lshl_or_b32 v77, s68, 1, 1\n"
    "s_waitcnt vmcnt(3)\n"
    "s_mov_b64 exec, 1\n"
    "buffer_atomic_smax v77, off, s[36:39], s58 sc1\n"
    "s_mov_b64 exec, -1\n"
    "v_mov_b32 v3, -1\n"
    "v_subrev_u32 v1, 81920, v1\n"
    "s_sub_u32 s52, s52, 24\n"
    "s_max_u32 s52, s52, 860\n"
    "s_sub_u32 s94, s52, 860\n"
    "s_lshr_b32 s94, s94, 3\n"
    "s_sub_u32 s95, s52, s35\n"
    "s_mul_i32 s95, s48, s95\n"
    "s_lshr_b32 s95, s95, 10\n"
    "s_add_u32 s32, s32, s95\n"
    "s_add_u32 s68, s68, 1\n"
    "s_and_b32 s70, s68, 3\n"
    "s_lshl_b32 s70, s70, 17\n"
    "s_add_u32 s70, s70, s66\n"
    "s_add_u32 s71, s70, 4096\n"
    "s_add_u32 s72, s70, 16384\n"
    "s_sub_u32 s46, s33, s46\n"
    "s_sub_u32 s47, s28, s47\n"
    "v_cvt_f32_u32 v40, s46\n"
    "v_cvt_f32_u32 v41, s47\n"
    "v_rcp_f32 v41, v41\n"
    "s_mov_b32 s46, s33\n"
    "s_mov_b32 s47, s28\n"
    "v_mul_f32 v40, v40, v41\n"
    "v_mov_b32 v42, 0x40c80000\n"
    "v_med3_f32 v40, v40, 4.0, v42\n"
    "v_rcp_f32 v41, v40\n"
    "v_mul_f32 v40, 0x43800000, v40\n"
    "v_mul_f32 v41, 0x44b54000, v41\n"
    "v_cvt_u32_f32 v40, v40\n"
    "v_cvt_u32_f32 v41, v41\n"
    "v_readfirstlane_b32 s48, v40\n"
    "v_readfirstlane_b32 s49, v41\n"
    "v_mov_b64 v[40:41], 0\n"
    "v_mov_b64 v[42:43], 0\n"
    "v_mov_b64 v[44:45], 0\n"
    "v_mov_b64 v[46:47], 0\n"
    "v_mov_b64 v[48:49], 0\n"
    "v_mov_b64 v[50:51], 0\n"
    "v_mov_b64 v[52:53], 0\n"
    "v_mov_b64 v[54:55], 0\n"
    "v_mov_b64 v[56:57], 0\n"
    "v_mov_b64 v[58:59], 0\n"
    "v_mov_b64 v[60:61], 0\n"
    "v_mov_b64 v[62:63], 0\n"
    "s_mov_b64 exec, 1\n"
    "buffer_atomic_smax v3, off, s[36:39], s59 sc0 sc1\n"
    "s_mov_b64 exec, -1\n"
    ".Lrec_entry_%=:\n"
    "buffer_load_dword v28, off, s[36:39], s62\n"
    "s_memtime s[96:97]\n"
    "s_nop 9\n"
    "s_branch .Lrec_b3_%=\n"
    ".Lrec_waitp_%=:\n"
    "v_mov_b32 v3, -1\n"
    "s_mov_b64 exec, 1\n"
    "buffer_atomic_smax v3, off, s[36:39], s59 sc0 sc1\n"
    "s_mov_b64 exec, -1\n"
    "s_waitcnt vmcnt(0)\n"
    "v_readfirstlane_b32 s81, v3\n"
    "s_nop 0\n"
    "s_cmp_gt_i32 s81, s73\n"
    "s_cbranch_scc1 .Lrec_waitp1_%=\n"
    "s_sleep 1\n"
    "s_sub_u32 s69, s69, 1\n"
    "s_cbranch_scc0 .Lrec_waitp_%=\n"
    "s_mov_b32 s69, 0\n"
    ".Lrec_waitp1_%=:\n"
    "s_branch .Lrec_haveP_%=\n"
    ".Lrec_FIN_%=:\n"
  );
}

typedef unsigned long long u64;
DEVI u64 ld8u(rsrc_t r, u32 voff) { v2u v = ld8(r, voff, 0); asm volatile("" : "+v"(v)); return (u64)v.x | ((u64)v.y << 32); }
DEVI u32 find_shift(rsrc_t rw, bool decide, u64 tag) {
  const int lane = threadIdx.x & 63;
  unsigned budget = SPIN_LIMIT;
  if (decide) {
    u32 best[2] = {~0u, ~0u};
    const u32 probe = 126 * CHUNK + 8192;
#pragma unroll 1
    for (int r = 0; r < 5; ++r) {
      const u32 a = lane == 0 ? probe + (u32)(r & 1) * 8192 : OOB;
      u32 v = 0;
      const unsigned long long t0 = __builtin_amdgcn_s_memtime();
      asm volatile("s_waitcnt lgkmcnt(0)\n\tbuffer_atomic_or %0, %1, %2, 0 offen sc0 sc1\n\ts_waitcnt vmcnt(0)" : "+v"(v) : "v"(a), "s"(rw) : "memory");
      const u32 dt = (u32)(__builtin_amdgcn_s_memtime() - t0);
      if (r & 1) best[1] = min(best[1], dt); else best[0] = min(best[0], dt);
    }
    for (;;) {
      v2u v[4];
      const u32 a = READY + (u32)lane * 8;
#pragma unroll
      for (int k = 0; k < 4; ++k) v[k] = (v2u){0u, 0u};
      asm volatile("buffer_atomic_or_x2 %0, %4, %5, 0 offen sc0 sc1\n\tbuffer_atomic_or_x2 %1, %4, %5, 0 offen offset:512 sc0 sc1\n\t"
                   "buffer_atomic_or_x2 %2, %4, %5, 0 offen offset:1024 sc0 sc1\n\tbuffer_atomic_or_x2 %3, %4, %5, 0 offen offset:1536 sc0 sc1\n\ts_waitcnt vmcnt(0)"
                   : "+v"(v[0]), "+v"(v[1]), "+v"(v[2]), "+v"(v[3]) : "v"(a), "s"(rw) : "memory");
      bool ok = true;
#pragma unroll
      for (int k = 0; k < 4; ++k) ok = ok & (v[k].x == (u32)tag) & (v[k].y == (u32)(tag >> 32));
      if (__all(ok) || budget == 0) break;
      --budget;
    }
    const u64 v = lane == 0 ? 2 * tag + (best[0] < best[1] ? 0u : 1u) : tag;
    st8(rw, (v2u){(u32)v, (u32)(v >> 32)}, lane == 0 ? PARW : (lane == 1 ? EPOCH : OOB), 0);
  }
  u64 v;
  for (;;) {
    cbar();
    v = ld8u(rw, PARW) ^ (2 * tag);
    if (__all(v < 2) || budget == 0) break;
    --budget;
    __builtin_amdgcn_s_sleep(4);
  }
  cbar();
  return (rfl((u32)v) & 1u) * 8192;
}
DEVI void hot_init(const Params& p, rsrc_t rw, int L, int n, u32 shift) {
  const int tid = threadIdx.x, w = (int)rfl(tid >> 6);
  const u32 hot = HOT + L * 8192 + shift, hot2 = HOT2 + (1 - L) * 8192 + shift;
  const v4u ones = {~0u, ~0u, ~0u, ~0u};
#pragma unroll
  for (int k = 0; k < 9; ++k) {
    st16(rw, ones, tid * 16, hot + (u32)(9 * n + k) * CHUNK);
    st16(rw, ones, tid * 16 + 4096, hot + (u32)(9 * n + k) * CHUNK);
  }
  {
    const u32 b = hot2 + (u32)(4 + n) * 2 * CHUNK;
    st16(rw, ones, tid * 16, b); st16(rw, ones, tid * 16 + 4096, b);
    st16(rw, ones, w == 0 ? (u32)tid * 16 : OOB, b + CHUNK);
  }
  if (n < 8) {
#pragma unroll
    for (int k = 0; k < 2; ++k) {
      st16(rw, ones, tid * 16, hot2 + (u32)(32 + 2 * n + k) * CHUNK);
      st16(rw, ones, tid * 16 + 4096, hot2 + (u32)(32 + 2 * n + k) * CHUNK);
    }
  }
  if (n >= 8) {
    const u32 b = hot2 + (u32)(n - 8) * 2 * CHUNK;
#pragma unroll
    for (int k = 0; k < 3; ++k) {
      const int pc = tid + 256 * k;
      if (pc < 576) {
        v4u v = *(const v4u*)(p.mem0 + (pc & 7) * 576 + (pc >> 3) * 8);
#pragma unroll
        for (int d = 0; d < 4; ++d) v[d] = min(v[d], 0xFFFFFFFEu);
        st16(rw, v, pc < 512 ? (u32)pc * 16 : CHUNK + (u32)(pc - 512) * 16, b);
      }
    }
  }
  drain();
  __syncthreads();
  if (tid == 0) st4(rw, 0u, INITF + L * 8192 + n * 128 + shift, 0);
}

DEVI void role_att(const Params& p, int L, int j, int a, u32 dbg, u32 shift) {
  const int tid = threadIdx.x, lane = tid & 63, w = (int)rfl(tid >> 6);
  const int c = lane & 15, g = lane >> 4;
  const int hl = 4 * a + w, h = 8 * j + hl;
  const v4u ws = make_srd(p.ws + shift, WS_END);
  v16u sa = cat4(ws, ws, ws, ws);
  v16u sb = {pga(L, B_QKV + 3 * h), pga(L, B_KVM + h), pgb(L, B_O + h), 0, dbg + w * 64,
             0, 0, 0, 0, 0, 0, 0, 0, 0, 0, 0};
  v16u va = {0, 0, 0, 0, 0, 0, 0, 0, 0, 0, 0, 0, 0, 0, 0, 0};
  va[0] = lane * 16;
  va[1] = (u32)(g * 128 + (c & 7) * 16);
  va[2] = lane < 3 ? mbox(L, j, SL_WO + lane, hl) : mbox(L, j, SL_P + hl, 48);
  va[4] = lane < 16 ? mbox_att(L, j, hl, lane) : OOB;
  va[5] = (u32)(lane < 4 ? 0 : (lane < 7 ? 2 * RO : ((lane >> 2) == 2 ? -2 : 1000)));
  va[6] = (u32)(c - 4 * g);
  va[7] = 0;
  RUN_ASM(
    "s_nop 4\n"
    "s_mov_b32 s20, 0\n"
    "s_mov_b32 s21, 0x80000000\n"
    "s_getpc_b64 s[84:85]\n"
    ".Latt_PC_%=:\n"
    "s_and_b32 s85, s85, 0xffff\n"
    "s_mov_b32 s86, .Latt_FIN_%=-.Latt_PC_%=\n"
    "s_mov_b32 s87, 0x20000\n"
    "v_mbcnt_lo_u32_b32 v240, -1, 0\n"
    "v_mbcnt_hi_u32_b32 v240, -1, v240\n"
    "v_lshlrev_b32 v240, 7, v240\n"
    "buffer_load_dword v241, v240, s[84:87], 0 offen\n"
    "v_add_u32 v240, 8192, v240\n"
    "buffer_load_dword v241, v240, s[84:87], 0 offen\n"
    "v_cmp_gt_i32 s[10:11], 0, v6\n"
    "v_cmp_gt_i32 s[12:13], 1, v6\n"
    "v_cmp_gt_i32 s[14:15], 2, v6\n"
    "v_cmp_gt_i32 s[16:17], 3, v6\n"
    "v_mov_b32 v10, 0xff61b1e6\n"
    "s_mov_b32 s18, 0x3e0293ee\n"
    "s_mov_b32 s68, 0\n"
    "s_mov_b32 s77, 1\n"
    "v_mov_b32 v8, -1\n"
    "v_mov_b32 v12, -1\n"
    "s_mov_b32 s69, 262144\n"
    "s_branch .Latt_waitp_%=\n"
    ".Latt_LOOPB_%=:\n"
    ".Latt_top_%=:\n"
    "s_waitcnt vmcnt(15)\n"
    "v_mfma_f32_16x16x32_bf16 v[160:163], v[16:19], v[48:51], 0\n"
    "v_mfma_f32_16x16x32_bf16 v[164:167], v[16:19], v[52:55], 0\n"
    "v_mfma_f32_16x16x32_bf16 v[168:171], v[20:23], v[52:55], 0\n"
    "s_waitcnt vmcnt(12)\n"
    "v_mfma_f32_16x16x32_bf16 v[160:163], v[24:27], v[56:59], v[160:163]\n"
    "v_mfma_f32_16x16x32_bf16 v[164:167], v[24:27], v[60:63], v[164:167]\n"
    "v_mfma_f32_16x16x32_bf16 v[168:171], v[28:31], v[60:63], v[168:171]\n"
    "s_waitcnt vmcnt(10)\n"
    "v_mfma_f32_16x16x32_bf16 v[160:163], v[32:35], v[64:67], v[160:163]\n"
    "v_mfma_f32_16x16x32_bf16 v[164:167], v[32:35], v[68:71], v[164:167]\n"
    "v_mfma_f32_16x16x32_bf16 v[168:171], v[36:39], v[68:71], v[168:171]\n"
    "s_waitcnt vmcnt(8)\n"
    "v_mfma_f32_16x16x32_bf16 v[160:163], v[40:43], v[72:75], v[160:163]\n"
    "v_mfma_f32_16x16x32_bf16 v[164:167], v[40:43], v[76:79], v[164:167]\n"
    "v_mfma_f32_16x16x32_bf16 v[168:171], v[44:47], v[76:79], v[168:171]\n"
    "v_add_u32 v11, v8, v5\n"
    "v_cmp_gt_i32 vcc, s77, v11\n"
    "s_nop 0\n"
    "s_or_b64 exec, vcc, s[20:21]\n"
    "buffer_atomic_smax v8, v4, s[36:39], 0 offen sc0 sc1\n"
    "s_mov_b64 exec, -1\n"
    "s_add_u32 s72, s68, 1\n"
    "s_and_b32 s4, s72, 7\n"
    "s_mul_i32 s4, s4, 4194304\n"
    "s_add_u32 s4, s4, s52\n"
    "s_add_u32 s5, s4, 4096\n"
    "s_add_u32 s6, s4, 16384\n"
    "s_add_u32 s7, s4, 20480\n"
    "s_add_u32 s8, s4, 32768\n"
    "s_add_u32 s9, s4, 36864\n"
    "s_and_b32 s73, s68, 7\n"
    "s_mul_i32 s73, s73, 4194304\n"
    "s_add_u32 s73, s73, s53\n"
    "s_add_u32 s74, s73, 3072\n"
    "s_and_b32 s75, s68, 7\n"
    "s_lshl_b32 s75, s75, 22\n"
    "s_add_u32 s75, s75, s54\n"
    "s_add_u32 s76, s75, 4096\n"
    "v_cndmask_b32 v160, v160, v10, s[10:11]\n"
    "v_cndmask_b32 v161, v161, v10, s[12:13]\n"
    "v_cndmask_b32 v162, v162, v10, s[14:15]\n"
    "v_cndmask_b32 v163, v163, v10, s[16:17]\n"
    "v_cndmask_b32 v168, v168, v10, s[10:11]\n"
    "v_cndmask_b32 v169, v169, v10, s[12:13]\n"
    "v_cndmask_b32 v170, v170, v10, s[14:15]\n"
    "v_cndmask_b32 v171, v171, v10, s[16:17]\n"
    "v_max3_f32 v224, v160, v161, v162\n"
    "v_max3_f32 v226, v164, v165, v166\n"
    "v_max_f32 v224, v224, v163\n"
    "v_max3_f32 v226, v226, v167, v168\n"
    "v_max3_f32 v226, v226, v169, v170\n"
    "v_max_f32 v226, v226, v171\n"
    "v_mov_b32 v238, v224\n"
    "v_mov_b32 v239, v226\n"
    "s_nop 0\n"
    "v_permlane16_swap_b32 v224, v238\n"
    "v_permlane16_swap_b32 v226, v239\n"
    "v_max_f32 v224, v224, v238\n"
    "v_max_f32 v226, v226, v239\n"
    "v_mov_b32 v238, v224\n"
    "v_mov_b32 v239, v226\n"
    "s_nop 0\n"
    "v_permlane32_swap_b32 v224, v238\n"
    "v_permlane32_swap_b32 v226, v239\n"
    "v_max_f32 v224, v224, v238\n"
    "v_max_f32 v226, v226, v239\n"
    "v_mul_f32 v230, 0xbe0293ee, v224\n"
    "v_mul_f32 v232, 0xbe0293ee, v226\n"
    "v_pk_fma_f32 v[160:161], v[160:161], s[18:19], v[230:231] op_sel_hi:[1,0,0]\n"
    "v_pk_fma_f32 v[162:163], v[162:163], s[18:19], v[230:231] op_sel_hi:[1,0,0]\n"
    "v_pk_fma_f32 v[164:165], v[164:165], s[18:19], v[232:233] op_sel_hi:[1,0,0]\n"
    "v_pk_fma_f32 v[166:167], v[166:167], s[18:19], v[232:233] op_sel_hi:[1,0,0]\n"
    "v_pk_fma_f32 v[168:169], v[168:169], s[18:19], v[232:233] op_sel_hi:[1,0,0]\n"
    "v_pk_fma_f32 v[170:171], v[170:171], s[18:19], v[232:233] op_sel_hi:[1,0,0]\n"
    "v_exp_f32 v160, v160\n"
    "v_exp_f32 v161, v161\n"
    "v_exp_f32 v162, v162\n"
    "v_exp_f32 v163, v163\n"
    "v_exp_f32 v164, v164\n"
    "v_exp_f32 v165, v165\n"
    "v_exp_f32 v166, v166\n"
    "v_exp_f32 v167, v167\n"
    "v_exp_f32 v168, v168\n"
    "v_exp_f32 v169, v169\n"
    "v_exp_f32 v170, v170\n"
    "v_exp_f32 v171, v171\n"
    "v_pk_add_f32 v[230:231], v[160:161], v[162:163]\n"
    "v_pk_add_f32 v[232:233], v[164:165], v[166:167]\n"
    "v_pk_add_f32 v[234:235], v[168:169], v[170:171]\n"
    "v_pk_add_f32 v[232:233], v[232:233], v[234:235]\n"
    "v_add_f32 v228, v230, v231\n"
    "v_add_f32 v229, v232, v233\n"
    "v_mov_b32 v238, v228\n"
    "v_mov_b32 v239, v229\n"
    "s_nop 0\n"
    "v_permlane16_swap_b32 v228, v238\n"
    "v_permlane16_swap_b32 v229, v239\n"
    "v_add_f32 v228, v228, v238\n"
    "v_add_f32 v229, v229, v239\n"
    "v_mov_b32 v238, v228\n"
    "v_mov_b32 v239, v229\n"
    "s_nop 0\n"
    "v_permlane32_swap_b32 v228, v238\n"
    "v_permlane32_swap_b32 v229, v239\n"
    "v_add_f32 v228, v228, v238\n"
    "v_add_f32 v229, v229, v239\n"
    "v_add_u32 v11, v12, v5\n"
    "v_cmp_gt_i32 vcc, s77, v11\n"
    "s_nop 0\n"
    "s_or_b64 exec, vcc, s[20:21]\n"
    "buffer_atomic_smax v12, v4, s[36:39], 0 offen sc0 sc1\n"
    "s_mov_b64 exec, -1\n"
    "s_waitcnt vmcnt(1)\n"
    "v_add_u32 v11, v8, v5\n"
    "v_cmp_gt_i32 vcc, s77, v11\n"
    "s_nop 0\n"
    "s_and_b32 s71, vcc_lo, 0xff\n"
    "s_cbranch_scc1 .Latt_waitr_%=\n"
    ".Latt_gor_%=:\n"
    "buffer_load_dwordx4 v[128:131], v1, s[36:39], s73 offen sc1\n"
    "buffer_load_dwordx4 v[132:135], v1, s[36:39], s73 offen offset:1536 sc1\n"
    "buffer_load_dwordx4 v[136:139], v1, s[36:39], s74 offen sc1\n"
    "buffer_load_dwordx4 v[140:143], v1, s[36:39], s74 offen offset:1536 sc1\n"
    "buffer_load_dwordx4 v[144:147], v0, s[36:39], s73 offen offset:512 sc1\n"
    "buffer_load_dwordx4 v[148:151], v0, s[36:39], s73 offen offset:2048 sc1\n"
    "buffer_load_dwordx4 v[152:155], v0, s[36:39], s74 offen offset:512 sc1\n"
    "buffer_load_dwordx4 v[156:159], v0, s[36:39], s74 offen offset:2048 sc1\n"
    "s_and_b32 s71, vcc_lo, 0xf00\n"
    "s_cselect_b32 s70, 0, 1\n"
    "s_cselect_b32 s71, 0x80000000, 0\n"
    "v_or_b32 v7, s71, v0\n"
    "s_nop 15\n"
    "buffer_load_dwordx4 v[16:19], v7, s[36:39], s6 offen sc1\n"
    "s_nop 15\n"
    "buffer_load_dwordx4 v[20:23], v7, s[36:39], s6 offen offset:1024 sc1\n"
    "s_waitcnt vmcnt(9)\n"
    "v_mfma_f32_16x16x32_bf16 v[172:175], v[128:131], v[48:51], 0\n"
    "v_mfma_f32_16x16x32_bf16 v[176:179], v[128:131], v[52:55], 0\n"
    "s_waitcnt vmcnt(8)\n"
    "v_mfma_f32_16x16x32_bf16 v[172:175], v[132:135], v[56:59], v[172:175]\n"
    "v_mfma_f32_16x16x32_bf16 v[176:179], v[132:135], v[60:63], v[176:179]\n"
    "s_waitcnt vmcnt(7)\n"
    "v_mfma_f32_16x16x32_bf16 v[172:175], v[136:139], v[64:67], v[172:175]\n"
    "v_mfma_f32_16x16x32_bf16 v[176:179], v[136:139], v[68:71], v[176:179]\n"
    "s_waitcnt vmcnt(6)\n"
    "v_mfma_f32_16x16x32_bf16 v[172:175], v[140:143], v[72:75], v[172:175]\n"
    "v_mfma_f32_16x16x32_bf16 v[176:179], v[140:143], v[76:79], v[176:179]\n"
    "s_waitcnt vmcnt(2)\n"
    "v_mov_b64 v[80:81], v[144:145]\n"
    "v_mov_b64 v[86:87], v[146:147]\n"
    "v_mov_b64 v[92:93], v[148:149]\n"
    "v_mov_b64 v[98:99], v[150:151]\n"
    "v_mov_b64 v[104:105], v[152:153]\n"
    "v_mov_b64 v[110:111], v[154:155]\n"
    "v_mov_b64 v[116:117], v[156:157]\n"
    "v_mov_b64 v[122:123], v[158:159]\n"
    "v_max3_f32 v230, v172, v173, v174\n"
    "v_max3_f32 v232, v176, v177, v178\n"
    "v_max3_f32 v230, v230, v175, v224\n"
    "v_max3_f32 v232, v232, v179, v226\n"
    "v_mov_b32 v238, v230\n"
    "v_mov_b32 v239, v232\n"
    "s_nop 0\n"
    "v_permlane16_swap_b32 v230, v238\n"
    "v_permlane16_swap_b32 v232, v239\n"
    "v_max_f32 v230, v230, v238\n"
    "v_max_f32 v232, v232, v239\n"
    "v_mul_f32 v230, 0xbe0293ee, v230\n"
    "v_mul_f32 v232, 0xbe0293ee, v232\n"
    "v_pk_fma_f32 v[172:173], v[172:173], s[18:19], v[230:231] op_sel_hi:[1,0,0]\n"
    "v_pk_fma_f32 v[174:175], v[174:175], s[18:19], v[230:231] op_sel_hi:[1,0,0]\n"
    "v_pk_fma_f32 v[176:177], v[176:177], s[18:19], v[232:233] op_sel_hi:[1,0,0]\n"
    "v_pk_fma_f32 v[178:179], v[178:179], s[18:19], v[232:233] op_sel_hi:[1,0,0]\n"
    "v_fma_f32 v224, v224, s18, v230\n"
    "v_fma_f32 v226, v226, s18, v232\n"
    "v_exp_f32 v172, v172\n"
    "v_exp_f32 v173, v173\n"
    "v_exp_f32 v174, v174\n"
    "v_exp_f32 v175, v175\n"
    "buffer_load_dwordx4 v[24:27], v7, s[36:39], s6 offen offset:2048 sc1\n"
    "v_exp_f32 v176, v176\n"
    "v_exp_f32 v177, v177\n"
    "v_exp_f32 v178, v178\n"
    "v_exp_f32 v179, v179\n"
    "buffer_load_dwordx4 v[28:31], v7, s[36:39], s6 offen offset:3072 sc1\n"
    "v_exp_f32 v224, v224\n"
    "v_exp_f32 v226, v226\n"
    "v_pk_add_f32 v[230:231], v[172:173], v[174:175]\n"
    "v_pk_add_f32 v[232:233], v[176:177], v[178:179]\n"
    "v_add_f32 v230, v230, v231\n"
    "v_add_f32 v232, v232, v233\n"
    "v_mov_b32 v238, v230\n"
    "v_mov_b32 v239, v232\n"
    "s_nop 0\n"
    "v_permlane16_swap_b32 v230, v238\n"
    "v_permlane16_swap_b32 v232, v239\n"
    "v_add_f32 v230, v230, v238\n"
    "v_add_f32 v232, v232, v239\n"
    "v_fmac_f32 v230, v228, v224\n"
    "v_fmac_f32 v232, v229, v226\n"
    "v_rcp_f32 v230, v230\n"
    "v_rcp_f32 v232, v232\n"
    "buffer_load_dwordx4 v[32:35], v7, s[36:39], s7 offen sc1\n"
    "v_mul_f32 v224, v224, v230\n"
    "v_mul_f32 v226, v226, v232\n"
    "v_pk_mul_f32 v[164:165], v[164:165], v[226:227] op_sel_hi:[1,0]\n"
    "v_pk_mul_f32 v[166:167], v[166:167], v[226:227] op_sel_hi:[1,0]\n"
    "v_pk_mul_f32 v[168:169], v[168:169], v[226:227] op_sel_hi:[1,0]\n"
    "v_pk_mul_f32 v[170:171], v[170:171], v[226:227] op_sel_hi:[1,0]\n"
    "v_pk_mul_f32 v[176:177], v[176:177], v[232:233] op_sel_hi:[1,0]\n"
    "v_pk_mul_f32 v[178:179], v[178:179], v[232:233] op_sel_hi:[1,0]\n"
    "v_pk_mul_f32 v[172:173], v[172:173], v[230:231] op_sel_hi:[1,0]\n"
    "v_pk_mul_f32 v[174:175], v[174:175], v[230:231] op_sel_hi:[1,0]\n"
    "v_pk_mul_f32 v[160:161], v[160:161], v[224:225] op_sel_hi:[1,0]\n"
    "v_pk_mul_f32 v[162:163], v[162:163], v[224:225] op_sel_hi:[1,0]\n"
    "v_cvt_pk_bf16_f32 v216, v164, v165\n"
    "v_cvt_pk_bf16_f32 v217, v166, v167\n"
    "v_cvt_pk_bf16_f32 v218, v168, v169\n"
    "v_cvt_pk_bf16_f32 v219, v170, v171\n"
    "v_cvt_pk_bf16_f32 v220, v176, v177\n"
    "v_cvt_pk_bf16_f32 v221, v178, v179\n"
    "v_cvt_pk_bf16_f32 v212, v172, v173\n"
    "v_cvt_pk_bf16_f32 v213, v174, v175\n"
    "v_cvt_pk_bf16_f32 v214, v160, v161\n"
    "v_cvt_pk_bf16_f32 v215, v162, v163\n"
    "v_mfma_f32_16x16x32_bf16 v[188:191], v[82:85], v[216:219], 0\n"
    "v_mfma_f32_16x16x32_bf16 v[192:195], v[88:91], v[216:219], 0\n"
    "v_mfma_f32_16x16x32_bf16 v[180:183], v[80:83], v[212:215], 0\n"
    "v_mfma_f32_16x16x32_bf16 v[184:187], v[86:89], v[212:215], 0\n"
    "v_mfma_f32_16x16x16_bf16 v[188:191], v[80:81], v[220:221], v[188:191]\n"
    "v_mfma_f32_16x16x16_bf16 v[192:195], v[86:87], v[220:221], v[192:195]\n"
    "v_mfma_f32_16x16x32_bf16 v[204:207], v[94:97], v[216:219], 0\n"
    "v_mfma_f32_16x16x32_bf16 v[208:211], v[100:103], v[216:219], 0\n"
    "v_cvt_pk_bf16_f32 v180, v180, v181\n"
    "v_cvt_pk_bf16_f32 v181, v182, v183\n"
    "v_mfma_f32_16x16x32_bf16 v[196:199], v[92:95], v[212:215], 0\n"
    "v_cvt_pk_bf16_f32 v182, v184, v185\n"
    "v_cvt_pk_bf16_f32 v183, v186, v187\n"
    "v_mfma_f32_16x16x32_bf16 v[200:203], v[98:101], v[212:215], 0\n"
    "buffer_store_dwordx4 v[180:183], v0, s[36:39], s75 offen sc1\n"
    "v_cvt_pk_bf16_f32 v188, v188, v189\n"
    "v_mfma_f32_16x16x16_bf16 v[204:207], v[92:93], v[220:221], v[204:207]\n"
    "v_cvt_pk_bf16_f32 v189, v190, v191\n"
    "v_cvt_pk_bf16_f32 v190, v192, v193\n"
    "v_mfma_f32_16x16x16_bf16 v[208:211], v[98:99], v[220:221], v[208:211]\n"
    "v_cvt_pk_bf16_f32 v191, v194, v195\n"
    "buffer_store_dwordx4 v[188:191], v0, s[36:39], s75 offen offset:1024 sc1\n"
    "s_nop 1\n"
    "v_mfma_f32_16x16x32_bf16 v[188:191], v[106:109], v[216:219], 0\n"
    "v_mfma_f32_16x16x32_bf16 v[192:195], v[112:115], v[216:219], 0\n"
    "v_cvt_pk_bf16_f32 v196, v196, v197\n"
    "v_cvt_pk_bf16_f32 v197, v198, v199\n"
    "v_mfma_f32_16x16x32_bf16 v[180:183], v[104:107], v[212:215], 0\n"
    "v_cvt_pk_bf16_f32 v198, v200, v201\n"
    "v_cvt_pk_bf16_f32 v199, v202, v203\n"
    "v_mfma_f32_16x16x32_bf16 v[184:187], v[110:113], v[212:215], 0\n"
    "buffer_store_dwordx4 v[196:199], v0, s[36:39], s75 offen offset:2048 sc1\n"
    "v_cvt_pk_bf16_f32 v204, v204, v205\n"
    "v_mfma_f32_16x16x16_bf16 v[188:191], v[104:105], v[220:221], v[188:191]\n"
    "v_cvt_pk_bf16_f32 v205, v206, v207\n"
    "v_cvt_pk_bf16_f32 v206, v208, v209\n"
    "v_mfma_f32_16x16x16_bf16 v[192:195], v[110:111], v[220:221], v[192:195]\n"
    "v_cvt_pk_bf16_f32 v207, v210, v211\n"
    "buffer_store_dwordx4 v[204:207], v0, s[36:39], s75 offen offset:3072 sc1\n"
    "s_nop 1\n"
    "v_mfma_f32_16x16x32_bf16 v[204:207], v[118:121], v[216:219], 0\n"
    "v_mfma_f32_16x16x32_bf16 v[208:211], v[124:127], v[216:219], 0\n"
    "v_cvt_pk_bf16_f32 v180, v180, v181\n"
    "v_cvt_pk_bf16_f32 v181, v182, v183\n"
    "v_mfma_f32_16x16x32_bf16 v[196:199], v[116:119], v[212:215], 0\n"
    "v_cvt_pk_bf16_f32 v182, v184, v185\n"
    "v_cvt_pk_bf16_f32 v183, v186, v187\n"
    "v_mfma_f32_16x16x32_bf16 v[200:203], v[122:125], v[212:215], 0\n"
    "buffer_store_dwordx4 v[180:183], v0, s[36:39], s76 offen sc1\n"
    "v_cvt_pk_bf16_f32 v188, v188, v189\n"
    "v_mfma_f32_16x16x16_bf16 v[204:207], v[116:117], v[220:221], v[204:207]\n"
    "v_cvt_pk_bf16_f32 v189, v190, v191\n"
    "v_cvt_pk_bf16_f32 v190, v192, v193\n"
    "v_mfma_f32_16x16x16_bf16 v[208:211], v[122:123], v[220:221], v[208:211]\n"
    "v_cvt_pk_bf16_f32 v191, v194, v195\n"
    "buffer_store_dwordx4 v[188:191], v0, s[36:39], s76 offen offset:1024 sc1\n"
    "v_cvt_pk_bf16_f32 v196, v196, v197\n"
    "v_cvt_pk_bf16_f32 v197, v198, v199\n"
    "v_cvt_pk_bf16_f32 v198, v200, v201\n"
    "v_cvt_pk_bf16_f32 v199, v202, v203\n"
    "buffer_store_dwordx4 v[196:199], v0, s[36:39], s76 offen offset:2048 sc1\n"
    "v_cvt_pk_bf16_f32 v204, v204, v205\n"
    "v_cvt_pk_bf16_f32 v205, v206, v207\n"
    "v_cvt_pk_bf16_f32 v206, v208, v209\n"
    "v_cvt_pk_bf16_f32 v207, v210, v211\n"
    "buffer_store_dwordx4 v[204:207], v0, s[36:39], s76 offen offset:3072 sc1\n"
    "s_nop 13\n"
    "buffer_load_dwordx4 v[36:39], v7, s[36:39], s7 offen offset:1024 sc1\n"
    "s_nop 13\n"
    "buffer_load_dwordx4 v[40:43], v7, s[36:39], s7 offen offset:2048 sc1\n"
    "s_nop 13\n"
    "buffer_load_dwordx4 v[44:47], v7, s[36:39], s7 offen offset:3072 sc1\n"
    "s_nop 13\n"
    "buffer_load_dwordx4 v[48:51], v7, s[36:39], s4 offen sc1\n"
    "s_nop 13\n"
    "buffer_load_dwordx4 v[52:55], v7, s[36:39], s4 offen offset:1024 sc1\n"
    "v_mov_b32 v9, s77\n"
    "s_waitcnt vmcnt(5)\n"
    "s_mov_b64 exec, 15\n"
    "buffer_atomic_smax v9, v2, s[36:39], 0 offen sc1\n"
    "s_mov_b64 exec, -1\n"
    "buffer_load_dwordx4 v[56:59], v7, s[36:39], s4 offen offset:2048 sc1\n"
    "buffer_load_dwordx4 v[60:63], v7, s[36:39], s4 offen offset:3072 sc1\n"
    "buffer_load_dwordx4 v[64:67], v7, s[36:39], s5 offen sc1\n"
    "buffer_load_dwordx4 v[68:71], v7, s[36:39], s5 offen offset:1024 sc1\n"
    "buffer_load_dwordx4 v[72:75], v7, s[36:39], s5 offen offset:2048 sc1\n"
    "buffer_load_dwordx4 v[76:79], v7, s[36:39], s5 offen offset:3072 sc1\n"
    "buffer_load_dwordx4 v[82:85], v7, s[36:39], s8 offen sc1\n"
    "buffer_load_dwordx4 v[88:91], v7, s[36:39], s8 offen offset:1024 sc1\n"
    "buffer_load_dwordx4 v[94:97], v7, s[36:39], s8 offen offset:2048 sc1\n"
    "buffer_load_dwordx4 v[100:103], v7, s[36:39], s8 offen offset:3072 sc1\n"
    "buffer_load_dwordx4 v[106:109], v7, s[36:39], s9 offen sc1\n"
    "buffer_load_dwordx4 v[112:115], v7, s[36:39], s9 offen offset:1024 sc1\n"
    "buffer_load_dwordx4 v[118:121], v7, s[36:39], s9 offen offset:2048 sc1\n"
    "buffer_load_dwordx4 v[124:127], v7, s[36:39], s9 offen offset:3072 sc1\n"
    "s_add_u32 s68, s68, 1\n"
    "s_add_u32 s77, s77, 2\n"
    "s_cmp_lg_u32 s70, 0\n"
    "s_cbranch_scc1 .Latt_top_%=\n"
    ".Latt_LOOPE_%=:\n"
    "s_cmp_lg_u32 s68, 64\n"
    "s_cbranch_scc1 .Latt_waitp_%=\n"
    "s_waitcnt vmcnt(0)\n"
    "s_branch .Latt_FIN_%=\n"
    ".Latt_waitp_%=:\n"
    "s_sub_u32 s72, s77, 2\n"
    "v_add_u32 v11, v8, v5\n"
    "v_cmp_gt_i32 vcc, s72, v11\n"
    "s_nop 0\n"
    "s_or_b64 exec, vcc, s[20:21]\n"
    "buffer_atomic_smax v8, v4, s[36:39], 0 offen sc0 sc1\n"
    "s_mov_b64 exec, -1\n"
    "s_waitcnt vmcnt(0)\n"
    "v_add_u32 v11, v8, v5\n"
    "v_cmp_gt_i32 vcc, s72, v11\n"
    "s_cbranch_vccz .Latt_gop_%=\n"
    "s_sleep 3\n"
    "s_sub_u32 s69, s69, 1\n"
    "s_cbranch_scc0 .Latt_waitp_%=\n"
    "s_mov_b32 s69, 0\n"
    ".Latt_gop_%=:\n"
    "s_and_b32 s4, s68, 7\n"
    "s_mul_i32 s4, s4, 4194304\n"
    "s_add_u32 s4, s4, s52\n"
    "s_add_u32 s5, s4, 4096\n"
    "s_add_u32 s6, s4, 16384\n"
    "s_add_u32 s7, s4, 20480\n"
    "s_add_u32 s8, s4, 32768\n"
    "s_add_u32 s9, s4, 36864\n"
    "buffer_load_dwordx4 v[16:19], v0, s[36:39], s6 offen sc1\n"
    "buffer_load_dwordx4 v[20:23], v0, s[36:39], s6 offen offset:1024 sc1\n"
    "buffer_load_dwordx4 v[24:27], v0, s[36:39], s6 offen offset:2048 sc1\n"
    "buffer_load_dwordx4 v[28:31], v0, s[36:39], s6 offen offset:3072 sc1\n"
    "buffer_load_dwordx4 v[32:35], v0, s[36:39], s7 offen sc1\n"
    "buffer_load_dwordx4 v[36:39], v0, s[36:39], s7 offen offset:1024 sc1\n"
    "buffer_load_dwordx4 v[40:43], v0, s[36:39], s7 offen offset:2048 sc1\n"
    "buffer_load_dwordx4 v[44:47], v0, s[36:39], s7 offen offset:3072 sc1\n"
    "buffer_load_dwordx4 v[48:51], v0, s[36:39], s4 offen sc1\n"
    "buffer_load_dwordx4 v[52:55], v0, s[36:39], s4 offen offset:1024 sc1\n"
    "buffer_load_dwordx4 v[56:59], v0, s[36:39], s4 offen offset:2048 sc1\n"
    "buffer_load_dwordx4 v[60:63], v0, s[36:39], s4 offen offset:3072 sc1\n"
    "buffer_load_dwordx4 v[64:67], v0, s[36:39], s5 offen sc1\n"
    "buffer_load_dwordx4 v[68:71], v0, s[36:39], s5 offen offset:1024 sc1\n"
    "buffer_load_dwordx4 v[72:75], v0, s[36:39], s5 offen offset:2048 sc1\n"
    "buffer_load_dwordx4 v[76:79], v0, s[36:39], s5 offen offset:3072 sc1\n"
    "buffer_load_dwordx4 v[82:85], v0, s[36:39], s8 offen sc1\n"
    "buffer_load_dwordx4 v[88:91], v0, s[36:39], s8 offen offset:1024 sc1\n"
    "buffer_load_dwordx4 v[94:97], v0, s[36:39], s8 offen offset:2048 sc1\n"
    "buffer_load_dwordx4 v[100:103], v0, s[36:39], s8 offen offset:3072 sc1\n"
    "buffer_load_dwordx4 v[106:109], v0, s[36:39], s9 offen sc1\n"
    "buffer_load_dwordx4 v[112:115], v0, s[36:39], s9 offen offset:1024 sc1\n"
    "buffer_load_dwordx4 v[118:121], v0, s[36:39], s9 offen offset:2048 sc1\n"
    "buffer_load_dwordx4 v[124:127], v0, s[36:39], s9 offen offset:3072 sc1\n"
    "s_waitcnt vmcnt(8)\n"
    "s_branch .Latt_top_%=\n"
    ".Latt_waitr_%=:\n"
    "s_or_b64 exec, vcc, s[20:21]\n"
    "buffer_atomic_smax v8, v4, s[36:39], 0 offen sc0 sc1\n"
    "s_mov_b64 exec, -1\n"
    "s_sleep 3\n"
    "s_waitcnt vmcnt(1)\n"
    "v_add_u32 v11, v12, v5\n"
    "v_cmp_gt_i32 vcc, s77, v11\n"
    "s_nop 0\n"
    "s_and_b32 s71, vcc_lo, 0xff\n"
    "s_cbranch_scc0 .Latt_gor_%=\n"
    "s_or_b64 exec, vcc, s[20:21]\n"
    "buffer_atomic_smax v12, v4, s[36:39], 0 offen sc0 sc1\n"
    "s_mov_b64 exec, -1\n"
    "s_sleep 3\n"
    "s_waitcnt vmcnt(1)\n"
    "v_add_u32 v11, v8, v5\n"
    "v_cmp_gt_i32 vcc, s77, v11\n"
    "s_nop 0\n"
    "s_and_b32 s71, vcc_lo, 0xff\n"
    "s_cbranch_scc0 .Latt_gor_%=\n"
    "s_sub_u32 s69, s69, 1\n"
    "s_cbranch_scc0 .Latt_waitr_%=\n"
    ".Latt_deadr_%=:\n"
    "s_mov_b32 s69, 0\n"
    "s_branch .Latt_gor_%=\n"
    ".Latt_FIN_%=:\n"
  );
}

template <int MODE>
DEVI void red_args(const Params& p, int L, int q, u32 dbg, u64 tag, v16u& sa, v16u& sb, v16u& va, v16u& vb) {
  const int tid = threadIdx.x, lane = tid & 63, w = (int)rfl(tid >> 6);
  const int c = lane & 15, g = lane >> 4;
  const bool rowres = MODE == 0 || (MODE == 1 && L == 0);
  const bool fin = MODE == 2 && L == 1;
  const v4u ws = make_srd(p.ws, WS_END), rx = make_srd(p.x, 2048 * 1152), ro = make_srd(p.out, 2048 * 1152);
  u32 ft = OOB;
  if (lane < 32) {
    if (MODE == 1) { if (lane < 24) ft = mbox(L, lane / 6, SL_UP + lane % 6, 4 * q + w); }
    else if (!fin) ft = mbox(MODE == 0 ? 0 : 1, lane >> 3, SL_P + (lane & 7), (MODE == 0 ? 16 * L : 0) + 4 * q + w);
  } else if (MODE != 0) {
    if (lane < 44) ft = mbox(L, (lane - 32) / 3, (MODE == 1 ? SL_WO : SL_DOWN) + (lane - 32) % 3, (MODE == 1 ? 8 : 24) + 4 * q + w);
    else if (lane == 44 && !rowres) ft = MODE == 1 ? mbox(0, q, SL_REDF, 48 + w) : mbox(L, q, SL_REDA, 48 + w);
  }
  const unsigned long long fm = __ballot(ft != OOB);
  sa = cat4(ws, rowres ? rx : ws, fin ? ro : ws, (v4u){(u32)(2 * tag), (u32)((2 * tag) >> 32), 0u, 0u});
  sb = (v16u){pgb(L, MODE == 1 ? B_PA : B_PF),
              rowres ? 0u : (MODE == 1 ? pgb(1, B_X) : pgb(L, B_X1)), rowres ? 36864u : SLOT, (u32)(rowres ? 63 : (MODE == 1 ? RX2 - 1 : RX - 1)), rowres ? 128u : 1024u,
              MODE == 1 ? pgb(L, B_X1) : pgb(1, B_X), (u32)((MODE == 0 || fin) ? 63 : (MODE == 1 ? RX - 1 : RX2 - 1)),
              fin ? 0u : (MODE == 0 ? OFF_XN0 : (MODE == 1 ? pgb(L, B_XN2) : pgb(1, B_XN))), 4096u,
              rowres ? 0u : ~0u, dbg + w * 64, fin ? 0u : ~0u, (u32)fm, (u32)(fm >> 32), (u32)(L == 0 ? 0 : (L == 1 ? LN_B : LN_C)), (u32)(L == 0 ? LN_B - 1 : (L == 1 ? LN_C - 1 : 63))};
  const int need = w == 0 ? 0x10F : (w == 1 ? 0x23F : (w == 2 ? 0x4F0 : 0xF00));
  const bool in = MODE != 0 && lane < 48 && ((need >> (lane % 12)) & 1);
  const bool back = MODE != 0 && lane >= 48 && lane < 52 && !fin;
  auto frag = [&](int I) { return kb_off(2 * (I >> 1)) + (u32)((I & 1) * 512 + (q >> 1) * 1024 + (c >> 3) * 256 + (8 * (q & 1) + (c & 7)) * 16); };
  auto rowm = [&](int I) { return (u32)((8 * q + (c & 7)) * 1152 + (2 * I + (c >> 3)) * 16); };
  const int I0 = 8 * w + 2 * (g & 1) + (g >> 1);
  const u32 pv = (u32)(q * CHUNK + w * 2048 + lane * 16), pv8 = (u32)(4 * CHUNK + (4 * q + w) * 256 + lane * 16);
  va[0] = lane == 63 ? OOB : mbox(L, q, MODE == 1 ? SL_REDA : SL_REDF, lane);
  va[1] = (u32)(in ? -(q >> 1) : (back ? 2 * (MODE == 1 ? RX : RX2) : 1000));
  va[2] = (u32)g * 5 * CHUNK + frag(8 * w);
  va[3] = (u32)g * 5 * CHUNK + frag(32 + w);
  va[4] = fin ? (u32)(q * 9216 + tid * 16) : frag(I0);
  va[5] = fin ? (w == 0 ? (u32)(q * 9216 + 8192 + tid * 16) : OOB) : (g == 0 ? frag(32 + w) : OOB);
  va[6] = rowres ? rowm(I0) : pv;
  va[7] = g == 0 ? (rowres ? rowm(32 + w) : pv8) : OOB;
  va[8] = pv;
  va[9] = g == 0 ? pv8 : OOB;
  va[10] = fin ? rowm(I0) - q * 9216 : (u32)(((c & 7) * 4 + w) * 8);
  va[11] = fin ? (u32)tid * 16 : (u32)((c & 7) * 32);
  va[12] = fin ? (g == 0 ? rowm(32 + w) - q * 9216 : (u32)(12288 + tid * 16)) : (g < 2 ? 0x3F800000u : 0u);
  va[13] = ft; va[14] = 0; va[15] = 0;
  const u32 one = 0x3F80u << (16 * (c & 1));
#pragma unroll
  for (int d = 0; d < 4; ++d) {
    const bool on = ((c & 7) >> 1) == d;
    vb[d] = (on && c < 8) ? one : 0u;
    vb[4 + d] = (on && c >= 8) ? one : 0u;
    vb[8 + d] = (on && g == 2 * (c >> 3)) ? one : 0u;
    vb[12 + d] = (on && g == 2 * (c >> 3) + 1) ? one : 0u;
  }
#pragma unroll
  for (int i = 0; i < 16; ++i) { sa[i] = rfl(sa[i]); sb[i] = rfl(sb[i]); }
}
#define RUN_RED(TEXT) asm volatile(TEXT : "+{s[36:51]}"(sa), "+{s[52:67]}"(sb), "+{v[0:15]}"(va), "+{v[16:31]}"(vb) :: CLOB_RED)
#define CLOB_RED \
  "v32", "v33", "v34", "v35", "v36", "v37", "v38", "v39", "v40", "v41", "v42", "v43", "v44", "v45", "v46", "v47", "v48", "v49", "v50", "v51", "v52", "v53", "v54", "v55", \
  "v56", "v57", "v58", "v59", "v60", "v61", "v62", "v63", "v64", "v65", "v66", "v67", "v68", "v69", "v70", "v71", "v72", "v73", "v74", "v75", "v76", "v77", "v78", "v79", \
  "v80", "v81", "v82", "v83", "v84", "v85", "v86", "v87", "v88", "v89", "v90", "v91", "v92", "v93", "v94", "v95", "v96", "v97", "v98", "v99", "v100", "v101", "v102", "v103", \
  "v104", "v105", "v106", "v107", "v108", "v109", "v110", "v111", "v112", "v113", "v114", "v115", "v116", "v117", "v118", "v119", "v120", "v121", "v122", "v123", "v124", "v125", "v126", "v127", \
  "v128", "v129", "v130", "v131", "v132", "v133", "v134", "v135", "s68", "s69", "s70", "s71", "s72", "s73", "s74", "s75", "s76", "s77", "s78", "s79", "s80", "s81", "s82", "s83", \
  "s84", "s85", "s86", "s87", "s88", "s89", "s90", "s91", "s92", "s93", "s94", "s95", "s96", "s97", "s98", "s99", "vcc", "scc", "memory"
DEVI void role_ln0(const Params& p, int grp, int q, u32 dbg, u64 tag) {
  v16u sa, sb, va, vb;
  red_args<0>(p, grp, q, dbg, tag, sa, sb, va, vb);
  RUN_RED(
    "s_nop 4\n"
    "s_mov_b32 s50, 0\n"
    "s_mov_b32 s51, 0x80000000\n"
    "s_getpc_b64 s[84:85]\n"
    ".LrLN0_PC_%=:\n"
    "s_and_b32 s85, s85, 0xffff\n"
    "s_mov_b32 s86, .LrLN0_FIN_%=-.LrLN0_PC_%=\n"
    "s_mov_b32 s87, 0x20000\n"
    "v_mbcnt_lo_u32_b32 v134, -1, 0\n"
    "v_mbcnt_hi_u32_b32 v134, -1, v134\n"
    "v_lshlrev_b32 v134, 7, v134\n"
    "buffer_load_dword v135, v134, s[84:87], 0 offen\n"
    "v_add_u32 v134, 8192, v134\n"
    "buffer_load_dword v135, v134, s[84:87], 0 offen\n"
    "s_mov_b32 s69, 0\n"
    "v_mov_b32 v122, -1\n"
    "v_mov_b32 v123, -1\n"
    "s_mov_b32 s70, 262144\n"
    "s_add_u32 s69, s66, 0\n"
    "s_and_b32 s73, s69, s55\n"
    "s_mul_i32 s73, s73, s54\n"
    "s_add_u32 s73, s73, s53\n"
    "s_add_u32 s74, s73, s56\n"
    "buffer_load_dwordx4 v[40:43], v6, s[40:43], s73 offen sc1\n"
    "buffer_load_dwordx4 v[44:47], v6, s[40:43], s74 offen sc1\n"
    "buffer_load_dwordx4 v[48:51], v7, s[40:43], s73 offen sc1\n"
    "s_add_u32 s69, s66, 1\n"
    "s_and_b32 s73, s69, s55\n"
    "s_mul_i32 s73, s73, s54\n"
    "s_add_u32 s73, s73, s53\n"
    "s_add_u32 s74, s73, s56\n"
    "buffer_load_dwordx4 v[52:55], v6, s[40:43], s73 offen sc1\n"
    "buffer_load_dwordx4 v[56:59], v6, s[40:43], s74 offen sc1\n"
    "buffer_load_dwordx4 v[60:63], v7, s[40:43], s73 offen sc1\n"
    "s_add_u32 s69, s66, 2\n"
    "s_and_b32 s73, s69, s55\n"
    "s_mul_i32 s73, s73, s54\n"
    "s_add_u32 s73, s73, s53\n"
    "s_add_u32 s74, s73, s56\n"
    "buffer_load_dwordx4 v[68:71], v6, s[40:43], s73 offen sc1\n"
    "buffer_load_dwordx4 v[72:75], v6, s[40:43], s74 offen sc1\n"
    "buffer_load_dwordx4 v[76:79], v7, s[40:43], s73 offen sc1\n"
    "s_add_u32 s69, s66, 3\n"
    "s_and_b32 s73, s69, s55\n"
    "s_mul_i32 s73, s73, s54\n"
    "s_add_u32 s73, s73, s53\n"
    "s_add_u32 s74, s73, s56\n"
    "buffer_load_dwordx4 v[124:127], v6, s[40:43], s73 offen sc1\n"
    "buffer_load_dwordx4 v[128:131], v6, s[40:43], s74 offen sc1\n"
    "buffer_load_dwordx4 v[132:135], v7, s[40:43], s73 offen sc1\n"
    "s_sub_u32 s68, s66, 1\n"
    ".LrLN0_par_%=:\n"
    "buffer_load_dwordx2 v[14:15], off, s[36:39], 0 sc1\n"
    "s_waitcnt vmcnt(0)\n"
    "v_readfirstlane_b32 s75, v14\n"
    "v_readfirstlane_b32 s76, v15\n"
    "s_xor_b32 s75, s75, s48\n"
    "s_xor_b32 s76, s76, s49\n"
    "s_cselect_b32 s76, 2, 0\n"
    "s_or_b32 s76, s76, s75\n"
    "s_cmp_lt_u32 s76, 2\n"
    "s_cbranch_scc1 .LrLN0_par1_%=\n"
    "s_sleep 4\n"
    "s_sub_u32 s70, s70, 1\n"
    "s_cbranch_scc0 .LrLN0_par_%=\n"
    "s_mov_b32 s70, 0\n"
    ".LrLN0_par1_%=:\n"
    "s_and_b32 s75, s75, 1\n"
    "s_lshl_b32 s75, s75, 13\n"
    "s_add_u32 s36, s36, s75\n"
    "s_addc_u32 s37, s37, 0\n"
    "s_and_b32 s76, s75, s61\n"
    "s_add_u32 s40, s40, s76\n"
    "s_addc_u32 s41, s41, 0\n"
    "s_and_b32 s76, s75, s63\n"
    "s_add_u32 s44, s44, s76\n"
    "s_addc_u32 s45, s45, 0\n"
    ".LrLN0_LOOPB_%=:\n"
    ".LrLN0_top_%=:\n"
    "s_add_u32 s68, s68, 1\n"
    "s_add_u32 s69, s68, 4\n"
    "s_and_b32 s75, s68, s58\n"
    "s_mul_i32 s77, s75, 81920\n"
    "s_add_u32 s77, s77, s59\n"
    "s_add_u32 s78, s77, s60\n"
    "s_and_b32 s73, s69, s55\n"
    "s_mul_i32 s73, s73, s54\n"
    "s_add_u32 s73, s73, s53\n"
    "s_add_u32 s74, s73, s56\n"
    "s_waitcnt vmcnt(27)\n"
    "v_mfma_f32_16x16x32_bf16 v[80:83], v[24:27], v[40:43], 0\n"
    "v_mfma_f32_16x16x32_bf16 v[84:87], v[28:31], v[40:43], 0\n"
    "s_waitcnt vmcnt(26)\n"
    "v_mfma_f32_16x16x32_bf16 v[88:91], v[24:27], v[44:47], 0\n"
    "v_mfma_f32_16x16x32_bf16 v[92:95], v[28:31], v[44:47], 0\n"
    "s_waitcnt vmcnt(25)\n"
    "v_mfma_f32_16x16x32_bf16 v[96:99], v[24:27], v[48:51], 0\n"
    "buffer_load_dwordx4 v[40:43], v6, s[40:43], s73 offen sc1\n"
    "buffer_load_dwordx4 v[44:47], v6, s[40:43], s74 offen sc1\n"
    "buffer_load_dwordx4 v[48:51], v7, s[40:43], s73 offen sc1\n"
    "v_pk_add_f32 v[112:113], v[80:81], v[82:83]\n"
    "v_pk_add_f32 v[114:115], v[84:85], v[86:87]\n"
    "v_pk_add_f32 v[112:113], v[112:113], v[88:89]\n"
    "v_pk_add_f32 v[114:115], v[114:115], v[90:91]\n"
    "v_pk_add_f32 v[112:113], v[112:113], v[92:93]\n"
    "v_pk_add_f32 v[114:115], v[114:115], v[94:95]\n"
    "v_pk_add_f32 v[112:113], v[112:113], v[96:97]\n"
    "v_pk_add_f32 v[114:115], v[114:115], v[98:99]\n"
    "v_pk_add_f32 v[112:113], v[112:113], v[114:115]\n"
    "v_add_f32 v112, v112, v113\n"
    "s_nop 1\n"
    "v_add_f32_dpp v14, v112, v112 row_ror:8 row_mask:0xf bank_mask:0xf\n"
    "v_add_f32_dpp v15, v112, v112 row_ror:8 row_mask:0xf bank_mask:0xf\n"
    "s_nop 1\n"
    "v_permlane16_swap_b32 v14, v15\n"
    "v_add_f32 v112, v14, v15\n"
    "v_add_f32 v14, v14, v15\n"
    "s_nop 1\n"
    "v_permlane32_swap_b32 v112, v14\n"
    "v_add_f32 v112, v112, v14\n"
    "v_mul_f32 v112, 0x3be38e39, v112\n"
    "v_mul_f32 v114, v112, v12\n"
    "v_pk_add_f32 v[80:81], v[80:81], v[112:113] op_sel_hi:[1,0] neg_lo:[0,1] neg_hi:[0,1]\n"
    "v_pk_add_f32 v[82:83], v[82:83], v[112:113] op_sel_hi:[1,0] neg_lo:[0,1] neg_hi:[0,1]\n"
    "v_pk_add_f32 v[84:85], v[84:85], v[112:113] op_sel_hi:[1,0] neg_lo:[0,1] neg_hi:[0,1]\n"
    "v_pk_add_f32 v[86:87], v[86:87], v[112:113] op_sel_hi:[1,0] neg_lo:[0,1] neg_hi:[0,1]\n"
    "v_pk_add_f32 v[88:89], v[88:89], v[112:113] op_sel_hi:[1,0] neg_lo:[0,1] neg_hi:[0,1]\n"
    "v_pk_add_f32 v[90:91], v[90:91], v[112:113] op_sel_hi:[1,0] neg_lo:[0,1] neg_hi:[0,1]\n"
    "v_pk_add_f32 v[92:93], v[92:93], v[112:113] op_sel_hi:[1,0] neg_lo:[0,1] neg_hi:[0,1]\n"
    "v_pk_add_f32 v[94:95], v[94:95], v[112:113] op_sel_hi:[1,0] neg_lo:[0,1] neg_hi:[0,1]\n"
    "v_pk_add_f32 v[96:97], v[96:97], v[114:115] op_sel_hi:[1,0] neg_lo:[0,1] neg_hi:[0,1]\n"
    "v_pk_add_f32 v[98:99], v[98:99], v[114:115] op_sel_hi:[1,0] neg_lo:[0,1] neg_hi:[0,1]\n"
    "v_pk_mul_f32 v[116:117], v[80:81], v[80:81]\n"
    "v_pk_mul_f32 v[118:119], v[82:83], v[82:83]\n"
    "v_pk_fma_f32 v[116:117], v[84:85], v[84:85], v[116:117]\n"
    "v_pk_fma_f32 v[118:119], v[86:87], v[86:87], v[118:119]\n"
    "v_pk_fma_f32 v[116:117], v[88:89], v[88:89], v[116:117]\n"
    "v_pk_fma_f32 v[118:119], v[90:91], v[90:91], v[118:119]\n"
    "v_pk_fma_f32 v[116:117], v[92:93], v[92:93], v[116:117]\n"
    "v_pk_fma_f32 v[118:119], v[94:95], v[94:95], v[118:119]\n"
    "v_pk_fma_f32 v[116:117], v[96:97], v[96:97], v[116:117]\n"
    "v_pk_fma_f32 v[118:119], v[98:99], v[98:99], v[118:119]\n"
    "v_pk_add_f32 v[116:117], v[116:117], v[118:119]\n"
    "v_add_f32 v113, v116, v117\n"
    "s_nop 1\n"
    "v_add_f32_dpp v14, v113, v113 row_ror:8 row_mask:0xf bank_mask:0xf\n"
    "v_add_f32_dpp v15, v113, v113 row_ror:8 row_mask:0xf bank_mask:0xf\n"
    "s_nop 1\n"
    "v_permlane16_swap_b32 v14, v15\n"
    "v_add_f32 v113, v14, v15\n"
    "v_add_f32 v14, v14, v15\n"
    "s_nop 1\n"
    "v_permlane32_swap_b32 v113, v14\n"
    "v_add_f32 v113, v113, v14\n"
    "ds_write_b64 v10, v[112:113]\n"
    "s_waitcnt vmcnt(17)\n"
    "s_add_u32 s75, s66, 1\n"
    "s_min_i32 s75, s75, s68\n"
    "s_sub_u32 s76, s68, 2\n"
    "s_max_i32 s75, s75, s76\n"
    "s_sub_u32 s75, s75, 1\n"
    "v_lshl_or_b32 v14, s75, 1, 1\n"
    "s_mov_b64 exec, s[64:65]\n"
    "buffer_atomic_smax v14, v13, s[36:39], 0 offen sc1\n"
    "s_mov_b64 exec, -1\n"
    "s_waitcnt lgkmcnt(0)\n"
    "s_barrier\n"
    "ds_read_b128 v[32:35], v11\n"
    "ds_read_b128 v[36:39], v11 offset:16\n"
    "v_xor_b32 v10, 256, v10\n"
    "v_xor_b32 v11, 256, v11\n"
    "s_waitcnt lgkmcnt(0)\n"
    "v_pk_add_f32 v[114:115], v[32:33], v[34:35]\n"
    "v_pk_add_f32 v[116:117], v[36:37], v[38:39]\n"
    "v_pk_add_f32 v[114:115], v[114:115], v[116:117]\n"
    "v_fmac_f32 v32, 0xbe800000, v114\n"
    "v_fmac_f32 v34, 0xbe800000, v114\n"
    "v_fmac_f32 v36, 0xbe800000, v114\n"
    "v_fmac_f32 v38, 0xbe800000, v114\n"
    "v_fmac_f32 v112, 0xbe800000, v114\n"
    "v_mul_f32 v116, v32, v32\n"
    "v_fmac_f32 v116, v34, v34\n"
    "v_fmac_f32 v116, v36, v36\n"
    "v_fmac_f32 v116, v38, v38\n"
    "v_fmac_f32 v115, 0x43100000, v116\n"
    "v_mov_b32 v116, 0x3727c5ac\n"
    "v_fmac_f32 v116, 0x3ae38e39, v115\n"
    "v_rsq_f32 v116, v116\n"
    "s_nop 0\n"
    "v_mul_f32 v114, v112, v116\n"
    "v_pk_fma_f32 v[80:81], v[80:81], v[116:117], v[114:115] op_sel_hi:[1,0,0]\n"
    "v_pk_fma_f32 v[82:83], v[82:83], v[116:117], v[114:115] op_sel_hi:[1,0,0]\n"
    "v_pk_fma_f32 v[84:85], v[84:85], v[116:117], v[114:115] op_sel_hi:[1,0,0]\n"
    "v_pk_fma_f32 v[86:87], v[86:87], v[116:117], v[114:115] op_sel_hi:[1,0,0]\n"
    "v_pk_fma_f32 v[88:89], v[88:89], v[116:117], v[114:115] op_sel_hi:[1,0,0]\n"
    "v_pk_fma_f32 v[90:91], v[90:91], v[116:117], v[114:115] op_sel_hi:[1,0,0]\n"
    "v_pk_fma_f32 v[92:93], v[92:93], v[116:117], v[114:115] op_sel_hi:[1,0,0]\n"
    "v_pk_fma_f32 v[94:95], v[94:95], v[116:117], v[114:115] op_sel_hi:[1,0,0]\n"
    "v_pk_fma_f32 v[96:97], v[96:97], v[116:117], v[114:115] op_sel_hi:[1,0,0]\n"
    "v_pk_fma_f32 v[98:99], v[98:99], v[116:117], v[114:115] op_sel_hi:[1,0,0]\n"
    "v_cvt_pk_bf16_f32 v100, v80, v81\n"
    "v_cvt_pk_bf16_f32 v101, v82, v83\n"
    "v_cvt_pk_bf16_f32 v102, v84, v85\n"
    "v_cvt_pk_bf16_f32 v103, v86, v87\n"
    "v_cvt_pk_bf16_f32 v104, v88, v89\n"
    "v_cvt_pk_bf16_f32 v105, v90, v91\n"
    "v_cvt_pk_bf16_f32 v106, v92, v93\n"
    "v_cvt_pk_bf16_f32 v107, v94, v95\n"
    "v_cvt_pk_bf16_f32 v108, v96, v97\n"
    "v_cvt_pk_bf16_f32 v109, v98, v99\n"
    "v_permlane16_swap_b32 v100, v102\n"
    "v_permlane16_swap_b32 v101, v103\n"
    "buffer_store_dwordx4 v[100:103], v4, s[44:47], s77 offen sc1\n"
    "v_permlane16_swap_b32 v104, v106\n"
    "v_permlane16_swap_b32 v105, v107\n"
    "buffer_store_dwordx4 v[104:107], v4, s[44:47], s78 offen sc1\n"
    "v_permlane16_swap_b32 v108, v110\n"
    "v_permlane16_swap_b32 v109, v111\n"
    "buffer_store_dwordx4 v[108:111], v5, s[44:47], s77 offen sc1\n"
    "s_cmp_eq_u32 s68, s66\n"
    "s_cbranch_scc1 .LrLN0_first_%=\n"
    ".LrLN0_first1_%=:\n"
    "s_add_u32 s68, s68, 1\n"
    "s_add_u32 s69, s68, 4\n"
    "s_and_b32 s75, s68, s58\n"
    "s_mul_i32 s77, s75, 81920\n"
    "s_add_u32 s77, s77, s59\n"
    "s_add_u32 s78, s77, s60\n"
    "s_and_b32 s73, s69, s55\n"
    "s_mul_i32 s73, s73, s54\n"
    "s_add_u32 s73, s73, s53\n"
    "s_add_u32 s74, s73, s56\n"
    "s_waitcnt vmcnt(27)\n"
    "v_mfma_f32_16x16x32_bf16 v[80:83], v[24:27], v[52:55], 0\n"
    "v_mfma_f32_16x16x32_bf16 v[84:87], v[28:31], v[52:55], 0\n"
    "s_waitcnt vmcnt(26)\n"
    "v_mfma_f32_16x16x32_bf16 v[88:91], v[24:27], v[56:59], 0\n"
    "v_mfma_f32_16x16x32_bf16 v[92:95], v[28:31], v[56:59], 0\n"
    "s_waitcnt vmcnt(25)\n"
    "v_mfma_f32_16x16x32_bf16 v[96:99], v[24:27], v[60:63], 0\n"
    "buffer_load_dwordx4 v[52:55], v6, s[40:43], s73 offen sc1\n"
    "buffer_load_dwordx4 v[56:59], v6, s[40:43], s74 offen sc1\n"
    "buffer_load_dwordx4 v[60:63], v7, s[40:43], s73 offen sc1\n"
    "v_pk_add_f32 v[112:113], v[80:81], v[82:83]\n"
    "v_pk_add_f32 v[114:115], v[84:85], v[86:87]\n"
    "v_pk_add_f32 v[112:113], v[112:113], v[88:89]\n"
    "v_pk_add_f32 v[114:115], v[114:115], v[90:91]\n"
    "v_pk_add_f32 v[112:113], v[112:113], v[92:93]\n"
    "v_pk_add_f32 v[114:115], v[114:115], v[94:95]\n"
    "v_pk_add_f32 v[112:113], v[112:113], v[96:97]\n"
    "v_pk_add_f32 v[114:115], v[114:115], v[98:99]\n"
    "v_pk_add_f32 v[112:113], v[112:113], v[114:115]\n"
    "v_add_f32 v112, v112, v113\n"
    "s_nop 1\n"
    "v_add_f32_dpp v14, v112, v112 row_ror:8 row_mask:0xf bank_mask:0xf\n"
    "v_add_f32_dpp v15, v112, v112 row_ror:8 row_mask:0xf bank_mask:0xf\n"
    "s_nop 1\n"
    "v_permlane16_swap_b32 v14, v15\n"
    "v_add_f32 v112, v14, v15\n"
    "v_add_f32 v14, v14, v15\n"
    "s_nop 1\n"
    "v_permlane32_swap_b32 v112, v14\n"
    "v_add_f32 v112, v112, v14\n"
    "v_mul_f32 v112, 0x3be38e39, v112\n"
    "v_mul_f32 v114, v112, v12\n"
    "v_pk_add_f32 v[80:81], v[80:81], v[112:113] op_sel_hi:[1,0] neg_lo:[0,1] neg_hi:[0,1]\n"
    "v_pk_add_f32 v[82:83], v[82:83], v[112:113] op_sel_hi:[1,0] neg_lo:[0,1] neg_hi:[0,1]\n"
    "v_pk_add_f32 v[84:85], v[84:85], v[112:113] op_sel_hi:[1,0] neg_lo:[0,1] neg_hi:[0,1]\n"
    "v_pk_add_f32 v[86:87], v[86:87], v[112:113] op_sel_hi:[1,0] neg_lo:[0,1] neg_hi:[0,1]\n"
    "v_pk_add_f32 v[88:89], v[88:89], v[112:113] op_sel_hi:[1,0] neg_lo:[0,1] neg_hi:[0,1]\n"
    "v_pk_add_f32 v[90:91], v[90:91], v[112:113] op_sel_hi:[1,0] neg_lo:[0,1] neg_hi:[0,1]\n"
    "v_pk_add_f32 v[92:93], v[92:93], v[112:113] op_sel_hi:[1,0] neg_lo:[0,1] neg_hi:[0,1]\n"
    "v_pk_add_f32 v[94:95], v[94:95], v[112:113] op_sel_hi:[1,0] neg_lo:[0,1] neg_hi:[0,1]\n"
    "v_pk_add_f32 v[96:97], v[96:97], v[114:115] op_sel_hi:[1,0] neg_lo:[0,1] neg_hi:[0,1]\n"
    "v_pk_add_f32 v[98:99], v[98:99], v[114:115] op_sel_hi:[1,0] neg_lo:[0,1] neg_hi:[0,1]\n"
    "v_pk_mul_f32 v[116:117], v[80:81], v[80:81]\n"
    "v_pk_mul_f32 v[118:119], v[82:83], v[82:83]\n"
    "v_pk_fma_f32 v[116:117], v[84:85], v[84:85], v[116:117]\n"
    "v_pk_fma_f32 v[118:119], v[86:87], v[86:87], v[118:119]\n"
    "v_pk_fma_f32 v[116:117], v[88:89], v[88:89], v[116:117]\n"
    "v_pk_fma_f32 v[118:119], v[90:91], v[90:91], v[118:119]\n"
    "v_pk_fma_f32 v[116:117], v[92:93], v[92:93], v[116:117]\n"
    "v_pk_fma_f32 v[118:119], v[94:95], v[94:95], v[118:119]\n"
    "v_pk_fma_f32 v[116:117], v[96:97], v[96:97], v[116:117]\n"
    "v_pk_fma_f32 v[118:119], v[98:99], v[98:99], v[118:119]\n"
    "v_pk_add_f32 v[116:117], v[116:117], v[118:119]\n"
    "v_add_f32 v113, v116, v117\n"
    "s_nop 1\n"
    "v_add_f32_dpp v14, v113, v113 row_ror:8 row_mask:0xf bank_mask:0xf\n"
    "v_add_f32_dpp v15, v113, v113 row_ror:8 row_mask:0xf bank_mask:0xf\n"
    "s_nop 1\n"
    "v_permlane16_swap_b32 v14, v15\n"
    "v_add_f32 v113, v14, v15\n"
    "v_add_f32 v14, v14, v15\n"
    "s_nop 1\n"
    "v_permlane32_swap_b32 v113, v14\n"
    "v_add_f32 v113, v113, v14\n"
    "ds_write_b64 v10, v[112:113]\n"
    "s_waitcnt vmcnt(17)\n"
    "s_add_u32 s75, s66, 1\n"
    "s_min_i32 s75, s75, s68\n"
    "s_sub_u32 s76, s68, 2\n"
    "s_max_i32 s75, s75, s76\n"
    "s_sub_u32 s75, s75, 1\n"
    "v_lshl_or_b32 v14, s75, 1, 1\n"
    "s_mov_b64 exec, s[64:65]\n"
    "buffer_atomic_smax v14, v13, s[36:39], 0 offen sc1\n"
    "s_mov_b64 exec, -1\n"
    "s_waitcnt lgkmcnt(0)\n"
    "s_barrier\n"
    "ds_read_b128 v[32:35], v11\n"
    "ds_read_b128 v[36:39], v11 offset:16\n"
    "v_xor_b32 v10, 256, v10\n"
    "v_xor_b32 v11, 256, v11\n"
    "s_waitcnt lgkmcnt(0)\n"
    "v_pk_add_f32 v[114:115], v[32:33], v[34:35]\n"
    "v_pk_add_f32 v[116:117], v[36:37], v[38:39]\n"
    "v_pk_add_f32 v[114:115], v[114:115], v[116:117]\n"
    "v_fmac_f32 v32, 0xbe800000, v114\n"
    "v_fmac_f32 v34, 0xbe800000, v114\n"
    "v_fmac_f32 v36, 0xbe800000, v114\n"
    "v_fmac_f32 v38, 0xbe800000, v114\n"
    "v_fmac_f32 v112, 0xbe800000, v114\n"
    "v_mul_f32 v116, v32, v32\n"
    "v_fmac_f32 v116, v34, v34\n"
    "v_fmac_f32 v116, v36, v36\n"
    "v_fmac_f32 v116, v38, v38\n"
    "v_fmac_f32 v115, 0x43100000, v116\n"
    "v_mov_b32 v116, 0x3727c5ac\n"
    "v_fmac_f32 v116, 0x3ae38e39, v115\n"
    "v_rsq_f32 v116, v116\n"
    "s_nop 0\n"
    "v_mul_f32 v114, v112, v116\n"
    "v_pk_fma_f32 v[80:81], v[80:81], v[116:117], v[114:115] op_sel_hi:[1,0,0]\n"
    "v_pk_fma_f32 v[82:83], v[82:83], v[116:117], v[114:115] op_sel_hi:[1,0,0]\n"
    "v_pk_fma_f32 v[84:85], v[84:85], v[116:117], v[114:115] op_sel_hi:[1,0,0]\n"
    "v_pk_fma_f32 v[86:87], v[86:87], v[116:117], v[114:115] op_sel_hi:[1,0,0]\n"
    "v_pk_fma_f32 v[88:89], v[88:89], v[116:117], v[114:115] op_sel_hi:[1,0,0]\n"
    "v_pk_fma_f32 v[90:91], v[90:91], v[116:117], v[114:115] op_sel_hi:[1,0,0]\n"
    "v_pk_fma_f32 v[92:93], v[92:93], v[116:117], v[114:115] op_sel_hi:[1,0,0]\n"
    "v_pk_fma_f32 v[94:95], v[94:95], v[116:117], v[114:115] op_sel_hi:[1,0,0]\n"
    "v_pk_fma_f32 v[96:97], v[96:97], v[116:117], v[114:115] op_sel_hi:[1,0,0]\n"
    "v_pk_fma_f32 v[98:99], v[98:99], v[116:117], v[114:115] op_sel_hi:[1,0,0]\n"
    "v_cvt_pk_bf16_f32 v100, v80, v81\n"
    "v_cvt_pk_bf16_f32 v101, v82, v83\n"
    "v_cvt_pk_bf16_f32 v102, v84, v85\n"
    "v_cvt_pk_bf16_f32 v103, v86, v87\n"
    "v_cvt_pk_bf16_f32 v104, v88, v89\n"
    "v_cvt_pk_bf16_f32 v105, v90, v91\n"
    "v_cvt_pk_bf16_f32 v106, v92, v93\n"
    "v_cvt_pk_bf16_f32 v107, v94, v95\n"
    "v_cvt_pk_bf16_f32 v108, v96, v97\n"
    "v_cvt_pk_bf16_f32 v109, v98, v99\n"
    "v_permlane16_swap_b32 v100, v102\n"
    "v_permlane16_swap_b32 v101, v103\n"
    "buffer_store_dwordx4 v[100:103], v4, s[44:47], s77 offen sc1\n"
    "v_permlane16_swap_b32 v104, v106\n"
    "v_permlane16_swap_b32 v105, v107\n"
    "buffer_store_dwordx4 v[104:107], v4, s[44:47], s78 offen sc1\n"
    "v_permlane16_swap_b32 v108, v110\n"
    "v_permlane16_swap_b32 v109, v111\n"
    "buffer_store_dwordx4 v[108:111], v5, s[44:47], s77 offen sc1\n"
    "s_add_u32 s68, s68, 1\n"
    "s_add_u32 s69, s68, 4\n"
    "s_and_b32 s75, s68, s58\n"
    "s_mul_i32 s77, s75, 81920\n"
    "s_add_u32 s77, s77, s59\n"
    "s_add_u32 s78, s77, s60\n"
    "s_and_b32 s73, s69, s55\n"
    "s_mul_i32 s73, s73, s54\n"
    "s_add_u32 s73, s73, s53\n"
    "s_add_u32 s74, s73, s56\n"
    "s_waitcnt vmcnt(27)\n"
    "v_mfma_f32_16x16x32_bf16 v[80:83], v[24:27], v[68:71], 0\n"
    "v_mfma_f32_16x16x32_bf16 v[84:87], v[28:31], v[68:71], 0\n"
    "s_waitcnt vmcnt(26)\n"
    "v_mfma_f32_16x16x32_bf16 v[88:91], v[24:27], v[72:75], 0\n"
    "v_mfma_f32_16x16x32_bf16 v[92:95], v[28:31], v[72:75], 0\n"
    "s_waitcnt vmcnt(25)\n"
    "v_mfma_f32_16x16x32_bf16 v[96:99], v[24:27], v[76:79], 0\n"
    "buffer_load_dwordx4 v[68:71], v6, s[40:43], s73 offen sc1\n"
    "buffer_load_dwordx4 v[72:75], v6, s[40:43], s74 offen sc1\n"
    "buffer_load_dwordx4 v[76:79], v7, s[40:43], s73 offen sc1\n"
    "v_pk_add_f32 v[112:113], v[80:81], v[82:83]\n"
    "v_pk_add_f32 v[114:115], v[84:85], v[86:87]\n"
    "v_pk_add_f32 v[112:113], v[112:113], v[88:89]\n"
    "v_pk_add_f32 v[114:115], v[114:115], v[90:91]\n"
    "v_pk_add_f32 v[112:113], v[112:113], v[92:93]\n"
    "v_pk_add_f32 v[114:115], v[114:115], v[94:95]\n"
    "v_pk_add_f32 v[112:113], v[112:113], v[96:97]\n"
    "v_pk_add_f32 v[114:115], v[114:115], v[98:99]\n"
    "v_pk_add_f32 v[112:113], v[112:113], v[114:115]\n"
    "v_add_f32 v112, v112, v113\n"
    "s_nop 1\n"
    "v_add_f32_dpp v14, v112, v112 row_ror:8 row_mask:0xf bank_mask:0xf\n"
    "v_add_f32_dpp v15, v112, v112 row_ror:8 row_mask:0xf bank_mask:0xf\n"
    "s_nop 1\n"
    "v_permlane16_swap_b32 v14, v15\n"
    "v_add_f32 v112, v14, v15\n"
    "v_add_f32 v14, v14, v15\n"
    "s_nop 1\n"
    "v_permlane32_swap_b32 v112, v14\n"
    "v_add_f32 v112, v112, v14\n"
    "v_mul_f32 v112, 0x3be38e39, v112\n"
    "v_mul_f32 v114, v112, v12\n"
    "v_pk_add_f32 v[80:81], v[80:81], v[112:113] op_sel_hi:[1,0] neg_lo:[0,1] neg_hi:[0,1]\n"
    "v_pk_add_f32 v[82:83], v[82:83], v[112:113] op_sel_hi:[1,0] neg_lo:[0,1] neg_hi:[0,1]\n"
    "v_pk_add_f32 v[84:85], v[84:85], v[112:113] op_sel_hi:[1,0] neg_lo:[0,1] neg_hi:[0,1]\n"
    "v_pk_add_f32 v[86:87], v[86:87], v[112:113] op_sel_hi:[1,0] neg_lo:[0,1] neg_hi:[0,1]\n"
    "v_pk_add_f32 v[88:89], v[88:89], v[112:113] op_sel_hi:[1,0] neg_lo:[0,1] neg_hi:[0,1]\n"
    "v_pk_add_f32 v[90:91], v[90:91], v[112:113] op_sel_hi:[1,0] neg_lo:[0,1] neg_hi:[0,1]\n"
    "v_pk_add_f32 v[92:93], v[92:93], v[112:113] op_sel_hi:[1,0] neg_lo:[0,1] neg_hi:[0,1]\n"
    "v_pk_add_f32 v[94:95], v[94:95], v[112:113] op_sel_hi:[1,0] neg_lo:[0,1] neg_hi:[0,1]\n"
    "v_pk_add_f32 v[96:97], v[96:97], v[114:115] op_sel_hi:[1,0] neg_lo:[0,1] neg_hi:[0,1]\n"
    "v_pk_add_f32 v[98:99], v[98:99], v[114:115] op_sel_hi:[1,0] neg_lo:[0,1] neg_hi:[0,1]\n"
    "v_pk_mul_f32 v[116:117], v[80:81], v[80:81]\n"
    "v_pk_mul_f32 v[118:119], v[82:83], v[82:83]\n"
    "v_pk_fma_f32 v[116:117], v[84:85], v[84:85], v[116:117]\n"
    "v_pk_fma_f32 v[118:119], v[86:87], v[86:87], v[118:119]\n"
    "v_pk_fma_f32 v[116:117], v[88:89], v[88:89], v[116:117]\n"
    "v_pk_fma_f32 v[118:119], v[90:91], v[90:91], v[118:119]\n"
    "v_pk_fma_f32 v[116:117], v[92:93], v[92:93], v[116:117]\n"
    "v_pk_fma_f32 v[118:119], v[94:95], v[94:95], v[118:119]\n"
    "v_pk_fma_f32 v[116:117], v[96:97], v[96:97], v[116:117]\n"
    "v_pk_fma_f32 v[118:119], v[98:99], v[98:99], v[118:119]\n"
    "v_pk_add_f32 v[116:117], v[116:117], v[118:119]\n"
    "v_add_f32 v113, v116, v117\n"
    "s_nop 1\n"
    "v_add_f32_dpp v14, v113, v113 row_ror:8 row_mask:0xf bank_mask:0xf\n"
    "v_add_f32_dpp v15, v113, v113 row_ror:8 row_mask:0xf bank_mask:0xf\n"
    "s_nop 1\n"
    "v_permlane16_swap_b32 v14, v15\n"
    "v_add_f32 v113, v14, v15\n"
    "v_add_f32 v14, v14, v15\n"
    "s_nop 1\n"
    "v_permlane32_swap_b32 v113, v14\n"
    "v_add_f32 v113, v113, v14\n"
    "ds_write_b64 v10, v[112:113]\n"
    "s_waitcnt vmcnt(17)\n"
    "s_add_u32 s75, s66, 1\n"
    "s_min_i32 s75, s75, s68\n"
    "s_sub_u32 s76, s68, 2\n"
    "s_max_i32 s75, s75, s76\n"
    "s_sub_u32 s75, s75, 1\n"
    "v_lshl_or_b32 v14, s75, 1, 1\n"
    "s_mov_b64 exec, s[64:65]\n"
    "buffer_atomic_smax v14, v13, s[36:39], 0 offen sc1\n"
    "s_mov_b64 exec, -1\n"
    "s_waitcnt lgkmcnt(0)\n"
    "s_barrier\n"
    "ds_read_b128 v[32:35], v11\n"
    "ds_read_b128 v[36:39], v11 offset:16\n"
    "v_xor_b32 v10, 256, v10\n"
    "v_xor_b32 v11, 256, v11\n"
    "s_waitcnt lgkmcnt(0)\n"
    "v_pk_add_f32 v[114:115], v[32:33], v[34:35]\n"
    "v_pk_add_f32 v[116:117], v[36:37], v[38:39]\n"
    "v_pk_add_f32 v[114:115], v[114:115], v[116:117]\n"
    "v_fmac_f32 v32, 0xbe800000, v114\n"
    "v_fmac_f32 v34, 0xbe800000, v114\n"
    "v_fmac_f32 v36, 0xbe800000, v114\n"
    "v_fmac_f32 v38, 0xbe800000, v114\n"
    "v_fmac_f32 v112, 0xbe800000, v114\n"
    "v_mul_f32 v116, v32, v32\n"
    "v_fmac_f32 v116, v34, v34\n"
    "v_fmac_f32 v116, v36, v36\n"
    "v_fmac_f32 v116, v38, v38\n"
    "v_fmac_f32 v115, 0x43100000, v116\n"
    "v_mov_b32 v116, 0x3727c5ac\n"
    "v_fmac_f32 v116, 0x3ae38e39, v115\n"
    "v_rsq_f32 v116, v116\n"
    "s_nop 0\n"
    "v_mul_f32 v114, v112, v116\n"
    "v_pk_fma_f32 v[80:81], v[80:81], v[116:117], v[114:115] op_sel_hi:[1,0,0]\n"
    "v_pk_fma_f32 v[82:83], v[82:83], v[116:117], v[114:115] op_sel_hi:[1,0,0]\n"
    "v_pk_fma_f32 v[84:85], v[84:85], v[116:117], v[114:115] op_sel_hi:[1,0,0]\n"
    "v_pk_fma_f32 v[86:87], v[86:87], v[116:117], v[114:115] op_sel_hi:[1,0,0]\n"
    "v_pk_fma_f32 v[88:89], v[88:89], v[116:117], v[114:115] op_sel_hi:[1,0,0]\n"
    "v_pk_fma_f32 v[90:91], v[90:91], v[116:117], v[114:115] op_sel_hi:[1,0,0]\n"
    "v_pk_fma_f32 v[92:93], v[92:93], v[116:117], v[114:115] op_sel_hi:[1,0,0]\n"
    "v_pk_fma_f32 v[94:95], v[94:95], v[116:117], v[114:115] op_sel_hi:[1,0,0]\n"
    "v_pk_fma_f32 v[96:97], v[96:97], v[116:117], v[114:115] op_sel_hi:[1,0,0]\n"
    "v_pk_fma_f32 v[98:99], v[98:99], v[116:117], v[114:115] op_sel_hi:[1,0,0]\n"
    "v_cvt_pk_bf16_f32 v100, v80, v81\n"
    "v_cvt_pk_bf16_f32 v101, v82, v83\n"
    "v_cvt_pk_bf16_f32 v102, v84, v85\n"
    "v_cvt_pk_bf16_f32 v103, v86, v87\n"
    "v_cvt_pk_bf16_f32 v104, v88, v89\n"
    "v_cvt_pk_bf16_f32 v105, v90, v91\n"
    "v_cvt_pk_bf16_f32 v106, v92, v93\n"
    "v_cvt_pk_bf16_f32 v107, v94, v95\n"
    "v_cvt_pk_bf16_f32 v108, v96, v97\n"
    "v_cvt_pk_bf16_f32 v109, v98, v99\n"
    "v_permlane16_swap_b32 v100, v102\n"
    "v_permlane16_swap_b32 v101, v103\n"
    "buffer_store_dwordx4 v[100:103], v4, s[44:47], s77 offen sc1\n"
    "v_permlane16_swap_b32 v104, v106\n"
    "v_permlane16_swap_b32 v105, v107\n"
    "buffer_store_dwordx4 v[104:107], v4, s[44:47], s78 offen sc1\n"
    "v_permlane16_swap_b32 v108, v110\n"
    "v_permlane16_swap_b32 v109, v111\n"
    "buffer_store_dwordx4 v[108:111], v5, s[44:47], s77 offen sc1\n"
    "s_add_u32 s68, s68, 1\n"
    "s_add_u32 s69, s68, 4\n"
    "s_and_b32 s75, s68, s58\n"
    "s_mul_i32 s77, s75, 81920\n"
    "s_add_u32 s77, s77, s59\n"
    "s_add_u32 s78, s77, s60\n"
    "s_and_b32 s73, s69, s55\n"
    "s_mul_i32 s73, s73, s54\n"
    "s_add_u32 s73, s73, s53\n"
    "s_add_u32 s74, s73, s56\n"
    "s_waitcnt vmcnt(27)\n"
    "v_mfma_f32_16x16x32_bf16 v[80:83], v[24:27], v[124:127], 0\n"
    "v_mfma_f32_16x16x32_bf16 v[84:87], v[28:31], v[124:127], 0\n"
    "s_waitcnt vmcnt(26)\n"
    "v_mfma_f32_16x16x32_bf16 v[88:91], v[24:27], v[128:131], 0\n"
    "v_mfma_f32_16x16x32_bf16 v[92:95], v[28:31], v[128:131], 0\n"
    "s_waitcnt vmcnt(25)\n"
    "v_mfma_f32_16x16x32_bf16 v[96:99], v[24:27], v[132:135], 0\n"
    "buffer_load_dwordx4 v[124:127], v6, s[40:43], s73 offen sc1\n"
    "buffer_load_dwordx4 v[128:131], v6, s[40:43], s74 offen sc1\n"
    "buffer_load_dwordx4 v[132:135], v7, s[40:43], s73 offen sc1\n"
    "v_pk_add_f32 v[112:113], v[80:81], v[82:83]\n"
    "v_pk_add_f32 v[114:115], v[84:85], v[86:87]\n"
    "v_pk_add_f32 v[112:113], v[112:113], v[88:89]\n"
    "v_pk_add_f32 v[114:115], v[114:115], v[90:91]\n"
    "v_pk_add_f32 v[112:113], v[112:113], v[92:93]\n"
    "v_pk_add_f32 v[114:115], v[114:115], v[94:95]\n"
    "v_pk_add_f32 v[112:113], v[112:113], v[96:97]\n"
    "v_pk_add_f32 v[114:115], v[114:115], v[98:99]\n"
    "v_pk_add_f32 v[112:113], v[112:113], v[114:115]\n"
    "v_add_f32 v112, v112, v113\n"
    "s_nop 1\n"
    "v_add_f32_dpp v14, v112, v112 row_ror:8 row_mask:0xf bank_mask:0xf\n"
    "v_add_f32_dpp v15, v112, v112 row_ror:8 row_mask:0xf bank_mask:0xf\n"
    "s_nop 1\n"
    "v_permlane16_swap_b32 v14, v15\n"
    "v_add_f32 v112, v14, v15\n"
    "v_add_f32 v14, v14, v15\n"
    "s_nop 1\n"
    "v_permlane32_swap_b32 v112, v14\n"
    "v_add_f32 v112, v112, v14\n"
    "v_mul_f32 v112, 0x3be38e39, v112\n"
    "v_mul_f32 v114, v112, v12\n"
    "v_pk_add_f32 v[80:81], v[80:81], v[112:113] op_sel_hi:[1,0] neg_lo:[0,1] neg_hi:[0,1]\n"
    "v_pk_add_f32 v[82:83], v[82:83], v[112:113] op_sel_hi:[1,0] neg_lo:[0,1] neg_hi:[0,1]\n"
    "v_pk_add_f32 v[84:85], v[84:85], v[112:113] op_sel_hi:[1,0] neg_lo:[0,1] neg_hi:[0,1]\n"
    "v_pk_add_f32 v[86:87], v[86:87], v[112:113] op_sel_hi:[1,0] neg_lo:[0,1] neg_hi:[0,1]\n"
    "v_pk_add_f32 v[88:89], v[88:89], v[112:113] op_sel_hi:[1,0] neg_lo:[0,1] neg_hi:[0,1]\n"
    "v_pk_add_f32 v[90:91], v[90:91], v[112:113] op_sel_hi:[1,0] neg_lo:[0,1] neg_hi:[0,1]\n"
    "v_pk_add_f32 v[92:93], v[92:93], v[112:113] op_sel_hi:[1,0] neg_lo:[0,1] neg_hi:[0,1]\n"
    "v_pk_add_f32 v[94:95], v[94:95], v[112:113] op_sel_hi:[1,0] neg_lo:[0,1] neg_hi:[0,1]\n"
    "v_pk_add_f32 v[96:97], v[96:97], v[114:115] op_sel_hi:[1,0] neg_lo:[0,1] neg_hi:[0,1]\n"
    "v_pk_add_f32 v[98:99], v[98:99], v[114:115] op_sel_hi:[1,0] neg_lo:[0,1] neg_hi:[0,1]\n"
    "v_pk_mul_f32 v[116:117], v[80:81], v[80:81]\n"
    "v_pk_mul_f32 v[118:119], v[82:83], v[82:83]\n"
    "v_pk_fma_f32 v[116:117], v[84:85], v[84:85], v[116:117]\n"
    "v_pk_fma_f32 v[118:119], v[86:87], v[86:87], v[118:119]\n"
    "v_pk_fma_f32 v[116:117], v[88:89], v[88:89], v[116:117]\n"
    "v_pk_fma_f32 v[118:119], v[90:91], v[90:91], v[118:119]\n"
    "v_pk_fma_f32 v[116:117], v[92:93], v[92:93], v[116:117]\n"
    "v_pk_fma_f32 v[118:119], v[94:95], v[94:95], v[118:119]\n"
    "v_pk_fma_f32 v[116:117], v[96:97], v[96:97], v[116:117]\n"
    "v_pk_fma_f32 v[118:119], v[98:99], v[98:99], v[118:119]\n"
    "v_pk_add_f32 v[116:117], v[116:117], v[118:119]\n"
    "v_add_f32 v113, v116, v117\n"
    "s_nop 1\n"
    "v_add_f32_dpp v14, v113, v113 row_ror:8 row_mask:0xf bank_mask:0xf\n"
    "v_add_f32_dpp v15, v113, v113 row_ror:8 row_mask:0xf bank_mask:0xf\n"
    "s_nop 1\n"
    "v_permlane16_swap_b32 v14, v15\n"
    "v_add_f32 v113, v14, v15\n"
    "v_add_f32 v14, v14, v15\n"
    "s_nop 1\n"
    "v_permlane32_swap_b32 v113, v14\n"
    "v_add_f32 v113, v113, v14\n"
    "ds_write_b64 v10, v[112:113]\n"
    "s_waitcnt vmcnt(17)\n"
    "s_add_u32 s75, s66, 1\n"
    "s_min_i32 s75, s75, s68\n"
    "s_sub_u32 s76, s68, 2\n"
    "s_max_i32 s75, s75, s76\n"
    "s_sub_u32 s75, s75, 1\n"
    "v_lshl_or_b32 v14, s75, 1, 1\n"
    "s_mov_b64 exec, s[64:65]\n"
    "buffer_atomic_smax v14, v13, s[36:39], 0 offen sc1\n"
    "s_mov_b64 exec, -1\n"
    "s_waitcnt lgkmcnt(0)\n"
    "s_barrier\n"
    "ds_read_b128 v[32:35], v11\n"
    "ds_read_b128 v[36:39], v11 offset:16\n"
    "v_xor_b32 v10, 256, v10\n"
    "v_xor_b32 v11, 256, v11\n"
    "s_waitcnt lgkmcnt(0)\n"
    "v_pk_add_f32 v[114:115], v[32:33], v[34:35]\n"
    "v_pk_add_f32 v[116:117], v[36:37], v[38:39]\n"
    "v_pk_add_f32 v[114:115], v[114:115], v[116:117]\n"
    "v_fmac_f32 v32, 0xbe800000, v114\n"
    "v_fmac_f32 v34, 0xbe800000, v114\n"
    "v_fmac_f32 v36, 0xbe800000, v114\n"
    "v_fmac_f32 v38, 0xbe800000, v114\n"
    "v_fmac_f32 v112, 0xbe800000, v114\n"
    "v_mul_f32 v116, v32, v32\n"
    "v_fmac_f32 v116, v34, v34\n"
    "v_fmac_f32 v116, v36, v36\n"
    "v_fmac_f32 v116, v38, v38\n"
    "v_fmac_f32 v115, 0x43100000, v116\n"
    "v_mov_b32 v116, 0x3727c5ac\n"
    "v_fmac_f32 v116, 0x3ae38e39, v115\n"
    "v_rsq_f32 v116, v116\n"
    "s_nop 0\n"
    "v_mul_f32 v114, v112, v116\n"
    "v_pk_fma_f32 v[80:81], v[80:81], v[116:117], v[114:115] op_sel_hi:[1,0,0]\n"
    "v_pk_fma_f32 v[82:83], v[82:83], v[116:117], v[114:115] op_sel_hi:[1,0,0]\n"
    "v_pk_fma_f32 v[84:85], v[84:85], v[116:117], v[114:115] op_sel_hi:[1,0,0]\n"
    "v_pk_fma_f32 v[86:87], v[86:87], v[116:117], v[114:115] op_sel_hi:[1,0,0]\n"
    "v_pk_fma_f32 v[88:89], v[88:89], v[116:117], v[114:115] op_sel_hi:[1,0,0]\n"
    "v_pk_fma_f32 v[90:91], v[90:91], v[116:117], v[114:115] op_sel_hi:[1,0,0]\n"
    "v_pk_fma_f32 v[92:93], v[92:93], v[116:117], v[114:115] op_sel_hi:[1,0,0]\n"
    "v_pk_fma_f32 v[94:95], v[94:95], v[116:117], v[114:115] op_sel_hi:[1,0,0]\n"
    "v_pk_fma_f32 v[96:97], v[96:97], v[116:117], v[114:115] op_sel_hi:[1,0,0]\n"
    "v_pk_fma_f32 v[98:99], v[98:99], v[116:117], v[114:115] op_sel_hi:[1,0,0]\n"
    "v_cvt_pk_bf16_f32 v100, v80, v81\n"
    "v_cvt_pk_bf16_f32 v101, v82, v83\n"
    "v_cvt_pk_bf16_f32 v102, v84, v85\n"
    "v_cvt_pk_bf16_f32 v103, v86, v87\n"
    "v_cvt_pk_bf16_f32 v104, v88, v89\n"
    "v_cvt_pk_bf16_f32 v105, v90, v91\n"
    "v_cvt_pk_bf16_f32 v106, v92, v93\n"
    "v_cvt_pk_bf16_f32 v107, v94, v95\n"
    "v_cvt_pk_bf16_f32 v108, v96, v97\n"
    "v_cvt_pk_bf16_f32 v109, v98, v99\n"
    "v_permlane16_swap_b32 v100, v102\n"
    "v_permlane16_swap_b32 v101, v103\n"
    "buffer_store_dwordx4 v[100:103], v4, s[44:47], s77 offen sc1\n"
    "v_permlane16_swap_b32 v104, v106\n"
    "v_permlane16_swap_b32 v105, v107\n"
    "buffer_store_dwordx4 v[104:107], v4, s[44:47], s78 offen sc1\n"
    "v_permlane16_swap_b32 v108, v110\n"
    "v_permlane16_swap_b32 v109, v111\n"
    "buffer_store_dwordx4 v[108:111], v5, s[44:47], s77 offen sc1\n"
    "s_cmp_eq_u32 s68, s67\n"
    "s_cbranch_scc1 .LrLN0_fin_%=\n"
    "s_branch .LrLN0_top_%=\n"
    ".LrLN0_LOOPE_%=:\n"
    ".LrLN0_fin_%=:\n"
    "s_waitcnt vmcnt(0)\n"
    "s_mov_b32 s68, 63\n"
    "v_lshl_or_b32 v14, s68, 1, 1\n"
    "s_mov_b64 exec, s[64:65]\n"
    "buffer_atomic_smax v14, v13, s[36:39], 0 offen sc1\n"
    "s_mov_b64 exec, -1\n"
    "s_waitcnt vmcnt(0)\n"
    "s_barrier\n"
    "s_branch .LrLN0_FIN_%=\n"
    ".LrLN0_first_%=:\n"
    "s_waitcnt vmcnt(0)\n"
    "v_lshl_or_b32 v14, s68, 1, 1\n"
    "s_mov_b64 exec, s[64:65]\n"
    "buffer_atomic_smax v14, v13, s[36:39], 0 offen sc1\n"
    "s_mov_b64 exec, -1\n"
    "s_branch .LrLN0_first1_%=\n"
    ".LrLN0_FIN_%=:\n"
  );
}
DEVI void role_mid(v16u sa, v16u sb, v16u va, v16u vb) {
  RUN_RED(
    "s_nop 4\n"
    "s_mov_b32 s50, 0\n"
    "s_mov_b32 s51, 0x80000000\n"
    "s_getpc_b64 s[84:85]\n"
    ".LrMID_PC_%=:\n"
    "s_and_b32 s85, s85, 0xffff\n"
    "s_mov_b32 s86, .LrMID_FIN_%=-.LrMID_PC_%=\n"
    "s_mov_b32 s87, 0x20000\n"
    "v_mbcnt_lo_u32_b32 v134, -1, 0\n"
    "v_mbcnt_hi_u32_b32 v134, -1, v134\n"
    "v_lshlrev_b32 v134, 7, v134\n"
    "buffer_load_dword v135, v134, s[84:87], 0 offen\n"
    "v_add_u32 v134, 8192, v134\n"
    "buffer_load_dword v135, v134, s[84:87], 0 offen\n"
    "s_mov_b32 s69, 0\n"
    "v_mov_b32 v122, -1\n"
    "v_mov_b32 v123, -1\n"
    "s_mov_b32 s70, 262144\n"
    ".LrMID_par_%=:\n"
    "buffer_load_dwordx2 v[14:15], off, s[36:39], 0 sc1\n"
    "s_waitcnt vmcnt(0)\n"
    "v_readfirstlane_b32 s75, v14\n"
    "v_readfirstlane_b32 s76, v15\n"
    "s_xor_b32 s75, s75, s48\n"
    "s_xor_b32 s76, s76, s49\n"
    "s_cselect_b32 s76, 2, 0\n"
    "s_or_b32 s76, s76, s75\n"
    "s_cmp_lt_u32 s76, 2\n"
    "s_cbranch_scc1 .LrMID_par1_%=\n"
    "s_sleep 4\n"
    "s_sub_u32 s70, s70, 1\n"
    "s_cbranch_scc0 .LrMID_par_%=\n"
    "s_mov_b32 s70, 0\n"
    ".LrMID_par1_%=:\n"
    "s_and_b32 s75, s75, 1\n"
    "s_lshl_b32 s75, s75, 13\n"
    "s_add_u32 s36, s36, s75\n"
    "s_addc_u32 s37, s37, 0\n"
    "s_and_b32 s76, s75, s61\n"
    "s_add_u32 s40, s40, s76\n"
    "s_addc_u32 s41, s41, 0\n"
    "s_and_b32 s76, s75, s63\n"
    "s_add_u32 s44, s44, s76\n"
    "s_addc_u32 s45, s45, 0\n"
    "s_branch .LrMID_block_%=\n"
    ".LrMID_LOOPB_%=:\n"
    ".LrMID_top_%=:\n"
    "s_mov_b32 s68, s69\n"
    "s_add_u32 s69, s68, 1\n"
    "s_and_b32 s75, s68, s58\n"
    "s_mul_i32 s77, s75, 4194304\n"
    "s_add_u32 s77, s77, s59\n"
    "s_add_u32 s78, s77, s60\n"
    "s_and_b32 s71, s69, 7\n"
    "s_mul_i32 s71, s71, 4194304\n"
    "s_add_u32 s71, s71, s52\n"
    "s_add_u32 s72, s71, 4096\n"
    "s_and_b32 s73, s69, s55\n"
    "s_mul_i32 s73, s73, s54\n"
    "s_add_u32 s73, s73, s53\n"
    "s_add_u32 s74, s73, s56\n"
    "s_mul_i32 s75, s75, 4194304\n"
    "s_add_u32 s75, s75, s57\n"
    "s_add_u32 s76, s75, 1024\n"
    "s_waitcnt vmcnt(12)\n"
    "v_mfma_f32_16x16x32_bf16 v[80:83], v[16:19], v[32:35], 0\n"
    "s_waitcnt vmcnt(11)\n"
    "v_mfma_f32_16x16x32_bf16 v[80:83], v[20:23], v[36:39], v[80:83]\n"
    "s_waitcnt vmcnt(10)\n"
    "v_mfma_f32_16x16x32_bf16 v[84:87], v[16:19], v[40:43], 0\n"
    "s_waitcnt vmcnt(9)\n"
    "v_mfma_f32_16x16x32_bf16 v[84:87], v[20:23], v[44:47], v[84:87]\n"
    "s_waitcnt vmcnt(8)\n"
    "v_mfma_f32_16x16x32_bf16 v[88:91], v[16:19], v[48:51], 0\n"
    "s_waitcnt vmcnt(7)\n"
    "v_mfma_f32_16x16x32_bf16 v[88:91], v[20:23], v[52:55], v[88:91]\n"
    "s_waitcnt vmcnt(6)\n"
    "v_mfma_f32_16x16x32_bf16 v[92:95], v[16:19], v[56:59], 0\n"
    "s_waitcnt vmcnt(5)\n"
    "v_mfma_f32_16x16x32_bf16 v[92:95], v[20:23], v[60:63], v[92:95]\n"
    "s_waitcnt vmcnt(4)\n"
    "v_mfma_f32_16x16x32_bf16 v[96:99], v[16:19], v[64:67], 0\n"
    "s_waitcnt vmcnt(3)\n"
    "v_mfma_f32_16x16x32_bf16 v[80:83], v[24:27], v[68:71], v[80:83]\n"
    "v_mfma_f32_16x16x32_bf16 v[84:87], v[28:31], v[68:71], v[84:87]\n"
    "s_waitcnt vmcnt(2)\n"
    "v_mfma_f32_16x16x32_bf16 v[88:91], v[24:27], v[72:75], v[88:91]\n"
    "v_mfma_f32_16x16x32_bf16 v[92:95], v[28:31], v[72:75], v[92:95]\n"
    "s_waitcnt vmcnt(1)\n"
    "v_mfma_f32_16x16x32_bf16 v[96:99], v[24:27], v[76:79], v[96:99]\n"
    "v_pk_add_f32 v[112:113], v[80:81], v[82:83]\n"
    "v_pk_add_f32 v[114:115], v[84:85], v[86:87]\n"
    "v_pk_add_f32 v[112:113], v[112:113], v[88:89]\n"
    "v_pk_add_f32 v[114:115], v[114:115], v[90:91]\n"
    "v_pk_add_f32 v[112:113], v[112:113], v[92:93]\n"
    "v_pk_add_f32 v[114:115], v[114:115], v[94:95]\n"
    "s_nop 1\n"
    "v_pk_add_f32 v[112:113], v[112:113], v[96:97]\n"
    "v_pk_add_f32 v[114:115], v[114:115], v[98:99]\n"
    "v_pk_add_f32 v[112:113], v[112:113], v[114:115]\n"
    "v_add_f32 v112, v112, v113\n"
    "v_cvt_pk_bf16_f32 v100, v80, v81\n"
    "v_cvt_pk_bf16_f32 v101, v82, v83\n"
    "v_cvt_pk_bf16_f32 v102, v84, v85\n"
    "v_cvt_pk_bf16_f32 v103, v86, v87\n"
    "v_cvt_pk_bf16_f32 v104, v88, v89\n"
    "v_cvt_pk_bf16_f32 v105, v90, v91\n"
    "v_cvt_pk_bf16_f32 v106, v92, v93\n"
    "v_cvt_pk_bf16_f32 v107, v94, v95\n"
    "v_cvt_pk_bf16_f32 v108, v96, v97\n"
    "v_cvt_pk_bf16_f32 v109, v98, v99\n"
    "v_add_f32_dpp v14, v112, v112 row_ror:8 row_mask:0xf bank_mask:0xf\n"
    "v_add_f32_dpp v15, v112, v112 row_ror:8 row_mask:0xf bank_mask:0xf\n"
    "s_nop 1\n"
    "v_permlane16_swap_b32 v14, v15\n"
    "v_add_f32 v112, v14, v15\n"
    "v_add_f32 v14, v14, v15\n"
    "s_nop 1\n"
    "v_permlane32_swap_b32 v112, v14\n"
    "v_add_f32 v112, v112, v14\n"
    "v_mul_f32 v112, 0x3be38e39, v112\n"
    "v_mul_f32 v114, v112, v12\n"
    "v_pk_add_f32 v[80:81], v[80:81], v[112:113] op_sel_hi:[1,0] neg_lo:[0,1] neg_hi:[0,1]\n"
    "v_pk_add_f32 v[82:83], v[82:83], v[112:113] op_sel_hi:[1,0] neg_lo:[0,1] neg_hi:[0,1]\n"
    "v_pk_add_f32 v[84:85], v[84:85], v[112:113] op_sel_hi:[1,0] neg_lo:[0,1] neg_hi:[0,1]\n"
    "v_pk_add_f32 v[86:87], v[86:87], v[112:113] op_sel_hi:[1,0] neg_lo:[0,1] neg_hi:[0,1]\n"
    "v_pk_add_f32 v[88:89], v[88:89], v[112:113] op_sel_hi:[1,0] neg_lo:[0,1] neg_hi:[0,1]\n"
    "v_pk_add_f32 v[90:91], v[90:91], v[112:113] op_sel_hi:[1,0] neg_lo:[0,1] neg_hi:[0,1]\n"
    "v_pk_add_f32 v[92:93], v[92:93], v[112:113] op_sel_hi:[1,0] neg_lo:[0,1] neg_hi:[0,1]\n"
    "v_pk_add_f32 v[94:95], v[94:95], v[112:113] op_sel_hi:[1,0] neg_lo:[0,1] neg_hi:[0,1]\n"
    "v_pk_add_f32 v[96:97], v[96:97], v[114:115] op_sel_hi:[1,0] neg_lo:[0,1] neg_hi:[0,1]\n"
    "v_pk_add_f32 v[98:99], v[98:99], v[114:115] op_sel_hi:[1,0] neg_lo:[0,1] neg_hi:[0,1]\n"
    "v_pk_mul_f32 v[116:117], v[80:81], v[80:81]\n"
    "v_pk_mul_f32 v[118:119], v[82:83], v[82:83]\n"
    "v_pk_fma_f32 v[116:117], v[84:85], v[84:85], v[116:117]\n"
    "v_pk_fma_f32 v[118:119], v[86:87], v[86:87], v[118:119]\n"
    "v_pk_fma_f32 v[116:117], v[88:89], v[88:89], v[116:117]\n"
    "v_pk_fma_f32 v[118:119], v[90:91], v[90:91], v[118:119]\n"
    "v_pk_fma_f32 v[116:117], v[92:93], v[92:93], v[116:117]\n"
    "v_pk_fma_f32 v[118:119], v[94:95], v[94:95], v[118:119]\n"
    "v_pk_fma_f32 v[116:117], v[96:97], v[96:97], v[116:117]\n"
    "v_pk_fma_f32 v[118:119], v[98:99], v[98:99], v[118:119]\n"
    "v_pk_add_f32 v[116:117], v[116:117], v[118:119]\n"
    "v_add_f32 v113, v116, v117\n"
    "s_nop 1\n"
    "v_add_f32_dpp v14, v113, v113 row_ror:8 row_mask:0xf bank_mask:0xf\n"
    "v_add_f32_dpp v15, v113, v113 row_ror:8 row_mask:0xf bank_mask:0xf\n"
    "s_nop 1\n"
    "v_permlane16_swap_b32 v14, v15\n"
    "v_add_f32 v113, v14, v15\n"
    "v_add_f32 v14, v14, v15\n"
    "s_nop 1\n"
    "v_permlane32_swap_b32 v113, v14\n"
    "v_add_f32 v113, v113, v14\n"
    "ds_write_b64 v10, v[112:113]\n"
    "v_add_u32 v120, v122, v1\n"
    "v_ashrrev_i32 v120, 1, v120\n"
    "v_cmp_gt_i32 vcc, s69, v120\n"
    "s_nop 0\n"
    "s_or_b64 exec, vcc, s[50:51]\n"
    "buffer_atomic_smax v122, v0, s[36:39], 0 offen sc0 sc1\n"
    "s_mov_b64 exec, -1\n"
    "s_nop 0\n"
    "v_permlane16_swap_b32 v100, v102\n"
    "v_permlane16_swap_b32 v101, v103\n"
    "v_permlane16_swap_b32 v104, v106\n"
    "v_permlane16_swap_b32 v105, v107\n"
    "v_permlane16_swap_b32 v108, v110\n"
    "v_permlane16_swap_b32 v109, v111\n"
    "s_waitcnt lgkmcnt(0)\n"
    "s_barrier\n"
    "ds_read_b128 v[32:35], v11\n"
    "ds_read_b128 v[36:39], v11 offset:16\n"
    "buffer_store_dwordx4 v[100:103], v8, s[36:39], s75 offen sc1\n"
    "buffer_store_dwordx4 v[104:107], v8, s[36:39], s76 offen sc1\n"
    "buffer_store_dwordx4 v[108:111], v9, s[36:39], s75 offen sc1\n"
    "v_xor_b32 v10, 256, v10\n"
    "v_xor_b32 v11, 256, v11\n"
    "s_waitcnt lgkmcnt(0)\n"
    "v_pk_add_f32 v[114:115], v[32:33], v[34:35]\n"
    "v_pk_add_f32 v[116:117], v[36:37], v[38:39]\n"
    "v_pk_add_f32 v[114:115], v[114:115], v[116:117]\n"
    "v_fmac_f32 v32, 0xbe800000, v114\n"
    "v_fmac_f32 v34, 0xbe800000, v114\n"
    "v_fmac_f32 v36, 0xbe800000, v114\n"
    "v_fmac_f32 v38, 0xbe800000, v114\n"
    "v_fmac_f32 v112, 0xbe800000, v114\n"
    "v_mul_f32 v116, v32, v32\n"
    "v_fmac_f32 v116, v34, v34\n"
    "v_fmac_f32 v116, v36, v36\n"
    "v_fmac_f32 v116, v38, v38\n"
    "v_fmac_f32 v115, 0x43100000, v116\n"
    "v_mov_b32 v116, 0x3727c5ac\n"
    "v_fmac_f32 v116, 0x3ae38e39, v115\n"
    "v_rsq_f32 v116, v116\n"
    "s_nop 0\n"
    "v_mul_f32 v114, v112, v116\n"
    "v_pk_fma_f32 v[80:81], v[80:81], v[116:117], v[114:115] op_sel_hi:[1,0,0]\n"
    "v_pk_fma_f32 v[82:83], v[82:83], v[116:117], v[114:115] op_sel_hi:[1,0,0]\n"
    "v_pk_fma_f32 v[84:85], v[84:85], v[116:117], v[114:115] op_sel_hi:[1,0,0]\n"
    "v_pk_fma_f32 v[86:87], v[86:87], v[116:117], v[114:115] op_sel_hi:[1,0,0]\n"
    "v_pk_fma_f32 v[88:89], v[88:89], v[116:117], v[114:115] op_sel_hi:[1,0,0]\n"
    "v_pk_fma_f32 v[90:91], v[90:91], v[116:117], v[114:115] op_sel_hi:[1,0,0]\n"
    "v_pk_fma_f32 v[92:93], v[92:93], v[116:117], v[114:115] op_sel_hi:[1,0,0]\n"
    "v_pk_fma_f32 v[94:95], v[94:95], v[116:117], v[114:115] op_sel_hi:[1,0,0]\n"
    "v_pk_fma_f32 v[96:97], v[96:97], v[116:117], v[114:115] op_sel_hi:[1,0,0]\n"
    "v_pk_fma_f32 v[98:99], v[98:99], v[116:117], v[114:115] op_sel_hi:[1,0,0]\n"
    "v_cvt_pk_bf16_f32 v100, v80, v81\n"
    "v_cvt_pk_bf16_f32 v101, v82, v83\n"
    "v_cvt_pk_bf16_f32 v102, v84, v85\n"
    "v_cvt_pk_bf16_f32 v103, v86, v87\n"
    "v_cvt_pk_bf16_f32 v104, v88, v89\n"
    "v_cvt_pk_bf16_f32 v105, v90, v91\n"
    "v_cvt_pk_bf16_f32 v106, v92, v93\n"
    "v_cvt_pk_bf16_f32 v107, v94, v95\n"
    "v_cvt_pk_bf16_f32 v108, v96, v97\n"
    "v_cvt_pk_bf16_f32 v109, v98, v99\n"
    "v_permlane16_swap_b32 v100, v102\n"
    "v_permlane16_swap_b32 v101, v103\n"
    "buffer_store_dwordx4 v[100:103], v4, s[44:47], s77 offen sc1\n"
    "v_permlane16_swap_b32 v104, v106\n"
    "v_permlane16_swap_b32 v105, v107\n"
    "buffer_store_dwordx4 v[104:107], v4, s[44:47], s78 offen sc1\n"
    "v_permlane16_swap_b32 v108, v110\n"
    "v_permlane16_swap_b32 v109, v111\n"
    "buffer_store_dwordx4 v[108:111], v5, s[44:47], s77 offen sc1\n"
    "s_cmp_eq_u32 s69, 64\n"
    "s_cbranch_scc1 .LrMID_fin_%=\n"
    "s_waitcnt vmcnt(6)\n"
    "v_add_u32 v120, v122, v1\n"
    "v_ashrrev_i32 v120, 1, v120\n"
    "v_cmp_gt_i32 vcc, s69, v120\n"
    "s_cbranch_vccnz .LrMID_idle_%=\n"
    "buffer_load_dwordx4 v[32:35], v2, s[36:39], s71 offen sc1\n"
    "buffer_load_dwordx4 v[36:39], v2, s[36:39], s71 offen offset:512 sc1\n"
    "buffer_load_dwordx4 v[40:43], v2, s[36:39], s71 offen offset:2048 sc1\n"
    "buffer_load_dwordx4 v[44:47], v2, s[36:39], s71 offen offset:2560 sc1\n"
    "buffer_load_dwordx4 v[48:51], v2, s[36:39], s72 offen sc1\n"
    "buffer_load_dwordx4 v[52:55], v2, s[36:39], s72 offen offset:512 sc1\n"
    "buffer_load_dwordx4 v[56:59], v2, s[36:39], s72 offen offset:2048 sc1\n"
    "buffer_load_dwordx4 v[60:63], v2, s[36:39], s72 offen offset:2560 sc1\n"
    "buffer_load_dwordx4 v[64:67], v3, s[36:39], s71 offen sc1\n"
    "buffer_load_dwordx4 v[68:71], v6, s[40:43], s73 offen sc1\n"
    "buffer_load_dwordx4 v[72:75], v6, s[40:43], s74 offen sc1\n"
    "buffer_load_dwordx4 v[76:79], v7, s[40:43], s73 offen sc1\n"
    "s_waitcnt vmcnt(12)\n"
    "v_lshl_or_b32 v14, s68, 1, 1\n"
    "s_mov_b64 exec, s[64:65]\n"
    "buffer_atomic_smax v14, v13, s[36:39], 0 offen sc1\n"
    "s_mov_b64 exec, -1\n"
    "s_branch .LrMID_top_%=\n"
    ".LrMID_LOOPE_%=:\n"
    ".LrMID_fin_%=:\n"
    "s_waitcnt vmcnt(0)\n"
    "s_mov_b32 s68, 63\n"
    "v_lshl_or_b32 v14, s68, 1, 1\n"
    "s_mov_b64 exec, s[64:65]\n"
    "buffer_atomic_smax v14, v13, s[36:39], 0 offen sc1\n"
    "s_mov_b64 exec, -1\n"
    "s_waitcnt vmcnt(0)\n"
    "s_barrier\n"
    "s_branch .LrMID_FIN_%=\n"
    ".LrMID_idle_%=:\n"
    "v_add_u32 v120, v123, v1\n"
    "v_ashrrev_i32 v120, 1, v120\n"
    "v_cmp_gt_i32 vcc, s69, v120\n"
    "s_nop 0\n"
    "s_or_b64 exec, vcc, s[50:51]\n"
    "buffer_atomic_smax v123, v0, s[36:39], 0 offen sc0 sc1\n"
    "s_mov_b64 exec, -1\n"
    "s_waitcnt vmcnt(1)\n"
    "v_lshl_or_b32 v14, s68, 1, 1\n"
    "s_mov_b64 exec, s[64:65]\n"
    "buffer_atomic_smax v14, v13, s[36:39], 0 offen sc1\n"
    "s_mov_b64 exec, -1\n"
    ".LrMID_block_%=:\n"
    "v_add_u32 v120, v122, v1\n"
    "v_ashrrev_i32 v120, 1, v120\n"
    "v_cmp_gt_i32 vcc, s69, v120\n"
    "s_nop 0\n"
    "s_or_b64 exec, vcc, s[50:51]\n"
    "buffer_atomic_smax v122, v0, s[36:39], 0 offen sc0 sc1\n"
    "s_mov_b64 exec, -1\n"
    "s_sleep 3\n"
    "s_waitcnt vmcnt(1)\n"
    "v_add_u32 v120, v123, v1\n"
    "v_ashrrev_i32 v120, 1, v120\n"
    "v_cmp_gt_i32 vcc, s69, v120\n"
    "s_cbranch_vccz .LrMID_go_%=\n"
    "v_add_u32 v120, v123, v1\n"
    "v_ashrrev_i32 v120, 1, v120\n"
    "v_cmp_gt_i32 vcc, s69, v120\n"
    "s_nop 0\n"
    "s_or_b64 exec, vcc, s[50:51]\n"
    "buffer_atomic_smax v123, v0, s[36:39], 0 offen sc0 sc1\n"
    "s_mov_b64 exec, -1\n"
    "s_sleep 3\n"
    "s_waitcnt vmcnt(1)\n"
    "v_add_u32 v120, v122, v1\n"
    "v_ashrrev_i32 v120, 1, v120\n"
    "v_cmp_gt_i32 vcc, s69, v120\n"
    "s_cbranch_vccz .LrMID_go_%=\n"
    "s_sub_u32 s70, s70, 1\n"
    "s_cbranch_scc0 .LrMID_block_%=\n"
    "s_mov_b32 s70, 0\n"
    ".LrMID_go_%=:\n"
    "s_and_b32 s71, s69, 7\n"
    "s_mul_i32 s71, s71, 4194304\n"
    "s_add_u32 s71, s71, s52\n"
    "s_add_u32 s72, s71, 4096\n"
    "s_and_b32 s73, s69, s55\n"
    "s_mul_i32 s73, s73, s54\n"
    "s_add_u32 s73, s73, s53\n"
    "s_add_u32 s74, s73, s56\n"
    "buffer_load_dwordx4 v[32:35], v2, s[36:39], s71 offen sc1\n"
    "buffer_load_dwordx4 v[36:39], v2, s[36:39], s71 offen offset:512 sc1\n"
    "buffer_load_dwordx4 v[40:43], v2, s[36:39], s71 offen offset:2048 sc1\n"
    "buffer_load_dwordx4 v[44:47], v2, s[36:39], s71 offen offset:2560 sc1\n"
    "buffer_load_dwordx4 v[48:51], v2, s[36:39], s72 offen sc1\n"
    "buffer_load_dwordx4 v[52:55], v2, s[36:39], s72 offen offset:512 sc1\n"
    "buffer_load_dwordx4 v[56:59], v2, s[36:39], s72 offen offset:2048 sc1\n"
    "buffer_load_dwordx4 v[60:63], v2, s[36:39], s72 offen offset:2560 sc1\n"
    "buffer_load_dwordx4 v[64:67], v3, s[36:39], s71 offen sc1\n"
    "buffer_load_dwordx4 v[68:71], v6, s[40:43], s73 offen sc1\n"
    "buffer_load_dwordx4 v[72:75], v6, s[40:43], s74 offen sc1\n"
    "buffer_load_dwordx4 v[76:79], v7, s[40:43], s73 offen sc1\n"
    "buffer_load_dword v121, v7, s[40:43], s73 offen\n"
    "s_branch .LrMID_top_%=\n"
    ".LrMID_FIN_%=:\n"
  );
}
DEVI void role_fin(const Params& p, int q, u32 dbg, u64 tag) {
  v16u sa, sb, va, vb;
  red_args<2>(p, 1, q, dbg, tag, sa, sb, va, vb);
  RUN_RED(
    "s_nop 4\n"
    "s_mov_b32 s50, 0\n"
    "s_mov_b32 s51, 0x80000000\n"
    "s_getpc_b64 s[84:85]\n"
    ".LrFIN_PC_%=:\n"
    "s_and_b32 s85, s85, 0xffff\n"
    "s_mov_b32 s86, .LrFIN_FIN_%=-.LrFIN_PC_%=\n"
    "s_mov_b32 s87, 0x20000\n"
    "v_mbcnt_lo_u32_b32 v134, -1, 0\n"
    "v_mbcnt_hi_u32_b32 v134, -1, v134\n"
    "v_lshlrev_b32 v134, 7, v134\n"
    "buffer_load_dword v135, v134, s[84:87], 0 offen\n"
    "v_add_u32 v134, 8192, v134\n"
    "buffer_load_dword v135, v134, s[84:87], 0 offen\n"
    "s_mov_b32 s69, 0\n"
    "v_mov_b32 v122, -1\n"
    "v_mov_b32 v123, -1\n"
    "s_mov_b32 s70, 262144\n"
    ".LrFIN_par_%=:\n"
    "buffer_load_dwordx2 v[14:15], off, s[36:39], 0 sc1\n"
    "s_waitcnt vmcnt(0)\n"
    "v_readfirstlane_b32 s75, v14\n"
    "v_readfirstlane_b32 s76, v15\n"
    "s_xor_b32 s75, s75, s48\n"
    "s_xor_b32 s76, s76, s49\n"
    "s_cselect_b32 s76, 2, 0\n"
    "s_or_b32 s76, s76, s75\n"
    "s_cmp_lt_u32 s76, 2\n"
    "s_cbranch_scc1 .LrFIN_par1_%=\n"
    "s_sleep 4\n"
    "s_sub_u32 s70, s70, 1\n"
    "s_cbranch_scc0 .LrFIN_par_%=\n"
    "s_mov_b32 s70, 0\n"
    ".LrFIN_par1_%=:\n"
    "s_and_b32 s75, s75, 1\n"
    "s_lshl_b32 s75, s75, 13\n"
    "s_add_u32 s36, s36, s75\n"
    "s_addc_u32 s37, s37, 0\n"
    "s_and_b32 s76, s75, s61\n"
    "s_add_u32 s40, s40, s76\n"
    "s_addc_u32 s41, s41, 0\n"
    "s_and_b32 s76, s75, s63\n"
    "s_add_u32 s44, s44, s76\n"
    "s_addc_u32 s45, s45, 0\n"
    "s_branch .LrFIN_block_%=\n"
    ".LrFIN_LOOPB_%=:\n"
    ".LrFIN_top_%=:\n"
    "s_mov_b32 s68, s69\n"
    "s_add_u32 s69, s68, 1\n"
    "s_and_b32 s75, s68, s58\n"
    "s_mul_i32 s77, s75, 36864\n"
    "s_add_u32 s77, s77, s59\n"
    "s_add_u32 s78, s77, s60\n"
    "s_and_b32 s71, s69, 7\n"
    "s_mul_i32 s71, s71, 4194304\n"
    "s_add_u32 s71, s71, s52\n"
    "s_add_u32 s72, s71, 4096\n"
    "s_and_b32 s73, s69, s55\n"
    "s_mul_i32 s73, s73, s54\n"
    "s_add_u32 s73, s73, s53\n"
    "s_add_u32 s74, s73, s56\n"
    "s_waitcnt vmcnt(11)\n"
    "v_mfma_f32_16x16x32_bf16 v[80:83], v[16:19], v[32:35], 0\n"
    "s_waitcnt vmcnt(10)\n"
    "v_mfma_f32_16x16x32_bf16 v[80:83], v[20:23], v[36:39], v[80:83]\n"
    "s_waitcnt vmcnt(9)\n"
    "v_mfma_f32_16x16x32_bf16 v[84:87], v[16:19], v[40:43], 0\n"
    "s_waitcnt vmcnt(8)\n"
    "v_mfma_f32_16x16x32_bf16 v[84:87], v[20:23], v[44:47], v[84:87]\n"
    "s_waitcnt vmcnt(7)\n"
    "v_mfma_f32_16x16x32_bf16 v[88:91], v[16:19], v[48:51], 0\n"
    "s_waitcnt vmcnt(6)\n"
    "v_mfma_f32_16x16x32_bf16 v[88:91], v[20:23], v[52:55], v[88:91]\n"
    "s_waitcnt vmcnt(5)\n"
    "v_mfma_f32_16x16x32_bf16 v[92:95], v[16:19], v[56:59], 0\n"
    "s_waitcnt vmcnt(4)\n"
    "v_mfma_f32_16x16x32_bf16 v[92:95], v[20:23], v[60:63], v[92:95]\n"
    "s_waitcnt vmcnt(3)\n"
    "v_mfma_f32_16x16x32_bf16 v[96:99], v[16:19], v[64:67], 0\n"
    "s_waitcnt vmcnt(2)\n"
    "v_mfma_f32_16x16x32_bf16 v[80:83], v[24:27], v[68:71], v[80:83]\n"
    "v_mfma_f32_16x16x32_bf16 v[84:87], v[28:31], v[68:71], v[84:87]\n"
    "s_waitcnt vmcnt(1)\n"
    "v_mfma_f32_16x16x32_bf16 v[88:91], v[24:27], v[72:75], v[88:91]\n"
    "v_mfma_f32_16x16x32_bf16 v[92:95], v[28:31], v[72:75], v[92:95]\n"
    "s_waitcnt vmcnt(0)\n"
    "v_mfma_f32_16x16x32_bf16 v[96:99], v[24:27], v[76:79], v[96:99]\n"
    "v_add_u32 v120, v122, v1\n"
    "v_ashrrev_i32 v120, 1, v120\n"
    "v_cmp_gt_i32 vcc, s69, v120\n"
    "s_nop 0\n"
    "s_or_b64 exec, vcc, s[50:51]\n"
    "buffer_atomic_smax v122, v0, s[36:39], 0 offen sc0 sc1\n"
    "s_mov_b64 exec, -1\n"
    "v_lshl_or_b32 v14, s68, 1, 1\n"
    "s_mov_b64 exec, s[64:65]\n"
    "buffer_atomic_smax v14, v13, s[36:39], 0 offen sc1\n"
    "s_mov_b64 exec, -1\n"
    "v_cvt_pk_bf16_f32 v100, v80, v81\n"
    "v_cvt_pk_bf16_f32 v101, v82, v83\n"
    "v_cvt_pk_bf16_f32 v102, v84, v85\n"
    "v_cvt_pk_bf16_f32 v103, v86, v87\n"
    "v_cvt_pk_bf16_f32 v104, v88, v89\n"
    "v_cvt_pk_bf16_f32 v105, v90, v91\n"
    "v_cvt_pk_bf16_f32 v106, v92, v93\n"
    "v_cvt_pk_bf16_f32 v107, v94, v95\n"
    "v_cvt_pk_bf16_f32 v108, v96, v97\n"
    "v_cvt_pk_bf16_f32 v109, v98, v99\n"
    "v_permlane16_swap_b32 v100, v102\n"
    "v_permlane16_swap_b32 v101, v103\n"
    "ds_write_b128 v10, v[100:103]\n"
    "v_permlane16_swap_b32 v104, v106\n"
    "v_permlane16_swap_b32 v105, v107\n"
    "ds_write_b128 v10, v[104:107] offset:128\n"
    "v_permlane16_swap_b32 v108, v110\n"
    "v_permlane16_swap_b32 v109, v111\n"
    "ds_write_b128 v12, v[108:111]\n"
    "v_xor_b32 v10, 16384, v10\n"
    "v_xor_b32 v12, 16384, v12\n"
    "s_waitcnt lgkmcnt(0)\n"
    "s_barrier\n"
    "ds_read_b128 v[100:103], v11 offset:0\n"
    "ds_read_b128 v[104:107], v11 offset:4096\n"
    "ds_read_b128 v[108:111], v11 offset:8192\n"
    "v_xor_b32 v11, 16384, v11\n"
    "s_waitcnt lgkmcnt(2)\n"
    "buffer_store_dwordx4 v[100:103], v4, s[44:47], s77 offen sc1\n"
    "s_waitcnt lgkmcnt(1)\n"
    "buffer_store_dwordx4 v[104:107], v4, s[44:47], s78 offen sc1\n"
    "s_waitcnt lgkmcnt(0)\n"
    "buffer_store_dwordx4 v[108:111], v5, s[44:47], s77 offen sc1\n"
    "s_cmp_eq_u32 s69, 64\n"
    "s_cbranch_scc1 .LrFIN_fin_%=\n"
    "s_waitcnt vmcnt(4)\n"
    "v_add_u32 v120, v122, v1\n"
    "v_ashrrev_i32 v120, 1, v120\n"
    "v_cmp_gt_i32 vcc, s69, v120\n"
    "s_cbranch_vccnz .LrFIN_idle_%=\n"
    "buffer_load_dwordx4 v[32:35], v2, s[36:39], s71 offen sc1\n"
    "buffer_load_dwordx4 v[36:39], v2, s[36:39], s71 offen offset:512 sc1\n"
    "buffer_load_dwordx4 v[40:43], v2, s[36:39], s71 offen offset:2048 sc1\n"
    "buffer_load_dwordx4 v[44:47], v2, s[36:39], s71 offen offset:2560 sc1\n"
    "buffer_load_dwordx4 v[48:51], v2, s[36:39], s72 offen sc1\n"
    "buffer_load_dwordx4 v[52:55], v2, s[36:39], s72 offen offset:512 sc1\n"
    "buffer_load_dwordx4 v[56:59], v2, s[36:39], s72 offen offset:2048 sc1\n"
    "buffer_load_dwordx4 v[60:63], v2, s[36:39], s72 offen offset:2560 sc1\n"
    "buffer_load_dwordx4 v[64:67], v3, s[36:39], s71 offen sc1\n"
    "buffer_load_dwordx4 v[68:71], v6, s[40:43], s73 offen sc1\n"
    "buffer_load_dwordx4 v[72:75], v6, s[40:43], s74 offen sc1\n"
    "buffer_load_dwordx4 v[76:79], v7, s[40:43], s73 offen sc1\n"
    "s_branch .LrFIN_top_%=\n"
    ".LrFIN_LOOPE_%=:\n"
    ".LrFIN_fin_%=:\n"
    "s_waitcnt vmcnt(0)\n"
    "s_branch .LrFIN_FIN_%=\n"
    ".LrFIN_idle_%=:\n"
    ".LrFIN_block_%=:\n"
    "v_add_u32 v120, v122, v1\n"
    "v_ashrrev_i32 v120, 1, v120\n"
    "v_cmp_gt_i32 vcc, s69, v120\n"
    "s_nop 0\n"
    "s_or_b64 exec, vcc, s[50:51]\n"
    "buffer_atomic_smax v122, v0, s[36:39], 0 offen sc0 sc1\n"
    "s_mov_b64 exec, -1\n"
    "s_sleep 3\n"
    "s_waitcnt vmcnt(1)\n"
    "v_add_u32 v120, v123, v1\n"
    "v_ashrrev_i32 v120, 1, v120\n"
    "v_cmp_gt_i32 vcc, s69, v120\n"
    "s_cbranch_vccz .LrFIN_go_%=\n"
    "v_add_u32 v120, v123, v1\n"
    "v_ashrrev_i32 v120, 1, v120\n"
    "v_cmp_gt_i32 vcc, s69, v120\n"
    "s_nop 0\n"
    "s_or_b64 exec, vcc, s[50:51]\n"
    "buffer_atomic_smax v123, v0, s[36:39], 0 offen sc0 sc1\n"
    "s_mov_b64 exec, -1\n"
    "s_sleep 3\n"
    "s_waitcnt vmcnt(1)\n"
    "v_add_u32 v120, v122, v1\n"
    "v_ashrrev_i32 v120, 1, v120\n"
    "v_cmp_gt_i32 vcc, s69, v120\n"
    "s_cbranch_vccz .LrFIN_go_%=\n"
    "s_sub_u32 s70, s70, 1\n"
    "s_cbranch_scc0 .LrFIN_block_%=\n"
    "s_mov_b32 s70, 0\n"
    ".LrFIN_go_%=:\n"
    "s_and_b32 s71, s69, 7\n"
    "s_mul_i32 s71, s71, 4194304\n"
    "s_add_u32 s71, s71, s52\n"
    "s_add_u32 s72, s71, 4096\n"
    "s_and_b32 s73, s69, s55\n"
    "s_mul_i32 s73, s73, s54\n"
    "s_add_u32 s73, s73, s53\n"
    "s_add_u32 s74, s73, s56\n"
    "buffer_load_dwordx4 v[32:35], v2, s[36:39], s71 offen sc1\n"
    "buffer_load_dwordx4 v[36:39], v2, s[36:39], s71 offen offset:512 sc1\n"
    "buffer_load_dwordx4 v[40:43], v2, s[36:39], s71 offen offset:2048 sc1\n"
    "buffer_load_dwordx4 v[44:47], v2, s[36:39], s71 offen offset:2560 sc1\n"
    "buffer_load_dwordx4 v[48:51], v2, s[36:39], s72 offen sc1\n"
    "buffer_load_dwordx4 v[52:55], v2, s[36:39], s72 offen offset:512 sc1\n"
    "buffer_load_dwordx4 v[56:59], v2, s[36:39], s72 offen offset:2048 sc1\n"
    "buffer_load_dwordx4 v[60:63], v2, s[36:39], s72 offen offset:2560 sc1\n"
    "buffer_load_dwordx4 v[64:67], v3, s[36:39], s71 offen sc1\n"
    "buffer_load_dwordx4 v[68:71], v6, s[40:43], s73 offen sc1\n"
    "buffer_load_dwordx4 v[72:75], v6, s[40:43], s74 offen sc1\n"
    "buffer_load_dwordx4 v[76:79], v7, s[40:43], s73 offen sc1\n"
    "s_branch .LrFIN_top_%=\n"
    ".LrFIN_FIN_%=:\n"
  );
}

__global__ void __launch_bounds__(256, 1) mega(Params p) {
  extern __shared__ __attribute__((aligned(16))) unsigned char smem[];
  const rsrc_t rw = __builtin_amdgcn_make_buffer_rsrc(p.ws, 0, WS_END + 8192, 0x00020000);
  const unsigned long long t00 = __builtin_amdgcn_s_memrealtime();
  if (threadIdx.x < 64) {
    const int lane = threadIdx.x, b = blockIdx.x;
    if (p.role_mask & 0x40000) st16(rw, (v4u){~0u, ~0u, ~0u, ~0u}, lane < 22 ? CHUNK + (u32)b * 352 + lane * 16 : OOB, 0);
    const u64 tg = ld8u(rw, EPOCH) + 1;
    drain();
    if (lane == 0) {
      {
        const v2u tv = {(u32)tg, (u32)(tg >> 32)};
        const u32 a = READY + (u32)b * 8;
        asm volatile("buffer_atomic_swap_x2 %0, %1, %2, 0 offen sc1" :: "v"(tv), "v"(a), "s"(rw) : "memory");
      }
      int rid = (int)(__builtin_amdgcn_s_getreg(20 | (3 << 11)) & 7u) * 32 + (b >> 3);
#pragma unroll 1
      for (int k = 0; k < 256; ++k) {
        if (__hip_atomic_exchange((u64*)(p.ws + CLAIM) + ((rid & 31) * 8 + (rid >> 5)), tg, __ATOMIC_RELAXED, __HIP_MEMORY_SCOPE_AGENT) != tg) break;
        rid = (rid + 1) & 255;
      }
      *(volatile v4u*)smem = (v4u){(u32)rid, (u32)tg, (u32)(tg >> 32), 0u};
    }
  }
  int rid = (int)(__builtin_amdgcn_s_getreg(20 | (3 << 11)) & 7u) * 32 + (int)(blockIdx.x >> 3);
  u64 tag = 0;
  bool sure = false;
  for (;;) {
  if (!sure && (rid >= 128 || (rid & 31) >= SL_ATT)) { confirm(rid, tag, sure, smem); continue; }
  const int L = rid >> 7, j = (rid >> 5) & 3, sl = rid & 31;
  const u32 dbg = OFF_DBG + (u32)rid * 66 * 256;
  auto on = [&](int r) { return ((p.role_mask >> (2 + 8 * L + r)) & 1) != 0; };
  STAMP(65, 8);
  STAMP(65 + 32, 9);
  if (WARM && threadIdx.x >= 64) {
    const u32 n = threadIdx.x - 64;
    const u32 a = n < OFF_DBG / (1u << 20) ? (n << 20) + 4096 : OOB;
    asm volatile("s_mov_b32 m0, 163584\n\ts_nop 0\n\tbuffer_load_dword %0, %1, 0 offen lds" :: "v"(a), "s"(rw) : "memory");
  }
  if (((PULL >> L) & 1) && sl >= SL_WO && sl < SL_REDA) pull_weights(p, j, sl - SL_WO);
  if (sl < SL_P) { delay_start(L ? DL_R1 : 0, t00); if (on(1)) { role_rec(p, L, j, sl, dbg, rid, tag, sure, smem); continue; } if (!confirm(rid, tag, sure, smem)) continue; }
  else if (sl < SL_ATT) { delay_start(L ? DL_R1 : 0, t00); if (on(0)) { role_p(p, L, j, sl - SL_P, dbg, rid, tag, sure, smem); continue; } if (!confirm(rid, tag, sure, smem)) continue; }
  else if (sl < SL_WO) {
    const u32 shift = find_shift(rw, rid == 128 + SL_ATT && threadIdx.x < 64, tag);
    if (p.role_mask & 2) hot_init(p, rw, L, 2 * j + sl - SL_ATT, shift);
    if (on(2)) role_att(p, L, j, sl - SL_ATT, dbg, shift);
  }
  else if (sl < SL_UP) { delay_start(L ? DL_W1 : DL_W0, t00); if (on(3)) role_wo(p, L, j, sl - SL_WO, dbg, tag); }
  else if (sl < SL_DOWN) { delay_start(L ? DL_U1 : DL_U0, t00); if (on(5)) role_up(p, L, j, sl - SL_UP, dbg, tag); }
  else if (sl < SL_REDA) { delay_start(L ? DL_D1 : DL_D0, t00); if (on(6)) role_down(p, L, j, sl - SL_DOWN, dbg, tag); }
  else {
    if (sl == SL_REDA && (p.role_mask & 2)) hot_init(p, rw, L, 8 + j, find_shift(rw, false, tag));
    if ((p.role_mask & 1) && (L == 1 || sl == SL_REDF)) role_ln0(p, L == 0 ? 2 : (sl == SL_REDA ? 1 : 0), j, dbg, tag);
    if (sl == SL_REDF && L == 1) {
      if (on(7)) role_fin(p, j, dbg, tag);
    } else {
      v16u sa, sb, va, vb;
      if (sl == SL_REDA) red_args<1>(p, L, j, dbg, tag, sa, sb, va, vb);
      else red_args<2>(p, 0, j, dbg, tag, sa, sb, va, vb);
      if (on(sl == SL_REDA ? 4 : 7)) role_mid(sa, sb, va, vb);
    }
  }
  break;
  }
}

static void fill_params(Params& p, void* const* d_in, void* d_out, void* d_ws) {
  p.x = (const u16*)d_in[0]; p.mem0 = (const u16*)d_in[1];
  p.Wq = (const u16*)d_in[2]; p.Wk = (const u16*)d_in[3]; p.Wv = (const u16*)d_in[4]; p.Wo = (const u16*)d_in[5];
  p.w1 = (const u16*)d_in[8]; p.w3 = (const u16*)d_in[9]; p.w2 = (const u16*)d_in[10];
  p.out = (u16*)d_out; p.ws = (unsigned char*)d_ws;
}

extern "C" void kernel_launch(void* const* d_in, const int* in_sizes, int n_in, void* d_out, int out_size, void* d_ws,
                              size_t ws_size, hipStream_t stream) {
  Params p;
  fill_params(p, d_in, d_out, d_ws);
  (void)hipFuncSetAttribute((const void*)mega, hipFuncAttributeMaxDynamicSharedMemorySize, LDS_BYTES);
  p.role_mask = 0x7FFFF; p.unused = 0;
  mega<<<256, 256, LDS_BYTES, stream>>>(p);
}
```
